```python
import torch

def _softmax(x, axis=-1):
    return torch.softmax(x, dim=axis)

def _gelu(x):
    return torch.nn.functional.gelu(x, approximate='tanh')

def _silu(x):
    return torch.nn.functional.silu(x)

def _sigmoid(x):
    return torch.sigmoid(x)

def _layernorm(x, axis=-1, eps=1e-05):
    m = x.mean(dim=axis, keepdim=True)
    v = x.var(dim=axis, keepdim=True, unbiased=False)
    return (x - m) / torch.sqrt(v + eps)

def _scatter_add(tgt, idx, src, axis=0):
    return tgt.clone().index_add_(axis, idx, src)

def _segment_sum(src, seg, n):
    out = torch.zeros((n,) + src.shape[1:], dtype=src.dtype)
    return out.index_add_(0, seg, src)

def _pad(x, pw):
    flat = tuple((v for pair in reversed(pw) for v in pair))
    return torch.nn.functional.pad(x, flat)
kernel_name = 'adaLN_cond__ssm_conv_h3__glu_gated_bilinear__prompt'

def setup_inputs():

    _w = lambda a: (lambda t: t.to(torch.bfloat16) if t.is_floating_point() else t)(torch.as_tensor(a))

    def _A_setup():
        B, S, D = (1, 2048, 640)
        ks = 4
        return {'q': _w((lambda *s: torch.randn(*s, dtype=torch.bfloat16) * 0.05)(B, S, D)), 'k': _w((lambda *s: torch.randn(*s, dtype=torch.bfloat16) * 1.6)(B, S, D)), 'v': _w((lambda *s: torch.randn(*s, dtype=torch.bfloat16) * 1.6)(B, S, D)), 'shift_k': _w((lambda *s: torch.randn(*s, dtype=torch.bfloat16) * 1.6)(D, ks) * 0.5), 'ssm_k': _w((lambda *s: torch.randn(*s, dtype=torch.bfloat16) * 1.6)(D, S) * (1.0 / S ** 0.5)), 'ssm_D': _w((lambda *s: torch.randn(*s, dtype=torch.bfloat16) * 1.6)(D))}
    _A_in = _A_setup()

    def _B_setup():
        B, S, D, F = (1, 2048, 640, 1536)
        return {'x': _w((lambda *s: torch.randn(*s, dtype=torch.bfloat16) * 0.05)(B, S, D)), 'w_gate': _w((lambda *s: torch.randn(*s, dtype=torch.bfloat16) * 3.2)(D, F) * D ** (-0.5)), 'w_up': _w((lambda *s: torch.randn(*s, dtype=torch.bfloat16) * 3.2)(D, F) * D ** (-0.5)), 'w_down': _w((lambda *s: torch.randn(*s, dtype=torch.bfloat16) * 3.2)(F, D) * F ** (-0.5))}
    _B_in = _B_setup()
    return {'A_q': _A_in['q'], 'A_k': _A_in['k'], 'A_v': _A_in['v'], 'A_shift_k': _A_in['shift_k'], 'A_ssm_k': _A_in['ssm_k'], 'A_ssm_D': _A_in['ssm_D'], 'B_x': _B_in['x'], 'B_w_gate': _B_in['w_gate'], 'B_w_up': _B_in['w_up'], 'B_w_down': _B_in['w_down'], 'c': _w((lambda *s: torch.randn(*s, dtype=torch.bfloat16) * 0.2)(1, 640) * 8.0), 'w_mod': _w((lambda *s: torch.randn(*s, dtype=torch.bfloat16) * 0.2)(640, 6 * 640) * 640 ** (-0.5) * 8.0)}

def reference(A_q, A_k, A_v, A_shift_k, A_ssm_k, A_ssm_D, B_x, B_w_gate, B_w_up, B_w_down, c, w_mod):

    def _A_ref(q, k, v, shift_k, ssm_k, ssm_D):
        B, S, D = q.shape
        ks = shift_k.shape[-1]
        k_pad = _pad(k, ((0, 0), (ks - 1, 0), (0, 0)))
        k_shift = torch.zeros_like(k)
        for t in range(ks):
            k_shift = k_shift + k_pad[:, t:t + S, :] * shift_k[:, ks - 1 - t][None, None, :]
        kv = k_shift * v
        L = 2 * S
        kv_pad = _pad(kv, ((0, 0), (0, S), (0, 0)))
        kf_pad = _pad(ssm_k, ((0, 0), (0, S)))
        kv_f = (lambda x, axes=(-2, -1): torch.fft.rfft2(x.float(), dim=axes))(kv_pad[:, None, :, :], axes=(1, 2))
        kf_f = (lambda x, axes=(-2, -1): torch.fft.rfft2(x.float(), dim=axes))(kf_pad[:, None, :], axes=(1, 2))
        kf_f = (lambda x, *d: x.permute(*d))(kf_f, 1, 2, 0)
        y_f = kv_f * kf_f[None, :, :, :]
        y = (lambda x, s, axes=(-2, -1): torch.fft.irfft2(x, s=s, dim=axes).to(torch.bfloat16))(y_f, (1, L), axes=(1, 2))[:, 0, :S, :]
        y = y + kv * ssm_D[None, None, :]
        return q * y

    def _B_ref(x, w_gate, w_up, w_down):
        g = torch.einsum('bsd,df->bsf', x, w_gate)
        u = torch.einsum('bsd,df->bsf', x, w_up)
        h = g * u
        return torch.einsum('bsf,fd->bsd', h, w_down)
    x = A_q
    mod = (_silu(c) @ w_mod).reshape(c.shape[0], 6, 1, -1)
    sa, ca, ga, sb, cb, gb = (mod[:, 0].clone(), mod[:, 1].clone(), mod[:, 2].clone(), mod[:, 3].clone(), mod[:, 4].clone(), mod[:, 5].clone())
    h = _layernorm(x, axis=-1) * (1 + ca) + sa
    x = x + ga * _A_ref(h, A_k, A_v, A_shift_k, A_ssm_k, A_ssm_D)
    h = _layernorm(x, axis=-1) * (1 + cb) + sb
    x = x + gb * _B_ref(h, B_w_gate, B_w_up, B_w_down)
    return x
```

```cpp
#include <hip/hip_runtime.h>
#include <hip/hip_bf16.h>
#include <stdint.h>
#include <type_traits>

typedef unsigned short u16;
typedef unsigned long long u64;
typedef __attribute__((ext_vector_type(8))) short bf16x8;
typedef __attribute__((ext_vector_type(4))) float f32x4;
typedef __attribute__((ext_vector_type(8))) float f32x8;
typedef __attribute__((ext_vector_type(16))) float f32x16;
typedef __attribute__((ext_vector_type(32))) float f32x32;
typedef __attribute__((ext_vector_type(4))) unsigned int u32x4;
typedef __attribute__((ext_vector_type(2))) unsigned int u32x2;
typedef __attribute__((ext_vector_type(3))) unsigned int u32x3;
typedef __attribute__((ext_vector_type(8))) unsigned int u32x8;
typedef __attribute__((ext_vector_type(16))) unsigned int u32x16;
typedef __attribute__((ext_vector_type(32))) unsigned int u32x32;
typedef __attribute__((ext_vector_type(2))) __bf16 bf16x2_t;
typedef __attribute__((ext_vector_type(2))) float f32x2;
typedef __attribute__((address_space(3))) char lds_char;
typedef u32x4 u32x4_a4 __attribute__((aligned(4)));
#define GAS __attribute__((address_space(1)))

static constexpr int S_ = 2048, D_ = 640, F_ = 1536, NMOD = 6 * 640;

__device__ __forceinline__ float bf2f(u16 v) { return __uint_as_float(((unsigned)v) << 16); }
__device__ __forceinline__ float lo16(unsigned w) { return __uint_as_float(w << 16); }
__device__ __forceinline__ float hi16(unsigned w) { return __uint_as_float(w & 0xFFFF0000u); }
__device__ __forceinline__ unsigned pk2(float a, float b) {
  f32x2 v = {a, b};
  bf16x2_t r = __builtin_convertvector(v, bf16x2_t);
  return __builtin_bit_cast(unsigned, r);
}
__device__ __forceinline__ float rb1(float a) { return __uint_as_float(pk2(0.f, a)); }
__device__ __forceinline__ f32x2 rb2(f32x2 a) { return (f32x2){rb1(a[0]), rb1(a[1])}; }
__device__ __forceinline__ f32x2 up2(unsigned a) { return (f32x2){lo16(a), hi16(a)}; }
__device__ __forceinline__ unsigned umin_(unsigned a, unsigned b) { return a < b ? a : b; }
__device__ __forceinline__ unsigned umax_(unsigned a, unsigned b) { return a > b ? a : b; }
__device__ __forceinline__ unsigned san(unsigned a) { return umin_(a, 0xFFFFFFFEu); }
__device__ __forceinline__ u32x4 san4(u32x4 a) { return (u32x4){san(a[0]), san(a[1]), san(a[2]), san(a[3])}; }
__device__ __forceinline__ float modv(u32x4 a, u32x4 b) {
  return rb1((__uint_as_float(a[0]) + __uint_as_float(a[2])) + (__uint_as_float(b[0]) + __uint_as_float(b[2])));
}
__device__ __forceinline__ unsigned tagx(u32x4 f, u32x2 t) { return (f[0] ^ t[0]) | (f[1] ^ t[1]) | (f[2] ^ t[0]) | (f[3] ^ t[1]); }
__device__ __forceinline__ unsigned modx(u32x4 a, u32x4 b, unsigned tag) { return (a[1] ^ tag) | (a[3] ^ tag) | (b[1] ^ tag) | (b[3] ^ tag); }
__device__ __forceinline__ unsigned mx4(u32x4 a) { return umax_(umax_(a[0], a[1]), umax_(a[2], a[3])); }
__device__ __forceinline__ unsigned ror_(unsigned a, unsigned s) { return __builtin_amdgcn_alignbit(a, a, s); }
#define DPP_ADDU(v, ctrl) v += __builtin_amdgcn_update_dpp(0u, v, ctrl, 0xf, 0xf, true)
__device__ __forceinline__ unsigned cks(u32x4 d, unsigned sh) {
  unsigned s = ror_(d[0] + ror_(d[1], 7) + (ror_(d[2], 13) + ror_(d[3], 19)), sh);
  DPP_ADDU(s, 0xB1); DPP_ADDU(s, 0x4E); DPP_ADDU(s, 0x141);
  return s;
}
__device__ __forceinline__ unsigned cks2(unsigned d0, unsigned d1, int lane) {
  unsigned s = ror_(d0, (lane & 1) * 13) + ror_(d1, 7 + (lane & 1) * 12);
  DPP_ADDU(s, 0xB1);
  s = ror_(s, (lane & 14) * 2);
  DPP_ADDU(s, 0x4E); DPP_ADDU(s, 0x141); DPP_ADDU(s, 0x140);
  return s;
}
#define SCHEDB() __builtin_amdgcn_sched_barrier(0)
#define LDS32(p) (*(__attribute__((address_space(3))) unsigned*)(p))
#define LDS64(p) (*(__attribute__((address_space(3))) u32x2*)(p))
#define LDS128(p) (*(__attribute__((address_space(3))) u32x4*)(p))
#define LDSF(p) (*(__attribute__((address_space(3))) float*)(p))
#define LDSF2(p) (*(__attribute__((address_space(3))) f32x2*)(p))
#define LDSF4(p) (*(__attribute__((address_space(3))) f32x4*)(p))
#define WAITV(n) asm volatile("s_waitcnt vmcnt(" #n ")" ::: "memory")
#define WAITL() asm volatile("s_waitcnt lgkmcnt(0)" ::: "memory")
#define BAR() do { asm volatile("s_waitcnt lgkmcnt(0)\n\ts_barrier" ::: "memory"); } while (0)
#define DPP_ADD(v, ctrl) v += __builtin_bit_cast(float, __builtin_amdgcn_update_dpp(0, __builtin_bit_cast(int, v), ctrl, 0xf, 0xf, true))
#define DSRTR(dst, addr, off) asm volatile("ds_read_b64_tr_b16 %0, %1 offset:%2" : "=v"(dst) : "v"(addr), "i"(off))
#define LD128(off) __builtin_amdgcn_raw_buffer_load_b128(rs, (off), 0, 16)
#define LD64(off) __builtin_amdgcn_raw_buffer_load_b64(rs, (off), 0, 16)
#define LD32(off) __builtin_amdgcn_raw_buffer_load_b32(rs, (off), 0, 16)
#define ST128(off, v) __builtin_amdgcn_raw_buffer_store_b128((v), rs, (off), 0, 16)
#define ST64(off, v) __builtin_amdgcn_raw_buffer_store_b64((v), rs, (off), 0, 16)
#define ST32(off, v) __builtin_amdgcn_raw_buffer_store_b32((v), rs, (off), 0, 16)
#define MFMA(a, b, c) __builtin_amdgcn_mfma_f32_16x16x32_bf16(__builtin_bit_cast(bf16x8, a), __builtin_bit_cast(bf16x8, b), c, 0, 0, 0)
#define MFMA0(d, a, b) asm volatile("v_mfma_f32_16x16x32_bf16 %0, %1, %2, 0" : "=&v"(d) : "v"(a), "v"(b))
#define MFMAC(d, a, b) asm volatile("v_mfma_f32_16x16x32_bf16 %0, %1, %2, %0" : "+v"(d) : "v"(a), "v"(b))
static constexpr int SPIN_CAP = 1 << 18;
static constexpr int CODE0_BYTES = 52288;

static constexpr unsigned WS_EPOCH = 0;
static constexpr unsigned WS_FP2 = 196608;
static constexpr unsigned WS_FP1 = 4096;
static constexpr unsigned WS_FW = WS_FP1 + 16384;
static constexpr unsigned WS_FD = WS_FW + 40960;
static constexpr unsigned WS_MOD = 786432;
static constexpr unsigned WS_STAT = 131072 + 16384;
static constexpr unsigned WS_PART = 262144;
static constexpr unsigned WS_KVT = 1u << 20;
static constexpr unsigned WS_QT = WS_KVT + (3u << 20);
static constexpr unsigned WS_CK = 2686976;
static constexpr unsigned WS_X1T = WS_QT + (3u << 20);
static constexpr unsigned WS_WP = WS_X1T + (3u << 20);
static constexpr unsigned WS_WD = WS_WP + (4u << 20);
static constexpr unsigned WS_HH = WS_WD + (2u << 20);
static constexpr unsigned WS_HC = WS_HH + (6u << 20);
static constexpr unsigned WS_END = WS_HC + (1u << 20);

static constexpr int KCP = 4160, KCH = 4 * KCP;
static constexpr int XR_CH = 7680;
static constexpr int L_XI = 3 * KCH;
static constexpr int L_TR = 72960;
static constexpr int TRW = 11008;
static constexpr int L_EX = L_TR;
static constexpr int L_Q = L_TR + 4 * 9216;
static constexpr int L_GW = L_Q + 28672;
static constexpr int L_GS = L_GW + 20480;
static constexpr int L_FL = L_GS + 1536;
static constexpr int L_KS = L_Q + 256;
static constexpr int L_VS = L_KS + 17 * 656;
static constexpr int L_WK = L_VS + 16 * 656;
static constexpr int L_CR = L_WK + 640;
static constexpr int L_HX = 131072;
static constexpr int L_XR = L_HX + 12288;
static constexpr int L_DJ = L_XR;
static constexpr int L_CB = L_XR + 12288;
static constexpr int L_RS = L_CB + 1536;
static constexpr int L_GB = L_RS + 512;
static constexpr int LDS_TOTAL = L_FL + 128 + 160;

__device__ __forceinline__ int kpos(int d) {
  const int g = (d * 205) >> 10, m = d - 5 * g;
  return m == 2 ? g : 128 + 2 * g + (m >= 3) + ((m == 1 || m == 4) ? 256 : 0);
}
__device__ __forceinline__ int kchn(int ks, int r) { return (ks < 4 ? 160 * ks + 2 : 80 * ((ks - 4) & 7) + (ks >= 12)) + (((ks < 4 ? 10 : 5) * r + 1) >> 1); }
__device__ __forceinline__ u32x4 cat2(u32x2 a, u32x2 b) { return __builtin_shufflevector(a, b, 0, 1, 2, 3); }

__global__ __launch_bounds__(256, 1) void mega(const u16* qin_, const u16* kin_, const u16* vin_, const u16* shiftk_, const u16* ssmk_,
                                               const u16* ssmD_, const u16* wg_, const u16* wu_, const u16* wdn_, const u16* cin_,
                                               const u16* wmod_, char* ws_, u16* out_) {
  u32x16 ka;
  u32x8 kb;
  u32x2 kc;
  asm volatile("s_load_dwordx16 %0, %3, 0x0\n\ts_load_dwordx8 %1, %3, 0x40\n\ts_load_dwordx2 %2, %3, 0x60"
               : "=&s"(ka), "=&s"(kb), "=&s"(kc) : "s"(__builtin_amdgcn_kernarg_segment_ptr()));
  SCHEDB();
  __shared__ __attribute__((aligned(16))) char lds_[LDS_TOTAL];
  lds_char* lds = (lds_char*)lds_;
  const unsigned lbase = (unsigned)(size_t)lds_;
  const int tid = threadIdx.x, lane = tid & 63;
  int w = __builtin_amdgcn_readfirstlane(tid >> 6), b = blockIdx.x;
  const u32x4 FILL = {~0u, ~0u, ~0u, ~0u};
  const u32x2 SET = {1u, 1u};
  if (w == 3 && b < (CODE0_BYTES + 4095) / 4096 * 8) {
    const size_t pc = __builtin_amdgcn_s_getpc() & ~255ull;
    unsigned keep;
    asm volatile("s_mov_b32 %0, m0\n\ts_mov_b32 m0, %3\n\ts_nop 0\n\tglobal_load_lds_dword %1, %2\n\ts_mov_b32 m0, %0"
                 : "=&s"(keep) : "v"(umin_(((b >> 3) * 64 + lane) * 64, CODE0_BYTES - 64)), "s"(pc), "s"(lbase + L_Q) : "memory");
  }
#define GLD128(d, vo, sb, off) asm volatile("global_load_dwordx4 %0, %1, %2 offset:%3" : "=v"(d) : "v"(vo), "s"(sb), "i"(off) : "memory")
  u64 T64;
  unsigned vK, vV, v9, o3;
  {
    const int xcd = b & 7, idx = b >> 3;
    const int r10 = (lane * 26) >> 8;
    vV = (unsigned)((xcd * 4 + (idx >> 3)) * 64 + 4 * w + r10) * (D_ * 2) + (idx & 7) * 160 + (lane - 10 * r10) * 16;
    vK = vV - 4 * D_ * 2;
    const int g = idx < 30 ? xcd * 30 + idx : 0, gq = g & 3, gcol = (g >> 2) * 64;
    v9 = w == 0 ? vV + 60 * D_ * 2 : (w == 1 ? (idx & 7) * 640 + lane * 16 : gq * 320 + (lane < 19 ? lane : 19) * 16);
    const int r = lane >> 3, c = lane & 7;
    o3 = (gq * 160 + w * 40 + r) * (NMOD * 2) + ((((c >> 1) ^ ((r >> 1) & 3)) << 1) + (c & 1)) * 16 + gcol * 2;
    asm volatile("" : "+v"(vK), "+v"(vV), "+v"(v9), "+v"(o3));
  }
  asm volatile("s_waitcnt lgkmcnt(0)" : "+s"(w), "+s"(ka), "+s"(kb), "+s"(kc));
#define KARG(v, i) (((u64)v[2 * (i) + 1] << 32) | v[2 * (i)])
  const GAS u16 *qin = (const GAS u16*)KARG(ka, 0), *kin = (const GAS u16*)KARG(ka, 1), *vin = (const GAS u16*)KARG(ka, 2);
  const GAS u16 *shiftk = (const GAS u16*)KARG(ka, 3), *ssmk = (const GAS u16*)KARG(ka, 4), *ssmD = (const GAS u16*)KARG(ka, 5);
  const GAS u16 *wg = (const GAS u16*)KARG(ka, 6), *wu = (const GAS u16*)KARG(ka, 7), *wdn = (const GAS u16*)KARG(kb, 0);
  const GAS u16 *cin = (const GAS u16*)KARG(kb, 1), *wmod = (const GAS u16*)KARG(kb, 2);
  GAS char* ws = (GAS char*)KARG(kb, 3);
  GAS u16* out = (GAS u16*)KARG(kc, 0);
  asm volatile("s_load_dwordx2 %0, %1, 0x0" : "=&s"(T64) : "s"(ws) : "memory");
  {
    const u32x4 rk = {ka[2], ka[3] & 0xFFFFu, (unsigned)(S_ * D_ * 2), 0x00020000u}, rv = {ka[4], ka[5] & 0xFFFFu, (unsigned)(S_ * D_ * 2), 0x00020000u};
    asm volatile(
      "s_mov_b32 s45, m0\n\t"
      "s_mov_b32 m0, %6\n\t"
      "s_mov_b32 exec_hi, 0xff\n\t"
      "buffer_load_dwordx4 %0, %3, 0 offen lds\n\t"
      "s_add_u32 m0, m0, 2624\n\t"
      "s_mov_b32 s44, 15360\n\t"
      "buffer_load_dwordx4 %1, %3, s44 offen lds\n\t"
      "s_add_u32 m0, m0, 2624\n\t"
      "s_mov_b32 s44, 35840\n\t"
      "buffer_load_dwordx4 %1, %3, s44 offen lds\n\t"
      "s_add_u32 m0, m0, 2624\n\t"
      "s_mov_b32 s44, 56320\n\t"
      "buffer_load_dwordx4 %1, %3, s44 offen lds\n\t"
      "s_mov_b32 m0, %7\n\t"
      "s_nop 0\n\t"
      "global_load_lds_dwordx4 %2, %5\n\t"
      "s_add_u32 m0, %6, 11152\n\t"
      "s_nop 0\n\t"
      "buffer_load_dwordx4 %1, %4, 0 offen lds\n\t"
      "s_add_u32 m0, m0, 2624\n\t"
      "s_mov_b32 s44, 20480\n\t"
      "buffer_load_dwordx4 %1, %4, s44 offen lds\n\t"
      "s_add_u32 m0, m0, 2624\n\t"
      "s_mov_b32 s44, 40960\n\t"
      "buffer_load_dwordx4 %1, %4, s44 offen lds\n\t"
      "s_add_u32 m0, m0, 2624\n\t"
      "s_mov_b32 s44, 61440\n\t"
      "buffer_load_dwordx4 %1, %4, s44 offen lds\n\t"
      "s_mov_b32 exec_hi, -1\n\t"
      "s_mov_b32 m0, %11\n\t"
      "s_mov_b32 s42, %9\n\t"
      "s_mov_b32 s43, %10\n\t"
      "global_load_lds_dwordx4 %8, s[42:43]\n\t"
      "s_add_u32 m0, m0, 1024\n\t"
      "s_add_u32 s42, s42, 61440\n\t"
      "s_addc_u32 s43, s43, 0\n\t"
      "global_load_lds_dwordx4 %8, s[42:43]\n\t"
      "s_add_u32 m0, m0, 1024\n\t"
      "s_add_u32 s42, s42, 61440\n\t"
      "s_addc_u32 s43, s43, 0\n\t"
      "global_load_lds_dwordx4 %8, s[42:43]\n\t"
      "s_add_u32 m0, m0, 1024\n\t"
      "s_add_u32 s42, s42, 61440\n\t"
      "s_addc_u32 s43, s43, 0\n\t"
      "global_load_lds_dwordx4 %8, s[42:43]\n\t"
      "s_add_u32 m0, m0, 1024\n\t"
      "s_add_u32 s42, s42, 61440\n\t"
      "s_addc_u32 s43, s43, 0\n\t"
      "global_load_lds_dwordx4 %8, s[42:43]\n\t"
      "s_mov_b32 m0, s45\n\t"
        :: "v"(vK), "v"(vV), "v"(v9), "s"(rk), "s"(rv), "s"(w == 0 ? kin : (w == 1 ? shiftk : cin)), "s"(lbase + L_KS + w * 656),
           "s"(lbase + (w == 0 ? L_KS + 16 * 656 : (w == 1 ? L_WK : L_CR))), "v"(o3), "s"(kb[4]), "s"(kb[5]), "s"(lbase + L_GW + w * 5120)
        : "memory", "scc", "s42", "s43", "s44", "s45");
  }
  asm volatile("" : "+s"(w), "+s"(b));
  asm volatile("s_mov_b64 exec, 0\n\t" "Lpd74eef_ret0:\n\ts_mov_b64 exec, -1" :: "s"(w) : "scc");
  const int xcd = b & 7, idx = b >> 3;
  const __amdgpu_buffer_rsrc_t rs = __builtin_amdgcn_make_buffer_rsrc((void*)(size_t)ws, 0, WS_END, 0x00020000);
  const int L = xcd * 32 + idx;
  const int ct = L & 7, rt = L >> 3;
  const unsigned qto = WS_QT + rt * 128;
  const int srow = xcd * 256 + idx * 8;
  const int side = L & 1, ch0 = 5 * (L >> 1) + 2 * side;
  const int ip = lane & 15, fq = lane >> 4, q4 = ip >> 2, p4 = lane & 3;
  const int chA = ch0 + side, chH = ch0 + 2 - 2 * side;
  const int rowH = 512 * (side + (3 - 2 * side) * (w >> 1)) + 256 * (w & 1) + 4 * lane;
  const int rowA = 512 * w + 8 * lane;
  const int slot = w * 32 + idx;
  const bool jobW = slot < 80;
  const int j_ks = 5 * (xcd >> 1) + (jobW ? slot >> 4 : 0), j_bn = 8 * (xcd & 1) + ((slot & 15) >> 1), j_h = slot & 1;
  const int d_ks = (L * 205) >> 10, d_cb = 4 * (L - 5 * d_ks) + w;
  const int bm = (xcd >> 2) * 8 + (idx >> 2), bn = (xcd & 3) * 4 + (idx & 3);
  const int bm2 = (xcd >> 1) * 8 + (idx >> 2), bn2 = (xcd & 1) * 4 + (idx & 3);
  u32x4 fd[3];
  u32x2 fp[3], T;
#define SLD128(d, vo, off) asm volatile("global_load_dwordx4 %0, %1, %2 offset:%3 sc1" : "=v"(d) : "v"(vo), "s"(ws), "i"(off) : "memory")
  auto filter_loads = [&]() __attribute__((always_inline)) {
    const GAS u16* src = ssmk + (size_t)ch0 * S_ + S_;
    GLD128(fd[0], tid * 16, src, -4096); GLD128(fd[1], tid * 16, src, 0);
    GLD128(fd[2], tid * 16, src + S_, 0);
  };
  u32x2 dA;
  unsigned dH;
  {
    const GAS u16* src = ssmk + (size_t)ch0 * S_ + (w ? 512 * w - 4 : 0);
    asm volatile("s_load_dwordx2 %0, %5, 0x0\n\ts_load_dwordx2 %1, %5, 0x1000\n\ts_load_dwordx2 %2, %5, 0x2000\n\t"
                 "s_load_dwordx2 %3, %6, %7\n\ts_load_dword %4, %6, %8"
                 : "=&s"(fp[0]), "=&s"(fp[1]), "=&s"(fp[2]), "=&s"(dA), "=&s"(dH)
                 : "s"(src), "s"(ssmD), "s"((chA < 638 ? chA & ~1 : 636) * 2), "s"((chH & ~1) * 2) : "memory");
  }
#define FILT_REGS "+v"(fd[0]), "+v"(fd[1]), "+v"(fd[2])
  auto filter_copies = [&]() __attribute__((always_inline)) {
#pragma unroll
  for (int i = 0; i < 3; ++i) {
    const unsigned e0 = fd[i][0], e1 = fd[i][1], e2 = fd[i][2], e3 = fd[i][3];
    const unsigned p2 = __builtin_amdgcn_update_dpp(w ? fp[i][0] : 0u, e2, 0x138, 0xf, 0xf, false);
    const unsigned p3 = __builtin_amdgcn_update_dpp(w ? fp[i][1] : 0u, e3, 0x138, 0xf, 0xf, false);
    const unsigned A0 = __builtin_amdgcn_alignbit(p3, p2, 16), A1 = __builtin_amdgcn_alignbit(e0, p3, 16);
    const unsigned A2 = __builtin_amdgcn_alignbit(e1, e0, 16), A3 = __builtin_amdgcn_alignbit(e2, e1, 16);
    const unsigned A4 = __builtin_amdgcn_alignbit(e3, e2, 16);
    lds_char* base = lds + i * KCH + 16 * tid + 56;
    LDS64(base + 0 * KCP) = (u32x2){A0, A1};
    LDS64(base + 1 * KCP) = (u32x2){p3, e0};
    LDS64(base + 2 * KCP) = (u32x2){A1, A2};
    LDS64(base + 0 * KCP + 8) = (u32x2){A2, A3};
    LDS64(base + 1 * KCP + 8) = (u32x2){e1, e2};
    LDS64(base + 2 * KCP + 8) = (u32x2){A3, A4};
    LDS64(base + 3 * KCP) = (u32x2){e0, e1};
    LDS64(base + 3 * KCP + 8) = (u32x2){e2, e3};
    if (tid == 255) {
      LDS64(base + 0 * KCP + 16) = (u32x2){A4, e3 >> 16};
      LDS64(base + 1 * KCP + 16) = (u32x2){e3, 0u};
      LDS64(base + 2 * KCP + 16) = (u32x2){e3 >> 16, 0u};
    }
  }
  SCHEDB();
  };
  unsigned kva = lbase + L_KS + (lane >> 2) * 656 + 160 + (12 * w + (lane & 3)) * 4, vva = kva + (L_VS - L_KS - 160);
  unsigned wka = lbase + L_WK + (12 * w + (lane & 3)) * 16;
  lds_char* tk = lds + L_TR + w * TRW + (lane & 3) * 288 + (30 - 2 * (lane >> 2)) * 4;
  struct KvOps { u32x2 k[4], v[2]; u32x4 wk; };
#define KV_READ(o, j)                                                                                                          \
  asm volatile("ds_read_b128 %6, %9 offset:%c10\n\t"                                                                            \
               "ds_read2_b32 %0, %7 offset0:%c11 offset1:%c11+40\n\tds_read2_b32 %1, %7 offset0:%c11+80 offset1:%c11+124\n\t"     \
               "ds_read2_b32 %2, %7 offset0:%c11+164 offset1:%c11+204\n\tds_read2_b32 %3, %7 offset0:%c11+244 offset1:%c11+244\n\t" \
               "ds_read2_b32 %4, %8 offset0:%c11 offset1:%c11+40\n\tds_read2_b32 %5, %8 offset0:%c11+80 offset1:%c11+120"         \
               : "=&v"(o.k[0]), "=&v"(o.k[1]), "=&v"(o.k[2]), "=&v"(o.k[3]), "=&v"(o.v[0]), "=&v"(o.v[1]), "=&v"(o.wk)            \
               : "v"(kva), "v"(vva), "v"(wka), "i"(j * 64), "i"(j * 4) : "memory")
#define KV_REGS(o) "+v"(o.k[0]), "+v"(o.k[1]), "+v"(o.k[2]), "+v"(o.k[3]), "+v"(o.v[0]), "+v"(o.v[1]), "+v"(o.wk)
  auto kv_math = [&](const KvOps& o, int j) __attribute__((always_inline)) {
    f32x2 kf[7], y[4];
#pragma unroll
    for (int i = 0; i < 7; ++i) kf[i] = up2(o.k[i >> 1][i & 1]);
    const f32x2 w0 = {lo16(o.wk[0]), lo16(o.wk[2])}, w1 = {hi16(o.wk[0]), hi16(o.wk[2])};
    const f32x2 w2 = {lo16(o.wk[1]), lo16(o.wk[3])}, w3 = {hi16(o.wk[1]), hi16(o.wk[3])};
#pragma unroll
    for (int r = 0; r < 4; ++r) {
      f32x2 a = rb2(kf[r] * w3);
      a = rb2(a + rb2(kf[r + 1] * w2));
      a = rb2(a + rb2(kf[r + 2] * w1));
      a = rb2(a + rb2(kf[r + 3] * w0));
      y[r] = a * up2(o.v[r >> 1][r & 1]);
    }
#pragma unroll
    for (int c = 0; c < 2; ++c)
      LDS64(tk + (8 * j + c) * 144) = (u32x2){pk2(y[3][c], y[2][c]), pk2(y[1][c], y[0][c])};
  };
  const unsigned kvs = WS_KVT + (1984 - 64 * rt) * 2 + (unsigned)(ct * 80 + 24 * w + (lane >> 3)) * 4096 + (lane & 7) * 16;
  lds_char* kvl = lds + L_TR + w * TRW + (lane >> 3) * 144 + (lane & 7) * 16;
  unsigned wo[3];
#pragma unroll
  for (int i = 0; i < 3; ++i) {
    const int e = lane + 64 * i, r = (e * 171) >> 10;
    wo[i] = (unsigned)kchn(j_ks, r) * (F_ * 2) + (96 * j_bn + 48 * j_h) * 2 + (e - 6 * r) * 16;
  }
  const int cl = w < 2 ? (lane & 3) : (lane & 1);
  unsigned vA = (unsigned)(rt * 64 + (lane & 60)) * (D_ * 2) + (ct * 80 + (4 * w + cl) * 8) * 2;
  unsigned tro = qto + (unsigned)(ct * 80 + 32 * w + (lane >> 3)) * 4096 + (lane & 7) * 16;
  lds_char* tq = lds + L_TR + w * TRW + cl * 8 * 144 + ((2 * (lane >> 2)) ^ (4 * cl)) * 4;
  LDS32(lds + L_FL + 4 * w) = 0u;
  asm volatile("" : "+v"(tk), "+v"(kvl), "+v"(kva), "+v"(vva), "+v"(wka), "+v"(wo[0]), "+v"(wo[1]), "+v"(wo[2]), "+v"(vA), "+v"(tro), "+v"(tq));
  unsigned x1o[3], pmo, qoA, qoH, soA, soH, fqo, fso;
  u32x16 ia;
  u32x8 ib;
  auto conv_addr = [&]() __attribute__((always_inline)) {
#pragma unroll
    for (int it = 0; it < 3; ++it) {
      const int k = it < 2 ? 128 + 256 * it + L : L >> 1, r = it < 2 ? rowA : rowH;
      x1o[it] = WS_X1T + (unsigned)((r >> 6) * D_ + k) * 128 + (((r >> 4) ^ (k >> 1)) & 3) * 32 + (r & 15) * 2;
    }
    pmo = WS_MOD + ((lane & 8) ? chH : chA + ((lane >> 2) & 1)) * 32 + ((lane & 3) < 3 ? (lane & 3) : 0) * 20480;
    qoA = WS_QT + (unsigned)chA * 4096 + rowA * 2;
    qoH = WS_QT + (unsigned)chH * 4096 + rowH * 2;
    soA = WS_STAT + w * 4096 + lane * 16;
    soH = WS_STAT + (rowH >> 9) * 4096 + ((rowH >> 2) & 1) * 2048 + ((rowH >> 3) & 63) * 16;
    fqo = WS_QT + WS_CK + ((rowA >> 6) * D_ + chA) * 4;
    fso = WS_QT + WS_CK + ((rowH >> 6) * D_ + chH) * 4;
    const unsigned fo = lbase + (ip & 3) * KCP + 2 * ((ip & 12) + 8 * (fq & 1) + 256 * (fq >> 1));
    const unsigned go = lbase + L_XI + (15 - ip + 8 * (fq >> 1)) * 96 + 16 * (fq & 1);
    const int lcA = side + (w >> 1), lcH = 2 - 2 * side, uA = 2040 - rowA, uH = 2044 - rowH;
    ia[0] = fo + lcA * KCH + (w & 1) * 256;
    ia[1] = go + lcA * XR_CH + (w & 1) * 384;
    ia[2] = fo + lcH * KCH + w * 128;
    ia[3] = go + lcH * XR_CH + w * 192;
    ia[4] = lbase + L_EX + w * 8192 + ip * 128 + (fq ^ (ip & 7)) * 16;
    ia[5] = lbase + L_EX + 32768 + w * 1024 + (lane >> 3) * 128 + ((lane & 7) ^ (lane >> 3)) * 16;
    ia[6] = lbase + L_XI + side * XR_CH + (uA >> 5) * 96 + (uA & 31) * 2;
    ia[7] = lbase + L_XI + lcH * XR_CH + (uH >> 5) * 96 + (uH & 31) * 2;
    ia[8] = WS_FW + xcd * 5120 + (bn * 20 + (lane < 20 ? lane : 19)) * 16;
    ia[9] = WS_FP2 + xcd * 8192 + w * 2048 + lane * 16;
    ia[10] = 0;
    ia[11] = WS_FP2 + (lane & 7) * 8192 + (b * 4 + w) * 8;
    ia[12] = ia[13] = 0;
    ia[14] = qoA + 2048; ia[15] = qoH;
    ib[0] = soA; ib[1] = soH; ib[2] = pmo; ib[3] = ia[4] ^ 64;
    ib[4] = fqo; ib[5] = fso;
    ib[6] = jobW ? WS_FW + (lane & 7) * 5120 + ((j_bn * 20 + j_ks) * 2 + j_h) * 8 : WS_FP1 + 8192 + (b * 4 + w) * 8;
    const int ks3 = (d_ks * 43) >> 7, c6 = 3 * ((lane >> 3) & 1) + d_ks - 3 * ks3;
    ib[7] = L < 240 ? (unsigned)(96 * ks3 + 16 * (c6 >> 1) + 4 * (lane >> 4) + 2 * (c6 & 1) + ((lane >> 2) & 1)) * (D_ * 2) + d_cb * 64 + (lane & 3) * 16 : 0u;
    const int e = tid < 84 ? tid : 83, e7 = (e * 37) >> 8;
    LDS64(lds + e7 * KCP + (e - 7 * e7) * 8) = (u32x2){0u, 0u};
#pragma unroll
    for (int i = 0; i < 3; ++i) LDS128(lds + L_XI + i * XR_CH + 64 * 96 + (tid < 90 ? tid : 89) * 16) = (u32x4){0u, 0u, 0u, 0u};
    asm volatile("" : "+v"(ia), "+v"(ib));
  };
  unsigned mo = WS_MOD + (1920 + 640 * (lane >> 5) + kchn(j_ks, lane & 31)) * 32;
  unsigned cko = (WS_KVT + WS_CK + ((31 - rt) * D_ + ct * 80 + 24 * w + 8 * (lane & 7) + (lane >> 3)) * 4) | (((w < 3 ? 2 : 0) - (lane & 7)) & 0x80000000u);
  unsigned cqo = (WS_QT + WS_CK + (rt * D_ + ct * 80 + 32 * w + 8 * (lane & 7) + (lane >> 3)) * 4) | (((w < 2 ? 3 : 1) - (lane & 7)) & 0x80000000u);
  u32x4 xv[3];
  u32x3 xc;
  unsigned xo = WS_KVT + (unsigned)ch0 * 4096 + 4096 + tid * 16, xco = WS_KVT + WS_CK + ((tid >> 3) * D_ + ch0) * 4, sh = (lane & 7) * 4;
  asm volatile("" : "+v"(mo), "+v"(cko), "+v"(cqo), "+v"(xo), "+v"(xco), "+v"(sh));
  SCHEDB();
  asm volatile("Lpd74eef_1:");
  asm volatile("s_waitcnt vmcnt(5) lgkmcnt(0)\n\ts_barrier" : "+s"(T64), "+s"(fp[0]), "+s"(fp[1]), "+s"(fp[2]), "+s"(dA), "+s"(dH)::"memory");
  asm volatile("Lpd74eef_2:");
  T64 += 1;
  T = (u32x2){(unsigned)T64, (unsigned)(T64 >> 32)};
  const unsigned wslo = kb[6], wshi = kb[7];
  auto x_request = [&]() __attribute__((always_inline)) {
    SLD128(xv[0], xo, -4096); SLD128(xv[1], xo, 0);
    SLD128(xv[2], xo + 4096, 0);
    asm volatile("global_load_dwordx3 %0, %1, %2 sc1" : "=v"(xc) : "v"(xco), "s"(ws) : "memory");
  };
  auto x_finish = [&]() __attribute__((always_inline)) {
    auto x_ok = [&]() __attribute__((always_inline)) -> bool {
      return __all(((cks(xv[0], sh) ^ xc[0] ^ T[0]) | (cks(xv[1], sh) ^ xc[1] ^ T[0]) | (cks(xv[2], sh) ^ xc[2] ^ T[0])) == 0);
    };
    if (__builtin_expect(!x_ok(), 0)) {
#pragma unroll 1
      for (int sp = 0; sp < SPIN_CAP; ++sp) {
        asm volatile("" ::: "memory");
#pragma unroll
        for (int i = 0; i < 3; ++i) { xv[i] = LD128(xo + i * 4096 - 4096); xc[i] = LD32(xco + 4 * i); }
        if (x_ok()) break;
      }
    }
#pragma unroll
    for (int i = 0; i < 3; ++i) LDS128(lds + L_XI + i * XR_CH + (tid >> 2) * 96 + (tid & 3) * 16) = xv[i];
  };
  auto x1_fill = [&]() __attribute__((always_inline)) {
    ST128(x1o[0], FILL); ST128(x1o[1], FILL);
    ST64(x1o[2], ((u32x2){~0u, ~0u}));
  };
#define X_REGS "+v"(xv[0]), "+v"(xv[1]), "+v"(xv[2]), "+v"(xc)
  if (__builtin_expect(w < 3, 1)) {
    u32x4 qr[4];
    KvOps o0_, o1_, o2_;
    KV_READ(o0_, 0);
    KV_READ(o1_, 1);
    filter_loads();
#pragma unroll
    for (int i = 0; i < 4; ++i) GLD128(qr[i], vA, qin, i * D_ * 2);
    asm volatile("s_waitcnt lgkmcnt(7)" : KV_REGS(o0_)::"memory");
    KV_READ(o2_, 2);
  asm volatile("Lpd74eef_3:");
    kv_math(o0_, 0);
    SCHEDB();
  asm volatile("Lpd74eef_4:");
    asm volatile("s_waitcnt lgkmcnt(0)" : KV_REGS(o1_), KV_REGS(o2_)::"memory");
    kv_math(o1_, 1);
    SCHEDB();
    asm volatile("s_waitcnt vmcnt(7)" : KV_REGS(o2_)::"memory");
    LDS32(lds + L_FL + 4 * w) = 1u;
    asm volatile("" : KV_REGS(o2_)::"memory");
    SCHEDB();
  asm volatile("Lpd74eef_5:");
    kv_math(o2_, 2);
    SCHEDB();
  asm volatile("Lpd74eef_6:");
    WAITL();
    {
      unsigned c[3];
#pragma unroll
      for (int i = 0; i < 3; ++i) {
        const u32x4 d = LDS128(kvl + i * 8 * 144);
        ST128(kvs + i * 32768, d);
        c[i] = cks(d, sh);
      }
      ST32(cko, ((lane & 2) ? c[2] : ((lane & 1) ? c[1] : c[0])) ^ T[0]);
    }
    SCHEDB();
  asm volatile("Lpd74eef_7:");
#pragma unroll 1
    for (int sp = 0; sp < 64; ++sp) {
      unsigned f;
      asm volatile("ds_read_b32 %0, %1\n\ts_waitcnt lgkmcnt(0)" : "=v"(f) : "v"(lbase + L_FL + 12) : "memory");
      if (__all(f == 1u)) break;
    }
    {
      unsigned keep;
      asm volatile("s_mov_b32 %0, m0\n\ts_mov_b32 m0, %6\n\ts_nop 0\n\tglobal_load_lds_dwordx4 %1, %4\n\tglobal_load_lds_dwordx4 %1, %5 offset:3072\n\t"
                   "s_add_u32 m0, m0, 1024\n\ts_nop 0\n\tglobal_load_lds_dwordx4 %2, %4\n\tglobal_load_lds_dwordx4 %2, %5 offset:3072\n\t"
                   "s_add_u32 m0, m0, 1024\n\ts_nop 0\n\tglobal_load_lds_dwordx4 %3, %4\n\tglobal_load_lds_dwordx4 %3, %5 offset:3072\n\ts_mov_b32 m0, %0"
                   : "=&s"(keep) : "v"(wo[0]), "v"(wo[1]), "v"(wo[2]), "s"(wg), "s"(wu - 1536), "s"(lbase + L_TR + w * TRW + 4608) : "memory", "scc");
    }
  asm volatile("Lpd74eef_8:");
    asm volatile("s_waitcnt vmcnt(10)" : FILT_REGS, "+v"(qr[0]), "+v"(qr[1]), "+v"(qr[2]), "+v"(qr[3])::"memory");
  asm volatile("Lpd74eef_9:");
    filter_copies();
  asm volatile("Lpd74eef_10:");
#pragma unroll
    for (int j = 0; j < 4; ++j) {
      LDS64(tq + (2 * j) * 144) = (u32x2){__builtin_amdgcn_perm(qr[1][j], qr[0][j], 0x05040100u), __builtin_amdgcn_perm(qr[3][j], qr[2][j], 0x05040100u)};
      LDS64(tq + (2 * j + 1) * 144) = (u32x2){__builtin_amdgcn_perm(qr[1][j], qr[0][j], 0x07060302u), __builtin_amdgcn_perm(qr[3][j], qr[2][j], 0x07060302u)};
    }
    WAITL();
    unsigned qc;
    {
      unsigned c[4];
#pragma unroll
      for (int i = 0; i < 4; ++i) {
        qr[i] = LDS128(lds + L_TR + w * TRW + ((8 * i + (lane >> 3)) * 36 + 4 * ((lane & 7) ^ i)) * 4);
        c[i] = cks(qr[i], sh);
      }
      qc = ((lane & 2) ? ((lane & 1) ? c[3] : c[2]) : ((lane & 1) ? c[1] : c[0])) ^ T[0];
      asm volatile("" : "+v"(qc));
    }
    SCHEDB();
  asm volatile("Lpd74eef_11:");
    u32x4 m0_, m1_;
    SLD128(m0_, mo, 0);
    SLD128(m1_, mo, 16);
    x_request();
    SCHEDB();
    unsigned wpo = WS_WP + (unsigned)((j_bn * 4 + 2 * j_h) * 20 + j_ks) * 3072 + lane * 16;
    unsigned pso = ip < 12 ? WS_PART + (unsigned)j_ks * 24576 + (ip >= 6 ? 12288 - 384 : 0) + (j_bn * 192 + j_h * 96 + ip * 16 + fq * 4) * 4 : 0x80000000u;
    if (!jobW) wpo = pso = 0x80000000u;
    conv_addr();
    asm volatile("" : "+v"(wpo), "+v"(pso));
    SCHEDB();
  asm volatile("Lpd74eef_12:");
    u32x4 raw[6];
    {
      u32x2 r0[6], r1[6];
      const unsigned ta = lbase + L_TR + w * TRW + 4608 + (4 * fq + q4) * 96 + p4 * 8;
      WAITV(6);
#pragma unroll
      for (int c = 0; c < 6; ++c) { DSRTR(r0[c], ta, (c & 1) * 3072 + (c >> 1) * 32); DSRTR(r1[c], ta, (c & 1) * 3072 + 16 * 96 + (c >> 1) * 32); }
      asm volatile("s_waitcnt lgkmcnt(0)" : "+v"(r0[0]), "+v"(r0[1]), "+v"(r0[2]), "+v"(r0[3]), "+v"(r0[4]), "+v"(r0[5]),
                   "+v"(r1[0]), "+v"(r1[1]), "+v"(r1[2]), "+v"(r1[3]), "+v"(r1[4]), "+v"(r1[5])::"memory");
#pragma unroll
      for (int c = 0; c < 3; ++c) {
        unsigned x[4], y[4];
#define QP(d, s0, s1, p) "v_cndmask_b32_dpp %" #d ", %" #s0 ", %" #s1 ", vcc quad_perm:" p " row_mask:0xf bank_mask:0xf\n\t"
        asm volatile("s_mov_b32 vcc_lo, 0x33333333\n\ts_mov_b32 vcc_hi, 0x33333333\n\t"
                     QP(0, 12, 8, "[0,1,0,1]") QP(1, 13, 9, "[0,1,0,1]") QP(2, 14, 10, "[0,1,0,1]") QP(3, 15, 11, "[0,1,0,1]")
                     "s_not_b64 vcc, vcc\n\t"
                     QP(4, 8, 12, "[2,3,2,3]") QP(5, 9, 13, "[2,3,2,3]") QP(6, 10, 14, "[2,3,2,3]") QP(7, 11, 15, "[2,3,2,3]")
                     : "=&v"(x[0]), "=&v"(x[1]), "=&v"(x[2]), "=&v"(x[3]), "=&v"(y[0]), "=&v"(y[1]), "=&v"(y[2]), "=&v"(y[3])
                     : "v"(r0[2 * c][0]), "v"(r0[2 * c][1]), "v"(r1[2 * c][0]), "v"(r1[2 * c][1]), "v"(r0[2 * c + 1][0]), "v"(r0[2 * c + 1][1]),
                       "v"(r1[2 * c + 1][0]), "v"(r1[2 * c + 1][1])
                     : "vcc", "scc");
        raw[2 * c] = (u32x4){x[0], x[1], x[2], x[3]};
        raw[2 * c + 1] = (u32x4){y[0], y[1], y[2], y[3]};
      }
      asm volatile("" : "+v"(raw[0]), "+v"(raw[1]), "+v"(raw[2]), "+v"(raw[3]), "+v"(raw[4]), "+v"(raw[5]));
    }
  asm volatile("Lpd74eef_13:");
    asm volatile("s_waitcnt vmcnt(0)" : "+v"(m0_), "+v"(m1_), X_REGS::"memory");
  asm volatile("Lpd74eef_14:");
    x_finish();
    if (__builtin_expect(jobW && !__all(modx(m0_, m1_, T[0]) == 0), 0)) {
#pragma unroll 1
      for (int sp = 0; sp < SPIN_CAP; ++sp) {
        asm volatile("" ::: "memory");
        m0_ = LD128(mo);
        m1_ = LD128(mo + 16);
        if (__all(modx(m0_, m1_, T[0]) == 0)) break;
      }
      asm volatile("" : "+v"(m0_), "+v"(m1_));
    }
  asm volatile("Lpd74eef_15:");
#pragma unroll
    for (int i = 0; i < 4; ++i) ST128((w < 2 || i < 2) ? tro + i * 32768 : 0x80000000u, qr[i]);
    ST32(cqo, qc);
    x1_fill();
    SCHEDB();
  asm volatile("Lpd74eef_16:");
    {
      const unsigned scb = lbase + L_TR + w * TRW + 10752;
      *(__attribute__((address_space(3))) u16*)(lds + L_TR + w * TRW + 10752 + lane * 2) = (u16)pk2(modv(m0_, m1_) + (lane < 32 ? 0.f : 1.f), 0.f);
      WAITL();
      u32x2 sl, sh;
      unsigned lo, hi;
      asm volatile("ds_read_b64 %0, %4\n\tds_read_b64 %1, %4 offset:32\n\tds_read_b32 %2, %5 offset:64\n\tds_read_b32 %3, %5 offset:96\n\ts_waitcnt lgkmcnt(0)"
                   : "=&v"(sl), "=&v"(sh), "=&v"(lo), "=&v"(hi) : "v"(scb + fq * 8), "v"(scb + (ip >> 1) * 4) : "memory");
      const unsigned msk = fq == q4 ? ((ip & 1) ? 0xFFFF0000u : 0xFFFFu) : 0u, mk0 = (ip & 2) ? 0u : msk, mk1 = (ip & 2) ? msk : 0u;
      u32x4 dg0 = {lo & mk0, lo & mk1, 0u, 0u}, dg1 = {0u, 0u, hi & mk0, hi & mk1};
      const u32x4 sbf = cat2(sl, sh);
      asm volatile("s_nop 1" : "+v"(dg0), "+v"(dg1));
      f32x4 a0[6], a1[6], ps;
#pragma unroll
      for (int c = 0; c < 6; ++c) { MFMA0(a0[c], dg0, raw[c]); MFMA0(a1[c], dg1, raw[c]); }
      u32x4 sel[6], wp[6];
#pragma unroll
      for (int c = 0; c < 6; ++c) {
        const bool m = ip == 6 + c;
        sel[c] = (u32x4){m ? sbf[0] : 0u, m ? sbf[1] : 0u, m ? sbf[2] : 0u, m ? sbf[3] : 0u};
      }
      asm volatile("s_nop 1" : "+v"(sel[0]), "+v"(sel[1]), "+v"(sel[2]), "+v"(sel[3]), "+v"(sel[4]), "+v"(sel[5]));
      MFMA0(ps, raw[0], sel[0]);
#pragma unroll
      for (int c = 1; c < 6; ++c) MFMAC(ps, raw[c], sel[c]);
      asm volatile("" : "+v"(a0[0]), "+v"(a0[1]), "+v"(a0[2]), "+v"(a0[3]), "+v"(a0[4]), "+v"(a0[5]), "+v"(a1[0]), "+v"(a1[1]),
                   "+v"(a1[2]), "+v"(a1[3]), "+v"(a1[4]), "+v"(a1[5]));
#pragma unroll
      for (int c = 0; c < 6; ++c) {
        wp[c] = (u32x4){pk2(a0[c][0], a0[c][1]), pk2(a0[c][2], a0[c][3]), pk2(a1[c][0], a1[c][1]), pk2(a1[c][2], a1[c][3])};
        const unsigned o1 = ip == c ? 0x3F803F80u : 0u;
        sel[c] = (u32x4){o1, o1, o1, o1};
      }
      asm volatile("s_nop 1" : "+v"(wp[0]), "+v"(wp[1]), "+v"(wp[2]), "+v"(wp[3]), "+v"(wp[4]), "+v"(wp[5]), "+v"(sel[0]), "+v"(sel[1]),
                   "+v"(sel[2]), "+v"(sel[3]), "+v"(sel[4]), "+v"(sel[5]));
#pragma unroll
      for (int c = 0; c < 6; ++c) MFMAC(ps, wp[c], sel[c]);
#pragma unroll
      for (int c = 0; c < 6; ++c) ST128(wpo + (c / 3) * 61440 + (c % 3) * 1024, wp[c]);
      asm volatile("s_nop 7" : "+v"(ps));
      ST128(pso, __builtin_bit_cast(u32x4, ps));
      SCHEDB();
    }
  asm volatile("Lpd74eef_17:");
  } else {
  asm volatile("Lpd74eef_18:");
    u32x4 sx[10];
    KvOps o0_;
    KV_READ(o0_, 0);
    filter_loads();
#pragma unroll
    for (int i = 0; i < 10; ++i) GLD128(sx[i], (lane >> 3) * 1280 + (lane & 7) * 16, qin + (size_t)srow * D_, i * 128);
    const u32x2 cv = LDS64(lds + L_CR + (lane < 40 ? lane : 39) * 8);
    asm volatile("s_waitcnt lgkmcnt(0)" : KV_REGS(o0_)::"memory");
    kv_math(o0_, 0);
    WAITL();
    u32x4 kvd = LDS128(kvl);
    unsigned kvc = cks(kvd, sh) ^ T[0];
    asm volatile("" : "+v"(kvd), "+v"(kvc));
    SCHEDB();
    {
      const f32x2 c0 = up2(cv[0]), c1 = up2(cv[1]);
      LDS64(lds + L_GS + (lane < 40 ? lane : 39) * 8) =
          (u32x2){pk2(c0[0] / (1.0f + __expf(-c0[0])), c0[1] / (1.0f + __expf(-c0[1]))),
                  pk2(c1[0] / (1.0f + __expf(-c1[0])), c1[1] / (1.0f + __expf(-c1[1])))};
    }
    WAITL();
    SCHEDB();
  asm volatile("Lpd74eef_19:");
    {
      const int r8 = 4 * fq + q4;
      const unsigned ta = lbase + L_GW + r8 * 128 + p4 * 8, sa = lbase + L_GS + fq * 8;
      u32x2 a[10], r[4][10];
#pragma unroll
      for (int i = 0; i < 10; ++i) asm volatile("ds_read_b64 %0, %1 offset:%2" : "=v"(a[i]) : "v"(sa), "i"(i * 32));
      unsigned mdo = idx < 30 ? WS_MOD + ((xcd * 30 + idx) >> 2) * 2048 + ((xcd * 30 + idx) & 3) * 8 + lane * 32 : 0x80000000u;
      asm volatile("" : "+v"(mdo));
      SCHEDB();
      WAITV(13);
#pragma unroll 1
      for (int sp = 0; sp < SPIN_CAP; ++sp) {
        unsigned f;
        asm volatile("ds_read_b32 %0, %1\n\ts_waitcnt lgkmcnt(0)" : "=v"(f) : "v"(lbase + L_FL + ((lane & 3) < 3 ? 4 * (lane & 3) : 0)) : "memory");
        if (__all(f == 1u)) break;
      }
      SCHEDB();
#pragma unroll
      for (int n = 0; n < 4; ++n) {
        const unsigned tb = ta + ((n ^ ((r8 >> 1) & 3)) * 32);
#pragma unroll
        for (int i = 0; i < 10; ++i) DSRTR(r[n][i], tb, i * 2048);
      }
      asm volatile("s_waitcnt lgkmcnt(0)"
                   : "+v"(a[0]), "+v"(a[1]), "+v"(a[2]), "+v"(a[3]), "+v"(a[4]), "+v"(a[5]), "+v"(a[6]), "+v"(a[7]), "+v"(a[8]), "+v"(a[9])::"memory");
      f32x4 acc[4];
#pragma unroll
      for (int n = 0; n < 4; ++n) {
        asm volatile("" : "+v"(r[n][0]), "+v"(r[n][1]), "+v"(r[n][2]), "+v"(r[n][3]), "+v"(r[n][4]), "+v"(r[n][5]), "+v"(r[n][6]),
                     "+v"(r[n][7]), "+v"(r[n][8]), "+v"(r[n][9])::"memory");
        MFMA0(acc[n], cat2(a[0], a[1]), cat2(r[n][0], r[n][1]));
#pragma unroll
        for (int i = 1; i < 5; ++i) MFMAC(acc[n], cat2(a[2 * i], a[2 * i + 1]), cat2(r[n][2 * i], r[n][2 * i + 1]));
      }
      asm volatile("s_nop 7" : "+v"(acc[0]), "+v"(acc[1]), "+v"(acc[2]), "+v"(acc[3]));
      const float o01 = (fq & 1) ? acc[1][0] : acc[0][0], o23 = (fq & 1) ? acc[3][0] : acc[2][0];
      ST64(mdo, ((u32x2){__float_as_uint((fq & 2) ? o23 : o01), T[0]}));
      ST128(kvs, kvd);
      ST32(cko, kvc);
    }
    SCHEDB();
    LDS32(lds + L_FL + 12) = 1u;
  asm volatile("Lpd74eef_20:");
    asm volatile("s_waitcnt vmcnt(13)" : FILT_REGS::"memory");
    filter_copies();
  asm volatile("Lpd74eef_21:");
    u32x4 stv;
    asm volatile("s_waitcnt vmcnt(3)"
                 : "+v"(sx[0]), "+v"(sx[1]), "+v"(sx[2]), "+v"(sx[3]), "+v"(sx[4]), "+v"(sx[5]), "+v"(sx[6]), "+v"(sx[7]),
                   "+v"(sx[8]), "+v"(sx[9])::"memory");
    {
      f32x2 s2 = {0.f, 0.f};
#pragma unroll
      for (int i = 0; i < 10; ++i)
#pragma unroll
        for (int j = 0; j < 4; ++j) s2 += up2(sx[i][j]);
      float s = s2[0] + s2[1];
      DPP_ADD(s, 0xB1); DPP_ADD(s, 0x4E); DPP_ADD(s, 0x141);
      const float mean = s * (1.0f / D_);
      f32x2 v2 = {0.f, 0.f};
#pragma unroll
      for (int i = 0; i < 10; ++i)
#pragma unroll
        for (int j = 0; j < 4; ++j) { const f32x2 d = up2(sx[i][j]) - mean; v2 += d * d; }
      float vs = v2[0] + v2[1];
      DPP_ADD(vs, 0xB1); DPP_ADD(vs, 0x4E); DPP_ADD(vs, 0x141);
      const float var = vs * (1.0f / D_);
      const float rsd = 1.0f / rb1(sqrtf(rb1(rb1(var) + 1e-5f)));
      const float m1 = __builtin_bit_cast(float, __builtin_amdgcn_update_dpp(0, __builtin_bit_cast(int, mean), 0x108, 0xf, 0xf, true));
      const float r1 = __builtin_bit_cast(float, __builtin_amdgcn_update_dpp(0, __builtin_bit_cast(int, rsd), 0x108, 0xf, 0xf, true));
      stv = (u32x4){__float_as_uint(rsd), __float_as_uint(r1), pk2(mean, m1), T[0]};
      asm volatile("" : "+v"(stv));
    }
    SCHEDB();
  asm volatile("Lpd74eef_22:");
    x_request();
    SCHEDB();
    conv_addr();
    SCHEDB();
    asm volatile("s_waitcnt vmcnt(0)" : X_REGS::"memory");
    x_finish();
    ST128((lane & 15) == 0 ? WS_STAT + (srow >> 9) * 4096 + (lane >> 4) * 1024 + ((srow >> 3) & 63) * 16 : 0x80000000u, stv);
    x1_fill();
#pragma unroll
    for (int i = 0; i < 7; ++i) ST32(0x80000000u + 64 * i, 0u);
    asm volatile("s_mov_b64 exec, 0\n\t" "Lpd74eef_ret1:\n\ts_mov_b64 exec, -1" :: "s"(w) : "scc");
    SCHEDB();
  }
  unsigned Dw[3];
  {
    const u64 d2 = (((u64)dA[1] << 32) | dA[0]) >> (16 * (chA < 638 ? chA & 1 : 2));
    Dw[0] = (unsigned)d2 << 16; Dw[1] = (unsigned)d2 & 0xFFFF0000u; Dw[2] = (chH & 1) ? dH & 0xFFFF0000u : dH << 16;
  }
  asm volatile("Lpd74eef_23:");
  const size_t wpb = (size_t)ws + WS_WP + (size_t)(bn * 4 + w) * 61440;
  const unsigned wplo = __builtin_amdgcn_readfirstlane((unsigned)wpb), wphi = __builtin_amdgcn_readfirstlane((unsigned)(wpb >> 32));
  f32x16 hp;
  u32x32 oa;
  u32x16 ob;
  u32x4 oc, od, f0, f1, f2;
  ia[12] = T[0]; ia[13] = T[1];
  BAR();
  asm volatile("Lpd74eef_24:");
  asm volatile(
      "s_cmp_lg_u32 %[side], 0\n\t"
      "s_cbranch_scc1 L_cv_b%=\n\t"
      "ds_read_b128 v[160:163], v43 offset:4608\n\t"
      "ds_read_b64 v[112:113], v42 offset:0\n\t"
      "ds_read_b64 v[114:115], v42 offset:8\n\t"
      "ds_read_b64 v[148:149], v42 offset:3072\n\t"
      "ds_read_b64 v[150:151], v42 offset:3080\n\t"
      "ds_read_b128 v[172:175], v43 offset:0\n\t"
      "ds_read_b128 v[168:171], v43 offset:1536\n\t"
      "ds_read_b64 v[124:125], v42 offset:1024\n\t"
      "ds_read_b64 v[126:127], v42 offset:1032\n\t"
      "ds_read_b128 v[164:167], v43 offset:3072\n\t"
      "ds_read_b64 v[136:137], v42 offset:2048\n\t"
      "ds_read_b64 v[138:139], v42 offset:2056\n\t"
      "ds_read_b64 v[116:117], v42 offset:32\n\t"
      "ds_read_b64 v[118:119], v42 offset:40\n\t"
      "ds_read_b64 v[128:129], v42 offset:1056\n\t"
      "ds_read_b64 v[130:131], v42 offset:1064\n\t"
      "ds_read_b64 v[140:141], v42 offset:2080\n\t"
      "ds_read_b64 v[142:143], v42 offset:2088\n\t"
      "ds_read_b64 v[152:153], v42 offset:3104\n\t"
      "ds_read_b64 v[154:155], v42 offset:3112\n\t"
      "s_waitcnt lgkmcnt(8)\n\t"
      "v_mfma_f32_16x16x32_bf16 v[96:99], v[112:115], v[160:163], 0\n\t"
      "ds_read_b128 v[176:179], v43 offset:4640\n\t"
      "ds_read_b64 v[120:121], v42 offset:64\n\t"
      "v_mfma_f32_16x16x32_bf16 v[104:107], v[148:151], v[160:163], 0\n\t"
      "ds_read_b64 v[122:123], v42 offset:72\n\t"
      "v_mfma_f32_16x16x32_bf16 v[104:107], v[112:115], v[172:175], v[104:107]\n\t"
      "ds_read_b128 v[180:183], v43 offset:3104\n\t"
      "ds_read_b64 v[132:133], v42 offset:1088\n\t"
      "v_mfma_f32_16x16x32_bf16 v[104:107], v[124:127], v[168:171], v[104:107]\n\t"
      "ds_read_b64 v[134:135], v42 offset:1096\n\t"
      "v_mfma_f32_16x16x32_bf16 v[104:107], v[136:139], v[164:167], v[104:107]\n\t"
      "ds_read_b128 v[184:187], v43 offset:1568\n\t"
      "ds_read_b64 v[144:145], v42 offset:2112\n\t"
      "s_waitcnt lgkmcnt(8)\n\t"
      "v_mfma_f32_16x16x32_bf16 v[100:103], v[116:119], v[160:163], 0\n\t"
      "ds_read_b64 v[146:147], v42 offset:2120\n\t"
      "v_mfma_f32_16x16x32_bf16 v[108:111], v[152:155], v[160:163], 0\n\t"
      "ds_read_b128 v[188:191], v43 offset:32\n\t"
      "ds_read_b64 v[156:157], v42 offset:3136\n\t"
      "v_mfma_f32_16x16x32_bf16 v[108:111], v[116:119], v[172:175], v[108:111]\n\t"
      "ds_read_b64 v[158:159], v42 offset:3144\n\t"
      "v_mfma_f32_16x16x32_bf16 v[108:111], v[128:131], v[168:171], v[108:111]\n\t"
      "v_mfma_f32_16x16x32_bf16 v[108:111], v[140:143], v[164:167], v[108:111]\n\t"
      "s_waitcnt lgkmcnt(0)\n\t"
      "v_mfma_f32_16x16x32_bf16 v[96:99], v[116:119], v[176:179], v[96:99]\n\t"
      "ds_read_b128 v[160:163], v43 offset:4704\n\t"
      "ds_read_b64 v[112:113], v42 offset:96\n\t"
      "v_mfma_f32_16x16x32_bf16 v[104:107], v[152:155], v[176:179], v[104:107]\n\t"
      "ds_read_b64 v[114:115], v42 offset:104\n\t"
      "v_mfma_f32_16x16x32_bf16 v[104:107], v[116:119], v[188:191], v[104:107]\n\t"
      "ds_read_b128 v[164:167], v43 offset:3168\n\t"
      "ds_read_b64 v[124:125], v42 offset:1120\n\t"
      "v_mfma_f32_16x16x32_bf16 v[104:107], v[128:131], v[184:187], v[104:107]\n\t"
      "ds_read_b64 v[126:127], v42 offset:1128\n\t"
      "v_mfma_f32_16x16x32_bf16 v[104:107], v[140:143], v[180:183], v[104:107]\n\t"
      "ds_read_b128 v[168:171], v43 offset:1632\n\t"
      "ds_read_b64 v[136:137], v42 offset:2144\n\t"
      "v_mfma_f32_16x16x32_bf16 v[100:103], v[120:123], v[176:179], v[100:103]\n\t"
      "ds_read_b64 v[138:139], v42 offset:2152\n\t"
      "v_mfma_f32_16x16x32_bf16 v[108:111], v[156:159], v[176:179], v[108:111]\n\t"
      "ds_read_b128 v[172:175], v43 offset:96\n\t"
      "ds_read_b64 v[148:149], v42 offset:3168\n\t"
      "v_mfma_f32_16x16x32_bf16 v[108:111], v[120:123], v[188:191], v[108:111]\n\t"
      "ds_read_b64 v[150:151], v42 offset:3176\n\t"
      "v_mfma_f32_16x16x32_bf16 v[108:111], v[132:135], v[184:187], v[108:111]\n\t"
      "v_mfma_f32_16x16x32_bf16 v[108:111], v[144:147], v[180:183], v[108:111]\n\t"
      "s_waitcnt lgkmcnt(0)\n\t"
      "v_mfma_f32_16x16x32_bf16 v[96:99], v[120:123], v[160:163], v[96:99]\n\t"
      "ds_read_b128 v[176:179], v43 offset:4736\n\t"
      "ds_read_b64 v[116:117], v42 offset:128\n\t"
      "v_mfma_f32_16x16x32_bf16 v[104:107], v[156:159], v[160:163], v[104:107]\n\t"
      "ds_read_b64 v[118:119], v42 offset:136\n\t"
      "v_mfma_f32_16x16x32_bf16 v[104:107], v[120:123], v[172:175], v[104:107]\n\t"
      "ds_read_b128 v[180:183], v43 offset:3200\n\t"
      "ds_read_b64 v[128:129], v42 offset:1152\n\t"
      "v_mfma_f32_16x16x32_bf16 v[104:107], v[132:135], v[168:171], v[104:107]\n\t"
      "ds_read_b64 v[130:131], v42 offset:1160\n\t"
      "v_mfma_f32_16x16x32_bf16 v[104:107], v[144:147], v[164:167], v[104:107]\n\t"
      "ds_read_b128 v[184:187], v43 offset:1664\n\t"
      "ds_read_b64 v[140:141], v42 offset:2176\n\t"
      "v_mfma_f32_16x16x32_bf16 v[100:103], v[112:115], v[160:163], v[100:103]\n\t"
      "ds_read_b64 v[142:143], v42 offset:2184\n\t"
      "v_mfma_f32_16x16x32_bf16 v[108:111], v[148:151], v[160:163], v[108:111]\n\t"
      "ds_read_b128 v[188:191], v43 offset:128\n\t"
      "ds_read_b64 v[152:153], v42 offset:3200\n\t"
      "v_mfma_f32_16x16x32_bf16 v[108:111], v[112:115], v[172:175], v[108:111]\n\t"
      "ds_read_b64 v[154:155], v42 offset:3208\n\t"
      "v_mfma_f32_16x16x32_bf16 v[108:111], v[124:127], v[168:171], v[108:111]\n\t"
      "v_mfma_f32_16x16x32_bf16 v[108:111], v[136:139], v[164:167], v[108:111]\n\t"
      "s_waitcnt lgkmcnt(0)\n\t"
      "v_mfma_f32_16x16x32_bf16 v[96:99], v[112:115], v[176:179], v[96:99]\n\t"
      "ds_read_b128 v[160:163], v41 offset:4608\n\t"
      "ds_read_b64 v[120:121], v40 offset:0\n\t"
      "v_mfma_f32_16x16x32_bf16 v[104:107], v[148:151], v[176:179], v[104:107]\n\t"
      "ds_read_b64 v[122:123], v40 offset:8\n\t"
      "ds_read_b128 v[164:167], v41 offset:3072\n\t"
      "v_mfma_f32_16x16x32_bf16 v[104:107], v[112:115], v[188:191], v[104:107]\n\t"
      "ds_read_b64 v[132:133], v40 offset:1024\n\t"
      "ds_read_b64 v[134:135], v40 offset:1032\n\t"
      "v_mfma_f32_16x16x32_bf16 v[104:107], v[124:127], v[184:187], v[104:107]\n\t"
      "ds_read_b128 v[168:171], v41 offset:1536\n\t"
      "ds_read_b64 v[144:145], v40 offset:2048\n\t"
      "v_mfma_f32_16x16x32_bf16 v[104:107], v[136:139], v[180:183], v[104:107]\n\t"
      "ds_read_b64 v[146:147], v40 offset:2056\n\t"
      "ds_read_b128 v[172:175], v41 offset:0\n\t"
      "v_mfma_f32_16x16x32_bf16 v[100:103], v[116:119], v[176:179], v[100:103]\n\t"
      "ds_read_b64 v[156:157], v40 offset:3072\n\t"
      "ds_read_b64 v[158:159], v40 offset:3080\n\t"
      "v_mfma_f32_16x16x32_bf16 v[108:111], v[152:155], v[176:179], v[108:111]\n\t"
      "ds_read_b64 v[112:113], v40 offset:32\n\t"
      "ds_read_b64 v[114:115], v40 offset:40\n\t"
      "v_mfma_f32_16x16x32_bf16 v[108:111], v[116:119], v[188:191], v[108:111]\n\t"
      "ds_read_b64 v[124:125], v40 offset:1056\n\t"
      "ds_read_b64 v[126:127], v40 offset:1064\n\t"
      "v_mfma_f32_16x16x32_bf16 v[108:111], v[128:131], v[184:187], v[108:111]\n\t"
      "ds_read_b64 v[136:137], v40 offset:2080\n\t"
      "ds_read_b64 v[138:139], v40 offset:2088\n\t"
      "v_mfma_f32_16x16x32_bf16 v[108:111], v[140:143], v[180:183], v[108:111]\n\t"
      "ds_read_b64 v[148:149], v40 offset:3104\n\t"
      "ds_read_b64 v[150:151], v40 offset:3112\n\t"
      "s_branch L_cv_e%=\n\t"
      "L_cv_b%=:\n\t"
      "ds_read_b128 v[164:167], v43 offset:3072\n\t"
      "ds_read_b64 v[112:113], v42 offset:0\n\t"
      "ds_read_b64 v[114:115], v42 offset:8\n\t"
      "ds_read_b64 v[124:125], v42 offset:1024\n\t"
      "ds_read_b64 v[126:127], v42 offset:1032\n\t"
      "ds_read_b128 v[160:163], v43 offset:4608\n\t"
      "ds_read_b64 v[136:137], v42 offset:2048\n\t"
      "ds_read_b64 v[138:139], v42 offset:2056\n\t"
      "ds_read_b128 v[168:171], v43 offset:1536\n\t"
      "ds_read_b64 v[116:117], v42 offset:32\n\t"
      "ds_read_b64 v[118:119], v42 offset:40\n\t"
      "ds_read_b64 v[128:129], v42 offset:1056\n\t"
      "ds_read_b64 v[130:131], v42 offset:1064\n\t"
      "ds_read_b64 v[140:141], v42 offset:2080\n\t"
      "ds_read_b64 v[142:143], v42 offset:2088\n\t"
      "s_waitcnt lgkmcnt(6)\n\t"
      "v_mfma_f32_16x16x32_bf16 v[96:99], v[112:115], v[164:167], 0\n\t"
      "ds_read_b128 v[176:179], v43 offset:4640\n\t"
      "ds_read_b64 v[120:121], v42 offset:64\n\t"
      "v_mfma_f32_16x16x32_bf16 v[104:107], v[124:127], v[164:167], 0\n\t"
      "ds_read_b64 v[122:123], v42 offset:72\n\t"
      "v_mfma_f32_16x16x32_bf16 v[96:99], v[124:127], v[160:163], v[96:99]\n\t"
      "ds_read_b128 v[180:183], v43 offset:3104\n\t"
      "v_mfma_f32_16x16x32_bf16 v[104:107], v[136:139], v[160:163], v[104:107]\n\t"
      "ds_read_b64 v[132:133], v42 offset:1088\n\t"
      "v_mfma_f32_16x16x32_bf16 v[104:107], v[112:115], v[168:171], v[104:107]\n\t"
      "ds_read_b64 v[134:135], v42 offset:1096\n\t"
      "s_waitcnt lgkmcnt(6)\n\t"
      "v_mfma_f32_16x16x32_bf16 v[100:103], v[116:119], v[164:167], 0\n\t"
      "ds_read_b128 v[184:187], v43 offset:1568\n\t"
      "v_mfma_f32_16x16x32_bf16 v[108:111], v[128:131], v[164:167], 0\n\t"
      "ds_read_b64 v[144:145], v42 offset:2112\n\t"
      "v_mfma_f32_16x16x32_bf16 v[100:103], v[128:131], v[160:163], v[100:103]\n\t"
      "ds_read_b64 v[146:147], v42 offset:2120\n\t"
      "v_mfma_f32_16x16x32_bf16 v[108:111], v[140:143], v[160:163], v[108:111]\n\t"
      "v_mfma_f32_16x16x32_bf16 v[108:111], v[116:119], v[168:171], v[108:111]\n\t"
      "s_waitcnt lgkmcnt(0)\n\t"
      "v_mfma_f32_16x16x32_bf16 v[96:99], v[116:119], v[180:183], v[96:99]\n\t"
      "ds_read_b128 v[160:163], v43 offset:4704\n\t"
      "ds_read_b64 v[112:113], v42 offset:96\n\t"
      "v_mfma_f32_16x16x32_bf16 v[104:107], v[128:131], v[180:183], v[104:107]\n\t"
      "ds_read_b64 v[114:115], v42 offset:104\n\t"
      "v_mfma_f32_16x16x32_bf16 v[96:99], v[128:131], v[176:179], v[96:99]\n\t"
      "ds_read_b128 v[164:167], v43 offset:3168\n\t"
      "v_mfma_f32_16x16x32_bf16 v[104:107], v[140:143], v[176:179], v[104:107]\n\t"
      "ds_read_b64 v[124:125], v42 offset:1120\n\t"
      "v_mfma_f32_16x16x32_bf16 v[104:107], v[116:119], v[184:187], v[104:107]\n\t"
      "ds_read_b64 v[126:127], v42 offset:1128\n\t"
      "v_mfma_f32_16x16x32_bf16 v[100:103], v[120:123], v[180:183], v[100:103]\n\t"
      "ds_read_b128 v[168:171], v43 offset:1632\n\t"
      "v_mfma_f32_16x16x32_bf16 v[108:111], v[132:135], v[180:183], v[108:111]\n\t"
      "ds_read_b64 v[136:137], v42 offset:2144\n\t"
      "v_mfma_f32_16x16x32_bf16 v[100:103], v[132:135], v[176:179], v[100:103]\n\t"
      "ds_read_b64 v[138:139], v42 offset:2152\n\t"
      "v_mfma_f32_16x16x32_bf16 v[108:111], v[144:147], v[176:179], v[108:111]\n\t"
      "v_mfma_f32_16x16x32_bf16 v[108:111], v[120:123], v[184:187], v[108:111]\n\t"
      "s_waitcnt lgkmcnt(0)\n\t"
      "v_mfma_f32_16x16x32_bf16 v[96:99], v[120:123], v[164:167], v[96:99]\n\t"
      "ds_read_b128 v[176:179], v43 offset:4736\n\t"
      "ds_read_b64 v[116:117], v42 offset:128\n\t"
      "v_mfma_f32_16x16x32_bf16 v[104:107], v[132:135], v[164:167], v[104:107]\n\t"
      "ds_read_b64 v[118:119], v42 offset:136\n\t"
      "v_mfma_f32_16x16x32_bf16 v[96:99], v[132:135], v[160:163], v[96:99]\n\t"
      "ds_read_b128 v[180:183], v43 offset:3200\n\t"
      "v_mfma_f32_16x16x32_bf16 v[104:107], v[144:147], v[160:163], v[104:107]\n\t"
      "ds_read_b64 v[128:129], v42 offset:1152\n\t"
      "v_mfma_f32_16x16x32_bf16 v[104:107], v[120:123], v[168:171], v[104:107]\n\t"
      "ds_read_b64 v[130:131], v42 offset:1160\n\t"
      "v_mfma_f32_16x16x32_bf16 v[100:103], v[112:115], v[164:167], v[100:103]\n\t"
      "ds_read_b128 v[184:187], v43 offset:1664\n\t"
      "v_mfma_f32_16x16x32_bf16 v[108:111], v[124:127], v[164:167], v[108:111]\n\t"
      "ds_read_b64 v[140:141], v42 offset:2176\n\t"
      "v_mfma_f32_16x16x32_bf16 v[100:103], v[124:127], v[160:163], v[100:103]\n\t"
      "ds_read_b64 v[142:143], v42 offset:2184\n\t"
      "v_mfma_f32_16x16x32_bf16 v[108:111], v[136:139], v[160:163], v[108:111]\n\t"
      "v_mfma_f32_16x16x32_bf16 v[108:111], v[112:115], v[168:171], v[108:111]\n\t"
      "s_waitcnt lgkmcnt(0)\n\t"
      "v_mfma_f32_16x16x32_bf16 v[96:99], v[112:115], v[180:183], v[96:99]\n\t"
      "ds_read_b128 v[160:163], v41 offset:4608\n\t"
      "ds_read_b64 v[120:121], v40 offset:0\n\t"
      "v_mfma_f32_16x16x32_bf16 v[104:107], v[124:127], v[180:183], v[104:107]\n\t"
      "ds_read_b64 v[122:123], v40 offset:8\n\t"
      "ds_read_b128 v[164:167], v41 offset:3072\n\t"
      "v_mfma_f32_16x16x32_bf16 v[96:99], v[124:127], v[176:179], v[96:99]\n\t"
      "ds_read_b64 v[132:133], v40 offset:1024\n\t"
      "ds_read_b64 v[134:135], v40 offset:1032\n\t"
      "v_mfma_f32_16x16x32_bf16 v[104:107], v[136:139], v[176:179], v[104:107]\n\t"
      "ds_read_b128 v[168:171], v41 offset:1536\n\t"
      "ds_read_b64 v[144:145], v40 offset:2048\n\t"
      "v_mfma_f32_16x16x32_bf16 v[104:107], v[112:115], v[184:187], v[104:107]\n\t"
      "ds_read_b64 v[146:147], v40 offset:2056\n\t"
      "ds_read_b128 v[172:175], v41 offset:0\n\t"
      "v_mfma_f32_16x16x32_bf16 v[100:103], v[116:119], v[180:183], v[100:103]\n\t"
      "ds_read_b64 v[156:157], v40 offset:3072\n\t"
      "ds_read_b64 v[158:159], v40 offset:3080\n\t"
      "v_mfma_f32_16x16x32_bf16 v[108:111], v[128:131], v[180:183], v[108:111]\n\t"
      "ds_read_b64 v[112:113], v40 offset:32\n\t"
      "ds_read_b64 v[114:115], v40 offset:40\n\t"
      "v_mfma_f32_16x16x32_bf16 v[100:103], v[128:131], v[176:179], v[100:103]\n\t"
      "ds_read_b64 v[124:125], v40 offset:1056\n\t"
      "ds_read_b64 v[126:127], v40 offset:1064\n\t"
      "v_mfma_f32_16x16x32_bf16 v[108:111], v[140:143], v[176:179], v[108:111]\n\t"
      "ds_read_b64 v[136:137], v40 offset:2080\n\t"
      "ds_read_b64 v[138:139], v40 offset:2088\n\t"
      "v_mfma_f32_16x16x32_bf16 v[108:111], v[116:119], v[184:187], v[108:111]\n\t"
      "ds_read_b64 v[148:149], v40 offset:3104\n\t"
      "ds_read_b64 v[150:151], v40 offset:3112\n\t"
      "L_cv_e%=:\n\t"
      "s_mov_b32 s42, %[wlo]\n\t"
      "s_mov_b32 s43, %[whi]\n\t"
      "s_waitcnt vmcnt(7)\n\t"
      "s_mov_b64 exec, 0xff\n\t"
      "global_store_dwordx2 v51, v[52:53], s[42:43] sc1\n\t"
      "s_mov_b64 exec, -1\n\t"
      "Lpd74eef_25:\n\t"
      "s_waitcnt lgkmcnt(8)\n\t"
      "v_mfma_f32_16x16x32_bf16 v[64:67], v[120:123], v[160:163], 0\n\t"
      "ds_read_b128 v[176:179], v41 offset:4640\n\t"
      "v_mfma_f32_16x16x32_bf16 v[72:75], v[120:123], v[164:167], 0\n\t"
      "ds_read_b64 v[116:117], v40 offset:64\n\t"
      "v_mfma_f32_16x16x32_bf16 v[80:83], v[120:123], v[168:171], 0\n\t"
      "ds_read_b64 v[118:119], v40 offset:72\n\t"
      "v_mfma_f32_16x16x32_bf16 v[88:91], v[120:123], v[172:175], 0\n\t"
      "ds_read_b128 v[180:183], v41 offset:3104\n\t"
      "v_mfma_f32_16x16x32_bf16 v[72:75], v[132:135], v[160:163], v[72:75]\n\t"
      "ds_read_b64 v[128:129], v40 offset:1088\n\t"
      "v_mfma_f32_16x16x32_bf16 v[80:83], v[132:135], v[164:167], v[80:83]\n\t"
      "ds_read_b64 v[130:131], v40 offset:1096\n\t"
      "v_mfma_f32_16x16x32_bf16 v[88:91], v[132:135], v[168:171], v[88:91]\n\t"
      "ds_read_b128 v[184:187], v41 offset:1568\n\t"
      "v_mfma_f32_16x16x32_bf16 v[80:83], v[144:147], v[160:163], v[80:83]\n\t"
      "ds_read_b64 v[140:141], v40 offset:2112\n\t"
      "v_mfma_f32_16x16x32_bf16 v[88:91], v[144:147], v[164:167], v[88:91]\n\t"
      "ds_read_b64 v[142:143], v40 offset:2120\n\t"
      "v_mfma_f32_16x16x32_bf16 v[88:91], v[156:159], v[160:163], v[88:91]\n\t"
      "ds_read_b128 v[188:191], v41 offset:32\n\t"
      "s_waitcnt lgkmcnt(10)\n\t"
      "v_mfma_f32_16x16x32_bf16 v[68:71], v[112:115], v[160:163], 0\n\t"
      "ds_read_b64 v[152:153], v40 offset:3136\n\t"
      "v_mfma_f32_16x16x32_bf16 v[76:79], v[112:115], v[164:167], 0\n\t"
      "ds_read_b64 v[154:155], v40 offset:3144\n\t"
      "v_mfma_f32_16x16x32_bf16 v[84:87], v[112:115], v[168:171], 0\n\t"
      "v_add_u32 v34, %[exd], v44\n\t"
      "v_add_u32 v35, %[exd], v59\n\t"
      "v_mfma_f32_16x16x32_bf16 v[92:95], v[112:115], v[172:175], 0\n\t"
      "ds_write_b128 v34, v[96:99]\n\t"
      "v_mfma_f32_16x16x32_bf16 v[76:79], v[124:127], v[160:163], v[76:79]\n\t"
      "ds_write_b128 v34, v[104:107] offset:2048\n\t"
      "v_mfma_f32_16x16x32_bf16 v[84:87], v[124:127], v[164:167], v[84:87]\n\t"
      "v_mfma_f32_16x16x32_bf16 v[92:95], v[124:127], v[168:171], v[92:95]\n\t"
      "v_mfma_f32_16x16x32_bf16 v[84:87], v[136:139], v[160:163], v[84:87]\n\t"
      "v_mfma_f32_16x16x32_bf16 v[92:95], v[136:139], v[164:167], v[92:95]\n\t"
      "v_mfma_f32_16x16x32_bf16 v[92:95], v[148:151], v[160:163], v[92:95]\n\t"
      "Lpd74eef_26:\n\t"
      "s_waitcnt lgkmcnt(2)\n\t"
      "v_mfma_f32_16x16x32_bf16 v[64:67], v[112:115], v[176:179], v[64:67]\n\t"
      "ds_read_b128 v[160:163], v41 offset:4704\n\t"
      "v_mfma_f32_16x16x32_bf16 v[72:75], v[112:115], v[180:183], v[72:75]\n\t"
      "ds_read_b64 v[120:121], v40 offset:96\n\t"
      "v_mfma_f32_16x16x32_bf16 v[80:83], v[112:115], v[184:187], v[80:83]\n\t"
      "ds_read_b64 v[122:123], v40 offset:104\n\t"
      "v_mfma_f32_16x16x32_bf16 v[88:91], v[112:115], v[188:191], v[88:91]\n\t"
      "ds_read_b128 v[164:167], v41 offset:3168\n\t"
      "v_mfma_f32_16x16x32_bf16 v[72:75], v[124:127], v[176:179], v[72:75]\n\t"
      "ds_read_b64 v[132:133], v40 offset:1120\n\t"
      "v_mfma_f32_16x16x32_bf16 v[80:83], v[124:127], v[180:183], v[80:83]\n\t"
      "ds_read_b64 v[134:135], v40 offset:1128\n\t"
      "v_mfma_f32_16x16x32_bf16 v[88:91], v[124:127], v[184:187], v[88:91]\n\t"
      "ds_read_b128 v[168:171], v41 offset:1632\n\t"
      "v_mfma_f32_16x16x32_bf16 v[80:83], v[136:139], v[176:179], v[80:83]\n\t"
      "ds_read_b64 v[144:145], v40 offset:2144\n\t"
      "v_mfma_f32_16x16x32_bf16 v[88:91], v[136:139], v[180:183], v[88:91]\n\t"
      "ds_read_b64 v[146:147], v40 offset:2152\n\t"
      "v_mfma_f32_16x16x32_bf16 v[88:91], v[148:151], v[176:179], v[88:91]\n\t"
      "ds_read_b128 v[172:175], v41 offset:96\n\t"
      "v_mfma_f32_16x16x32_bf16 v[68:71], v[116:119], v[176:179], v[68:71]\n\t"
      "ds_read_b64 v[156:157], v40 offset:3168\n\t"
      "v_mfma_f32_16x16x32_bf16 v[76:79], v[116:119], v[180:183], v[76:79]\n\t"
      "ds_read_b64 v[158:159], v40 offset:3176\n\t"
      "v_mfma_f32_16x16x32_bf16 v[84:87], v[116:119], v[184:187], v[84:87]\n\t"
      "ds_write_b128 v35, v[100:103]\n\t"
      "v_mfma_f32_16x16x32_bf16 v[92:95], v[116:119], v[188:191], v[92:95]\n\t"
      "ds_write_b128 v35, v[108:111] offset:2048\n\t"
      "v_mfma_f32_16x16x32_bf16 v[76:79], v[128:131], v[176:179], v[76:79]\n\t"
      "v_mfma_f32_16x16x32_bf16 v[84:87], v[128:131], v[180:183], v[84:87]\n\t"
      "v_mfma_f32_16x16x32_bf16 v[92:95], v[128:131], v[184:187], v[92:95]\n\t"
      "v_mfma_f32_16x16x32_bf16 v[84:87], v[140:143], v[176:179], v[84:87]\n\t"
      "v_mfma_f32_16x16x32_bf16 v[92:95], v[140:143], v[180:183], v[92:95]\n\t"
      "v_mfma_f32_16x16x32_bf16 v[92:95], v[152:155], v[176:179], v[92:95]\n\t"
      "Lpd74eef_27:\n\t"
      "s_waitcnt lgkmcnt(2)\n\t"
      "v_mfma_f32_16x16x32_bf16 v[64:67], v[116:119], v[160:163], v[64:67]\n\t"
      "ds_read_b128 v[176:179], v41 offset:4736\n\t"
      "v_mfma_f32_16x16x32_bf16 v[72:75], v[116:119], v[164:167], v[72:75]\n\t"
      "ds_read_b64 v[112:113], v40 offset:128\n\t"
      "v_mfma_f32_16x16x32_bf16 v[80:83], v[116:119], v[168:171], v[80:83]\n\t"
      "ds_read_b64 v[114:115], v40 offset:136\n\t"
      "v_mfma_f32_16x16x32_bf16 v[88:91], v[116:119], v[172:175], v[88:91]\n\t"
      "ds_read_b128 v[180:183], v41 offset:3200\n\t"
      "v_mfma_f32_16x16x32_bf16 v[72:75], v[128:131], v[160:163], v[72:75]\n\t"
      "ds_read_b64 v[124:125], v40 offset:1152\n\t"
      "v_mfma_f32_16x16x32_bf16 v[80:83], v[128:131], v[164:167], v[80:83]\n\t"
      "ds_read_b64 v[126:127], v40 offset:1160\n\t"
      "v_mfma_f32_16x16x32_bf16 v[88:91], v[128:131], v[168:171], v[88:91]\n\t"
      "ds_read_b128 v[184:187], v41 offset:1664\n\t"
      "v_mfma_f32_16x16x32_bf16 v[80:83], v[140:143], v[160:163], v[80:83]\n\t"
      "ds_read_b64 v[136:137], v40 offset:2176\n\t"
      "v_mfma_f32_16x16x32_bf16 v[88:91], v[140:143], v[164:167], v[88:91]\n\t"
      "ds_read_b64 v[138:139], v40 offset:2184\n\t"
      "v_mfma_f32_16x16x32_bf16 v[88:91], v[152:155], v[160:163], v[88:91]\n\t"
      "ds_read_b128 v[188:191], v41 offset:128\n\t"
      "v_mfma_f32_16x16x32_bf16 v[68:71], v[120:123], v[160:163], v[68:71]\n\t"
      "ds_read_b64 v[148:149], v40 offset:3200\n\t"
      "v_mfma_f32_16x16x32_bf16 v[76:79], v[120:123], v[164:167], v[76:79]\n\t"
      "ds_read_b64 v[150:151], v40 offset:3208\n\t"
      "v_mfma_f32_16x16x32_bf16 v[84:87], v[120:123], v[168:171], v[84:87]\n\t"
      "v_mfma_f32_16x16x32_bf16 v[92:95], v[120:123], v[172:175], v[92:95]\n\t"
      "v_mfma_f32_16x16x32_bf16 v[76:79], v[132:135], v[160:163], v[76:79]\n\t"
      "v_mfma_f32_16x16x32_bf16 v[84:87], v[132:135], v[164:167], v[84:87]\n\t"
      "v_mfma_f32_16x16x32_bf16 v[92:95], v[132:135], v[168:171], v[92:95]\n\t"
      "v_mfma_f32_16x16x32_bf16 v[84:87], v[144:147], v[160:163], v[84:87]\n\t"
      "v_mfma_f32_16x16x32_bf16 v[92:95], v[144:147], v[164:167], v[92:95]\n\t"
      "v_mfma_f32_16x16x32_bf16 v[92:95], v[156:159], v[160:163], v[92:95]\n\t"
      "s_waitcnt vmcnt(1)\n\t"
      "s_mov_b64 exec, 0xff\n\t"
      "global_store_dwordx2 v62, v[52:53], s[42:43] sc1\n\t"
      "s_mov_b64 exec, -1\n\t"
      "Lpd74eef_28:\n\t"
      "s_waitcnt lgkmcnt(0)\n\t"
      "s_barrier\n\t"
      "v_mfma_f32_16x16x32_bf16 v[64:67], v[120:123], v[176:179], v[64:67]\n\t"
      "ds_read_b128 v[160:163], v41 offset:4800\n\t"
      "v_mfma_f32_16x16x32_bf16 v[72:75], v[120:123], v[180:183], v[72:75]\n\t"
      "ds_read_b64 v[116:117], v40 offset:160\n\t"
      "v_mfma_f32_16x16x32_bf16 v[80:83], v[120:123], v[184:187], v[80:83]\n\t"
      "ds_read_b64 v[118:119], v40 offset:168\n\t"
      "v_mfma_f32_16x16x32_bf16 v[88:91], v[120:123], v[188:191], v[88:91]\n\t"
      "ds_read_b128 v[164:167], v41 offset:3264\n\t"
      "v_mfma_f32_16x16x32_bf16 v[72:75], v[132:135], v[176:179], v[72:75]\n\t"
      "global_load_dwordx4 v[192:195], v54, s[42:43] offset:-2048 sc1\n\t"
      "v_mfma_f32_16x16x32_bf16 v[80:83], v[132:135], v[180:183], v[80:83]\n\t"
      "ds_read_b64 v[128:129], v40 offset:1184\n\t"
      "v_mfma_f32_16x16x32_bf16 v[88:91], v[132:135], v[184:187], v[88:91]\n\t"
      "ds_read_b64 v[130:131], v40 offset:1192\n\t"
      "v_mfma_f32_16x16x32_bf16 v[80:83], v[144:147], v[176:179], v[80:83]\n\t"
      "ds_read_b128 v[168:171], v41 offset:1728\n\t"
      "v_mfma_f32_16x16x32_bf16 v[88:91], v[144:147], v[180:183], v[88:91]\n\t"
      "ds_read_b64 v[140:141], v40 offset:2208\n\t"
      "v_mfma_f32_16x16x32_bf16 v[88:91], v[156:159], v[176:179], v[88:91]\n\t"
      "global_load_dwordx4 v[196:199], v54, s[42:43] offset:2048 sc1\n\t"
      "v_mfma_f32_16x16x32_bf16 v[68:71], v[112:115], v[176:179], v[68:71]\n\t"
      "ds_read_b64 v[142:143], v40 offset:2216\n\t"
      "v_mfma_f32_16x16x32_bf16 v[76:79], v[112:115], v[180:183], v[76:79]\n\t"
      "ds_read_b128 v[172:175], v41 offset:192\n\t"
      "v_mfma_f32_16x16x32_bf16 v[84:87], v[112:115], v[184:187], v[84:87]\n\t"
      "ds_read_b64 v[152:153], v40 offset:3232\n\t"
      "v_mfma_f32_16x16x32_bf16 v[92:95], v[112:115], v[188:191], v[92:95]\n\t"
      "ds_read_b64 v[154:155], v40 offset:3240\n\t"
      "v_mfma_f32_16x16x32_bf16 v[76:79], v[124:127], v[176:179], v[76:79]\n\t"
      "global_load_dwordx2 v[200:201], v55, s[42:43] offset:0 sc1\n\t"
      "v_mfma_f32_16x16x32_bf16 v[84:87], v[124:127], v[180:183], v[84:87]\n\t"
      "ds_read_b128 v[96:99], v45 offset:0\n\t"
      "v_mfma_f32_16x16x32_bf16 v[92:95], v[124:127], v[184:187], v[92:95]\n\t"
      "ds_read_b128 v[100:103], v45 offset:4096\n\t"
      "v_mfma_f32_16x16x32_bf16 v[84:87], v[136:139], v[176:179], v[84:87]\n\t"
      "ds_read_b128 v[104:107], v45 offset:8192\n\t"
      "v_mfma_f32_16x16x32_bf16 v[92:95], v[136:139], v[180:183], v[92:95]\n\t"
      "ds_read_b128 v[108:111], v45 offset:12288\n\t"
      "v_mfma_f32_16x16x32_bf16 v[92:95], v[148:151], v[176:179], v[92:95]\n\t"
      "global_load_dwordx2 v[248:249], v60, s[42:43] offset:0 sc1\n\t"
      "Lpd74eef_29:\n\t"
      "s_waitcnt lgkmcnt(4)\n\t"
      "v_mfma_f32_16x16x32_bf16 v[64:67], v[112:115], v[160:163], v[64:67]\n\t"
      "ds_read_b128 v[176:179], v41 offset:4832\n\t"
      "v_mfma_f32_16x16x32_bf16 v[72:75], v[112:115], v[164:167], v[72:75]\n\t"
      "ds_read_b64 v[120:121], v40 offset:192\n\t"
      "v_mfma_f32_16x16x32_bf16 v[80:83], v[112:115], v[168:171], v[80:83]\n\t"
      "ds_read_b64 v[122:123], v40 offset:200\n\t"
      "v_mfma_f32_16x16x32_bf16 v[88:91], v[112:115], v[172:175], v[88:91]\n\t"
      "ds_read_b128 v[180:183], v41 offset:3296\n\t"
      "v_mfma_f32_16x16x32_bf16 v[72:75], v[124:127], v[160:163], v[72:75]\n\t"
      "global_load_dword v250, v61, s[42:43] offset:0 sc1\n\t"
      "v_mfma_f32_16x16x32_bf16 v[80:83], v[124:127], v[164:167], v[80:83]\n\t"
      "ds_read_b64 v[132:133], v40 offset:1216\n\t"
      "v_mfma_f32_16x16x32_bf16 v[88:91], v[124:127], v[168:171], v[88:91]\n\t"
      "ds_read_b64 v[134:135], v40 offset:1224\n\t"
      "v_mfma_f32_16x16x32_bf16 v[80:83], v[136:139], v[160:163], v[80:83]\n\t"
      "ds_read_b128 v[184:187], v41 offset:1760\n\t"
      "v_mfma_f32_16x16x32_bf16 v[88:91], v[136:139], v[164:167], v[88:91]\n\t"
      "ds_read_b64 v[144:145], v40 offset:2240\n\t"
      "v_mfma_f32_16x16x32_bf16 v[88:91], v[148:151], v[160:163], v[88:91]\n\t"
      "global_load_dwordx4 v[204:207], v58, s[42:43] offset:0 sc1\n\t"
      "v_mfma_f32_16x16x32_bf16 v[68:71], v[116:119], v[160:163], v[68:71]\n\t"
      "ds_read_b64 v[146:147], v40 offset:2248\n\t"
      "v_mfma_f32_16x16x32_bf16 v[76:79], v[116:119], v[164:167], v[76:79]\n\t"
      "ds_read_b128 v[188:191], v41 offset:224\n\t"
      "v_mfma_f32_16x16x32_bf16 v[84:87], v[116:119], v[168:171], v[84:87]\n\t"
      "ds_read_b64 v[156:157], v40 offset:3264\n\t"
      "v_mfma_f32_16x16x32_bf16 v[92:95], v[116:119], v[172:175], v[92:95]\n\t"
      "ds_read_b64 v[158:159], v40 offset:3272\n\t"
      "v_mfma_f32_16x16x32_bf16 v[76:79], v[128:131], v[160:163], v[76:79]\n\t"
      "global_load_dwordx4 v[36:39], v58, s[42:43] offset:16 sc1\n\t"
      "v_mfma_f32_16x16x32_bf16 v[84:87], v[128:131], v[164:167], v[84:87]\n\t"
      "ds_read_b128 v[232:235], v46\n\t"
      "v_mfma_f32_16x16x32_bf16 v[92:95], v[128:131], v[168:171], v[92:95]\n\t"
      "ds_read_b128 v[236:239], v46 offset:7680\n\t"
      "v_mfma_f32_16x16x32_bf16 v[84:87], v[140:143], v[160:163], v[84:87]\n\t"
      "ds_read_b64 v[202:203], v47\n\t"
      "v_mfma_f32_16x16x32_bf16 v[92:95], v[140:143], v[164:167], v[92:95]\n\t"
      "v_mfma_f32_16x16x32_bf16 v[92:95], v[152:155], v[160:163], v[92:95]\n\t"
      "global_load_dwordx4 v[208:211], v56, s[42:43] offset:0 sc1\n\t"
      "Lpd74eef_30:\n\t"
      "s_waitcnt lgkmcnt(3)\n\t"
      "v_mfma_f32_16x16x32_bf16 v[64:67], v[116:119], v[176:179], v[64:67]\n\t"
      "ds_read_b128 v[160:163], v41 offset:4896\n\t"
      "v_mfma_f32_16x16x32_bf16 v[72:75], v[116:119], v[180:183], v[72:75]\n\t"
      "ds_read_b64 v[112:113], v40 offset:224\n\t"
      "v_mfma_f32_16x16x32_bf16 v[80:83], v[116:119], v[184:187], v[80:83]\n\t"
      "ds_read_b64 v[114:115], v40 offset:232\n\t"
      "v_mfma_f32_16x16x32_bf16 v[88:91], v[116:119], v[188:191], v[88:91]\n\t"
      "ds_read_b128 v[164:167], v41 offset:3360\n\t"
      "v_mfma_f32_16x16x32_bf16 v[72:75], v[128:131], v[176:179], v[72:75]\n\t"
      "global_load_dwordx4 v[212:215], v56, s[42:43] offset:1024 sc1\n\t"
      "v_mfma_f32_16x16x32_bf16 v[80:83], v[128:131], v[180:183], v[80:83]\n\t"
      "ds_read_b64 v[124:125], v40 offset:1248\n\t"
      "v_mfma_f32_16x16x32_bf16 v[88:91], v[128:131], v[184:187], v[88:91]\n\t"
      "ds_read_b64 v[126:127], v40 offset:1256\n\t"
      "v_mfma_f32_16x16x32_bf16 v[80:83], v[140:143], v[176:179], v[80:83]\n\t"
      "ds_read_b128 v[168:171], v41 offset:1824\n\t"
      "v_mfma_f32_16x16x32_bf16 v[88:91], v[140:143], v[180:183], v[88:91]\n\t"
      "ds_read_b64 v[136:137], v40 offset:2272\n\t"
      "v_mfma_f32_16x16x32_bf16 v[88:91], v[152:155], v[176:179], v[88:91]\n\t"
      "global_load_dwordx4 v[216:219], v56, s[42:43] offset:2048 sc1\n\t"
      "v_mfma_f32_16x16x32_bf16 v[68:71], v[120:123], v[176:179], v[68:71]\n\t"
      "ds_read_b64 v[138:139], v40 offset:2280\n\t"
      "v_mfma_f32_16x16x32_bf16 v[76:79], v[120:123], v[180:183], v[76:79]\n\t"
      "ds_read_b128 v[172:175], v41 offset:288\n\t"
      "v_mfma_f32_16x16x32_bf16 v[84:87], v[120:123], v[184:187], v[84:87]\n\t"
      "ds_read_b64 v[148:149], v40 offset:3296\n\t"
      "v_mfma_f32_16x16x32_bf16 v[92:95], v[120:123], v[188:191], v[92:95]\n\t"
      "ds_read_b64 v[150:151], v40 offset:3304\n\t"
      "v_mfma_f32_16x16x32_bf16 v[76:79], v[132:135], v[176:179], v[76:79]\n\t"
      "global_load_dwordx4 v[220:223], v56, s[42:43] offset:3072 sc1\n\t"
      "v_mfma_f32_16x16x32_bf16 v[84:87], v[132:135], v[180:183], v[84:87]\n\t"
      "v_mfma_f32_16x16x32_bf16 v[92:95], v[132:135], v[184:187], v[92:95]\n\t"
      "v_mfma_f32_16x16x32_bf16 v[84:87], v[144:147], v[176:179], v[84:87]\n\t"
      "v_mfma_f32_16x16x32_bf16 v[92:95], v[144:147], v[180:183], v[92:95]\n\t"
      "v_mfma_f32_16x16x32_bf16 v[92:95], v[156:159], v[176:179], v[92:95]\n\t"
      "global_load_dwordx4 v[224:227], v57, s[42:43] offset:0 sc1\n\t"
      "Lpd74eef_31:\n\t"
      "s_waitcnt lgkmcnt(0)\n\t"
      "v_mfma_f32_16x16x32_bf16 v[64:67], v[120:123], v[160:163], v[64:67]\n\t"
      "ds_read_b128 v[176:179], v41 offset:4928\n\t"
      "v_mfma_f32_16x16x32_bf16 v[72:75], v[120:123], v[164:167], v[72:75]\n\t"
      "ds_read_b64 v[116:117], v40 offset:256\n\t"
      "v_mfma_f32_16x16x32_bf16 v[80:83], v[120:123], v[168:171], v[80:83]\n\t"
      "ds_read_b64 v[118:119], v40 offset:264\n\t"
      "v_mfma_f32_16x16x32_bf16 v[88:91], v[120:123], v[172:175], v[88:91]\n\t"
      "ds_read_b128 v[180:183], v41 offset:3392\n\t"
      "v_mfma_f32_16x16x32_bf16 v[72:75], v[132:135], v[160:163], v[72:75]\n\t"
      "global_load_dwordx4 v[228:231], v57, s[42:43] offset:1024 sc1\n\t"
      "v_mfma_f32_16x16x32_bf16 v[80:83], v[132:135], v[164:167], v[80:83]\n\t"
      "ds_read_b64 v[128:129], v40 offset:1280\n\t"
      "v_mfma_f32_16x16x32_bf16 v[88:91], v[132:135], v[168:171], v[88:91]\n\t"
      "ds_read_b64 v[130:131], v40 offset:1288\n\t"
      "v_mfma_f32_16x16x32_bf16 v[80:83], v[144:147], v[160:163], v[80:83]\n\t"
      "ds_read_b128 v[184:187], v41 offset:1856\n\t"
      "v_mfma_f32_16x16x32_bf16 v[88:91], v[144:147], v[164:167], v[88:91]\n\t"
      "ds_read_b64 v[140:141], v40 offset:2304\n\t"
      "v_mfma_f32_16x16x32_bf16 v[88:91], v[156:159], v[160:163], v[88:91]\n\t"
      "ds_read_b64 v[142:143], v40 offset:2312\n\t"
      "v_mfma_f32_16x16x32_bf16 v[68:71], v[112:115], v[160:163], v[68:71]\n\t"
      "ds_read_b128 v[188:191], v41 offset:320\n\t"
      "v_mfma_f32_16x16x32_bf16 v[76:79], v[112:115], v[164:167], v[76:79]\n\t"
      "s_mov_b32 s45, m0\n\t"
      "s_mov_b32 m0, %[djm]\n\t"
      "s_nop 0\n\t"
      "global_load_lds_dwordx4 v63, %[dj0]\n\t"
      "v_mfma_f32_16x16x32_bf16 v[84:87], v[112:115], v[168:171], v[84:87]\n\t"
      "ds_read_b64 v[152:153], v40 offset:3328\n\t"
      "v_mfma_f32_16x16x32_bf16 v[92:95], v[112:115], v[172:175], v[92:95]\n\t"
      "ds_read_b64 v[154:155], v40 offset:3336\n\t"
      "v_mfma_f32_16x16x32_bf16 v[76:79], v[124:127], v[160:163], v[76:79]\n\t"
      "v_mfma_f32_16x16x32_bf16 v[84:87], v[124:127], v[164:167], v[84:87]\n\t"
      "v_mfma_f32_16x16x32_bf16 v[92:95], v[124:127], v[168:171], v[92:95]\n\t"
      "v_mfma_f32_16x16x32_bf16 v[84:87], v[136:139], v[160:163], v[84:87]\n\t"
      "v_mfma_f32_16x16x32_bf16 v[92:95], v[136:139], v[164:167], v[92:95]\n\t"
      "v_mfma_f32_16x16x32_bf16 v[92:95], v[148:151], v[160:163], v[92:95]\n\t"
      "global_load_lds_dwordx4 v63, %[dj1] offset:1024\n\t"
      "s_mov_b32 m0, s45\n\t"
      "Lpd74eef_32:\n\t"
      "s_waitcnt lgkmcnt(0)\n\t"
      "v_mfma_f32_16x16x32_bf16 v[64:67], v[112:115], v[176:179], v[64:67]\n\t"
      "v_mfma_f32_16x16x32_bf16 v[68:71], v[116:119], v[176:179], v[68:71]\n\t"
      "global_load_dwordx4 v[240:243], v48, s[42:43] offset:0 sc1\n\t"
      "v_mfma_f32_16x16x32_bf16 v[72:75], v[112:115], v[180:183], v[72:75]\n\t"
      "v_mfma_f32_16x16x32_bf16 v[72:75], v[124:127], v[176:179], v[72:75]\n\t"
      "global_load_dwordx4 v[244:247], v49, s[42:43] offset:0 sc1\n\t"
      "v_mfma_f32_16x16x32_bf16 v[76:79], v[116:119], v[180:183], v[76:79]\n\t"
      "ds_write_b128 v44, v[64:67] offset:0\n\t"
      "v_mfma_f32_16x16x32_bf16 v[76:79], v[128:131], v[176:179], v[76:79]\n\t"
      "global_load_dwordx4 v[252:255], v49, s[42:43] offset:1024 sc1\n\t"
      "v_mfma_f32_16x16x32_bf16 v[80:83], v[112:115], v[184:187], v[80:83]\n\t"
      "ds_write_b128 v59, v[68:71] offset:0\n\t"
      "v_mfma_f32_16x16x32_bf16 v[80:83], v[124:127], v[180:183], v[80:83]\n\t"
      "v_mfma_f32_16x16x32_bf16 v[80:83], v[136:139], v[176:179], v[80:83]\n\t"
      "ds_write_b128 v44, v[72:75] offset:2048\n\t"
      "v_mfma_f32_16x16x32_bf16 v[84:87], v[116:119], v[184:187], v[84:87]\n\t"
      "v_mfma_f32_16x16x32_bf16 v[84:87], v[128:131], v[180:183], v[84:87]\n\t"
      "ds_write_b128 v59, v[76:79] offset:2048\n\t"
      "v_mfma_f32_16x16x32_bf16 v[84:87], v[140:143], v[176:179], v[84:87]\n\t"
      "v_mfma_f32_16x16x32_bf16 v[88:91], v[112:115], v[188:191], v[88:91]\n\t"
      "ds_write_b128 v44, v[80:83] offset:4096\n\t"
      "v_mfma_f32_16x16x32_bf16 v[88:91], v[124:127], v[184:187], v[88:91]\n\t"
      "v_mfma_f32_16x16x32_bf16 v[88:91], v[136:139], v[180:183], v[88:91]\n\t"
      "v_mfma_f32_16x16x32_bf16 v[88:91], v[148:151], v[176:179], v[88:91]\n\t"
      "ds_write_b128 v59, v[84:87] offset:4096\n\t"
      "v_mfma_f32_16x16x32_bf16 v[92:95], v[116:119], v[188:191], v[92:95]\n\t"
      "v_mfma_f32_16x16x32_bf16 v[92:95], v[128:131], v[184:187], v[92:95]\n\t"
      "v_mfma_f32_16x16x32_bf16 v[92:95], v[140:143], v[180:183], v[92:95]\n\t"
      "v_mfma_f32_16x16x32_bf16 v[92:95], v[152:155], v[176:179], v[92:95]\n\t"
      "ds_write_b128 v44, v[88:91] offset:6144\n\t"
      "s_waitcnt vmcnt(5)\n\t"
      "s_nop 5\n\t"
      "ds_write_b128 v59, v[92:95] offset:6144\n\t"
      : "=&{v[96:111]}"(hp), "=&{v[192:223]}"(oa), "=&{v[224:239]}"(ob), "=&{v[248:251]}"(oc), "=&{v[36:39]}"(od), "=&{v[240:243]}"(f0),
        "=&{v[244:247]}"(f1), "=&{v[252:255]}"(f2)
      : [wlo] "s"(wslo), [whi] "s"(wshi), [side] "s"(side), [exd] "s"(32768 - w * 4096), [djm] "s"(__builtin_amdgcn_readfirstlane(lbase + L_DJ + w * 2048)), [dj0] "s"(wdn),
        [dj1] "s"(wdn + 48 * D_ - 512), "{v[40:55]}"(ia), "{v[56:63]}"(ib)
      : "memory", "scc", "s42", "s43", "s45", "v34", "v35", "v64", "v65", "v66", "v67", "v68", "v69", "v70", "v71", "v72", "v73", "v74", "v75", "v76", "v77", "v78", "v79", "v80", "v81", "v82", "v83", "v84", "v85", "v86", "v87", "v88", "v89", "v90", "v91", "v92", "v93", "v94", "v95", "v112", "v113", "v114", "v115", "v116", "v117", "v118", "v119", "v120", "v121", "v122", "v123", "v124", "v125", "v126", "v127", "v128", "v129", "v130", "v131", "v132", "v133", "v134", "v135", "v136", "v137", "v138", "v139", "v140", "v141", "v142", "v143", "v144", "v145", "v146", "v147", "v148", "v149", "v150", "v151", "v152", "v153", "v154", "v155", "v156", "v157", "v158", "v159", "v160", "v161", "v162", "v163", "v164", "v165", "v166", "v167", "v168", "v169", "v170", "v171", "v172", "v173", "v174", "v175", "v176", "v177", "v178", "v179", "v180", "v181", "v182", "v183", "v184", "v185", "v186", "v187", "v188", "v189", "v190", "v191");
  asm volatile("Lpd74eef_33:");
  {
    auto ok = [&]() -> bool {
      unsigned d = modx((u32x4){oa[12], oa[13], oa[14], oa[15]}, od, T[0]);
      d |= (cks((u32x4){oa[0], oa[1], oa[2], oa[3]}, sh) ^ oc[0] ^ T[0]) | (cks((u32x4){oa[4], oa[5], oa[6], oa[7]}, sh) ^ oc[1] ^ T[0]);
      d |= cks2(oa[8], oa[9], lane) ^ oc[2] ^ T[0];
      d |= (oa[19] ^ T[0]) | (oa[23] ^ T[0]) | (oa[27] ^ T[0]) | (oa[31] ^ T[0]) | (ob[3] ^ T[0]) | (ob[7] ^ T[0]);
      return __all(d == 0);
    };
    if (__builtin_expect(!ok(), 0)) {
#pragma unroll 1
      for (int sp = 0; sp < SPIN_CAP; ++sp) {
        asm volatile("" ::: "memory");
        u32x4 x[9];
        x[0] = LD128(qoA); x[1] = LD128(qoA + 4096); x[2] = LD128(pmo);
        od = LD128(pmo + 16);
#pragma unroll
        for (int i = 0; i < 4; ++i) x[3 + i] = LD128(soA + i * 1024);
        x[7] = LD128(soH); x[8] = LD128(soH + 1024);
        oc[0] = LD32(fqo); oc[1] = LD32(fqo + 4); oc[2] = LD32(fso);
        const u32x2 y = LD64(qoH);
#pragma unroll
        for (int i = 0; i < 8; ++i) { oa[i] = x[i >> 2][i & 3]; ob[i] = x[7 + (i >> 2)][i & 3]; }
#pragma unroll
        for (int i = 0; i < 20; ++i) oa[12 + i] = x[2 + (i >> 2)][i & 3];
        oa[8] = y[0]; oa[9] = y[1];
        if (ok()) break;
      }
      asm volatile("" : "+v"(oa), "+v"(ob));
    }
  }
  asm volatile("Lpd74eef_34:");
  const unsigned pm = __float_as_uint(modv((u32x4){oa[12], oa[13], oa[14], oa[15]}, od));
  auto x1_pair = [&](int it, unsigned q, f32x2 mean, f32x2 rstd, f32x2 sum, unsigned x) __attribute__((always_inline)) -> unsigned {
    const float sa = __uint_as_float(__builtin_amdgcn_readlane(pm, 4 * it)), ga = __uint_as_float(__builtin_amdgcn_readlane(pm, 4 * it + 2));
    const float c1 = rb1(1.0f + __uint_as_float(__builtin_amdgcn_readlane(pm, 4 * it + 1))), Dv = __uint_as_float(Dw[it]);
    const f32x2 qf = up2(q);
    f32x2 u = rb2(rb2(qf - mean) * rstd);
    u = rb2(rb2(u * c1) + sa);
    const f32x2 y = rb2(rb2(sum) + rb2((f32x2){hi16(x), lo16(x)} * Dv));
    u = qf + rb2(rb2(u * y) * ga);
    return san(pk2(u[0], u[1]));
  };
#define F2(v, i) ((f32x2){__uint_as_float(v[i]), __uint_as_float(v[(i) + 1])})
#define WL(x) asm volatile(x :: "v"(lane * 16), "s"(wplo), "s"(wphi) : "memory", "scc", "s40", "s41", "a0", "a1", "a2", "a3", "a4", "a5", "a6", "a7", "a8", "a9", "a10", "a11", "a12", "a13", "a14", "a15", "a16", "a17", "a18", "a19", "a20", "a21", "a22", "a23", "a24", "a25", "a26", "a27", "a28", "a29", "a30", "a31", "a32", "a33", "a34", "a35", "a36", "a37", "a38", "a39", "a40", "a41", "a42", "a43", "a44", "a45", "a46", "a47", "a48", "a49", "a50", "a51", "a52", "a53", "a54", "a55", "a56", "a57", "a58", "a59", "a60", "a61", "a62", "a63", "a64", "a65", "a66", "a67", "a68", "a69", "a70", "a71", "a72", "a73", "a74", "a75", "a76", "a77", "a78", "a79", "a80", "a81", "a82", "a83", "a84", "a85", "a86", "a87", "a88", "a89", "a90", "a91", "a92", "a93", "a94", "a95", "a96", "a97", "a98", "a99", "a100", "a101", "a102", "a103", "a104", "a105", "a106", "a107", "a108", "a109", "a110", "a111", "a112", "a113", "a114", "a115", "a116", "a117", "a118", "a119", "a120", "a121", "a122", "a123", "a124", "a125", "a126", "a127", "a128", "a129", "a130", "a131", "a132", "a133", "a134", "a135", "a136", "a137", "a138", "a139", "a140", "a141", "a142", "a143", "a144", "a145", "a146", "a147", "a148", "a149", "a150", "a151", "a152", "a153", "a154", "a155", "a156", "a157", "a158", "a159", "a160", "a161", "a162", "a163", "a164", "a165", "a166", "a167", "a168", "a169", "a170", "a171", "a172", "a173", "a174", "a175", "a176", "a177", "a178", "a179", "a180", "a181", "a182", "a183", "a184", "a185", "a186", "a187", "a188", "a189", "a190", "a191", "a192", "a193", "a194", "a195", "a196", "a197", "a198", "a199", "a200", "a201", "a202", "a203", "a204", "a205", "a206", "a207", "a208", "a209", "a210", "a211", "a212", "a213", "a214", "a215", "a216", "a217", "a218", "a219", "a220", "a221", "a222", "a223", "a224", "a225", "a226", "a227", "a228", "a229", "a230", "a231", "a232", "a233", "a234", "a235", "a236", "a237", "a238", "a239")
  f32x4 cp[8];
  bool wok;
  const unsigned fwo = WS_FW + xcd * 5120 + (bn * 20 + (lane < 20 ? lane : 19)) * 16;
  {
    u32x2 r;
#pragma unroll
    for (int e = 0; e < 2; ++e) {
      const f32x2 s = ((f32x2){hp[2 * e], hp[2 * e + 1]} + (f32x2){hp[4 + 2 * e], hp[5 + 2 * e]}) + ((f32x2){hp[8 + 2 * e], hp[9 + 2 * e]} + (f32x2){hp[12 + 2 * e], hp[13 + 2 * e]});
      r[e] = x1_pair(2, oa[8 + e], up2(ob[2 + 4 * e]), F2(ob, 4 * e), s, oa[11 - e]);
    }
    ST64(x1o[2], r);
    asm volatile("s_waitcnt vmcnt(1)" : "+v"(f0), "+v"(f1), "+v"(f2)::"memory");
    wok = __all(lane >= 8 || tagx(f0, T) == 0);
    LDS32(lds + L_FL + 16 + 4 * w) = __all((tagx(f1, T) | tagx(f2, T)) == 0);
    const unsigned cr = L_EX + w * 2048 + (lane >> 2) * 128 + ((2 * (lane & 3)) ^ ((lane >> 2) & 7)) * 16;
    BAR();
#pragma unroll
    for (int i = 0; i < 8; ++i) cp[i] = LDSF4(lds + (cr ^ ((i & 1) * 16)) + (i >> 1) * 8192);
  }
  const u32x4 xok = LDS128(lds + L_FL + 16);
  SCHEDB();
  asm volatile("Lpd74eef_35:");
  {
    u32x4 r;
    auto piece = [&](int it, int e) __attribute__((always_inline)) {
      const f32x4 s = cp[4 * it + (e >> 1)] + cp[4 * it + 2 + (e >> 1)];
      unsigned q = oa[4 * it + e];
      asm volatile("" : "+v"(q));
      r[e] = x1_pair(it, q, up2(oa[18 + 4 * e]), F2(oa, 16 + 4 * e), (f32x2){s[2 * (e & 1)], s[2 * (e & 1) + 1]}, ob[8 + 4 * it + 3 - e]);
      asm volatile("" : "+v"(r[e]));
    };
    WL(
      "s_add_u32 s40, %1, 0\n\t"
      "s_addc_u32 s41, %2, 0\n\t"
      "global_load_dwordx4 a[0:3], %0, s[40:41] offset:0 sc1\n\t"
      "global_load_dwordx4 a[4:7], %0, s[40:41] offset:1024 sc1\n\t"
      "global_load_dwordx4 a[8:11], %0, s[40:41] offset:2048 sc1\n\t"
      "global_load_dwordx4 a[12:15], %0, s[40:41] offset:3072 sc1\n\t"
    );
    piece(0, 0);
    WL(
      "s_add_u32 s40, %1, 4096\n\t"
      "s_addc_u32 s41, %2, 0\n\t"
      "global_load_dwordx4 a[16:19], %0, s[40:41] offset:0 sc1\n\t"
      "global_load_dwordx4 a[20:23], %0, s[40:41] offset:1024 sc1\n\t"
      "global_load_dwordx4 a[24:27], %0, s[40:41] offset:2048 sc1\n\t"
      "global_load_dwordx4 a[28:31], %0, s[40:41] offset:3072 sc1\n\t"
    );
    piece(0, 1);
    WL(
      "s_add_u32 s40, %1, 8192\n\t"
      "s_addc_u32 s41, %2, 0\n\t"
      "global_load_dwordx4 a[32:35], %0, s[40:41] offset:0 sc1\n\t"
      "global_load_dwordx4 a[36:39], %0, s[40:41] offset:1024 sc1\n\t"
      "global_load_dwordx4 a[40:43], %0, s[40:41] offset:2048 sc1\n\t"
      "global_load_dwordx4 a[44:47], %0, s[40:41] offset:3072 sc1\n\t"
    );
    piece(0, 2);
    piece(0, 3);
    ST128(x1o[0], r);
  asm volatile("Lpd74eef_36:");
    SLD128(f0, fwo, 0);
    WL(
      "s_add_u32 s40, %1, 12288\n\t"
      "s_addc_u32 s41, %2, 0\n\t"
      "global_load_dwordx4 a[48:51], %0, s[40:41] offset:0 sc1\n\t"
      "global_load_dwordx4 a[52:55], %0, s[40:41] offset:1024 sc1\n\t"
      "global_load_dwordx4 a[56:59], %0, s[40:41] offset:2048 sc1\n\t"
      "global_load_dwordx4 a[60:63], %0, s[40:41] offset:3072 sc1\n\t"
    );
    piece(1, 0);
    WL(
      "s_add_u32 s40, %1, 16384\n\t"
      "s_addc_u32 s41, %2, 0\n\t"
      "global_load_dwordx4 a[64:67], %0, s[40:41] offset:0 sc1\n\t"
      "global_load_dwordx4 a[68:71], %0, s[40:41] offset:1024 sc1\n\t"
      "global_load_dwordx4 a[72:75], %0, s[40:41] offset:2048 sc1\n\t"
      "global_load_dwordx4 a[76:79], %0, s[40:41] offset:3072 sc1\n\t"
    );
    piece(1, 1);
    WL(
      "s_add_u32 s40, %1, 20480\n\t"
      "s_addc_u32 s41, %2, 0\n\t"
      "global_load_dwordx4 a[80:83], %0, s[40:41] offset:0 sc1\n\t"
      "global_load_dwordx4 a[84:87], %0, s[40:41] offset:1024 sc1\n\t"
      "global_load_dwordx4 a[88:91], %0, s[40:41] offset:2048 sc1\n\t"
      "global_load_dwordx4 a[92:95], %0, s[40:41] offset:3072 sc1\n\t"
    );
    piece(1, 2);
    piece(1, 3);
    ST128(x1o[1], r);
    SCHEDB();
  }
  asm volatile("Lpd74eef_37:");
  if (__builtin_expect(!__all((xok[0] & xok[1] & xok[2] & xok[3]) != 0), 0)) {
#pragma unroll 1
    for (int sp = 0; sp < SPIN_CAP; ++sp) {
      asm volatile("" ::: "memory");
      unsigned d = 0;
#pragma unroll
      for (int i = 0; i < 8; ++i) d |= tagx(LD128(WS_FP2 + xcd * 8192 + i * 1024 + lane * 16), T);
      if (__all(d == 0)) break;
    }
  }
  asm volatile("s_waitcnt vmcnt(13)" : "+v"(f0)::"memory");
  if (__builtin_expect(!__all(tagx(f0, T) == 0), 0)) {
#pragma unroll 1
    for (int sp = 0; sp < SPIN_CAP; ++sp) {
      asm volatile("" ::: "memory");
      if (__all(tagx(LD128(fwo), T) == 0)) break;
    }
  }
  if (__builtin_expect(!wok, 0)) {
    WL(
      "s_add_u32 s40, %1, 0\n\t"
      "s_addc_u32 s41, %2, 0\n\t"
      "global_load_dwordx4 a[0:3], %0, s[40:41] offset:0 sc1\n\t"
      "global_load_dwordx4 a[4:7], %0, s[40:41] offset:1024 sc1\n\t"
      "global_load_dwordx4 a[8:11], %0, s[40:41] offset:2048 sc1\n\t"
      "global_load_dwordx4 a[12:15], %0, s[40:41] offset:3072 sc1\n\t"
      "s_add_u32 s40, %1, 4096\n\t"
      "s_addc_u32 s41, %2, 0\n\t"
      "global_load_dwordx4 a[16:19], %0, s[40:41] offset:0 sc1\n\t"
      "global_load_dwordx4 a[20:23], %0, s[40:41] offset:1024 sc1\n\t"
      "global_load_dwordx4 a[24:27], %0, s[40:41] offset:2048 sc1\n\t"
      "global_load_dwordx4 a[28:31], %0, s[40:41] offset:3072 sc1\n\t"
      "s_add_u32 s40, %1, 8192\n\t"
      "s_addc_u32 s41, %2, 0\n\t"
      "global_load_dwordx4 a[32:35], %0, s[40:41] offset:0 sc1\n\t"
      "global_load_dwordx4 a[36:39], %0, s[40:41] offset:1024 sc1\n\t"
      "global_load_dwordx4 a[40:43], %0, s[40:41] offset:2048 sc1\n\t"
      "global_load_dwordx4 a[44:47], %0, s[40:41] offset:3072 sc1\n\t"
      "s_add_u32 s40, %1, 12288\n\t"
      "s_addc_u32 s41, %2, 0\n\t"
      "global_load_dwordx4 a[48:51], %0, s[40:41] offset:0 sc1\n\t"
      "global_load_dwordx4 a[52:55], %0, s[40:41] offset:1024 sc1\n\t"
      "global_load_dwordx4 a[56:59], %0, s[40:41] offset:2048 sc1\n\t"
      "global_load_dwordx4 a[60:63], %0, s[40:41] offset:3072 sc1\n\t"
      "s_add_u32 s40, %1, 16384\n\t"
      "s_addc_u32 s41, %2, 0\n\t"
      "global_load_dwordx4 a[64:67], %0, s[40:41] offset:0 sc1\n\t"
      "global_load_dwordx4 a[68:71], %0, s[40:41] offset:1024 sc1\n\t"
      "global_load_dwordx4 a[72:75], %0, s[40:41] offset:2048 sc1\n\t"
      "global_load_dwordx4 a[76:79], %0, s[40:41] offset:3072 sc1\n\t"
      "s_add_u32 s40, %1, 20480\n\t"
      "s_addc_u32 s41, %2, 0\n\t"
      "global_load_dwordx4 a[80:83], %0, s[40:41] offset:0 sc1\n\t"
      "global_load_dwordx4 a[84:87], %0, s[40:41] offset:1024 sc1\n\t"
      "global_load_dwordx4 a[88:91], %0, s[40:41] offset:2048 sc1\n\t"
      "global_load_dwordx4 a[92:95], %0, s[40:41] offset:3072 sc1\n\t"
    );
  }
  const size_t xb = (size_t)ws + WS_X1T + (size_t)bm * 163840;
  asm volatile(
      "s_mov_b32 s45, m0\n\t"
      "s_add_u32 m0, %[m0b], 0\n\t"
      "s_add_u32 s42, %[xlo], 0\n\t"
      "s_addc_u32 s43, %[xhi], 0\n\t"
      "global_load_lds_dwordx4 %[voff], s[42:43] offset:0 sc1\n\t"
      "global_load_lds_dwordx4 %[voff], s[42:43] offset:1024 sc1\n\t"
      "global_load_lds_dwordx4 %[voff], s[42:43] offset:2048 sc1\n\t"
      "global_load_lds_dwordx4 %[voff], s[42:43] offset:3072 sc1\n\t"
      "s_add_u32 m0, %[m0b], 16384\n\t"
      "s_add_u32 s42, %[xlo], 16384\n\t"
      "s_addc_u32 s43, %[xhi], 0\n\t"
      "global_load_lds_dwordx4 %[voff], s[42:43] offset:0 sc1\n\t"
      "global_load_lds_dwordx4 %[voff], s[42:43] offset:1024 sc1\n\t"
      "global_load_lds_dwordx4 %[voff], s[42:43] offset:2048 sc1\n\t"
      "global_load_lds_dwordx4 %[voff], s[42:43] offset:3072 sc1\n\t"
      "s_mov_b32 m0, s45\n\t"
      :: [voff] "v"(w * 4096 + lane * 16), [xlo] "s"((unsigned)xb), [xhi] "s"((unsigned)(xb >> 32)), [m0b] "s"(lbase + w * 4096) : "memory", "scc", "s42", "s43", "s45");
  WL(
""
  );
  asm volatile("s_mov_b64 exec, 0\n\t" "Lpd74eef_ret2:\n\ts_mov_b64 exec, -1" :: "s"(w) : "scc");
  asm volatile("Lpd74eef_38:");
  const unsigned hho = WS_HH + (unsigned)(((2 * bm + (w >> 1)) * 48 + 3 * bn) * 4 + (w & 1)) * 1024 + lane * 16;
  const size_t wb = (size_t)ws + WS_WD + (size_t)(bn2 * 48 + 12 * w) * 5120;
  const size_t hb = (size_t)ws + WS_HH + (size_t)(bm2 * 48 + 12 * w) * 4096;
  const int c24 = (lane & 31) < 24 ? (lane & 31) : 23;
  u32x8 aad, px;
  {
#pragma unroll
    for (int j = 0; j < 8; ++j) aad[j] = lbase + (j >> 2) * 65536 + (4 * fq + q4) * 128 + ((j ^ w ^ (2 * fq + (q4 >> 1))) & 3) * 32 + p4 * 8;
    px[0] = L < 240 ? WS_FD + (lane & 7) * 7680 + (d_ks * 20 + d_cb) * 8 : WS_FP1 + 8192 + (b * 4 + w) * 8;
    px[1] = lbase + L_DJ + w * 2048 + (4 * fq + q4) * 64 + p4 * 8;
#pragma unroll
    for (int c = 0; c < 2; ++c) {
      const int tc = 2 * d_cb + c, b5 = (tc * 205) >> 10;
      px[2 + c] = WS_WD + (L < 240 ? (unsigned)((b5 * 48 + d_ks) * 5 + tc - 5 * b5) : 1920u) * 1024 + lane * 16;
    }
    px[4] = WS_PART + (unsigned)(lane >> 5) * 245760 + (c24 >= 12 ? 12288 - 192 : 0) + (bn * 192 + 48 * w + 4 * c24) * 4;
    const int l3 = ((lane < 36 ? lane : 35) * 43) >> 7;
    px[5] = WS_FD + xcd * 7680 + ((12 * w + l3) * 20 + ((5 * bn2) >> 1) + (lane < 36 ? lane : 35) - 3 * l3) * 8;
    px[6] = WS_MOD + (3200 + bn2 * 80 + (lane < 40 ? lane : 39) * 2) * 32;
    px[7] = 0;
  }
  const unsigned hco = WS_HC + (unsigned)(bm2 * 16 + 4 * w) * 512 + lane * 4;
#define G1_IN [vchk] "v"(lbase + w * 4096 + lane * 16), [voff] "v"(w * 4096 + lane * 16), [l16] "v"(lane * 16), [T] "v"(T), [xlo] "s"((unsigned)xb),       \
              [xhi] "s"((unsigned)(xb >> 32)), [dlo] "s"((unsigned)wb), [dhi] "s"((unsigned)(wb >> 32)), [m0b] "s"(lbase + w * 4096), [plo] "s"(wplo),     \
              [phi] "s"(wphi), [wlo] "s"(wslo), [whi] "s"(wshi), [hlo] "s"((unsigned)hb), [hhi] "s"((unsigned)(hb >> 32)), [cko] "v"(hco), [wsb] "s"(ws),               \
              "{v[40:47]}"(aad), "{v[32:39]}"(px)
#define G1_CL "memory", "scc", "vcc", "s36", "s37", "s38", "s39", "s40", "s41", "s42", "s43", "s44", "s45", "s46", "s47", "s50", "s51"
  f32x32 accA;
  f32x16 accB;
  f32x8 accS;
  f32x4 cs[3], bs[3];
  u32x32 h0, h1, h2;
  u32x4 hc;
#define EPB(x) asm volatile(x : "+{v[64:95]}"(h0), "+{v[96:127]}"(h1), "+{v[184:187]}"(hc)                                                     \
                            : [l16] "v"(lane * 16), [hlo] "s"((unsigned)hb), [hhi] "s"((unsigned)(hb >> 32)), [dlo] "s"((unsigned)wb),         \
                              [dhi] "s"((unsigned)(wb >> 32)), [cko] "v"(hco), [wsb] "s"(ws) : "memory", "scc", "s38", "s39", "s40", "s41", "a0", "a1", "a2", "a3", "a4", "a5", "a6", "a7", "a8", "a9", "a10", "a11", "a12", "a13", "a14", "a15", "a16", "a17", "a18", "a19", "a20", "a21", "a22", "a23", "a24", "a25", "a26", "a27", "a28", "a29", "a30", "a31", "a32", "a33", "a34", "a35", "a36", "a37", "a38", "a39", "a40", "a41", "a42", "a43", "a44", "a45", "a46", "a47", "a48", "a49", "a50", "a51", "a52", "a53", "a54", "a55", "a56", "a57", "a58", "a59", "a60", "a61", "a62", "a63", "a64", "a65", "a66", "a67", "a68", "a69", "a70", "a71", "a72", "a73", "a74", "a75", "a76", "a77", "a78", "a79", "a80", "a81", "a82", "a83", "a84", "a85", "a86", "a87", "a88", "a89", "a90", "a91", "a92", "a93", "a94", "a95", "a96", "a97", "a98", "a99", "a100", "a101", "a102", "a103", "a104", "a105", "a106", "a107", "a108", "a109", "a110", "a111", "a112", "a113", "a114", "a115", "a116", "a117", "a118", "a119", "a120", "a121", "a122", "a123", "a124", "a125", "a126", "a127", "a128", "a129", "a130", "a131", "a132", "a133", "a134", "a135", "a136", "a137", "a138", "a139", "a140", "a141", "a142", "a143", "a144", "a145", "a146", "a147", "a148", "a149", "a150", "a151", "a152", "a153", "a154", "a155", "a156", "a157", "a158", "a159", "a160", "a161", "a162", "a163", "a164", "a165", "a166", "a167", "a168", "a169", "a170", "a171", "a172", "a173", "a174", "a175", "a176", "a177", "a178", "a179", "a180", "a181", "a182", "a183", "a184", "a185", "a186", "a187", "a188", "a189", "a190", "a191", "a192", "a193", "a194", "a195", "a196", "a197", "a198", "a199", "a200", "a201", "a202", "a203", "a204", "a205", "a206", "a207", "a208", "a209", "a210", "a211", "a212", "a213", "a214", "a215", "a216", "a217", "a218", "a219", "a220", "a221", "a222", "a223", "a224", "a225", "a226", "a227", "a228", "a229", "a230", "a231", "a232", "a233", "a234", "a235", "a236", "a237", "a238", "a239")
  auto g1_epi = [&](int ps) __attribute__((always_inline)) {
    if (ps) EPB(
      "global_load_dword v184, %[cko], %[wsb] offset:0 sc1\n\t"
      "global_load_dword v185, %[cko], %[wsb] offset:512 sc1\n\t"
      "s_add_u32 s38, %[hlo], 28672\n\t"
      "s_addc_u32 s39, %[hhi], 0\n\t"
      "global_load_dwordx4 v[80:83], %[l16], s[38:39] offset:-4096 sc1\n\t"
      "global_load_dwordx4 v[84:87], %[l16], s[38:39] offset:-3072 sc1\n\t"
      "s_nop 0\n\t"
    );
    {
      const u64 m1 = __ballot(ip & 1), m2 = __ballot(ip & 2);
      float s01, s23, sq;
      asm("v_cndmask_b32 %0, %1, %2, %3" : "=v"(s01) : "v"(accS[4]), "v"(accS[5]), "s"(m1));
      asm("v_cndmask_b32 %0, %1, %2, %3" : "=v"(s23) : "v"(accS[6]), "v"(accS[7]), "s"(m1));
      asm("v_cndmask_b32 %0, %1, %2, %3" : "=v"(sq) : "v"(s01), "v"(s23), "s"(m2));
      const float mean = accS[0] * (1.0f / D_);
      float var = sq * (1.0f / D_) - mean * mean;
      var = var > 0.f ? var : 0.f;
      f32x2 rsv = {rb1(mean), __builtin_amdgcn_rcpf(rb1(__builtin_amdgcn_sqrtf(rb1(rb1(var) + 1e-5f))))};
      asm volatile("" : "+v"(rsv));
      if (fq == q4) LDSF2(lds + L_RS + (16 * w + ip) * 8) = rsv;
    }
#pragma unroll
    for (int n = 0; n < 3; ++n) {
      cs[n] = LDSF4(lds + L_CB + (w * 24 + 4 * n + fq) * 16);
      bs[n] = LDSF4(lds + L_CB + (w * 24 + 12 + 4 * n + fq) * 16);
    }
    asm volatile("" : "+v"(cs[0]), "+v"(cs[1]), "+v"(cs[2]), "+v"(bs[0]), "+v"(bs[1]), "+v"(bs[2]));
    if (ps) EPB(
      "s_add_u32 s38, %[hlo], 28672\n\t"
      "s_addc_u32 s39, %[hhi], 0\n\t"
      "global_load_dwordx4 v[88:91], %[l16], s[38:39] offset:0 sc1\n\t"
      "global_load_dwordx4 v[92:95], %[l16], s[38:39] offset:1024 sc1\n\t"
      "s_add_u32 s38, %[hlo], 36864\n\t"
      "s_addc_u32 s39, %[hhi], 0\n\t"
      "global_load_dwordx4 v[96:99], %[l16], s[38:39] offset:-4096 sc1\n\t"
      "global_load_dwordx4 v[100:103], %[l16], s[38:39] offset:-3072 sc1\n\t"
      "s_nop 0\n\t"
    );
    BAR();
    f32x2 r[4];
#pragma unroll
    for (int j = 0; j < 4; ++j) r[j] = LDSF2(lds + L_RS + (16 * (j ^ w) + ip) * 8);
#pragma unroll
    for (int j = 0; j < 4; ++j) {
      if (ps && j == 0) EPB(
      "global_load_dword v186, %[cko], %[wsb] offset:1024 sc1\n\t"
      "s_add_u32 s38, %[hlo], 40960\n\t"
      "s_addc_u32 s39, %[hhi], 0\n\t"
      "global_load_dwordx4 v[104:107], %[l16], s[38:39] offset:-4096 sc1\n\t"
      "global_load_dwordx4 v[108:111], %[l16], s[38:39] offset:-3072 sc1\n\t"
      "global_load_dwordx4 v[112:115], %[l16], s[38:39] offset:0 sc1\n\t"
      "s_nop 0\n\t"
      );
      if (ps && j == 2) EPB(
      "s_add_u32 s38, %[hlo], 40960\n\t"
      "s_addc_u32 s39, %[hhi], 0\n\t"
      "global_load_dwordx4 v[116:119], %[l16], s[38:39] offset:1024 sc1\n\t"
      "s_add_u32 s38, %[hlo], 49152\n\t"
      "s_addc_u32 s39, %[hhi], 0\n\t"
      "global_load_dwordx4 v[120:123], %[l16], s[38:39] offset:-4096 sc1\n\t"
      "global_load_dwordx4 v[124:127], %[l16], s[38:39] offset:-3072 sc1\n\t"
      "global_load_dword v187, %[cko], %[wsb] offset:1536 sc1\n\t"
      "s_nop 0\n\t"
      );
      lds_char* p = lds + L_HX + ((j ^ w) * 192 + lane) * 16 + 4 * w;
#pragma unroll
      for (int n = 0; n < 3; ++n) {
        const int o = 16 * n + 4 * j;
        const f32x4 a = n < 2 ? (f32x4){accA[o & 31], accA[(o + 1) & 31], accA[(o + 2) & 31], accA[(o + 3) & 31]}
                              : (f32x4){accB[o & 15], accB[(o + 1) & 15], accB[(o + 2) & 15], accB[(o + 3) & 15]};
        const f32x4 g = (a - r[j][0] * cs[n]) * r[j][1] + bs[n];
        LDS32(p + n * 1024) = pk2(g[0] * g[2], g[1] * g[3]);
      }
    }
    BAR();
    unsigned c = T[0];
#pragma unroll
    for (int i = 0; i < 6; ++i) {
      const u32x4 d = LDS128(lds + L_HX + ((3 * (i < 3 ? w : w ^ 1) + i % 3) * 64 + lane) * 16);
      if (i < 3) ST128(hho + i * 4096 + ps * 2048, d);
      c += (d[0] + d[1]) + (d[2] + d[3]);
    }
    ST32((w & 1) ? 0x80000000u : WS_HC + (unsigned)((2 * bm + (w >> 1)) * 16 + bn) * 512 + ps * 256 + lane * 4, c);
    SCHEDB();
  };
  {
    f32x32 pa;
    f32x8 pb;
    u32x2 fdv;
    u32x16 gv;
    asm volatile(
      "s_mov_b32 s45, m0\n\t"
      "s_mov_b32 s44, 0x40000\n\t"
      "v_add_u32 v57, 0x10000, %[vchk]\n\t"
      "v_mov_b32 v52, 0x3f803f80\n\t"
      "v_mov_b32 v53, 0x3f803f80\n\t"
      "v_mov_b32 v54, 0x3f803f80\n\t"
      "v_mov_b32 v55, 0x3f803f80\n\t"
      "s_waitcnt vmcnt(4)\n\t"
      "ds_read_b128 v[240:243], %[vchk] offset:0\n\t"
      "ds_read_b128 v[244:247], %[vchk] offset:1024\n\t"
      "ds_read_b128 v[248:251], %[vchk] offset:2048\n\t"
      "ds_read_b128 v[252:255], %[vchk] offset:3072\n\t"
      "s_waitcnt lgkmcnt(0)\n\t"
      "v_max3_u32 v56, v240, v241, v242\n\t"
      "v_max3_u32 v56, v56, v243, v244\n\t"
      "v_max3_u32 v56, v56, v245, v246\n\t"
      "v_max3_u32 v56, v56, v247, v248\n\t"
      "v_max3_u32 v56, v56, v249, v250\n\t"
      "v_max3_u32 v56, v56, v251, v252\n\t"
      "v_max3_u32 v56, v56, v253, v254\n\t"
      "v_max_u32 v56, v56, v255\n\t"
      "v_cmp_eq_u32 vcc, -1, v56\n\t"
      "s_cbranch_vccnz L_g1_slow0_%=\n\t"
      "L_g1_back0_%=:\n\t"
      "s_barrier\n\t"
      "ds_read_b64_tr_b16 v[64:65], v40 offset:0\n\t"
      "ds_read_b64_tr_b16 v[66:67], v40 offset:2048\n\t"
      "ds_read_b64_tr_b16 v[68:69], v41 offset:0\n\t"
      "ds_read_b64_tr_b16 v[70:71], v41 offset:2048\n\t"
      "ds_read_b64_tr_b16 v[72:73], v42 offset:0\n\t"
      "ds_read_b64_tr_b16 v[74:75], v42 offset:2048\n\t"
      "ds_read_b64_tr_b16 v[76:77], v43 offset:0\n\t"
      "ds_read_b64_tr_b16 v[78:79], v43 offset:2048\n\t"
      "ds_read_b64_tr_b16 v[80:81], v40 offset:4096\n\t"
      "ds_read_b64_tr_b16 v[82:83], v40 offset:6144\n\t"
      "ds_read_b64_tr_b16 v[84:85], v41 offset:4096\n\t"
      "ds_read_b64_tr_b16 v[86:87], v41 offset:6144\n\t"
      "ds_read_b64_tr_b16 v[88:89], v42 offset:4096\n\t"
      "ds_read_b64_tr_b16 v[90:91], v42 offset:6144\n\t"
      "ds_read_b64_tr_b16 v[92:93], v43 offset:4096\n\t"
      "ds_read_b64_tr_b16 v[94:95], v43 offset:6144\n\t"
      "Lpd74eef_39:\n\t"
      "s_waitcnt lgkmcnt(0)\n\t"
      "v_mfma_f32_16x16x32_bf16 v[128:131], a[0:3], v[64:67], 0\n\t"
      "ds_read_b64_tr_b16 v[96:97], v40 offset:8192\n\t"
      "s_add_u32 m0, %[m0b], 32768\n\t"
      "s_add_u32 s36, %[xlo], 32768\n\t"
      "v_mfma_f32_16x16x32_bf16 v[144:147], a[4:7], v[64:67], 0\n\t"
      "ds_read_b64_tr_b16 v[98:99], v40 offset:10240\n\t"
      "v_mfma_f32_16x16x32_bf16 v[160:163], a[8:11], v[64:67], 0\n\t"
      "s_addc_u32 s37, %[xhi], 0\n\t"
      "global_load_lds_dwordx4 %[voff], s[36:37] offset:0 sc1\n\t"
      "v_mfma_f32_16x16x32_bf16 v[176:179], v[52:55], v[64:67], 0\n\t"
      "ds_read_b64_tr_b16 v[100:101], v41 offset:8192\n\t"
      "ds_read_b64_tr_b16 v[102:103], v41 offset:10240\n\t"
      "ds_read_b64_tr_b16 v[104:105], v42 offset:8192\n\t"
      "v_mfma_f32_16x16x32_bf16 v[180:183], v[64:67], v[64:67], 0\n\t"
      "global_load_lds_dwordx4 %[voff], s[36:37] offset:1024 sc1\n\t"
      "ds_read_b64_tr_b16 v[106:107], v42 offset:10240\n\t"
      "v_mfma_f32_16x16x32_bf16 v[132:135], a[0:3], v[68:71], 0\n\t"
      "ds_read_b64_tr_b16 v[108:109], v43 offset:8192\n\t"
      "s_add_u32 s38, %[plo], 28672\n\t"
      "s_addc_u32 s39, %[phi], 0\n\t"
      "v_mfma_f32_16x16x32_bf16 v[148:151], a[4:7], v[68:71], 0\n\t"
      "ds_read_b64_tr_b16 v[110:111], v43 offset:10240\n\t"
      "global_load_dwordx4 a[96:99], %[l16], s[38:39] offset:-4096 sc1\n\t"
      "v_mfma_f32_16x16x32_bf16 v[164:167], a[8:11], v[68:71], 0\n\t"
      "ds_read_b64_tr_b16 v[112:113], v40 offset:12288\n\t"
      "ds_read_b64_tr_b16 v[114:115], v40 offset:14336\n\t"
      "v_mfma_f32_16x16x32_bf16 v[136:139], a[0:3], v[72:75], 0\n\t"
      "global_load_dwordx4 a[100:103], %[l16], s[38:39] offset:-3072 sc1\n\t"
      "ds_read_b64_tr_b16 v[116:117], v41 offset:12288\n\t"
      "v_mfma_f32_16x16x32_bf16 v[152:155], a[4:7], v[72:75], 0\n\t"
      "ds_read_b64_tr_b16 v[118:119], v41 offset:14336\n\t"
      "global_load_dwordx4 a[104:107], %[l16], s[38:39] offset:-2048 sc1\n\t"
      "v_mfma_f32_16x16x32_bf16 v[168:171], a[8:11], v[72:75], 0\n\t"
      "ds_read_b64_tr_b16 v[120:121], v42 offset:12288\n\t"
      "ds_read_b64_tr_b16 v[122:123], v42 offset:14336\n\t"
      "v_mfma_f32_16x16x32_bf16 v[140:143], a[0:3], v[76:79], 0\n\t"
      "global_load_dwordx4 a[108:111], %[l16], s[38:39] offset:-1024 sc1\n\t"
      "ds_read_b64_tr_b16 v[124:125], v43 offset:12288\n\t"
      "v_mfma_f32_16x16x32_bf16 v[156:159], a[4:7], v[76:79], 0\n\t"
      "global_load_dwordx4 a[112:115], %[l16], s[38:39] offset:0 sc1\n\t"
      "ds_read_b64_tr_b16 v[126:127], v43 offset:14336\n\t"
      "v_mfma_f32_16x16x32_bf16 v[172:175], a[8:11], v[76:79], 0\n\t"
      "ds_read_b64_tr_b16 v[184:185], v33 offset:0\n\t"
      "ds_read_b64_tr_b16 v[186:187], v33 offset:1024\n\t"
      "ds_read_b64_tr_b16 v[188:189], v33 offset:32\n\t"
      "v_mfma_f32_16x16x32_bf16 v[128:131], a[12:15], v[80:83], v[128:131]\n\t"
      "ds_read_b64_tr_b16 v[190:191], v33 offset:1056\n\t"
      "v_mfma_f32_16x16x32_bf16 v[144:147], a[16:19], v[80:83], v[144:147]\n\t"
      "v_mfma_f32_16x16x32_bf16 v[160:163], a[20:23], v[80:83], v[160:163]\n\t"
      "v_mfma_f32_16x16x32_bf16 v[176:179], v[52:55], v[80:83], v[176:179]\n\t"
      "s_waitcnt vmcnt(7)\n\t"
      "ds_read_b128 v[240:243], %[vchk] offset:16384\n\t"
      "v_mfma_f32_16x16x32_bf16 v[180:183], v[80:83], v[80:83], v[180:183]\n\t"
      "ds_read_b128 v[244:247], %[vchk] offset:17408\n\t"
      "ds_read_b128 v[248:251], %[vchk] offset:18432\n\t"
      "v_mfma_f32_16x16x32_bf16 v[132:135], a[12:15], v[84:87], v[132:135]\n\t"
      "ds_read_b128 v[252:255], %[vchk] offset:19456\n\t"
      "v_mfma_f32_16x16x32_bf16 v[148:151], a[16:19], v[84:87], v[148:151]\n\t"
      "v_mfma_f32_16x16x32_bf16 v[164:167], a[20:23], v[84:87], v[164:167]\n\t"
      "v_mfma_f32_16x16x32_bf16 v[136:139], a[12:15], v[88:91], v[136:139]\n\t"
      "s_waitcnt lgkmcnt(0)\n\t"
      "v_max3_u32 v56, v240, v241, v242\n\t"
      "v_mfma_f32_16x16x32_bf16 v[152:155], a[16:19], v[88:91], v[152:155]\n\t"
      "v_max3_u32 v56, v56, v243, v244\n\t"
      "v_max3_u32 v56, v56, v245, v246\n\t"
      "v_mfma_f32_16x16x32_bf16 v[168:171], a[20:23], v[88:91], v[168:171]\n\t"
      "v_max3_u32 v56, v56, v247, v248\n\t"
      "v_max3_u32 v56, v56, v249, v250\n\t"
      "v_mfma_f32_16x16x32_bf16 v[140:143], a[12:15], v[92:95], v[140:143]\n\t"
      "v_max3_u32 v56, v56, v251, v252\n\t"
      "v_max3_u32 v56, v56, v253, v254\n\t"
      "v_mfma_f32_16x16x32_bf16 v[156:159], a[16:19], v[92:95], v[156:159]\n\t"
      "v_max_u32 v56, v56, v255\n\t"
      "v_cmp_eq_u32 vcc, -1, v56\n\t"
      "v_mfma_f32_16x16x32_bf16 v[172:175], a[20:23], v[92:95], v[172:175]\n\t"
      "s_cbranch_vccnz L_g1_slow1_%=\n\t"
      "L_g1_back1_%=:\n\t"
      "Lpd74eef_40:\n\t"
      "s_waitcnt lgkmcnt(0)\n\t"
      "s_barrier\n\t"
      "v_mfma_f32_16x16x32_bf16 v[128:131], a[24:27], v[96:99], v[128:131]\n\t"
      "ds_read_b64_tr_b16 v[64:65], v40 offset:16384\n\t"
      "v_mfma_f32_16x16x32_bf16 v[144:147], a[28:31], v[96:99], v[144:147]\n\t"
      "s_add_u32 m0, %[m0b], 32768\n\t"
      "s_add_u32 s36, %[xlo], 32768\n\t"
      "v_mfma_f32_16x16x32_bf16 v[160:163], a[32:35], v[96:99], v[160:163]\n\t"
      "ds_read_b64_tr_b16 v[66:67], v40 offset:18432\n\t"
      "v_mfma_f32_16x16x32_bf16 v[176:179], v[52:55], v[96:99], v[176:179]\n\t"
      "s_addc_u32 s37, %[xhi], 0\n\t"
      "global_load_lds_dwordx4 %[voff], s[36:37] offset:2048 sc1\n\t"
      "v_mfma_f32_16x16x32_bf16 v[180:183], v[96:99], v[96:99], v[180:183]\n\t"
      "ds_read_b64_tr_b16 v[68:69], v41 offset:16384\n\t"
      "ds_read_b64_tr_b16 v[70:71], v41 offset:18432\n\t"
      "v_mfma_f32_16x16x32_bf16 v[132:135], a[24:27], v[100:103], v[132:135]\n\t"
      "ds_read_b64_tr_b16 v[72:73], v42 offset:16384\n\t"
      "v_mfma_f32_16x16x32_bf16 v[148:151], a[28:31], v[100:103], v[148:151]\n\t"
      "global_load_lds_dwordx4 %[voff], s[36:37] offset:3072 sc1\n\t"
      "v_mfma_f32_16x16x32_bf16 v[164:167], a[32:35], v[100:103], v[164:167]\n\t"
      "ds_read_b64_tr_b16 v[74:75], v42 offset:18432\n\t"
      "v_mfma_f32_16x16x32_bf16 v[136:139], a[24:27], v[104:107], v[136:139]\n\t"
      "global_load_dwordx4 a[116:119], %[l16], s[38:39] offset:1024 sc1\n\t"
      "v_mfma_f32_16x16x32_bf16 v[152:155], a[28:31], v[104:107], v[152:155]\n\t"
      "ds_read_b64_tr_b16 v[76:77], v43 offset:16384\n\t"
      "ds_read_b64_tr_b16 v[78:79], v43 offset:18432\n\t"
      "v_mfma_f32_16x16x32_bf16 v[168:171], a[32:35], v[104:107], v[168:171]\n\t"
      "global_load_dwordx4 a[120:123], %[l16], s[38:39] offset:2048 sc1\n\t"
      "v_mfma_f32_16x16x32_bf16 v[140:143], a[24:27], v[108:111], v[140:143]\n\t"
      "ds_read_b64_tr_b16 v[80:81], v40 offset:20480\n\t"
      "v_mfma_f32_16x16x32_bf16 v[156:159], a[28:31], v[108:111], v[156:159]\n\t"
      "global_load_dwordx4 a[124:127], %[l16], s[38:39] offset:3072 sc1\n\t"
      "v_mfma_f32_16x16x32_bf16 v[172:175], a[32:35], v[108:111], v[172:175]\n\t"
      "ds_read_b64_tr_b16 v[82:83], v40 offset:22528\n\t"
      "ds_read_b64_tr_b16 v[84:85], v41 offset:20480\n\t"
      "v_mfma_f32_16x16x32_bf16 v[128:131], a[36:39], v[112:115], v[128:131]\n\t"
      "s_add_u32 s38, %[plo], 36864\n\t"
      "s_addc_u32 s39, %[phi], 0\n\t"
      "v_mfma_f32_16x16x32_bf16 v[144:147], a[40:43], v[112:115], v[144:147]\n\t"
      "ds_read_b64_tr_b16 v[86:87], v41 offset:22528\n\t"
      "v_mfma_f32_16x16x32_bf16 v[160:163], a[44:47], v[112:115], v[160:163]\n\t"
      "global_load_dwordx4 a[128:131], %[l16], s[38:39] offset:-4096 sc1\n\t"
      "v_mfma_f32_16x16x32_bf16 v[176:179], v[52:55], v[112:115], v[176:179]\n\t"
      "ds_read_b64_tr_b16 v[88:89], v42 offset:20480\n\t"
      "ds_read_b64_tr_b16 v[90:91], v42 offset:22528\n\t"
      "v_mfma_f32_16x16x32_bf16 v[180:183], v[112:115], v[112:115], v[180:183]\n\t"
      "global_load_dwordx4 a[132:135], %[l16], s[38:39] offset:-3072 sc1\n\t"
      "v_mfma_f32_16x16x32_bf16 v[132:135], a[36:39], v[116:119], v[132:135]\n\t"
      "ds_read_b64_tr_b16 v[92:93], v43 offset:20480\n\t"
      "ds_read_b64_tr_b16 v[94:95], v43 offset:22528\n\t"
      "v_mfma_f32_16x16x32_bf16 v[148:151], a[40:43], v[116:119], v[148:151]\n\t"
      "s_mov_b32 s42, %[wlo]\n\t"
      "s_mov_b32 s43, %[whi]\n\t"
      "v_mfma_f32_16x16x32_bf16 v[164:167], a[44:47], v[116:119], v[164:167]\n\t"
      "global_store_dwordx4 v34, v[184:187], s[42:43] sc1\n\t"
      "v_mfma_f32_16x16x32_bf16 v[136:139], a[36:39], v[120:123], v[136:139]\n\t"
      "global_store_dwordx4 v35, v[188:191], s[42:43] sc1\n\t"
      "v_mfma_f32_16x16x32_bf16 v[152:155], a[40:43], v[120:123], v[152:155]\n\t"
      "v_mfma_f32_16x16x32_bf16 v[168:171], a[44:47], v[120:123], v[168:171]\n\t"
      "v_mfma_f32_16x16x32_bf16 v[140:143], a[36:39], v[124:127], v[140:143]\n\t"
      "v_mfma_f32_16x16x32_bf16 v[156:159], a[40:43], v[124:127], v[156:159]\n\t"
      "v_mfma_f32_16x16x32_bf16 v[172:175], a[44:47], v[124:127], v[172:175]\n\t"
      "Lpd74eef_41:\n\t"
      "s_waitcnt lgkmcnt(0)\n\t"
      "v_mfma_f32_16x16x32_bf16 v[128:131], a[48:51], v[64:67], v[128:131]\n\t"
      "ds_read_b64_tr_b16 v[96:97], v40 offset:24576\n\t"
      "v_mfma_f32_16x16x32_bf16 v[144:147], a[52:55], v[64:67], v[144:147]\n\t"
      "s_add_u32 m0, %[m0b], 49152\n\t"
      "s_add_u32 s36, %[xlo], 49152\n\t"
      "v_mfma_f32_16x16x32_bf16 v[160:163], a[56:59], v[64:67], v[160:163]\n\t"
      "ds_read_b64_tr_b16 v[98:99], v40 offset:26624\n\t"
      "ds_read_b64_tr_b16 v[100:101], v41 offset:24576\n\t"
      "v_mfma_f32_16x16x32_bf16 v[176:179], v[52:55], v[64:67], v[176:179]\n\t"
      "s_addc_u32 s37, %[xhi], 0\n\t"
      "global_load_lds_dwordx4 %[voff], s[36:37] offset:0 sc1\n\t"
      "v_mfma_f32_16x16x32_bf16 v[180:183], v[64:67], v[64:67], v[180:183]\n\t"
      "ds_read_b64_tr_b16 v[102:103], v41 offset:26624\n\t"
      "ds_read_b64_tr_b16 v[104:105], v42 offset:24576\n\t"
      "v_mfma_f32_16x16x32_bf16 v[132:135], a[48:51], v[68:71], v[132:135]\n\t"
      "ds_read_b64_tr_b16 v[106:107], v42 offset:26624\n\t"
      "v_mfma_f32_16x16x32_bf16 v[148:151], a[52:55], v[68:71], v[148:151]\n\t"
      "global_load_lds_dwordx4 %[voff], s[36:37] offset:1024 sc1\n\t"
      "v_mfma_f32_16x16x32_bf16 v[164:167], a[56:59], v[68:71], v[164:167]\n\t"
      "ds_read_b64_tr_b16 v[108:109], v43 offset:24576\n\t"
      "ds_read_b64_tr_b16 v[110:111], v43 offset:26624\n\t"
      "v_mfma_f32_16x16x32_bf16 v[136:139], a[48:51], v[72:75], v[136:139]\n\t"
      "ds_read_b64_tr_b16 v[112:113], v40 offset:28672\n\t"
      "v_mfma_f32_16x16x32_bf16 v[152:155], a[52:55], v[72:75], v[152:155]\n\t"
      "global_load_dwordx4 a[136:139], %[l16], s[38:39] offset:-2048 sc1\n\t"
      "v_mfma_f32_16x16x32_bf16 v[168:171], a[56:59], v[72:75], v[168:171]\n\t"
      "ds_read_b64_tr_b16 v[114:115], v40 offset:30720\n\t"
      "ds_read_b64_tr_b16 v[116:117], v41 offset:28672\n\t"
      "v_mfma_f32_16x16x32_bf16 v[140:143], a[48:51], v[76:79], v[140:143]\n\t"
      "global_load_dwordx4 a[140:143], %[l16], s[38:39] offset:-1024 sc1\n\t"
      "v_mfma_f32_16x16x32_bf16 v[156:159], a[52:55], v[76:79], v[156:159]\n\t"
      "ds_read_b64_tr_b16 v[118:119], v41 offset:30720\n\t"
      "ds_read_b64_tr_b16 v[120:121], v42 offset:28672\n\t"
      "v_mfma_f32_16x16x32_bf16 v[172:175], a[56:59], v[76:79], v[172:175]\n\t"
      "global_load_dwordx4 a[144:147], %[l16], s[38:39] offset:0 sc1\n\t"
      "v_mfma_f32_16x16x32_bf16 v[128:131], a[60:63], v[80:83], v[128:131]\n\t"
      "ds_read_b64_tr_b16 v[122:123], v42 offset:30720\n\t"
      "ds_read_b64_tr_b16 v[124:125], v43 offset:28672\n\t"
      "v_mfma_f32_16x16x32_bf16 v[144:147], a[64:67], v[80:83], v[144:147]\n\t"
      "ds_read_b64_tr_b16 v[126:127], v43 offset:30720\n\t"
      "v_mfma_f32_16x16x32_bf16 v[160:163], a[68:71], v[80:83], v[160:163]\n\t"
      "global_load_dwordx4 a[148:151], %[l16], s[38:39] offset:1024 sc1\n\t"
      "v_mfma_f32_16x16x32_bf16 v[176:179], v[52:55], v[80:83], v[176:179]\n\t"
      "s_waitcnt vmcnt(13)\n\t"
      "ds_read_b128 v[240:243], %[vchk] offset:32768\n\t"
      "v_mfma_f32_16x16x32_bf16 v[180:183], v[80:83], v[80:83], v[180:183]\n\t"
      "ds_read_b128 v[244:247], %[vchk] offset:33792\n\t"
      "ds_read_b128 v[248:251], %[vchk] offset:34816\n\t"
      "v_mfma_f32_16x16x32_bf16 v[132:135], a[60:63], v[84:87], v[132:135]\n\t"
      "ds_read_b128 v[252:255], %[vchk] offset:35840\n\t"
      "v_mfma_f32_16x16x32_bf16 v[148:151], a[64:67], v[84:87], v[148:151]\n\t"
      "global_load_dwordx4 a[152:155], %[l16], s[38:39] offset:2048 sc1\n\t"
      "v_mfma_f32_16x16x32_bf16 v[164:167], a[68:71], v[84:87], v[164:167]\n\t"
      "v_mfma_f32_16x16x32_bf16 v[136:139], a[60:63], v[88:91], v[136:139]\n\t"
      "s_waitcnt lgkmcnt(0)\n\t"
      "v_max3_u32 v56, v240, v241, v242\n\t"
      "v_mfma_f32_16x16x32_bf16 v[152:155], a[64:67], v[88:91], v[152:155]\n\t"
      "v_max3_u32 v56, v56, v243, v244\n\t"
      "v_max3_u32 v56, v56, v245, v246\n\t"
      "v_mfma_f32_16x16x32_bf16 v[168:171], a[68:71], v[88:91], v[168:171]\n\t"
      "v_max3_u32 v56, v56, v247, v248\n\t"
      "v_max3_u32 v56, v56, v249, v250\n\t"
      "v_mfma_f32_16x16x32_bf16 v[140:143], a[60:63], v[92:95], v[140:143]\n\t"
      "v_max3_u32 v56, v56, v251, v252\n\t"
      "v_max3_u32 v56, v56, v253, v254\n\t"
      "v_mfma_f32_16x16x32_bf16 v[156:159], a[64:67], v[92:95], v[156:159]\n\t"
      "v_max_u32 v56, v56, v255\n\t"
      "v_cmp_eq_u32 vcc, -1, v56\n\t"
      "v_mfma_f32_16x16x32_bf16 v[172:175], a[68:71], v[92:95], v[172:175]\n\t"
      "s_cbranch_vccnz L_g1_slow2_%=\n\t"
      "L_g1_back2_%=:\n\t"
      "Lpd74eef_42:\n\t"
      "s_waitcnt lgkmcnt(0)\n\t"
      "s_barrier\n\t"
      "v_mfma_f32_16x16x32_bf16 v[128:131], a[72:75], v[96:99], v[128:131]\n\t"
      "ds_read_b64_tr_b16 v[64:65], v40 offset:32768\n\t"
      "v_mfma_f32_16x16x32_bf16 v[144:147], a[76:79], v[96:99], v[144:147]\n\t"
      "s_add_u32 m0, %[m0b], 49152\n\t"
      "s_add_u32 s36, %[xlo], 49152\n\t"
      "v_mfma_f32_16x16x32_bf16 v[160:163], a[80:83], v[96:99], v[160:163]\n\t"
      "ds_read_b64_tr_b16 v[66:67], v40 offset:34816\n\t"
      "ds_read_b64_tr_b16 v[68:69], v41 offset:32768\n\t"
      "v_mfma_f32_16x16x32_bf16 v[176:179], v[52:55], v[96:99], v[176:179]\n\t"
      "s_addc_u32 s37, %[xhi], 0\n\t"
      "global_load_lds_dwordx4 %[voff], s[36:37] offset:2048 sc1\n\t"
      "v_mfma_f32_16x16x32_bf16 v[180:183], v[96:99], v[96:99], v[180:183]\n\t"
      "ds_read_b64_tr_b16 v[70:71], v41 offset:34816\n\t"
      "ds_read_b64_tr_b16 v[72:73], v42 offset:32768\n\t"
      "v_mfma_f32_16x16x32_bf16 v[132:135], a[72:75], v[100:103], v[132:135]\n\t"
      "ds_read_b64_tr_b16 v[74:75], v42 offset:34816\n\t"
      "v_mfma_f32_16x16x32_bf16 v[148:151], a[76:79], v[100:103], v[148:151]\n\t"
      "global_load_lds_dwordx4 %[voff], s[36:37] offset:3072 sc1\n\t"
      "v_mfma_f32_16x16x32_bf16 v[164:167], a[80:83], v[100:103], v[164:167]\n\t"
      "ds_read_b64_tr_b16 v[76:77], v43 offset:32768\n\t"
      "ds_read_b64_tr_b16 v[78:79], v43 offset:34816\n\t"
      "v_mfma_f32_16x16x32_bf16 v[136:139], a[72:75], v[104:107], v[136:139]\n\t"
      "global_load_dwordx4 a[156:159], %[l16], s[38:39] offset:3072 sc1\n\t"
      "v_mfma_f32_16x16x32_bf16 v[152:155], a[76:79], v[104:107], v[152:155]\n\t"
      "ds_read_b64_tr_b16 v[80:81], v40 offset:36864\n\t"
      "ds_read_b64_tr_b16 v[82:83], v40 offset:38912\n\t"
      "v_mfma_f32_16x16x32_bf16 v[168:171], a[80:83], v[104:107], v[168:171]\n\t"
      "s_add_u32 s38, %[plo], 45056\n\t"
      "s_addc_u32 s39, %[phi], 0\n\t"
      "v_mfma_f32_16x16x32_bf16 v[140:143], a[72:75], v[108:111], v[140:143]\n\t"
      "ds_read_b64_tr_b16 v[84:85], v41 offset:36864\n\t"
      "ds_read_b64_tr_b16 v[86:87], v41 offset:38912\n\t"
      "v_mfma_f32_16x16x32_bf16 v[156:159], a[76:79], v[108:111], v[156:159]\n\t"
      "global_load_dwordx4 a[160:163], %[l16], s[38:39] offset:-4096 sc1\n\t"
      "v_mfma_f32_16x16x32_bf16 v[172:175], a[80:83], v[108:111], v[172:175]\n\t"
      "ds_read_b64_tr_b16 v[88:89], v42 offset:36864\n\t"
      "ds_read_b64_tr_b16 v[90:91], v42 offset:38912\n\t"
      "v_mfma_f32_16x16x32_bf16 v[128:131], a[84:87], v[112:115], v[128:131]\n\t"
      "global_load_dwordx4 a[164:167], %[l16], s[38:39] offset:-3072 sc1\n\t"
      "v_mfma_f32_16x16x32_bf16 v[144:147], a[88:91], v[112:115], v[144:147]\n\t"
      "ds_read_b64_tr_b16 v[92:93], v43 offset:36864\n\t"
      "ds_read_b64_tr_b16 v[94:95], v43 offset:38912\n\t"
      "v_mfma_f32_16x16x32_bf16 v[160:163], a[92:95], v[112:115], v[160:163]\n\t"
      "global_load_dwordx4 a[168:171], %[l16], s[38:39] offset:-2048 sc1\n\t"
      "v_mfma_f32_16x16x32_bf16 v[176:179], v[52:55], v[112:115], v[176:179]\n\t"
      "global_load_dwordx4 a[172:175], %[l16], s[38:39] offset:-1024 sc1\n\t"
      "v_mfma_f32_16x16x32_bf16 v[180:183], v[112:115], v[112:115], v[180:183]\n\t"
      "v_mfma_f32_16x16x32_bf16 v[132:135], a[84:87], v[116:119], v[132:135]\n\t"
      "v_mfma_f32_16x16x32_bf16 v[148:151], a[88:91], v[116:119], v[148:151]\n\t"
      "v_mfma_f32_16x16x32_bf16 v[164:167], a[92:95], v[116:119], v[164:167]\n\t"
      "v_mfma_f32_16x16x32_bf16 v[136:139], a[84:87], v[120:123], v[136:139]\n\t"
      "v_mfma_f32_16x16x32_bf16 v[152:155], a[88:91], v[120:123], v[152:155]\n\t"
      "v_mfma_f32_16x16x32_bf16 v[168:171], a[92:95], v[120:123], v[168:171]\n\t"
      "v_mfma_f32_16x16x32_bf16 v[140:143], a[84:87], v[124:127], v[140:143]\n\t"
      "v_mfma_f32_16x16x32_bf16 v[156:159], a[88:91], v[124:127], v[156:159]\n\t"
      "v_mfma_f32_16x16x32_bf16 v[172:175], a[92:95], v[124:127], v[172:175]\n\t"
      "Lpd74eef_43:\n\t"
      "s_waitcnt lgkmcnt(0)\n\t"
      "s_waitcnt vmcnt(20)\n\t"
      "v_mfma_f32_16x16x32_bf16 v[128:131], a[96:99], v[64:67], v[128:131]\n\t"
      "ds_read_b64_tr_b16 v[96:97], v40 offset:40960\n\t"
      "s_add_u32 m0, %[m0b], 65536\n\t"
      "s_add_u32 s36, %[xlo], 65536\n\t"
      "v_mfma_f32_16x16x32_bf16 v[144:147], a[100:103], v[64:67], v[144:147]\n\t"
      "ds_read_b64_tr_b16 v[98:99], v40 offset:43008\n\t"
      "v_mfma_f32_16x16x32_bf16 v[160:163], a[104:107], v[64:67], v[160:163]\n\t"
      "s_addc_u32 s37, %[xhi], 0\n\t"
      "global_load_lds_dwordx4 %[voff], s[36:37] offset:0 sc1\n\t"
      "v_mfma_f32_16x16x32_bf16 v[176:179], v[52:55], v[64:67], v[176:179]\n\t"
      "ds_read_b64_tr_b16 v[100:101], v41 offset:40960\n\t"
      "ds_read_b64_tr_b16 v[102:103], v41 offset:43008\n\t"
      "v_mfma_f32_16x16x32_bf16 v[180:183], v[64:67], v[64:67], v[180:183]\n\t"
      "global_load_lds_dwordx4 %[voff], s[36:37] offset:1024 sc1\n\t"
      "ds_read_b64_tr_b16 v[104:105], v42 offset:40960\n\t"
      "v_mfma_f32_16x16x32_bf16 v[132:135], a[96:99], v[68:71], v[132:135]\n\t"
      "ds_read_b64_tr_b16 v[106:107], v42 offset:43008\n\t"
      "global_load_dwordx4 a[176:179], %[l16], s[38:39] offset:0 sc1\n\t"
      "v_mfma_f32_16x16x32_bf16 v[148:151], a[100:103], v[68:71], v[148:151]\n\t"
      "ds_read_b64_tr_b16 v[108:109], v43 offset:40960\n\t"
      "global_load_dwordx4 a[180:183], %[l16], s[38:39] offset:1024 sc1\n\t"
      "v_mfma_f32_16x16x32_bf16 v[164:167], a[104:107], v[68:71], v[164:167]\n\t"
      "ds_read_b64_tr_b16 v[110:111], v43 offset:43008\n\t"
      "ds_read_b64_tr_b16 v[112:113], v40 offset:45056\n\t"
      "v_mfma_f32_16x16x32_bf16 v[136:139], a[96:99], v[72:75], v[136:139]\n\t"
      "global_load_dwordx4 a[184:187], %[l16], s[38:39] offset:2048 sc1\n\t"
      "ds_read_b64_tr_b16 v[114:115], v40 offset:47104\n\t"
      "v_mfma_f32_16x16x32_bf16 v[152:155], a[100:103], v[72:75], v[152:155]\n\t"
      "ds_read_b64_tr_b16 v[116:117], v41 offset:45056\n\t"
      "global_load_dwordx4 a[188:191], %[l16], s[38:39] offset:3072 sc1\n\t"
      "v_mfma_f32_16x16x32_bf16 v[168:171], a[104:107], v[72:75], v[168:171]\n\t"
      "ds_read_b64_tr_b16 v[118:119], v41 offset:47104\n\t"
      "s_add_u32 s38, %[plo], 53248\n\t"
      "s_addc_u32 s39, %[phi], 0\n\t"
      "v_mfma_f32_16x16x32_bf16 v[140:143], a[96:99], v[76:79], v[140:143]\n\t"
      "ds_read_b64_tr_b16 v[120:121], v42 offset:45056\n\t"
      "ds_read_b64_tr_b16 v[122:123], v42 offset:47104\n\t"
      "v_mfma_f32_16x16x32_bf16 v[156:159], a[100:103], v[76:79], v[156:159]\n\t"
      "global_load_dwordx4 a[192:195], %[l16], s[38:39] offset:-4096 sc1\n\t"
      "ds_read_b64_tr_b16 v[124:125], v43 offset:45056\n\t"
      "v_mfma_f32_16x16x32_bf16 v[172:175], a[104:107], v[76:79], v[172:175]\n\t"
      "s_mov_b32 s42, %[wlo]\n\t"
      "s_mov_b32 s43, %[whi]\n\t"
      "s_waitcnt vmcnt(21)\n\t"
      "s_mov_b64 exec, 0xff\n\t"
      "global_store_dwordx2 v32, %[T], s[42:43] sc1\n\t"
      "s_mov_b64 exec, -1\n\t"
      "v_mfma_f32_16x16x32_bf16 v[128:131], a[108:111], v[80:83], v[128:131]\n\t"
      "ds_read_b64_tr_b16 v[126:127], v43 offset:47104\n\t"
      "v_mfma_f32_16x16x32_bf16 v[144:147], a[112:115], v[80:83], v[144:147]\n\t"
      "v_mfma_f32_16x16x32_bf16 v[160:163], a[116:119], v[80:83], v[160:163]\n\t"
      "v_mfma_f32_16x16x32_bf16 v[176:179], v[52:55], v[80:83], v[176:179]\n\t"
      "s_waitcnt vmcnt(13)\n\t"
      "ds_read_b128 v[240:243], %[vchk] offset:49152\n\t"
      "v_mfma_f32_16x16x32_bf16 v[180:183], v[80:83], v[80:83], v[180:183]\n\t"
      "ds_read_b128 v[244:247], %[vchk] offset:50176\n\t"
      "ds_read_b128 v[248:251], %[vchk] offset:51200\n\t"
      "v_mfma_f32_16x16x32_bf16 v[132:135], a[108:111], v[84:87], v[132:135]\n\t"
      "ds_read_b128 v[252:255], %[vchk] offset:52224\n\t"
      "v_mfma_f32_16x16x32_bf16 v[148:151], a[112:115], v[84:87], v[148:151]\n\t"
      "v_mfma_f32_16x16x32_bf16 v[164:167], a[116:119], v[84:87], v[164:167]\n\t"
      "v_mfma_f32_16x16x32_bf16 v[136:139], a[108:111], v[88:91], v[136:139]\n\t"
      "s_waitcnt lgkmcnt(0)\n\t"
      "v_max3_u32 v56, v240, v241, v242\n\t"
      "v_mfma_f32_16x16x32_bf16 v[152:155], a[112:115], v[88:91], v[152:155]\n\t"
      "v_max3_u32 v56, v56, v243, v244\n\t"
      "v_max3_u32 v56, v56, v245, v246\n\t"
      "v_mfma_f32_16x16x32_bf16 v[168:171], a[116:119], v[88:91], v[168:171]\n\t"
      "v_max3_u32 v56, v56, v247, v248\n\t"
      "v_max3_u32 v56, v56, v249, v250\n\t"
      "v_mfma_f32_16x16x32_bf16 v[140:143], a[108:111], v[92:95], v[140:143]\n\t"
      "v_max3_u32 v56, v56, v251, v252\n\t"
      "v_max3_u32 v56, v56, v253, v254\n\t"
      "v_mfma_f32_16x16x32_bf16 v[156:159], a[112:115], v[92:95], v[156:159]\n\t"
      "v_max_u32 v56, v56, v255\n\t"
      "v_cmp_eq_u32 vcc, -1, v56\n\t"
      "v_mfma_f32_16x16x32_bf16 v[172:175], a[116:119], v[92:95], v[172:175]\n\t"
      "s_cbranch_vccnz L_g1_slow3_%=\n\t"
      "L_g1_back3_%=:\n\t"
      "Lpd74eef_44:\n\t"
      "s_waitcnt lgkmcnt(0)\n\t"
      "s_barrier\n\t"
      "s_waitcnt vmcnt(18)\n\t"
      "v_mfma_f32_16x16x32_bf16 v[128:131], a[120:123], v[96:99], v[128:131]\n\t"
      "ds_read_b64_tr_b16 v[64:65], v40 offset:49152\n\t"
      "v_mfma_f32_16x16x32_bf16 v[144:147], a[124:127], v[96:99], v[144:147]\n\t"
      "s_add_u32 m0, %[m0b], 65536\n\t"
      "s_add_u32 s36, %[xlo], 65536\n\t"
      "v_mfma_f32_16x16x32_bf16 v[160:163], a[128:131], v[96:99], v[160:163]\n\t"
      "ds_read_b64_tr_b16 v[66:67], v40 offset:51200\n\t"
      "ds_read_b64_tr_b16 v[68:69], v41 offset:49152\n\t"
      "v_mfma_f32_16x16x32_bf16 v[176:179], v[52:55], v[96:99], v[176:179]\n\t"
      "s_addc_u32 s37, %[xhi], 0\n\t"
      "global_load_lds_dwordx4 %[voff], s[36:37] offset:2048 sc1\n\t"
      "v_mfma_f32_16x16x32_bf16 v[180:183], v[96:99], v[96:99], v[180:183]\n\t"
      "ds_read_b64_tr_b16 v[70:71], v41 offset:51200\n\t"
      "ds_read_b64_tr_b16 v[72:73], v42 offset:49152\n\t"
      "v_mfma_f32_16x16x32_bf16 v[132:135], a[120:123], v[100:103], v[132:135]\n\t"
      "ds_read_b64_tr_b16 v[74:75], v42 offset:51200\n\t"
      "v_mfma_f32_16x16x32_bf16 v[148:151], a[124:127], v[100:103], v[148:151]\n\t"
      "global_load_lds_dwordx4 %[voff], s[36:37] offset:3072 sc1\n\t"
      "v_mfma_f32_16x16x32_bf16 v[164:167], a[128:131], v[100:103], v[164:167]\n\t"
      "ds_read_b64_tr_b16 v[76:77], v43 offset:49152\n\t"
      "ds_read_b64_tr_b16 v[78:79], v43 offset:51200\n\t"
      "v_mfma_f32_16x16x32_bf16 v[136:139], a[120:123], v[104:107], v[136:139]\n\t"
      "ds_read_b64_tr_b16 v[80:81], v40 offset:53248\n\t"
      "v_mfma_f32_16x16x32_bf16 v[152:155], a[124:127], v[104:107], v[152:155]\n\t"
      "global_load_dwordx4 a[196:199], %[l16], s[38:39] offset:-3072 sc1\n\t"
      "v_mfma_f32_16x16x32_bf16 v[168:171], a[128:131], v[104:107], v[168:171]\n\t"
      "ds_read_b64_tr_b16 v[82:83], v40 offset:55296\n\t"
      "ds_read_b64_tr_b16 v[84:85], v41 offset:53248\n\t"
      "v_mfma_f32_16x16x32_bf16 v[140:143], a[120:123], v[108:111], v[140:143]\n\t"
      "global_load_dwordx4 a[200:203], %[l16], s[38:39] offset:-2048 sc1\n\t"
      "v_mfma_f32_16x16x32_bf16 v[156:159], a[124:127], v[108:111], v[156:159]\n\t"
      "ds_read_b64_tr_b16 v[86:87], v41 offset:55296\n\t"
      "ds_read_b64_tr_b16 v[88:89], v42 offset:53248\n\t"
      "v_mfma_f32_16x16x32_bf16 v[172:175], a[128:131], v[108:111], v[172:175]\n\t"
      "global_load_dwordx4 a[204:207], %[l16], s[38:39] offset:-1024 sc1\n\t"
      "v_mfma_f32_16x16x32_bf16 v[128:131], a[132:135], v[112:115], v[128:131]\n\t"
      "ds_read_b64_tr_b16 v[90:91], v42 offset:55296\n\t"
      "ds_read_b64_tr_b16 v[92:93], v43 offset:53248\n\t"
      "v_mfma_f32_16x16x32_bf16 v[144:147], a[136:139], v[112:115], v[144:147]\n\t"
      "ds_read_b64_tr_b16 v[94:95], v43 offset:55296\n\t"
      "v_mfma_f32_16x16x32_bf16 v[160:163], a[140:143], v[112:115], v[160:163]\n\t"
      "global_load_dwordx4 a[208:211], %[l16], s[38:39] offset:0 sc1\n\t"
      "v_mfma_f32_16x16x32_bf16 v[176:179], v[52:55], v[112:115], v[176:179]\n\t"
      "global_load_dwordx4 a[212:215], %[l16], s[38:39] offset:1024 sc1\n\t"
      "v_mfma_f32_16x16x32_bf16 v[180:183], v[112:115], v[112:115], v[180:183]\n\t"
      "v_mfma_f32_16x16x32_bf16 v[132:135], a[132:135], v[116:119], v[132:135]\n\t"
      "v_mfma_f32_16x16x32_bf16 v[148:151], a[136:139], v[116:119], v[148:151]\n\t"
      "v_mfma_f32_16x16x32_bf16 v[164:167], a[140:143], v[116:119], v[164:167]\n\t"
      "v_mfma_f32_16x16x32_bf16 v[136:139], a[132:135], v[120:123], v[136:139]\n\t"
      "v_mfma_f32_16x16x32_bf16 v[152:155], a[136:139], v[120:123], v[152:155]\n\t"
      "v_mfma_f32_16x16x32_bf16 v[168:171], a[140:143], v[120:123], v[168:171]\n\t"
      "v_mfma_f32_16x16x32_bf16 v[140:143], a[132:135], v[124:127], v[140:143]\n\t"
      "v_mfma_f32_16x16x32_bf16 v[156:159], a[136:139], v[124:127], v[156:159]\n\t"
      "v_mfma_f32_16x16x32_bf16 v[172:175], a[140:143], v[124:127], v[172:175]\n\t"
      "Lpd74eef_45:\n\t"
      "s_waitcnt lgkmcnt(0)\n\t"
      "s_waitcnt vmcnt(17)\n\t"
      "v_mfma_f32_16x16x32_bf16 v[128:131], a[144:147], v[64:67], v[128:131]\n\t"
      "ds_read_b64_tr_b16 v[96:97], v40 offset:57344\n\t"
      "v_mfma_f32_16x16x32_bf16 v[144:147], a[148:151], v[64:67], v[144:147]\n\t"
      "s_add_u32 m0, %[m0b], 81920\n\t"
      "s_add_u32 s36, %[xlo], 81920\n\t"
      "v_mfma_f32_16x16x32_bf16 v[160:163], a[152:155], v[64:67], v[160:163]\n\t"
      "ds_read_b64_tr_b16 v[98:99], v40 offset:59392\n\t"
      "ds_read_b64_tr_b16 v[100:101], v41 offset:57344\n\t"
      "v_mfma_f32_16x16x32_bf16 v[176:179], v[52:55], v[64:67], v[176:179]\n\t"
      "s_addc_u32 s37, %[xhi], 0\n\t"
      "global_load_lds_dwordx4 %[voff], s[36:37] offset:0 sc1\n\t"
      "v_mfma_f32_16x16x32_bf16 v[180:183], v[64:67], v[64:67], v[180:183]\n\t"
      "ds_read_b64_tr_b16 v[102:103], v41 offset:59392\n\t"
      "ds_read_b64_tr_b16 v[104:105], v42 offset:57344\n\t"
      "v_mfma_f32_16x16x32_bf16 v[132:135], a[144:147], v[68:71], v[132:135]\n\t"
      "ds_read_b64_tr_b16 v[106:107], v42 offset:59392\n\t"
      "v_mfma_f32_16x16x32_bf16 v[148:151], a[148:151], v[68:71], v[148:151]\n\t"
      "global_load_lds_dwordx4 %[voff], s[36:37] offset:1024 sc1\n\t"
      "v_mfma_f32_16x16x32_bf16 v[164:167], a[152:155], v[68:71], v[164:167]\n\t"
      "ds_read_b64_tr_b16 v[108:109], v43 offset:57344\n\t"
      "ds_read_b64_tr_b16 v[110:111], v43 offset:59392\n\t"
      "v_mfma_f32_16x16x32_bf16 v[136:139], a[144:147], v[72:75], v[136:139]\n\t"
      "global_load_dwordx4 a[216:219], %[l16], s[38:39] offset:2048 sc1\n\t"
      "v_mfma_f32_16x16x32_bf16 v[152:155], a[148:151], v[72:75], v[152:155]\n\t"
      "ds_read_b64_tr_b16 v[112:113], v40 offset:61440\n\t"
      "ds_read_b64_tr_b16 v[114:115], v40 offset:63488\n\t"
      "v_mfma_f32_16x16x32_bf16 v[168:171], a[152:155], v[72:75], v[168:171]\n\t"
      "global_load_dwordx4 a[220:223], %[l16], s[38:39] offset:3072 sc1\n\t"
      "v_mfma_f32_16x16x32_bf16 v[140:143], a[144:147], v[76:79], v[140:143]\n\t"
      "ds_read_b64_tr_b16 v[116:117], v41 offset:61440\n\t"
      "ds_read_b64_tr_b16 v[118:119], v41 offset:63488\n\t"
      "v_mfma_f32_16x16x32_bf16 v[156:159], a[148:151], v[76:79], v[156:159]\n\t"
      "s_add_u32 s38, %[plo], 61440\n\t"
      "s_addc_u32 s39, %[phi], 0\n\t"
      "v_mfma_f32_16x16x32_bf16 v[172:175], a[152:155], v[76:79], v[172:175]\n\t"
      "ds_read_b64_tr_b16 v[120:121], v42 offset:61440\n\t"
      "ds_read_b64_tr_b16 v[122:123], v42 offset:63488\n\t"
      "v_mfma_f32_16x16x32_bf16 v[128:131], a[156:159], v[80:83], v[128:131]\n\t"
      "global_load_dwordx4 a[224:227], %[l16], s[38:39] offset:-4096 sc1\n\t"
      "v_mfma_f32_16x16x32_bf16 v[144:147], a[160:163], v[80:83], v[144:147]\n\t"
      "ds_read_b64_tr_b16 v[124:125], v43 offset:61440\n\t"
      "ds_read_b64_tr_b16 v[126:127], v43 offset:63488\n\t"
      "v_mfma_f32_16x16x32_bf16 v[160:163], a[164:167], v[80:83], v[160:163]\n\t"
      "global_load_dwordx4 a[228:231], %[l16], s[38:39] offset:-3072 sc1\n\t"
      "v_mfma_f32_16x16x32_bf16 v[176:179], v[52:55], v[80:83], v[176:179]\n\t"
      "s_waitcnt vmcnt(11)\n\t"
      "ds_read_b128 v[240:243], v57 offset:0\n\t"
      "v_mfma_f32_16x16x32_bf16 v[180:183], v[80:83], v[80:83], v[180:183]\n\t"
      "ds_read_b128 v[244:247], v57 offset:1024\n\t"
      "ds_read_b128 v[248:251], v57 offset:2048\n\t"
      "v_mfma_f32_16x16x32_bf16 v[132:135], a[156:159], v[84:87], v[132:135]\n\t"
      "ds_read_b128 v[252:255], v57 offset:3072\n\t"
      "v_mfma_f32_16x16x32_bf16 v[148:151], a[160:163], v[84:87], v[148:151]\n\t"
      "global_load_dwordx4 a[232:235], %[l16], s[38:39] offset:-2048 sc1\n\t"
      "v_mfma_f32_16x16x32_bf16 v[164:167], a[164:167], v[84:87], v[164:167]\n\t"
      "v_mfma_f32_16x16x32_bf16 v[136:139], a[156:159], v[88:91], v[136:139]\n\t"
      "s_waitcnt lgkmcnt(0)\n\t"
      "v_max3_u32 v56, v240, v241, v242\n\t"
      "v_mfma_f32_16x16x32_bf16 v[152:155], a[160:163], v[88:91], v[152:155]\n\t"
      "v_max3_u32 v56, v56, v243, v244\n\t"
      "v_max3_u32 v56, v56, v245, v246\n\t"
      "v_mfma_f32_16x16x32_bf16 v[168:171], a[164:167], v[88:91], v[168:171]\n\t"
      "v_max3_u32 v56, v56, v247, v248\n\t"
      "v_max3_u32 v56, v56, v249, v250\n\t"
      "v_mfma_f32_16x16x32_bf16 v[140:143], a[156:159], v[92:95], v[140:143]\n\t"
      "v_max3_u32 v56, v56, v251, v252\n\t"
      "v_max3_u32 v56, v56, v253, v254\n\t"
      "v_mfma_f32_16x16x32_bf16 v[156:159], a[160:163], v[92:95], v[156:159]\n\t"
      "v_max_u32 v56, v56, v255\n\t"
      "v_cmp_eq_u32 vcc, -1, v56\n\t"
      "v_mfma_f32_16x16x32_bf16 v[172:175], a[164:167], v[92:95], v[172:175]\n\t"
      "s_cbranch_vccnz L_g1_slow4_%=\n\t"
      "L_g1_back4_%=:\n\t"
      "Lpd74eef_46:\n\t"
      "s_waitcnt lgkmcnt(0)\n\t"
      "s_barrier\n\t"
      "s_waitcnt vmcnt(16)\n\t"
      "v_mfma_f32_16x16x32_bf16 v[128:131], a[168:171], v[96:99], v[128:131]\n\t"
      "ds_read_b64_tr_b16 v[64:65], v44 offset:0\n\t"
      "v_mfma_f32_16x16x32_bf16 v[144:147], a[172:175], v[96:99], v[144:147]\n\t"
      "s_add_u32 m0, %[m0b], 81920\n\t"
      "s_add_u32 s36, %[xlo], 81920\n\t"
      "v_mfma_f32_16x16x32_bf16 v[160:163], a[176:179], v[96:99], v[160:163]\n\t"
      "ds_read_b64_tr_b16 v[66:67], v44 offset:2048\n\t"
      "v_mfma_f32_16x16x32_bf16 v[176:179], v[52:55], v[96:99], v[176:179]\n\t"
      "s_addc_u32 s37, %[xhi], 0\n\t"
      "global_load_lds_dwordx4 %[voff], s[36:37] offset:2048 sc1\n\t"
      "v_mfma_f32_16x16x32_bf16 v[180:183], v[96:99], v[96:99], v[180:183]\n\t"
      "ds_read_b64_tr_b16 v[68:69], v45 offset:0\n\t"
      "ds_read_b64_tr_b16 v[70:71], v45 offset:2048\n\t"
      "v_mfma_f32_16x16x32_bf16 v[132:135], a[168:171], v[100:103], v[132:135]\n\t"
      "global_load_lds_dwordx4 %[voff], s[36:37] offset:3072 sc1\n\t"
      "v_mfma_f32_16x16x32_bf16 v[148:151], a[172:175], v[100:103], v[148:151]\n\t"
      "ds_read_b64_tr_b16 v[72:73], v46 offset:0\n\t"
      "ds_read_b64_tr_b16 v[74:75], v46 offset:2048\n\t"
      "v_mfma_f32_16x16x32_bf16 v[164:167], a[176:179], v[100:103], v[164:167]\n\t"
      "s_add_u32 m0, %[m0b], 98304\n\t"
      "s_add_u32 s36, %[xlo], 98304\n\t"
      "v_mfma_f32_16x16x32_bf16 v[136:139], a[168:171], v[104:107], v[136:139]\n\t"
      "ds_read_b64_tr_b16 v[76:77], v47 offset:0\n\t"
      "v_mfma_f32_16x16x32_bf16 v[152:155], a[172:175], v[104:107], v[152:155]\n\t"
      "s_addc_u32 s37, %[xhi], 0\n\t"
      "global_load_lds_dwordx4 %[voff], s[36:37] offset:0 sc1\n\t"
      "v_mfma_f32_16x16x32_bf16 v[168:171], a[176:179], v[104:107], v[168:171]\n\t"
      "ds_read_b64_tr_b16 v[78:79], v47 offset:2048\n\t"
      "ds_read_b64_tr_b16 v[80:81], v44 offset:4096\n\t"
      "v_mfma_f32_16x16x32_bf16 v[140:143], a[168:171], v[108:111], v[140:143]\n\t"
      "global_load_lds_dwordx4 %[voff], s[36:37] offset:1024 sc1\n\t"
      "v_mfma_f32_16x16x32_bf16 v[156:159], a[172:175], v[108:111], v[156:159]\n\t"
      "ds_read_b64_tr_b16 v[82:83], v44 offset:6144\n\t"
      "ds_read_b64_tr_b16 v[84:85], v45 offset:4096\n\t"
      "v_mfma_f32_16x16x32_bf16 v[172:175], a[176:179], v[108:111], v[172:175]\n\t"
      "global_load_dwordx4 a[236:239], %[l16], s[38:39] offset:-1024 sc1\n\t"
      "v_mfma_f32_16x16x32_bf16 v[128:131], a[180:183], v[112:115], v[128:131]\n\t"
      "ds_read_b64_tr_b16 v[86:87], v45 offset:6144\n\t"
      "v_mfma_f32_16x16x32_bf16 v[144:147], a[184:187], v[112:115], v[144:147]\n\t"
      "s_add_u32 s38, %[wlo], 0\n\t"
      "s_addc_u32 s39, %[whi], 0\n\t"
      "v_mfma_f32_16x16x32_bf16 v[160:163], a[188:191], v[112:115], v[160:163]\n\t"
      "ds_read_b64_tr_b16 v[88:89], v46 offset:4096\n\t"
      "v_mfma_f32_16x16x32_bf16 v[176:179], v[52:55], v[112:115], v[176:179]\n\t"
      "global_load_dwordx4 v[184:187], v36, s[38:39] sc1\n\t"
      "v_mfma_f32_16x16x32_bf16 v[180:183], v[112:115], v[112:115], v[180:183]\n\t"
      "ds_read_b64_tr_b16 v[90:91], v46 offset:6144\n\t"
      "ds_read_b64_tr_b16 v[92:93], v47 offset:4096\n\t"
      "v_mfma_f32_16x16x32_bf16 v[132:135], a[180:183], v[116:119], v[132:135]\n\t"
      "s_add_u32 s38, %[wlo], 24576\n\t"
      "s_addc_u32 s39, %[whi], 0\n\t"
      "v_mfma_f32_16x16x32_bf16 v[148:151], a[184:187], v[116:119], v[148:151]\n\t"
      "ds_read_b64_tr_b16 v[94:95], v47 offset:6144\n\t"
      "v_mfma_f32_16x16x32_bf16 v[164:167], a[188:191], v[116:119], v[164:167]\n\t"
      "global_load_dwordx4 v[188:191], v36, s[38:39] sc1\n\t"
      "v_mfma_f32_16x16x32_bf16 v[136:139], a[180:183], v[120:123], v[136:139]\n\t"
      "s_add_u32 s38, %[wlo], 49152\n\t"
      "s_addc_u32 s39, %[whi], 0\n\t"
      "v_mfma_f32_16x16x32_bf16 v[152:155], a[184:187], v[120:123], v[152:155]\n\t"
      "global_load_dwordx4 v[192:195], v36, s[38:39] sc1\n\t"
      "v_mfma_f32_16x16x32_bf16 v[168:171], a[188:191], v[120:123], v[168:171]\n\t"
      "v_mfma_f32_16x16x32_bf16 v[140:143], a[180:183], v[124:127], v[140:143]\n\t"
      "v_mfma_f32_16x16x32_bf16 v[156:159], a[184:187], v[124:127], v[156:159]\n\t"
      "v_mfma_f32_16x16x32_bf16 v[172:175], a[188:191], v[124:127], v[172:175]\n\t"
      "Lpd74eef_47:\n\t"
      "s_waitcnt lgkmcnt(0)\n\t"
      "s_waitcnt vmcnt(15)\n\t"
      "v_mfma_f32_16x16x32_bf16 v[128:131], a[192:195], v[64:67], v[128:131]\n\t"
      "ds_read_b64_tr_b16 v[96:97], v44 offset:8192\n\t"
      "s_add_u32 m0, %[m0b], 98304\n\t"
      "s_add_u32 s36, %[xlo], 98304\n\t"
      "v_mfma_f32_16x16x32_bf16 v[144:147], a[196:199], v[64:67], v[144:147]\n\t"
      "ds_read_b64_tr_b16 v[98:99], v44 offset:10240\n\t"
      "v_mfma_f32_16x16x32_bf16 v[160:163], a[200:203], v[64:67], v[160:163]\n\t"
      "s_addc_u32 s37, %[xhi], 0\n\t"
      "global_load_lds_dwordx4 %[voff], s[36:37] offset:2048 sc1\n\t"
      "v_mfma_f32_16x16x32_bf16 v[176:179], v[52:55], v[64:67], v[176:179]\n\t"
      "ds_read_b64_tr_b16 v[100:101], v45 offset:8192\n\t"
      "ds_read_b64_tr_b16 v[102:103], v45 offset:10240\n\t"
      "v_mfma_f32_16x16x32_bf16 v[180:183], v[64:67], v[64:67], v[180:183]\n\t"
      "global_load_lds_dwordx4 %[voff], s[36:37] offset:3072 sc1\n\t"
      "ds_read_b64_tr_b16 v[104:105], v46 offset:8192\n\t"
      "v_mfma_f32_16x16x32_bf16 v[132:135], a[192:195], v[68:71], v[132:135]\n\t"
      "s_add_u32 m0, %[m0b], 114688\n\t"
      "s_add_u32 s36, %[xlo], 114688\n\t"
      "ds_read_b64_tr_b16 v[106:107], v46 offset:10240\n\t"
      "v_mfma_f32_16x16x32_bf16 v[148:151], a[196:199], v[68:71], v[148:151]\n\t"
      "s_addc_u32 s37, %[xhi], 0\n\t"
      "global_load_lds_dwordx4 %[voff], s[36:37] offset:0 sc1\n\t"
      "v_mfma_f32_16x16x32_bf16 v[164:167], a[200:203], v[68:71], v[164:167]\n\t"
      "ds_read_b64_tr_b16 v[108:109], v47 offset:8192\n\t"
      "ds_read_b64_tr_b16 v[110:111], v47 offset:10240\n\t"
      "ds_read_b64_tr_b16 v[112:113], v44 offset:12288\n\t"
      "v_mfma_f32_16x16x32_bf16 v[136:139], a[192:195], v[72:75], v[136:139]\n\t"
      "global_load_lds_dwordx4 %[voff], s[36:37] offset:1024 sc1\n\t"
      "ds_read_b64_tr_b16 v[114:115], v44 offset:14336\n\t"
      "v_mfma_f32_16x16x32_bf16 v[152:155], a[196:199], v[72:75], v[152:155]\n\t"
      "s_add_u32 s38, %[wlo], 73728\n\t"
      "s_addc_u32 s39, %[whi], 0\n\t"
      "ds_read_b64_tr_b16 v[116:117], v45 offset:12288\n\t"
      "v_mfma_f32_16x16x32_bf16 v[168:171], a[200:203], v[72:75], v[168:171]\n\t"
      "global_load_dwordx4 v[196:199], v36, s[38:39] sc1\n\t"
      "ds_read_b64_tr_b16 v[118:119], v45 offset:14336\n\t"
      "v_mfma_f32_16x16x32_bf16 v[140:143], a[192:195], v[76:79], v[140:143]\n\t"
      "s_add_u32 s38, %[wlo], 98304\n\t"
      "s_addc_u32 s39, %[whi], 0\n\t"
      "ds_read_b64_tr_b16 v[120:121], v46 offset:12288\n\t"
      "v_mfma_f32_16x16x32_bf16 v[156:159], a[196:199], v[76:79], v[156:159]\n\t"
      "ds_read_b64_tr_b16 v[122:123], v46 offset:14336\n\t"
      "global_load_dwordx4 v[200:203], v36, s[38:39] sc1\n\t"
      "v_mfma_f32_16x16x32_bf16 v[172:175], a[200:203], v[76:79], v[172:175]\n\t"
      "ds_read_b64_tr_b16 v[124:125], v47 offset:12288\n\t"
      "s_add_u32 s38, %[wlo], 122880\n\t"
      "s_addc_u32 s39, %[whi], 0\n\t"
      "v_mfma_f32_16x16x32_bf16 v[128:131], a[204:207], v[80:83], v[128:131]\n\t"
      "ds_read_b64_tr_b16 v[126:127], v47 offset:14336\n\t"
      "global_load_dwordx4 v[204:207], v36, s[38:39] sc1\n\t"
      "v_mfma_f32_16x16x32_bf16 v[144:147], a[208:211], v[80:83], v[144:147]\n\t"
      "s_add_u32 s38, %[wlo], 147456\n\t"
      "s_addc_u32 s39, %[whi], 0\n\t"
      "v_mfma_f32_16x16x32_bf16 v[160:163], a[212:215], v[80:83], v[160:163]\n\t"
      "global_load_dwordx4 v[208:211], v36, s[38:39] sc1\n\t"
      "v_mfma_f32_16x16x32_bf16 v[176:179], v[52:55], v[80:83], v[176:179]\n\t"
      "s_waitcnt vmcnt(14)\n\t"
      "ds_read_b128 v[240:243], v57 offset:16384\n\t"
      "v_mfma_f32_16x16x32_bf16 v[180:183], v[80:83], v[80:83], v[180:183]\n\t"
      "ds_read_b128 v[244:247], v57 offset:17408\n\t"
      "ds_read_b128 v[248:251], v57 offset:18432\n\t"
      "v_mfma_f32_16x16x32_bf16 v[132:135], a[204:207], v[84:87], v[132:135]\n\t"
      "ds_read_b128 v[252:255], v57 offset:19456\n\t"
      "v_mfma_f32_16x16x32_bf16 v[148:151], a[208:211], v[84:87], v[148:151]\n\t"
      "v_mfma_f32_16x16x32_bf16 v[164:167], a[212:215], v[84:87], v[164:167]\n\t"
      "v_mfma_f32_16x16x32_bf16 v[136:139], a[204:207], v[88:91], v[136:139]\n\t"
      "s_waitcnt lgkmcnt(0)\n\t"
      "v_max3_u32 v56, v240, v241, v242\n\t"
      "v_mfma_f32_16x16x32_bf16 v[152:155], a[208:211], v[88:91], v[152:155]\n\t"
      "v_max3_u32 v56, v56, v243, v244\n\t"
      "v_max3_u32 v56, v56, v245, v246\n\t"
      "v_mfma_f32_16x16x32_bf16 v[168:171], a[212:215], v[88:91], v[168:171]\n\t"
      "v_max3_u32 v56, v56, v247, v248\n\t"
      "v_max3_u32 v56, v56, v249, v250\n\t"
      "v_mfma_f32_16x16x32_bf16 v[140:143], a[204:207], v[92:95], v[140:143]\n\t"
      "v_max3_u32 v56, v56, v251, v252\n\t"
      "v_max3_u32 v56, v56, v253, v254\n\t"
      "v_mfma_f32_16x16x32_bf16 v[156:159], a[208:211], v[92:95], v[156:159]\n\t"
      "v_max_u32 v56, v56, v255\n\t"
      "v_cmp_eq_u32 vcc, -1, v56\n\t"
      "v_mfma_f32_16x16x32_bf16 v[172:175], a[212:215], v[92:95], v[172:175]\n\t"
      "s_cbranch_vccnz L_g1_slow5_%=\n\t"
      "L_g1_back5_%=:\n\t"
      "Lpd74eef_48:\n\t"
      "s_waitcnt lgkmcnt(0)\n\t"
      "s_barrier\n\t"
      "s_waitcnt vmcnt(11)\n\t"
      "v_mfma_f32_16x16x32_bf16 v[128:131], a[216:219], v[96:99], v[128:131]\n\t"
      "s_add_u32 m0, %[m0b], 114688\n\t"
      "s_add_u32 s36, %[xlo], 114688\n\t"
      "v_mfma_f32_16x16x32_bf16 v[144:147], a[220:223], v[96:99], v[144:147]\n\t"
      "s_addc_u32 s37, %[xhi], 0\n\t"
      "global_load_lds_dwordx4 %[voff], s[36:37] offset:2048 sc1\n\t"
      "v_mfma_f32_16x16x32_bf16 v[160:163], a[224:227], v[96:99], v[160:163]\n\t"
      "global_load_lds_dwordx4 %[voff], s[36:37] offset:3072 sc1\n\t"
      "v_mfma_f32_16x16x32_bf16 v[176:179], v[52:55], v[96:99], v[176:179]\n\t"
      "s_add_u32 m0, %[m0b], 0\n\t"
      "s_add_u32 s36, %[xlo], 131072\n\t"
      "v_mfma_f32_16x16x32_bf16 v[180:183], v[96:99], v[96:99], v[180:183]\n\t"
      "s_addc_u32 s37, %[xhi], 0\n\t"
      "global_load_lds_dwordx4 %[voff], s[36:37] offset:0 sc1\n\t"
      "v_mfma_f32_16x16x32_bf16 v[132:135], a[216:219], v[100:103], v[132:135]\n\t"
      "global_load_lds_dwordx4 %[voff], s[36:37] offset:1024 sc1\n\t"
      "v_mfma_f32_16x16x32_bf16 v[148:151], a[220:223], v[100:103], v[148:151]\n\t"
      "s_add_u32 s38, %[wlo], 172032\n\t"
      "s_addc_u32 s39, %[whi], 0\n\t"
      "v_mfma_f32_16x16x32_bf16 v[164:167], a[224:227], v[100:103], v[164:167]\n\t"
      "global_load_dwordx4 v[212:215], v36, s[38:39] sc1\n\t"
      "v_mfma_f32_16x16x32_bf16 v[136:139], a[216:219], v[104:107], v[136:139]\n\t"
      "s_add_u32 s38, %[wlo], 196608\n\t"
      "s_addc_u32 s39, %[whi], 0\n\t"
      "v_mfma_f32_16x16x32_bf16 v[152:155], a[220:223], v[104:107], v[152:155]\n\t"
      "global_load_dwordx4 v[216:219], v36, s[38:39] sc1\n\t"
      "v_mfma_f32_16x16x32_bf16 v[168:171], a[224:227], v[104:107], v[168:171]\n\t"
      "s_add_u32 s38, %[wlo], 221184\n\t"
      "s_addc_u32 s39, %[whi], 0\n\t"
      "v_mfma_f32_16x16x32_bf16 v[140:143], a[216:219], v[108:111], v[140:143]\n\t"
      "global_load_dwordx4 v[220:223], v36, s[38:39] sc1\n\t"
      "v_mfma_f32_16x16x32_bf16 v[156:159], a[220:223], v[108:111], v[156:159]\n\t"
      "v_mfma_f32_16x16x32_bf16 v[172:175], a[224:227], v[108:111], v[172:175]\n\t"
      "v_mfma_f32_16x16x32_bf16 v[128:131], a[228:231], v[112:115], v[128:131]\n\t"
      "v_mfma_f32_16x16x32_bf16 v[144:147], a[232:235], v[112:115], v[144:147]\n\t"
      "v_mfma_f32_16x16x32_bf16 v[160:163], a[236:239], v[112:115], v[160:163]\n\t"
      "v_mfma_f32_16x16x32_bf16 v[176:179], v[52:55], v[112:115], v[176:179]\n\t"
      "v_mfma_f32_16x16x32_bf16 v[180:183], v[112:115], v[112:115], v[180:183]\n\t"
      "v_mfma_f32_16x16x32_bf16 v[132:135], a[228:231], v[116:119], v[132:135]\n\t"
      "v_mfma_f32_16x16x32_bf16 v[148:151], a[232:235], v[116:119], v[148:151]\n\t"
      "v_mfma_f32_16x16x32_bf16 v[164:167], a[236:239], v[116:119], v[164:167]\n\t"
      "v_mfma_f32_16x16x32_bf16 v[136:139], a[228:231], v[120:123], v[136:139]\n\t"
      "v_mfma_f32_16x16x32_bf16 v[152:155], a[232:235], v[120:123], v[152:155]\n\t"
      "v_mfma_f32_16x16x32_bf16 v[168:171], a[236:239], v[120:123], v[168:171]\n\t"
      "v_mfma_f32_16x16x32_bf16 v[140:143], a[228:231], v[124:127], v[140:143]\n\t"
      "v_mfma_f32_16x16x32_bf16 v[156:159], a[232:235], v[124:127], v[156:159]\n\t"
      "v_mfma_f32_16x16x32_bf16 v[172:175], a[236:239], v[124:127], v[172:175]\n\t"
      "global_load_dwordx4 v[224:227], v38, %[wsb] offset:0 sc1\n\t"
      "global_load_dwordx4 v[228:231], v38, %[wsb] offset:16 sc1\n\t"
      "global_load_dwordx4 v[232:235], v38, %[wsb] offset:32 sc1\n\t"
      "global_load_dwordx4 v[236:239], v38, %[wsb] offset:48 sc1\n\t"
      "global_load_dwordx2 v[58:59], v37, %[wsb] sc1\n\t"
      "s_waitcnt vmcnt(5)\n\t"
      "s_mov_b32 m0, s45\n\t"
      "s_nop 7\n\t"
      "s_branch L_g1_end%=\n\t"
      "L_g1_slow0_%=:\n\t"
      "s_add_u32 m0, %[m0b], 0\n\t"
      "s_add_u32 s42, %[xlo], 0\n\t"
      "s_addc_u32 s43, %[xhi], 0\n\t"
      "global_load_lds_dwordx4 %[voff], s[42:43] offset:0 sc1\n\t"
      "global_load_lds_dwordx4 %[voff], s[42:43] offset:1024 sc1\n\t"
      "global_load_lds_dwordx4 %[voff], s[42:43] offset:2048 sc1\n\t"
      "global_load_lds_dwordx4 %[voff], s[42:43] offset:3072 sc1\n\t"
      "s_waitcnt vmcnt(0)\n\t"
      "ds_read_b128 v[240:243], %[vchk] offset:0\n\t"
      "ds_read_b128 v[244:247], %[vchk] offset:1024\n\t"
      "ds_read_b128 v[248:251], %[vchk] offset:2048\n\t"
      "ds_read_b128 v[252:255], %[vchk] offset:3072\n\t"
      "s_waitcnt lgkmcnt(0)\n\t"
      "v_max3_u32 v56, v240, v241, v242\n\t"
      "v_max3_u32 v56, v56, v243, v244\n\t"
      "v_max3_u32 v56, v56, v245, v246\n\t"
      "v_max3_u32 v56, v56, v247, v248\n\t"
      "v_max3_u32 v56, v56, v249, v250\n\t"
      "v_max3_u32 v56, v56, v251, v252\n\t"
      "v_max3_u32 v56, v56, v253, v254\n\t"
      "v_max_u32 v56, v56, v255\n\t"
      "v_cmp_eq_u32 vcc, -1, v56\n\t"
      "s_sub_u32 s44, s44, 1\n\t"
      "s_cbranch_vccz L_g1_back0_%=\n\t"
      "s_cmp_lg_u32 s44, 0\n\t"
      "s_cbranch_scc1 L_g1_slow0_%=\n\t"
      "s_branch L_g1_back0_%=\n\t"
      "L_g1_slow1_%=:\n\t"
      "s_add_u32 m0, %[m0b], 16384\n\t"
      "s_add_u32 s42, %[xlo], 16384\n\t"
      "s_addc_u32 s43, %[xhi], 0\n\t"
      "global_load_lds_dwordx4 %[voff], s[42:43] offset:0 sc1\n\t"
      "global_load_lds_dwordx4 %[voff], s[42:43] offset:1024 sc1\n\t"
      "global_load_lds_dwordx4 %[voff], s[42:43] offset:2048 sc1\n\t"
      "global_load_lds_dwordx4 %[voff], s[42:43] offset:3072 sc1\n\t"
      "s_waitcnt vmcnt(0)\n\t"
      "ds_read_b128 v[240:243], %[vchk] offset:16384\n\t"
      "ds_read_b128 v[244:247], %[vchk] offset:17408\n\t"
      "ds_read_b128 v[248:251], %[vchk] offset:18432\n\t"
      "ds_read_b128 v[252:255], %[vchk] offset:19456\n\t"
      "s_waitcnt lgkmcnt(0)\n\t"
      "v_max3_u32 v56, v240, v241, v242\n\t"
      "v_max3_u32 v56, v56, v243, v244\n\t"
      "v_max3_u32 v56, v56, v245, v246\n\t"
      "v_max3_u32 v56, v56, v247, v248\n\t"
      "v_max3_u32 v56, v56, v249, v250\n\t"
      "v_max3_u32 v56, v56, v251, v252\n\t"
      "v_max3_u32 v56, v56, v253, v254\n\t"
      "v_max_u32 v56, v56, v255\n\t"
      "v_cmp_eq_u32 vcc, -1, v56\n\t"
      "s_sub_u32 s44, s44, 1\n\t"
      "s_cbranch_vccz L_g1_back1_%=\n\t"
      "s_cmp_lg_u32 s44, 0\n\t"
      "s_cbranch_scc1 L_g1_slow1_%=\n\t"
      "s_branch L_g1_back1_%=\n\t"
      "L_g1_slow2_%=:\n\t"
      "s_add_u32 m0, %[m0b], 32768\n\t"
      "s_add_u32 s42, %[xlo], 32768\n\t"
      "s_addc_u32 s43, %[xhi], 0\n\t"
      "global_load_lds_dwordx4 %[voff], s[42:43] offset:0 sc1\n\t"
      "global_load_lds_dwordx4 %[voff], s[42:43] offset:1024 sc1\n\t"
      "global_load_lds_dwordx4 %[voff], s[42:43] offset:2048 sc1\n\t"
      "global_load_lds_dwordx4 %[voff], s[42:43] offset:3072 sc1\n\t"
      "s_waitcnt vmcnt(0)\n\t"
      "ds_read_b128 v[240:243], %[vchk] offset:32768\n\t"
      "ds_read_b128 v[244:247], %[vchk] offset:33792\n\t"
      "ds_read_b128 v[248:251], %[vchk] offset:34816\n\t"
      "ds_read_b128 v[252:255], %[vchk] offset:35840\n\t"
      "s_waitcnt lgkmcnt(0)\n\t"
      "v_max3_u32 v56, v240, v241, v242\n\t"
      "v_max3_u32 v56, v56, v243, v244\n\t"
      "v_max3_u32 v56, v56, v245, v246\n\t"
      "v_max3_u32 v56, v56, v247, v248\n\t"
      "v_max3_u32 v56, v56, v249, v250\n\t"
      "v_max3_u32 v56, v56, v251, v252\n\t"
      "v_max3_u32 v56, v56, v253, v254\n\t"
      "v_max_u32 v56, v56, v255\n\t"
      "v_cmp_eq_u32 vcc, -1, v56\n\t"
      "s_sub_u32 s44, s44, 1\n\t"
      "s_cbranch_vccz L_g1_back2_%=\n\t"
      "s_cmp_lg_u32 s44, 0\n\t"
      "s_cbranch_scc1 L_g1_slow2_%=\n\t"
      "s_branch L_g1_back2_%=\n\t"
      "L_g1_slow3_%=:\n\t"
      "s_add_u32 m0, %[m0b], 49152\n\t"
      "s_add_u32 s42, %[xlo], 49152\n\t"
      "s_addc_u32 s43, %[xhi], 0\n\t"
      "global_load_lds_dwordx4 %[voff], s[42:43] offset:0 sc1\n\t"
      "global_load_lds_dwordx4 %[voff], s[42:43] offset:1024 sc1\n\t"
      "global_load_lds_dwordx4 %[voff], s[42:43] offset:2048 sc1\n\t"
      "global_load_lds_dwordx4 %[voff], s[42:43] offset:3072 sc1\n\t"
      "s_waitcnt vmcnt(0)\n\t"
      "ds_read_b128 v[240:243], %[vchk] offset:49152\n\t"
      "ds_read_b128 v[244:247], %[vchk] offset:50176\n\t"
      "ds_read_b128 v[248:251], %[vchk] offset:51200\n\t"
      "ds_read_b128 v[252:255], %[vchk] offset:52224\n\t"
      "s_waitcnt lgkmcnt(0)\n\t"
      "v_max3_u32 v56, v240, v241, v242\n\t"
      "v_max3_u32 v56, v56, v243, v244\n\t"
      "v_max3_u32 v56, v56, v245, v246\n\t"
      "v_max3_u32 v56, v56, v247, v248\n\t"
      "v_max3_u32 v56, v56, v249, v250\n\t"
      "v_max3_u32 v56, v56, v251, v252\n\t"
      "v_max3_u32 v56, v56, v253, v254\n\t"
      "v_max_u32 v56, v56, v255\n\t"
      "v_cmp_eq_u32 vcc, -1, v56\n\t"
      "s_sub_u32 s44, s44, 1\n\t"
      "s_cbranch_vccz L_g1_back3_%=\n\t"
      "s_cmp_lg_u32 s44, 0\n\t"
      "s_cbranch_scc1 L_g1_slow3_%=\n\t"
      "s_branch L_g1_back3_%=\n\t"
      "L_g1_slow4_%=:\n\t"
      "s_add_u32 m0, %[m0b], 65536\n\t"
      "s_add_u32 s42, %[xlo], 65536\n\t"
      "s_addc_u32 s43, %[xhi], 0\n\t"
      "global_load_lds_dwordx4 %[voff], s[42:43] offset:0 sc1\n\t"
      "global_load_lds_dwordx4 %[voff], s[42:43] offset:1024 sc1\n\t"
      "global_load_lds_dwordx4 %[voff], s[42:43] offset:2048 sc1\n\t"
      "global_load_lds_dwordx4 %[voff], s[42:43] offset:3072 sc1\n\t"
      "s_waitcnt vmcnt(0)\n\t"
      "ds_read_b128 v[240:243], v57 offset:0\n\t"
      "ds_read_b128 v[244:247], v57 offset:1024\n\t"
      "ds_read_b128 v[248:251], v57 offset:2048\n\t"
      "ds_read_b128 v[252:255], v57 offset:3072\n\t"
      "s_waitcnt lgkmcnt(0)\n\t"
      "v_max3_u32 v56, v240, v241, v242\n\t"
      "v_max3_u32 v56, v56, v243, v244\n\t"
      "v_max3_u32 v56, v56, v245, v246\n\t"
      "v_max3_u32 v56, v56, v247, v248\n\t"
      "v_max3_u32 v56, v56, v249, v250\n\t"
      "v_max3_u32 v56, v56, v251, v252\n\t"
      "v_max3_u32 v56, v56, v253, v254\n\t"
      "v_max_u32 v56, v56, v255\n\t"
      "v_cmp_eq_u32 vcc, -1, v56\n\t"
      "s_sub_u32 s44, s44, 1\n\t"
      "s_cbranch_vccz L_g1_back4_%=\n\t"
      "s_cmp_lg_u32 s44, 0\n\t"
      "s_cbranch_scc1 L_g1_slow4_%=\n\t"
      "s_branch L_g1_back4_%=\n\t"
      "L_g1_slow5_%=:\n\t"
      "s_add_u32 m0, %[m0b], 81920\n\t"
      "s_add_u32 s42, %[xlo], 81920\n\t"
      "s_addc_u32 s43, %[xhi], 0\n\t"
      "global_load_lds_dwordx4 %[voff], s[42:43] offset:0 sc1\n\t"
      "global_load_lds_dwordx4 %[voff], s[42:43] offset:1024 sc1\n\t"
      "global_load_lds_dwordx4 %[voff], s[42:43] offset:2048 sc1\n\t"
      "global_load_lds_dwordx4 %[voff], s[42:43] offset:3072 sc1\n\t"
      "s_waitcnt vmcnt(0)\n\t"
      "ds_read_b128 v[240:243], v57 offset:16384\n\t"
      "ds_read_b128 v[244:247], v57 offset:17408\n\t"
      "ds_read_b128 v[248:251], v57 offset:18432\n\t"
      "ds_read_b128 v[252:255], v57 offset:19456\n\t"
      "s_waitcnt lgkmcnt(0)\n\t"
      "v_max3_u32 v56, v240, v241, v242\n\t"
      "v_max3_u32 v56, v56, v243, v244\n\t"
      "v_max3_u32 v56, v56, v245, v246\n\t"
      "v_max3_u32 v56, v56, v247, v248\n\t"
      "v_max3_u32 v56, v56, v249, v250\n\t"
      "v_max3_u32 v56, v56, v251, v252\n\t"
      "v_max3_u32 v56, v56, v253, v254\n\t"
      "v_max_u32 v56, v56, v255\n\t"
      "v_cmp_eq_u32 vcc, -1, v56\n\t"
      "s_sub_u32 s44, s44, 1\n\t"
      "s_cbranch_vccz L_g1_back5_%=\n\t"
      "s_cmp_lg_u32 s44, 0\n\t"
      "s_cbranch_scc1 L_g1_slow5_%=\n\t"
      "s_branch L_g1_back5_%=\n\t"
      "\n\t"
      "L_g1_end%=:\n\t"
      "Lpd74eef_49:\n\t"
        : "=&{v[128:159]}"(accA), "=&{v[160:175]}"(accB), "=&{v[176:183]}"(accS), "=&{v[184:215]}"(pa), "=&{v[216:223]}"(pb), "=&{v[58:59]}"(fdv), "=&{v[224:239]}"(gv)
        : G1_IN : G1_CL,  "v48", "v49", "v50", "v51", "v52", "v53", "v54", "v55", "v56", "v57", "v60", "v61", "v62", "v63", "v64", "v65", "v66", "v67", "v68", "v69", "v70", "v71", "v72", "v73", "v74", "v75", "v76", "v77", "v78", "v79", "v80", "v81", "v82", "v83", "v84", "v85", "v86", "v87", "v88", "v89", "v90", "v91", "v92", "v93", "v94", "v95", "v96", "v97", "v98", "v99", "v100", "v101", "v102", "v103", "v104", "v105", "v106", "v107", "v108", "v109", "v110", "v111", "v112", "v113", "v114", "v115", "v116", "v117", "v118", "v119", "v120", "v121", "v122", "v123", "v124", "v125", "v126", "v127", "v240", "v241", "v242", "v243", "v244", "v245", "v246", "v247", "v248", "v249", "v250", "v251", "v252", "v253", "v254", "v255", "a0", "a1", "a2", "a3", "a4", "a5", "a6", "a7", "a8", "a9", "a10", "a11", "a12", "a13", "a14", "a15", "a16", "a17", "a18", "a19", "a20", "a21", "a22", "a23", "a24", "a25", "a26", "a27", "a28", "a29", "a30", "a31", "a32", "a33", "a34", "a35", "a36", "a37", "a38", "a39", "a40", "a41", "a42", "a43", "a44", "a45", "a46", "a47", "a48", "a49", "a50", "a51", "a52", "a53", "a54", "a55", "a56", "a57", "a58", "a59", "a60", "a61", "a62", "a63", "a64", "a65", "a66", "a67", "a68", "a69", "a70", "a71", "a72", "a73", "a74", "a75", "a76", "a77", "a78", "a79", "a80", "a81", "a82", "a83", "a84", "a85", "a86", "a87", "a88", "a89", "a90", "a91", "a92", "a93", "a94", "a95", "a96", "a97", "a98", "a99", "a100", "a101", "a102", "a103", "a104", "a105", "a106", "a107", "a108", "a109", "a110", "a111", "a112", "a113", "a114", "a115", "a116", "a117", "a118", "a119", "a120", "a121", "a122", "a123", "a124", "a125", "a126", "a127", "a128", "a129", "a130", "a131", "a132", "a133", "a134", "a135", "a136", "a137", "a138", "a139", "a140", "a141", "a142", "a143", "a144", "a145", "a146", "a147", "a148", "a149", "a150", "a151", "a152", "a153", "a154", "a155", "a156", "a157", "a158", "a159", "a160", "a161", "a162", "a163", "a164", "a165", "a166", "a167", "a168", "a169", "a170", "a171", "a172", "a173", "a174", "a175", "a176", "a177", "a178", "a179", "a180", "a181", "a182", "a183", "a184", "a185", "a186", "a187", "a188", "a189", "a190", "a191", "a192", "a193", "a194", "a195", "a196", "a197", "a198", "a199", "a200", "a201", "a202", "a203", "a204", "a205", "a206", "a207", "a208", "a209", "a210", "a211", "a212", "a213", "a214", "a215", "a216", "a217", "a218", "a219", "a220", "a221", "a222", "a223", "a224", "a225", "a226", "a227", "a228", "a229", "a230", "a231", "a232", "a233", "a234", "a235", "a236", "a237", "a238", "a239");

  asm volatile("Lpd74eef_50:");
    f32x4 s = {pb[0] + pb[4], pb[1] + pb[5], pb[2] + pb[6], pb[3] + pb[7]};
#pragma unroll
    for (int i = 0; i < 8; ++i) s += (f32x4){pa[4 * i], pa[4 * i + 1], pa[4 * i + 2], pa[4 * i + 3]};
#pragma unroll
    for (int i = 0; i < 4; ++i) {
      auto r = __builtin_amdgcn_permlane32_swap(__float_as_uint(s[i]), __float_as_uint(s[i]), false, false);
      s[i] = __uint_as_float(r[0]) + __uint_as_float(r[1]);
    }
    LDSF4(lds + L_CB + (w * 24 + c24) * 16) = s;
  asm volatile("Lpd74eef_51:");
    g1_epi(0);
  asm volatile("Lpd74eef_52:");
    asm volatile("s_waitcnt vmcnt(4)" : "+v"(fdv), "+v"(gv)::"memory");
    if (b == 0 && tid == 0) ST64(WS_EPOCH, T);
    u32x4 gbv[4];
#pragma unroll
    for (int i = 0; i < 4; ++i) gbv[i] = (u32x4){gv[4 * i], gv[4 * i + 1], gv[4 * i + 2], gv[4 * i + 3]};
    auto ok = [&]() -> bool { return __all((modx(gbv[0], gbv[1], T[0]) | modx(gbv[2], gbv[3], T[0]) | (fdv[0] ^ T[0]) | (fdv[1] ^ T[1])) == 0); };
    if (__builtin_expect(!ok(), 0)) {
#pragma unroll 1
      for (int sp = 0; sp < SPIN_CAP; ++sp) {
        asm volatile("" ::: "memory");
        fdv = LD64(px[5]);
#pragma unroll
        for (int i = 0; i < 4; ++i) gbv[i] = LD128(px[6] + i * 16);
        if (ok()) break;
      }
    }
    LDSF2(lds + L_GB + (lane < 40 ? lane : 39) * 8) = (f32x2){modv(gbv[0], gbv[1]), modv(gbv[2], gbv[3])};
  }
  asm volatile("Lpd74eef_53:");
  asm volatile(
      "s_mov_b32 s45, m0\n\t"
      "ds_read_b64_tr_b16 v[64:65], v44 offset:16384\n\t"
      "ds_read_b64_tr_b16 v[66:67], v44 offset:18432\n\t"
      "ds_read_b64_tr_b16 v[68:69], v45 offset:16384\n\t"
      "ds_read_b64_tr_b16 v[70:71], v45 offset:18432\n\t"
      "ds_read_b64_tr_b16 v[72:73], v46 offset:16384\n\t"
      "ds_read_b64_tr_b16 v[74:75], v46 offset:18432\n\t"
      "ds_read_b64_tr_b16 v[76:77], v47 offset:16384\n\t"
      "ds_read_b64_tr_b16 v[78:79], v47 offset:18432\n\t"
      "ds_read_b64_tr_b16 v[80:81], v44 offset:20480\n\t"
      "ds_read_b64_tr_b16 v[82:83], v44 offset:22528\n\t"
      "ds_read_b64_tr_b16 v[84:85], v45 offset:20480\n\t"
      "ds_read_b64_tr_b16 v[86:87], v45 offset:22528\n\t"
      "ds_read_b64_tr_b16 v[88:89], v46 offset:20480\n\t"
      "ds_read_b64_tr_b16 v[90:91], v46 offset:22528\n\t"
      "ds_read_b64_tr_b16 v[92:93], v47 offset:20480\n\t"
      "ds_read_b64_tr_b16 v[94:95], v47 offset:22528\n\t"
      "s_mov_b32 s44, 0x40000\n\t"
      "v_add_u32 v57, 0x10000, %[vchk]\n\t"
      "v_mov_b32 v52, 0x3f803f80\n\t"
      "v_mov_b32 v53, 0x3f803f80\n\t"
      "v_mov_b32 v54, 0x3f803f80\n\t"
      "v_mov_b32 v55, 0x3f803f80\n\t"
      "Lpd74eef_54:\n\t"
      "s_waitcnt lgkmcnt(0)\n\t"
      "v_mfma_f32_16x16x32_bf16 v[128:131], a[0:3], v[64:67], 0\n\t"
      "ds_read_b64_tr_b16 v[96:97], v44 offset:24576\n\t"
      "v_mfma_f32_16x16x32_bf16 v[144:147], a[4:7], v[64:67], 0\n\t"
      "s_add_u32 m0, %[m0b], 0\n\t"
      "s_add_u32 s36, %[xlo], 131072\n\t"
      "v_mfma_f32_16x16x32_bf16 v[160:163], a[8:11], v[64:67], 0\n\t"
      "ds_read_b64_tr_b16 v[98:99], v44 offset:26624\n\t"
      "ds_read_b64_tr_b16 v[100:101], v45 offset:24576\n\t"
      "v_mfma_f32_16x16x32_bf16 v[176:179], v[52:55], v[64:67], 0\n\t"
      "s_addc_u32 s37, %[xhi], 0\n\t"
      "global_load_lds_dwordx4 %[voff], s[36:37] offset:2048 sc1\n\t"
      "v_mfma_f32_16x16x32_bf16 v[180:183], v[64:67], v[64:67], 0\n\t"
      "ds_read_b64_tr_b16 v[102:103], v45 offset:26624\n\t"
      "ds_read_b64_tr_b16 v[104:105], v46 offset:24576\n\t"
      "v_mfma_f32_16x16x32_bf16 v[132:135], a[0:3], v[68:71], 0\n\t"
      "ds_read_b64_tr_b16 v[106:107], v46 offset:26624\n\t"
      "ds_read_b64_tr_b16 v[108:109], v47 offset:24576\n\t"
      "v_mfma_f32_16x16x32_bf16 v[148:151], a[4:7], v[68:71], 0\n\t"
      "ds_read_b64_tr_b16 v[110:111], v47 offset:26624\n\t"
      "v_mfma_f32_16x16x32_bf16 v[164:167], a[8:11], v[68:71], 0\n\t"
      "global_load_lds_dwordx4 %[voff], s[36:37] offset:3072 sc1\n\t"
      "v_mfma_f32_16x16x32_bf16 v[136:139], a[0:3], v[72:75], 0\n\t"
      "ds_read_b64_tr_b16 v[112:113], v44 offset:28672\n\t"
      "ds_read_b64_tr_b16 v[114:115], v44 offset:30720\n\t"
      "v_mfma_f32_16x16x32_bf16 v[152:155], a[4:7], v[72:75], 0\n\t"
      "ds_read_b64_tr_b16 v[116:117], v45 offset:28672\n\t"
      "v_mfma_f32_16x16x32_bf16 v[168:171], a[8:11], v[72:75], 0\n\t"
      "s_add_u32 m0, %[m0b], 16384\n\t"
      "s_add_u32 s36, %[xlo], 147456\n\t"
      "v_mfma_f32_16x16x32_bf16 v[140:143], a[0:3], v[76:79], 0\n\t"
      "ds_read_b64_tr_b16 v[118:119], v45 offset:30720\n\t"
      "ds_read_b64_tr_b16 v[120:121], v46 offset:28672\n\t"
      "v_mfma_f32_16x16x32_bf16 v[156:159], a[4:7], v[76:79], 0\n\t"
      "s_addc_u32 s37, %[xhi], 0\n\t"
      "global_load_lds_dwordx4 %[voff], s[36:37] offset:0 sc1\n\t"
      "v_mfma_f32_16x16x32_bf16 v[172:175], a[8:11], v[76:79], 0\n\t"
      "ds_read_b64_tr_b16 v[122:123], v46 offset:30720\n\t"
      "ds_read_b64_tr_b16 v[124:125], v47 offset:28672\n\t"
      "v_mfma_f32_16x16x32_bf16 v[128:131], a[12:15], v[80:83], v[128:131]\n\t"
      "ds_read_b64_tr_b16 v[126:127], v47 offset:30720\n\t"
      "v_mfma_f32_16x16x32_bf16 v[144:147], a[16:19], v[80:83], v[144:147]\n\t"
      "global_load_lds_dwordx4 %[voff], s[36:37] offset:1024 sc1\n\t"
      "v_mfma_f32_16x16x32_bf16 v[160:163], a[20:23], v[80:83], v[160:163]\n\t"
      "v_mfma_f32_16x16x32_bf16 v[176:179], v[52:55], v[80:83], v[176:179]\n\t"
      "ds_read_b128 v[240:243], v57 offset:32768\n\t"
      "v_mfma_f32_16x16x32_bf16 v[180:183], v[80:83], v[80:83], v[180:183]\n\t"
      "ds_read_b128 v[244:247], v57 offset:33792\n\t"
      "ds_read_b128 v[248:251], v57 offset:34816\n\t"
      "v_mfma_f32_16x16x32_bf16 v[132:135], a[12:15], v[84:87], v[132:135]\n\t"
      "ds_read_b128 v[252:255], v57 offset:35840\n\t"
      "v_mfma_f32_16x16x32_bf16 v[148:151], a[16:19], v[84:87], v[148:151]\n\t"
      "v_mfma_f32_16x16x32_bf16 v[164:167], a[20:23], v[84:87], v[164:167]\n\t"
      "v_mfma_f32_16x16x32_bf16 v[136:139], a[12:15], v[88:91], v[136:139]\n\t"
      "s_waitcnt lgkmcnt(0)\n\t"
      "v_max3_u32 v56, v240, v241, v242\n\t"
      "v_mfma_f32_16x16x32_bf16 v[152:155], a[16:19], v[88:91], v[152:155]\n\t"
      "v_max3_u32 v56, v56, v243, v244\n\t"
      "v_max3_u32 v56, v56, v245, v246\n\t"
      "v_mfma_f32_16x16x32_bf16 v[168:171], a[20:23], v[88:91], v[168:171]\n\t"
      "v_max3_u32 v56, v56, v247, v248\n\t"
      "v_max3_u32 v56, v56, v249, v250\n\t"
      "v_mfma_f32_16x16x32_bf16 v[140:143], a[12:15], v[92:95], v[140:143]\n\t"
      "v_max3_u32 v56, v56, v251, v252\n\t"
      "v_max3_u32 v56, v56, v253, v254\n\t"
      "v_mfma_f32_16x16x32_bf16 v[156:159], a[16:19], v[92:95], v[156:159]\n\t"
      "v_max_u32 v56, v56, v255\n\t"
      "v_cmp_eq_u32 vcc, -1, v56\n\t"
      "v_mfma_f32_16x16x32_bf16 v[172:175], a[20:23], v[92:95], v[172:175]\n\t"
      "s_cbranch_vccnz L_g1_slow6_%=\n\t"
      "L_g1_back6_%=:\n\t"
      "Lpd74eef_55:\n\t"
      "s_waitcnt lgkmcnt(0)\n\t"
      "s_barrier\n\t"
      "v_mfma_f32_16x16x32_bf16 v[128:131], a[24:27], v[96:99], v[128:131]\n\t"
      "ds_read_b64_tr_b16 v[64:65], v44 offset:32768\n\t"
      "v_mfma_f32_16x16x32_bf16 v[144:147], a[28:31], v[96:99], v[144:147]\n\t"
      "s_add_u32 m0, %[m0b], 16384\n\t"
      "s_add_u32 s36, %[xlo], 147456\n\t"
      "v_mfma_f32_16x16x32_bf16 v[160:163], a[32:35], v[96:99], v[160:163]\n\t"
      "ds_read_b64_tr_b16 v[66:67], v44 offset:34816\n\t"
      "v_mfma_f32_16x16x32_bf16 v[176:179], v[52:55], v[96:99], v[176:179]\n\t"
      "s_addc_u32 s37, %[xhi], 0\n\t"
      "global_load_lds_dwordx4 %[voff], s[36:37] offset:2048 sc1\n\t"
      "v_mfma_f32_16x16x32_bf16 v[180:183], v[96:99], v[96:99], v[180:183]\n\t"
      "ds_read_b64_tr_b16 v[68:69], v45 offset:32768\n\t"
      "ds_read_b64_tr_b16 v[70:71], v45 offset:34816\n\t"
      "v_mfma_f32_16x16x32_bf16 v[132:135], a[24:27], v[100:103], v[132:135]\n\t"
      "ds_read_b64_tr_b16 v[72:73], v46 offset:32768\n\t"
      "v_mfma_f32_16x16x32_bf16 v[148:151], a[28:31], v[100:103], v[148:151]\n\t"
      "global_load_lds_dwordx4 %[voff], s[36:37] offset:3072 sc1\n\t"
      "v_mfma_f32_16x16x32_bf16 v[164:167], a[32:35], v[100:103], v[164:167]\n\t"
      "ds_read_b64_tr_b16 v[74:75], v46 offset:34816\n\t"
      "ds_read_b64_tr_b16 v[76:77], v47 offset:32768\n\t"
      "v_mfma_f32_16x16x32_bf16 v[136:139], a[24:27], v[104:107], v[136:139]\n\t"
      "s_add_u32 s40, %[dlo], 4096\n\t"
      "s_addc_u32 s41, %[dhi], 0\n\t"
      "v_mfma_f32_16x16x32_bf16 v[152:155], a[28:31], v[104:107], v[152:155]\n\t"
      "ds_read_b64_tr_b16 v[78:79], v47 offset:34816\n\t"
      "ds_read_b64_tr_b16 v[80:81], v44 offset:36864\n\t"
      "v_mfma_f32_16x16x32_bf16 v[168:171], a[32:35], v[104:107], v[168:171]\n\t"
      "global_load_dwordx4 a[0:3], %[l16], s[40:41] offset:-4096 sc1\n\t"
      "v_mfma_f32_16x16x32_bf16 v[140:143], a[24:27], v[108:111], v[140:143]\n\t"
      "ds_read_b64_tr_b16 v[82:83], v44 offset:38912\n\t"
      "ds_read_b64_tr_b16 v[84:85], v45 offset:36864\n\t"
      "v_mfma_f32_16x16x32_bf16 v[156:159], a[28:31], v[108:111], v[156:159]\n\t"
      "global_load_dwordx4 a[4:7], %[l16], s[40:41] offset:-3072 sc1\n\t"
      "v_mfma_f32_16x16x32_bf16 v[172:175], a[32:35], v[108:111], v[172:175]\n\t"
      "ds_read_b64_tr_b16 v[86:87], v45 offset:38912\n\t"
      "ds_read_b64_tr_b16 v[88:89], v46 offset:36864\n\t"
      "v_mfma_f32_16x16x32_bf16 v[128:131], a[36:39], v[112:115], v[128:131]\n\t"
      "global_load_dwordx4 a[8:11], %[l16], s[40:41] offset:-2048 sc1\n\t"
      "v_mfma_f32_16x16x32_bf16 v[144:147], a[40:43], v[112:115], v[144:147]\n\t"
      "ds_read_b64_tr_b16 v[90:91], v46 offset:38912\n\t"
      "ds_read_b64_tr_b16 v[92:93], v47 offset:36864\n\t"
      "v_mfma_f32_16x16x32_bf16 v[160:163], a[44:47], v[112:115], v[160:163]\n\t"
      "global_load_dwordx4 a[12:15], %[l16], s[40:41] offset:-1024 sc1\n\t"
      "v_mfma_f32_16x16x32_bf16 v[176:179], v[52:55], v[112:115], v[176:179]\n\t"
      "ds_read_b64_tr_b16 v[94:95], v47 offset:38912\n\t"
      "v_mfma_f32_16x16x32_bf16 v[180:183], v[112:115], v[112:115], v[180:183]\n\t"
      "global_load_dwordx4 a[16:19], %[l16], s[40:41] offset:0 sc1\n\t"
      "v_mfma_f32_16x16x32_bf16 v[132:135], a[36:39], v[116:119], v[132:135]\n\t"
      "global_load_dwordx4 a[20:23], %[l16], s[40:41] offset:1024 sc1\n\t"
      "v_mfma_f32_16x16x32_bf16 v[148:151], a[40:43], v[116:119], v[148:151]\n\t"
      "v_mfma_f32_16x16x32_bf16 v[164:167], a[44:47], v[116:119], v[164:167]\n\t"
      "v_mfma_f32_16x16x32_bf16 v[136:139], a[36:39], v[120:123], v[136:139]\n\t"
      "v_mfma_f32_16x16x32_bf16 v[152:155], a[40:43], v[120:123], v[152:155]\n\t"
      "v_mfma_f32_16x16x32_bf16 v[168:171], a[44:47], v[120:123], v[168:171]\n\t"
      "v_mfma_f32_16x16x32_bf16 v[140:143], a[36:39], v[124:127], v[140:143]\n\t"
      "v_mfma_f32_16x16x32_bf16 v[156:159], a[40:43], v[124:127], v[156:159]\n\t"
      "v_mfma_f32_16x16x32_bf16 v[172:175], a[44:47], v[124:127], v[172:175]\n\t"
      "Lpd74eef_56:\n\t"
      "s_waitcnt lgkmcnt(0)\n\t"
      "v_mfma_f32_16x16x32_bf16 v[128:131], a[48:51], v[64:67], v[128:131]\n\t"
      "ds_read_b64_tr_b16 v[96:97], v44 offset:40960\n\t"
      "v_mfma_f32_16x16x32_bf16 v[144:147], a[52:55], v[64:67], v[144:147]\n\t"
      "global_load_dwordx4 a[24:27], %[l16], s[40:41] offset:2048 sc1\n\t"
      "v_mfma_f32_16x16x32_bf16 v[160:163], a[56:59], v[64:67], v[160:163]\n\t"
      "ds_read_b64_tr_b16 v[98:99], v44 offset:43008\n\t"
      "ds_read_b64_tr_b16 v[100:101], v45 offset:40960\n\t"
      "v_mfma_f32_16x16x32_bf16 v[176:179], v[52:55], v[64:67], v[176:179]\n\t"
      "global_load_dwordx4 a[28:31], %[l16], s[40:41] offset:3072 sc1\n\t"
      "v_mfma_f32_16x16x32_bf16 v[180:183], v[64:67], v[64:67], v[180:183]\n\t"
      "ds_read_b64_tr_b16 v[102:103], v45 offset:43008\n\t"
      "ds_read_b64_tr_b16 v[104:105], v46 offset:40960\n\t"
      "v_mfma_f32_16x16x32_bf16 v[132:135], a[48:51], v[68:71], v[132:135]\n\t"
      "ds_read_b64_tr_b16 v[106:107], v46 offset:43008\n\t"
      "v_mfma_f32_16x16x32_bf16 v[148:151], a[52:55], v[68:71], v[148:151]\n\t"
      "s_add_u32 s40, %[dlo], 12288\n\t"
      "s_addc_u32 s41, %[dhi], 0\n\t"
      "v_mfma_f32_16x16x32_bf16 v[164:167], a[56:59], v[68:71], v[164:167]\n\t"
      "ds_read_b64_tr_b16 v[108:109], v47 offset:40960\n\t"
      "ds_read_b64_tr_b16 v[110:111], v47 offset:43008\n\t"
      "v_mfma_f32_16x16x32_bf16 v[136:139], a[48:51], v[72:75], v[136:139]\n\t"
      "ds_read_b64_tr_b16 v[112:113], v44 offset:45056\n\t"
      "v_mfma_f32_16x16x32_bf16 v[152:155], a[52:55], v[72:75], v[152:155]\n\t"
      "global_load_dwordx4 a[32:35], %[l16], s[40:41] offset:-4096 sc1\n\t"
      "v_mfma_f32_16x16x32_bf16 v[168:171], a[56:59], v[72:75], v[168:171]\n\t"
      "ds_read_b64_tr_b16 v[114:115], v44 offset:47104\n\t"
      "ds_read_b64_tr_b16 v[116:117], v45 offset:45056\n\t"
      "v_mfma_f32_16x16x32_bf16 v[140:143], a[48:51], v[76:79], v[140:143]\n\t"
      "ds_read_b64_tr_b16 v[118:119], v45 offset:47104\n\t"
      "v_mfma_f32_16x16x32_bf16 v[156:159], a[52:55], v[76:79], v[156:159]\n\t"
      "global_load_dwordx4 a[36:39], %[l16], s[40:41] offset:-3072 sc1\n\t"
      "v_mfma_f32_16x16x32_bf16 v[172:175], a[56:59], v[76:79], v[172:175]\n\t"
      "ds_read_b64_tr_b16 v[120:121], v46 offset:45056\n\t"
      "ds_read_b64_tr_b16 v[122:123], v46 offset:47104\n\t"
      "v_mfma_f32_16x16x32_bf16 v[128:131], a[60:63], v[80:83], v[128:131]\n\t"
      "ds_read_b64_tr_b16 v[124:125], v47 offset:45056\n\t"
      "v_mfma_f32_16x16x32_bf16 v[144:147], a[64:67], v[80:83], v[144:147]\n\t"
      "global_load_dwordx4 a[40:43], %[l16], s[40:41] offset:-2048 sc1\n\t"
      "v_mfma_f32_16x16x32_bf16 v[160:163], a[68:71], v[80:83], v[160:163]\n\t"
      "ds_read_b64_tr_b16 v[126:127], v47 offset:47104\n\t"
      "v_mfma_f32_16x16x32_bf16 v[176:179], v[52:55], v[80:83], v[176:179]\n\t"
      "ds_read_b128 v[240:243], v57 offset:49152\n\t"
      "v_mfma_f32_16x16x32_bf16 v[180:183], v[80:83], v[80:83], v[180:183]\n\t"
      "ds_read_b128 v[244:247], v57 offset:50176\n\t"
      "ds_read_b128 v[248:251], v57 offset:51200\n\t"
      "v_mfma_f32_16x16x32_bf16 v[132:135], a[60:63], v[84:87], v[132:135]\n\t"
      "ds_read_b128 v[252:255], v57 offset:52224\n\t"
      "v_mfma_f32_16x16x32_bf16 v[148:151], a[64:67], v[84:87], v[148:151]\n\t"
      "global_load_dwordx4 a[44:47], %[l16], s[40:41] offset:-1024 sc1\n\t"
      "v_mfma_f32_16x16x32_bf16 v[164:167], a[68:71], v[84:87], v[164:167]\n\t"
      "v_mfma_f32_16x16x32_bf16 v[136:139], a[60:63], v[88:91], v[136:139]\n\t"
      "s_waitcnt lgkmcnt(0)\n\t"
      "v_max3_u32 v56, v240, v241, v242\n\t"
      "v_mfma_f32_16x16x32_bf16 v[152:155], a[64:67], v[88:91], v[152:155]\n\t"
      "v_max3_u32 v56, v56, v243, v244\n\t"
      "v_max3_u32 v56, v56, v245, v246\n\t"
      "v_mfma_f32_16x16x32_bf16 v[168:171], a[68:71], v[88:91], v[168:171]\n\t"
      "v_max3_u32 v56, v56, v247, v248\n\t"
      "v_max3_u32 v56, v56, v249, v250\n\t"
      "v_mfma_f32_16x16x32_bf16 v[140:143], a[60:63], v[92:95], v[140:143]\n\t"
      "v_max3_u32 v56, v56, v251, v252\n\t"
      "v_max3_u32 v56, v56, v253, v254\n\t"
      "v_mfma_f32_16x16x32_bf16 v[156:159], a[64:67], v[92:95], v[156:159]\n\t"
      "v_max_u32 v56, v56, v255\n\t"
      "v_cmp_eq_u32 vcc, -1, v56\n\t"
      "v_mfma_f32_16x16x32_bf16 v[172:175], a[68:71], v[92:95], v[172:175]\n\t"
      "s_cbranch_vccnz L_g1_slow7_%=\n\t"
      "L_g1_back7_%=:\n\t"
      "Lpd74eef_57:\n\t"
      "s_waitcnt lgkmcnt(0)\n\t"
      "s_barrier\n\t"
      "v_mfma_f32_16x16x32_bf16 v[128:131], a[72:75], v[96:99], v[128:131]\n\t"
      "ds_read_b64_tr_b16 v[64:65], v44 offset:49152\n\t"
      "v_mfma_f32_16x16x32_bf16 v[144:147], a[76:79], v[96:99], v[144:147]\n\t"
      "global_load_dwordx4 a[48:51], %[l16], s[40:41] offset:0 sc1\n\t"
      "v_mfma_f32_16x16x32_bf16 v[160:163], a[80:83], v[96:99], v[160:163]\n\t"
      "ds_read_b64_tr_b16 v[66:67], v44 offset:51200\n\t"
      "ds_read_b64_tr_b16 v[68:69], v45 offset:49152\n\t"
      "v_mfma_f32_16x16x32_bf16 v[176:179], v[52:55], v[96:99], v[176:179]\n\t"
      "global_load_dwordx4 a[52:55], %[l16], s[40:41] offset:1024 sc1\n\t"
      "v_mfma_f32_16x16x32_bf16 v[180:183], v[96:99], v[96:99], v[180:183]\n\t"
      "ds_read_b64_tr_b16 v[70:71], v45 offset:51200\n\t"
      "ds_read_b64_tr_b16 v[72:73], v46 offset:49152\n\t"
      "v_mfma_f32_16x16x32_bf16 v[132:135], a[72:75], v[100:103], v[132:135]\n\t"
      "ds_read_b64_tr_b16 v[74:75], v46 offset:51200\n\t"
      "v_mfma_f32_16x16x32_bf16 v[148:151], a[76:79], v[100:103], v[148:151]\n\t"
      "global_load_dwordx4 a[56:59], %[l16], s[40:41] offset:2048 sc1\n\t"
      "v_mfma_f32_16x16x32_bf16 v[164:167], a[80:83], v[100:103], v[164:167]\n\t"
      "ds_read_b64_tr_b16 v[76:77], v47 offset:49152\n\t"
      "ds_read_b64_tr_b16 v[78:79], v47 offset:51200\n\t"
      "v_mfma_f32_16x16x32_bf16 v[136:139], a[72:75], v[104:107], v[136:139]\n\t"
      "ds_read_b64_tr_b16 v[80:81], v44 offset:53248\n\t"
      "v_mfma_f32_16x16x32_bf16 v[152:155], a[76:79], v[104:107], v[152:155]\n\t"
      "global_load_dwordx4 a[60:63], %[l16], s[40:41] offset:3072 sc1\n\t"
      "v_mfma_f32_16x16x32_bf16 v[168:171], a[80:83], v[104:107], v[168:171]\n\t"
      "ds_read_b64_tr_b16 v[82:83], v44 offset:55296\n\t"
      "ds_read_b64_tr_b16 v[84:85], v45 offset:53248\n\t"
      "v_mfma_f32_16x16x32_bf16 v[140:143], a[72:75], v[108:111], v[140:143]\n\t"
      "ds_read_b64_tr_b16 v[86:87], v45 offset:55296\n\t"
      "v_mfma_f32_16x16x32_bf16 v[156:159], a[76:79], v[108:111], v[156:159]\n\t"
      "s_add_u32 s40, %[dlo], 20480\n\t"
      "s_addc_u32 s41, %[dhi], 0\n\t"
      "v_mfma_f32_16x16x32_bf16 v[172:175], a[80:83], v[108:111], v[172:175]\n\t"
      "ds_read_b64_tr_b16 v[88:89], v46 offset:53248\n\t"
      "ds_read_b64_tr_b16 v[90:91], v46 offset:55296\n\t"
      "v_mfma_f32_16x16x32_bf16 v[128:131], a[84:87], v[112:115], v[128:131]\n\t"
      "ds_read_b64_tr_b16 v[92:93], v47 offset:53248\n\t"
      "v_mfma_f32_16x16x32_bf16 v[144:147], a[88:91], v[112:115], v[144:147]\n\t"
      "global_load_dwordx4 a[64:67], %[l16], s[40:41] offset:-4096 sc1\n\t"
      "v_mfma_f32_16x16x32_bf16 v[160:163], a[92:95], v[112:115], v[160:163]\n\t"
      "ds_read_b64_tr_b16 v[94:95], v47 offset:55296\n\t"
      "v_mfma_f32_16x16x32_bf16 v[176:179], v[52:55], v[112:115], v[176:179]\n\t"
      "global_load_dwordx4 a[68:71], %[l16], s[40:41] offset:-3072 sc1\n\t"
      "v_mfma_f32_16x16x32_bf16 v[180:183], v[112:115], v[112:115], v[180:183]\n\t"
      "v_mfma_f32_16x16x32_bf16 v[132:135], a[84:87], v[116:119], v[132:135]\n\t"
      "v_mfma_f32_16x16x32_bf16 v[148:151], a[88:91], v[116:119], v[148:151]\n\t"
      "v_mfma_f32_16x16x32_bf16 v[164:167], a[92:95], v[116:119], v[164:167]\n\t"
      "v_mfma_f32_16x16x32_bf16 v[136:139], a[84:87], v[120:123], v[136:139]\n\t"
      "v_mfma_f32_16x16x32_bf16 v[152:155], a[88:91], v[120:123], v[152:155]\n\t"
      "v_mfma_f32_16x16x32_bf16 v[168:171], a[92:95], v[120:123], v[168:171]\n\t"
      "v_mfma_f32_16x16x32_bf16 v[140:143], a[84:87], v[124:127], v[140:143]\n\t"
      "v_mfma_f32_16x16x32_bf16 v[156:159], a[88:91], v[124:127], v[156:159]\n\t"
      "v_mfma_f32_16x16x32_bf16 v[172:175], a[92:95], v[124:127], v[172:175]\n\t"
      "Lpd74eef_58:\n\t"
      "s_waitcnt lgkmcnt(0)\n\t"
      "v_mfma_f32_16x16x32_bf16 v[128:131], a[96:99], v[64:67], v[128:131]\n\t"
      "ds_read_b64_tr_b16 v[96:97], v44 offset:57344\n\t"
      "v_mfma_f32_16x16x32_bf16 v[144:147], a[100:103], v[64:67], v[144:147]\n\t"
      "global_load_dwordx4 a[72:75], %[l16], s[40:41] offset:-2048 sc1\n\t"
      "v_mfma_f32_16x16x32_bf16 v[160:163], a[104:107], v[64:67], v[160:163]\n\t"
      "ds_read_b64_tr_b16 v[98:99], v44 offset:59392\n\t"
      "ds_read_b64_tr_b16 v[100:101], v45 offset:57344\n\t"
      "v_mfma_f32_16x16x32_bf16 v[176:179], v[52:55], v[64:67], v[176:179]\n\t"
      "ds_read_b64_tr_b16 v[102:103], v45 offset:59392\n\t"
      "v_mfma_f32_16x16x32_bf16 v[180:183], v[64:67], v[64:67], v[180:183]\n\t"
      "global_load_dwordx4 a[76:79], %[l16], s[40:41] offset:-1024 sc1\n\t"
      "v_mfma_f32_16x16x32_bf16 v[132:135], a[96:99], v[68:71], v[132:135]\n\t"
      "ds_read_b64_tr_b16 v[104:105], v46 offset:57344\n\t"
      "ds_read_b64_tr_b16 v[106:107], v46 offset:59392\n\t"
      "v_mfma_f32_16x16x32_bf16 v[148:151], a[100:103], v[68:71], v[148:151]\n\t"
      "ds_read_b64_tr_b16 v[108:109], v47 offset:57344\n\t"
      "v_mfma_f32_16x16x32_bf16 v[164:167], a[104:107], v[68:71], v[164:167]\n\t"
      "global_load_dwordx4 a[80:83], %[l16], s[40:41] offset:0 sc1\n\t"
      "v_mfma_f32_16x16x32_bf16 v[136:139], a[96:99], v[72:75], v[136:139]\n\t"
      "ds_read_b64_tr_b16 v[110:111], v47 offset:59392\n\t"
      "ds_read_b64_tr_b16 v[112:113], v44 offset:61440\n\t"
      "v_mfma_f32_16x16x32_bf16 v[152:155], a[100:103], v[72:75], v[152:155]\n\t"
      "ds_read_b64_tr_b16 v[114:115], v44 offset:63488\n\t"
      "ds_read_b64_tr_b16 v[116:117], v45 offset:61440\n\t"
      "v_mfma_f32_16x16x32_bf16 v[168:171], a[104:107], v[72:75], v[168:171]\n\t"
      "global_load_dwordx4 a[84:87], %[l16], s[40:41] offset:1024 sc1\n\t"
      "v_mfma_f32_16x16x32_bf16 v[140:143], a[96:99], v[76:79], v[140:143]\n\t"
      "ds_read_b64_tr_b16 v[118:119], v45 offset:63488\n\t"
      "ds_read_b64_tr_b16 v[120:121], v46 offset:61440\n\t"
      "v_mfma_f32_16x16x32_bf16 v[156:159], a[100:103], v[76:79], v[156:159]\n\t"
      "ds_read_b64_tr_b16 v[122:123], v46 offset:63488\n\t"
      "v_mfma_f32_16x16x32_bf16 v[172:175], a[104:107], v[76:79], v[172:175]\n\t"
      "global_load_dwordx4 a[88:91], %[l16], s[40:41] offset:2048 sc1\n\t"
      "v_mfma_f32_16x16x32_bf16 v[128:131], a[108:111], v[80:83], v[128:131]\n\t"
      "ds_read_b64_tr_b16 v[124:125], v47 offset:61440\n\t"
      "ds_read_b64_tr_b16 v[126:127], v47 offset:63488\n\t"
      "v_mfma_f32_16x16x32_bf16 v[144:147], a[112:115], v[80:83], v[144:147]\n\t"
      "global_load_dwordx4 a[92:95], %[l16], s[40:41] offset:3072 sc1\n\t"
      "v_mfma_f32_16x16x32_bf16 v[160:163], a[116:119], v[80:83], v[160:163]\n\t"
      "v_mfma_f32_16x16x32_bf16 v[176:179], v[52:55], v[80:83], v[176:179]\n\t"
      "s_waitcnt vmcnt(28)\n\t"
      "ds_read_b128 v[240:243], %[vchk] offset:0\n\t"
      "v_mfma_f32_16x16x32_bf16 v[180:183], v[80:83], v[80:83], v[180:183]\n\t"
      "ds_read_b128 v[244:247], %[vchk] offset:1024\n\t"
      "ds_read_b128 v[248:251], %[vchk] offset:2048\n\t"
      "v_mfma_f32_16x16x32_bf16 v[132:135], a[108:111], v[84:87], v[132:135]\n\t"
      "ds_read_b128 v[252:255], %[vchk] offset:3072\n\t"
      "v_mfma_f32_16x16x32_bf16 v[148:151], a[112:115], v[84:87], v[148:151]\n\t"
      "v_mfma_f32_16x16x32_bf16 v[164:167], a[116:119], v[84:87], v[164:167]\n\t"
      "v_mfma_f32_16x16x32_bf16 v[136:139], a[108:111], v[88:91], v[136:139]\n\t"
      "s_waitcnt lgkmcnt(0)\n\t"
      "v_max3_u32 v56, v240, v241, v242\n\t"
      "v_mfma_f32_16x16x32_bf16 v[152:155], a[112:115], v[88:91], v[152:155]\n\t"
      "v_max3_u32 v56, v56, v243, v244\n\t"
      "v_max3_u32 v56, v56, v245, v246\n\t"
      "v_mfma_f32_16x16x32_bf16 v[168:171], a[116:119], v[88:91], v[168:171]\n\t"
      "v_max3_u32 v56, v56, v247, v248\n\t"
      "v_max3_u32 v56, v56, v249, v250\n\t"
      "v_mfma_f32_16x16x32_bf16 v[140:143], a[108:111], v[92:95], v[140:143]\n\t"
      "v_max3_u32 v56, v56, v251, v252\n\t"
      "v_max3_u32 v56, v56, v253, v254\n\t"
      "v_mfma_f32_16x16x32_bf16 v[156:159], a[112:115], v[92:95], v[156:159]\n\t"
      "v_max_u32 v56, v56, v255\n\t"
      "v_cmp_eq_u32 vcc, -1, v56\n\t"
      "v_mfma_f32_16x16x32_bf16 v[172:175], a[116:119], v[92:95], v[172:175]\n\t"
      "s_cbranch_vccnz L_g1_slow8_%=\n\t"
      "L_g1_back8_%=:\n\t"
      "Lpd74eef_59:\n\t"
      "s_waitcnt lgkmcnt(0)\n\t"
      "s_barrier\n\t"
      "v_mfma_f32_16x16x32_bf16 v[128:131], a[120:123], v[96:99], v[128:131]\n\t"
      "ds_read_b64_tr_b16 v[64:65], v40 offset:0\n\t"
      "v_mfma_f32_16x16x32_bf16 v[144:147], a[124:127], v[96:99], v[144:147]\n\t"
      "s_add_u32 s40, %[dlo], 28672\n\t"
      "s_addc_u32 s41, %[dhi], 0\n\t"
      "v_mfma_f32_16x16x32_bf16 v[160:163], a[128:131], v[96:99], v[160:163]\n\t"
      "ds_read_b64_tr_b16 v[66:67], v40 offset:2048\n\t"
      "ds_read_b64_tr_b16 v[68:69], v41 offset:0\n\t"
      "v_mfma_f32_16x16x32_bf16 v[176:179], v[52:55], v[96:99], v[176:179]\n\t"
      "global_load_dwordx4 a[96:99], %[l16], s[40:41] offset:-4096 sc1\n\t"
      "v_mfma_f32_16x16x32_bf16 v[180:183], v[96:99], v[96:99], v[180:183]\n\t"
      "ds_read_b64_tr_b16 v[70:71], v41 offset:2048\n\t"
      "ds_read_b64_tr_b16 v[72:73], v42 offset:0\n\t"
      "v_mfma_f32_16x16x32_bf16 v[132:135], a[120:123], v[100:103], v[132:135]\n\t"
      "global_load_dwordx4 a[100:103], %[l16], s[40:41] offset:-3072 sc1\n\t"
      "v_mfma_f32_16x16x32_bf16 v[148:151], a[124:127], v[100:103], v[148:151]\n\t"
      "ds_read_b64_tr_b16 v[74:75], v42 offset:2048\n\t"
      "v_mfma_f32_16x16x32_bf16 v[164:167], a[128:131], v[100:103], v[164:167]\n\t"
      "global_load_dwordx4 a[104:107], %[l16], s[40:41] offset:-2048 sc1\n\t"
      "v_mfma_f32_16x16x32_bf16 v[136:139], a[120:123], v[104:107], v[136:139]\n\t"
      "ds_read_b64_tr_b16 v[76:77], v43 offset:0\n\t"
      "ds_read_b64_tr_b16 v[78:79], v43 offset:2048\n\t"
      "v_mfma_f32_16x16x32_bf16 v[152:155], a[124:127], v[104:107], v[152:155]\n\t"
      "ds_read_b64_tr_b16 v[80:81], v40 offset:4096\n\t"
      "v_mfma_f32_16x16x32_bf16 v[168:171], a[128:131], v[104:107], v[168:171]\n\t"
      "global_load_dwordx4 a[108:111], %[l16], s[40:41] offset:-1024 sc1\n\t"
      "v_mfma_f32_16x16x32_bf16 v[140:143], a[120:123], v[108:111], v[140:143]\n\t"
      "ds_read_b64_tr_b16 v[82:83], v40 offset:6144\n\t"
      "ds_read_b64_tr_b16 v[84:85], v41 offset:4096\n\t"
      "v_mfma_f32_16x16x32_bf16 v[156:159], a[124:127], v[108:111], v[156:159]\n\t"
      "global_load_dwordx4 a[112:115], %[l16], s[40:41] offset:0 sc1\n\t"
      "v_mfma_f32_16x16x32_bf16 v[172:175], a[128:131], v[108:111], v[172:175]\n\t"
      "ds_read_b64_tr_b16 v[86:87], v41 offset:6144\n\t"
      "v_mfma_f32_16x16x32_bf16 v[128:131], a[132:135], v[112:115], v[128:131]\n\t"
      "global_load_dwordx4 a[116:119], %[l16], s[40:41] offset:1024 sc1\n\t"
      "v_mfma_f32_16x16x32_bf16 v[144:147], a[136:139], v[112:115], v[144:147]\n\t"
      "ds_read_b64_tr_b16 v[88:89], v42 offset:4096\n\t"
      "ds_read_b64_tr_b16 v[90:91], v42 offset:6144\n\t"
      "v_mfma_f32_16x16x32_bf16 v[160:163], a[140:143], v[112:115], v[160:163]\n\t"
      "s_add_u32 s38, %[hlo], 4096\n\t"
      "s_addc_u32 s39, %[hhi], 0\n\t"
      "v_mfma_f32_16x16x32_bf16 v[176:179], v[52:55], v[112:115], v[176:179]\n\t"
      "ds_read_b64_tr_b16 v[92:93], v43 offset:4096\n\t"
      "ds_read_b64_tr_b16 v[94:95], v43 offset:6144\n\t"
      "v_mfma_f32_16x16x32_bf16 v[180:183], v[112:115], v[112:115], v[180:183]\n\t"
      "global_load_dwordx4 v[208:211], %[l16], s[38:39] offset:-4096 sc1\n\t"
      "v_mfma_f32_16x16x32_bf16 v[132:135], a[132:135], v[116:119], v[132:135]\n\t"
      "global_load_dwordx4 v[212:215], %[l16], s[38:39] offset:-3072 sc1\n\t"
      "v_mfma_f32_16x16x32_bf16 v[148:151], a[136:139], v[116:119], v[148:151]\n\t"
      "v_mfma_f32_16x16x32_bf16 v[164:167], a[140:143], v[116:119], v[164:167]\n\t"
      "v_mfma_f32_16x16x32_bf16 v[136:139], a[132:135], v[120:123], v[136:139]\n\t"
      "v_mfma_f32_16x16x32_bf16 v[152:155], a[136:139], v[120:123], v[152:155]\n\t"
      "v_mfma_f32_16x16x32_bf16 v[168:171], a[140:143], v[120:123], v[168:171]\n\t"
      "v_mfma_f32_16x16x32_bf16 v[140:143], a[132:135], v[124:127], v[140:143]\n\t"
      "v_mfma_f32_16x16x32_bf16 v[156:159], a[136:139], v[124:127], v[156:159]\n\t"
      "v_mfma_f32_16x16x32_bf16 v[172:175], a[140:143], v[124:127], v[172:175]\n\t"
      "Lpd74eef_60:\n\t"
      "s_waitcnt lgkmcnt(0)\n\t"
      "v_mfma_f32_16x16x32_bf16 v[128:131], a[144:147], v[64:67], v[128:131]\n\t"
      "ds_read_b64_tr_b16 v[96:97], v40 offset:8192\n\t"
      "v_mfma_f32_16x16x32_bf16 v[144:147], a[148:151], v[64:67], v[144:147]\n\t"
      "global_load_dwordx4 a[120:123], %[l16], s[40:41] offset:2048 sc1\n\t"
      "v_mfma_f32_16x16x32_bf16 v[160:163], a[152:155], v[64:67], v[160:163]\n\t"
      "ds_read_b64_tr_b16 v[98:99], v40 offset:10240\n\t"
      "ds_read_b64_tr_b16 v[100:101], v41 offset:8192\n\t"
      "v_mfma_f32_16x16x32_bf16 v[176:179], v[52:55], v[64:67], v[176:179]\n\t"
      "global_load_dwordx4 a[124:127], %[l16], s[40:41] offset:3072 sc1\n\t"
      "v_mfma_f32_16x16x32_bf16 v[180:183], v[64:67], v[64:67], v[180:183]\n\t"
      "ds_read_b64_tr_b16 v[102:103], v41 offset:10240\n\t"
      "ds_read_b64_tr_b16 v[104:105], v42 offset:8192\n\t"
      "v_mfma_f32_16x16x32_bf16 v[132:135], a[144:147], v[68:71], v[132:135]\n\t"
      "s_add_u32 s40, %[dlo], 36864\n\t"
      "s_addc_u32 s41, %[dhi], 0\n\t"
      "v_mfma_f32_16x16x32_bf16 v[148:151], a[148:151], v[68:71], v[148:151]\n\t"
      "ds_read_b64_tr_b16 v[106:107], v42 offset:10240\n\t"
      "ds_read_b64_tr_b16 v[108:109], v43 offset:8192\n\t"
      "v_mfma_f32_16x16x32_bf16 v[164:167], a[152:155], v[68:71], v[164:167]\n\t"
      "global_load_dwordx4 a[128:131], %[l16], s[40:41] offset:-4096 sc1\n\t"
      "v_mfma_f32_16x16x32_bf16 v[136:139], a[144:147], v[72:75], v[136:139]\n\t"
      "ds_read_b64_tr_b16 v[110:111], v43 offset:10240\n\t"
      "ds_read_b64_tr_b16 v[112:113], v40 offset:12288\n\t"
      "v_mfma_f32_16x16x32_bf16 v[152:155], a[148:151], v[72:75], v[152:155]\n\t"
      "global_load_dwordx4 a[132:135], %[l16], s[40:41] offset:-3072 sc1\n\t"
      "v_mfma_f32_16x16x32_bf16 v[168:171], a[152:155], v[72:75], v[168:171]\n\t"
      "ds_read_b64_tr_b16 v[114:115], v40 offset:14336\n\t"
      "ds_read_b64_tr_b16 v[116:117], v41 offset:12288\n\t"
      "v_mfma_f32_16x16x32_bf16 v[140:143], a[144:147], v[76:79], v[140:143]\n\t"
      "ds_read_b64_tr_b16 v[118:119], v41 offset:14336\n\t"
      "v_mfma_f32_16x16x32_bf16 v[156:159], a[148:151], v[76:79], v[156:159]\n\t"
      "global_load_dwordx4 a[136:139], %[l16], s[40:41] offset:-2048 sc1\n\t"
      "v_mfma_f32_16x16x32_bf16 v[172:175], a[152:155], v[76:79], v[172:175]\n\t"
      "ds_read_b64_tr_b16 v[120:121], v42 offset:12288\n\t"
      "ds_read_b64_tr_b16 v[122:123], v42 offset:14336\n\t"
      "v_mfma_f32_16x16x32_bf16 v[128:131], a[156:159], v[80:83], v[128:131]\n\t"
      "global_load_dwordx4 a[140:143], %[l16], s[40:41] offset:-1024 sc1\n\t"
      "v_mfma_f32_16x16x32_bf16 v[144:147], a[160:163], v[80:83], v[144:147]\n\t"
      "ds_read_b64_tr_b16 v[124:125], v43 offset:12288\n\t"
      "ds_read_b64_tr_b16 v[126:127], v43 offset:14336\n\t"
      "v_mfma_f32_16x16x32_bf16 v[160:163], a[164:167], v[80:83], v[160:163]\n\t"
      "global_load_dwordx4 v[216:219], %[l16], s[38:39] offset:0 sc1\n\t"
      "v_mfma_f32_16x16x32_bf16 v[176:179], v[52:55], v[80:83], v[176:179]\n\t"
      "s_waitcnt vmcnt(39)\n\t"
      "ds_read_b128 v[240:243], %[vchk] offset:16384\n\t"
      "v_mfma_f32_16x16x32_bf16 v[180:183], v[80:83], v[80:83], v[180:183]\n\t"
      "ds_read_b128 v[244:247], %[vchk] offset:17408\n\t"
      "ds_read_b128 v[248:251], %[vchk] offset:18432\n\t"
      "v_mfma_f32_16x16x32_bf16 v[132:135], a[156:159], v[84:87], v[132:135]\n\t"
      "ds_read_b128 v[252:255], %[vchk] offset:19456\n\t"
      "v_mfma_f32_16x16x32_bf16 v[148:151], a[160:163], v[84:87], v[148:151]\n\t"
      "global_load_dwordx4 v[220:223], %[l16], s[38:39] offset:1024 sc1\n\t"
      "v_mfma_f32_16x16x32_bf16 v[164:167], a[164:167], v[84:87], v[164:167]\n\t"
      "v_mfma_f32_16x16x32_bf16 v[136:139], a[156:159], v[88:91], v[136:139]\n\t"
      "s_waitcnt lgkmcnt(0)\n\t"
      "v_max3_u32 v56, v240, v241, v242\n\t"
      "v_mfma_f32_16x16x32_bf16 v[152:155], a[160:163], v[88:91], v[152:155]\n\t"
      "v_max3_u32 v56, v56, v243, v244\n\t"
      "v_max3_u32 v56, v56, v245, v246\n\t"
      "v_mfma_f32_16x16x32_bf16 v[168:171], a[164:167], v[88:91], v[168:171]\n\t"
      "v_max3_u32 v56, v56, v247, v248\n\t"
      "v_max3_u32 v56, v56, v249, v250\n\t"
      "v_mfma_f32_16x16x32_bf16 v[140:143], a[156:159], v[92:95], v[140:143]\n\t"
      "v_max3_u32 v56, v56, v251, v252\n\t"
      "v_max3_u32 v56, v56, v253, v254\n\t"
      "v_mfma_f32_16x16x32_bf16 v[156:159], a[160:163], v[92:95], v[156:159]\n\t"
      "v_max_u32 v56, v56, v255\n\t"
      "v_cmp_eq_u32 vcc, -1, v56\n\t"
      "v_mfma_f32_16x16x32_bf16 v[172:175], a[164:167], v[92:95], v[172:175]\n\t"
      "s_cbranch_vccnz L_g1_slow9_%=\n\t"
      "L_g1_back9_%=:\n\t"
      "Lpd74eef_61:\n\t"
      "s_waitcnt lgkmcnt(0)\n\t"
      "s_barrier\n\t"
      "v_mfma_f32_16x16x32_bf16 v[128:131], a[168:171], v[96:99], v[128:131]\n\t"
      "ds_read_b64_tr_b16 v[64:65], v40 offset:16384\n\t"
      "v_mfma_f32_16x16x32_bf16 v[144:147], a[172:175], v[96:99], v[144:147]\n\t"
      "global_load_dwordx4 a[144:147], %[l16], s[40:41] offset:0 sc1\n\t"
      "v_mfma_f32_16x16x32_bf16 v[160:163], a[176:179], v[96:99], v[160:163]\n\t"
      "ds_read_b64_tr_b16 v[66:67], v40 offset:18432\n\t"
      "ds_read_b64_tr_b16 v[68:69], v41 offset:16384\n\t"
      "v_mfma_f32_16x16x32_bf16 v[176:179], v[52:55], v[96:99], v[176:179]\n\t"
      "global_load_dwordx4 a[148:151], %[l16], s[40:41] offset:1024 sc1\n\t"
      "v_mfma_f32_16x16x32_bf16 v[180:183], v[96:99], v[96:99], v[180:183]\n\t"
      "ds_read_b64_tr_b16 v[70:71], v41 offset:18432\n\t"
      "ds_read_b64_tr_b16 v[72:73], v42 offset:16384\n\t"
      "v_mfma_f32_16x16x32_bf16 v[132:135], a[168:171], v[100:103], v[132:135]\n\t"
      "global_load_dwordx4 a[152:155], %[l16], s[40:41] offset:2048 sc1\n\t"
      "v_mfma_f32_16x16x32_bf16 v[148:151], a[172:175], v[100:103], v[148:151]\n\t"
      "ds_read_b64_tr_b16 v[74:75], v42 offset:18432\n\t"
      "v_mfma_f32_16x16x32_bf16 v[164:167], a[176:179], v[100:103], v[164:167]\n\t"
      "global_load_dwordx4 a[156:159], %[l16], s[40:41] offset:3072 sc1\n\t"
      "v_mfma_f32_16x16x32_bf16 v[136:139], a[168:171], v[104:107], v[136:139]\n\t"
      "ds_read_b64_tr_b16 v[76:77], v43 offset:16384\n\t"
      "ds_read_b64_tr_b16 v[78:79], v43 offset:18432\n\t"
      "v_mfma_f32_16x16x32_bf16 v[152:155], a[172:175], v[104:107], v[152:155]\n\t"
      "ds_read_b64_tr_b16 v[80:81], v40 offset:20480\n\t"
      "v_mfma_f32_16x16x32_bf16 v[168:171], a[176:179], v[104:107], v[168:171]\n\t"
      "s_add_u32 s40, %[dlo], 45056\n\t"
      "s_addc_u32 s41, %[dhi], 0\n\t"
      "v_mfma_f32_16x16x32_bf16 v[140:143], a[168:171], v[108:111], v[140:143]\n\t"
      "ds_read_b64_tr_b16 v[82:83], v40 offset:22528\n\t"
      "ds_read_b64_tr_b16 v[84:85], v41 offset:20480\n\t"
      "v_mfma_f32_16x16x32_bf16 v[156:159], a[172:175], v[108:111], v[156:159]\n\t"
      "global_load_dwordx4 a[160:163], %[l16], s[40:41] offset:-4096 sc1\n\t"
      "v_mfma_f32_16x16x32_bf16 v[172:175], a[176:179], v[108:111], v[172:175]\n\t"
      "ds_read_b64_tr_b16 v[86:87], v41 offset:22528\n\t"
      "v_mfma_f32_16x16x32_bf16 v[128:131], a[180:183], v[112:115], v[128:131]\n\t"
      "global_load_dwordx4 a[164:167], %[l16], s[40:41] offset:-3072 sc1\n\t"
      "v_mfma_f32_16x16x32_bf16 v[144:147], a[184:187], v[112:115], v[144:147]\n\t"
      "ds_read_b64_tr_b16 v[88:89], v42 offset:20480\n\t"
      "ds_read_b64_tr_b16 v[90:91], v42 offset:22528\n\t"
      "v_mfma_f32_16x16x32_bf16 v[160:163], a[188:191], v[112:115], v[160:163]\n\t"
      "s_add_u32 s38, %[hlo], 12288\n\t"
      "s_addc_u32 s39, %[hhi], 0\n\t"
      "v_mfma_f32_16x16x32_bf16 v[176:179], v[52:55], v[112:115], v[176:179]\n\t"
      "ds_read_b64_tr_b16 v[92:93], v43 offset:20480\n\t"
      "ds_read_b64_tr_b16 v[94:95], v43 offset:22528\n\t"
      "v_mfma_f32_16x16x32_bf16 v[180:183], v[112:115], v[112:115], v[180:183]\n\t"
      "global_load_dwordx4 v[224:227], %[l16], s[38:39] offset:-4096 sc1\n\t"
      "v_mfma_f32_16x16x32_bf16 v[132:135], a[180:183], v[116:119], v[132:135]\n\t"
      "global_load_dwordx4 v[228:231], %[l16], s[38:39] offset:-3072 sc1\n\t"
      "v_mfma_f32_16x16x32_bf16 v[148:151], a[184:187], v[116:119], v[148:151]\n\t"
      "v_mfma_f32_16x16x32_bf16 v[164:167], a[188:191], v[116:119], v[164:167]\n\t"
      "v_mfma_f32_16x16x32_bf16 v[136:139], a[180:183], v[120:123], v[136:139]\n\t"
      "v_mfma_f32_16x16x32_bf16 v[152:155], a[184:187], v[120:123], v[152:155]\n\t"
      "v_mfma_f32_16x16x32_bf16 v[168:171], a[188:191], v[120:123], v[168:171]\n\t"
      "v_mfma_f32_16x16x32_bf16 v[140:143], a[180:183], v[124:127], v[140:143]\n\t"
      "v_mfma_f32_16x16x32_bf16 v[156:159], a[184:187], v[124:127], v[156:159]\n\t"
      "v_mfma_f32_16x16x32_bf16 v[172:175], a[188:191], v[124:127], v[172:175]\n\t"
      "Lpd74eef_62:\n\t"
      "s_waitcnt lgkmcnt(0)\n\t"
      "v_mfma_f32_16x16x32_bf16 v[128:131], a[192:195], v[64:67], v[128:131]\n\t"
      "ds_read_b64_tr_b16 v[96:97], v40 offset:24576\n\t"
      "v_mfma_f32_16x16x32_bf16 v[144:147], a[196:199], v[64:67], v[144:147]\n\t"
      "global_load_dwordx4 a[168:171], %[l16], s[40:41] offset:-2048 sc1\n\t"
      "v_mfma_f32_16x16x32_bf16 v[160:163], a[200:203], v[64:67], v[160:163]\n\t"
      "ds_read_b64_tr_b16 v[98:99], v40 offset:26624\n\t"
      "ds_read_b64_tr_b16 v[100:101], v41 offset:24576\n\t"
      "v_mfma_f32_16x16x32_bf16 v[176:179], v[52:55], v[64:67], v[176:179]\n\t"
      "global_load_dwordx4 a[172:175], %[l16], s[40:41] offset:-1024 sc1\n\t"
      "v_mfma_f32_16x16x32_bf16 v[180:183], v[64:67], v[64:67], v[180:183]\n\t"
      "ds_read_b64_tr_b16 v[102:103], v41 offset:26624\n\t"
      "ds_read_b64_tr_b16 v[104:105], v42 offset:24576\n\t"
      "v_mfma_f32_16x16x32_bf16 v[132:135], a[192:195], v[68:71], v[132:135]\n\t"
      "global_load_dwordx4 a[176:179], %[l16], s[40:41] offset:0 sc1\n\t"
      "v_mfma_f32_16x16x32_bf16 v[148:151], a[196:199], v[68:71], v[148:151]\n\t"
      "ds_read_b64_tr_b16 v[106:107], v42 offset:26624\n\t"
      "ds_read_b64_tr_b16 v[108:109], v43 offset:24576\n\t"
      "v_mfma_f32_16x16x32_bf16 v[164:167], a[200:203], v[68:71], v[164:167]\n\t"
      "global_load_dwordx4 a[180:183], %[l16], s[40:41] offset:1024 sc1\n\t"
      "v_mfma_f32_16x16x32_bf16 v[136:139], a[192:195], v[72:75], v[136:139]\n\t"
      "ds_read_b64_tr_b16 v[110:111], v43 offset:26624\n\t"
      "ds_read_b64_tr_b16 v[112:113], v40 offset:28672\n\t"
      "v_mfma_f32_16x16x32_bf16 v[152:155], a[196:199], v[72:75], v[152:155]\n\t"
      "global_load_dwordx4 a[184:187], %[l16], s[40:41] offset:2048 sc1\n\t"
      "v_mfma_f32_16x16x32_bf16 v[168:171], a[200:203], v[72:75], v[168:171]\n\t"
      "ds_read_b64_tr_b16 v[114:115], v40 offset:30720\n\t"
      "ds_read_b64_tr_b16 v[116:117], v41 offset:28672\n\t"
      "v_mfma_f32_16x16x32_bf16 v[140:143], a[192:195], v[76:79], v[140:143]\n\t"
      "ds_read_b64_tr_b16 v[118:119], v41 offset:30720\n\t"
      "v_mfma_f32_16x16x32_bf16 v[156:159], a[196:199], v[76:79], v[156:159]\n\t"
      "global_load_dwordx4 a[188:191], %[l16], s[40:41] offset:3072 sc1\n\t"
      "v_mfma_f32_16x16x32_bf16 v[172:175], a[200:203], v[76:79], v[172:175]\n\t"
      "ds_read_b64_tr_b16 v[120:121], v42 offset:28672\n\t"
      "ds_read_b64_tr_b16 v[122:123], v42 offset:30720\n\t"
      "v_mfma_f32_16x16x32_bf16 v[128:131], a[204:207], v[80:83], v[128:131]\n\t"
      "s_add_u32 s38, %[hlo], 16384\n\t"
      "s_addc_u32 s39, %[hhi], 0\n\t"
      "v_mfma_f32_16x16x32_bf16 v[144:147], a[208:211], v[80:83], v[144:147]\n\t"
      "ds_read_b64_tr_b16 v[124:125], v43 offset:28672\n\t"
      "ds_read_b64_tr_b16 v[126:127], v43 offset:30720\n\t"
      "v_mfma_f32_16x16x32_bf16 v[160:163], a[212:215], v[80:83], v[160:163]\n\t"
      "global_load_dwordx4 v[232:235], %[l16], s[38:39] offset:-4096 sc1\n\t"
      "v_mfma_f32_16x16x32_bf16 v[176:179], v[52:55], v[80:83], v[176:179]\n\t"
      "global_load_dwordx4 v[236:239], %[l16], s[38:39] offset:-3072 sc1\n\t"
      "v_mfma_f32_16x16x32_bf16 v[180:183], v[80:83], v[80:83], v[180:183]\n\t"
      "v_mfma_f32_16x16x32_bf16 v[132:135], a[204:207], v[84:87], v[132:135]\n\t"
      "v_mfma_f32_16x16x32_bf16 v[148:151], a[208:211], v[84:87], v[148:151]\n\t"
      "v_mfma_f32_16x16x32_bf16 v[164:167], a[212:215], v[84:87], v[164:167]\n\t"
      "v_mfma_f32_16x16x32_bf16 v[136:139], a[204:207], v[88:91], v[136:139]\n\t"
      "v_mfma_f32_16x16x32_bf16 v[152:155], a[208:211], v[88:91], v[152:155]\n\t"
      "v_mfma_f32_16x16x32_bf16 v[168:171], a[212:215], v[88:91], v[168:171]\n\t"
      "v_mfma_f32_16x16x32_bf16 v[140:143], a[204:207], v[92:95], v[140:143]\n\t"
      "v_mfma_f32_16x16x32_bf16 v[156:159], a[208:211], v[92:95], v[156:159]\n\t"
      "v_mfma_f32_16x16x32_bf16 v[172:175], a[212:215], v[92:95], v[172:175]\n\t"
      "Lpd74eef_63:\n\t"
      "s_waitcnt lgkmcnt(0)\n\t"
      "v_mfma_f32_16x16x32_bf16 v[128:131], a[216:219], v[96:99], v[128:131]\n\t"
      "s_add_u32 s40, %[dlo], 53248\n\t"
      "s_addc_u32 s41, %[dhi], 0\n\t"
      "v_mfma_f32_16x16x32_bf16 v[144:147], a[220:223], v[96:99], v[144:147]\n\t"
      "global_load_dwordx4 a[192:195], %[l16], s[40:41] offset:-4096 sc1\n\t"
      "v_mfma_f32_16x16x32_bf16 v[160:163], a[224:227], v[96:99], v[160:163]\n\t"
      "global_load_dwordx4 a[196:199], %[l16], s[40:41] offset:-3072 sc1\n\t"
      "v_mfma_f32_16x16x32_bf16 v[176:179], v[52:55], v[96:99], v[176:179]\n\t"
      "global_load_dwordx4 a[200:203], %[l16], s[40:41] offset:-2048 sc1\n\t"
      "v_mfma_f32_16x16x32_bf16 v[180:183], v[96:99], v[96:99], v[180:183]\n\t"
      "global_load_dwordx4 v[64:67], %[l16], s[38:39] offset:0 sc1\n\t"
      "v_mfma_f32_16x16x32_bf16 v[132:135], a[216:219], v[100:103], v[132:135]\n\t"
      "global_load_dwordx4 v[68:71], %[l16], s[38:39] offset:1024 sc1\n\t"
      "v_mfma_f32_16x16x32_bf16 v[148:151], a[220:223], v[100:103], v[148:151]\n\t"
      "s_add_u32 s38, %[hlo], 24576\n\t"
      "s_addc_u32 s39, %[hhi], 0\n\t"
      "v_mfma_f32_16x16x32_bf16 v[164:167], a[224:227], v[100:103], v[164:167]\n\t"
      "global_load_dwordx4 v[72:75], %[l16], s[38:39] offset:-4096 sc1\n\t"
      "v_mfma_f32_16x16x32_bf16 v[136:139], a[216:219], v[104:107], v[136:139]\n\t"
      "global_load_dwordx4 v[76:79], %[l16], s[38:39] offset:-3072 sc1\n\t"
      "v_mfma_f32_16x16x32_bf16 v[152:155], a[220:223], v[104:107], v[152:155]\n\t"
      "v_mfma_f32_16x16x32_bf16 v[168:171], a[224:227], v[104:107], v[168:171]\n\t"
      "v_mfma_f32_16x16x32_bf16 v[140:143], a[216:219], v[108:111], v[140:143]\n\t"
      "v_mfma_f32_16x16x32_bf16 v[156:159], a[220:223], v[108:111], v[156:159]\n\t"
      "v_mfma_f32_16x16x32_bf16 v[172:175], a[224:227], v[108:111], v[172:175]\n\t"
      "v_mfma_f32_16x16x32_bf16 v[128:131], a[228:231], v[112:115], v[128:131]\n\t"
      "v_mfma_f32_16x16x32_bf16 v[144:147], a[232:235], v[112:115], v[144:147]\n\t"
      "v_mfma_f32_16x16x32_bf16 v[160:163], a[236:239], v[112:115], v[160:163]\n\t"
      "v_mfma_f32_16x16x32_bf16 v[176:179], v[52:55], v[112:115], v[176:179]\n\t"
      "v_mfma_f32_16x16x32_bf16 v[180:183], v[112:115], v[112:115], v[180:183]\n\t"
      "v_mfma_f32_16x16x32_bf16 v[132:135], a[228:231], v[116:119], v[132:135]\n\t"
      "v_mfma_f32_16x16x32_bf16 v[148:151], a[232:235], v[116:119], v[148:151]\n\t"
      "v_mfma_f32_16x16x32_bf16 v[164:167], a[236:239], v[116:119], v[164:167]\n\t"
      "v_mfma_f32_16x16x32_bf16 v[136:139], a[228:231], v[120:123], v[136:139]\n\t"
      "v_mfma_f32_16x16x32_bf16 v[152:155], a[232:235], v[120:123], v[152:155]\n\t"
      "v_mfma_f32_16x16x32_bf16 v[168:171], a[236:239], v[120:123], v[168:171]\n\t"
      "v_mfma_f32_16x16x32_bf16 v[140:143], a[228:231], v[124:127], v[140:143]\n\t"
      "v_mfma_f32_16x16x32_bf16 v[156:159], a[232:235], v[124:127], v[156:159]\n\t"
      "v_mfma_f32_16x16x32_bf16 v[172:175], a[236:239], v[124:127], v[172:175]\n\t"
      "s_mov_b32 m0, s45\n\t"
      "s_nop 7\n\t"
      "s_branch L_g1_end%=\n\t"
      "L_g1_slow6_%=:\n\t"
      "s_add_u32 m0, %[m0b], 98304\n\t"
      "s_add_u32 s42, %[xlo], 98304\n\t"
      "s_addc_u32 s43, %[xhi], 0\n\t"
      "global_load_lds_dwordx4 %[voff], s[42:43] offset:0 sc1\n\t"
      "global_load_lds_dwordx4 %[voff], s[42:43] offset:1024 sc1\n\t"
      "global_load_lds_dwordx4 %[voff], s[42:43] offset:2048 sc1\n\t"
      "global_load_lds_dwordx4 %[voff], s[42:43] offset:3072 sc1\n\t"
      "s_waitcnt vmcnt(0)\n\t"
      "ds_read_b128 v[240:243], v57 offset:32768\n\t"
      "ds_read_b128 v[244:247], v57 offset:33792\n\t"
      "ds_read_b128 v[248:251], v57 offset:34816\n\t"
      "ds_read_b128 v[252:255], v57 offset:35840\n\t"
      "s_waitcnt lgkmcnt(0)\n\t"
      "v_max3_u32 v56, v240, v241, v242\n\t"
      "v_max3_u32 v56, v56, v243, v244\n\t"
      "v_max3_u32 v56, v56, v245, v246\n\t"
      "v_max3_u32 v56, v56, v247, v248\n\t"
      "v_max3_u32 v56, v56, v249, v250\n\t"
      "v_max3_u32 v56, v56, v251, v252\n\t"
      "v_max3_u32 v56, v56, v253, v254\n\t"
      "v_max_u32 v56, v56, v255\n\t"
      "v_cmp_eq_u32 vcc, -1, v56\n\t"
      "s_sub_u32 s44, s44, 1\n\t"
      "s_cbranch_vccz L_g1_back6_%=\n\t"
      "s_cmp_lg_u32 s44, 0\n\t"
      "s_cbranch_scc1 L_g1_slow6_%=\n\t"
      "s_branch L_g1_back6_%=\n\t"
      "L_g1_slow7_%=:\n\t"
      "s_add_u32 m0, %[m0b], 114688\n\t"
      "s_add_u32 s42, %[xlo], 114688\n\t"
      "s_addc_u32 s43, %[xhi], 0\n\t"
      "global_load_lds_dwordx4 %[voff], s[42:43] offset:0 sc1\n\t"
      "global_load_lds_dwordx4 %[voff], s[42:43] offset:1024 sc1\n\t"
      "global_load_lds_dwordx4 %[voff], s[42:43] offset:2048 sc1\n\t"
      "global_load_lds_dwordx4 %[voff], s[42:43] offset:3072 sc1\n\t"
      "s_waitcnt vmcnt(0)\n\t"
      "ds_read_b128 v[240:243], v57 offset:49152\n\t"
      "ds_read_b128 v[244:247], v57 offset:50176\n\t"
      "ds_read_b128 v[248:251], v57 offset:51200\n\t"
      "ds_read_b128 v[252:255], v57 offset:52224\n\t"
      "s_waitcnt lgkmcnt(0)\n\t"
      "v_max3_u32 v56, v240, v241, v242\n\t"
      "v_max3_u32 v56, v56, v243, v244\n\t"
      "v_max3_u32 v56, v56, v245, v246\n\t"
      "v_max3_u32 v56, v56, v247, v248\n\t"
      "v_max3_u32 v56, v56, v249, v250\n\t"
      "v_max3_u32 v56, v56, v251, v252\n\t"
      "v_max3_u32 v56, v56, v253, v254\n\t"
      "v_max_u32 v56, v56, v255\n\t"
      "v_cmp_eq_u32 vcc, -1, v56\n\t"
      "s_sub_u32 s44, s44, 1\n\t"
      "s_cbranch_vccz L_g1_back7_%=\n\t"
      "s_cmp_lg_u32 s44, 0\n\t"
      "s_cbranch_scc1 L_g1_slow7_%=\n\t"
      "s_branch L_g1_back7_%=\n\t"
      "L_g1_slow8_%=:\n\t"
      "s_add_u32 m0, %[m0b], 0\n\t"
      "s_add_u32 s42, %[xlo], 131072\n\t"
      "s_addc_u32 s43, %[xhi], 0\n\t"
      "global_load_lds_dwordx4 %[voff], s[42:43] offset:0 sc1\n\t"
      "global_load_lds_dwordx4 %[voff], s[42:43] offset:1024 sc1\n\t"
      "global_load_lds_dwordx4 %[voff], s[42:43] offset:2048 sc1\n\t"
      "global_load_lds_dwordx4 %[voff], s[42:43] offset:3072 sc1\n\t"
      "s_waitcnt vmcnt(0)\n\t"
      "ds_read_b128 v[240:243], %[vchk] offset:0\n\t"
      "ds_read_b128 v[244:247], %[vchk] offset:1024\n\t"
      "ds_read_b128 v[248:251], %[vchk] offset:2048\n\t"
      "ds_read_b128 v[252:255], %[vchk] offset:3072\n\t"
      "s_waitcnt lgkmcnt(0)\n\t"
      "v_max3_u32 v56, v240, v241, v242\n\t"
      "v_max3_u32 v56, v56, v243, v244\n\t"
      "v_max3_u32 v56, v56, v245, v246\n\t"
      "v_max3_u32 v56, v56, v247, v248\n\t"
      "v_max3_u32 v56, v56, v249, v250\n\t"
      "v_max3_u32 v56, v56, v251, v252\n\t"
      "v_max3_u32 v56, v56, v253, v254\n\t"
      "v_max_u32 v56, v56, v255\n\t"
      "v_cmp_eq_u32 vcc, -1, v56\n\t"
      "s_sub_u32 s44, s44, 1\n\t"
      "s_cbranch_vccz L_g1_back8_%=\n\t"
      "s_cmp_lg_u32 s44, 0\n\t"
      "s_cbranch_scc1 L_g1_slow8_%=\n\t"
      "s_branch L_g1_back8_%=\n\t"
      "L_g1_slow9_%=:\n\t"
      "s_add_u32 m0, %[m0b], 16384\n\t"
      "s_add_u32 s42, %[xlo], 147456\n\t"
      "s_addc_u32 s43, %[xhi], 0\n\t"
      "global_load_lds_dwordx4 %[voff], s[42:43] offset:0 sc1\n\t"
      "global_load_lds_dwordx4 %[voff], s[42:43] offset:1024 sc1\n\t"
      "global_load_lds_dwordx4 %[voff], s[42:43] offset:2048 sc1\n\t"
      "global_load_lds_dwordx4 %[voff], s[42:43] offset:3072 sc1\n\t"
      "s_waitcnt vmcnt(0)\n\t"
      "ds_read_b128 v[240:243], %[vchk] offset:16384\n\t"
      "ds_read_b128 v[244:247], %[vchk] offset:17408\n\t"
      "ds_read_b128 v[248:251], %[vchk] offset:18432\n\t"
      "ds_read_b128 v[252:255], %[vchk] offset:19456\n\t"
      "s_waitcnt lgkmcnt(0)\n\t"
      "v_max3_u32 v56, v240, v241, v242\n\t"
      "v_max3_u32 v56, v56, v243, v244\n\t"
      "v_max3_u32 v56, v56, v245, v246\n\t"
      "v_max3_u32 v56, v56, v247, v248\n\t"
      "v_max3_u32 v56, v56, v249, v250\n\t"
      "v_max3_u32 v56, v56, v251, v252\n\t"
      "v_max3_u32 v56, v56, v253, v254\n\t"
      "v_max_u32 v56, v56, v255\n\t"
      "v_cmp_eq_u32 vcc, -1, v56\n\t"
      "s_sub_u32 s44, s44, 1\n\t"
      "s_cbranch_vccz L_g1_back9_%=\n\t"
      "s_cmp_lg_u32 s44, 0\n\t"
      "s_cbranch_scc1 L_g1_slow9_%=\n\t"
      "s_branch L_g1_back9_%=\n\t"
      "\n\t"
      "L_g1_end%=:\n\t"
      "Lpd74eef_64:\n\t"
      : "=&{v[128:159]}"(accA), "=&{v[160:175]}"(accB), "=&{v[176:183]}"(accS), "=&{v[64:95]}"(h0), "=&{v[96:127]}"(h1), "=&{v[208:239]}"(h2), "=&{v[184:187]}"(hc)
      : G1_IN : G1_CL,  "v48", "v49", "v50", "v51", "v52", "v53", "v54", "v55", "v56", "v57", "v58", "v59", "v60", "v61", "v62", "v63", "v240", "v241", "v242", "v243", "v244", "v245", "v246", "v247", "v248", "v249", "v250", "v251", "v252", "v253", "v254", "v255", "a0", "a1", "a2", "a3", "a4", "a5", "a6", "a7", "a8", "a9", "a10", "a11", "a12", "a13", "a14", "a15", "a16", "a17", "a18", "a19", "a20", "a21", "a22", "a23", "a24", "a25", "a26", "a27", "a28", "a29", "a30", "a31", "a32", "a33", "a34", "a35", "a36", "a37", "a38", "a39", "a40", "a41", "a42", "a43", "a44", "a45", "a46", "a47", "a48", "a49", "a50", "a51", "a52", "a53", "a54", "a55", "a56", "a57", "a58", "a59", "a60", "a61", "a62", "a63", "a64", "a65", "a66", "a67", "a68", "a69", "a70", "a71", "a72", "a73", "a74", "a75", "a76", "a77", "a78", "a79", "a80", "a81", "a82", "a83", "a84", "a85", "a86", "a87", "a88", "a89", "a90", "a91", "a92", "a93", "a94", "a95", "a96", "a97", "a98", "a99", "a100", "a101", "a102", "a103", "a104", "a105", "a106", "a107", "a108", "a109", "a110", "a111", "a112", "a113", "a114", "a115", "a116", "a117", "a118", "a119", "a120", "a121", "a122", "a123", "a124", "a125", "a126", "a127", "a128", "a129", "a130", "a131", "a132", "a133", "a134", "a135", "a136", "a137", "a138", "a139", "a140", "a141", "a142", "a143", "a144", "a145", "a146", "a147", "a148", "a149", "a150", "a151", "a152", "a153", "a154", "a155", "a156", "a157", "a158", "a159", "a160", "a161", "a162", "a163", "a164", "a165", "a166", "a167", "a168", "a169", "a170", "a171", "a172", "a173", "a174", "a175", "a176", "a177", "a178", "a179", "a180", "a181", "a182", "a183", "a184", "a185", "a186", "a187", "a188", "a189", "a190", "a191", "a192", "a193", "a194", "a195", "a196", "a197", "a198", "a199", "a200", "a201", "a202", "a203", "a204", "a205", "a206", "a207", "a208", "a209", "a210", "a211", "a212", "a213", "a214", "a215", "a216", "a217", "a218", "a219", "a220", "a221", "a222", "a223", "a224", "a225", "a226", "a227", "a228", "a229", "a230", "a231", "a232", "a233", "a234", "a235", "a236", "a237", "a238", "a239");

  asm volatile("Lpd74eef_65:");
  g1_epi(1);
  asm volatile("Lpd74eef_66:");
  u32x4 xg;
#pragma unroll
  for (int i = 0; i < 3; ++i) {
    const int e = lane + 64 * i < 160 ? lane + 64 * i : 159;
    const int k = kpos(bn2 * 80 + (e >> 1));
    xg[i] = WS_X1T + (unsigned)(((bm2 & ~1) + (w >> 1)) * D_ + k) * 128 + (((2 * bm2 + (w & 1)) ^ (k >> 1)) & 3) * 32 + (e & 1) * 16 - i * 1024;
  }
  xg[3] = 0;
  asm volatile(
      "s_mov_b32 s44, 0x40000\n\t"
      "s_mov_b32 s45, m0\n\t"
      "s_waitcnt vmcnt(4)\n\t"
      "v_add3_u32 v46, v208, v209, %[t0]\n\t"
      "v_add3_u32 v46, v46, v210, v211\n\t"
      "v_add3_u32 v46, v46, v212, v213\n\t"
      "v_add3_u32 v46, v46, v214, v215\n\t"
      "v_add3_u32 v46, v46, v216, v217\n\t"
      "v_add3_u32 v46, v46, v218, v219\n\t"
      "v_add3_u32 v46, v46, v220, v221\n\t"
      "v_add3_u32 v46, v46, v222, v223\n\t"
      "v_add3_u32 v46, v46, v224, v225\n\t"
      "v_add3_u32 v46, v46, v226, v227\n\t"
      "v_add3_u32 v46, v46, v228, v229\n\t"
      "v_add3_u32 v46, v46, v230, v231\n\t"
      "v_cmp_ne_u32 vcc, v46, v184\n\t"
      "s_cbranch_vccnz L_g2_slow0_%=\n\t"
      "L_g2_back0_%=:\n\t"
      "Lpd74eef_67:\n\t"
      "v_mfma_f32_16x16x32_bf16 v[128:131], a[0:3], v[208:211], 0\n\t"
      "s_add_u32 s40, %[dlo], 53248\n\t"
      "s_addc_u32 s41, %[dhi], 0\n\t"
      "global_load_dwordx4 a[204:207], %[l16], s[40:41] offset:-1024 sc1\n\t"
      "v_mfma_f32_16x16x32_bf16 v[136:139], a[4:7], v[208:211], 0\n\t"
      "v_mfma_f32_16x16x32_bf16 v[144:147], a[8:11], v[208:211], 0\n\t"
      "v_mfma_f32_16x16x32_bf16 v[152:155], a[12:15], v[208:211], 0\n\t"
      "v_mfma_f32_16x16x32_bf16 v[160:163], a[16:19], v[208:211], 0\n\t"
      "global_load_dwordx4 a[208:211], %[l16], s[40:41] offset:0 sc1\n\t"
      "v_mfma_f32_16x16x32_bf16 v[132:135], a[0:3], v[212:215], 0\n\t"
      "v_mfma_f32_16x16x32_bf16 v[140:143], a[4:7], v[212:215], 0\n\t"
      "v_mfma_f32_16x16x32_bf16 v[148:151], a[8:11], v[212:215], 0\n\t"
      "v_mfma_f32_16x16x32_bf16 v[156:159], a[12:15], v[212:215], 0\n\t"
      "global_load_dwordx4 a[212:215], %[l16], s[40:41] offset:1024 sc1\n\t"
      "v_mfma_f32_16x16x32_bf16 v[164:167], a[16:19], v[212:215], 0\n\t"
      "v_mfma_f32_16x16x32_bf16 v[128:131], a[20:23], v[216:219], v[128:131]\n\t"
      "v_mfma_f32_16x16x32_bf16 v[136:139], a[24:27], v[216:219], v[136:139]\n\t"
      "v_mfma_f32_16x16x32_bf16 v[144:147], a[28:31], v[216:219], v[144:147]\n\t"
      "global_load_dwordx4 a[216:219], %[l16], s[40:41] offset:2048 sc1\n\t"
      "v_mfma_f32_16x16x32_bf16 v[152:155], a[32:35], v[216:219], v[152:155]\n\t"
      "v_mfma_f32_16x16x32_bf16 v[160:163], a[36:39], v[216:219], v[160:163]\n\t"
      "v_mfma_f32_16x16x32_bf16 v[132:135], a[20:23], v[220:223], v[132:135]\n\t"
      "v_mfma_f32_16x16x32_bf16 v[140:143], a[24:27], v[220:223], v[140:143]\n\t"
      "global_load_dwordx4 a[220:223], %[l16], s[40:41] offset:3072 sc1\n\t"
      "v_add3_u32 v46, v232, v233, %[t0]\n\t"
      "v_mfma_f32_16x16x32_bf16 v[148:151], a[28:31], v[220:223], v[148:151]\n\t"
      "v_add3_u32 v46, v46, v234, v235\n\t"
      "v_mfma_f32_16x16x32_bf16 v[156:159], a[32:35], v[220:223], v[156:159]\n\t"
      "v_add3_u32 v46, v46, v236, v237\n\t"
      "v_mfma_f32_16x16x32_bf16 v[164:167], a[36:39], v[220:223], v[164:167]\n\t"
      "v_add3_u32 v46, v46, v238, v239\n\t"
      "v_mfma_f32_16x16x32_bf16 v[128:131], a[40:43], v[224:227], v[128:131]\n\t"
      "s_add_u32 s40, %[dlo], 61440\n\t"
      "s_addc_u32 s41, %[dhi], 0\n\t"
      "global_load_dwordx4 a[224:227], %[l16], s[40:41] offset:-4096 sc1\n\t"
      "v_add3_u32 v46, v46, v64, v65\n\t"
      "v_mfma_f32_16x16x32_bf16 v[136:139], a[44:47], v[224:227], v[136:139]\n\t"
      "v_add3_u32 v46, v46, v66, v67\n\t"
      "v_mfma_f32_16x16x32_bf16 v[144:147], a[48:51], v[224:227], v[144:147]\n\t"
      "v_add3_u32 v46, v46, v68, v69\n\t"
      "v_mfma_f32_16x16x32_bf16 v[152:155], a[52:55], v[224:227], v[152:155]\n\t"
      "v_add3_u32 v46, v46, v70, v71\n\t"
      "v_mfma_f32_16x16x32_bf16 v[160:163], a[56:59], v[224:227], v[160:163]\n\t"
      "global_load_dwordx4 a[228:231], %[l16], s[40:41] offset:-3072 sc1\n\t"
      "v_add3_u32 v46, v46, v72, v73\n\t"
      "v_mfma_f32_16x16x32_bf16 v[132:135], a[40:43], v[228:231], v[132:135]\n\t"
      "v_add3_u32 v46, v46, v74, v75\n\t"
      "v_mfma_f32_16x16x32_bf16 v[140:143], a[44:47], v[228:231], v[140:143]\n\t"
      "v_add3_u32 v46, v46, v76, v77\n\t"
      "v_mfma_f32_16x16x32_bf16 v[148:151], a[48:51], v[228:231], v[148:151]\n\t"
      "v_add3_u32 v46, v46, v78, v79\n\t"
      "v_mfma_f32_16x16x32_bf16 v[156:159], a[52:55], v[228:231], v[156:159]\n\t"
      "global_load_dwordx4 a[232:235], %[l16], s[40:41] offset:-2048 sc1\n\t"
      "v_cmp_ne_u32 vcc, v46, v185\n\t"
      "v_mfma_f32_16x16x32_bf16 v[164:167], a[56:59], v[228:231], v[164:167]\n\t"
      "s_cbranch_vccnz L_g2_slow1_%=\n\t"
      "L_g2_back1_%=:\n\t"
      "Lpd74eef_68:\n\t"
      "v_mfma_f32_16x16x32_bf16 v[128:131], a[60:63], v[232:235], v[128:131]\n\t"
      "global_load_dwordx4 a[236:239], %[l16], s[40:41] offset:-1024 sc1\n\t"
      "v_mfma_f32_16x16x32_bf16 v[136:139], a[64:67], v[232:235], v[136:139]\n\t"
      "v_mfma_f32_16x16x32_bf16 v[144:147], a[68:71], v[232:235], v[144:147]\n\t"
      "v_mfma_f32_16x16x32_bf16 v[152:155], a[72:75], v[232:235], v[152:155]\n\t"
      "v_mfma_f32_16x16x32_bf16 v[160:163], a[76:79], v[232:235], v[160:163]\n\t"
      "v_mfma_f32_16x16x32_bf16 v[132:135], a[60:63], v[236:239], v[132:135]\n\t"
      "v_mfma_f32_16x16x32_bf16 v[140:143], a[64:67], v[236:239], v[140:143]\n\t"
      "v_mfma_f32_16x16x32_bf16 v[148:151], a[68:71], v[236:239], v[148:151]\n\t"
      "v_mfma_f32_16x16x32_bf16 v[156:159], a[72:75], v[236:239], v[156:159]\n\t"
      "v_mfma_f32_16x16x32_bf16 v[164:167], a[76:79], v[236:239], v[164:167]\n\t"
      "v_mfma_f32_16x16x32_bf16 v[128:131], a[80:83], v[64:67], v[128:131]\n\t"
      "v_mfma_f32_16x16x32_bf16 v[136:139], a[84:87], v[64:67], v[136:139]\n\t"
      "v_mfma_f32_16x16x32_bf16 v[144:147], a[88:91], v[64:67], v[144:147]\n\t"
      "v_mfma_f32_16x16x32_bf16 v[152:155], a[92:95], v[64:67], v[152:155]\n\t"
      "v_mfma_f32_16x16x32_bf16 v[160:163], a[96:99], v[64:67], v[160:163]\n\t"
      "v_mfma_f32_16x16x32_bf16 v[132:135], a[80:83], v[68:71], v[132:135]\n\t"
      "v_mfma_f32_16x16x32_bf16 v[140:143], a[84:87], v[68:71], v[140:143]\n\t"
      "v_add3_u32 v46, v80, v81, %[t0]\n\t"
      "v_mfma_f32_16x16x32_bf16 v[148:151], a[88:91], v[68:71], v[148:151]\n\t"
      "v_add3_u32 v46, v46, v82, v83\n\t"
      "v_mfma_f32_16x16x32_bf16 v[156:159], a[92:95], v[68:71], v[156:159]\n\t"
      "v_add3_u32 v46, v46, v84, v85\n\t"
      "v_mfma_f32_16x16x32_bf16 v[164:167], a[96:99], v[68:71], v[164:167]\n\t"
      "v_add3_u32 v46, v46, v86, v87\n\t"
      "v_mfma_f32_16x16x32_bf16 v[128:131], a[100:103], v[72:75], v[128:131]\n\t"
      "v_add3_u32 v46, v46, v88, v89\n\t"
      "v_mfma_f32_16x16x32_bf16 v[136:139], a[104:107], v[72:75], v[136:139]\n\t"
      "v_add3_u32 v46, v46, v90, v91\n\t"
      "v_mfma_f32_16x16x32_bf16 v[144:147], a[108:111], v[72:75], v[144:147]\n\t"
      "v_add3_u32 v46, v46, v92, v93\n\t"
      "v_mfma_f32_16x16x32_bf16 v[152:155], a[112:115], v[72:75], v[152:155]\n\t"
      "v_add3_u32 v46, v46, v94, v95\n\t"
      "v_mfma_f32_16x16x32_bf16 v[160:163], a[116:119], v[72:75], v[160:163]\n\t"
      "v_add3_u32 v46, v46, v96, v97\n\t"
      "v_mfma_f32_16x16x32_bf16 v[132:135], a[100:103], v[76:79], v[132:135]\n\t"
      "v_add3_u32 v46, v46, v98, v99\n\t"
      "v_mfma_f32_16x16x32_bf16 v[140:143], a[104:107], v[76:79], v[140:143]\n\t"
      "v_add3_u32 v46, v46, v100, v101\n\t"
      "v_mfma_f32_16x16x32_bf16 v[148:151], a[108:111], v[76:79], v[148:151]\n\t"
      "v_add3_u32 v46, v46, v102, v103\n\t"
      "v_mfma_f32_16x16x32_bf16 v[156:159], a[112:115], v[76:79], v[156:159]\n\t"
      "v_cmp_ne_u32 vcc, v46, v186\n\t"
      "v_mfma_f32_16x16x32_bf16 v[164:167], a[116:119], v[76:79], v[164:167]\n\t"
      "s_cbranch_vccnz L_g2_slow2_%=\n\t"
      "L_g2_back2_%=:\n\t"
      "Lpd74eef_69:\n\t"
      "v_mfma_f32_16x16x32_bf16 v[128:131], a[120:123], v[80:83], v[128:131]\n\t"
      "s_add_u32 s38, %[hlo], 6144\n\t"
      "s_addc_u32 s39, %[hhi], 0\n\t"
      "global_load_dwordx4 v[48:51], %[l16], s[38:39] offset:-4096 sc1\n\t"
      "v_mfma_f32_16x16x32_bf16 v[136:139], a[124:127], v[80:83], v[136:139]\n\t"
      "v_mfma_f32_16x16x32_bf16 v[144:147], a[128:131], v[80:83], v[144:147]\n\t"
      "v_mfma_f32_16x16x32_bf16 v[152:155], a[132:135], v[80:83], v[152:155]\n\t"
      "v_mfma_f32_16x16x32_bf16 v[160:163], a[136:139], v[80:83], v[160:163]\n\t"
      "global_load_dwordx4 v[52:55], %[l16], s[38:39] offset:-3072 sc1\n\t"
      "v_mfma_f32_16x16x32_bf16 v[132:135], a[120:123], v[84:87], v[132:135]\n\t"
      "v_mfma_f32_16x16x32_bf16 v[140:143], a[124:127], v[84:87], v[140:143]\n\t"
      "v_mfma_f32_16x16x32_bf16 v[148:151], a[128:131], v[84:87], v[148:151]\n\t"
      "v_mfma_f32_16x16x32_bf16 v[156:159], a[132:135], v[84:87], v[156:159]\n\t"
      "global_load_dwordx4 v[56:59], %[l16], s[38:39] offset:0 sc1\n\t"
      "v_mfma_f32_16x16x32_bf16 v[164:167], a[136:139], v[84:87], v[164:167]\n\t"
      "v_mfma_f32_16x16x32_bf16 v[128:131], a[140:143], v[88:91], v[128:131]\n\t"
      "v_mfma_f32_16x16x32_bf16 v[136:139], a[144:147], v[88:91], v[136:139]\n\t"
      "v_mfma_f32_16x16x32_bf16 v[144:147], a[148:151], v[88:91], v[144:147]\n\t"
      "global_load_dwordx4 v[60:63], %[l16], s[38:39] offset:1024 sc1\n\t"
      "v_mfma_f32_16x16x32_bf16 v[152:155], a[152:155], v[88:91], v[152:155]\n\t"
      "v_mfma_f32_16x16x32_bf16 v[160:163], a[156:159], v[88:91], v[160:163]\n\t"
      "v_mfma_f32_16x16x32_bf16 v[132:135], a[140:143], v[92:95], v[132:135]\n\t"
      "s_waitcnt vmcnt(4)\n\t"
      "v_mfma_f32_16x16x32_bf16 v[140:143], a[144:147], v[92:95], v[140:143]\n\t"
      "s_add_u32 s38, %[hlo], 14336\n\t"
      "s_addc_u32 s39, %[hhi], 0\n\t"
      "global_load_dwordx4 v[240:243], %[l16], s[38:39] offset:-4096 sc1\n\t"
      "v_add3_u32 v46, v104, v105, %[t0]\n\t"
      "v_mfma_f32_16x16x32_bf16 v[148:151], a[148:151], v[92:95], v[148:151]\n\t"
      "v_add3_u32 v46, v46, v106, v107\n\t"
      "v_mfma_f32_16x16x32_bf16 v[156:159], a[152:155], v[92:95], v[156:159]\n\t"
      "v_add3_u32 v46, v46, v108, v109\n\t"
      "v_mfma_f32_16x16x32_bf16 v[164:167], a[156:159], v[92:95], v[164:167]\n\t"
      "v_add3_u32 v46, v46, v110, v111\n\t"
      "v_mfma_f32_16x16x32_bf16 v[128:131], a[160:163], v[96:99], v[128:131]\n\t"
      "global_load_dwordx4 v[244:247], %[l16], s[38:39] offset:-3072 sc1\n\t"
      "v_add3_u32 v46, v46, v112, v113\n\t"
      "v_mfma_f32_16x16x32_bf16 v[136:139], a[164:167], v[96:99], v[136:139]\n\t"
      "v_add3_u32 v46, v46, v114, v115\n\t"
      "v_mfma_f32_16x16x32_bf16 v[144:147], a[168:171], v[96:99], v[144:147]\n\t"
      "v_add3_u32 v46, v46, v116, v117\n\t"
      "v_mfma_f32_16x16x32_bf16 v[152:155], a[172:175], v[96:99], v[152:155]\n\t"
      "v_add3_u32 v46, v46, v118, v119\n\t"
      "v_mfma_f32_16x16x32_bf16 v[160:163], a[176:179], v[96:99], v[160:163]\n\t"
      "global_load_dword v32, %[cko], %[wsb] offset:256 sc1\n\t"
      "v_add3_u32 v46, v46, v120, v121\n\t"
      "v_mfma_f32_16x16x32_bf16 v[132:135], a[160:163], v[100:103], v[132:135]\n\t"
      "v_add3_u32 v46, v46, v122, v123\n\t"
      "v_mfma_f32_16x16x32_bf16 v[140:143], a[164:167], v[100:103], v[140:143]\n\t"
      "v_add3_u32 v46, v46, v124, v125\n\t"
      "v_mfma_f32_16x16x32_bf16 v[148:151], a[168:171], v[100:103], v[148:151]\n\t"
      "v_add3_u32 v46, v46, v126, v127\n\t"
      "v_mfma_f32_16x16x32_bf16 v[156:159], a[172:175], v[100:103], v[156:159]\n\t"
      "s_add_u32 s38, %[hlo], 18432\n\t"
      "s_addc_u32 s39, %[hhi], 0\n\t"
      "global_load_dwordx4 v[248:251], %[l16], s[38:39] offset:-4096 sc1\n\t"
      "v_cmp_ne_u32 vcc, v46, v187\n\t"
      "v_mfma_f32_16x16x32_bf16 v[164:167], a[176:179], v[100:103], v[164:167]\n\t"
      "s_cbranch_vccnz L_g2_slow3_%=\n\t"
      "L_g2_back3_%=:\n\t"
      "Lpd74eef_70:\n\t"
      "v_mfma_f32_16x16x32_bf16 v[128:131], a[180:183], v[104:107], v[128:131]\n\t"
      "global_load_dwordx4 v[252:255], %[l16], s[38:39] offset:-3072 sc1\n\t"
      "v_mfma_f32_16x16x32_bf16 v[136:139], a[184:187], v[104:107], v[136:139]\n\t"
      "v_mfma_f32_16x16x32_bf16 v[144:147], a[188:191], v[104:107], v[144:147]\n\t"
      "v_mfma_f32_16x16x32_bf16 v[152:155], a[192:195], v[104:107], v[152:155]\n\t"
      "v_mfma_f32_16x16x32_bf16 v[160:163], a[196:199], v[104:107], v[160:163]\n\t"
      "global_load_dwordx4 v[208:211], %[l16], s[38:39] offset:0 sc1\n\t"
      "v_mfma_f32_16x16x32_bf16 v[132:135], a[180:183], v[108:111], v[132:135]\n\t"
      "v_mfma_f32_16x16x32_bf16 v[140:143], a[184:187], v[108:111], v[140:143]\n\t"
      "v_mfma_f32_16x16x32_bf16 v[148:151], a[188:191], v[108:111], v[148:151]\n\t"
      "v_mfma_f32_16x16x32_bf16 v[156:159], a[192:195], v[108:111], v[156:159]\n\t"
      "global_load_dwordx4 v[212:215], %[l16], s[38:39] offset:1024 sc1\n\t"
      "v_mfma_f32_16x16x32_bf16 v[164:167], a[196:199], v[108:111], v[164:167]\n\t"
      "v_mfma_f32_16x16x32_bf16 v[128:131], a[200:203], v[112:115], v[128:131]\n\t"
      "v_mfma_f32_16x16x32_bf16 v[136:139], a[204:207], v[112:115], v[136:139]\n\t"
      "v_mfma_f32_16x16x32_bf16 v[144:147], a[208:211], v[112:115], v[144:147]\n\t"
      "s_add_u32 s38, %[hlo], 26624\n\t"
      "s_addc_u32 s39, %[hhi], 0\n\t"
      "global_load_dwordx4 v[216:219], %[l16], s[38:39] offset:-4096 sc1\n\t"
      "v_mfma_f32_16x16x32_bf16 v[152:155], a[212:215], v[112:115], v[152:155]\n\t"
      "v_mfma_f32_16x16x32_bf16 v[160:163], a[216:219], v[112:115], v[160:163]\n\t"
      "v_mfma_f32_16x16x32_bf16 v[132:135], a[200:203], v[116:119], v[132:135]\n\t"
      "s_waitcnt vmcnt(5)\n\t"
      "v_mfma_f32_16x16x32_bf16 v[140:143], a[204:207], v[116:119], v[140:143]\n\t"
      "global_load_dwordx4 v[220:223], %[l16], s[38:39] offset:-3072 sc1\n\t"
      "v_add3_u32 v46, v48, v49, %[t0]\n\t"
      "v_mfma_f32_16x16x32_bf16 v[148:151], a[208:211], v[116:119], v[148:151]\n\t"
      "v_add3_u32 v46, v46, v50, v51\n\t"
      "v_mfma_f32_16x16x32_bf16 v[156:159], a[212:215], v[116:119], v[156:159]\n\t"
      "v_add3_u32 v46, v46, v52, v53\n\t"
      "v_mfma_f32_16x16x32_bf16 v[164:167], a[216:219], v[116:119], v[164:167]\n\t"
      "v_add3_u32 v46, v46, v54, v55\n\t"
      "v_mfma_f32_16x16x32_bf16 v[128:131], a[220:223], v[120:123], v[128:131]\n\t"
      "global_load_dword v33, %[cko], %[wsb] offset:768 sc1\n\t"
      "v_add3_u32 v46, v46, v56, v57\n\t"
      "v_mfma_f32_16x16x32_bf16 v[136:139], a[224:227], v[120:123], v[136:139]\n\t"
      "v_add3_u32 v46, v46, v58, v59\n\t"
      "v_mfma_f32_16x16x32_bf16 v[144:147], a[228:231], v[120:123], v[144:147]\n\t"
      "v_add3_u32 v46, v46, v60, v61\n\t"
      "v_mfma_f32_16x16x32_bf16 v[152:155], a[232:235], v[120:123], v[152:155]\n\t"
      "v_add3_u32 v46, v46, v62, v63\n\t"
      "v_mfma_f32_16x16x32_bf16 v[160:163], a[236:239], v[120:123], v[160:163]\n\t"
      "s_add_u32 s38, %[hlo], 30720\n\t"
      "s_addc_u32 s39, %[hhi], 0\n\t"
      "global_load_dwordx4 v[224:227], %[l16], s[38:39] offset:-4096 sc1\n\t"
      "v_add3_u32 v46, v46, v240, v241\n\t"
      "v_mfma_f32_16x16x32_bf16 v[132:135], a[220:223], v[124:127], v[132:135]\n\t"
      "v_add3_u32 v46, v46, v242, v243\n\t"
      "v_mfma_f32_16x16x32_bf16 v[140:143], a[224:227], v[124:127], v[140:143]\n\t"
      "v_add3_u32 v46, v46, v244, v245\n\t"
      "v_mfma_f32_16x16x32_bf16 v[148:151], a[228:231], v[124:127], v[148:151]\n\t"
      "v_add3_u32 v46, v46, v246, v247\n\t"
      "v_mfma_f32_16x16x32_bf16 v[156:159], a[232:235], v[124:127], v[156:159]\n\t"
      "global_load_dwordx4 v[228:231], %[l16], s[38:39] offset:-3072 sc1\n\t"
      "v_cmp_ne_u32 vcc, v46, v32\n\t"
      "v_mfma_f32_16x16x32_bf16 v[164:167], a[236:239], v[124:127], v[164:167]\n\t"
      "s_cbranch_vccnz L_g2_slow4_%=\n\t"
      "L_g2_back4_%=:\n\t"
      "Lpd74eef_71:\n\t"
      "v_mfma_f32_16x16x32_bf16 v[168:171], a[0:3], v[48:51], 0\n\t"
      "global_load_dwordx4 v[232:235], %[l16], s[38:39] offset:0 sc1\n\t"
      "v_mfma_f32_16x16x32_bf16 v[176:179], a[4:7], v[48:51], 0\n\t"
      "v_mfma_f32_16x16x32_bf16 v[184:187], a[8:11], v[48:51], 0\n\t"
      "v_mfma_f32_16x16x32_bf16 v[192:195], a[12:15], v[48:51], 0\n\t"
      "v_mfma_f32_16x16x32_bf16 v[200:203], a[16:19], v[48:51], 0\n\t"
      "global_load_dwordx4 v[236:239], %[l16], s[38:39] offset:1024 sc1\n\t"
      "v_mfma_f32_16x16x32_bf16 v[172:175], a[0:3], v[52:55], 0\n\t"
      "ds_write_b128 %[exw], v[128:131] offset:0\n\t"
      "v_mfma_f32_16x16x32_bf16 v[180:183], a[4:7], v[52:55], 0\n\t"
      "v_mfma_f32_16x16x32_bf16 v[188:191], a[8:11], v[52:55], 0\n\t"
      "v_mfma_f32_16x16x32_bf16 v[196:199], a[12:15], v[52:55], 0\n\t"
      "s_add_u32 s38, %[hlo], 38912\n\t"
      "s_addc_u32 s39, %[hhi], 0\n\t"
      "global_load_dwordx4 v[64:67], %[l16], s[38:39] offset:-4096 sc1\n\t"
      "ds_write_b128 %[exw], v[132:135] offset:20480\n\t"
      "v_mfma_f32_16x16x32_bf16 v[204:207], a[16:19], v[52:55], 0\n\t"
      "v_mfma_f32_16x16x32_bf16 v[168:171], a[20:23], v[56:59], v[168:171]\n\t"
      "v_mfma_f32_16x16x32_bf16 v[176:179], a[24:27], v[56:59], v[176:179]\n\t"
      "ds_write_b128 %[exw], v[136:139] offset:1024\n\t"
      "v_mfma_f32_16x16x32_bf16 v[184:187], a[28:31], v[56:59], v[184:187]\n\t"
      "global_load_dwordx4 v[68:71], %[l16], s[38:39] offset:-3072 sc1\n\t"
      "v_mfma_f32_16x16x32_bf16 v[192:195], a[32:35], v[56:59], v[192:195]\n\t"
      "v_mfma_f32_16x16x32_bf16 v[200:203], a[36:39], v[56:59], v[200:203]\n\t"
      "ds_write_b128 %[exw], v[140:143] offset:21504\n\t"
      "v_mfma_f32_16x16x32_bf16 v[172:175], a[20:23], v[60:63], v[172:175]\n\t"
      "s_waitcnt vmcnt(6)\n\t"
      "v_mfma_f32_16x16x32_bf16 v[180:183], a[24:27], v[60:63], v[180:183]\n\t"
      "global_load_dword v34, %[cko], %[wsb] offset:1280 sc1\n\t"
      "v_add3_u32 v46, v248, v249, %[t0]\n\t"
      "v_mfma_f32_16x16x32_bf16 v[188:191], a[28:31], v[60:63], v[188:191]\n\t"
      "v_add3_u32 v46, v46, v250, v251\n\t"
      "v_mfma_f32_16x16x32_bf16 v[196:199], a[32:35], v[60:63], v[196:199]\n\t"
      "v_add3_u32 v46, v46, v252, v253\n\t"
      "v_mfma_f32_16x16x32_bf16 v[204:207], a[36:39], v[60:63], v[204:207]\n\t"
      "v_add3_u32 v46, v46, v254, v255\n\t"
      "v_mfma_f32_16x16x32_bf16 v[168:171], a[40:43], v[240:243], v[168:171]\n\t"
      "s_add_u32 s38, %[hlo], 43008\n\t"
      "s_addc_u32 s39, %[hhi], 0\n\t"
      "global_load_dwordx4 v[72:75], %[l16], s[38:39] offset:-4096 sc1\n\t"
      "v_add3_u32 v46, v46, v208, v209\n\t"
      "v_mfma_f32_16x16x32_bf16 v[176:179], a[44:47], v[240:243], v[176:179]\n\t"
      "v_add3_u32 v46, v46, v210, v211\n\t"
      "v_mfma_f32_16x16x32_bf16 v[184:187], a[48:51], v[240:243], v[184:187]\n\t"
      "v_add3_u32 v46, v46, v212, v213\n\t"
      "v_mfma_f32_16x16x32_bf16 v[192:195], a[52:55], v[240:243], v[192:195]\n\t"
      "v_add3_u32 v46, v46, v214, v215\n\t"
      "v_mfma_f32_16x16x32_bf16 v[200:203], a[56:59], v[240:243], v[200:203]\n\t"
      "global_load_dwordx4 v[76:79], %[l16], s[38:39] offset:-3072 sc1\n\t"
      "v_add3_u32 v46, v46, v216, v217\n\t"
      "v_mfma_f32_16x16x32_bf16 v[172:175], a[40:43], v[244:247], v[172:175]\n\t"
      "v_add3_u32 v46, v46, v218, v219\n\t"
      "v_mfma_f32_16x16x32_bf16 v[180:183], a[44:47], v[244:247], v[180:183]\n\t"
      "v_add3_u32 v46, v46, v220, v221\n\t"
      "v_mfma_f32_16x16x32_bf16 v[188:191], a[48:51], v[244:247], v[188:191]\n\t"
      "v_add3_u32 v46, v46, v222, v223\n\t"
      "v_mfma_f32_16x16x32_bf16 v[196:199], a[52:55], v[244:247], v[196:199]\n\t"
      "global_load_dwordx4 v[80:83], %[l16], s[38:39] offset:0 sc1\n\t"
      "v_cmp_ne_u32 vcc, v46, v33\n\t"
      "v_mfma_f32_16x16x32_bf16 v[204:207], a[56:59], v[244:247], v[204:207]\n\t"
      "s_cbranch_vccnz L_g2_slow5_%=\n\t"
      "L_g2_back5_%=:\n\t"
      "Lpd74eef_72:\n\t"
      "v_mfma_f32_16x16x32_bf16 v[168:171], a[60:63], v[248:251], v[168:171]\n\t"
      "global_load_dwordx4 v[84:87], %[l16], s[38:39] offset:1024 sc1\n\t"
      "v_mfma_f32_16x16x32_bf16 v[176:179], a[64:67], v[248:251], v[176:179]\n\t"
      "v_mfma_f32_16x16x32_bf16 v[184:187], a[68:71], v[248:251], v[184:187]\n\t"
      "v_mfma_f32_16x16x32_bf16 v[192:195], a[72:75], v[248:251], v[192:195]\n\t"
      "v_mfma_f32_16x16x32_bf16 v[200:203], a[76:79], v[248:251], v[200:203]\n\t"
      "s_add_u32 s38, %[hlo], 51200\n\t"
      "s_addc_u32 s39, %[hhi], 0\n\t"
      "global_load_dwordx4 v[88:91], %[l16], s[38:39] offset:-4096 sc1\n\t"
      "v_mfma_f32_16x16x32_bf16 v[172:175], a[60:63], v[252:255], v[172:175]\n\t"
      "ds_write_b128 %[exw], v[144:147] offset:2048\n\t"
      "v_mfma_f32_16x16x32_bf16 v[180:183], a[64:67], v[252:255], v[180:183]\n\t"
      "v_mfma_f32_16x16x32_bf16 v[188:191], a[68:71], v[252:255], v[188:191]\n\t"
      "v_mfma_f32_16x16x32_bf16 v[196:199], a[72:75], v[252:255], v[196:199]\n\t"
      "global_load_dwordx4 v[92:95], %[l16], s[38:39] offset:-3072 sc1\n\t"
      "ds_write_b128 %[exw], v[148:151] offset:22528\n\t"
      "v_mfma_f32_16x16x32_bf16 v[204:207], a[76:79], v[252:255], v[204:207]\n\t"
      "v_mfma_f32_16x16x32_bf16 v[168:171], a[80:83], v[208:211], v[168:171]\n\t"
      "v_mfma_f32_16x16x32_bf16 v[176:179], a[84:87], v[208:211], v[176:179]\n\t"
      "ds_write_b128 %[exw], v[152:155] offset:3072\n\t"
      "v_mfma_f32_16x16x32_bf16 v[184:187], a[88:91], v[208:211], v[184:187]\n\t"
      "global_load_dword v35, %[cko], %[wsb] offset:1792 sc1\n\t"
      "v_mfma_f32_16x16x32_bf16 v[192:195], a[92:95], v[208:211], v[192:195]\n\t"
      "v_mfma_f32_16x16x32_bf16 v[200:203], a[96:99], v[208:211], v[200:203]\n\t"
      "ds_write_b128 %[exw], v[156:159] offset:23552\n\t"
      "v_mfma_f32_16x16x32_bf16 v[172:175], a[80:83], v[212:215], v[172:175]\n\t"
      "s_waitcnt vmcnt(7)\n\t"
      "v_mfma_f32_16x16x32_bf16 v[180:183], a[84:87], v[212:215], v[180:183]\n\t"
      "s_mov_b32 m0, %[xrm]\n\t"
      "s_nop 0\n\t"
      "global_load_lds_dwordx4 v40, %[wsb] sc1\n\t"
      "v_add3_u32 v46, v224, v225, %[t0]\n\t"
      "v_mfma_f32_16x16x32_bf16 v[188:191], a[88:91], v[212:215], v[188:191]\n\t"
      "v_add3_u32 v46, v46, v226, v227\n\t"
      "v_mfma_f32_16x16x32_bf16 v[196:199], a[92:95], v[212:215], v[196:199]\n\t"
      "v_add3_u32 v46, v46, v228, v229\n\t"
      "v_mfma_f32_16x16x32_bf16 v[204:207], a[96:99], v[212:215], v[204:207]\n\t"
      "v_add3_u32 v46, v46, v230, v231\n\t"
      "v_mfma_f32_16x16x32_bf16 v[168:171], a[100:103], v[216:219], v[168:171]\n\t"
      "v_add3_u32 v46, v46, v232, v233\n\t"
      "v_mfma_f32_16x16x32_bf16 v[176:179], a[104:107], v[216:219], v[176:179]\n\t"
      "v_add3_u32 v46, v46, v234, v235\n\t"
      "v_mfma_f32_16x16x32_bf16 v[184:187], a[108:111], v[216:219], v[184:187]\n\t"
      "v_add3_u32 v46, v46, v236, v237\n\t"
      "v_mfma_f32_16x16x32_bf16 v[192:195], a[112:115], v[216:219], v[192:195]\n\t"
      "v_add3_u32 v46, v46, v238, v239\n\t"
      "v_mfma_f32_16x16x32_bf16 v[200:203], a[116:119], v[216:219], v[200:203]\n\t"
      "v_add3_u32 v46, v46, v64, v65\n\t"
      "v_mfma_f32_16x16x32_bf16 v[172:175], a[100:103], v[220:223], v[172:175]\n\t"
      "v_add3_u32 v46, v46, v66, v67\n\t"
      "v_mfma_f32_16x16x32_bf16 v[180:183], a[104:107], v[220:223], v[180:183]\n\t"
      "v_add3_u32 v46, v46, v68, v69\n\t"
      "v_mfma_f32_16x16x32_bf16 v[188:191], a[108:111], v[220:223], v[188:191]\n\t"
      "v_add3_u32 v46, v46, v70, v71\n\t"
      "v_mfma_f32_16x16x32_bf16 v[196:199], a[112:115], v[220:223], v[196:199]\n\t"
      "v_cmp_ne_u32 vcc, v46, v34\n\t"
      "v_mfma_f32_16x16x32_bf16 v[204:207], a[116:119], v[220:223], v[204:207]\n\t"
      "s_cbranch_vccnz L_g2_slow6_%=\n\t"
      "L_g2_back6_%=:\n\t"
      "Lpd74eef_73:\n\t"
      "v_mfma_f32_16x16x32_bf16 v[168:171], a[120:123], v[224:227], v[168:171]\n\t"
      "v_mfma_f32_16x16x32_bf16 v[176:179], a[124:127], v[224:227], v[176:179]\n\t"
      "v_mfma_f32_16x16x32_bf16 v[184:187], a[128:131], v[224:227], v[184:187]\n\t"
      "v_mfma_f32_16x16x32_bf16 v[192:195], a[132:135], v[224:227], v[192:195]\n\t"
      "v_mfma_f32_16x16x32_bf16 v[200:203], a[136:139], v[224:227], v[200:203]\n\t"
      "global_load_lds_dwordx4 v41, %[wsb] offset:1024 sc1\n\t"
      "v_mfma_f32_16x16x32_bf16 v[172:175], a[120:123], v[228:231], v[172:175]\n\t"
      "ds_write_b128 %[exw], v[160:163] offset:4096\n\t"
      "v_mfma_f32_16x16x32_bf16 v[180:183], a[124:127], v[228:231], v[180:183]\n\t"
      "v_mfma_f32_16x16x32_bf16 v[188:191], a[128:131], v[228:231], v[188:191]\n\t"
      "v_mfma_f32_16x16x32_bf16 v[196:199], a[132:135], v[228:231], v[196:199]\n\t"
      "ds_write_b128 %[exw], v[164:167] offset:24576\n\t"
      "v_mfma_f32_16x16x32_bf16 v[204:207], a[136:139], v[228:231], v[204:207]\n\t"
      "v_mfma_f32_16x16x32_bf16 v[168:171], a[140:143], v[232:235], v[168:171]\n\t"
      "v_mfma_f32_16x16x32_bf16 v[176:179], a[144:147], v[232:235], v[176:179]\n\t"
      "v_mfma_f32_16x16x32_bf16 v[184:187], a[148:151], v[232:235], v[184:187]\n\t"
      "v_mfma_f32_16x16x32_bf16 v[192:195], a[152:155], v[232:235], v[192:195]\n\t"
      "v_mfma_f32_16x16x32_bf16 v[200:203], a[156:159], v[232:235], v[200:203]\n\t"
      "v_mfma_f32_16x16x32_bf16 v[172:175], a[140:143], v[236:239], v[172:175]\n\t"
      "s_waitcnt vmcnt(2)\n\t"
      "v_mfma_f32_16x16x32_bf16 v[180:183], a[144:147], v[236:239], v[180:183]\n\t"
      "v_add3_u32 v46, v72, v73, %[t0]\n\t"
      "v_mfma_f32_16x16x32_bf16 v[188:191], a[148:151], v[236:239], v[188:191]\n\t"
      "v_add3_u32 v46, v46, v74, v75\n\t"
      "v_mfma_f32_16x16x32_bf16 v[196:199], a[152:155], v[236:239], v[196:199]\n\t"
      "v_add3_u32 v46, v46, v76, v77\n\t"
      "v_mfma_f32_16x16x32_bf16 v[204:207], a[156:159], v[236:239], v[204:207]\n\t"
      "v_add3_u32 v46, v46, v78, v79\n\t"
      "v_mfma_f32_16x16x32_bf16 v[168:171], a[160:163], v[64:67], v[168:171]\n\t"
      "global_load_lds_dwordx4 v42, %[wsb] offset:2048 sc1\n\t"
      "v_add3_u32 v46, v46, v80, v81\n\t"
      "v_mfma_f32_16x16x32_bf16 v[176:179], a[164:167], v[64:67], v[176:179]\n\t"
      "v_add3_u32 v46, v46, v82, v83\n\t"
      "v_mfma_f32_16x16x32_bf16 v[184:187], a[168:171], v[64:67], v[184:187]\n\t"
      "v_add3_u32 v46, v46, v84, v85\n\t"
      "v_mfma_f32_16x16x32_bf16 v[192:195], a[172:175], v[64:67], v[192:195]\n\t"
      "v_add3_u32 v46, v46, v86, v87\n\t"
      "v_mfma_f32_16x16x32_bf16 v[200:203], a[176:179], v[64:67], v[200:203]\n\t"
      "v_add3_u32 v46, v46, v88, v89\n\t"
      "v_mfma_f32_16x16x32_bf16 v[172:175], a[160:163], v[68:71], v[172:175]\n\t"
      "v_add3_u32 v46, v46, v90, v91\n\t"
      "v_mfma_f32_16x16x32_bf16 v[180:183], a[164:167], v[68:71], v[180:183]\n\t"
      "v_add3_u32 v46, v46, v92, v93\n\t"
      "v_mfma_f32_16x16x32_bf16 v[188:191], a[168:171], v[68:71], v[188:191]\n\t"
      "v_add3_u32 v46, v46, v94, v95\n\t"
      "v_mfma_f32_16x16x32_bf16 v[196:199], a[172:175], v[68:71], v[196:199]\n\t"
      "v_cmp_ne_u32 vcc, v46, v35\n\t"
      "v_mfma_f32_16x16x32_bf16 v[204:207], a[176:179], v[68:71], v[204:207]\n\t"
      "s_cbranch_vccnz L_g2_slow7_%=\n\t"
      "L_g2_back7_%=:\n\t"
      "Lpd74eef_74:\n\t"
      "v_mfma_f32_16x16x32_bf16 v[168:171], a[180:183], v[72:75], v[168:171]\n\t"
      "v_add_u32 v46, 0xa000, %[exw]\n\t"
      "v_mfma_f32_16x16x32_bf16 v[168:171], a[200:203], v[80:83], v[168:171]\n\t"
      "v_mfma_f32_16x16x32_bf16 v[168:171], a[220:223], v[88:91], v[168:171]\n\t"
      "v_mfma_f32_16x16x32_bf16 v[172:175], a[180:183], v[76:79], v[172:175]\n\t"
      "v_mfma_f32_16x16x32_bf16 v[172:175], a[200:203], v[84:87], v[172:175]\n\t"
      "v_mfma_f32_16x16x32_bf16 v[172:175], a[220:223], v[92:95], v[172:175]\n\t"
      "v_mfma_f32_16x16x32_bf16 v[176:179], a[184:187], v[72:75], v[176:179]\n\t"
      "ds_write_b128 v46, v[168:171] offset:0\n\t"
      "v_mfma_f32_16x16x32_bf16 v[176:179], a[204:207], v[80:83], v[176:179]\n\t"
      "v_mfma_f32_16x16x32_bf16 v[176:179], a[224:227], v[88:91], v[176:179]\n\t"
      "v_mfma_f32_16x16x32_bf16 v[180:183], a[184:187], v[76:79], v[180:183]\n\t"
      "ds_write_b128 v46, v[172:175] offset:20480\n\t"
      "v_mfma_f32_16x16x32_bf16 v[180:183], a[204:207], v[84:87], v[180:183]\n\t"
      "v_mfma_f32_16x16x32_bf16 v[180:183], a[224:227], v[92:95], v[180:183]\n\t"
      "v_mfma_f32_16x16x32_bf16 v[184:187], a[188:191], v[72:75], v[184:187]\n\t"
      "ds_write_b128 v46, v[176:179] offset:1024\n\t"
      "v_mfma_f32_16x16x32_bf16 v[184:187], a[208:211], v[80:83], v[184:187]\n\t"
      "v_mfma_f32_16x16x32_bf16 v[184:187], a[228:231], v[88:91], v[184:187]\n\t"
      "v_mfma_f32_16x16x32_bf16 v[188:191], a[188:191], v[76:79], v[188:191]\n\t"
      "ds_write_b128 v46, v[180:183] offset:21504\n\t"
      "v_mfma_f32_16x16x32_bf16 v[188:191], a[208:211], v[84:87], v[188:191]\n\t"
      "v_mfma_f32_16x16x32_bf16 v[188:191], a[228:231], v[92:95], v[188:191]\n\t"
      "v_mfma_f32_16x16x32_bf16 v[192:195], a[192:195], v[72:75], v[192:195]\n\t"
      "ds_write_b128 v46, v[184:187] offset:2048\n\t"
      "v_mfma_f32_16x16x32_bf16 v[192:195], a[212:215], v[80:83], v[192:195]\n\t"
      "v_mfma_f32_16x16x32_bf16 v[192:195], a[232:235], v[88:91], v[192:195]\n\t"
      "v_mfma_f32_16x16x32_bf16 v[196:199], a[192:195], v[76:79], v[196:199]\n\t"
      "ds_write_b128 v46, v[188:191] offset:22528\n\t"
      "v_mfma_f32_16x16x32_bf16 v[196:199], a[212:215], v[84:87], v[196:199]\n\t"
      "v_mfma_f32_16x16x32_bf16 v[196:199], a[232:235], v[92:95], v[196:199]\n\t"
      "v_mfma_f32_16x16x32_bf16 v[200:203], a[196:199], v[72:75], v[200:203]\n\t"
      "ds_write_b128 v46, v[192:195] offset:3072\n\t"
      "v_mfma_f32_16x16x32_bf16 v[200:203], a[216:219], v[80:83], v[200:203]\n\t"
      "v_mfma_f32_16x16x32_bf16 v[200:203], a[236:239], v[88:91], v[200:203]\n\t"
      "v_mfma_f32_16x16x32_bf16 v[204:207], a[196:199], v[76:79], v[204:207]\n\t"
      "ds_write_b128 v46, v[196:199] offset:23552\n\t"
      "v_mfma_f32_16x16x32_bf16 v[204:207], a[216:219], v[84:87], v[204:207]\n\t"
      "v_mfma_f32_16x16x32_bf16 v[204:207], a[236:239], v[92:95], v[204:207]\n\t"
      "ds_write_b128 v46, v[200:203] offset:4096\n\t"
      "s_mov_b32 m0, s45\n\t"
      "s_waitcnt vmcnt(0)\n\t"
      "s_nop 5\n\t"
      "ds_write_b128 v46, v[204:207] offset:24576\n\t"
      "s_branch L_g2_end%=\n\t"
      "L_g2_slow0_%=:\n\t"
      "s_mov_b64 s[42:43], s[38:39]\n\t"
      "s_add_u32 s38, %[hlo], 4096\n\t"
      "s_addc_u32 s39, %[hhi], 0\n\t"
      "global_load_dwordx4 v[208:211], %[l16], s[38:39] offset:-4096 sc1\n\t"
      "global_load_dwordx4 v[212:215], %[l16], s[38:39] offset:-3072 sc1\n\t"
      "global_load_dwordx4 v[216:219], %[l16], s[38:39] offset:0 sc1\n\t"
      "global_load_dwordx4 v[220:223], %[l16], s[38:39] offset:1024 sc1\n\t"
      "s_add_u32 s38, %[hlo], 12288\n\t"
      "s_addc_u32 s39, %[hhi], 0\n\t"
      "global_load_dwordx4 v[224:227], %[l16], s[38:39] offset:-4096 sc1\n\t"
      "global_load_dwordx4 v[228:231], %[l16], s[38:39] offset:-3072 sc1\n\t"
      "global_load_dword v184, %[cko], %[wsb] offset:0 sc1\n\t"
      "s_mov_b64 s[38:39], s[42:43]\n\t"
      "s_waitcnt vmcnt(0)\n\t"
      "v_add3_u32 v46, v208, v209, %[t0]\n\t"
      "v_add3_u32 v46, v46, v210, v211\n\t"
      "v_add3_u32 v46, v46, v212, v213\n\t"
      "v_add3_u32 v46, v46, v214, v215\n\t"
      "v_add3_u32 v46, v46, v216, v217\n\t"
      "v_add3_u32 v46, v46, v218, v219\n\t"
      "v_add3_u32 v46, v46, v220, v221\n\t"
      "v_add3_u32 v46, v46, v222, v223\n\t"
      "v_add3_u32 v46, v46, v224, v225\n\t"
      "v_add3_u32 v46, v46, v226, v227\n\t"
      "v_add3_u32 v46, v46, v228, v229\n\t"
      "v_add3_u32 v46, v46, v230, v231\n\t"
      "v_cmp_ne_u32 vcc, v46, v184\n\t"
      "s_sub_u32 s44, s44, 1\n\t"
      "s_cbranch_vccz L_g2_back0_%=\n\t"
      "s_cmp_lg_u32 s44, 0\n\t"
      "s_cbranch_scc1 L_g2_slow0_%=\n\t"
      "s_branch L_g2_back0_%=\n\t"
      "L_g2_slow1_%=:\n\t"
      "s_mov_b64 s[42:43], s[38:39]\n\t"
      "s_add_u32 s38, %[hlo], 16384\n\t"
      "s_addc_u32 s39, %[hhi], 0\n\t"
      "global_load_dwordx4 v[232:235], %[l16], s[38:39] offset:-4096 sc1\n\t"
      "global_load_dwordx4 v[236:239], %[l16], s[38:39] offset:-3072 sc1\n\t"
      "global_load_dwordx4 v[64:67], %[l16], s[38:39] offset:0 sc1\n\t"
      "global_load_dwordx4 v[68:71], %[l16], s[38:39] offset:1024 sc1\n\t"
      "s_add_u32 s38, %[hlo], 24576\n\t"
      "s_addc_u32 s39, %[hhi], 0\n\t"
      "global_load_dwordx4 v[72:75], %[l16], s[38:39] offset:-4096 sc1\n\t"
      "global_load_dwordx4 v[76:79], %[l16], s[38:39] offset:-3072 sc1\n\t"
      "global_load_dword v185, %[cko], %[wsb] offset:512 sc1\n\t"
      "s_mov_b64 s[38:39], s[42:43]\n\t"
      "s_waitcnt vmcnt(0)\n\t"
      "v_add3_u32 v46, v232, v233, %[t0]\n\t"
      "v_add3_u32 v46, v46, v234, v235\n\t"
      "v_add3_u32 v46, v46, v236, v237\n\t"
      "v_add3_u32 v46, v46, v238, v239\n\t"
      "v_add3_u32 v46, v46, v64, v65\n\t"
      "v_add3_u32 v46, v46, v66, v67\n\t"
      "v_add3_u32 v46, v46, v68, v69\n\t"
      "v_add3_u32 v46, v46, v70, v71\n\t"
      "v_add3_u32 v46, v46, v72, v73\n\t"
      "v_add3_u32 v46, v46, v74, v75\n\t"
      "v_add3_u32 v46, v46, v76, v77\n\t"
      "v_add3_u32 v46, v46, v78, v79\n\t"
      "v_cmp_ne_u32 vcc, v46, v185\n\t"
      "s_sub_u32 s44, s44, 1\n\t"
      "s_cbranch_vccz L_g2_back1_%=\n\t"
      "s_cmp_lg_u32 s44, 0\n\t"
      "s_cbranch_scc1 L_g2_slow1_%=\n\t"
      "s_branch L_g2_back1_%=\n\t"
      "L_g2_slow2_%=:\n\t"
      "s_mov_b64 s[42:43], s[38:39]\n\t"
      "s_add_u32 s38, %[hlo], 28672\n\t"
      "s_addc_u32 s39, %[hhi], 0\n\t"
      "global_load_dwordx4 v[80:83], %[l16], s[38:39] offset:-4096 sc1\n\t"
      "global_load_dwordx4 v[84:87], %[l16], s[38:39] offset:-3072 sc1\n\t"
      "global_load_dwordx4 v[88:91], %[l16], s[38:39] offset:0 sc1\n\t"
      "global_load_dwordx4 v[92:95], %[l16], s[38:39] offset:1024 sc1\n\t"
      "s_add_u32 s38, %[hlo], 36864\n\t"
      "s_addc_u32 s39, %[hhi], 0\n\t"
      "global_load_dwordx4 v[96:99], %[l16], s[38:39] offset:-4096 sc1\n\t"
      "global_load_dwordx4 v[100:103], %[l16], s[38:39] offset:-3072 sc1\n\t"
      "global_load_dword v186, %[cko], %[wsb] offset:1024 sc1\n\t"
      "s_mov_b64 s[38:39], s[42:43]\n\t"
      "s_waitcnt vmcnt(0)\n\t"
      "v_add3_u32 v46, v80, v81, %[t0]\n\t"
      "v_add3_u32 v46, v46, v82, v83\n\t"
      "v_add3_u32 v46, v46, v84, v85\n\t"
      "v_add3_u32 v46, v46, v86, v87\n\t"
      "v_add3_u32 v46, v46, v88, v89\n\t"
      "v_add3_u32 v46, v46, v90, v91\n\t"
      "v_add3_u32 v46, v46, v92, v93\n\t"
      "v_add3_u32 v46, v46, v94, v95\n\t"
      "v_add3_u32 v46, v46, v96, v97\n\t"
      "v_add3_u32 v46, v46, v98, v99\n\t"
      "v_add3_u32 v46, v46, v100, v101\n\t"
      "v_add3_u32 v46, v46, v102, v103\n\t"
      "v_cmp_ne_u32 vcc, v46, v186\n\t"
      "s_sub_u32 s44, s44, 1\n\t"
      "s_cbranch_vccz L_g2_back2_%=\n\t"
      "s_cmp_lg_u32 s44, 0\n\t"
      "s_cbranch_scc1 L_g2_slow2_%=\n\t"
      "s_branch L_g2_back2_%=\n\t"
      "L_g2_slow3_%=:\n\t"
      "s_mov_b64 s[42:43], s[38:39]\n\t"
      "s_add_u32 s38, %[hlo], 40960\n\t"
      "s_addc_u32 s39, %[hhi], 0\n\t"
      "global_load_dwordx4 v[104:107], %[l16], s[38:39] offset:-4096 sc1\n\t"
      "global_load_dwordx4 v[108:111], %[l16], s[38:39] offset:-3072 sc1\n\t"
      "global_load_dwordx4 v[112:115], %[l16], s[38:39] offset:0 sc1\n\t"
      "global_load_dwordx4 v[116:119], %[l16], s[38:39] offset:1024 sc1\n\t"
      "s_add_u32 s38, %[hlo], 49152\n\t"
      "s_addc_u32 s39, %[hhi], 0\n\t"
      "global_load_dwordx4 v[120:123], %[l16], s[38:39] offset:-4096 sc1\n\t"
      "global_load_dwordx4 v[124:127], %[l16], s[38:39] offset:-3072 sc1\n\t"
      "global_load_dword v187, %[cko], %[wsb] offset:1536 sc1\n\t"
      "s_mov_b64 s[38:39], s[42:43]\n\t"
      "s_waitcnt vmcnt(0)\n\t"
      "v_add3_u32 v46, v104, v105, %[t0]\n\t"
      "v_add3_u32 v46, v46, v106, v107\n\t"
      "v_add3_u32 v46, v46, v108, v109\n\t"
      "v_add3_u32 v46, v46, v110, v111\n\t"
      "v_add3_u32 v46, v46, v112, v113\n\t"
      "v_add3_u32 v46, v46, v114, v115\n\t"
      "v_add3_u32 v46, v46, v116, v117\n\t"
      "v_add3_u32 v46, v46, v118, v119\n\t"
      "v_add3_u32 v46, v46, v120, v121\n\t"
      "v_add3_u32 v46, v46, v122, v123\n\t"
      "v_add3_u32 v46, v46, v124, v125\n\t"
      "v_add3_u32 v46, v46, v126, v127\n\t"
      "v_cmp_ne_u32 vcc, v46, v187\n\t"
      "s_sub_u32 s44, s44, 1\n\t"
      "s_cbranch_vccz L_g2_back3_%=\n\t"
      "s_cmp_lg_u32 s44, 0\n\t"
      "s_cbranch_scc1 L_g2_slow3_%=\n\t"
      "s_branch L_g2_back3_%=\n\t"
      "L_g2_slow4_%=:\n\t"
      "s_mov_b64 s[42:43], s[38:39]\n\t"
      "s_add_u32 s38, %[hlo], 6144\n\t"
      "s_addc_u32 s39, %[hhi], 0\n\t"
      "global_load_dwordx4 v[48:51], %[l16], s[38:39] offset:-4096 sc1\n\t"
      "global_load_dwordx4 v[52:55], %[l16], s[38:39] offset:-3072 sc1\n\t"
      "global_load_dwordx4 v[56:59], %[l16], s[38:39] offset:0 sc1\n\t"
      "global_load_dwordx4 v[60:63], %[l16], s[38:39] offset:1024 sc1\n\t"
      "s_add_u32 s38, %[hlo], 14336\n\t"
      "s_addc_u32 s39, %[hhi], 0\n\t"
      "global_load_dwordx4 v[240:243], %[l16], s[38:39] offset:-4096 sc1\n\t"
      "global_load_dwordx4 v[244:247], %[l16], s[38:39] offset:-3072 sc1\n\t"
      "global_load_dword v32, %[cko], %[wsb] offset:256 sc1\n\t"
      "s_mov_b64 s[38:39], s[42:43]\n\t"
      "s_waitcnt vmcnt(0)\n\t"
      "v_add3_u32 v46, v48, v49, %[t0]\n\t"
      "v_add3_u32 v46, v46, v50, v51\n\t"
      "v_add3_u32 v46, v46, v52, v53\n\t"
      "v_add3_u32 v46, v46, v54, v55\n\t"
      "v_add3_u32 v46, v46, v56, v57\n\t"
      "v_add3_u32 v46, v46, v58, v59\n\t"
      "v_add3_u32 v46, v46, v60, v61\n\t"
      "v_add3_u32 v46, v46, v62, v63\n\t"
      "v_add3_u32 v46, v46, v240, v241\n\t"
      "v_add3_u32 v46, v46, v242, v243\n\t"
      "v_add3_u32 v46, v46, v244, v245\n\t"
      "v_add3_u32 v46, v46, v246, v247\n\t"
      "v_cmp_ne_u32 vcc, v46, v32\n\t"
      "s_sub_u32 s44, s44, 1\n\t"
      "s_cbranch_vccz L_g2_back4_%=\n\t"
      "s_cmp_lg_u32 s44, 0\n\t"
      "s_cbranch_scc1 L_g2_slow4_%=\n\t"
      "s_branch L_g2_back4_%=\n\t"
      "L_g2_slow5_%=:\n\t"
      "s_mov_b64 s[42:43], s[38:39]\n\t"
      "s_add_u32 s38, %[hlo], 18432\n\t"
      "s_addc_u32 s39, %[hhi], 0\n\t"
      "global_load_dwordx4 v[248:251], %[l16], s[38:39] offset:-4096 sc1\n\t"
      "global_load_dwordx4 v[252:255], %[l16], s[38:39] offset:-3072 sc1\n\t"
      "global_load_dwordx4 v[208:211], %[l16], s[38:39] offset:0 sc1\n\t"
      "global_load_dwordx4 v[212:215], %[l16], s[38:39] offset:1024 sc1\n\t"
      "s_add_u32 s38, %[hlo], 26624\n\t"
      "s_addc_u32 s39, %[hhi], 0\n\t"
      "global_load_dwordx4 v[216:219], %[l16], s[38:39] offset:-4096 sc1\n\t"
      "global_load_dwordx4 v[220:223], %[l16], s[38:39] offset:-3072 sc1\n\t"
      "global_load_dword v33, %[cko], %[wsb] offset:768 sc1\n\t"
      "s_mov_b64 s[38:39], s[42:43]\n\t"
      "s_waitcnt vmcnt(0)\n\t"
      "v_add3_u32 v46, v248, v249, %[t0]\n\t"
      "v_add3_u32 v46, v46, v250, v251\n\t"
      "v_add3_u32 v46, v46, v252, v253\n\t"
      "v_add3_u32 v46, v46, v254, v255\n\t"
      "v_add3_u32 v46, v46, v208, v209\n\t"
      "v_add3_u32 v46, v46, v210, v211\n\t"
      "v_add3_u32 v46, v46, v212, v213\n\t"
      "v_add3_u32 v46, v46, v214, v215\n\t"
      "v_add3_u32 v46, v46, v216, v217\n\t"
      "v_add3_u32 v46, v46, v218, v219\n\t"
      "v_add3_u32 v46, v46, v220, v221\n\t"
      "v_add3_u32 v46, v46, v222, v223\n\t"
      "v_cmp_ne_u32 vcc, v46, v33\n\t"
      "s_sub_u32 s44, s44, 1\n\t"
      "s_cbranch_vccz L_g2_back5_%=\n\t"
      "s_cmp_lg_u32 s44, 0\n\t"
      "s_cbranch_scc1 L_g2_slow5_%=\n\t"
      "s_branch L_g2_back5_%=\n\t"
      "L_g2_slow6_%=:\n\t"
      "s_mov_b64 s[42:43], s[38:39]\n\t"
      "s_add_u32 s38, %[hlo], 30720\n\t"
      "s_addc_u32 s39, %[hhi], 0\n\t"
      "global_load_dwordx4 v[224:227], %[l16], s[38:39] offset:-4096 sc1\n\t"
      "global_load_dwordx4 v[228:231], %[l16], s[38:39] offset:-3072 sc1\n\t"
      "global_load_dwordx4 v[232:235], %[l16], s[38:39] offset:0 sc1\n\t"
      "global_load_dwordx4 v[236:239], %[l16], s[38:39] offset:1024 sc1\n\t"
      "s_add_u32 s38, %[hlo], 38912\n\t"
      "s_addc_u32 s39, %[hhi], 0\n\t"
      "global_load_dwordx4 v[64:67], %[l16], s[38:39] offset:-4096 sc1\n\t"
      "global_load_dwordx4 v[68:71], %[l16], s[38:39] offset:-3072 sc1\n\t"
      "global_load_dword v34, %[cko], %[wsb] offset:1280 sc1\n\t"
      "s_mov_b64 s[38:39], s[42:43]\n\t"
      "s_waitcnt vmcnt(0)\n\t"
      "v_add3_u32 v46, v224, v225, %[t0]\n\t"
      "v_add3_u32 v46, v46, v226, v227\n\t"
      "v_add3_u32 v46, v46, v228, v229\n\t"
      "v_add3_u32 v46, v46, v230, v231\n\t"
      "v_add3_u32 v46, v46, v232, v233\n\t"
      "v_add3_u32 v46, v46, v234, v235\n\t"
      "v_add3_u32 v46, v46, v236, v237\n\t"
      "v_add3_u32 v46, v46, v238, v239\n\t"
      "v_add3_u32 v46, v46, v64, v65\n\t"
      "v_add3_u32 v46, v46, v66, v67\n\t"
      "v_add3_u32 v46, v46, v68, v69\n\t"
      "v_add3_u32 v46, v46, v70, v71\n\t"
      "v_cmp_ne_u32 vcc, v46, v34\n\t"
      "s_sub_u32 s44, s44, 1\n\t"
      "s_cbranch_vccz L_g2_back6_%=\n\t"
      "s_cmp_lg_u32 s44, 0\n\t"
      "s_cbranch_scc1 L_g2_slow6_%=\n\t"
      "s_branch L_g2_back6_%=\n\t"
      "L_g2_slow7_%=:\n\t"
      "s_mov_b64 s[42:43], s[38:39]\n\t"
      "s_add_u32 s38, %[hlo], 43008\n\t"
      "s_addc_u32 s39, %[hhi], 0\n\t"
      "global_load_dwordx4 v[72:75], %[l16], s[38:39] offset:-4096 sc1\n\t"
      "global_load_dwordx4 v[76:79], %[l16], s[38:39] offset:-3072 sc1\n\t"
      "global_load_dwordx4 v[80:83], %[l16], s[38:39] offset:0 sc1\n\t"
      "global_load_dwordx4 v[84:87], %[l16], s[38:39] offset:1024 sc1\n\t"
      "s_add_u32 s38, %[hlo], 51200\n\t"
      "s_addc_u32 s39, %[hhi], 0\n\t"
      "global_load_dwordx4 v[88:91], %[l16], s[38:39] offset:-4096 sc1\n\t"
      "global_load_dwordx4 v[92:95], %[l16], s[38:39] offset:-3072 sc1\n\t"
      "global_load_dword v35, %[cko], %[wsb] offset:1792 sc1\n\t"
      "s_mov_b64 s[38:39], s[42:43]\n\t"
      "s_waitcnt vmcnt(0)\n\t"
      "v_add3_u32 v46, v72, v73, %[t0]\n\t"
      "v_add3_u32 v46, v46, v74, v75\n\t"
      "v_add3_u32 v46, v46, v76, v77\n\t"
      "v_add3_u32 v46, v46, v78, v79\n\t"
      "v_add3_u32 v46, v46, v80, v81\n\t"
      "v_add3_u32 v46, v46, v82, v83\n\t"
      "v_add3_u32 v46, v46, v84, v85\n\t"
      "v_add3_u32 v46, v46, v86, v87\n\t"
      "v_add3_u32 v46, v46, v88, v89\n\t"
      "v_add3_u32 v46, v46, v90, v91\n\t"
      "v_add3_u32 v46, v46, v92, v93\n\t"
      "v_add3_u32 v46, v46, v94, v95\n\t"
      "v_cmp_ne_u32 vcc, v46, v35\n\t"
      "s_sub_u32 s44, s44, 1\n\t"
      "s_cbranch_vccz L_g2_back7_%=\n\t"
      "s_cmp_lg_u32 s44, 0\n\t"
      "s_cbranch_scc1 L_g2_slow7_%=\n\t"
      "s_branch L_g2_back7_%=\n\t"
      "\n\t"
      "L_g2_end%=:\n\t"
      "Lpd74eef_75:\n\t"
      : "+{v[64:95]}"(h0), "+{v[96:127]}"(h1), "+{v[208:239]}"(h2), "+{v[184:187]}"(hc)
      : [l16] "v"(lane * 16), [exw] "v"(lbase + w * 5120 + lane * 16), [hlo] "s"((unsigned)hb), [hhi] "s"((unsigned)(hb >> 32)), [dlo] "s"((unsigned)wb),
        [dhi] "s"((unsigned)(wb >> 32)), [cko] "v"(hco), [wsb] "s"(ws), [t0] "s"(T[0]), [xrm] "s"(lbase + L_XR + w * 3072), "{v[40:43]}"(xg)
      : "memory", "scc", "vcc", "s38", "s39", "s40", "s41", "s42", "s43", "s44", "s45", "s46", "s47",  "v32", "v33", "v34", "v35", "v46", "v47", "v48", "v49", "v50", "v51", "v52", "v53", "v54", "v55", "v56", "v57", "v58", "v59", "v60", "v61", "v62", "v63", "v128", "v129", "v130", "v131", "v132", "v133", "v134", "v135", "v136", "v137", "v138", "v139", "v140", "v141", "v142", "v143", "v144", "v145", "v146", "v147", "v148", "v149", "v150", "v151", "v152", "v153", "v154", "v155", "v156", "v157", "v158", "v159", "v160", "v161", "v162", "v163", "v164", "v165", "v166", "v167", "v168", "v169", "v170", "v171", "v172", "v173", "v174", "v175", "v176", "v177", "v178", "v179", "v180", "v181", "v182", "v183", "v188", "v189", "v190", "v191", "v192", "v193", "v194", "v195", "v196", "v197", "v198", "v199", "v200", "v201", "v202", "v203", "v204", "v205", "v206", "v207", "v240", "v241", "v242", "v243", "v244", "v245", "v246", "v247", "v248", "v249", "v250", "v251", "v252", "v253", "v254", "v255", "a0", "a1", "a2", "a3", "a4", "a5", "a6", "a7", "a8", "a9", "a10", "a11", "a12", "a13", "a14", "a15", "a16", "a17", "a18", "a19", "a20", "a21", "a22", "a23", "a24", "a25", "a26", "a27", "a28", "a29", "a30", "a31", "a32", "a33", "a34", "a35", "a36", "a37", "a38", "a39", "a40", "a41", "a42", "a43", "a44", "a45", "a46", "a47", "a48", "a49", "a50", "a51", "a52", "a53", "a54", "a55", "a56", "a57", "a58", "a59", "a60", "a61", "a62", "a63", "a64", "a65", "a66", "a67", "a68", "a69", "a70", "a71", "a72", "a73", "a74", "a75", "a76", "a77", "a78", "a79", "a80", "a81", "a82", "a83", "a84", "a85", "a86", "a87", "a88", "a89", "a90", "a91", "a92", "a93", "a94", "a95", "a96", "a97", "a98", "a99", "a100", "a101", "a102", "a103", "a104", "a105", "a106", "a107", "a108", "a109", "a110", "a111", "a112", "a113", "a114", "a115", "a116", "a117", "a118", "a119", "a120", "a121", "a122", "a123", "a124", "a125", "a126", "a127", "a128", "a129", "a130", "a131", "a132", "a133", "a134", "a135", "a136", "a137", "a138", "a139", "a140", "a141", "a142", "a143", "a144", "a145", "a146", "a147", "a148", "a149", "a150", "a151", "a152", "a153", "a154", "a155", "a156", "a157", "a158", "a159", "a160", "a161", "a162", "a163", "a164", "a165", "a166", "a167", "a168", "a169", "a170", "a171", "a172", "a173", "a174", "a175", "a176", "a177", "a178", "a179", "a180", "a181", "a182", "a183", "a184", "a185", "a186", "a187", "a188", "a189", "a190", "a191", "a192", "a193", "a194", "a195", "a196", "a197", "a198", "a199", "a200", "a201", "a202", "a203", "a204", "a205", "a206", "a207", "a208", "a209", "a210", "a211", "a212", "a213", "a214", "a215", "a216", "a217", "a218", "a219", "a220", "a221", "a222", "a223", "a224", "a225", "a226", "a227", "a228", "a229", "a230", "a231", "a232", "a233", "a234", "a235", "a236", "a237", "a238", "a239");

  asm volatile("Lpd74eef_76:");
  {
    u32x2 xv[5];
    const unsigned xa = lbase + L_XR + w * 3072 + (4 * fq + (ip >> 2)) * 32 + (lane & 3) * 8;
#pragma unroll
    for (int n = 0; n < 5; ++n) DSRTR(xv[n], xa, n * 512);
    f32x4 gb[5];
#pragma unroll
    for (int n = 0; n < 5; ++n) gb[n] = LDSF4(lds + L_GB + (4 * n + fq) * 16);
    const int row = (bm2 >> 1) * 128 + (w >> 1) * 64 + (bm2 & 1) * 32 + (w & 1) * 16 + ip;
    GAS u16* op = out + (size_t)row * D_ + bn2 * 80 + fq * 4;
    asm volatile("" : "+v"(gb[0]), "+v"(gb[1]), "+v"(gb[2]), "+v"(gb[3]), "+v"(gb[4]));
    asm volatile("s_waitcnt lgkmcnt(0)\n\ts_barrier" : "+v"(xv[0]), "+v"(xv[1]), "+v"(xv[2]), "+v"(xv[3]), "+v"(xv[4])::"memory");
    f32x4 pp[20];
#pragma unroll
    for (int n = 0; n < 5; ++n)
#pragma unroll
      for (int j = 0; j < 4; ++j) pp[4 * n + j] = LDSF4(lds + ((w * 20 + j * 5 + n) * 64 + lane) * 16);
    SCHEDB();
#pragma unroll
    for (int n = 0; n < 5; ++n) {
      const f32x4 a = (pp[4 * n] + pp[4 * n + 1]) + (pp[4 * n + 2] + pp[4 * n + 3]);
      const f32x2 m01 = rb2((f32x2){gb[n][0], gb[n][1]} * rb2((f32x2){a[0], a[1]}));
      const f32x2 m23 = rb2((f32x2){gb[n][2], gb[n][3]} * rb2((f32x2){a[2], a[3]}));
      const f32x2 o01 = up2(xv[n][0]) + m01, o23 = up2(xv[n][1]) + m23;
      const u32x2 ov = {pk2(o01[0], o01[1]), pk2(o23[0], o23[1])};
      *(GAS u32x2*)(op + n * 16) = ov;
    }
  }
}

extern "C" void kernel_launch(void* const* d_in, const int* in_sizes, int n_in, void* d_out, int out_size,
                              void* d_ws, size_t ws_size, hipStream_t stream) {
  mega<<<256, 256, 0, stream>>>((const u16*)d_in[0], (const u16*)d_in[1], (const u16*)d_in[2], (const u16*)d_in[3],
                                (const u16*)d_in[4], (const u16*)d_in[5], (const u16*)d_in[7], (const u16*)d_in[8],
                                (const u16*)d_in[9], (const u16*)d_in[10], (const u16*)d_in[11], (char*)d_ws, (u16*)d_out);
}
```

```cpp
#include <hip/hip_runtime.h>
#include <hip/hip_bf16.h>
#include <stdint.h>
#include <type_traits>

typedef unsigned short u16;
typedef unsigned long long u64;
typedef __attribute__((ext_vector_type(8))) short bf16x8;
typedef __attribute__((ext_vector_type(4))) float f32x4;
typedef __attribute__((ext_vector_type(8))) float f32x8;
typedef __attribute__((ext_vector_type(16))) float f32x16;
typedef __attribute__((ext_vector_type(32))) float f32x32;
typedef __attribute__((ext_vector_type(4))) unsigned int u32x4;
typedef __attribute__((ext_vector_type(2))) unsigned int u32x2;
typedef __attribute__((ext_vector_type(3))) unsigned int u32x3;
typedef __attribute__((ext_vector_type(8))) unsigned int u32x8;
typedef __attribute__((ext_vector_type(16))) unsigned int u32x16;
typedef __attribute__((ext_vector_type(32))) unsigned int u32x32;
typedef __attribute__((ext_vector_type(2))) __bf16 bf16x2_t;
typedef __attribute__((ext_vector_type(2))) float f32x2;
typedef __attribute__((address_space(3))) char lds_char;
typedef u32x4 u32x4_a4 __attribute__((aligned(4)));
#define GAS __attribute__((address_space(1)))

static constexpr int S_ = 2048, D_ = 640, F_ = 1536, NMOD = 6 * 640;

__device__ __forceinline__ float bf2f(u16 v) { return __uint_as_float(((unsigned)v) << 16); }
__device__ __forceinline__ float lo16(unsigned w) { return __uint_as_float(w << 16); }
__device__ __forceinline__ float hi16(unsigned w) { return __uint_as_float(w & 0xFFFF0000u); }
__device__ __forceinline__ unsigned pk2(float a, float b) {
  f32x2 v = {a, b};
  bf16x2_t r = __builtin_convertvector(v, bf16x2_t);
  return __builtin_bit_cast(unsigned, r);
}
__device__ __forceinline__ float rb1(float a) { return __uint_as_float(pk2(0.f, a)); }
__device__ __forceinline__ f32x2 rb2(f32x2 a) { return (f32x2){rb1(a[0]), rb1(a[1])}; }
__device__ __forceinline__ f32x2 up2(unsigned a) { return (f32x2){lo16(a), hi16(a)}; }
__device__ __forceinline__ unsigned umin_(unsigned a, unsigned b) { return a < b ? a : b; }
__device__ __forceinline__ unsigned umax_(unsigned a, unsigned b) { return a > b ? a : b; }
__device__ __forceinline__ unsigned san(unsigned a) { return umin_(a, 0xFFFFFFFEu); }
__device__ __forceinline__ u32x4 san4(u32x4 a) { return (u32x4){san(a[0]), san(a[1]), san(a[2]), san(a[3])}; }
__device__ __forceinline__ float modv(u32x4 a, u32x4 b) {
  return rb1((__uint_as_float(a[0]) + __uint_as_float(a[2])) + (__uint_as_float(b[0]) + __uint_as_float(b[2])));
}
__device__ __forceinline__ unsigned tagx(u32x4 f, u32x2 t) { return (f[0] ^ t[0]) | (f[1] ^ t[1]) | (f[2] ^ t[0]) | (f[3] ^ t[1]); }
__device__ __forceinline__ unsigned modx(u32x4 a, u32x4 b, unsigned tag) { return (a[1] ^ tag) | (a[3] ^ tag) | (b[1] ^ tag) | (b[3] ^ tag); }
__device__ __forceinline__ unsigned mx4(u32x4 a) { return umax_(umax_(a[0], a[1]), umax_(a[2], a[3])); }
__device__ __forceinline__ unsigned ror_(unsigned a, unsigned s) { return __builtin_amdgcn_alignbit(a, a, s); }
#define DPP_ADDU(v, ctrl) v += __builtin_amdgcn_update_dpp(0u, v, ctrl, 0xf, 0xf, true)
__device__ __forceinline__ unsigned cks(u32x4 d, unsigned sh) {
  unsigned s = ror_(d[0] + ror_(d[1], 7) + (ror_(d[2], 13) + ror_(d[3], 19)), sh);
  DPP_ADDU(s, 0xB1); DPP_ADDU(s, 0x4E); DPP_ADDU(s, 0x141);
  return s;
}
__device__ __forceinline__ unsigned cks2(unsigned d0, unsigned d1, int lane) {
  unsigned s = ror_(d0, (lane & 1) * 13) + ror_(d1, 7 + (lane & 1) * 12);
  DPP_ADDU(s, 0xB1);
  s = ror_(s, (lane & 14) * 2);
  DPP_ADDU(s, 0x4E); DPP_ADDU(s, 0x141); DPP_ADDU(s, 0x140);
  return s;
}
#define SCHEDB() __builtin_amdgcn_sched_barrier(0)
#define LDS32(p) (*(__attribute__((address_space(3))) unsigned*)(p))
#define LDS64(p) (*(__attribute__((address_space(3))) u32x2*)(p))
#define LDS128(p) (*(__attribute__((address_space(3))) u32x4*)(p))
#define LDSF(p) (*(__attribute__((address_space(3))) float*)(p))
#define LDSF2(p) (*(__attribute__((address_space(3))) f32x2*)(p))
#define LDSF4(p) (*(__attribute__((address_space(3))) f32x4*)(p))
#define WAITV(n) asm volatile("s_waitcnt vmcnt(" #n ")" ::: "memory")
#define WAITL() asm volatile("s_waitcnt lgkmcnt(0)" ::: "memory")
#define BAR() do { asm volatile("s_waitcnt lgkmcnt(0)\n\ts_barrier" ::: "memory"); } while (0)
#define DPP_ADD(v, ctrl) v += __builtin_bit_cast(float, __builtin_amdgcn_update_dpp(0, __builtin_bit_cast(int, v), ctrl, 0xf, 0xf, true))
#define DSRTR(dst, addr, off) asm volatile("ds_read_b64_tr_b16 %0, %1 offset:%2" : "=v"(dst) : "v"(addr), "i"(off))
#define LD128(off) __builtin_amdgcn_raw_buffer_load_b128(rs, (off), 0, 16)
#define LD64(off) __builtin_amdgcn_raw_buffer_load_b64(rs, (off), 0, 16)
#define LD32(off) __builtin_amdgcn_raw_buffer_load_b32(rs, (off), 0, 16)
#define ST128(off, v) __builtin_amdgcn_raw_buffer_store_b128((v), rs, (off), 0, 16)
#define ST64(off, v) __builtin_amdgcn_raw_buffer_store_b64((v), rs, (off), 0, 16)
#define ST32(off, v) __builtin_amdgcn_raw_buffer_store_b32((v), rs, (off), 0, 16)
#define MFMA(a, b, c) __builtin_amdgcn_mfma_f32_16x16x32_bf16(__builtin_bit_cast(bf16x8, a), __builtin_bit_cast(bf16x8, b), c, 0, 0, 0)
#define MFMA0(d, a, b) asm volatile("v_mfma_f32_16x16x32_bf16 %0, %1, %2, 0" : "=&v"(d) : "v"(a), "v"(b))
#define MFMAC(d, a, b) asm volatile("v_mfma_f32_16x16x32_bf16 %0, %1, %2, %0" : "+v"(d) : "v"(a), "v"(b))
static constexpr int SPIN_CAP = 1 << 18;
static constexpr int CODE0_BYTES = 52224;

static constexpr unsigned WS_EPOCH = 0;
static constexpr unsigned WS_FP2 = 196608;
static constexpr unsigned WS_FP1 = 4096;
static constexpr unsigned WS_FW = WS_FP1 + 16384;
static constexpr unsigned WS_FD = WS_FW + 40960;
static constexpr unsigned WS_MOD = 786432;
static constexpr unsigned WS_STAT = 131072 + 16384;
static constexpr unsigned WS_PART = 262144;
static constexpr unsigned WS_KVT = 1u << 20;
static constexpr unsigned WS_QT = WS_KVT + (3u << 20);
static constexpr unsigned WS_CK = 2686976;
static constexpr unsigned WS_X1T = WS_QT + (3u << 20);
static constexpr unsigned WS_WP = WS_X1T + (3u << 20);
static constexpr unsigned WS_WD = WS_WP + (4u << 20);
static constexpr unsigned WS_HH = WS_WD + (2u << 20);
static constexpr unsigned WS_HC = WS_HH + (6u << 20);
static constexpr unsigned WS_END = WS_HC + (1u << 20);

static constexpr int KCP = 4160, KCH = 4 * KCP;
static constexpr int XR_CH = 7680;
static constexpr int L_XI = 3 * KCH;
static constexpr int L_TR = 72960;
static constexpr int TRW = 11008;
static constexpr int L_EX = L_TR;
static constexpr int L_Q = L_TR + 4 * 9216;
static constexpr int L_GW = L_Q + 28672;
static constexpr int L_GS = L_GW + 20480;
static constexpr int L_FL = L_GS + 1536;
static constexpr int L_KS = L_Q + 256;
static constexpr int L_VS = L_KS + 17 * 656;
static constexpr int L_WK = L_VS + 16 * 656;
static constexpr int L_CR = L_WK + 640;
static constexpr int L_HX = 131072;
static constexpr int L_XR = L_HX + 12288;
static constexpr int L_DJ = L_XR;
static constexpr int L_CB = L_XR + 12288;
static constexpr int L_RS = L_CB + 1536;
static constexpr int L_GB = L_RS + 512;
static constexpr int LDS_TOTAL = L_FL + 128 + 160;

__device__ __forceinline__ int kpos(int d) {
  const int g = (d * 205) >> 10, m = d - 5 * g;
  return m == 2 ? g : 128 + 2 * g + (m >= 3) + ((m == 1 || m == 4) ? 256 : 0);
}
__device__ __forceinline__ int kchn(int ks, int r) { return (ks < 4 ? 160 * ks + 2 : 80 * ((ks - 4) & 7) + (ks >= 12)) + (((ks < 4 ? 10 : 5) * r + 1) >> 1); }
__device__ __forceinline__ u32x4 cat2(u32x2 a, u32x2 b) { return __builtin_shufflevector(a, b, 0, 1, 2, 3); }

__global__ __launch_bounds__(256, 1) void mega(const u16* qin_, const u16* kin_, const u16* vin_, const u16* shiftk_, const u16* ssmk_,
                                               const u16* ssmD_, const u16* wg_, const u16* wu_, const u16* wdn_, const u16* cin_,
                                               const u16* wmod_, char* ws_, u16* out_) {
  u32x16 ka;
  u32x8 kb;
  u32x2 kc;
  asm volatile("s_load_dwordx16 %0, %3, 0x0\n\ts_load_dwordx8 %1, %3, 0x40\n\ts_load_dwordx2 %2, %3, 0x60"
               : "=&s"(ka), "=&s"(kb), "=&s"(kc) : "s"(__builtin_amdgcn_kernarg_segment_ptr()));
  SCHEDB();
  __shared__ __attribute__((aligned(16))) char lds_[LDS_TOTAL];
  lds_char* lds = (lds_char*)lds_;
  const unsigned lbase = (unsigned)(size_t)lds_;
  const int tid = threadIdx.x, lane = tid & 63;
  int w = __builtin_amdgcn_readfirstlane(tid >> 6), b = blockIdx.x;
  const u64 t0_ = wall_clock64(); u64 tk_ = t0_;
  const u32x4 FILL = {~0u, ~0u, ~0u, ~0u};
  const u32x2 SET = {1u, 1u};
  if (w == 3 && b < (CODE0_BYTES + 4095) / 4096 * 8) {
    const size_t pc = __builtin_amdgcn_s_getpc() & ~255ull;
    unsigned keep;
    asm volatile("s_mov_b32 %0, m0\n\ts_mov_b32 m0, %3\n\ts_nop 0\n\tglobal_load_lds_dword %1, %2\n\ts_mov_b32 m0, %0"
                 : "=&s"(keep) : "v"(umin_(((b >> 3) * 64 + lane) * 64, CODE0_BYTES - 64)), "s"(pc), "s"(lbase + L_Q) : "memory");
  }
#define GLD128(d, vo, sb, off) asm volatile("global_load_dwordx4 %0, %1, %2 offset:%3" : "=v"(d) : "v"(vo), "s"(sb), "i"(off) : "memory")
  u64 T64;
  unsigned vK, vV, v9, o3;
  {
    const int xcd = b & 7, idx = b >> 3;
    const int r10 = (lane * 26) >> 8;
    vV = (unsigned)((xcd * 4 + (idx >> 3)) * 64 + 4 * w + r10) * (D_ * 2) + (idx & 7) * 160 + (lane - 10 * r10) * 16;
    vK = vV - 4 * D_ * 2;
    const int g = idx < 30 ? xcd * 30 + idx : 0, gq = g & 3, gcol = (g >> 2) * 64;
    v9 = w == 0 ? vV + 60 * D_ * 2 : (w == 1 ? (idx & 7) * 640 + lane * 16 : gq * 320 + (lane < 19 ? lane : 19) * 16);
    const int r = lane >> 3, c = lane & 7;
    o3 = (gq * 160 + w * 40 + r) * (NMOD * 2) + ((((c >> 1) ^ ((r >> 1) & 3)) << 1) + (c & 1)) * 16 + gcol * 2;
    asm volatile("" : "+v"(vK), "+v"(vV), "+v"(v9), "+v"(o3));
  }
  asm volatile("s_waitcnt lgkmcnt(0)" : "+s"(w), "+s"(ka), "+s"(kb), "+s"(kc));
#define KARG(v, i) (((u64)v[2 * (i) + 1] << 32) | v[2 * (i)])
  const GAS u16 *qin = (const GAS u16*)KARG(ka, 0), *kin = (const GAS u16*)KARG(ka, 1), *vin = (const GAS u16*)KARG(ka, 2);
  const GAS u16 *shiftk = (const GAS u16*)KARG(ka, 3), *ssmk = (const GAS u16*)KARG(ka, 4), *ssmD = (const GAS u16*)KARG(ka, 5);
  const GAS u16 *wg = (const GAS u16*)KARG(ka, 6), *wu = (const GAS u16*)KARG(ka, 7), *wdn = (const GAS u16*)KARG(kb, 0);
  const GAS u16 *cin = (const GAS u16*)KARG(kb, 1), *wmod = (const GAS u16*)KARG(kb, 2);
  GAS char* ws = (GAS char*)KARG(kb, 3);
  GAS u16* out = (GAS u16*)KARG(kc, 0);
  asm volatile("s_load_dwordx2 %0, %1, 0x0" : "=&s"(T64) : "s"(ws) : "memory");
  {
    const u32x4 rk = {ka[2], ka[3] & 0xFFFFu, (unsigned)(S_ * D_ * 2), 0x00020000u}, rv = {ka[4], ka[5] & 0xFFFFu, (unsigned)(S_ * D_ * 2), 0x00020000u};
    asm volatile(
      "s_mov_b32 s45, m0\n\t"
      "s_mov_b32 m0, %6\n\t"
      "s_mov_b32 exec_hi, 0xff\n\t"
      "buffer_load_dwordx4 %0, %3, 0 offen lds\n\t"
      "s_add_u32 m0, m0, 2624\n\t"
      "s_mov_b32 s44, 15360\n\t"
      "buffer_load_dwordx4 %1, %3, s44 offen lds\n\t"
      "s_add_u32 m0, m0, 2624\n\t"
      "s_mov_b32 s44, 35840\n\t"
      "buffer_load_dwordx4 %1, %3, s44 offen lds\n\t"
      "s_add_u32 m0, m0, 2624\n\t"
      "s_mov_b32 s44, 56320\n\t"
      "buffer_load_dwordx4 %1, %3, s44 offen lds\n\t"
      "s_mov_b32 m0, %7\n\t"
      "s_nop 0\n\t"
      "global_load_lds_dwordx4 %2, %5\n\t"
      "s_add_u32 m0, %6, 11152\n\t"
      "s_nop 0\n\t"
      "buffer_load_dwordx4 %1, %4, 0 offen lds\n\t"
      "s_add_u32 m0, m0, 2624\n\t"
      "s_mov_b32 s44, 20480\n\t"
      "buffer_load_dwordx4 %1, %4, s44 offen lds\n\t"
      "s_add_u32 m0, m0, 2624\n\t"
      "s_mov_b32 s44, 40960\n\t"
      "buffer_load_dwordx4 %1, %4, s44 offen lds\n\t"
      "s_add_u32 m0, m0, 2624\n\t"
      "s_mov_b32 s44, 61440\n\t"
      "buffer_load_dwordx4 %1, %4, s44 offen lds\n\t"
      "s_mov_b32 exec_hi, -1\n\t"
      "s_mov_b32 m0, %11\n\t"
      "s_mov_b32 s42, %9\n\t"
      "s_mov_b32 s43, %10\n\t"
      "global_load_lds_dwordx4 %8, s[42:43]\n\t"
      "s_add_u32 m0, m0, 1024\n\t"
      "s_add_u32 s42, s42, 61440\n\t"
      "s_addc_u32 s43, s43, 0\n\t"
      "global_load_lds_dwordx4 %8, s[42:43]\n\t"
      "s_add_u32 m0, m0, 1024\n\t"
      "s_add_u32 s42, s42, 61440\n\t"
      "s_addc_u32 s43, s43, 0\n\t"
      "global_load_lds_dwordx4 %8, s[42:43]\n\t"
      "s_add_u32 m0, m0, 1024\n\t"
      "s_add_u32 s42, s42, 61440\n\t"
      "s_addc_u32 s43, s43, 0\n\t"
      "global_load_lds_dwordx4 %8, s[42:43]\n\t"
      "s_add_u32 m0, m0, 1024\n\t"
      "s_add_u32 s42, s42, 61440\n\t"
      "s_addc_u32 s43, s43, 0\n\t"
      "global_load_lds_dwordx4 %8, s[42:43]\n\t"
      "s_mov_b32 m0, s45\n\t"
        :: "v"(vK), "v"(vV), "v"(v9), "s"(rk), "s"(rv), "s"(w == 0 ? kin : (w == 1 ? shiftk : cin)), "s"(lbase + L_KS + w * 656),
           "s"(lbase + (w == 0 ? L_KS + 16 * 656 : (w == 1 ? L_WK : L_CR))), "v"(o3), "s"(kb[4]), "s"(kb[5]), "s"(lbase + L_GW + w * 5120)
        : "memory", "scc", "s42", "s43", "s44", "s45");
  }
  asm volatile("" : "+s"(w), "+s"(b));
  const int xcd = b & 7, idx = b >> 3;
  const __amdgpu_buffer_rsrc_t rs = __builtin_amdgcn_make_buffer_rsrc((void*)(size_t)ws, 0, WS_END, 0x00020000);
  const int L = xcd * 32 + idx;
  const int ct = L & 7, rt = L >> 3;
  const unsigned qto = WS_QT + rt * 128;
  const int srow = xcd * 256 + idx * 8;
  const int side = L & 1, ch0 = 5 * (L >> 1) + 2 * side;
  const int ip = lane & 15, fq = lane >> 4, q4 = ip >> 2, p4 = lane & 3;
  const int chA = ch0 + side, chH = ch0 + 2 - 2 * side;
  const int rowH = 512 * (side + (3 - 2 * side) * (w >> 1)) + 256 * (w & 1) + 4 * lane;
  const int rowA = 512 * w + 8 * lane;
  const int slot = w * 32 + idx;
  const bool jobW = slot < 80;
  const int j_ks = 5 * (xcd >> 1) + (jobW ? slot >> 4 : 0), j_bn = 8 * (xcd & 1) + ((slot & 15) >> 1), j_h = slot & 1;
  const int d_ks = (L * 205) >> 10, d_cb = 4 * (L - 5 * d_ks) + w;
  const int bm = (xcd >> 2) * 8 + (idx >> 2), bn = (xcd & 3) * 4 + (idx & 3);
  const int bm2 = (xcd >> 1) * 8 + (idx >> 2), bn2 = (xcd & 1) * 4 + (idx & 3);
  u32x4 fd[3];
  u32x2 fp[3], T;
#define SLD128(d, vo, off) asm volatile("global_load_dwordx4 %0, %1, %2 offset:%3 sc1" : "=v"(d) : "v"(vo), "s"(ws), "i"(off) : "memory")
  auto filter_loads = [&]() __attribute__((always_inline)) {
    const GAS u16* src = ssmk + (size_t)ch0 * S_ + S_;
    GLD128(fd[0], tid * 16, src, -4096); GLD128(fd[1], tid * 16, src, 0);
    GLD128(fd[2], tid * 16, src + S_, 0);
  };
  u32x2 dA;
  unsigned dH;
  {
    const GAS u16* src = ssmk + (size_t)ch0 * S_ + (w ? 512 * w - 4 : 0);
    asm volatile("s_load_dwordx2 %0, %5, 0x0\n\ts_load_dwordx2 %1, %5, 0x1000\n\ts_load_dwordx2 %2, %5, 0x2000\n\t"
                 "s_load_dwordx2 %3, %6, %7\n\ts_load_dword %4, %6, %8"
                 : "=&s"(fp[0]), "=&s"(fp[1]), "=&s"(fp[2]), "=&s"(dA), "=&s"(dH)
                 : "s"(src), "s"(ssmD), "s"((chA < 638 ? chA & ~1 : 636) * 2), "s"((chH & ~1) * 2) : "memory");
  }
#define FILT_REGS "+v"(fd[0]), "+v"(fd[1]), "+v"(fd[2])
  auto filter_copies = [&]() __attribute__((always_inline)) {
#pragma unroll
  for (int i = 0; i < 3; ++i) {
    const unsigned e0 = fd[i][0], e1 = fd[i][1], e2 = fd[i][2], e3 = fd[i][3];
    const unsigned p2 = __builtin_amdgcn_update_dpp(w ? fp[i][0] : 0u, e2, 0x138, 0xf, 0xf, false);
    const unsigned p3 = __builtin_amdgcn_update_dpp(w ? fp[i][1] : 0u, e3, 0x138, 0xf, 0xf, false);
    const unsigned A0 = __builtin_amdgcn_alignbit(p3, p2, 16), A1 = __builtin_amdgcn_alignbit(e0, p3, 16);
    const unsigned A2 = __builtin_amdgcn_alignbit(e1, e0, 16), A3 = __builtin_amdgcn_alignbit(e2, e1, 16);
    const unsigned A4 = __builtin_amdgcn_alignbit(e3, e2, 16);
    lds_char* base = lds + i * KCH + 16 * tid + 56;
    LDS64(base + 0 * KCP) = (u32x2){A0, A1};
    LDS64(base + 1 * KCP) = (u32x2){p3, e0};
    LDS64(base + 2 * KCP) = (u32x2){A1, A2};
    LDS64(base + 0 * KCP + 8) = (u32x2){A2, A3};
    LDS64(base + 1 * KCP + 8) = (u32x2){e1, e2};
    LDS64(base + 2 * KCP + 8) = (u32x2){A3, A4};
    LDS64(base + 3 * KCP) = (u32x2){e0, e1};
    LDS64(base + 3 * KCP + 8) = (u32x2){e2, e3};
    if (tid == 255) {
      LDS64(base + 0 * KCP + 16) = (u32x2){A4, e3 >> 16};
      LDS64(base + 1 * KCP + 16) = (u32x2){e3, 0u};
      LDS64(base + 2 * KCP + 16) = (u32x2){e3 >> 16, 0u};
    }
  }
  SCHEDB();
  };
  unsigned kva = lbase + L_KS + (lane >> 2) * 656 + 160 + (12 * w + (lane & 3)) * 4, vva = kva + (L_VS - L_KS - 160);
  unsigned wka = lbase + L_WK + (12 * w + (lane & 3)) * 16;
  lds_char* tk = lds + L_TR + w * TRW + (lane & 3) * 288 + (30 - 2 * (lane >> 2)) * 4;
  struct KvOps { u32x2 k[4], v[2]; u32x4 wk; };
#define KV_READ(o, j)                                                                                                          \
  asm volatile("ds_read_b128 %6, %9 offset:%c10\n\t"                                                                            \
               "ds_read2_b32 %0, %7 offset0:%c11 offset1:%c11+40\n\tds_read2_b32 %1, %7 offset0:%c11+80 offset1:%c11+124\n\t"     \
               "ds_read2_b32 %2, %7 offset0:%c11+164 offset1:%c11+204\n\tds_read2_b32 %3, %7 offset0:%c11+244 offset1:%c11+244\n\t" \
               "ds_read2_b32 %4, %8 offset0:%c11 offset1:%c11+40\n\tds_read2_b32 %5, %8 offset0:%c11+80 offset1:%c11+120"         \
               : "=&v"(o.k[0]), "=&v"(o.k[1]), "=&v"(o.k[2]), "=&v"(o.k[3]), "=&v"(o.v[0]), "=&v"(o.v[1]), "=&v"(o.wk)            \
               : "v"(kva), "v"(vva), "v"(wka), "i"(j * 64), "i"(j * 4) : "memory")
#define KV_REGS(o) "+v"(o.k[0]), "+v"(o.k[1]), "+v"(o.k[2]), "+v"(o.k[3]), "+v"(o.v[0]), "+v"(o.v[1]), "+v"(o.wk)
  auto kv_math = [&](const KvOps& o, int j) __attribute__((always_inline)) {
    f32x2 kf[7], y[4];
#pragma unroll
    for (int i = 0; i < 7; ++i) kf[i] = up2(o.k[i >> 1][i & 1]);
    const f32x2 w0 = {lo16(o.wk[0]), lo16(o.wk[2])}, w1 = {hi16(o.wk[0]), hi16(o.wk[2])};
    const f32x2 w2 = {lo16(o.wk[1]), lo16(o.wk[3])}, w3 = {hi16(o.wk[1]), hi16(o.wk[3])};
#pragma unroll
    for (int r = 0; r < 4; ++r) {
      f32x2 a = rb2(kf[r] * w3);
      a = rb2(a + rb2(kf[r + 1] * w2));
      a = rb2(a + rb2(kf[r + 2] * w1));
      a = rb2(a + rb2(kf[r + 3] * w0));
      y[r] = a * up2(o.v[r >> 1][r & 1]);
    }
#pragma unroll
    for (int c = 0; c < 2; ++c)
      LDS64(tk + (8 * j + c) * 144) = (u32x2){pk2(y[3][c], y[2][c]), pk2(y[1][c], y[0][c])};
  };
  const unsigned kvs = WS_KVT + (1984 - 64 * rt) * 2 + (unsigned)(ct * 80 + 24 * w + (lane >> 3)) * 4096 + (lane & 7) * 16;
  lds_char* kvl = lds + L_TR + w * TRW + (lane >> 3) * 144 + (lane & 7) * 16;
  unsigned wo[3];
#pragma unroll
  for (int i = 0; i < 3; ++i) {
    const int e = lane + 64 * i, r = (e * 171) >> 10;
    wo[i] = (unsigned)kchn(j_ks, r) * (F_ * 2) + (96 * j_bn + 48 * j_h) * 2 + (e - 6 * r) * 16;
  }
  const int cl = w < 2 ? (lane & 3) : (lane & 1);
  unsigned vA = (unsigned)(rt * 64 + (lane & 60)) * (D_ * 2) + (ct * 80 + (4 * w + cl) * 8) * 2;
  unsigned tro = qto + (unsigned)(ct * 80 + 32 * w + (lane >> 3)) * 4096 + (lane & 7) * 16;
  lds_char* tq = lds + L_TR + w * TRW + cl * 8 * 144 + ((2 * (lane >> 2)) ^ (4 * cl)) * 4;
  LDS32(lds + L_FL + 4 * w) = 0u;
  asm volatile("" : "+v"(tk), "+v"(kvl), "+v"(kva), "+v"(vva), "+v"(wka), "+v"(wo[0]), "+v"(wo[1]), "+v"(wo[2]), "+v"(vA), "+v"(tro), "+v"(tq));
  unsigned x1o[3], pmo, qoA, qoH, soA, soH, fqo, fso;
  u32x16 ia;
  u32x8 ib;
  auto conv_addr = [&]() __attribute__((always_inline)) {
#pragma unroll
    for (int it = 0; it < 3; ++it) {
      const int k = it < 2 ? 128 + 256 * it + L : L >> 1, r = it < 2 ? rowA : rowH;
      x1o[it] = WS_X1T + (unsigned)((r >> 6) * D_ + k) * 128 + (((r >> 4) ^ (k >> 1)) & 3) * 32 + (r & 15) * 2;
    }
    pmo = WS_MOD + ((lane & 8) ? chH : chA + ((lane >> 2) & 1)) * 32 + ((lane & 3) < 3 ? (lane & 3) : 0) * 20480;
    qoA = WS_QT + (unsigned)chA * 4096 + rowA * 2;
    qoH = WS_QT + (unsigned)chH * 4096 + rowH * 2;
    soA = WS_STAT + w * 4096 + lane * 16;
    soH = WS_STAT + (rowH >> 9) * 4096 + ((rowH >> 2) & 1) * 2048 + ((rowH >> 3) & 63) * 16;
    fqo = WS_QT + WS_CK + ((rowA >> 6) * D_ + chA) * 4;
    fso = WS_QT + WS_CK + ((rowH >> 6) * D_ + chH) * 4;
    const unsigned fo = lbase + (ip & 3) * KCP + 2 * ((ip & 12) + 8 * (fq & 1) + 256 * (fq >> 1));
    const unsigned go = lbase + L_XI + (15 - ip + 8 * (fq >> 1)) * 96 + 16 * (fq & 1);
    const int lcA = side + (w >> 1), lcH = 2 - 2 * side, uA = 2040 - rowA, uH = 2044 - rowH;
    ia[0] = fo + lcA * KCH + (w & 1) * 256;
    ia[1] = go + lcA * XR_CH + (w & 1) * 384;
    ia[2] = fo + lcH * KCH + w * 128;
    ia[3] = go + lcH * XR_CH + w * 192;
    ia[4] = lbase + L_EX + w * 8192 + ip * 128 + (fq ^ (ip & 7)) * 16;
    ia[5] = lbase + L_EX + 32768 + w * 1024 + (lane >> 3) * 128 + ((lane & 7) ^ (lane >> 3)) * 16;
    ia[6] = lbase + L_XI + side * XR_CH + (uA >> 5) * 96 + (uA & 31) * 2;
    ia[7] = lbase + L_XI + lcH * XR_CH + (uH >> 5) * 96 + (uH & 31) * 2;
    ia[8] = WS_FW + xcd * 5120 + (bn * 20 + (lane < 20 ? lane : 19)) * 16;
    ia[9] = WS_FP2 + xcd * 8192 + w * 2048 + lane * 16;
    ia[10] = 0;
    ia[11] = WS_FP2 + (lane & 7) * 8192 + (b * 4 + w) * 8;
    ia[12] = ia[13] = 0;
    ia[14] = qoA + 2048; ia[15] = qoH;
    ib[0] = soA; ib[1] = soH; ib[2] = pmo; ib[3] = ia[4] ^ 64;
    ib[4] = fqo; ib[5] = fso;
    ib[6] = jobW ? WS_FW + (lane & 7) * 5120 + ((j_bn * 20 + j_ks) * 2 + j_h) * 8 : WS_FP1 + 8192 + (b * 4 + w) * 8;
    const int ks3 = (d_ks * 43) >> 7, c6 = 3 * ((lane >> 3) & 1) + d_ks - 3 * ks3;
    ib[7] = L < 240 ? (unsigned)(96 * ks3 + 16 * (c6 >> 1) + 4 * (lane >> 4) + 2 * (c6 & 1) + ((lane >> 2) & 1)) * (D_ * 2) + d_cb * 64 + (lane & 3) * 16 : 0u;
    const int e = tid < 84 ? tid : 83, e7 = (e * 37) >> 8;
    LDS64(lds + e7 * KCP + (e - 7 * e7) * 8) = (u32x2){0u, 0u};
#pragma unroll
    for (int i = 0; i < 3; ++i) LDS128(lds + L_XI + i * XR_CH + 64 * 96 + (tid < 90 ? tid : 89) * 16) = (u32x4){0u, 0u, 0u, 0u};
    asm volatile("" : "+v"(ia), "+v"(ib));
  };
  unsigned mo = WS_MOD + (1920 + 640 * (lane >> 5) + kchn(j_ks, lane & 31)) * 32;
  unsigned cko = (WS_KVT + WS_CK + ((31 - rt) * D_ + ct * 80 + 24 * w + 8 * (lane & 7) + (lane >> 3)) * 4) | (((w < 3 ? 2 : 0) - (lane & 7)) & 0x80000000u);
  unsigned cqo = (WS_QT + WS_CK + (rt * D_ + ct * 80 + 32 * w + 8 * (lane & 7) + (lane >> 3)) * 4) | (((w < 2 ? 3 : 1) - (lane & 7)) & 0x80000000u);
  u32x4 xv[3];
  u32x3 xc;
  unsigned xo = WS_KVT + (unsigned)ch0 * 4096 + 4096 + tid * 16, xco = WS_KVT + WS_CK + ((tid >> 3) * D_ + ch0) * 4, sh = (lane & 7) * 4;
  asm volatile("" : "+v"(mo), "+v"(cko), "+v"(cqo), "+v"(xo), "+v"(xco), "+v"(sh));
  SCHEDB();
  asm volatile("s_waitcnt vmcnt(5) lgkmcnt(0)\n\ts_barrier" : "+s"(T64), "+s"(fp[0]), "+s"(fp[1]), "+s"(fp[2]), "+s"(dA), "+s"(dH)::"memory");
  T64 += 1;
  T = (u32x2){(unsigned)T64, (unsigned)(T64 >> 32)};
  const unsigned wslo = kb[6], wshi = kb[7];
  auto x_request = [&]() __attribute__((always_inline)) {
    SLD128(xv[0], xo, -4096); SLD128(xv[1], xo, 0);
    SLD128(xv[2], xo + 4096, 0);
    asm volatile("global_load_dwordx3 %0, %1, %2 sc1" : "=v"(xc) : "v"(xco), "s"(ws) : "memory");
  };
  auto x_finish = [&]() __attribute__((always_inline)) {
    auto x_ok = [&]() __attribute__((always_inline)) -> bool {
      return __all(((cks(xv[0], sh) ^ xc[0] ^ T[0]) | (cks(xv[1], sh) ^ xc[1] ^ T[0]) | (cks(xv[2], sh) ^ xc[2] ^ T[0])) == 0);
    };
    if (__builtin_expect(!x_ok(), 0)) {
#pragma unroll 1
      for (int sp = 0; sp < SPIN_CAP; ++sp) {
        asm volatile("" ::: "memory");
#pragma unroll
        for (int i = 0; i < 3; ++i) { xv[i] = LD128(xo + i * 4096 - 4096); xc[i] = LD32(xco + 4 * i); }
        if (x_ok()) break;
      }
    }
#pragma unroll
    for (int i = 0; i < 3; ++i) LDS128(lds + L_XI + i * XR_CH + (tid >> 2) * 96 + (tid & 3) * 16) = xv[i];
  };
  auto x1_fill = [&]() __attribute__((always_inline)) {
    ST128(x1o[0], FILL); ST128(x1o[1], FILL);
    ST64(x1o[2], ((u32x2){~0u, ~0u}));
  };
#define X_REGS "+v"(xv[0]), "+v"(xv[1]), "+v"(xv[2]), "+v"(xc)
  if (__builtin_expect(w < 3, 1)) {
    u32x4 qr[4];
    KvOps o0_, o1_, o2_;
    KV_READ(o0_, 0);
    KV_READ(o1_, 1);
    filter_loads();
#pragma unroll
    for (int i = 0; i < 4; ++i) GLD128(qr[i], vA, qin, i * D_ * 2);
    asm volatile("s_waitcnt lgkmcnt(7)" : KV_REGS(o0_)::"memory");
    KV_READ(o2_, 2);
    kv_math(o0_, 0);
    SCHEDB();
    asm volatile("s_waitcnt lgkmcnt(0)" : KV_REGS(o1_), KV_REGS(o2_)::"memory");
    kv_math(o1_, 1);
    SCHEDB();
    asm volatile("s_waitcnt vmcnt(7)" : KV_REGS(o2_)::"memory");
    LDS32(lds + L_FL + 4 * w) = 1u;
    asm volatile("" : KV_REGS(o2_)::"memory");
    SCHEDB();
    kv_math(o2_, 2);
    SCHEDB();
    WAITL();
    {
      unsigned c[3];
#pragma unroll
      for (int i = 0; i < 3; ++i) {
        const u32x4 d = LDS128(kvl + i * 8 * 144);
        ST128(kvs + i * 32768, d);
        c[i] = cks(d, sh);
      }
      ST32(cko, ((lane & 2) ? c[2] : ((lane & 1) ? c[1] : c[0])) ^ T[0]);
    }
    SCHEDB();
#pragma unroll 1
    for (int sp = 0; sp < 64; ++sp) {
      unsigned f;
      asm volatile("ds_read_b32 %0, %1\n\ts_waitcnt lgkmcnt(0)" : "=v"(f) : "v"(lbase + L_FL + 12) : "memory");
      if (__all(f == 1u)) break;
    }
    {
      unsigned keep;
      asm volatile("s_mov_b32 %0, m0\n\ts_mov_b32 m0, %6\n\ts_nop 0\n\tglobal_load_lds_dwordx4 %1, %4\n\tglobal_load_lds_dwordx4 %1, %5 offset:3072\n\t"
                   "s_add_u32 m0, m0, 1024\n\ts_nop 0\n\tglobal_load_lds_dwordx4 %2, %4\n\tglobal_load_lds_dwordx4 %2, %5 offset:3072\n\t"
                   "s_add_u32 m0, m0, 1024\n\ts_nop 0\n\tglobal_load_lds_dwordx4 %3, %4\n\tglobal_load_lds_dwordx4 %3, %5 offset:3072\n\ts_mov_b32 m0, %0"
                   : "=&s"(keep) : "v"(wo[0]), "v"(wo[1]), "v"(wo[2]), "s"(wg), "s"(wu - 1536), "s"(lbase + L_TR + w * TRW + 4608) : "memory", "scc");
    }
    asm volatile("s_waitcnt vmcnt(10)" : FILT_REGS, "+v"(qr[0]), "+v"(qr[1]), "+v"(qr[2]), "+v"(qr[3])::"memory");
    filter_copies();
#pragma unroll
    for (int j = 0; j < 4; ++j) {
      LDS64(tq + (2 * j) * 144) = (u32x2){__builtin_amdgcn_perm(qr[1][j], qr[0][j], 0x05040100u), __builtin_amdgcn_perm(qr[3][j], qr[2][j], 0x05040100u)};
      LDS64(tq + (2 * j + 1) * 144) = (u32x2){__builtin_amdgcn_perm(qr[1][j], qr[0][j], 0x07060302u), __builtin_amdgcn_perm(qr[3][j], qr[2][j], 0x07060302u)};
    }
    WAITL();
    unsigned qc;
    {
      unsigned c[4];
#pragma unroll
      for (int i = 0; i < 4; ++i) {
        qr[i] = LDS128(lds + L_TR + w * TRW + ((8 * i + (lane >> 3)) * 36 + 4 * ((lane & 7) ^ i)) * 4);
        c[i] = cks(qr[i], sh);
      }
      qc = ((lane & 2) ? ((lane & 1) ? c[3] : c[2]) : ((lane & 1) ? c[1] : c[0])) ^ T[0];
      asm volatile("" : "+v"(qc));
    }
    SCHEDB();
    u32x4 m0_, m1_;
    SLD128(m0_, mo, 0);
    SLD128(m1_, mo, 16);
    x_request();
    SCHEDB();
    unsigned wpo = WS_WP + (unsigned)((j_bn * 4 + 2 * j_h) * 20 + j_ks) * 3072 + lane * 16;
    unsigned pso = ip < 12 ? WS_PART + (unsigned)j_ks * 24576 + (ip >= 6 ? 12288 - 384 : 0) + (j_bn * 192 + j_h * 96 + ip * 16 + fq * 4) * 4 : 0x80000000u;
    if (!jobW) wpo = pso = 0x80000000u;
    conv_addr();
    asm volatile("" : "+v"(wpo), "+v"(pso));
    SCHEDB();
    u32x4 raw[6];
    {
      u32x2 r0[6], r1[6];
      const unsigned ta = lbase + L_TR + w * TRW + 4608 + (4 * fq + q4) * 96 + p4 * 8;
      WAITV(6);
#pragma unroll
      for (int c = 0; c < 6; ++c) { DSRTR(r0[c], ta, (c & 1) * 3072 + (c >> 1) * 32); DSRTR(r1[c], ta, (c & 1) * 3072 + 16 * 96 + (c >> 1) * 32); }
      asm volatile("s_waitcnt lgkmcnt(0)" : "+v"(r0[0]), "+v"(r0[1]), "+v"(r0[2]), "+v"(r0[3]), "+v"(r0[4]), "+v"(r0[5]),
                   "+v"(r1[0]), "+v"(r1[1]), "+v"(r1[2]), "+v"(r1[3]), "+v"(r1[4]), "+v"(r1[5])::"memory");
#pragma unroll
      for (int c = 0; c < 3; ++c) {
        unsigned x[4], y[4];
#define QP(d, s0, s1, p) "v_cndmask_b32_dpp %" #d ", %" #s0 ", %" #s1 ", vcc quad_perm:" p " row_mask:0xf bank_mask:0xf\n\t"
        asm volatile("s_mov_b32 vcc_lo, 0x33333333\n\ts_mov_b32 vcc_hi, 0x33333333\n\t"
                     QP(0, 12, 8, "[0,1,0,1]") QP(1, 13, 9, "[0,1,0,1]") QP(2, 14, 10, "[0,1,0,1]") QP(3, 15, 11, "[0,1,0,1]")
                     "s_not_b64 vcc, vcc\n\t"
                     QP(4, 8, 12, "[2,3,2,3]") QP(5, 9, 13, "[2,3,2,3]") QP(6, 10, 14, "[2,3,2,3]") QP(7, 11, 15, "[2,3,2,3]")
                     : "=&v"(x[0]), "=&v"(x[1]), "=&v"(x[2]), "=&v"(x[3]), "=&v"(y[0]), "=&v"(y[1]), "=&v"(y[2]), "=&v"(y[3])
                     : "v"(r0[2 * c][0]), "v"(r0[2 * c][1]), "v"(r1[2 * c][0]), "v"(r1[2 * c][1]), "v"(r0[2 * c + 1][0]), "v"(r0[2 * c + 1][1]),
                       "v"(r1[2 * c + 1][0]), "v"(r1[2 * c + 1][1])
                     : "vcc", "scc");
        raw[2 * c] = (u32x4){x[0], x[1], x[2], x[3]};
        raw[2 * c + 1] = (u32x4){y[0], y[1], y[2], y[3]};
      }
      asm volatile("" : "+v"(raw[0]), "+v"(raw[1]), "+v"(raw[2]), "+v"(raw[3]), "+v"(raw[4]), "+v"(raw[5]));
    }
    asm volatile("s_waitcnt vmcnt(0)" : "+v"(m0_), "+v"(m1_), X_REGS::"memory");
    x_finish();
    if (__builtin_expect(jobW && !__all(modx(m0_, m1_, T[0]) == 0), 0)) {
#pragma unroll 1
      for (int sp = 0; sp < SPIN_CAP; ++sp) {
        asm volatile("" ::: "memory");
        m0_ = LD128(mo);
        m1_ = LD128(mo + 16);
        if (__all(modx(m0_, m1_, T[0]) == 0)) break;
      }
      asm volatile("" : "+v"(m0_), "+v"(m1_));
    }
#pragma unroll
    for (int i = 0; i < 4; ++i) ST128((w < 2 || i < 2) ? tro + i * 32768 : 0x80000000u, qr[i]);
    ST32(cqo, qc);
    x1_fill();
    SCHEDB();
    {
      const unsigned scb = lbase + L_TR + w * TRW + 10752;
      *(__attribute__((address_space(3))) u16*)(lds + L_TR + w * TRW + 10752 + lane * 2) = (u16)pk2(modv(m0_, m1_) + (lane < 32 ? 0.f : 1.f), 0.f);
      WAITL();
      u32x2 sl, sh;
      unsigned lo, hi;
      asm volatile("ds_read_b64 %0, %4\n\tds_read_b64 %1, %4 offset:32\n\tds_read_b32 %2, %5 offset:64\n\tds_read_b32 %3, %5 offset:96\n\ts_waitcnt lgkmcnt(0)"
                   : "=&v"(sl), "=&v"(sh), "=&v"(lo), "=&v"(hi) : "v"(scb + fq * 8), "v"(scb + (ip >> 1) * 4) : "memory");
      const unsigned msk = fq == q4 ? ((ip & 1) ? 0xFFFF0000u : 0xFFFFu) : 0u, mk0 = (ip & 2) ? 0u : msk, mk1 = (ip & 2) ? msk : 0u;
      u32x4 dg0 = {lo & mk0, lo & mk1, 0u, 0u}, dg1 = {0u, 0u, hi & mk0, hi & mk1};
      const u32x4 sbf = cat2(sl, sh);
      asm volatile("s_nop 1" : "+v"(dg0), "+v"(dg1));
      f32x4 a0[6], a1[6], ps;
#pragma unroll
      for (int c = 0; c < 6; ++c) { MFMA0(a0[c], dg0, raw[c]); MFMA0(a1[c], dg1, raw[c]); }
      u32x4 sel[6], wp[6];
#pragma unroll
      for (int c = 0; c < 6; ++c) {
        const bool m = ip == 6 + c;
        sel[c] = (u32x4){m ? sbf[0] : 0u, m ? sbf[1] : 0u, m ? sbf[2] : 0u, m ? sbf[3] : 0u};
      }
      asm volatile("s_nop 1" : "+v"(sel[0]), "+v"(sel[1]), "+v"(sel[2]), "+v"(sel[3]), "+v"(sel[4]), "+v"(sel[5]));
      MFMA0(ps, raw[0], sel[0]);
#pragma unroll
      for (int c = 1; c < 6; ++c) MFMAC(ps, raw[c], sel[c]);
      asm volatile("" : "+v"(a0[0]), "+v"(a0[1]), "+v"(a0[2]), "+v"(a0[3]), "+v"(a0[4]), "+v"(a0[5]), "+v"(a1[0]), "+v"(a1[1]),
                   "+v"(a1[2]), "+v"(a1[3]), "+v"(a1[4]), "+v"(a1[5]));
#pragma unroll
      for (int c = 0; c < 6; ++c) {
        wp[c] = (u32x4){pk2(a0[c][0], a0[c][1]), pk2(a0[c][2], a0[c][3]), pk2(a1[c][0], a1[c][1]), pk2(a1[c][2], a1[c][3])};
        const unsigned o1 = ip == c ? 0x3F803F80u : 0u;
        sel[c] = (u32x4){o1, o1, o1, o1};
      }
      asm volatile("s_nop 1" : "+v"(wp[0]), "+v"(wp[1]), "+v"(wp[2]), "+v"(wp[3]), "+v"(wp[4]), "+v"(wp[5]), "+v"(sel[0]), "+v"(sel[1]),
                   "+v"(sel[2]), "+v"(sel[3]), "+v"(sel[4]), "+v"(sel[5]));
#pragma unroll
      for (int c = 0; c < 6; ++c) MFMAC(ps, wp[c], sel[c]);
#pragma unroll
      for (int c = 0; c < 6; ++c) ST128(wpo + (c / 3) * 61440 + (c % 3) * 1024, wp[c]);
      asm volatile("s_nop 7" : "+v"(ps));
      ST128(pso, __builtin_bit_cast(u32x4, ps));
      SCHEDB();
    }
  } else {
    u32x4 sx[10];
    KvOps o0_;
    KV_READ(o0_, 0);
    filter_loads();
#pragma unroll
    for (int i = 0; i < 10; ++i) GLD128(sx[i], (lane >> 3) * 1280 + (lane & 7) * 16, qin + (size_t)srow * D_, i * 128);
    const u32x2 cv = LDS64(lds + L_CR + (lane < 40 ? lane : 39) * 8);
    asm volatile("s_waitcnt lgkmcnt(0)" : KV_REGS(o0_)::"memory");
    kv_math(o0_, 0);
    WAITL();
    u32x4 kvd = LDS128(kvl);
    unsigned kvc = cks(kvd, sh) ^ T[0];
    asm volatile("" : "+v"(kvd), "+v"(kvc));
    SCHEDB();
    {
      const f32x2 c0 = up2(cv[0]), c1 = up2(cv[1]);
      LDS64(lds + L_GS + (lane < 40 ? lane : 39) * 8) =
          (u32x2){pk2(c0[0] / (1.0f + __expf(-c0[0])), c0[1] / (1.0f + __expf(-c0[1]))),
                  pk2(c1[0] / (1.0f + __expf(-c1[0])), c1[1] / (1.0f + __expf(-c1[1])))};
    }
    WAITL();
    SCHEDB();
    {
      const int r8 = 4 * fq + q4;
      const unsigned ta = lbase + L_GW + r8 * 128 + p4 * 8, sa = lbase + L_GS + fq * 8;
      u32x2 a[10], r[4][10];
#pragma unroll
      for (int i = 0; i < 10; ++i) asm volatile("ds_read_b64 %0, %1 offset:%2" : "=v"(a[i]) : "v"(sa), "i"(i * 32));
      unsigned mdo = idx < 30 ? WS_MOD + ((xcd * 30 + idx) >> 2) * 2048 + ((xcd * 30 + idx) & 3) * 8 + lane * 32 : 0x80000000u;
      asm volatile("" : "+v"(mdo));
      SCHEDB();
      WAITV(13);
#pragma unroll 1
      for (int sp = 0; sp < SPIN_CAP; ++sp) {
        unsigned f;
        asm volatile("ds_read_b32 %0, %1\n\ts_waitcnt lgkmcnt(0)" : "=v"(f) : "v"(lbase + L_FL + ((lane & 3) < 3 ? 4 * (lane & 3) : 0)) : "memory");
        if (__all(f == 1u)) break;
      }
      SCHEDB();
#pragma unroll
      for (int n = 0; n < 4; ++n) {
        const unsigned tb = ta + ((n ^ ((r8 >> 1) & 3)) * 32);
#pragma unroll
        for (int i = 0; i < 10; ++i) DSRTR(r[n][i], tb, i * 2048);
      }
      asm volatile("s_waitcnt lgkmcnt(0)"
                   : "+v"(a[0]), "+v"(a[1]), "+v"(a[2]), "+v"(a[3]), "+v"(a[4]), "+v"(a[5]), "+v"(a[6]), "+v"(a[7]), "+v"(a[8]), "+v"(a[9])::"memory");
      f32x4 acc[4];
#pragma unroll
      for (int n = 0; n < 4; ++n) {
        asm volatile("" : "+v"(r[n][0]), "+v"(r[n][1]), "+v"(r[n][2]), "+v"(r[n][3]), "+v"(r[n][4]), "+v"(r[n][5]), "+v"(r[n][6]),
                     "+v"(r[n][7]), "+v"(r[n][8]), "+v"(r[n][9])::"memory");
        MFMA0(acc[n], cat2(a[0], a[1]), cat2(r[n][0], r[n][1]));
#pragma unroll
        for (int i = 1; i < 5; ++i) MFMAC(acc[n], cat2(a[2 * i], a[2 * i + 1]), cat2(r[n][2 * i], r[n][2 * i + 1]));
      }
      asm volatile("s_nop 7" : "+v"(acc[0]), "+v"(acc[1]), "+v"(acc[2]), "+v"(acc[3]));
      const float o01 = (fq & 1) ? acc[1][0] : acc[0][0], o23 = (fq & 1) ? acc[3][0] : acc[2][0];
      ST64(mdo, ((u32x2){__float_as_uint((fq & 2) ? o23 : o01), T[0]}));
      ST128(kvs, kvd);
      ST32(cko, kvc);
    }
    SCHEDB();
    LDS32(lds + L_FL + 12) = 1u;
    asm volatile("s_waitcnt vmcnt(13)" : FILT_REGS::"memory");
    filter_copies();
    u32x4 stv;
    asm volatile("s_waitcnt vmcnt(3)"
                 : "+v"(sx[0]), "+v"(sx[1]), "+v"(sx[2]), "+v"(sx[3]), "+v"(sx[4]), "+v"(sx[5]), "+v"(sx[6]), "+v"(sx[7]),
                   "+v"(sx[8]), "+v"(sx[9])::"memory");
    {
      f32x2 s2 = {0.f, 0.f};
#pragma unroll
      for (int i = 0; i < 10; ++i)
#pragma unroll
        for (int j = 0; j < 4; ++j) s2 += up2(sx[i][j]);
      float s = s2[0] + s2[1];
      DPP_ADD(s, 0xB1); DPP_ADD(s, 0x4E); DPP_ADD(s, 0x141);
      const float mean = s * (1.0f / D_);
      f32x2 v2 = {0.f, 0.f};
#pragma unroll
      for (int i = 0; i < 10; ++i)
#pragma unroll
        for (int j = 0; j < 4; ++j) { const f32x2 d = up2(sx[i][j]) - mean; v2 += d * d; }
      float vs = v2[0] + v2[1];
      DPP_ADD(vs, 0xB1); DPP_ADD(vs, 0x4E); DPP_ADD(vs, 0x141);
      const float var = vs * (1.0f / D_);
      const float rsd = 1.0f / rb1(sqrtf(rb1(rb1(var) + 1e-5f)));
      const float m1 = __builtin_bit_cast(float, __builtin_amdgcn_update_dpp(0, __builtin_bit_cast(int, mean), 0x108, 0xf, 0xf, true));
      const float r1 = __builtin_bit_cast(float, __builtin_amdgcn_update_dpp(0, __builtin_bit_cast(int, rsd), 0x108, 0xf, 0xf, true));
      stv = (u32x4){__float_as_uint(rsd), __float_as_uint(r1), pk2(mean, m1), T[0]};
      asm volatile("" : "+v"(stv));
    }
    SCHEDB();
    x_request();
    SCHEDB();
    conv_addr();
    SCHEDB();
    asm volatile("s_waitcnt vmcnt(0)" : X_REGS::"memory");
    x_finish();
    ST128((lane & 15) == 0 ? WS_STAT + (srow >> 9) * 4096 + (lane >> 4) * 1024 + ((srow >> 3) & 63) * 16 : 0x80000000u, stv);
    x1_fill();
#pragma unroll
    for (int i = 0; i < 7; ++i) ST32(0x80000000u + 64 * i, 0u);
    SCHEDB();
  }
  unsigned Dw[3];
  {
    const u64 d2 = (((u64)dA[1] << 32) | dA[0]) >> (16 * (chA < 638 ? chA & 1 : 2));
    Dw[0] = (unsigned)d2 << 16; Dw[1] = (unsigned)d2 & 0xFFFF0000u; Dw[2] = (chH & 1) ? dH & 0xFFFF0000u : dH << 16;
  }
  const size_t wpb = (size_t)ws + WS_WP + (size_t)(bn * 4 + w) * 61440;
  const unsigned wplo = __builtin_amdgcn_readfirstlane((unsigned)wpb), wphi = __builtin_amdgcn_readfirstlane((unsigned)(wpb >> 32));
  f32x16 hp;
  u32x32 oa;
  u32x16 ob;
  u32x4 oc, od, f0, f1, f2;
  ia[12] = T[0]; ia[13] = T[1];
  BAR();
  asm volatile(
      "s_cmp_lg_u32 %[side], 0\n\t"
      "s_cbranch_scc1 L_cv_b%=\n\t"
      "ds_read_b128 v[160:163], v43 offset:4608\n\t"
      "ds_read_b64 v[112:113], v42 offset:0\n\t"
      "ds_read_b64 v[114:115], v42 offset:8\n\t"
      "ds_read_b64 v[148:149], v42 offset:3072\n\t"
      "ds_read_b64 v[150:151], v42 offset:3080\n\t"
      "ds_read_b128 v[172:175], v43 offset:0\n\t"
      "ds_read_b128 v[168:171], v43 offset:1536\n\t"
      "ds_read_b64 v[124:125], v42 offset:1024\n\t"
      "ds_read_b64 v[126:127], v42 offset:1032\n\t"
      "ds_read_b128 v[164:167], v43 offset:3072\n\t"
      "ds_read_b64 v[136:137], v42 offset:2048\n\t"
      "ds_read_b64 v[138:139], v42 offset:2056\n\t"
      "ds_read_b64 v[116:117], v42 offset:32\n\t"
      "ds_read_b64 v[118:119], v42 offset:40\n\t"
      "ds_read_b64 v[128:129], v42 offset:1056\n\t"
      "ds_read_b64 v[130:131], v42 offset:1064\n\t"
      "ds_read_b64 v[140:141], v42 offset:2080\n\t"
      "ds_read_b64 v[142:143], v42 offset:2088\n\t"
      "ds_read_b64 v[152:153], v42 offset:3104\n\t"
      "ds_read_b64 v[154:155], v42 offset:3112\n\t"
      "s_waitcnt lgkmcnt(8)\n\t"
      "v_mfma_f32_16x16x32_bf16 v[96:99], v[112:115], v[160:163], 0\n\t"
      "ds_read_b128 v[176:179], v43 offset:4640\n\t"
      "ds_read_b64 v[120:121], v42 offset:64\n\t"
      "v_mfma_f32_16x16x32_bf16 v[104:107], v[148:151], v[160:163], 0\n\t"
      "ds_read_b64 v[122:123], v42 offset:72\n\t"
      "v_mfma_f32_16x16x32_bf16 v[104:107], v[112:115], v[172:175], v[104:107]\n\t"
      "ds_read_b128 v[180:183], v43 offset:3104\n\t"
      "ds_read_b64 v[132:133], v42 offset:1088\n\t"
      "v_mfma_f32_16x16x32_bf16 v[104:107], v[124:127], v[168:171], v[104:107]\n\t"
      "ds_read_b64 v[134:135], v42 offset:1096\n\t"
      "v_mfma_f32_16x16x32_bf16 v[104:107], v[136:139], v[164:167], v[104:107]\n\t"
      "ds_read_b128 v[184:187], v43 offset:1568\n\t"
      "ds_read_b64 v[144:145], v42 offset:2112\n\t"
      "s_waitcnt lgkmcnt(8)\n\t"
      "v_mfma_f32_16x16x32_bf16 v[100:103], v[116:119], v[160:163], 0\n\t"
      "ds_read_b64 v[146:147], v42 offset:2120\n\t"
      "v_mfma_f32_16x16x32_bf16 v[108:111], v[152:155], v[160:163], 0\n\t"
      "ds_read_b128 v[188:191], v43 offset:32\n\t"
      "ds_read_b64 v[156:157], v42 offset:3136\n\t"
      "v_mfma_f32_16x16x32_bf16 v[108:111], v[116:119], v[172:175], v[108:111]\n\t"
      "ds_read_b64 v[158:159], v42 offset:3144\n\t"
      "v_mfma_f32_16x16x32_bf16 v[108:111], v[128:131], v[168:171], v[108:111]\n\t"
      "v_mfma_f32_16x16x32_bf16 v[108:111], v[140:143], v[164:167], v[108:111]\n\t"
      "s_waitcnt lgkmcnt(0)\n\t"
      "v_mfma_f32_16x16x32_bf16 v[96:99], v[116:119], v[176:179], v[96:99]\n\t"
      "ds_read_b128 v[160:163], v43 offset:4704\n\t"
      "ds_read_b64 v[112:113], v42 offset:96\n\t"
      "v_mfma_f32_16x16x32_bf16 v[104:107], v[152:155], v[176:179], v[104:107]\n\t"
      "ds_read_b64 v[114:115], v42 offset:104\n\t"
      "v_mfma_f32_16x16x32_bf16 v[104:107], v[116:119], v[188:191], v[104:107]\n\t"
      "ds_read_b128 v[164:167], v43 offset:3168\n\t"
      "ds_read_b64 v[124:125], v42 offset:1120\n\t"
      "v_mfma_f32_16x16x32_bf16 v[104:107], v[128:131], v[184:187], v[104:107]\n\t"
      "ds_read_b64 v[126:127], v42 offset:1128\n\t"
      "v_mfma_f32_16x16x32_bf16 v[104:107], v[140:143], v[180:183], v[104:107]\n\t"
      "ds_read_b128 v[168:171], v43 offset:1632\n\t"
      "ds_read_b64 v[136:137], v42 offset:2144\n\t"
      "v_mfma_f32_16x16x32_bf16 v[100:103], v[120:123], v[176:179], v[100:103]\n\t"
      "ds_read_b64 v[138:139], v42 offset:2152\n\t"
      "v_mfma_f32_16x16x32_bf16 v[108:111], v[156:159], v[176:179], v[108:111]\n\t"
      "ds_read_b128 v[172:175], v43 offset:96\n\t"
      "ds_read_b64 v[148:149], v42 offset:3168\n\t"
      "v_mfma_f32_16x16x32_bf16 v[108:111], v[120:123], v[188:191], v[108:111]\n\t"
      "ds_read_b64 v[150:151], v42 offset:3176\n\t"
      "v_mfma_f32_16x16x32_bf16 v[108:111], v[132:135], v[184:187], v[108:111]\n\t"
      "v_mfma_f32_16x16x32_bf16 v[108:111], v[144:147], v[180:183], v[108:111]\n\t"
      "s_waitcnt lgkmcnt(0)\n\t"
      "v_mfma_f32_16x16x32_bf16 v[96:99], v[120:123], v[160:163], v[96:99]\n\t"
      "ds_read_b128 v[176:179], v43 offset:4736\n\t"
      "ds_read_b64 v[116:117], v42 offset:128\n\t"
      "v_mfma_f32_16x16x32_bf16 v[104:107], v[156:159], v[160:163], v[104:107]\n\t"
      "ds_read_b64 v[118:119], v42 offset:136\n\t"
      "v_mfma_f32_16x16x32_bf16 v[104:107], v[120:123], v[172:175], v[104:107]\n\t"
      "ds_read_b128 v[180:183], v43 offset:3200\n\t"
      "ds_read_b64 v[128:129], v42 offset:1152\n\t"
      "v_mfma_f32_16x16x32_bf16 v[104:107], v[132:135], v[168:171], v[104:107]\n\t"
      "ds_read_b64 v[130:131], v42 offset:1160\n\t"
      "v_mfma_f32_16x16x32_bf16 v[104:107], v[144:147], v[164:167], v[104:107]\n\t"
      "ds_read_b128 v[184:187], v43 offset:1664\n\t"
      "ds_read_b64 v[140:141], v42 offset:2176\n\t"
      "v_mfma_f32_16x16x32_bf16 v[100:103], v[112:115], v[160:163], v[100:103]\n\t"
      "ds_read_b64 v[142:143], v42 offset:2184\n\t"
      "v_mfma_f32_16x16x32_bf16 v[108:111], v[148:151], v[160:163], v[108:111]\n\t"
      "ds_read_b128 v[188:191], v43 offset:128\n\t"
      "ds_read_b64 v[152:153], v42 offset:3200\n\t"
      "v_mfma_f32_16x16x32_bf16 v[108:111], v[112:115], v[172:175], v[108:111]\n\t"
      "ds_read_b64 v[154:155], v42 offset:3208\n\t"
      "v_mfma_f32_16x16x32_bf16 v[108:111], v[124:127], v[168:171], v[108:111]\n\t"
      "v_mfma_f32_16x16x32_bf16 v[108:111], v[136:139], v[164:167], v[108:111]\n\t"
      "s_waitcnt lgkmcnt(0)\n\t"
      "v_mfma_f32_16x16x32_bf16 v[96:99], v[112:115], v[176:179], v[96:99]\n\t"
      "ds_read_b128 v[160:163], v41 offset:4608\n\t"
      "ds_read_b64 v[120:121], v40 offset:0\n\t"
      "v_mfma_f32_16x16x32_bf16 v[104:107], v[148:151], v[176:179], v[104:107]\n\t"
      "ds_read_b64 v[122:123], v40 offset:8\n\t"
      "ds_read_b128 v[164:167], v41 offset:3072\n\t"
      "v_mfma_f32_16x16x32_bf16 v[104:107], v[112:115], v[188:191], v[104:107]\n\t"
      "ds_read_b64 v[132:133], v40 offset:1024\n\t"
      "ds_read_b64 v[134:135], v40 offset:1032\n\t"
      "v_mfma_f32_16x16x32_bf16 v[104:107], v[124:127], v[184:187], v[104:107]\n\t"
      "ds_read_b128 v[168:171], v41 offset:1536\n\t"
      "ds_read_b64 v[144:145], v40 offset:2048\n\t"
      "v_mfma_f32_16x16x32_bf16 v[104:107], v[136:139], v[180:183], v[104:107]\n\t"
      "ds_read_b64 v[146:147], v40 offset:2056\n\t"
      "ds_read_b128 v[172:175], v41 offset:0\n\t"
      "v_mfma_f32_16x16x32_bf16 v[100:103], v[116:119], v[176:179], v[100:103]\n\t"
      "ds_read_b64 v[156:157], v40 offset:3072\n\t"
      "ds_read_b64 v[158:159], v40 offset:3080\n\t"
      "v_mfma_f32_16x16x32_bf16 v[108:111], v[152:155], v[176:179], v[108:111]\n\t"
      "ds_read_b64 v[112:113], v40 offset:32\n\t"
      "ds_read_b64 v[114:115], v40 offset:40\n\t"
      "v_mfma_f32_16x16x32_bf16 v[108:111], v[116:119], v[188:191], v[108:111]\n\t"
      "ds_read_b64 v[124:125], v40 offset:1056\n\t"
      "ds_read_b64 v[126:127], v40 offset:1064\n\t"
      "v_mfma_f32_16x16x32_bf16 v[108:111], v[128:131], v[184:187], v[108:111]\n\t"
      "ds_read_b64 v[136:137], v40 offset:2080\n\t"
      "ds_read_b64 v[138:139], v40 offset:2088\n\t"
      "v_mfma_f32_16x16x32_bf16 v[108:111], v[140:143], v[180:183], v[108:111]\n\t"
      "ds_read_b64 v[148:149], v40 offset:3104\n\t"
      "ds_read_b64 v[150:151], v40 offset:3112\n\t"
      "s_branch L_cv_e%=\n\t"
      "L_cv_b%=:\n\t"
      "ds_read_b128 v[164:167], v43 offset:3072\n\t"
      "ds_read_b64 v[112:113], v42 offset:0\n\t"
      "ds_read_b64 v[114:115], v42 offset:8\n\t"
      "ds_read_b64 v[124:125], v42 offset:1024\n\t"
      "ds_read_b64 v[126:127], v42 offset:1032\n\t"
      "ds_read_b128 v[160:163], v43 offset:4608\n\t"
      "ds_read_b64 v[136:137], v42 offset:2048\n\t"
      "ds_read_b64 v[138:139], v42 offset:2056\n\t"
      "ds_read_b128 v[168:171], v43 offset:1536\n\t"
      "ds_read_b64 v[116:117], v42 offset:32\n\t"
      "ds_read_b64 v[118:119], v42 offset:40\n\t"
      "ds_read_b64 v[128:129], v42 offset:1056\n\t"
      "ds_read_b64 v[130:131], v42 offset:1064\n\t"
      "ds_read_b64 v[140:141], v42 offset:2080\n\t"
      "ds_read_b64 v[142:143], v42 offset:2088\n\t"
      "s_waitcnt lgkmcnt(6)\n\t"
      "v_mfma_f32_16x16x32_bf16 v[96:99], v[112:115], v[164:167], 0\n\t"
      "ds_read_b128 v[176:179], v43 offset:4640\n\t"
      "ds_read_b64 v[120:121], v42 offset:64\n\t"
      "v_mfma_f32_16x16x32_bf16 v[104:107], v[124:127], v[164:167], 0\n\t"
      "ds_read_b64 v[122:123], v42 offset:72\n\t"
      "v_mfma_f32_16x16x32_bf16 v[96:99], v[124:127], v[160:163], v[96:99]\n\t"
      "ds_read_b128 v[180:183], v43 offset:3104\n\t"
      "v_mfma_f32_16x16x32_bf16 v[104:107], v[136:139], v[160:163], v[104:107]\n\t"
      "ds_read_b64 v[132:133], v42 offset:1088\n\t"
      "v_mfma_f32_16x16x32_bf16 v[104:107], v[112:115], v[168:171], v[104:107]\n\t"
      "ds_read_b64 v[134:135], v42 offset:1096\n\t"
      "s_waitcnt lgkmcnt(6)\n\t"
      "v_mfma_f32_16x16x32_bf16 v[100:103], v[116:119], v[164:167], 0\n\t"
      "ds_read_b128 v[184:187], v43 offset:1568\n\t"
      "v_mfma_f32_16x16x32_bf16 v[108:111], v[128:131], v[164:167], 0\n\t"
      "ds_read_b64 v[144:145], v42 offset:2112\n\t"
      "v_mfma_f32_16x16x32_bf16 v[100:103], v[128:131], v[160:163], v[100:103]\n\t"
      "ds_read_b64 v[146:147], v42 offset:2120\n\t"
      "v_mfma_f32_16x16x32_bf16 v[108:111], v[140:143], v[160:163], v[108:111]\n\t"
      "v_mfma_f32_16x16x32_bf16 v[108:111], v[116:119], v[168:171], v[108:111]\n\t"
      "s_waitcnt lgkmcnt(0)\n\t"
      "v_mfma_f32_16x16x32_bf16 v[96:99], v[116:119], v[180:183], v[96:99]\n\t"
      "ds_read_b128 v[160:163], v43 offset:4704\n\t"
      "ds_read_b64 v[112:113], v42 offset:96\n\t"
      "v_mfma_f32_16x16x32_bf16 v[104:107], v[128:131], v[180:183], v[104:107]\n\t"
      "ds_read_b64 v[114:115], v42 offset:104\n\t"
      "v_mfma_f32_16x16x32_bf16 v[96:99], v[128:131], v[176:179], v[96:99]\n\t"
      "ds_read_b128 v[164:167], v43 offset:3168\n\t"
      "v_mfma_f32_16x16x32_bf16 v[104:107], v[140:143], v[176:179], v[104:107]\n\t"
      "ds_read_b64 v[124:125], v42 offset:1120\n\t"
      "v_mfma_f32_16x16x32_bf16 v[104:107], v[116:119], v[184:187], v[104:107]\n\t"
      "ds_read_b64 v[126:127], v42 offset:1128\n\t"
      "v_mfma_f32_16x16x32_bf16 v[100:103], v[120:123], v[180:183], v[100:103]\n\t"
      "ds_read_b128 v[168:171], v43 offset:1632\n\t"
      "v_mfma_f32_16x16x32_bf16 v[108:111], v[132:135], v[180:183], v[108:111]\n\t"
      "ds_read_b64 v[136:137], v42 offset:2144\n\t"
      "v_mfma_f32_16x16x32_bf16 v[100:103], v[132:135], v[176:179], v[100:103]\n\t"
      "ds_read_b64 v[138:139], v42 offset:2152\n\t"
      "v_mfma_f32_16x16x32_bf16 v[108:111], v[144:147], v[176:179], v[108:111]\n\t"
      "v_mfma_f32_16x16x32_bf16 v[108:111], v[120:123], v[184:187], v[108:111]\n\t"
      "s_waitcnt lgkmcnt(0)\n\t"
      "v_mfma_f32_16x16x32_bf16 v[96:99], v[120:123], v[164:167], v[96:99]\n\t"
      "ds_read_b128 v[176:179], v43 offset:4736\n\t"
      "ds_read_b64 v[116:117], v42 offset:128\n\t"
      "v_mfma_f32_16x16x32_bf16 v[104:107], v[132:135], v[164:167], v[104:107]\n\t"
      "ds_read_b64 v[118:119], v42 offset:136\n\t"
      "v_mfma_f32_16x16x32_bf16 v[96:99], v[132:135], v[160:163], v[96:99]\n\t"
      "ds_read_b128 v[180:183], v43 offset:3200\n\t"
      "v_mfma_f32_16x16x32_bf16 v[104:107], v[144:147], v[160:163], v[104:107]\n\t"
      "ds_read_b64 v[128:129], v42 offset:1152\n\t"
      "v_mfma_f32_16x16x32_bf16 v[104:107], v[120:123], v[168:171], v[104:107]\n\t"
      "ds_read_b64 v[130:131], v42 offset:1160\n\t"
      "v_mfma_f32_16x16x32_bf16 v[100:103], v[112:115], v[164:167], v[100:103]\n\t"
      "ds_read_b128 v[184:187], v43 offset:1664\n\t"
      "v_mfma_f32_16x16x32_bf16 v[108:111], v[124:127], v[164:167], v[108:111]\n\t"
      "ds_read_b64 v[140:141], v42 offset:2176\n\t"
      "v_mfma_f32_16x16x32_bf16 v[100:103], v[124:127], v[160:163], v[100:103]\n\t"
      "ds_read_b64 v[142:143], v42 offset:2184\n\t"
      "v_mfma_f32_16x16x32_bf16 v[108:111], v[136:139], v[160:163], v[108:111]\n\t"
      "v_mfma_f32_16x16x32_bf16 v[108:111], v[112:115], v[168:171], v[108:111]\n\t"
      "s_waitcnt lgkmcnt(0)\n\t"
      "v_mfma_f32_16x16x32_bf16 v[96:99], v[112:115], v[180:183], v[96:99]\n\t"
      "ds_read_b128 v[160:163], v41 offset:4608\n\t"
      "ds_read_b64 v[120:121], v40 offset:0\n\t"
      "v_mfma_f32_16x16x32_bf16 v[104:107], v[124:127], v[180:183], v[104:107]\n\t"
      "ds_read_b64 v[122:123], v40 offset:8\n\t"
      "ds_read_b128 v[164:167], v41 offset:3072\n\t"
      "v_mfma_f32_16x16x32_bf16 v[96:99], v[124:127], v[176:179], v[96:99]\n\t"
      "ds_read_b64 v[132:133], v40 offset:1024\n\t"
      "ds_read_b64 v[134:135], v40 offset:1032\n\t"
      "v_mfma_f32_16x16x32_bf16 v[104:107], v[136:139], v[176:179], v[104:107]\n\t"
      "ds_read_b128 v[168:171], v41 offset:1536\n\t"
      "ds_read_b64 v[144:145], v40 offset:2048\n\t"
      "v_mfma_f32_16x16x32_bf16 v[104:107], v[112:115], v[184:187], v[104:107]\n\t"
      "ds_read_b64 v[146:147], v40 offset:2056\n\t"
      "ds_read_b128 v[172:175], v41 offset:0\n\t"
      "v_mfma_f32_16x16x32_bf16 v[100:103], v[116:119], v[180:183], v[100:103]\n\t"
      "ds_read_b64 v[156:157], v40 offset:3072\n\t"
      "ds_read_b64 v[158:159], v40 offset:3080\n\t"
      "v_mfma_f32_16x16x32_bf16 v[108:111], v[128:131], v[180:183], v[108:111]\n\t"
      "ds_read_b64 v[112:113], v40 offset:32\n\t"
      "ds_read_b64 v[114:115], v40 offset:40\n\t"
      "v_mfma_f32_16x16x32_bf16 v[100:103], v[128:131], v[176:179], v[100:103]\n\t"
      "ds_read_b64 v[124:125], v40 offset:1056\n\t"
      "ds_read_b64 v[126:127], v40 offset:1064\n\t"
      "v_mfma_f32_16x16x32_bf16 v[108:111], v[140:143], v[176:179], v[108:111]\n\t"
      "ds_read_b64 v[136:137], v40 offset:2080\n\t"
      "ds_read_b64 v[138:139], v40 offset:2088\n\t"
      "v_mfma_f32_16x16x32_bf16 v[108:111], v[116:119], v[184:187], v[108:111]\n\t"
      "ds_read_b64 v[148:149], v40 offset:3104\n\t"
      "ds_read_b64 v[150:151], v40 offset:3112\n\t"
      "L_cv_e%=:\n\t"
      "s_mov_b32 s42, %[wlo]\n\t"
      "s_mov_b32 s43, %[whi]\n\t"
      "s_waitcnt vmcnt(7)\n\t"
      "s_mov_b64 exec, 0xff\n\t"
      "global_store_dwordx2 v51, v[52:53], s[42:43] sc1\n\t"
      "s_mov_b64 exec, -1\n\t"
      "s_waitcnt lgkmcnt(8)\n\t"
      "v_mfma_f32_16x16x32_bf16 v[64:67], v[120:123], v[160:163], 0\n\t"
      "ds_read_b128 v[176:179], v41 offset:4640\n\t"
      "v_mfma_f32_16x16x32_bf16 v[72:75], v[120:123], v[164:167], 0\n\t"
      "ds_read_b64 v[116:117], v40 offset:64\n\t"
      "v_mfma_f32_16x16x32_bf16 v[80:83], v[120:123], v[168:171], 0\n\t"
      "ds_read_b64 v[118:119], v40 offset:72\n\t"
      "v_mfma_f32_16x16x32_bf16 v[88:91], v[120:123], v[172:175], 0\n\t"
      "ds_read_b128 v[180:183], v41 offset:3104\n\t"
      "v_mfma_f32_16x16x32_bf16 v[72:75], v[132:135], v[160:163], v[72:75]\n\t"
      "ds_read_b64 v[128:129], v40 offset:1088\n\t"
      "v_mfma_f32_16x16x32_bf16 v[80:83], v[132:135], v[164:167], v[80:83]\n\t"
      "ds_read_b64 v[130:131], v40 offset:1096\n\t"
      "v_mfma_f32_16x16x32_bf16 v[88:91], v[132:135], v[168:171], v[88:91]\n\t"
      "ds_read_b128 v[184:187], v41 offset:1568\n\t"
      "v_mfma_f32_16x16x32_bf16 v[80:83], v[144:147], v[160:163], v[80:83]\n\t"
      "ds_read_b64 v[140:141], v40 offset:2112\n\t"
      "v_mfma_f32_16x16x32_bf16 v[88:91], v[144:147], v[164:167], v[88:91]\n\t"
      "ds_read_b64 v[142:143], v40 offset:2120\n\t"
      "v_mfma_f32_16x16x32_bf16 v[88:91], v[156:159], v[160:163], v[88:91]\n\t"
      "ds_read_b128 v[188:191], v41 offset:32\n\t"
      "s_waitcnt lgkmcnt(10)\n\t"
      "v_mfma_f32_16x16x32_bf16 v[68:71], v[112:115], v[160:163], 0\n\t"
      "ds_read_b64 v[152:153], v40 offset:3136\n\t"
      "v_mfma_f32_16x16x32_bf16 v[76:79], v[112:115], v[164:167], 0\n\t"
      "ds_read_b64 v[154:155], v40 offset:3144\n\t"
      "v_mfma_f32_16x16x32_bf16 v[84:87], v[112:115], v[168:171], 0\n\t"
      "v_add_u32 v34, %[exd], v44\n\t"
      "v_add_u32 v35, %[exd], v59\n\t"
      "v_mfma_f32_16x16x32_bf16 v[92:95], v[112:115], v[172:175], 0\n\t"
      "ds_write_b128 v34, v[96:99]\n\t"
      "v_mfma_f32_16x16x32_bf16 v[76:79], v[124:127], v[160:163], v[76:79]\n\t"
      "ds_write_b128 v34, v[104:107] offset:2048\n\t"
      "v_mfma_f32_16x16x32_bf16 v[84:87], v[124:127], v[164:167], v[84:87]\n\t"
      "v_mfma_f32_16x16x32_bf16 v[92:95], v[124:127], v[168:171], v[92:95]\n\t"
      "v_mfma_f32_16x16x32_bf16 v[84:87], v[136:139], v[160:163], v[84:87]\n\t"
      "v_mfma_f32_16x16x32_bf16 v[92:95], v[136:139], v[164:167], v[92:95]\n\t"
      "v_mfma_f32_16x16x32_bf16 v[92:95], v[148:151], v[160:163], v[92:95]\n\t"
      "s_waitcnt lgkmcnt(2)\n\t"
      "v_mfma_f32_16x16x32_bf16 v[64:67], v[112:115], v[176:179], v[64:67]\n\t"
      "ds_read_b128 v[160:163], v41 offset:4704\n\t"
      "v_mfma_f32_16x16x32_bf16 v[72:75], v[112:115], v[180:183], v[72:75]\n\t"
      "ds_read_b64 v[120:121], v40 offset:96\n\t"
      "v_mfma_f32_16x16x32_bf16 v[80:83], v[112:115], v[184:187], v[80:83]\n\t"
      "ds_read_b64 v[122:123], v40 offset:104\n\t"
      "v_mfma_f32_16x16x32_bf16 v[88:91], v[112:115], v[188:191], v[88:91]\n\t"
      "ds_read_b128 v[164:167], v41 offset:3168\n\t"
      "v_mfma_f32_16x16x32_bf16 v[72:75], v[124:127], v[176:179], v[72:75]\n\t"
      "ds_read_b64 v[132:133], v40 offset:1120\n\t"
      "v_mfma_f32_16x16x32_bf16 v[80:83], v[124:127], v[180:183], v[80:83]\n\t"
      "ds_read_b64 v[134:135], v40 offset:1128\n\t"
      "v_mfma_f32_16x16x32_bf16 v[88:91], v[124:127], v[184:187], v[88:91]\n\t"
      "ds_read_b128 v[168:171], v41 offset:1632\n\t"
      "v_mfma_f32_16x16x32_bf16 v[80:83], v[136:139], v[176:179], v[80:83]\n\t"
      "ds_read_b64 v[144:145], v40 offset:2144\n\t"
      "v_mfma_f32_16x16x32_bf16 v[88:91], v[136:139], v[180:183], v[88:91]\n\t"
      "ds_read_b64 v[146:147], v40 offset:2152\n\t"
      "v_mfma_f32_16x16x32_bf16 v[88:91], v[148:151], v[176:179], v[88:91]\n\t"
      "ds_read_b128 v[172:175], v41 offset:96\n\t"
      "v_mfma_f32_16x16x32_bf16 v[68:71], v[116:119], v[176:179], v[68:71]\n\t"
      "ds_read_b64 v[156:157], v40 offset:3168\n\t"
      "v_mfma_f32_16x16x32_bf16 v[76:79], v[116:119], v[180:183], v[76:79]\n\t"
      "ds_read_b64 v[158:159], v40 offset:3176\n\t"
      "v_mfma_f32_16x16x32_bf16 v[84:87], v[116:119], v[184:187], v[84:87]\n\t"
      "ds_write_b128 v35, v[100:103]\n\t"
      "v_mfma_f32_16x16x32_bf16 v[92:95], v[116:119], v[188:191], v[92:95]\n\t"
      "ds_write_b128 v35, v[108:111] offset:2048\n\t"
      "v_mfma_f32_16x16x32_bf16 v[76:79], v[128:131], v[176:179], v[76:79]\n\t"
      "v_mfma_f32_16x16x32_bf16 v[84:87], v[128:131], v[180:183], v[84:87]\n\t"
      "v_mfma_f32_16x16x32_bf16 v[92:95], v[128:131], v[184:187], v[92:95]\n\t"
      "v_mfma_f32_16x16x32_bf16 v[84:87], v[140:143], v[176:179], v[84:87]\n\t"
      "v_mfma_f32_16x16x32_bf16 v[92:95], v[140:143], v[180:183], v[92:95]\n\t"
      "v_mfma_f32_16x16x32_bf16 v[92:95], v[152:155], v[176:179], v[92:95]\n\t"
      "s_waitcnt lgkmcnt(2)\n\t"
      "v_mfma_f32_16x16x32_bf16 v[64:67], v[116:119], v[160:163], v[64:67]\n\t"
      "ds_read_b128 v[176:179], v41 offset:4736\n\t"
      "v_mfma_f32_16x16x32_bf16 v[72:75], v[116:119], v[164:167], v[72:75]\n\t"
      "ds_read_b64 v[112:113], v40 offset:128\n\t"
      "v_mfma_f32_16x16x32_bf16 v[80:83], v[116:119], v[168:171], v[80:83]\n\t"
      "ds_read_b64 v[114:115], v40 offset:136\n\t"
      "v_mfma_f32_16x16x32_bf16 v[88:91], v[116:119], v[172:175], v[88:91]\n\t"
      "ds_read_b128 v[180:183], v41 offset:3200\n\t"
      "v_mfma_f32_16x16x32_bf16 v[72:75], v[128:131], v[160:163], v[72:75]\n\t"
      "ds_read_b64 v[124:125], v40 offset:1152\n\t"
      "v_mfma_f32_16x16x32_bf16 v[80:83], v[128:131], v[164:167], v[80:83]\n\t"
      "ds_read_b64 v[126:127], v40 offset:1160\n\t"
      "v_mfma_f32_16x16x32_bf16 v[88:91], v[128:131], v[168:171], v[88:91]\n\t"
      "ds_read_b128 v[184:187], v41 offset:1664\n\t"
      "v_mfma_f32_16x16x32_bf16 v[80:83], v[140:143], v[160:163], v[80:83]\n\t"
      "ds_read_b64 v[136:137], v40 offset:2176\n\t"
      "v_mfma_f32_16x16x32_bf16 v[88:91], v[140:143], v[164:167], v[88:91]\n\t"
      "ds_read_b64 v[138:139], v40 offset:2184\n\t"
      "v_mfma_f32_16x16x32_bf16 v[88:91], v[152:155], v[160:163], v[88:91]\n\t"
      "ds_read_b128 v[188:191], v41 offset:128\n\t"
      "v_mfma_f32_16x16x32_bf16 v[68:71], v[120:123], v[160:163], v[68:71]\n\t"
      "ds_read_b64 v[148:149], v40 offset:3200\n\t"
      "v_mfma_f32_16x16x32_bf16 v[76:79], v[120:123], v[164:167], v[76:79]\n\t"
      "ds_read_b64 v[150:151], v40 offset:3208\n\t"
      "v_mfma_f32_16x16x32_bf16 v[84:87], v[120:123], v[168:171], v[84:87]\n\t"
      "v_mfma_f32_16x16x32_bf16 v[92:95], v[120:123], v[172:175], v[92:95]\n\t"
      "v_mfma_f32_16x16x32_bf16 v[76:79], v[132:135], v[160:163], v[76:79]\n\t"
      "v_mfma_f32_16x16x32_bf16 v[84:87], v[132:135], v[164:167], v[84:87]\n\t"
      "v_mfma_f32_16x16x32_bf16 v[92:95], v[132:135], v[168:171], v[92:95]\n\t"
      "v_mfma_f32_16x16x32_bf16 v[84:87], v[144:147], v[160:163], v[84:87]\n\t"
      "v_mfma_f32_16x16x32_bf16 v[92:95], v[144:147], v[164:167], v[92:95]\n\t"
      "v_mfma_f32_16x16x32_bf16 v[92:95], v[156:159], v[160:163], v[92:95]\n\t"
      "s_waitcnt vmcnt(1)\n\t"
      "s_mov_b64 exec, 0xff\n\t"
      "global_store_dwordx2 v62, v[52:53], s[42:43] sc1\n\t"
      "s_mov_b64 exec, -1\n\t"
      "s_waitcnt lgkmcnt(0)\n\t"
      "s_barrier\n\t"
      "v_mfma_f32_16x16x32_bf16 v[64:67], v[120:123], v[176:179], v[64:67]\n\t"
      "ds_read_b128 v[160:163], v41 offset:4800\n\t"
      "v_mfma_f32_16x16x32_bf16 v[72:75], v[120:123], v[180:183], v[72:75]\n\t"
      "ds_read_b64 v[116:117], v40 offset:160\n\t"
      "v_mfma_f32_16x16x32_bf16 v[80:83], v[120:123], v[184:187], v[80:83]\n\t"
      "ds_read_b64 v[118:119], v40 offset:168\n\t"
      "v_mfma_f32_16x16x32_bf16 v[88:91], v[120:123], v[188:191], v[88:91]\n\t"
      "ds_read_b128 v[164:167], v41 offset:3264\n\t"
      "v_mfma_f32_16x16x32_bf16 v[72:75], v[132:135], v[176:179], v[72:75]\n\t"
      "global_load_dwordx4 v[192:195], v54, s[42:43] offset:-2048 sc1\n\t"
      "v_mfma_f32_16x16x32_bf16 v[80:83], v[132:135], v[180:183], v[80:83]\n\t"
      "ds_read_b64 v[128:129], v40 offset:1184\n\t"
      "v_mfma_f32_16x16x32_bf16 v[88:91], v[132:135], v[184:187], v[88:91]\n\t"
      "ds_read_b64 v[130:131], v40 offset:1192\n\t"
      "v_mfma_f32_16x16x32_bf16 v[80:83], v[144:147], v[176:179], v[80:83]\n\t"
      "ds_read_b128 v[168:171], v41 offset:1728\n\t"
      "v_mfma_f32_16x16x32_bf16 v[88:91], v[144:147], v[180:183], v[88:91]\n\t"
      "ds_read_b64 v[140:141], v40 offset:2208\n\t"
      "v_mfma_f32_16x16x32_bf16 v[88:91], v[156:159], v[176:179], v[88:91]\n\t"
      "global_load_dwordx4 v[196:199], v54, s[42:43] offset:2048 sc1\n\t"
      "v_mfma_f32_16x16x32_bf16 v[68:71], v[112:115], v[176:179], v[68:71]\n\t"
      "ds_read_b64 v[142:143], v40 offset:2216\n\t"
      "v_mfma_f32_16x16x32_bf16 v[76:79], v[112:115], v[180:183], v[76:79]\n\t"
      "ds_read_b128 v[172:175], v41 offset:192\n\t"
      "v_mfma_f32_16x16x32_bf16 v[84:87], v[112:115], v[184:187], v[84:87]\n\t"
      "ds_read_b64 v[152:153], v40 offset:3232\n\t"
      "v_mfma_f32_16x16x32_bf16 v[92:95], v[112:115], v[188:191], v[92:95]\n\t"
      "ds_read_b64 v[154:155], v40 offset:3240\n\t"
      "v_mfma_f32_16x16x32_bf16 v[76:79], v[124:127], v[176:179], v[76:79]\n\t"
      "global_load_dwordx2 v[200:201], v55, s[42:43] offset:0 sc1\n\t"
      "v_mfma_f32_16x16x32_bf16 v[84:87], v[124:127], v[180:183], v[84:87]\n\t"
      "ds_read_b128 v[96:99], v45 offset:0\n\t"
      "v_mfma_f32_16x16x32_bf16 v[92:95], v[124:127], v[184:187], v[92:95]\n\t"
      "ds_read_b128 v[100:103], v45 offset:4096\n\t"
      "v_mfma_f32_16x16x32_bf16 v[84:87], v[136:139], v[176:179], v[84:87]\n\t"
      "ds_read_b128 v[104:107], v45 offset:8192\n\t"
      "v_mfma_f32_16x16x32_bf16 v[92:95], v[136:139], v[180:183], v[92:95]\n\t"
      "ds_read_b128 v[108:111], v45 offset:12288\n\t"
      "v_mfma_f32_16x16x32_bf16 v[92:95], v[148:151], v[176:179], v[92:95]\n\t"
      "global_load_dwordx2 v[248:249], v60, s[42:43] offset:0 sc1\n\t"
      "s_waitcnt lgkmcnt(4)\n\t"
      "v_mfma_f32_16x16x32_bf16 v[64:67], v[112:115], v[160:163], v[64:67]\n\t"
      "ds_read_b128 v[176:179], v41 offset:4832\n\t"
      "v_mfma_f32_16x16x32_bf16 v[72:75], v[112:115], v[164:167], v[72:75]\n\t"
      "ds_read_b64 v[120:121], v40 offset:192\n\t"
      "v_mfma_f32_16x16x32_bf16 v[80:83], v[112:115], v[168:171], v[80:83]\n\t"
      "ds_read_b64 v[122:123], v40 offset:200\n\t"
      "v_mfma_f32_16x16x32_bf16 v[88:91], v[112:115], v[172:175], v[88:91]\n\t"
      "ds_read_b128 v[180:183], v41 offset:3296\n\t"
      "v_mfma_f32_16x16x32_bf16 v[72:75], v[124:127], v[160:163], v[72:75]\n\t"
      "global_load_dword v250, v61, s[42:43] offset:0 sc1\n\t"
      "v_mfma_f32_16x16x32_bf16 v[80:83], v[124:127], v[164:167], v[80:83]\n\t"
      "ds_read_b64 v[132:133], v40 offset:1216\n\t"
      "v_mfma_f32_16x16x32_bf16 v[88:91], v[124:127], v[168:171], v[88:91]\n\t"
      "ds_read_b64 v[134:135], v40 offset:1224\n\t"
      "v_mfma_f32_16x16x32_bf16 v[80:83], v[136:139], v[160:163], v[80:83]\n\t"
      "ds_read_b128 v[184:187], v41 offset:1760\n\t"
      "v_mfma_f32_16x16x32_bf16 v[88:91], v[136:139], v[164:167], v[88:91]\n\t"
      "ds_read_b64 v[144:145], v40 offset:2240\n\t"
      "v_mfma_f32_16x16x32_bf16 v[88:91], v[148:151], v[160:163], v[88:91]\n\t"
      "global_load_dwordx4 v[204:207], v58, s[42:43] offset:0 sc1\n\t"
      "v_mfma_f32_16x16x32_bf16 v[68:71], v[116:119], v[160:163], v[68:71]\n\t"
      "ds_read_b64 v[146:147], v40 offset:2248\n\t"
      "v_mfma_f32_16x16x32_bf16 v[76:79], v[116:119], v[164:167], v[76:79]\n\t"
      "ds_read_b128 v[188:191], v41 offset:224\n\t"
      "v_mfma_f32_16x16x32_bf16 v[84:87], v[116:119], v[168:171], v[84:87]\n\t"
      "ds_read_b64 v[156:157], v40 offset:3264\n\t"
      "v_mfma_f32_16x16x32_bf16 v[92:95], v[116:119], v[172:175], v[92:95]\n\t"
      "ds_read_b64 v[158:159], v40 offset:3272\n\t"
      "v_mfma_f32_16x16x32_bf16 v[76:79], v[128:131], v[160:163], v[76:79]\n\t"
      "global_load_dwordx4 v[36:39], v58, s[42:43] offset:16 sc1\n\t"
      "v_mfma_f32_16x16x32_bf16 v[84:87], v[128:131], v[164:167], v[84:87]\n\t"
      "ds_read_b128 v[232:235], v46\n\t"
      "v_mfma_f32_16x16x32_bf16 v[92:95], v[128:131], v[168:171], v[92:95]\n\t"
      "ds_read_b128 v[236:239], v46 offset:7680\n\t"
      "v_mfma_f32_16x16x32_bf16 v[84:87], v[140:143], v[160:163], v[84:87]\n\t"
      "ds_read_b64 v[202:203], v47\n\t"
      "v_mfma_f32_16x16x32_bf16 v[92:95], v[140:143], v[164:167], v[92:95]\n\t"
      "v_mfma_f32_16x16x32_bf16 v[92:95], v[152:155], v[160:163], v[92:95]\n\t"
      "global_load_dwordx4 v[208:211], v56, s[42:43] offset:0 sc1\n\t"
      "s_waitcnt lgkmcnt(3)\n\t"
      "v_mfma_f32_16x16x32_bf16 v[64:67], v[116:119], v[176:179], v[64:67]\n\t"
      "ds_read_b128 v[160:163], v41 offset:4896\n\t"
      "v_mfma_f32_16x16x32_bf16 v[72:75], v[116:119], v[180:183], v[72:75]\n\t"
      "ds_read_b64 v[112:113], v40 offset:224\n\t"
      "v_mfma_f32_16x16x32_bf16 v[80:83], v[116:119], v[184:187], v[80:83]\n\t"
      "ds_read_b64 v[114:115], v40 offset:232\n\t"
      "v_mfma_f32_16x16x32_bf16 v[88:91], v[116:119], v[188:191], v[88:91]\n\t"
      "ds_read_b128 v[164:167], v41 offset:3360\n\t"
      "v_mfma_f32_16x16x32_bf16 v[72:75], v[128:131], v[176:179], v[72:75]\n\t"
      "global_load_dwordx4 v[212:215], v56, s[42:43] offset:1024 sc1\n\t"
      "v_mfma_f32_16x16x32_bf16 v[80:83], v[128:131], v[180:183], v[80:83]\n\t"
      "ds_read_b64 v[124:125], v40 offset:1248\n\t"
      "v_mfma_f32_16x16x32_bf16 v[88:91], v[128:131], v[184:187], v[88:91]\n\t"
      "ds_read_b64 v[126:127], v40 offset:1256\n\t"
      "v_mfma_f32_16x16x32_bf16 v[80:83], v[140:143], v[176:179], v[80:83]\n\t"
      "ds_read_b128 v[168:171], v41 offset:1824\n\t"
      "v_mfma_f32_16x16x32_bf16 v[88:91], v[140:143], v[180:183], v[88:91]\n\t"
      "ds_read_b64 v[136:137], v40 offset:2272\n\t"
      "v_mfma_f32_16x16x32_bf16 v[88:91], v[152:155], v[176:179], v[88:91]\n\t"
      "global_load_dwordx4 v[216:219], v56, s[42:43] offset:2048 sc1\n\t"
      "v_mfma_f32_16x16x32_bf16 v[68:71], v[120:123], v[176:179], v[68:71]\n\t"
      "ds_read_b64 v[138:139], v40 offset:2280\n\t"
      "v_mfma_f32_16x16x32_bf16 v[76:79], v[120:123], v[180:183], v[76:79]\n\t"
      "ds_read_b128 v[172:175], v41 offset:288\n\t"
      "v_mfma_f32_16x16x32_bf16 v[84:87], v[120:123], v[184:187], v[84:87]\n\t"
      "ds_read_b64 v[148:149], v40 offset:3296\n\t"
      "v_mfma_f32_16x16x32_bf16 v[92:95], v[120:123], v[188:191], v[92:95]\n\t"
      "ds_read_b64 v[150:151], v40 offset:3304\n\t"
      "v_mfma_f32_16x16x32_bf16 v[76:79], v[132:135], v[176:179], v[76:79]\n\t"
      "global_load_dwordx4 v[220:223], v56, s[42:43] offset:3072 sc1\n\t"
      "v_mfma_f32_16x16x32_bf16 v[84:87], v[132:135], v[180:183], v[84:87]\n\t"
      "v_mfma_f32_16x16x32_bf16 v[92:95], v[132:135], v[184:187], v[92:95]\n\t"
      "v_mfma_f32_16x16x32_bf16 v[84:87], v[144:147], v[176:179], v[84:87]\n\t"
      "v_mfma_f32_16x16x32_bf16 v[92:95], v[144:147], v[180:183], v[92:95]\n\t"
      "v_mfma_f32_16x16x32_bf16 v[92:95], v[156:159], v[176:179], v[92:95]\n\t"
      "global_load_dwordx4 v[224:227], v57, s[42:43] offset:0 sc1\n\t"
      "s_waitcnt lgkmcnt(0)\n\t"
      "v_mfma_f32_16x16x32_bf16 v[64:67], v[120:123], v[160:163], v[64:67]\n\t"
      "ds_read_b128 v[176:179], v41 offset:4928\n\t"
      "v_mfma_f32_16x16x32_bf16 v[72:75], v[120:123], v[164:167], v[72:75]\n\t"
      "ds_read_b64 v[116:117], v40 offset:256\n\t"
      "v_mfma_f32_16x16x32_bf16 v[80:83], v[120:123], v[168:171], v[80:83]\n\t"
      "ds_read_b64 v[118:119], v40 offset:264\n\t"
      "v_mfma_f32_16x16x32_bf16 v[88:91], v[120:123], v[172:175], v[88:91]\n\t"
      "ds_read_b128 v[180:183], v41 offset:3392\n\t"
      "v_mfma_f32_16x16x32_bf16 v[72:75], v[132:135], v[160:163], v[72:75]\n\t"
      "global_load_dwordx4 v[228:231], v57, s[42:43] offset:1024 sc1\n\t"
      "v_mfma_f32_16x16x32_bf16 v[80:83], v[132:135], v[164:167], v[80:83]\n\t"
      "ds_read_b64 v[128:129], v40 offset:1280\n\t"
      "v_mfma_f32_16x16x32_bf16 v[88:91], v[132:135], v[168:171], v[88:91]\n\t"
      "ds_read_b64 v[130:131], v40 offset:1288\n\t"
      "v_mfma_f32_16x16x32_bf16 v[80:83], v[144:147], v[160:163], v[80:83]\n\t"
      "ds_read_b128 v[184:187], v41 offset:1856\n\t"
      "v_mfma_f32_16x16x32_bf16 v[88:91], v[144:147], v[164:167], v[88:91]\n\t"
      "ds_read_b64 v[140:141], v40 offset:2304\n\t"
      "v_mfma_f32_16x16x32_bf16 v[88:91], v[156:159], v[160:163], v[88:91]\n\t"
      "ds_read_b64 v[142:143], v40 offset:2312\n\t"
      "v_mfma_f32_16x16x32_bf16 v[68:71], v[112:115], v[160:163], v[68:71]\n\t"
      "ds_read_b128 v[188:191], v41 offset:320\n\t"
      "v_mfma_f32_16x16x32_bf16 v[76:79], v[112:115], v[164:167], v[76:79]\n\t"
      "s_mov_b32 s45, m0\n\t"
      "s_mov_b32 m0, %[djm]\n\t"
      "s_nop 0\n\t"
      "global_load_lds_dwordx4 v63, %[dj0]\n\t"
      "v_mfma_f32_16x16x32_bf16 v[84:87], v[112:115], v[168:171], v[84:87]\n\t"
      "ds_read_b64 v[152:153], v40 offset:3328\n\t"
      "v_mfma_f32_16x16x32_bf16 v[92:95], v[112:115], v[172:175], v[92:95]\n\t"
      "ds_read_b64 v[154:155], v40 offset:3336\n\t"
      "v_mfma_f32_16x16x32_bf16 v[76:79], v[124:127], v[160:163], v[76:79]\n\t"
      "v_mfma_f32_16x16x32_bf16 v[84:87], v[124:127], v[164:167], v[84:87]\n\t"
      "v_mfma_f32_16x16x32_bf16 v[92:95], v[124:127], v[168:171], v[92:95]\n\t"
      "v_mfma_f32_16x16x32_bf16 v[84:87], v[136:139], v[160:163], v[84:87]\n\t"
      "v_mfma_f32_16x16x32_bf16 v[92:95], v[136:139], v[164:167], v[92:95]\n\t"
      "v_mfma_f32_16x16x32_bf16 v[92:95], v[148:151], v[160:163], v[92:95]\n\t"
      "global_load_lds_dwordx4 v63, %[dj1] offset:1024\n\t"
      "s_mov_b32 m0, s45\n\t"
      "s_waitcnt lgkmcnt(0)\n\t"
      "v_mfma_f32_16x16x32_bf16 v[64:67], v[112:115], v[176:179], v[64:67]\n\t"
      "v_mfma_f32_16x16x32_bf16 v[68:71], v[116:119], v[176:179], v[68:71]\n\t"
      "global_load_dwordx4 v[240:243], v48, s[42:43] offset:0 sc1\n\t"
      "v_mfma_f32_16x16x32_bf16 v[72:75], v[112:115], v[180:183], v[72:75]\n\t"
      "v_mfma_f32_16x16x32_bf16 v[72:75], v[124:127], v[176:179], v[72:75]\n\t"
      "global_load_dwordx4 v[244:247], v49, s[42:43] offset:0 sc1\n\t"
      "v_mfma_f32_16x16x32_bf16 v[76:79], v[116:119], v[180:183], v[76:79]\n\t"
      "ds_write_b128 v44, v[64:67] offset:0\n\t"
      "v_mfma_f32_16x16x32_bf16 v[76:79], v[128:131], v[176:179], v[76:79]\n\t"
      "global_load_dwordx4 v[252:255], v49, s[42:43] offset:1024 sc1\n\t"
      "v_mfma_f32_16x16x32_bf16 v[80:83], v[112:115], v[184:187], v[80:83]\n\t"
      "ds_write_b128 v59, v[68:71] offset:0\n\t"
      "v_mfma_f32_16x16x32_bf16 v[80:83], v[124:127], v[180:183], v[80:83]\n\t"
      "v_mfma_f32_16x16x32_bf16 v[80:83], v[136:139], v[176:179], v[80:83]\n\t"
      "ds_write_b128 v44, v[72:75] offset:2048\n\t"
      "v_mfma_f32_16x16x32_bf16 v[84:87], v[116:119], v[184:187], v[84:87]\n\t"
      "v_mfma_f32_16x16x32_bf16 v[84:87], v[128:131], v[180:183], v[84:87]\n\t"
      "ds_write_b128 v59, v[76:79] offset:2048\n\t"
      "v_mfma_f32_16x16x32_bf16 v[84:87], v[140:143], v[176:179], v[84:87]\n\t"
      "v_mfma_f32_16x16x32_bf16 v[88:91], v[112:115], v[188:191], v[88:91]\n\t"
      "ds_write_b128 v44, v[80:83] offset:4096\n\t"
      "v_mfma_f32_16x16x32_bf16 v[88:91], v[124:127], v[184:187], v[88:91]\n\t"
      "v_mfma_f32_16x16x32_bf16 v[88:91], v[136:139], v[180:183], v[88:91]\n\t"
      "v_mfma_f32_16x16x32_bf16 v[88:91], v[148:151], v[176:179], v[88:91]\n\t"
      "ds_write_b128 v59, v[84:87] offset:4096\n\t"
      "v_mfma_f32_16x16x32_bf16 v[92:95], v[116:119], v[188:191], v[92:95]\n\t"
      "v_mfma_f32_16x16x32_bf16 v[92:95], v[128:131], v[184:187], v[92:95]\n\t"
      "v_mfma_f32_16x16x32_bf16 v[92:95], v[140:143], v[180:183], v[92:95]\n\t"
      "v_mfma_f32_16x16x32_bf16 v[92:95], v[152:155], v[176:179], v[92:95]\n\t"
      "ds_write_b128 v44, v[88:91] offset:6144\n\t"
      "s_waitcnt vmcnt(5)\n\t"
      "s_nop 5\n\t"
      "ds_write_b128 v59, v[92:95] offset:6144\n\t"
      : "=&{v[96:111]}"(hp), "=&{v[192:223]}"(oa), "=&{v[224:239]}"(ob), "=&{v[248:251]}"(oc), "=&{v[36:39]}"(od), "=&{v[240:243]}"(f0),
        "=&{v[244:247]}"(f1), "=&{v[252:255]}"(f2)
      : [wlo] "s"(wslo), [whi] "s"(wshi), [side] "s"(side), [exd] "s"(32768 - w * 4096), [djm] "s"(__builtin_amdgcn_readfirstlane(lbase + L_DJ + w * 2048)), [dj0] "s"(wdn),
        [dj1] "s"(wdn + 48 * D_ - 512), "{v[40:55]}"(ia), "{v[56:63]}"(ib)
      : "memory", "scc", "s42", "s43", "s45", "v34", "v35", "v64", "v65", "v66", "v67", "v68", "v69", "v70", "v71", "v72", "v73", "v74", "v75", "v76", "v77", "v78", "v79", "v80", "v81", "v82", "v83", "v84", "v85", "v86", "v87", "v88", "v89", "v90", "v91", "v92", "v93", "v94", "v95", "v112", "v113", "v114", "v115", "v116", "v117", "v118", "v119", "v120", "v121", "v122", "v123", "v124", "v125", "v126", "v127", "v128", "v129", "v130", "v131", "v132", "v133", "v134", "v135", "v136", "v137", "v138", "v139", "v140", "v141", "v142", "v143", "v144", "v145", "v146", "v147", "v148", "v149", "v150", "v151", "v152", "v153", "v154", "v155", "v156", "v157", "v158", "v159", "v160", "v161", "v162", "v163", "v164", "v165", "v166", "v167", "v168", "v169", "v170", "v171", "v172", "v173", "v174", "v175", "v176", "v177", "v178", "v179", "v180", "v181", "v182", "v183", "v184", "v185", "v186", "v187", "v188", "v189", "v190", "v191");
  {
    auto ok = [&]() -> bool {
      unsigned d = modx((u32x4){oa[12], oa[13], oa[14], oa[15]}, od, T[0]);
      d |= (cks((u32x4){oa[0], oa[1], oa[2], oa[3]}, sh) ^ oc[0] ^ T[0]) | (cks((u32x4){oa[4], oa[5], oa[6], oa[7]}, sh) ^ oc[1] ^ T[0]);
      d |= cks2(oa[8], oa[9], lane) ^ oc[2] ^ T[0];
      d |= (oa[19] ^ T[0]) | (oa[23] ^ T[0]) | (oa[27] ^ T[0]) | (oa[31] ^ T[0]) | (ob[3] ^ T[0]) | (ob[7] ^ T[0]);
      return __all(d == 0);
    };
    if (__builtin_expect(!ok(), 0)) {
#pragma unroll 1
      for (int sp = 0; sp < SPIN_CAP; ++sp) {
        asm volatile("" ::: "memory");
        u32x4 x[9];
        x[0] = LD128(qoA); x[1] = LD128(qoA + 4096); x[2] = LD128(pmo);
        od = LD128(pmo + 16);
#pragma unroll
        for (int i = 0; i < 4; ++i) x[3 + i] = LD128(soA + i * 1024);
        x[7] = LD128(soH); x[8] = LD128(soH + 1024);
        oc[0] = LD32(fqo); oc[1] = LD32(fqo + 4); oc[2] = LD32(fso);
        const u32x2 y = LD64(qoH);
#pragma unroll
        for (int i = 0; i < 8; ++i) { oa[i] = x[i >> 2][i & 3]; ob[i] = x[7 + (i >> 2)][i & 3]; }
#pragma unroll
        for (int i = 0; i < 20; ++i) oa[12 + i] = x[2 + (i >> 2)][i & 3];
        oa[8] = y[0]; oa[9] = y[1];
        if (ok()) break;
      }
      asm volatile("" : "+v"(oa), "+v"(ob));
    }
  }
  const unsigned pm = __float_as_uint(modv((u32x4){oa[12], oa[13], oa[14], oa[15]}, od));
  auto x1_pair = [&](int it, unsigned q, f32x2 mean, f32x2 rstd, f32x2 sum, unsigned x) __attribute__((always_inline)) -> unsigned {
    const float sa = __uint_as_float(__builtin_amdgcn_readlane(pm, 4 * it)), ga = __uint_as_float(__builtin_amdgcn_readlane(pm, 4 * it + 2));
    const float c1 = rb1(1.0f + __uint_as_float(__builtin_amdgcn_readlane(pm, 4 * it + 1))), Dv = __uint_as_float(Dw[it]);
    const f32x2 qf = up2(q);
    f32x2 u = rb2(rb2(qf - mean) * rstd);
    u = rb2(rb2(u * c1) + sa);
    const f32x2 y = rb2(rb2(sum) + rb2((f32x2){hi16(x), lo16(x)} * Dv));
    u = qf + rb2(rb2(u * y) * ga);
    return san(pk2(u[0], u[1]));
  };
#define F2(v, i) ((f32x2){__uint_as_float(v[i]), __uint_as_float(v[(i) + 1])})
#define WL(x) asm volatile(x :: "v"(lane * 16), "s"(wplo), "s"(wphi) : "memory", "scc", "s40", "s41", "a0", "a1", "a2", "a3", "a4", "a5", "a6", "a7", "a8", "a9", "a10", "a11", "a12", "a13", "a14", "a15", "a16", "a17", "a18", "a19", "a20", "a21", "a22", "a23", "a24", "a25", "a26", "a27", "a28", "a29", "a30", "a31", "a32", "a33", "a34", "a35", "a36", "a37", "a38", "a39", "a40", "a41", "a42", "a43", "a44", "a45", "a46", "a47", "a48", "a49", "a50", "a51", "a52", "a53", "a54", "a55", "a56", "a57", "a58", "a59", "a60", "a61", "a62", "a63", "a64", "a65", "a66", "a67", "a68", "a69", "a70", "a71", "a72", "a73", "a74", "a75", "a76", "a77", "a78", "a79", "a80", "a81", "a82", "a83", "a84", "a85", "a86", "a87", "a88", "a89", "a90", "a91", "a92", "a93", "a94", "a95", "a96", "a97", "a98", "a99", "a100", "a101", "a102", "a103", "a104", "a105", "a106", "a107", "a108", "a109", "a110", "a111", "a112", "a113", "a114", "a115", "a116", "a117", "a118", "a119", "a120", "a121", "a122", "a123", "a124", "a125", "a126", "a127", "a128", "a129", "a130", "a131", "a132", "a133", "a134", "a135", "a136", "a137", "a138", "a139", "a140", "a141", "a142", "a143", "a144", "a145", "a146", "a147", "a148", "a149", "a150", "a151", "a152", "a153", "a154", "a155", "a156", "a157", "a158", "a159", "a160", "a161", "a162", "a163", "a164", "a165", "a166", "a167", "a168", "a169", "a170", "a171", "a172", "a173", "a174", "a175", "a176", "a177", "a178", "a179", "a180", "a181", "a182", "a183", "a184", "a185", "a186", "a187", "a188", "a189", "a190", "a191", "a192", "a193", "a194", "a195", "a196", "a197", "a198", "a199", "a200", "a201", "a202", "a203", "a204", "a205", "a206", "a207", "a208", "a209", "a210", "a211", "a212", "a213", "a214", "a215", "a216", "a217", "a218", "a219", "a220", "a221", "a222", "a223", "a224", "a225", "a226", "a227", "a228", "a229", "a230", "a231", "a232", "a233", "a234", "a235", "a236", "a237", "a238", "a239")
  f32x4 cp[8];
  bool wok;
  const unsigned fwo = WS_FW + xcd * 5120 + (bn * 20 + (lane < 20 ? lane : 19)) * 16;
  {
    u32x2 r;
#pragma unroll
    for (int e = 0; e < 2; ++e) {
      const f32x2 s = ((f32x2){hp[2 * e], hp[2 * e + 1]} + (f32x2){hp[4 + 2 * e], hp[5 + 2 * e]}) + ((f32x2){hp[8 + 2 * e], hp[9 + 2 * e]} + (f32x2){hp[12 + 2 * e], hp[13 + 2 * e]});
      r[e] = x1_pair(2, oa[8 + e], up2(ob[2 + 4 * e]), F2(ob, 4 * e), s, oa[11 - e]);
    }
    ST64(x1o[2], r);
    asm volatile("s_waitcnt vmcnt(1)" : "+v"(f0), "+v"(f1), "+v"(f2)::"memory");
    wok = __all(lane >= 8 || tagx(f0, T) == 0);
    LDS32(lds + L_FL + 16 + 4 * w) = __all((tagx(f1, T) | tagx(f2, T)) == 0);
    const unsigned cr = L_EX + w * 2048 + (lane >> 2) * 128 + ((2 * (lane & 3)) ^ ((lane >> 2) & 7)) * 16;
    BAR();
#pragma unroll
    for (int i = 0; i < 8; ++i) cp[i] = LDSF4(lds + (cr ^ ((i & 1) * 16)) + (i >> 1) * 8192);
  }
  const u32x4 xok = LDS128(lds + L_FL + 16);
  SCHEDB();
  {
    u32x4 r;
    auto piece = [&](int it, int e) __attribute__((always_inline)) {
      const f32x4 s = cp[4 * it + (e >> 1)] + cp[4 * it + 2 + (e >> 1)];
      unsigned q = oa[4 * it + e];
      asm volatile("" : "+v"(q));
      r[e] = x1_pair(it, q, up2(oa[18 + 4 * e]), F2(oa, 16 + 4 * e), (f32x2){s[2 * (e & 1)], s[2 * (e & 1) + 1]}, ob[8 + 4 * it + 3 - e]);
      asm volatile("" : "+v"(r[e]));
    };
    WL(
      "s_add_u32 s40, %1, 0\n\t"
      "s_addc_u32 s41, %2, 0\n\t"
      "global_load_dwordx4 a[0:3], %0, s[40:41] offset:0 sc1\n\t"
      "global_load_dwordx4 a[4:7], %0, s[40:41] offset:1024 sc1\n\t"
      "global_load_dwordx4 a[8:11], %0, s[40:41] offset:2048 sc1\n\t"
      "global_load_dwordx4 a[12:15], %0, s[40:41] offset:3072 sc1\n\t"
    );
    piece(0, 0);
    WL(
      "s_add_u32 s40, %1, 4096\n\t"
      "s_addc_u32 s41, %2, 0\n\t"
      "global_load_dwordx4 a[16:19], %0, s[40:41] offset:0 sc1\n\t"
      "global_load_dwordx4 a[20:23], %0, s[40:41] offset:1024 sc1\n\t"
      "global_load_dwordx4 a[24:27], %0, s[40:41] offset:2048 sc1\n\t"
      "global_load_dwordx4 a[28:31], %0, s[40:41] offset:3072 sc1\n\t"
    );
    piece(0, 1);
    WL(
      "s_add_u32 s40, %1, 8192\n\t"
      "s_addc_u32 s41, %2, 0\n\t"
      "global_load_dwordx4 a[32:35], %0, s[40:41] offset:0 sc1\n\t"
      "global_load_dwordx4 a[36:39], %0, s[40:41] offset:1024 sc1\n\t"
      "global_load_dwordx4 a[40:43], %0, s[40:41] offset:2048 sc1\n\t"
      "global_load_dwordx4 a[44:47], %0, s[40:41] offset:3072 sc1\n\t"
    );
    piece(0, 2);
    piece(0, 3);
    ST128(x1o[0], r);
    SLD128(f0, fwo, 0);
    WL(
      "s_add_u32 s40, %1, 12288\n\t"
      "s_addc_u32 s41, %2, 0\n\t"
      "global_load_dwordx4 a[48:51], %0, s[40:41] offset:0 sc1\n\t"
      "global_load_dwordx4 a[52:55], %0, s[40:41] offset:1024 sc1\n\t"
      "global_load_dwordx4 a[56:59], %0, s[40:41] offset:2048 sc1\n\t"
      "global_load_dwordx4 a[60:63], %0, s[40:41] offset:3072 sc1\n\t"
    );
    piece(1, 0);
    WL(
      "s_add_u32 s40, %1, 16384\n\t"
      "s_addc_u32 s41, %2, 0\n\t"
      "global_load_dwordx4 a[64:67], %0, s[40:41] offset:0 sc1\n\t"
      "global_load_dwordx4 a[68:71], %0, s[40:41] offset:1024 sc1\n\t"
      "global_load_dwordx4 a[72:75], %0, s[40:41] offset:2048 sc1\n\t"
      "global_load_dwordx4 a[76:79], %0, s[40:41] offset:3072 sc1\n\t"
    );
    piece(1, 1);
    WL(
      "s_add_u32 s40, %1, 20480\n\t"
      "s_addc_u32 s41, %2, 0\n\t"
      "global_load_dwordx4 a[80:83], %0, s[40:41] offset:0 sc1\n\t"
      "global_load_dwordx4 a[84:87], %0, s[40:41] offset:1024 sc1\n\t"
      "global_load_dwordx4 a[88:91], %0, s[40:41] offset:2048 sc1\n\t"
      "global_load_dwordx4 a[92:95], %0, s[40:41] offset:3072 sc1\n\t"
    );
    piece(1, 2);
    piece(1, 3);
    ST128(x1o[1], r);
    SCHEDB();
  }
  if (__builtin_expect(!__all((xok[0] & xok[1] & xok[2] & xok[3]) != 0), 0)) {
#pragma unroll 1
    for (int sp = 0; sp < SPIN_CAP; ++sp) {
      asm volatile("" ::: "memory");
      unsigned d = 0;
#pragma unroll
      for (int i = 0; i < 8; ++i) d |= tagx(LD128(WS_FP2 + xcd * 8192 + i * 1024 + lane * 16), T);
      if (__all(d == 0)) break;
    }
  }
  asm volatile("s_waitcnt vmcnt(13)" : "+v"(f0)::"memory");
  if (__builtin_expect(!__all(tagx(f0, T) == 0), 0)) {
#pragma unroll 1
    for (int sp = 0; sp < SPIN_CAP; ++sp) {
      asm volatile("" ::: "memory");
      if (__all(tagx(LD128(fwo), T) == 0)) break;
    }
  }
  if (__builtin_expect(!wok, 0)) {
    WL(
      "s_add_u32 s40, %1, 0\n\t"
      "s_addc_u32 s41, %2, 0\n\t"
      "global_load_dwordx4 a[0:3], %0, s[40:41] offset:0 sc1\n\t"
      "global_load_dwordx4 a[4:7], %0, s[40:41] offset:1024 sc1\n\t"
      "global_load_dwordx4 a[8:11], %0, s[40:41] offset:2048 sc1\n\t"
      "global_load_dwordx4 a[12:15], %0, s[40:41] offset:3072 sc1\n\t"
      "s_add_u32 s40, %1, 4096\n\t"
      "s_addc_u32 s41, %2, 0\n\t"
      "global_load_dwordx4 a[16:19], %0, s[40:41] offset:0 sc1\n\t"
      "global_load_dwordx4 a[20:23], %0, s[40:41] offset:1024 sc1\n\t"
      "global_load_dwordx4 a[24:27], %0, s[40:41] offset:2048 sc1\n\t"
      "global_load_dwordx4 a[28:31], %0, s[40:41] offset:3072 sc1\n\t"
      "s_add_u32 s40, %1, 8192\n\t"
      "s_addc_u32 s41, %2, 0\n\t"
      "global_load_dwordx4 a[32:35], %0, s[40:41] offset:0 sc1\n\t"
      "global_load_dwordx4 a[36:39], %0, s[40:41] offset:1024 sc1\n\t"
      "global_load_dwordx4 a[40:43], %0, s[40:41] offset:2048 sc1\n\t"
      "global_load_dwordx4 a[44:47], %0, s[40:41] offset:3072 sc1\n\t"
      "s_add_u32 s40, %1, 12288\n\t"
      "s_addc_u32 s41, %2, 0\n\t"
      "global_load_dwordx4 a[48:51], %0, s[40:41] offset:0 sc1\n\t"
      "global_load_dwordx4 a[52:55], %0, s[40:41] offset:1024 sc1\n\t"
      "global_load_dwordx4 a[56:59], %0, s[40:41] offset:2048 sc1\n\t"
      "global_load_dwordx4 a[60:63], %0, s[40:41] offset:3072 sc1\n\t"
      "s_add_u32 s40, %1, 16384\n\t"
      "s_addc_u32 s41, %2, 0\n\t"
      "global_load_dwordx4 a[64:67], %0, s[40:41] offset:0 sc1\n\t"
      "global_load_dwordx4 a[68:71], %0, s[40:41] offset:1024 sc1\n\t"
      "global_load_dwordx4 a[72:75], %0, s[40:41] offset:2048 sc1\n\t"
      "global_load_dwordx4 a[76:79], %0, s[40:41] offset:3072 sc1\n\t"
      "s_add_u32 s40, %1, 20480\n\t"
      "s_addc_u32 s41, %2, 0\n\t"
      "global_load_dwordx4 a[80:83], %0, s[40:41] offset:0 sc1\n\t"
      "global_load_dwordx4 a[84:87], %0, s[40:41] offset:1024 sc1\n\t"
      "global_load_dwordx4 a[88:91], %0, s[40:41] offset:2048 sc1\n\t"
      "global_load_dwordx4 a[92:95], %0, s[40:41] offset:3072 sc1\n\t"
    );
  }
  const size_t xb = (size_t)ws + WS_X1T + (size_t)bm * 163840;
  asm volatile(
      "s_mov_b32 s45, m0\n\t"
      "s_add_u32 m0, %[m0b], 0\n\t"
      "s_add_u32 s42, %[xlo], 0\n\t"
      "s_addc_u32 s43, %[xhi], 0\n\t"
      "global_load_lds_dwordx4 %[voff], s[42:43] offset:0 sc1\n\t"
      "global_load_lds_dwordx4 %[voff], s[42:43] offset:1024 sc1\n\t"
      "global_load_lds_dwordx4 %[voff], s[42:43] offset:2048 sc1\n\t"
      "global_load_lds_dwordx4 %[voff], s[42:43] offset:3072 sc1\n\t"
      "s_add_u32 m0, %[m0b], 16384\n\t"
      "s_add_u32 s42, %[xlo], 16384\n\t"
      "s_addc_u32 s43, %[xhi], 0\n\t"
      "global_load_lds_dwordx4 %[voff], s[42:43] offset:0 sc1\n\t"
      "global_load_lds_dwordx4 %[voff], s[42:43] offset:1024 sc1\n\t"
      "global_load_lds_dwordx4 %[voff], s[42:43] offset:2048 sc1\n\t"
      "global_load_lds_dwordx4 %[voff], s[42:43] offset:3072 sc1\n\t"
      "s_mov_b32 m0, s45\n\t"
      :: [voff] "v"(w * 4096 + lane * 16), [xlo] "s"((unsigned)xb), [xhi] "s"((unsigned)(xb >> 32)), [m0b] "s"(lbase + w * 4096) : "memory", "scc", "s42", "s43", "s45");
  WL(
""
  );
  const unsigned hho = WS_HH + (unsigned)(((2 * bm + (w >> 1)) * 48 + 3 * bn) * 4 + (w & 1)) * 1024 + lane * 16;
  const size_t wb = (size_t)ws + WS_WD + (size_t)(bn2 * 48 + 12 * w) * 5120;
  const size_t hb = (size_t)ws + WS_HH + (size_t)(bm2 * 48 + 12 * w) * 4096;
  const int c24 = (lane & 31) < 24 ? (lane & 31) : 23;
  u32x8 aad, px;
  {
#pragma unroll
    for (int j = 0; j < 8; ++j) aad[j] = lbase + (j >> 2) * 65536 + (4 * fq + q4) * 128 + ((j ^ w ^ (2 * fq + (q4 >> 1))) & 3) * 32 + p4 * 8;
    px[0] = L < 240 ? WS_FD + (lane & 7) * 7680 + (d_ks * 20 + d_cb) * 8 : WS_FP1 + 8192 + (b * 4 + w) * 8;
    px[1] = lbase + L_DJ + w * 2048 + (4 * fq + q4) * 64 + p4 * 8;
#pragma unroll
    for (int c = 0; c < 2; ++c) {
      const int tc = 2 * d_cb + c, b5 = (tc * 205) >> 10;
      px[2 + c] = WS_WD + (L < 240 ? (unsigned)((b5 * 48 + d_ks) * 5 + tc - 5 * b5) : 1920u) * 1024 + lane * 16;
    }
    px[4] = WS_PART + (unsigned)(lane >> 5) * 245760 + (c24 >= 12 ? 12288 - 192 : 0) + (bn * 192 + 48 * w + 4 * c24) * 4;
    const int l3 = ((lane < 36 ? lane : 35) * 43) >> 7;
    px[5] = WS_FD + xcd * 7680 + ((12 * w + l3) * 20 + ((5 * bn2) >> 1) + (lane < 36 ? lane : 35) - 3 * l3) * 8;
    px[6] = WS_MOD + (3200 + bn2 * 80 + (lane < 40 ? lane : 39) * 2) * 32;
    px[7] = 0;
  }
  const unsigned hco = WS_HC + (unsigned)(bm2 * 16 + 4 * w) * 512 + lane * 4;
#define G1_IN [vchk] "v"(lbase + w * 4096 + lane * 16), [voff] "v"(w * 4096 + lane * 16), [l16] "v"(lane * 16), [T] "v"(T), [xlo] "s"((unsigned)xb),       \
              [xhi] "s"((unsigned)(xb >> 32)), [dlo] "s"((unsigned)wb), [dhi] "s"((unsigned)(wb >> 32)), [m0b] "s"(lbase + w * 4096), [plo] "s"(wplo),     \
              [phi] "s"(wphi), [wlo] "s"(wslo), [whi] "s"(wshi), [hlo] "s"((unsigned)hb), [hhi] "s"((unsigned)(hb >> 32)), [cko] "v"(hco), [wsb] "s"(ws),               \
              "{v[40:47]}"(aad), "{v[32:39]}"(px)
#define G1_CL "memory", "scc", "vcc", "s36", "s37", "s38", "s39", "s40", "s41", "s42", "s43", "s44", "s45", "s46", "s47", "s50", "s51"
  f32x32 accA;
  f32x16 accB;
  f32x8 accS;
  f32x4 cs[3], bs[3];
  u32x32 h0, h1, h2;
  u32x4 hc;
#define EPB(x) asm volatile(x : "+{v[64:95]}"(h0), "+{v[96:127]}"(h1), "+{v[184:187]}"(hc)                                                     \
                            : [l16] "v"(lane * 16), [hlo] "s"((unsigned)hb), [hhi] "s"((unsigned)(hb >> 32)), [dlo] "s"((unsigned)wb),         \
                              [dhi] "s"((unsigned)(wb >> 32)), [cko] "v"(hco), [wsb] "s"(ws) : "memory", "scc", "s38", "s39", "s40", "s41", "a0", "a1", "a2", "a3", "a4", "a5", "a6", "a7", "a8", "a9", "a10", "a11", "a12", "a13", "a14", "a15", "a16", "a17", "a18", "a19", "a20", "a21", "a22", "a23", "a24", "a25", "a26", "a27", "a28", "a29", "a30", "a31", "a32", "a33", "a34", "a35", "a36", "a37", "a38", "a39", "a40", "a41", "a42", "a43", "a44", "a45", "a46", "a47", "a48", "a49", "a50", "a51", "a52", "a53", "a54", "a55", "a56", "a57", "a58", "a59", "a60", "a61", "a62", "a63", "a64", "a65", "a66", "a67", "a68", "a69", "a70", "a71", "a72", "a73", "a74", "a75", "a76", "a77", "a78", "a79", "a80", "a81", "a82", "a83", "a84", "a85", "a86", "a87", "a88", "a89", "a90", "a91", "a92", "a93", "a94", "a95", "a96", "a97", "a98", "a99", "a100", "a101", "a102", "a103", "a104", "a105", "a106", "a107", "a108", "a109", "a110", "a111", "a112", "a113", "a114", "a115", "a116", "a117", "a118", "a119", "a120", "a121", "a122", "a123", "a124", "a125", "a126", "a127", "a128", "a129", "a130", "a131", "a132", "a133", "a134", "a135", "a136", "a137", "a138", "a139", "a140", "a141", "a142", "a143", "a144", "a145", "a146", "a147", "a148", "a149", "a150", "a151", "a152", "a153", "a154", "a155", "a156", "a157", "a158", "a159", "a160", "a161", "a162", "a163", "a164", "a165", "a166", "a167", "a168", "a169", "a170", "a171", "a172", "a173", "a174", "a175", "a176", "a177", "a178", "a179", "a180", "a181", "a182", "a183", "a184", "a185", "a186", "a187", "a188", "a189", "a190", "a191", "a192", "a193", "a194", "a195", "a196", "a197", "a198", "a199", "a200", "a201", "a202", "a203", "a204", "a205", "a206", "a207", "a208", "a209", "a210", "a211", "a212", "a213", "a214", "a215", "a216", "a217", "a218", "a219", "a220", "a221", "a222", "a223", "a224", "a225", "a226", "a227", "a228", "a229", "a230", "a231", "a232", "a233", "a234", "a235", "a236", "a237", "a238", "a239")
  auto g1_epi = [&](int ps) __attribute__((always_inline)) {
    if (ps) EPB(
      "global_load_dword v184, %[cko], %[wsb] offset:0 sc1\n\t"
      "global_load_dword v185, %[cko], %[wsb] offset:512 sc1\n\t"
      "s_add_u32 s38, %[hlo], 28672\n\t"
      "s_addc_u32 s39, %[hhi], 0\n\t"
      "global_load_dwordx4 v[80:83], %[l16], s[38:39] offset:-4096 sc1\n\t"
      "global_load_dwordx4 v[84:87], %[l16], s[38:39] offset:-3072 sc1\n\t"
      "s_nop 0\n\t"
    );
    {
      const u64 m1 = __ballot(ip & 1), m2 = __ballot(ip & 2);
      float s01, s23, sq;
      asm("v_cndmask_b32 %0, %1, %2, %3" : "=v"(s01) : "v"(accS[4]), "v"(accS[5]), "s"(m1));
      asm("v_cndmask_b32 %0, %1, %2, %3" : "=v"(s23) : "v"(accS[6]), "v"(accS[7]), "s"(m1));
      asm("v_cndmask_b32 %0, %1, %2, %3" : "=v"(sq) : "v"(s01), "v"(s23), "s"(m2));
      const float mean = accS[0] * (1.0f / D_);
      float var = sq * (1.0f / D_) - mean * mean;
      var = var > 0.f ? var : 0.f;
      f32x2 rsv = {rb1(mean), __builtin_amdgcn_rcpf(rb1(__builtin_amdgcn_sqrtf(rb1(rb1(var) + 1e-5f))))};
      asm volatile("" : "+v"(rsv));
      if (fq == q4) LDSF2(lds + L_RS + (16 * w + ip) * 8) = rsv;
    }
#pragma unroll
    for (int n = 0; n < 3; ++n) {
      cs[n] = LDSF4(lds + L_CB + (w * 24 + 4 * n + fq) * 16);
      bs[n] = LDSF4(lds + L_CB + (w * 24 + 12 + 4 * n + fq) * 16);
    }
    asm volatile("" : "+v"(cs[0]), "+v"(cs[1]), "+v"(cs[2]), "+v"(bs[0]), "+v"(bs[1]), "+v"(bs[2]));
    if (ps) EPB(
      "s_add_u32 s38, %[hlo], 28672\n\t"
      "s_addc_u32 s39, %[hhi], 0\n\t"
      "global_load_dwordx4 v[88:91], %[l16], s[38:39] offset:0 sc1\n\t"
      "global_load_dwordx4 v[92:95], %[l16], s[38:39] offset:1024 sc1\n\t"
      "s_add_u32 s38, %[hlo], 36864\n\t"
      "s_addc_u32 s39, %[hhi], 0\n\t"
      "global_load_dwordx4 v[96:99], %[l16], s[38:39] offset:-4096 sc1\n\t"
      "global_load_dwordx4 v[100:103], %[l16], s[38:39] offset:-3072 sc1\n\t"
      "s_nop 0\n\t"
    );
    BAR();
    f32x2 r[4];
#pragma unroll
    for (int j = 0; j < 4; ++j) r[j] = LDSF2(lds + L_RS + (16 * (j ^ w) + ip) * 8);
#pragma unroll
    for (int j = 0; j < 4; ++j) {
      if (ps && j == 0) EPB(
      "global_load_dword v186, %[cko], %[wsb] offset:1024 sc1\n\t"
      "s_add_u32 s38, %[hlo], 40960\n\t"
      "s_addc_u32 s39, %[hhi], 0\n\t"
      "global_load_dwordx4 v[104:107], %[l16], s[38:39] offset:-4096 sc1\n\t"
      "global_load_dwordx4 v[108:111], %[l16], s[38:39] offset:-3072 sc1\n\t"
      "global_load_dwordx4 v[112:115], %[l16], s[38:39] offset:0 sc1\n\t"
      "s_nop 0\n\t"
      );
      if (ps && j == 2) EPB(
      "s_add_u32 s38, %[hlo], 40960\n\t"
      "s_addc_u32 s39, %[hhi], 0\n\t"
      "global_load_dwordx4 v[116:119], %[l16], s[38:39] offset:1024 sc1\n\t"
      "s_add_u32 s38, %[hlo], 49152\n\t"
      "s_addc_u32 s39, %[hhi], 0\n\t"
      "global_load_dwordx4 v[120:123], %[l16], s[38:39] offset:-4096 sc1\n\t"
      "global_load_dwordx4 v[124:127], %[l16], s[38:39] offset:-3072 sc1\n\t"
      "global_load_dword v187, %[cko], %[wsb] offset:1536 sc1\n\t"
      "s_nop 0\n\t"
      );
      lds_char* p = lds + L_HX + ((j ^ w) * 192 + lane) * 16 + 4 * w;
#pragma unroll
      for (int n = 0; n < 3; ++n) {
        const int o = 16 * n + 4 * j;
        const f32x4 a = n < 2 ? (f32x4){accA[o & 31], accA[(o + 1) & 31], accA[(o + 2) & 31], accA[(o + 3) & 31]}
                              : (f32x4){accB[o & 15], accB[(o + 1) & 15], accB[(o + 2) & 15], accB[(o + 3) & 15]};
        const f32x4 g = (a - r[j][0] * cs[n]) * r[j][1] + bs[n];
        LDS32(p + n * 1024) = pk2(g[0] * g[2], g[1] * g[3]);
      }
    }
    BAR();
    unsigned c = T[0];
#pragma unroll
    for (int i = 0; i < 6; ++i) {
      const u32x4 d = LDS128(lds + L_HX + ((3 * (i < 3 ? w : w ^ 1) + i % 3) * 64 + lane) * 16);
      if (i < 3) ST128(hho + i * 4096 + ps * 2048, d);
      c += (d[0] + d[1]) + (d[2] + d[3]);
    }
    ST32((w & 1) ? 0x80000000u : WS_HC + (unsigned)((2 * bm + (w >> 1)) * 16 + bn) * 512 + ps * 256 + lane * 4, c);
    SCHEDB();
  };
  {
    f32x32 pa;
    f32x8 pb;
    u32x2 fdv;
    u32x16 gv;
    asm volatile(
      "s_mov_b32 s45, m0\n\t"
      "s_mov_b32 s44, 0x40000\n\t"
      "v_add_u32 v57, 0x10000, %[vchk]\n\t"
      "v_mov_b32 v52, 0x3f803f80\n\t"
      "v_mov_b32 v53, 0x3f803f80\n\t"
      "v_mov_b32 v54, 0x3f803f80\n\t"
      "v_mov_b32 v55, 0x3f803f80\n\t"
      "s_waitcnt vmcnt(4)\n\t"
      "ds_read_b128 v[240:243], %[vchk] offset:0\n\t"
      "ds_read_b128 v[244:247], %[vchk] offset:1024\n\t"
      "ds_read_b128 v[248:251], %[vchk] offset:2048\n\t"
      "ds_read_b128 v[252:255], %[vchk] offset:3072\n\t"
      "s_waitcnt lgkmcnt(0)\n\t"
      "v_max3_u32 v56, v240, v241, v242\n\t"
      "v_max3_u32 v56, v56, v243, v244\n\t"
      "v_max3_u32 v56, v56, v245, v246\n\t"
      "v_max3_u32 v56, v56, v247, v248\n\t"
      "v_max3_u32 v56, v56, v249, v250\n\t"
      "v_max3_u32 v56, v56, v251, v252\n\t"
      "v_max3_u32 v56, v56, v253, v254\n\t"
      "v_max_u32 v56, v56, v255\n\t"
      "v_cmp_eq_u32 vcc, -1, v56\n\t"
      "s_cbranch_vccnz L_g1_slow0_%=\n\t"
      "L_g1_back0_%=:\n\t"
      "s_barrier\n\t"
      "ds_read_b64_tr_b16 v[64:65], v40 offset:0\n\t"
      "ds_read_b64_tr_b16 v[66:67], v40 offset:2048\n\t"
      "ds_read_b64_tr_b16 v[68:69], v41 offset:0\n\t"
      "ds_read_b64_tr_b16 v[70:71], v41 offset:2048\n\t"
      "ds_read_b64_tr_b16 v[72:73], v42 offset:0\n\t"
      "ds_read_b64_tr_b16 v[74:75], v42 offset:2048\n\t"
      "ds_read_b64_tr_b16 v[76:77], v43 offset:0\n\t"
      "ds_read_b64_tr_b16 v[78:79], v43 offset:2048\n\t"
      "ds_read_b64_tr_b16 v[80:81], v40 offset:4096\n\t"
      "ds_read_b64_tr_b16 v[82:83], v40 offset:6144\n\t"
      "ds_read_b64_tr_b16 v[84:85], v41 offset:4096\n\t"
      "ds_read_b64_tr_b16 v[86:87], v41 offset:6144\n\t"
      "ds_read_b64_tr_b16 v[88:89], v42 offset:4096\n\t"
      "ds_read_b64_tr_b16 v[90:91], v42 offset:6144\n\t"
      "ds_read_b64_tr_b16 v[92:93], v43 offset:4096\n\t"
      "ds_read_b64_tr_b16 v[94:95], v43 offset:6144\n\t"
      "s_waitcnt lgkmcnt(0)\n\t"
      "v_mfma_f32_16x16x32_bf16 v[128:131], a[0:3], v[64:67], 0\n\t"
      "ds_read_b64_tr_b16 v[96:97], v40 offset:8192\n\t"
      "s_add_u32 m0, %[m0b], 32768\n\t"
      "s_add_u32 s36, %[xlo], 32768\n\t"
      "v_mfma_f32_16x16x32_bf16 v[144:147], a[4:7], v[64:67], 0\n\t"
      "ds_read_b64_tr_b16 v[98:99], v40 offset:10240\n\t"
      "v_mfma_f32_16x16x32_bf16 v[160:163], a[8:11], v[64:67], 0\n\t"
      "s_addc_u32 s37, %[xhi], 0\n\t"
      "global_load_lds_dwordx4 %[voff], s[36:37] offset:0 sc1\n\t"
      "v_mfma_f32_16x16x32_bf16 v[176:179], v[52:55], v[64:67], 0\n\t"
      "ds_read_b64_tr_b16 v[100:101], v41 offset:8192\n\t"
      "ds_read_b64_tr_b16 v[102:103], v41 offset:10240\n\t"
      "ds_read_b64_tr_b16 v[104:105], v42 offset:8192\n\t"
      "v_mfma_f32_16x16x32_bf16 v[180:183], v[64:67], v[64:67], 0\n\t"
      "global_load_lds_dwordx4 %[voff], s[36:37] offset:1024 sc1\n\t"
      "ds_read_b64_tr_b16 v[106:107], v42 offset:10240\n\t"
      "v_mfma_f32_16x16x32_bf16 v[132:135], a[0:3], v[68:71], 0\n\t"
      "ds_read_b64_tr_b16 v[108:109], v43 offset:8192\n\t"
      "s_add_u32 s38, %[plo], 28672\n\t"
      "s_addc_u32 s39, %[phi], 0\n\t"
      "v_mfma_f32_16x16x32_bf16 v[148:151], a[4:7], v[68:71], 0\n\t"
      "ds_read_b64_tr_b16 v[110:111], v43 offset:10240\n\t"
      "global_load_dwordx4 a[96:99], %[l16], s[38:39] offset:-4096 sc1\n\t"
      "v_mfma_f32_16x16x32_bf16 v[164:167], a[8:11], v[68:71], 0\n\t"
      "ds_read_b64_tr_b16 v[112:113], v40 offset:12288\n\t"
      "ds_read_b64_tr_b16 v[114:115], v40 offset:14336\n\t"
      "v_mfma_f32_16x16x32_bf16 v[136:139], a[0:3], v[72:75], 0\n\t"
      "global_load_dwordx4 a[100:103], %[l16], s[38:39] offset:-3072 sc1\n\t"
      "ds_read_b64_tr_b16 v[116:117], v41 offset:12288\n\t"
      "v_mfma_f32_16x16x32_bf16 v[152:155], a[4:7], v[72:75], 0\n\t"
      "ds_read_b64_tr_b16 v[118:119], v41 offset:14336\n\t"
      "global_load_dwordx4 a[104:107], %[l16], s[38:39] offset:-2048 sc1\n\t"
      "v_mfma_f32_16x16x32_bf16 v[168:171], a[8:11], v[72:75], 0\n\t"
      "ds_read_b64_tr_b16 v[120:121], v42 offset:12288\n\t"
      "ds_read_b64_tr_b16 v[122:123], v42 offset:14336\n\t"
      "v_mfma_f32_16x16x32_bf16 v[140:143], a[0:3], v[76:79], 0\n\t"
      "global_load_dwordx4 a[108:111], %[l16], s[38:39] offset:-1024 sc1\n\t"
      "ds_read_b64_tr_b16 v[124:125], v43 offset:12288\n\t"
      "v_mfma_f32_16x16x32_bf16 v[156:159], a[4:7], v[76:79], 0\n\t"
      "global_load_dwordx4 a[112:115], %[l16], s[38:39] offset:0 sc1\n\t"
      "ds_read_b64_tr_b16 v[126:127], v43 offset:14336\n\t"
      "v_mfma_f32_16x16x32_bf16 v[172:175], a[8:11], v[76:79], 0\n\t"
      "ds_read_b64_tr_b16 v[184:185], v33 offset:0\n\t"
      "ds_read_b64_tr_b16 v[186:187], v33 offset:1024\n\t"
      "ds_read_b64_tr_b16 v[188:189], v33 offset:32\n\t"
      "v_mfma_f32_16x16x32_bf16 v[128:131], a[12:15], v[80:83], v[128:131]\n\t"
      "ds_read_b64_tr_b16 v[190:191], v33 offset:1056\n\t"
      "v_mfma_f32_16x16x32_bf16 v[144:147], a[16:19], v[80:83], v[144:147]\n\t"
      "v_mfma_f32_16x16x32_bf16 v[160:163], a[20:23], v[80:83], v[160:163]\n\t"
      "v_mfma_f32_16x16x32_bf16 v[176:179], v[52:55], v[80:83], v[176:179]\n\t"
      "s_waitcnt vmcnt(7)\n\t"
      "ds_read_b128 v[240:243], %[vchk] offset:16384\n\t"
      "v_mfma_f32_16x16x32_bf16 v[180:183], v[80:83], v[80:83], v[180:183]\n\t"
      "ds_read_b128 v[244:247], %[vchk] offset:17408\n\t"
      "ds_read_b128 v[248:251], %[vchk] offset:18432\n\t"
      "v_mfma_f32_16x16x32_bf16 v[132:135], a[12:15], v[84:87], v[132:135]\n\t"
      "ds_read_b128 v[252:255], %[vchk] offset:19456\n\t"
      "v_mfma_f32_16x16x32_bf16 v[148:151], a[16:19], v[84:87], v[148:151]\n\t"
      "v_mfma_f32_16x16x32_bf16 v[164:167], a[20:23], v[84:87], v[164:167]\n\t"
      "v_mfma_f32_16x16x32_bf16 v[136:139], a[12:15], v[88:91], v[136:139]\n\t"
      "s_waitcnt lgkmcnt(0)\n\t"
      "v_max3_u32 v56, v240, v241, v242\n\t"
      "v_mfma_f32_16x16x32_bf16 v[152:155], a[16:19], v[88:91], v[152:155]\n\t"
      "v_max3_u32 v56, v56, v243, v244\n\t"
      "v_max3_u32 v56, v56, v245, v246\n\t"
      "v_mfma_f32_16x16x32_bf16 v[168:171], a[20:23], v[88:91], v[168:171]\n\t"
      "v_max3_u32 v56, v56, v247, v248\n\t"
      "v_max3_u32 v56, v56, v249, v250\n\t"
      "v_mfma_f32_16x16x32_bf16 v[140:143], a[12:15], v[92:95], v[140:143]\n\t"
      "v_max3_u32 v56, v56, v251, v252\n\t"
      "v_max3_u32 v56, v56, v253, v254\n\t"
      "v_mfma_f32_16x16x32_bf16 v[156:159], a[16:19], v[92:95], v[156:159]\n\t"
      "v_max_u32 v56, v56, v255\n\t"
      "v_cmp_eq_u32 vcc, -1, v56\n\t"
      "v_mfma_f32_16x16x32_bf16 v[172:175], a[20:23], v[92:95], v[172:175]\n\t"
      "s_cbranch_vccnz L_g1_slow1_%=\n\t"
      "L_g1_back1_%=:\n\t"
      "s_waitcnt lgkmcnt(0)\n\t"
      "s_barrier\n\t"
      "v_mfma_f32_16x16x32_bf16 v[128:131], a[24:27], v[96:99], v[128:131]\n\t"
      "ds_read_b64_tr_b16 v[64:65], v40 offset:16384\n\t"
      "v_mfma_f32_16x16x32_bf16 v[144:147], a[28:31], v[96:99], v[144:147]\n\t"
      "s_add_u32 m0, %[m0b], 32768\n\t"
      "s_add_u32 s36, %[xlo], 32768\n\t"
      "v_mfma_f32_16x16x32_bf16 v[160:163], a[32:35], v[96:99], v[160:163]\n\t"
      "ds_read_b64_tr_b16 v[66:67], v40 offset:18432\n\t"
      "v_mfma_f32_16x16x32_bf16 v[176:179], v[52:55], v[96:99], v[176:179]\n\t"
      "s_addc_u32 s37, %[xhi], 0\n\t"
      "global_load_lds_dwordx4 %[voff], s[36:37] offset:2048 sc1\n\t"
      "v_mfma_f32_16x16x32_bf16 v[180:183], v[96:99], v[96:99], v[180:183]\n\t"
      "ds_read_b64_tr_b16 v[68:69], v41 offset:16384\n\t"
      "ds_read_b64_tr_b16 v[70:71], v41 offset:18432\n\t"
      "v_mfma_f32_16x16x32_bf16 v[132:135], a[24:27], v[100:103], v[132:135]\n\t"
      "ds_read_b64_tr_b16 v[72:73], v42 offset:16384\n\t"
      "v_mfma_f32_16x16x32_bf16 v[148:151], a[28:31], v[100:103], v[148:151]\n\t"
      "global_load_lds_dwordx4 %[voff], s[36:37] offset:3072 sc1\n\t"
      "v_mfma_f32_16x16x32_bf16 v[164:167], a[32:35], v[100:103], v[164:167]\n\t"
      "ds_read_b64_tr_b16 v[74:75], v42 offset:18432\n\t"
      "v_mfma_f32_16x16x32_bf16 v[136:139], a[24:27], v[104:107], v[136:139]\n\t"
      "global_load_dwordx4 a[116:119], %[l16], s[38:39] offset:1024 sc1\n\t"
      "v_mfma_f32_16x16x32_bf16 v[152:155], a[28:31], v[104:107], v[152:155]\n\t"
      "ds_read_b64_tr_b16 v[76:77], v43 offset:16384\n\t"
      "ds_read_b64_tr_b16 v[78:79], v43 offset:18432\n\t"
      "v_mfma_f32_16x16x32_bf16 v[168:171], a[32:35], v[104:107], v[168:171]\n\t"
      "global_load_dwordx4 a[120:123], %[l16], s[38:39] offset:2048 sc1\n\t"
      "v_mfma_f32_16x16x32_bf16 v[140:143], a[24:27], v[108:111], v[140:143]\n\t"
      "ds_read_b64_tr_b16 v[80:81], v40 offset:20480\n\t"
      "v_mfma_f32_16x16x32_bf16 v[156:159], a[28:31], v[108:111], v[156:159]\n\t"
      "global_load_dwordx4 a[124:127], %[l16], s[38:39] offset:3072 sc1\n\t"
      "v_mfma_f32_16x16x32_bf16 v[172:175], a[32:35], v[108:111], v[172:175]\n\t"
      "ds_read_b64_tr_b16 v[82:83], v40 offset:22528\n\t"
      "ds_read_b64_tr_b16 v[84:85], v41 offset:20480\n\t"
      "v_mfma_f32_16x16x32_bf16 v[128:131], a[36:39], v[112:115], v[128:131]\n\t"
      "s_add_u32 s38, %[plo], 36864\n\t"
      "s_addc_u32 s39, %[phi], 0\n\t"
      "v_mfma_f32_16x16x32_bf16 v[144:147], a[40:43], v[112:115], v[144:147]\n\t"
      "ds_read_b64_tr_b16 v[86:87], v41 offset:22528\n\t"
      "v_mfma_f32_16x16x32_bf16 v[160:163], a[44:47], v[112:115], v[160:163]\n\t"
      "global_load_dwordx4 a[128:131], %[l16], s[38:39] offset:-4096 sc1\n\t"
      "v_mfma_f32_16x16x32_bf16 v[176:179], v[52:55], v[112:115], v[176:179]\n\t"
      "ds_read_b64_tr_b16 v[88:89], v42 offset:20480\n\t"
      "ds_read_b64_tr_b16 v[90:91], v42 offset:22528\n\t"
      "v_mfma_f32_16x16x32_bf16 v[180:183], v[112:115], v[112:115], v[180:183]\n\t"
      "global_load_dwordx4 a[132:135], %[l16], s[38:39] offset:-3072 sc1\n\t"
      "v_mfma_f32_16x16x32_bf16 v[132:135], a[36:39], v[116:119], v[132:135]\n\t"
      "ds_read_b64_tr_b16 v[92:93], v43 offset:20480\n\t"
      "ds_read_b64_tr_b16 v[94:95], v43 offset:22528\n\t"
      "v_mfma_f32_16x16x32_bf16 v[148:151], a[40:43], v[116:119], v[148:151]\n\t"
      "s_mov_b32 s42, %[wlo]\n\t"
      "s_mov_b32 s43, %[whi]\n\t"
      "v_mfma_f32_16x16x32_bf16 v[164:167], a[44:47], v[116:119], v[164:167]\n\t"
      "global_store_dwordx4 v34, v[184:187], s[42:43] sc1\n\t"
      "v_mfma_f32_16x16x32_bf16 v[136:139], a[36:39], v[120:123], v[136:139]\n\t"
      "global_store_dwordx4 v35, v[188:191], s[42:43] sc1\n\t"
      "v_mfma_f32_16x16x32_bf16 v[152:155], a[40:43], v[120:123], v[152:155]\n\t"
      "v_mfma_f32_16x16x32_bf16 v[168:171], a[44:47], v[120:123], v[168:171]\n\t"
      "v_mfma_f32_16x16x32_bf16 v[140:143], a[36:39], v[124:127], v[140:143]\n\t"
      "v_mfma_f32_16x16x32_bf16 v[156:159], a[40:43], v[124:127], v[156:159]\n\t"
      "v_mfma_f32_16x16x32_bf16 v[172:175], a[44:47], v[124:127], v[172:175]\n\t"
      "s_waitcnt lgkmcnt(0)\n\t"
      "v_mfma_f32_16x16x32_bf16 v[128:131], a[48:51], v[64:67], v[128:131]\n\t"
      "ds_read_b64_tr_b16 v[96:97], v40 offset:24576\n\t"
      "v_mfma_f32_16x16x32_bf16 v[144:147], a[52:55], v[64:67], v[144:147]\n\t"
      "s_add_u32 m0, %[m0b], 49152\n\t"
      "s_add_u32 s36, %[xlo], 49152\n\t"
      "v_mfma_f32_16x16x32_bf16 v[160:163], a[56:59], v[64:67], v[160:163]\n\t"
      "ds_read_b64_tr_b16 v[98:99], v40 offset:26624\n\t"
      "ds_read_b64_tr_b16 v[100:101], v41 offset:24576\n\t"
      "v_mfma_f32_16x16x32_bf16 v[176:179], v[52:55], v[64:67], v[176:179]\n\t"
      "s_addc_u32 s37, %[xhi], 0\n\t"
      "global_load_lds_dwordx4 %[voff], s[36:37] offset:0 sc1\n\t"
      "v_mfma_f32_16x16x32_bf16 v[180:183], v[64:67], v[64:67], v[180:183]\n\t"
      "ds_read_b64_tr_b16 v[102:103], v41 offset:26624\n\t"
      "ds_read_b64_tr_b16 v[104:105], v42 offset:24576\n\t"
      "v_mfma_f32_16x16x32_bf16 v[132:135], a[48:51], v[68:71], v[132:135]\n\t"
      "ds_read_b64_tr_b16 v[106:107], v42 offset:26624\n\t"
      "v_mfma_f32_16x16x32_bf16 v[148:151], a[52:55], v[68:71], v[148:151]\n\t"
      "global_load_lds_dwordx4 %[voff], s[36:37] offset:1024 sc1\n\t"
      "v_mfma_f32_16x16x32_bf16 v[164:167], a[56:59], v[68:71], v[164:167]\n\t"
      "ds_read_b64_tr_b16 v[108:109], v43 offset:24576\n\t"
      "ds_read_b64_tr_b16 v[110:111], v43 offset:26624\n\t"
      "v_mfma_f32_16x16x32_bf16 v[136:139], a[48:51], v[72:75], v[136:139]\n\t"
      "ds_read_b64_tr_b16 v[112:113], v40 offset:28672\n\t"
      "v_mfma_f32_16x16x32_bf16 v[152:155], a[52:55], v[72:75], v[152:155]\n\t"
      "global_load_dwordx4 a[136:139], %[l16], s[38:39] offset:-2048 sc1\n\t"
      "v_mfma_f32_16x16x32_bf16 v[168:171], a[56:59], v[72:75], v[168:171]\n\t"
      "ds_read_b64_tr_b16 v[114:115], v40 offset:30720\n\t"
      "ds_read_b64_tr_b16 v[116:117], v41 offset:28672\n\t"
      "v_mfma_f32_16x16x32_bf16 v[140:143], a[48:51], v[76:79], v[140:143]\n\t"
      "global_load_dwordx4 a[140:143], %[l16], s[38:39] offset:-1024 sc1\n\t"
      "v_mfma_f32_16x16x32_bf16 v[156:159], a[52:55], v[76:79], v[156:159]\n\t"
      "ds_read_b64_tr_b16 v[118:119], v41 offset:30720\n\t"
      "ds_read_b64_tr_b16 v[120:121], v42 offset:28672\n\t"
      "v_mfma_f32_16x16x32_bf16 v[172:175], a[56:59], v[76:79], v[172:175]\n\t"
      "global_load_dwordx4 a[144:147], %[l16], s[38:39] offset:0 sc1\n\t"
      "v_mfma_f32_16x16x32_bf16 v[128:131], a[60:63], v[80:83], v[128:131]\n\t"
      "ds_read_b64_tr_b16 v[122:123], v42 offset:30720\n\t"
      "ds_read_b64_tr_b16 v[124:125], v43 offset:28672\n\t"
      "v_mfma_f32_16x16x32_bf16 v[144:147], a[64:67], v[80:83], v[144:147]\n\t"
      "ds_read_b64_tr_b16 v[126:127], v43 offset:30720\n\t"
      "v_mfma_f32_16x16x32_bf16 v[160:163], a[68:71], v[80:83], v[160:163]\n\t"
      "global_load_dwordx4 a[148:151], %[l16], s[38:39] offset:1024 sc1\n\t"
      "v_mfma_f32_16x16x32_bf16 v[176:179], v[52:55], v[80:83], v[176:179]\n\t"
      "s_waitcnt vmcnt(13)\n\t"
      "ds_read_b128 v[240:243], %[vchk] offset:32768\n\t"
      "v_mfma_f32_16x16x32_bf16 v[180:183], v[80:83], v[80:83], v[180:183]\n\t"
      "ds_read_b128 v[244:247], %[vchk] offset:33792\n\t"
      "ds_read_b128 v[248:251], %[vchk] offset:34816\n\t"
      "v_mfma_f32_16x16x32_bf16 v[132:135], a[60:63], v[84:87], v[132:135]\n\t"
      "ds_read_b128 v[252:255], %[vchk] offset:35840\n\t"
      "v_mfma_f32_16x16x32_bf16 v[148:151], a[64:67], v[84:87], v[148:151]\n\t"
      "global_load_dwordx4 a[152:155], %[l16], s[38:39] offset:2048 sc1\n\t"
      "v_mfma_f32_16x16x32_bf16 v[164:167], a[68:71], v[84:87], v[164:167]\n\t"
      "v_mfma_f32_16x16x32_bf16 v[136:139], a[60:63], v[88:91], v[136:139]\n\t"
      "s_waitcnt lgkmcnt(0)\n\t"
      "v_max3_u32 v56, v240, v241, v242\n\t"
      "v_mfma_f32_16x16x32_bf16 v[152:155], a[64:67], v[88:91], v[152:155]\n\t"
      "v_max3_u32 v56, v56, v243, v244\n\t"
      "v_max3_u32 v56, v56, v245, v246\n\t"
      "v_mfma_f32_16x16x32_bf16 v[168:171], a[68:71], v[88:91], v[168:171]\n\t"
      "v_max3_u32 v56, v56, v247, v248\n\t"
      "v_max3_u32 v56, v56, v249, v250\n\t"
      "v_mfma_f32_16x16x32_bf16 v[140:143], a[60:63], v[92:95], v[140:143]\n\t"
      "v_max3_u32 v56, v56, v251, v252\n\t"
      "v_max3_u32 v56, v56, v253, v254\n\t"
      "v_mfma_f32_16x16x32_bf16 v[156:159], a[64:67], v[92:95], v[156:159]\n\t"
      "v_max_u32 v56, v56, v255\n\t"
      "v_cmp_eq_u32 vcc, -1, v56\n\t"
      "v_mfma_f32_16x16x32_bf16 v[172:175], a[68:71], v[92:95], v[172:175]\n\t"
      "s_cbranch_vccnz L_g1_slow2_%=\n\t"
      "L_g1_back2_%=:\n\t"
      "s_waitcnt lgkmcnt(0)\n\t"
      "s_barrier\n\t"
      "v_mfma_f32_16x16x32_bf16 v[128:131], a[72:75], v[96:99], v[128:131]\n\t"
      "ds_read_b64_tr_b16 v[64:65], v40 offset:32768\n\t"
      "v_mfma_f32_16x16x32_bf16 v[144:147], a[76:79], v[96:99], v[144:147]\n\t"
      "s_add_u32 m0, %[m0b], 49152\n\t"
      "s_add_u32 s36, %[xlo], 49152\n\t"
      "v_mfma_f32_16x16x32_bf16 v[160:163], a[80:83], v[96:99], v[160:163]\n\t"
      "ds_read_b64_tr_b16 v[66:67], v40 offset:34816\n\t"
      "ds_read_b64_tr_b16 v[68:69], v41 offset:32768\n\t"
      "v_mfma_f32_16x16x32_bf16 v[176:179], v[52:55], v[96:99], v[176:179]\n\t"
      "s_addc_u32 s37, %[xhi], 0\n\t"
      "global_load_lds_dwordx4 %[voff], s[36:37] offset:2048 sc1\n\t"
      "v_mfma_f32_16x16x32_bf16 v[180:183], v[96:99], v[96:99], v[180:183]\n\t"
      "ds_read_b64_tr_b16 v[70:71], v41 offset:34816\n\t"
      "ds_read_b64_tr_b16 v[72:73], v42 offset:32768\n\t"
      "v_mfma_f32_16x16x32_bf16 v[132:135], a[72:75], v[100:103], v[132:135]\n\t"
      "ds_read_b64_tr_b16 v[74:75], v42 offset:34816\n\t"
      "v_mfma_f32_16x16x32_bf16 v[148:151], a[76:79], v[100:103], v[148:151]\n\t"
      "global_load_lds_dwordx4 %[voff], s[36:37] offset:3072 sc1\n\t"
      "v_mfma_f32_16x16x32_bf16 v[164:167], a[80:83], v[100:103], v[164:167]\n\t"
      "ds_read_b64_tr_b16 v[76:77], v43 offset:32768\n\t"
      "ds_read_b64_tr_b16 v[78:79], v43 offset:34816\n\t"
      "v_mfma_f32_16x16x32_bf16 v[136:139], a[72:75], v[104:107], v[136:139]\n\t"
      "global_load_dwordx4 a[156:159], %[l16], s[38:39] offset:3072 sc1\n\t"
      "v_mfma_f32_16x16x32_bf16 v[152:155], a[76:79], v[104:107], v[152:155]\n\t"
      "ds_read_b64_tr_b16 v[80:81], v40 offset:36864\n\t"
      "ds_read_b64_tr_b16 v[82:83], v40 offset:38912\n\t"
      "v_mfma_f32_16x16x32_bf16 v[168:171], a[80:83], v[104:107], v[168:171]\n\t"
      "s_add_u32 s38, %[plo], 45056\n\t"
      "s_addc_u32 s39, %[phi], 0\n\t"
      "v_mfma_f32_16x16x32_bf16 v[140:143], a[72:75], v[108:111], v[140:143]\n\t"
      "ds_read_b64_tr_b16 v[84:85], v41 offset:36864\n\t"
      "ds_read_b64_tr_b16 v[86:87], v41 offset:38912\n\t"
      "v_mfma_f32_16x16x32_bf16 v[156:159], a[76:79], v[108:111], v[156:159]\n\t"
      "global_load_dwordx4 a[160:163], %[l16], s[38:39] offset:-4096 sc1\n\t"
      "v_mfma_f32_16x16x32_bf16 v[172:175], a[80:83], v[108:111], v[172:175]\n\t"
      "ds_read_b64_tr_b16 v[88:89], v42 offset:36864\n\t"
      "ds_read_b64_tr_b16 v[90:91], v42 offset:38912\n\t"
      "v_mfma_f32_16x16x32_bf16 v[128:131], a[84:87], v[112:115], v[128:131]\n\t"
      "global_load_dwordx4 a[164:167], %[l16], s[38:39] offset:-3072 sc1\n\t"
      "v_mfma_f32_16x16x32_bf16 v[144:147], a[88:91], v[112:115], v[144:147]\n\t"
      "ds_read_b64_tr_b16 v[92:93], v43 offset:36864\n\t"
      "ds_read_b64_tr_b16 v[94:95], v43 offset:38912\n\t"
      "v_mfma_f32_16x16x32_bf16 v[160:163], a[92:95], v[112:115], v[160:163]\n\t"
      "global_load_dwordx4 a[168:171], %[l16], s[38:39] offset:-2048 sc1\n\t"
      "v_mfma_f32_16x16x32_bf16 v[176:179], v[52:55], v[112:115], v[176:179]\n\t"
      "global_load_dwordx4 a[172:175], %[l16], s[38:39] offset:-1024 sc1\n\t"
      "v_mfma_f32_16x16x32_bf16 v[180:183], v[112:115], v[112:115], v[180:183]\n\t"
      "v_mfma_f32_16x16x32_bf16 v[132:135], a[84:87], v[116:119], v[132:135]\n\t"
      "v_mfma_f32_16x16x32_bf16 v[148:151], a[88:91], v[116:119], v[148:151]\n\t"
      "v_mfma_f32_16x16x32_bf16 v[164:167], a[92:95], v[116:119], v[164:167]\n\t"
      "v_mfma_f32_16x16x32_bf16 v[136:139], a[84:87], v[120:123], v[136:139]\n\t"
      "v_mfma_f32_16x16x32_bf16 v[152:155], a[88:91], v[120:123], v[152:155]\n\t"
      "v_mfma_f32_16x16x32_bf16 v[168:171], a[92:95], v[120:123], v[168:171]\n\t"
      "v_mfma_f32_16x16x32_bf16 v[140:143], a[84:87], v[124:127], v[140:143]\n\t"
      "v_mfma_f32_16x16x32_bf16 v[156:159], a[88:91], v[124:127], v[156:159]\n\t"
      "v_mfma_f32_16x16x32_bf16 v[172:175], a[92:95], v[124:127], v[172:175]\n\t"
      "s_waitcnt lgkmcnt(0)\n\t"
      "s_waitcnt vmcnt(20)\n\t"
      "v_mfma_f32_16x16x32_bf16 v[128:131], a[96:99], v[64:67], v[128:131]\n\t"
      "ds_read_b64_tr_b16 v[96:97], v40 offset:40960\n\t"
      "s_add_u32 m0, %[m0b], 65536\n\t"
      "s_add_u32 s36, %[xlo], 65536\n\t"
      "v_mfma_f32_16x16x32_bf16 v[144:147], a[100:103], v[64:67], v[144:147]\n\t"
      "ds_read_b64_tr_b16 v[98:99], v40 offset:43008\n\t"
      "v_mfma_f32_16x16x32_bf16 v[160:163], a[104:107], v[64:67], v[160:163]\n\t"
      "s_addc_u32 s37, %[xhi], 0\n\t"
      "global_load_lds_dwordx4 %[voff], s[36:37] offset:0 sc1\n\t"
      "v_mfma_f32_16x16x32_bf16 v[176:179], v[52:55], v[64:67], v[176:179]\n\t"
      "ds_read_b64_tr_b16 v[100:101], v41 offset:40960\n\t"
      "ds_read_b64_tr_b16 v[102:103], v41 offset:43008\n\t"
      "v_mfma_f32_16x16x32_bf16 v[180:183], v[64:67], v[64:67], v[180:183]\n\t"
      "global_load_lds_dwordx4 %[voff], s[36:37] offset:1024 sc1\n\t"
      "ds_read_b64_tr_b16 v[104:105], v42 offset:40960\n\t"
      "v_mfma_f32_16x16x32_bf16 v[132:135], a[96:99], v[68:71], v[132:135]\n\t"
      "ds_read_b64_tr_b16 v[106:107], v42 offset:43008\n\t"
      "global_load_dwordx4 a[176:179], %[l16], s[38:39] offset:0 sc1\n\t"
      "v_mfma_f32_16x16x32_bf16 v[148:151], a[100:103], v[68:71], v[148:151]\n\t"
      "ds_read_b64_tr_b16 v[108:109], v43 offset:40960\n\t"
      "global_load_dwordx4 a[180:183], %[l16], s[38:39] offset:1024 sc1\n\t"
      "v_mfma_f32_16x16x32_bf16 v[164:167], a[104:107], v[68:71], v[164:167]\n\t"
      "ds_read_b64_tr_b16 v[110:111], v43 offset:43008\n\t"
      "ds_read_b64_tr_b16 v[112:113], v40 offset:45056\n\t"
      "v_mfma_f32_16x16x32_bf16 v[136:139], a[96:99], v[72:75], v[136:139]\n\t"
      "global_load_dwordx4 a[184:187], %[l16], s[38:39] offset:2048 sc1\n\t"
      "ds_read_b64_tr_b16 v[114:115], v40 offset:47104\n\t"
      "v_mfma_f32_16x16x32_bf16 v[152:155], a[100:103], v[72:75], v[152:155]\n\t"
      "ds_read_b64_tr_b16 v[116:117], v41 offset:45056\n\t"
      "global_load_dwordx4 a[188:191], %[l16], s[38:39] offset:3072 sc1\n\t"
      "v_mfma_f32_16x16x32_bf16 v[168:171], a[104:107], v[72:75], v[168:171]\n\t"
      "ds_read_b64_tr_b16 v[118:119], v41 offset:47104\n\t"
      "s_add_u32 s38, %[plo], 53248\n\t"
      "s_addc_u32 s39, %[phi], 0\n\t"
      "v_mfma_f32_16x16x32_bf16 v[140:143], a[96:99], v[76:79], v[140:143]\n\t"
      "ds_read_b64_tr_b16 v[120:121], v42 offset:45056\n\t"
      "ds_read_b64_tr_b16 v[122:123], v42 offset:47104\n\t"
      "v_mfma_f32_16x16x32_bf16 v[156:159], a[100:103], v[76:79], v[156:159]\n\t"
      "global_load_dwordx4 a[192:195], %[l16], s[38:39] offset:-4096 sc1\n\t"
      "ds_read_b64_tr_b16 v[124:125], v43 offset:45056\n\t"
      "v_mfma_f32_16x16x32_bf16 v[172:175], a[104:107], v[76:79], v[172:175]\n\t"
      "s_mov_b32 s42, %[wlo]\n\t"
      "s_mov_b32 s43, %[whi]\n\t"
      "s_waitcnt vmcnt(21)\n\t"
      "s_mov_b64 exec, 0xff\n\t"
      "global_store_dwordx2 v32, %[T], s[42:43] sc1\n\t"
      "s_mov_b64 exec, -1\n\t"
      "v_mfma_f32_16x16x32_bf16 v[128:131], a[108:111], v[80:83], v[128:131]\n\t"
      "ds_read_b64_tr_b16 v[126:127], v43 offset:47104\n\t"
      "v_mfma_f32_16x16x32_bf16 v[144:147], a[112:115], v[80:83], v[144:147]\n\t"
      "v_mfma_f32_16x16x32_bf16 v[160:163], a[116:119], v[80:83], v[160:163]\n\t"
      "v_mfma_f32_16x16x32_bf16 v[176:179], v[52:55], v[80:83], v[176:179]\n\t"
      "s_waitcnt vmcnt(13)\n\t"
      "ds_read_b128 v[240:243], %[vchk] offset:49152\n\t"
      "v_mfma_f32_16x16x32_bf16 v[180:183], v[80:83], v[80:83], v[180:183]\n\t"
      "ds_read_b128 v[244:247], %[vchk] offset:50176\n\t"
      "ds_read_b128 v[248:251], %[vchk] offset:51200\n\t"
      "v_mfma_f32_16x16x32_bf16 v[132:135], a[108:111], v[84:87], v[132:135]\n\t"
      "ds_read_b128 v[252:255], %[vchk] offset:52224\n\t"
      "v_mfma_f32_16x16x32_bf16 v[148:151], a[112:115], v[84:87], v[148:151]\n\t"
      "v_mfma_f32_16x16x32_bf16 v[164:167], a[116:119], v[84:87], v[164:167]\n\t"
      "v_mfma_f32_16x16x32_bf16 v[136:139], a[108:111], v[88:91], v[136:139]\n\t"
      "s_waitcnt lgkmcnt(0)\n\t"
      "v_max3_u32 v56, v240, v241, v242\n\t"
      "v_mfma_f32_16x16x32_bf16 v[152:155], a[112:115], v[88:91], v[152:155]\n\t"
      "v_max3_u32 v56, v56, v243, v244\n\t"
      "v_max3_u32 v56, v56, v245, v246\n\t"
      "v_mfma_f32_16x16x32_bf16 v[168:171], a[116:119], v[88:91], v[168:171]\n\t"
      "v_max3_u32 v56, v56, v247, v248\n\t"
      "v_max3_u32 v56, v56, v249, v250\n\t"
      "v_mfma_f32_16x16x32_bf16 v[140:143], a[108:111], v[92:95], v[140:143]\n\t"
      "v_max3_u32 v56, v56, v251, v252\n\t"
      "v_max3_u32 v56, v56, v253, v254\n\t"
      "v_mfma_f32_16x16x32_bf16 v[156:159], a[112:115], v[92:95], v[156:159]\n\t"
      "v_max_u32 v56, v56, v255\n\t"
      "v_cmp_eq_u32 vcc, -1, v56\n\t"
      "v_mfma_f32_16x16x32_bf16 v[172:175], a[116:119], v[92:95], v[172:175]\n\t"
      "s_cbranch_vccnz L_g1_slow3_%=\n\t"
      "L_g1_back3_%=:\n\t"
      "s_waitcnt lgkmcnt(0)\n\t"
      "s_barrier\n\t"
      "s_waitcnt vmcnt(18)\n\t"
      "v_mfma_f32_16x16x32_bf16 v[128:131], a[120:123], v[96:99], v[128:131]\n\t"
      "ds_read_b64_tr_b16 v[64:65], v40 offset:49152\n\t"
      "v_mfma_f32_16x16x32_bf16 v[144:147], a[124:127], v[96:99], v[144:147]\n\t"
      "s_add_u32 m0, %[m0b], 65536\n\t"
      "s_add_u32 s36, %[xlo], 65536\n\t"
      "v_mfma_f32_16x16x32_bf16 v[160:163], a[128:131], v[96:99], v[160:163]\n\t"
      "ds_read_b64_tr_b16 v[66:67], v40 offset:51200\n\t"
      "ds_read_b64_tr_b16 v[68:69], v41 offset:49152\n\t"
      "v_mfma_f32_16x16x32_bf16 v[176:179], v[52:55], v[96:99], v[176:179]\n\t"
      "s_addc_u32 s37, %[xhi], 0\n\t"
      "global_load_lds_dwordx4 %[voff], s[36:37] offset:2048 sc1\n\t"
      "v_mfma_f32_16x16x32_bf16 v[180:183], v[96:99], v[96:99], v[180:183]\n\t"
      "ds_read_b64_tr_b16 v[70:71], v41 offset:51200\n\t"
      "ds_read_b64_tr_b16 v[72:73], v42 offset:49152\n\t"
      "v_mfma_f32_16x16x32_bf16 v[132:135], a[120:123], v[100:103], v[132:135]\n\t"
      "ds_read_b64_tr_b16 v[74:75], v42 offset:51200\n\t"
      "v_mfma_f32_16x16x32_bf16 v[148:151], a[124:127], v[100:103], v[148:151]\n\t"
      "global_load_lds_dwordx4 %[voff], s[36:37] offset:3072 sc1\n\t"
      "v_mfma_f32_16x16x32_bf16 v[164:167], a[128:131], v[100:103], v[164:167]\n\t"
      "ds_read_b64_tr_b16 v[76:77], v43 offset:49152\n\t"
      "ds_read_b64_tr_b16 v[78:79], v43 offset:51200\n\t"
      "v_mfma_f32_16x16x32_bf16 v[136:139], a[120:123], v[104:107], v[136:139]\n\t"
      "ds_read_b64_tr_b16 v[80:81], v40 offset:53248\n\t"
      "v_mfma_f32_16x16x32_bf16 v[152:155], a[124:127], v[104:107], v[152:155]\n\t"
      "global_load_dwordx4 a[196:199], %[l16], s[38:39] offset:-3072 sc1\n\t"
      "v_mfma_f32_16x16x32_bf16 v[168:171], a[128:131], v[104:107], v[168:171]\n\t"
      "ds_read_b64_tr_b16 v[82:83], v40 offset:55296\n\t"
      "ds_read_b64_tr_b16 v[84:85], v41 offset:53248\n\t"
      "v_mfma_f32_16x16x32_bf16 v[140:143], a[120:123], v[108:111], v[140:143]\n\t"
      "global_load_dwordx4 a[200:203], %[l16], s[38:39] offset:-2048 sc1\n\t"
      "v_mfma_f32_16x16x32_bf16 v[156:159], a[124:127], v[108:111], v[156:159]\n\t"
      "ds_read_b64_tr_b16 v[86:87], v41 offset:55296\n\t"
      "ds_read_b64_tr_b16 v[88:89], v42 offset:53248\n\t"
      "v_mfma_f32_16x16x32_bf16 v[172:175], a[128:131], v[108:111], v[172:175]\n\t"
      "global_load_dwordx4 a[204:207], %[l16], s[38:39] offset:-1024 sc1\n\t"
      "v_mfma_f32_16x16x32_bf16 v[128:131], a[132:135], v[112:115], v[128:131]\n\t"
      "ds_read_b64_tr_b16 v[90:91], v42 offset:55296\n\t"
      "ds_read_b64_tr_b16 v[92:93], v43 offset:53248\n\t"
      "v_mfma_f32_16x16x32_bf16 v[144:147], a[136:139], v[112:115], v[144:147]\n\t"
      "ds_read_b64_tr_b16 v[94:95], v43 offset:55296\n\t"
      "v_mfma_f32_16x16x32_bf16 v[160:163], a[140:143], v[112:115], v[160:163]\n\t"
      "global_load_dwordx4 a[208:211], %[l16], s[38:39] offset:0 sc1\n\t"
      "v_mfma_f32_16x16x32_bf16 v[176:179], v[52:55], v[112:115], v[176:179]\n\t"
      "global_load_dwordx4 a[212:215], %[l16], s[38:39] offset:1024 sc1\n\t"
      "v_mfma_f32_16x16x32_bf16 v[180:183], v[112:115], v[112:115], v[180:183]\n\t"
      "v_mfma_f32_16x16x32_bf16 v[132:135], a[132:135], v[116:119], v[132:135]\n\t"
      "v_mfma_f32_16x16x32_bf16 v[148:151], a[136:139], v[116:119], v[148:151]\n\t"
      "v_mfma_f32_16x16x32_bf16 v[164:167], a[140:143], v[116:119], v[164:167]\n\t"
      "v_mfma_f32_16x16x32_bf16 v[136:139], a[132:135], v[120:123], v[136:139]\n\t"
      "v_mfma_f32_16x16x32_bf16 v[152:155], a[136:139], v[120:123], v[152:155]\n\t"
      "v_mfma_f32_16x16x32_bf16 v[168:171], a[140:143], v[120:123], v[168:171]\n\t"
      "v_mfma_f32_16x16x32_bf16 v[140:143], a[132:135], v[124:127], v[140:143]\n\t"
      "v_mfma_f32_16x16x32_bf16 v[156:159], a[136:139], v[124:127], v[156:159]\n\t"
      "v_mfma_f32_16x16x32_bf16 v[172:175], a[140:143], v[124:127], v[172:175]\n\t"
      "s_waitcnt lgkmcnt(0)\n\t"
      "s_waitcnt vmcnt(17)\n\t"
      "v_mfma_f32_16x16x32_bf16 v[128:131], a[144:147], v[64:67], v[128:131]\n\t"
      "ds_read_b64_tr_b16 v[96:97], v40 offset:57344\n\t"
      "v_mfma_f32_16x16x32_bf16 v[144:147], a[148:151], v[64:67], v[144:147]\n\t"
      "s_add_u32 m0, %[m0b], 81920\n\t"
      "s_add_u32 s36, %[xlo], 81920\n\t"
      "v_mfma_f32_16x16x32_bf16 v[160:163], a[152:155], v[64:67], v[160:163]\n\t"
      "ds_read_b64_tr_b16 v[98:99], v40 offset:59392\n\t"
      "ds_read_b64_tr_b16 v[100:101], v41 offset:57344\n\t"
      "v_mfma_f32_16x16x32_bf16 v[176:179], v[52:55], v[64:67], v[176:179]\n\t"
      "s_addc_u32 s37, %[xhi], 0\n\t"
      "global_load_lds_dwordx4 %[voff], s[36:37] offset:0 sc1\n\t"
      "v_mfma_f32_16x16x32_bf16 v[180:183], v[64:67], v[64:67], v[180:183]\n\t"
      "ds_read_b64_tr_b16 v[102:103], v41 offset:59392\n\t"
      "ds_read_b64_tr_b16 v[104:105], v42 offset:57344\n\t"
      "v_mfma_f32_16x16x32_bf16 v[132:135], a[144:147], v[68:71], v[132:135]\n\t"
      "ds_read_b64_tr_b16 v[106:107], v42 offset:59392\n\t"
      "v_mfma_f32_16x16x32_bf16 v[148:151], a[148:151], v[68:71], v[148:151]\n\t"
      "global_load_lds_dwordx4 %[voff], s[36:37] offset:1024 sc1\n\t"
      "v_mfma_f32_16x16x32_bf16 v[164:167], a[152:155], v[68:71], v[164:167]\n\t"
      "ds_read_b64_tr_b16 v[108:109], v43 offset:57344\n\t"
      "ds_read_b64_tr_b16 v[110:111], v43 offset:59392\n\t"
      "v_mfma_f32_16x16x32_bf16 v[136:139], a[144:147], v[72:75], v[136:139]\n\t"
      "global_load_dwordx4 a[216:219], %[l16], s[38:39] offset:2048 sc1\n\t"
      "v_mfma_f32_16x16x32_bf16 v[152:155], a[148:151], v[72:75], v[152:155]\n\t"
      "ds_read_b64_tr_b16 v[112:113], v40 offset:61440\n\t"
      "ds_read_b64_tr_b16 v[114:115], v40 offset:63488\n\t"
      "v_mfma_f32_16x16x32_bf16 v[168:171], a[152:155], v[72:75], v[168:171]\n\t"
      "global_load_dwordx4 a[220:223], %[l16], s[38:39] offset:3072 sc1\n\t"
      "v_mfma_f32_16x16x32_bf16 v[140:143], a[144:147], v[76:79], v[140:143]\n\t"
      "ds_read_b64_tr_b16 v[116:117], v41 offset:61440\n\t"
      "ds_read_b64_tr_b16 v[118:119], v41 offset:63488\n\t"
      "v_mfma_f32_16x16x32_bf16 v[156:159], a[148:151], v[76:79], v[156:159]\n\t"
      "s_add_u32 s38, %[plo], 61440\n\t"
      "s_addc_u32 s39, %[phi], 0\n\t"
      "v_mfma_f32_16x16x32_bf16 v[172:175], a[152:155], v[76:79], v[172:175]\n\t"
      "ds_read_b64_tr_b16 v[120:121], v42 offset:61440\n\t"
      "ds_read_b64_tr_b16 v[122:123], v42 offset:63488\n\t"
      "v_mfma_f32_16x16x32_bf16 v[128:131], a[156:159], v[80:83], v[128:131]\n\t"
      "global_load_dwordx4 a[224:227], %[l16], s[38:39] offset:-4096 sc1\n\t"
      "v_mfma_f32_16x16x32_bf16 v[144:147], a[160:163], v[80:83], v[144:147]\n\t"
      "ds_read_b64_tr_b16 v[124:125], v43 offset:61440\n\t"
      "ds_read_b64_tr_b16 v[126:127], v43 offset:63488\n\t"
      "v_mfma_f32_16x16x32_bf16 v[160:163], a[164:167], v[80:83], v[160:163]\n\t"
      "global_load_dwordx4 a[228:231], %[l16], s[38:39] offset:-3072 sc1\n\t"
      "v_mfma_f32_16x16x32_bf16 v[176:179], v[52:55], v[80:83], v[176:179]\n\t"
      "s_waitcnt vmcnt(11)\n\t"
      "ds_read_b128 v[240:243], v57 offset:0\n\t"
      "v_mfma_f32_16x16x32_bf16 v[180:183], v[80:83], v[80:83], v[180:183]\n\t"
      "ds_read_b128 v[244:247], v57 offset:1024\n\t"
      "ds_read_b128 v[248:251], v57 offset:2048\n\t"
      "v_mfma_f32_16x16x32_bf16 v[132:135], a[156:159], v[84:87], v[132:135]\n\t"
      "ds_read_b128 v[252:255], v57 offset:3072\n\t"
      "v_mfma_f32_16x16x32_bf16 v[148:151], a[160:163], v[84:87], v[148:151]\n\t"
      "global_load_dwordx4 a[232:235], %[l16], s[38:39] offset:-2048 sc1\n\t"
      "v_mfma_f32_16x16x32_bf16 v[164:167], a[164:167], v[84:87], v[164:167]\n\t"
      "v_mfma_f32_16x16x32_bf16 v[136:139], a[156:159], v[88:91], v[136:139]\n\t"
      "s_waitcnt lgkmcnt(0)\n\t"
      "v_max3_u32 v56, v240, v241, v242\n\t"
      "v_mfma_f32_16x16x32_bf16 v[152:155], a[160:163], v[88:91], v[152:155]\n\t"
      "v_max3_u32 v56, v56, v243, v244\n\t"
      "v_max3_u32 v56, v56, v245, v246\n\t"
      "v_mfma_f32_16x16x32_bf16 v[168:171], a[164:167], v[88:91], v[168:171]\n\t"
      "v_max3_u32 v56, v56, v247, v248\n\t"
      "v_max3_u32 v56, v56, v249, v250\n\t"
      "v_mfma_f32_16x16x32_bf16 v[140:143], a[156:159], v[92:95], v[140:143]\n\t"
      "v_max3_u32 v56, v56, v251, v252\n\t"
      "v_max3_u32 v56, v56, v253, v254\n\t"
      "v_mfma_f32_16x16x32_bf16 v[156:159], a[160:163], v[92:95], v[156:159]\n\t"
      "v_max_u32 v56, v56, v255\n\t"
      "v_cmp_eq_u32 vcc, -1, v56\n\t"
      "v_mfma_f32_16x16x32_bf16 v[172:175], a[164:167], v[92:95], v[172:175]\n\t"
      "s_cbranch_vccnz L_g1_slow4_%=\n\t"
      "L_g1_back4_%=:\n\t"
      "s_waitcnt lgkmcnt(0)\n\t"
      "s_barrier\n\t"
      "s_waitcnt vmcnt(16)\n\t"
      "v_mfma_f32_16x16x32_bf16 v[128:131], a[168:171], v[96:99], v[128:131]\n\t"
      "ds_read_b64_tr_b16 v[64:65], v44 offset:0\n\t"
      "v_mfma_f32_16x16x32_bf16 v[144:147], a[172:175], v[96:99], v[144:147]\n\t"
      "s_add_u32 m0, %[m0b], 81920\n\t"
      "s_add_u32 s36, %[xlo], 81920\n\t"
      "v_mfma_f32_16x16x32_bf16 v[160:163], a[176:179], v[96:99], v[160:163]\n\t"
      "ds_read_b64_tr_b16 v[66:67], v44 offset:2048\n\t"
      "v_mfma_f32_16x16x32_bf16 v[176:179], v[52:55], v[96:99], v[176:179]\n\t"
      "s_addc_u32 s37, %[xhi], 0\n\t"
      "global_load_lds_dwordx4 %[voff], s[36:37] offset:2048 sc1\n\t"
      "v_mfma_f32_16x16x32_bf16 v[180:183], v[96:99], v[96:99], v[180:183]\n\t"
      "ds_read_b64_tr_b16 v[68:69], v45 offset:0\n\t"
      "ds_read_b64_tr_b16 v[70:71], v45 offset:2048\n\t"
      "v_mfma_f32_16x16x32_bf16 v[132:135], a[168:171], v[100:103], v[132:135]\n\t"
      "global_load_lds_dwordx4 %[voff], s[36:37] offset:3072 sc1\n\t"
      "v_mfma_f32_16x16x32_bf16 v[148:151], a[172:175], v[100:103], v[148:151]\n\t"
      "ds_read_b64_tr_b16 v[72:73], v46 offset:0\n\t"
      "ds_read_b64_tr_b16 v[74:75], v46 offset:2048\n\t"
      "v_mfma_f32_16x16x32_bf16 v[164:167], a[176:179], v[100:103], v[164:167]\n\t"
      "s_add_u32 m0, %[m0b], 98304\n\t"
      "s_add_u32 s36, %[xlo], 98304\n\t"
      "v_mfma_f32_16x16x32_bf16 v[136:139], a[168:171], v[104:107], v[136:139]\n\t"
      "ds_read_b64_tr_b16 v[76:77], v47 offset:0\n\t"
      "v_mfma_f32_16x16x32_bf16 v[152:155], a[172:175], v[104:107], v[152:155]\n\t"
      "s_addc_u32 s37, %[xhi], 0\n\t"
      "global_load_lds_dwordx4 %[voff], s[36:37] offset:0 sc1\n\t"
      "v_mfma_f32_16x16x32_bf16 v[168:171], a[176:179], v[104:107], v[168:171]\n\t"
      "ds_read_b64_tr_b16 v[78:79], v47 offset:2048\n\t"
      "ds_read_b64_tr_b16 v[80:81], v44 offset:4096\n\t"
      "v_mfma_f32_16x16x32_bf16 v[140:143], a[168:171], v[108:111], v[140:143]\n\t"
      "global_load_lds_dwordx4 %[voff], s[36:37] offset:1024 sc1\n\t"
      "v_mfma_f32_16x16x32_bf16 v[156:159], a[172:175], v[108:111], v[156:159]\n\t"
      "ds_read_b64_tr_b16 v[82:83], v44 offset:6144\n\t"
      "ds_read_b64_tr_b16 v[84:85], v45 offset:4096\n\t"
      "v_mfma_f32_16x16x32_bf16 v[172:175], a[176:179], v[108:111], v[172:175]\n\t"
      "global_load_dwordx4 a[236:239], %[l16], s[38:39] offset:-1024 sc1\n\t"
      "v_mfma_f32_16x16x32_bf16 v[128:131], a[180:183], v[112:115], v[128:131]\n\t"
      "ds_read_b64_tr_b16 v[86:87], v45 offset:6144\n\t"
      "v_mfma_f32_16x16x32_bf16 v[144:147], a[184:187], v[112:115], v[144:147]\n\t"
      "s_add_u32 s38, %[wlo], 0\n\t"
      "s_addc_u32 s39, %[whi], 0\n\t"
      "v_mfma_f32_16x16x32_bf16 v[160:163], a[188:191], v[112:115], v[160:163]\n\t"
      "ds_read_b64_tr_b16 v[88:89], v46 offset:4096\n\t"
      "v_mfma_f32_16x16x32_bf16 v[176:179], v[52:55], v[112:115], v[176:179]\n\t"
      "global_load_dwordx4 v[184:187], v36, s[38:39] sc1\n\t"
      "v_mfma_f32_16x16x32_bf16 v[180:183], v[112:115], v[112:115], v[180:183]\n\t"
      "ds_read_b64_tr_b16 v[90:91], v46 offset:6144\n\t"
      "ds_read_b64_tr_b16 v[92:93], v47 offset:4096\n\t"
      "v_mfma_f32_16x16x32_bf16 v[132:135], a[180:183], v[116:119], v[132:135]\n\t"
      "s_add_u32 s38, %[wlo], 24576\n\t"
      "s_addc_u32 s39, %[whi], 0\n\t"
      "v_mfma_f32_16x16x32_bf16 v[148:151], a[184:187], v[116:119], v[148:151]\n\t"
      "ds_read_b64_tr_b16 v[94:95], v47 offset:6144\n\t"
      "v_mfma_f32_16x16x32_bf16 v[164:167], a[188:191], v[116:119], v[164:167]\n\t"
      "global_load_dwordx4 v[188:191], v36, s[38:39] sc1\n\t"
      "v_mfma_f32_16x16x32_bf16 v[136:139], a[180:183], v[120:123], v[136:139]\n\t"
      "s_add_u32 s38, %[wlo], 49152\n\t"
      "s_addc_u32 s39, %[whi], 0\n\t"
      "v_mfma_f32_16x16x32_bf16 v[152:155], a[184:187], v[120:123], v[152:155]\n\t"
      "global_load_dwordx4 v[192:195], v36, s[38:39] sc1\n\t"
      "v_mfma_f32_16x16x32_bf16 v[168:171], a[188:191], v[120:123], v[168:171]\n\t"
      "v_mfma_f32_16x16x32_bf16 v[140:143], a[180:183], v[124:127], v[140:143]\n\t"
      "v_mfma_f32_16x16x32_bf16 v[156:159], a[184:187], v[124:127], v[156:159]\n\t"
      "v_mfma_f32_16x16x32_bf16 v[172:175], a[188:191], v[124:127], v[172:175]\n\t"
      "s_waitcnt lgkmcnt(0)\n\t"
      "s_waitcnt vmcnt(15)\n\t"
      "v_mfma_f32_16x16x32_bf16 v[128:131], a[192:195], v[64:67], v[128:131]\n\t"
      "ds_read_b64_tr_b16 v[96:97], v44 offset:8192\n\t"
      "s_add_u32 m0, %[m0b], 98304\n\t"
      "s_add_u32 s36, %[xlo], 98304\n\t"
      "v_mfma_f32_16x16x32_bf16 v[144:147], a[196:199], v[64:67], v[144:147]\n\t"
      "ds_read_b64_tr_b16 v[98:99], v44 offset:10240\n\t"
      "v_mfma_f32_16x16x32_bf16 v[160:163], a[200:203], v[64:67], v[160:163]\n\t"
      "s_addc_u32 s37, %[xhi], 0\n\t"
      "global_load_lds_dwordx4 %[voff], s[36:37] offset:2048 sc1\n\t"
      "v_mfma_f32_16x16x32_bf16 v[176:179], v[52:55], v[64:67], v[176:179]\n\t"
      "ds_read_b64_tr_b16 v[100:101], v45 offset:8192\n\t"
      "ds_read_b64_tr_b16 v[102:103], v45 offset:10240\n\t"
      "v_mfma_f32_16x16x32_bf16 v[180:183], v[64:67], v[64:67], v[180:183]\n\t"
      "global_load_lds_dwordx4 %[voff], s[36:37] offset:3072 sc1\n\t"
      "ds_read_b64_tr_b16 v[104:105], v46 offset:8192\n\t"
      "v_mfma_f32_16x16x32_bf16 v[132:135], a[192:195], v[68:71], v[132:135]\n\t"
      "s_add_u32 m0, %[m0b], 114688\n\t"
      "s_add_u32 s36, %[xlo], 114688\n\t"
      "ds_read_b64_tr_b16 v[106:107], v46 offset:10240\n\t"
      "v_mfma_f32_16x16x32_bf16 v[148:151], a[196:199], v[68:71], v[148:151]\n\t"
      "s_addc_u32 s37, %[xhi], 0\n\t"
      "global_load_lds_dwordx4 %[voff], s[36:37] offset:0 sc1\n\t"
      "v_mfma_f32_16x16x32_bf16 v[164:167], a[200:203], v[68:71], v[164:167]\n\t"
      "ds_read_b64_tr_b16 v[108:109], v47 offset:8192\n\t"
      "ds_read_b64_tr_b16 v[110:111], v47 offset:10240\n\t"
      "ds_read_b64_tr_b16 v[112:113], v44 offset:12288\n\t"
      "v_mfma_f32_16x16x32_bf16 v[136:139], a[192:195], v[72:75], v[136:139]\n\t"
      "global_load_lds_dwordx4 %[voff], s[36:37] offset:1024 sc1\n\t"
      "ds_read_b64_tr_b16 v[114:115], v44 offset:14336\n\t"
      "v_mfma_f32_16x16x32_bf16 v[152:155], a[196:199], v[72:75], v[152:155]\n\t"
      "s_add_u32 s38, %[wlo], 73728\n\t"
      "s_addc_u32 s39, %[whi], 0\n\t"
      "ds_read_b64_tr_b16 v[116:117], v45 offset:12288\n\t"
      "v_mfma_f32_16x16x32_bf16 v[168:171], a[200:203], v[72:75], v[168:171]\n\t"
      "global_load_dwordx4 v[196:199], v36, s[38:39] sc1\n\t"
      "ds_read_b64_tr_b16 v[118:119], v45 offset:14336\n\t"
      "v_mfma_f32_16x16x32_bf16 v[140:143], a[192:195], v[76:79], v[140:143]\n\t"
      "s_add_u32 s38, %[wlo], 98304\n\t"
      "s_addc_u32 s39, %[whi], 0\n\t"
      "ds_read_b64_tr_b16 v[120:121], v46 offset:12288\n\t"
      "v_mfma_f32_16x16x32_bf16 v[156:159], a[196:199], v[76:79], v[156:159]\n\t"
      "ds_read_b64_tr_b16 v[122:123], v46 offset:14336\n\t"
      "global_load_dwordx4 v[200:203], v36, s[38:39] sc1\n\t"
      "v_mfma_f32_16x16x32_bf16 v[172:175], a[200:203], v[76:79], v[172:175]\n\t"
      "ds_read_b64_tr_b16 v[124:125], v47 offset:12288\n\t"
      "s_add_u32 s38, %[wlo], 122880\n\t"
      "s_addc_u32 s39, %[whi], 0\n\t"
      "v_mfma_f32_16x16x32_bf16 v[128:131], a[204:207], v[80:83], v[128:131]\n\t"
      "ds_read_b64_tr_b16 v[126:127], v47 offset:14336\n\t"
      "global_load_dwordx4 v[204:207], v36, s[38:39] sc1\n\t"
      "v_mfma_f32_16x16x32_bf16 v[144:147], a[208:211], v[80:83], v[144:147]\n\t"
      "s_add_u32 s38, %[wlo], 147456\n\t"
      "s_addc_u32 s39, %[whi], 0\n\t"
      "v_mfma_f32_16x16x32_bf16 v[160:163], a[212:215], v[80:83], v[160:163]\n\t"
      "global_load_dwordx4 v[208:211], v36, s[38:39] sc1\n\t"
      "v_mfma_f32_16x16x32_bf16 v[176:179], v[52:55], v[80:83], v[176:179]\n\t"
      "s_waitcnt vmcnt(14)\n\t"
      "ds_read_b128 v[240:243], v57 offset:16384\n\t"
      "v_mfma_f32_16x16x32_bf16 v[180:183], v[80:83], v[80:83], v[180:183]\n\t"
      "ds_read_b128 v[244:247], v57 offset:17408\n\t"
      "ds_read_b128 v[248:251], v57 offset:18432\n\t"
      "v_mfma_f32_16x16x32_bf16 v[132:135], a[204:207], v[84:87], v[132:135]\n\t"
      "ds_read_b128 v[252:255], v57 offset:19456\n\t"
      "v_mfma_f32_16x16x32_bf16 v[148:151], a[208:211], v[84:87], v[148:151]\n\t"
      "v_mfma_f32_16x16x32_bf16 v[164:167], a[212:215], v[84:87], v[164:167]\n\t"
      "v_mfma_f32_16x16x32_bf16 v[136:139], a[204:207], v[88:91], v[136:139]\n\t"
      "s_waitcnt lgkmcnt(0)\n\t"
      "v_max3_u32 v56, v240, v241, v242\n\t"
      "v_mfma_f32_16x16x32_bf16 v[152:155], a[208:211], v[88:91], v[152:155]\n\t"
      "v_max3_u32 v56, v56, v243, v244\n\t"
      "v_max3_u32 v56, v56, v245, v246\n\t"
      "v_mfma_f32_16x16x32_bf16 v[168:171], a[212:215], v[88:91], v[168:171]\n\t"
      "v_max3_u32 v56, v56, v247, v248\n\t"
      "v_max3_u32 v56, v56, v249, v250\n\t"
      "v_mfma_f32_16x16x32_bf16 v[140:143], a[204:207], v[92:95], v[140:143]\n\t"
      "v_max3_u32 v56, v56, v251, v252\n\t"
      "v_max3_u32 v56, v56, v253, v254\n\t"
      "v_mfma_f32_16x16x32_bf16 v[156:159], a[208:211], v[92:95], v[156:159]\n\t"
      "v_max_u32 v56, v56, v255\n\t"
      "v_cmp_eq_u32 vcc, -1, v56\n\t"
      "v_mfma_f32_16x16x32_bf16 v[172:175], a[212:215], v[92:95], v[172:175]\n\t"
      "s_cbranch_vccnz L_g1_slow5_%=\n\t"
      "L_g1_back5_%=:\n\t"
      "s_waitcnt lgkmcnt(0)\n\t"
      "s_barrier\n\t"
      "s_waitcnt vmcnt(11)\n\t"
      "v_mfma_f32_16x16x32_bf16 v[128:131], a[216:219], v[96:99], v[128:131]\n\t"
      "s_add_u32 m0, %[m0b], 114688\n\t"
      "s_add_u32 s36, %[xlo], 114688\n\t"
      "v_mfma_f32_16x16x32_bf16 v[144:147], a[220:223], v[96:99], v[144:147]\n\t"
      "s_addc_u32 s37, %[xhi], 0\n\t"
      "global_load_lds_dwordx4 %[voff], s[36:37] offset:2048 sc1\n\t"
      "v_mfma_f32_16x16x32_bf16 v[160:163], a[224:227], v[96:99], v[160:163]\n\t"
      "global_load_lds_dwordx4 %[voff], s[36:37] offset:3072 sc1\n\t"
      "v_mfma_f32_16x16x32_bf16 v[176:179], v[52:55], v[96:99], v[176:179]\n\t"
      "s_add_u32 m0, %[m0b], 0\n\t"
      "s_add_u32 s36, %[xlo], 131072\n\t"
      "v_mfma_f32_16x16x32_bf16 v[180:183], v[96:99], v[96:99], v[180:183]\n\t"
      "s_addc_u32 s37, %[xhi], 0\n\t"
      "global_load_lds_dwordx4 %[voff], s[36:37] offset:0 sc1\n\t"
      "v_mfma_f32_16x16x32_bf16 v[132:135], a[216:219], v[100:103], v[132:135]\n\t"
      "global_load_lds_dwordx4 %[voff], s[36:37] offset:1024 sc1\n\t"
      "v_mfma_f32_16x16x32_bf16 v[148:151], a[220:223], v[100:103], v[148:151]\n\t"
      "s_add_u32 s38, %[wlo], 172032\n\t"
      "s_addc_u32 s39, %[whi], 0\n\t"
      "v_mfma_f32_16x16x32_bf16 v[164:167], a[224:227], v[100:103], v[164:167]\n\t"
      "global_load_dwordx4 v[212:215], v36, s[38:39] sc1\n\t"
      "v_mfma_f32_16x16x32_bf16 v[136:139], a[216:219], v[104:107], v[136:139]\n\t"
      "s_add_u32 s38, %[wlo], 196608\n\t"
      "s_addc_u32 s39, %[whi], 0\n\t"
      "v_mfma_f32_16x16x32_bf16 v[152:155], a[220:223], v[104:107], v[152:155]\n\t"
      "global_load_dwordx4 v[216:219], v36, s[38:39] sc1\n\t"
      "v_mfma_f32_16x16x32_bf16 v[168:171], a[224:227], v[104:107], v[168:171]\n\t"
      "s_add_u32 s38, %[wlo], 221184\n\t"
      "s_addc_u32 s39, %[whi], 0\n\t"
      "v_mfma_f32_16x16x32_bf16 v[140:143], a[216:219], v[108:111], v[140:143]\n\t"
      "global_load_dwordx4 v[220:223], v36, s[38:39] sc1\n\t"
      "v_mfma_f32_16x16x32_bf16 v[156:159], a[220:223], v[108:111], v[156:159]\n\t"
      "v_mfma_f32_16x16x32_bf16 v[172:175], a[224:227], v[108:111], v[172:175]\n\t"
      "v_mfma_f32_16x16x32_bf16 v[128:131], a[228:231], v[112:115], v[128:131]\n\t"
      "v_mfma_f32_16x16x32_bf16 v[144:147], a[232:235], v[112:115], v[144:147]\n\t"
      "v_mfma_f32_16x16x32_bf16 v[160:163], a[236:239], v[112:115], v[160:163]\n\t"
      "v_mfma_f32_16x16x32_bf16 v[176:179], v[52:55], v[112:115], v[176:179]\n\t"
      "v_mfma_f32_16x16x32_bf16 v[180:183], v[112:115], v[112:115], v[180:183]\n\t"
      "v_mfma_f32_16x16x32_bf16 v[132:135], a[228:231], v[116:119], v[132:135]\n\t"
      "v_mfma_f32_16x16x32_bf16 v[148:151], a[232:235], v[116:119], v[148:151]\n\t"
      "v_mfma_f32_16x16x32_bf16 v[164:167], a[236:239], v[116:119], v[164:167]\n\t"
      "v_mfma_f32_16x16x32_bf16 v[136:139], a[228:231], v[120:123], v[136:139]\n\t"
      "v_mfma_f32_16x16x32_bf16 v[152:155], a[232:235], v[120:123], v[152:155]\n\t"
      "v_mfma_f32_16x16x32_bf16 v[168:171], a[236:239], v[120:123], v[168:171]\n\t"
      "v_mfma_f32_16x16x32_bf16 v[140:143], a[228:231], v[124:127], v[140:143]\n\t"
      "v_mfma_f32_16x16x32_bf16 v[156:159], a[232:235], v[124:127], v[156:159]\n\t"
      "v_mfma_f32_16x16x32_bf16 v[172:175], a[236:239], v[124:127], v[172:175]\n\t"
      "global_load_dwordx4 v[224:227], v38, %[wsb] offset:0 sc1\n\t"
      "global_load_dwordx4 v[228:231], v38, %[wsb] offset:16 sc1\n\t"
      "global_load_dwordx4 v[232:235], v38, %[wsb] offset:32 sc1\n\t"
      "global_load_dwordx4 v[236:239], v38, %[wsb] offset:48 sc1\n\t"
      "global_load_dwordx2 v[58:59], v37, %[wsb] sc1\n\t"
      "s_waitcnt vmcnt(5)\n\t"
      "s_mov_b32 m0, s45\n\t"
      "s_nop 7\n\t"
      "s_branch L_g1_end%=\n\t"
      "L_g1_slow0_%=:\n\t"
      "s_add_u32 m0, %[m0b], 0\n\t"
      "s_add_u32 s42, %[xlo], 0\n\t"
      "s_addc_u32 s43, %[xhi], 0\n\t"
      "global_load_lds_dwordx4 %[voff], s[42:43] offset:0 sc1\n\t"
      "global_load_lds_dwordx4 %[voff], s[42:43] offset:1024 sc1\n\t"
      "global_load_lds_dwordx4 %[voff], s[42:43] offset:2048 sc1\n\t"
      "global_load_lds_dwordx4 %[voff], s[42:43] offset:3072 sc1\n\t"
      "s_waitcnt vmcnt(0)\n\t"
      "ds_read_b128 v[240:243], %[vchk] offset:0\n\t"
      "ds_read_b128 v[244:247], %[vchk] offset:1024\n\t"
      "ds_read_b128 v[248:251], %[vchk] offset:2048\n\t"
      "ds_read_b128 v[252:255], %[vchk] offset:3072\n\t"
      "s_waitcnt lgkmcnt(0)\n\t"
      "v_max3_u32 v56, v240, v241, v242\n\t"
      "v_max3_u32 v56, v56, v243, v244\n\t"
      "v_max3_u32 v56, v56, v245, v246\n\t"
      "v_max3_u32 v56, v56, v247, v248\n\t"
      "v_max3_u32 v56, v56, v249, v250\n\t"
      "v_max3_u32 v56, v56, v251, v252\n\t"
      "v_max3_u32 v56, v56, v253, v254\n\t"
      "v_max_u32 v56, v56, v255\n\t"
      "v_cmp_eq_u32 vcc, -1, v56\n\t"
      "s_sub_u32 s44, s44, 1\n\t"
      "s_cbranch_vccz L_g1_back0_%=\n\t"
      "s_cmp_lg_u32 s44, 0\n\t"
      "s_cbranch_scc1 L_g1_slow0_%=\n\t"
      "s_branch L_g1_back0_%=\n\t"
      "L_g1_slow1_%=:\n\t"
      "s_add_u32 m0, %[m0b], 16384\n\t"
      "s_add_u32 s42, %[xlo], 16384\n\t"
      "s_addc_u32 s43, %[xhi], 0\n\t"
      "global_load_lds_dwordx4 %[voff], s[42:43] offset:0 sc1\n\t"
      "global_load_lds_dwordx4 %[voff], s[42:43] offset:1024 sc1\n\t"
      "global_load_lds_dwordx4 %[voff], s[42:43] offset:2048 sc1\n\t"
      "global_load_lds_dwordx4 %[voff], s[42:43] offset:3072 sc1\n\t"
      "s_waitcnt vmcnt(0)\n\t"
      "ds_read_b128 v[240:243], %[vchk] offset:16384\n\t"
      "ds_read_b128 v[244:247], %[vchk] offset:17408\n\t"
      "ds_read_b128 v[248:251], %[vchk] offset:18432\n\t"
      "ds_read_b128 v[252:255], %[vchk] offset:19456\n\t"
      "s_waitcnt lgkmcnt(0)\n\t"
      "v_max3_u32 v56, v240, v241, v242\n\t"
      "v_max3_u32 v56, v56, v243, v244\n\t"
      "v_max3_u32 v56, v56, v245, v246\n\t"
      "v_max3_u32 v56, v56, v247, v248\n\t"
      "v_max3_u32 v56, v56, v249, v250\n\t"
      "v_max3_u32 v56, v56, v251, v252\n\t"
      "v_max3_u32 v56, v56, v253, v254\n\t"
      "v_max_u32 v56, v56, v255\n\t"
      "v_cmp_eq_u32 vcc, -1, v56\n\t"
      "s_sub_u32 s44, s44, 1\n\t"
      "s_cbranch_vccz L_g1_back1_%=\n\t"
      "s_cmp_lg_u32 s44, 0\n\t"
      "s_cbranch_scc1 L_g1_slow1_%=\n\t"
      "s_branch L_g1_back1_%=\n\t"
      "L_g1_slow2_%=:\n\t"
      "s_add_u32 m0, %[m0b], 32768\n\t"
      "s_add_u32 s42, %[xlo], 32768\n\t"
      "s_addc_u32 s43, %[xhi], 0\n\t"
      "global_load_lds_dwordx4 %[voff], s[42:43] offset:0 sc1\n\t"
      "global_load_lds_dwordx4 %[voff], s[42:43] offset:1024 sc1\n\t"
      "global_load_lds_dwordx4 %[voff], s[42:43] offset:2048 sc1\n\t"
      "global_load_lds_dwordx4 %[voff], s[42:43] offset:3072 sc1\n\t"
      "s_waitcnt vmcnt(0)\n\t"
      "ds_read_b128 v[240:243], %[vchk] offset:32768\n\t"
      "ds_read_b128 v[244:247], %[vchk] offset:33792\n\t"
      "ds_read_b128 v[248:251], %[vchk] offset:34816\n\t"
      "ds_read_b128 v[252:255], %[vchk] offset:35840\n\t"
      "s_waitcnt lgkmcnt(0)\n\t"
      "v_max3_u32 v56, v240, v241, v242\n\t"
      "v_max3_u32 v56, v56, v243, v244\n\t"
      "v_max3_u32 v56, v56, v245, v246\n\t"
      "v_max3_u32 v56, v56, v247, v248\n\t"
      "v_max3_u32 v56, v56, v249, v250\n\t"
      "v_max3_u32 v56, v56, v251, v252\n\t"
      "v_max3_u32 v56, v56, v253, v254\n\t"
      "v_max_u32 v56, v56, v255\n\t"
      "v_cmp_eq_u32 vcc, -1, v56\n\t"
      "s_sub_u32 s44, s44, 1\n\t"
      "s_cbranch_vccz L_g1_back2_%=\n\t"
      "s_cmp_lg_u32 s44, 0\n\t"
      "s_cbranch_scc1 L_g1_slow2_%=\n\t"
      "s_branch L_g1_back2_%=\n\t"
      "L_g1_slow3_%=:\n\t"
      "s_add_u32 m0, %[m0b], 49152\n\t"
      "s_add_u32 s42, %[xlo], 49152\n\t"
      "s_addc_u32 s43, %[xhi], 0\n\t"
      "global_load_lds_dwordx4 %[voff], s[42:43] offset:0 sc1\n\t"
      "global_load_lds_dwordx4 %[voff], s[42:43] offset:1024 sc1\n\t"
      "global_load_lds_dwordx4 %[voff], s[42:43] offset:2048 sc1\n\t"
      "global_load_lds_dwordx4 %[voff], s[42:43] offset:3072 sc1\n\t"
      "s_waitcnt vmcnt(0)\n\t"
      "ds_read_b128 v[240:243], %[vchk] offset:49152\n\t"
      "ds_read_b128 v[244:247], %[vchk] offset:50176\n\t"
      "ds_read_b128 v[248:251], %[vchk] offset:51200\n\t"
      "ds_read_b128 v[252:255], %[vchk] offset:52224\n\t"
      "s_waitcnt lgkmcnt(0)\n\t"
      "v_max3_u32 v56, v240, v241, v242\n\t"
      "v_max3_u32 v56, v56, v243, v244\n\t"
      "v_max3_u32 v56, v56, v245, v246\n\t"
      "v_max3_u32 v56, v56, v247, v248\n\t"
      "v_max3_u32 v56, v56, v249, v250\n\t"
      "v_max3_u32 v56, v56, v251, v252\n\t"
      "v_max3_u32 v56, v56, v253, v254\n\t"
      "v_max_u32 v56, v56, v255\n\t"
      "v_cmp_eq_u32 vcc, -1, v56\n\t"
      "s_sub_u32 s44, s44, 1\n\t"
      "s_cbranch_vccz L_g1_back3_%=\n\t"
      "s_cmp_lg_u32 s44, 0\n\t"
      "s_cbranch_scc1 L_g1_slow3_%=\n\t"
      "s_branch L_g1_back3_%=\n\t"
      "L_g1_slow4_%=:\n\t"
      "s_add_u32 m0, %[m0b], 65536\n\t"
      "s_add_u32 s42, %[xlo], 65536\n\t"
      "s_addc_u32 s43, %[xhi], 0\n\t"
      "global_load_lds_dwordx4 %[voff], s[42:43] offset:0 sc1\n\t"
      "global_load_lds_dwordx4 %[voff], s[42:43] offset:1024 sc1\n\t"
      "global_load_lds_dwordx4 %[voff], s[42:43] offset:2048 sc1\n\t"
      "global_load_lds_dwordx4 %[voff], s[42:43] offset:3072 sc1\n\t"
      "s_waitcnt vmcnt(0)\n\t"
      "ds_read_b128 v[240:243], v57 offset:0\n\t"
      "ds_read_b128 v[244:247], v57 offset:1024\n\t"
      "ds_read_b128 v[248:251], v57 offset:2048\n\t"
      "ds_read_b128 v[252:255], v57 offset:3072\n\t"
      "s_waitcnt lgkmcnt(0)\n\t"
      "v_max3_u32 v56, v240, v241, v242\n\t"
      "v_max3_u32 v56, v56, v243, v244\n\t"
      "v_max3_u32 v56, v56, v245, v246\n\t"
      "v_max3_u32 v56, v56, v247, v248\n\t"
      "v_max3_u32 v56, v56, v249, v250\n\t"
      "v_max3_u32 v56, v56, v251, v252\n\t"
      "v_max3_u32 v56, v56, v253, v254\n\t"
      "v_max_u32 v56, v56, v255\n\t"
      "v_cmp_eq_u32 vcc, -1, v56\n\t"
      "s_sub_u32 s44, s44, 1\n\t"
      "s_cbranch_vccz L_g1_back4_%=\n\t"
      "s_cmp_lg_u32 s44, 0\n\t"
      "s_cbranch_scc1 L_g1_slow4_%=\n\t"
      "s_branch L_g1_back4_%=\n\t"
      "L_g1_slow5_%=:\n\t"
      "s_add_u32 m0, %[m0b], 81920\n\t"
      "s_add_u32 s42, %[xlo], 81920\n\t"
      "s_addc_u32 s43, %[xhi], 0\n\t"
      "global_load_lds_dwordx4 %[voff], s[42:43] offset:0 sc1\n\t"
      "global_load_lds_dwordx4 %[voff], s[42:43] offset:1024 sc1\n\t"
      "global_load_lds_dwordx4 %[voff], s[42:43] offset:2048 sc1\n\t"
      "global_load_lds_dwordx4 %[voff], s[42:43] offset:3072 sc1\n\t"
      "s_waitcnt vmcnt(0)\n\t"
      "ds_read_b128 v[240:243], v57 offset:16384\n\t"
      "ds_read_b128 v[244:247], v57 offset:17408\n\t"
      "ds_read_b128 v[248:251], v57 offset:18432\n\t"
      "ds_read_b128 v[252:255], v57 offset:19456\n\t"
      "s_waitcnt lgkmcnt(0)\n\t"
      "v_max3_u32 v56, v240, v241, v242\n\t"
      "v_max3_u32 v56, v56, v243, v244\n\t"
      "v_max3_u32 v56, v56, v245, v246\n\t"
      "v_max3_u32 v56, v56, v247, v248\n\t"
      "v_max3_u32 v56, v56, v249, v250\n\t"
      "v_max3_u32 v56, v56, v251, v252\n\t"
      "v_max3_u32 v56, v56, v253, v254\n\t"
      "v_max_u32 v56, v56, v255\n\t"
      "v_cmp_eq_u32 vcc, -1, v56\n\t"
      "s_sub_u32 s44, s44, 1\n\t"
      "s_cbranch_vccz L_g1_back5_%=\n\t"
      "s_cmp_lg_u32 s44, 0\n\t"
      "s_cbranch_scc1 L_g1_slow5_%=\n\t"
      "s_branch L_g1_back5_%=\n\t"
      "L_g1_end%=:\n\t"
        : "=&{v[128:159]}"(accA), "=&{v[160:175]}"(accB), "=&{v[176:183]}"(accS), "=&{v[184:215]}"(pa), "=&{v[216:223]}"(pb), "=&{v[58:59]}"(fdv), "=&{v[224:239]}"(gv)
        : G1_IN : G1_CL,  "v48", "v49", "v50", "v51", "v52", "v53", "v54", "v55", "v56", "v57", "v60", "v61", "v62", "v63", "v64", "v65", "v66", "v67", "v68", "v69", "v70", "v71", "v72", "v73", "v74", "v75", "v76", "v77", "v78", "v79", "v80", "v81", "v82", "v83", "v84", "v85", "v86", "v87", "v88", "v89", "v90", "v91", "v92", "v93", "v94", "v95", "v96", "v97", "v98", "v99", "v100", "v101", "v102", "v103", "v104", "v105", "v106", "v107", "v108", "v109", "v110", "v111", "v112", "v113", "v114", "v115", "v116", "v117", "v118", "v119", "v120", "v121", "v122", "v123", "v124", "v125", "v126", "v127", "v240", "v241", "v242", "v243", "v244", "v245", "v246", "v247", "v248", "v249", "v250", "v251", "v252", "v253", "v254", "v255", "a0", "a1", "a2", "a3", "a4", "a5", "a6", "a7", "a8", "a9", "a10", "a11", "a12", "a13", "a14", "a15", "a16", "a17", "a18", "a19", "a20", "a21", "a22", "a23", "a24", "a25", "a26", "a27", "a28", "a29", "a30", "a31", "a32", "a33", "a34", "a35", "a36", "a37", "a38", "a39", "a40", "a41", "a42", "a43", "a44", "a45", "a46", "a47", "a48", "a49", "a50", "a51", "a52", "a53", "a54", "a55", "a56", "a57", "a58", "a59", "a60", "a61", "a62", "a63", "a64", "a65", "a66", "a67", "a68", "a69", "a70", "a71", "a72", "a73", "a74", "a75", "a76", "a77", "a78", "a79", "a80", "a81", "a82", "a83", "a84", "a85", "a86", "a87", "a88", "a89", "a90", "a91", "a92", "a93", "a94", "a95", "a96", "a97", "a98", "a99", "a100", "a101", "a102", "a103", "a104", "a105", "a106", "a107", "a108", "a109", "a110", "a111", "a112", "a113", "a114", "a115", "a116", "a117", "a118", "a119", "a120", "a121", "a122", "a123", "a124", "a125", "a126", "a127", "a128", "a129", "a130", "a131", "a132", "a133", "a134", "a135", "a136", "a137", "a138", "a139", "a140", "a141", "a142", "a143", "a144", "a145", "a146", "a147", "a148", "a149", "a150", "a151", "a152", "a153", "a154", "a155", "a156", "a157", "a158", "a159", "a160", "a161", "a162", "a163", "a164", "a165", "a166", "a167", "a168", "a169", "a170", "a171", "a172", "a173", "a174", "a175", "a176", "a177", "a178", "a179", "a180", "a181", "a182", "a183", "a184", "a185", "a186", "a187", "a188", "a189", "a190", "a191", "a192", "a193", "a194", "a195", "a196", "a197", "a198", "a199", "a200", "a201", "a202", "a203", "a204", "a205", "a206", "a207", "a208", "a209", "a210", "a211", "a212", "a213", "a214", "a215", "a216", "a217", "a218", "a219", "a220", "a221", "a222", "a223", "a224", "a225", "a226", "a227", "a228", "a229", "a230", "a231", "a232", "a233", "a234", "a235", "a236", "a237", "a238", "a239");

    f32x4 s = {pb[0] + pb[4], pb[1] + pb[5], pb[2] + pb[6], pb[3] + pb[7]};
#pragma unroll
    for (int i = 0; i < 8; ++i) s += (f32x4){pa[4 * i], pa[4 * i + 1], pa[4 * i + 2], pa[4 * i + 3]};
#pragma unroll
    for (int i = 0; i < 4; ++i) {
      auto r = __builtin_amdgcn_permlane32_swap(__float_as_uint(s[i]), __float_as_uint(s[i]), false, false);
      s[i] = __uint_as_float(r[0]) + __uint_as_float(r[1]);
    }
    LDSF4(lds + L_CB + (w * 24 + c24) * 16) = s;
    g1_epi(0);
    asm volatile("s_waitcnt vmcnt(4)" : "+v"(fdv), "+v"(gv)::"memory");
    if (b == 0 && tid == 0) ST64(WS_EPOCH, T);
    u32x4 gbv[4];
#pragma unroll
    for (int i = 0; i < 4; ++i) gbv[i] = (u32x4){gv[4 * i], gv[4 * i + 1], gv[4 * i + 2], gv[4 * i + 3]};
    auto ok = [&]() -> bool { return __all((modx(gbv[0], gbv[1], T[0]) | modx(gbv[2], gbv[3], T[0]) | (fdv[0] ^ T[0]) | (fdv[1] ^ T[1])) == 0); };
    if (__builtin_expect(!ok(), 0)) {
#pragma unroll 1
      for (int sp = 0; sp < SPIN_CAP; ++sp) {
        asm volatile("" ::: "memory");
        fdv = LD64(px[5]);
#pragma unroll
        for (int i = 0; i < 4; ++i) gbv[i] = LD128(px[6] + i * 16);
        if (ok()) break;
      }
    }
    LDSF2(lds + L_GB + (lane < 40 ? lane : 39) * 8) = (f32x2){modv(gbv[0], gbv[1]), modv(gbv[2], gbv[3])};
  }
  asm volatile(
      "s_mov_b32 s45, m0\n\t"
      "ds_read_b64_tr_b16 v[64:65], v44 offset:16384\n\t"
      "ds_read_b64_tr_b16 v[66:67], v44 offset:18432\n\t"
      "ds_read_b64_tr_b16 v[68:69], v45 offset:16384\n\t"
      "ds_read_b64_tr_b16 v[70:71], v45 offset:18432\n\t"
      "ds_read_b64_tr_b16 v[72:73], v46 offset:16384\n\t"
      "ds_read_b64_tr_b16 v[74:75], v46 offset:18432\n\t"
      "ds_read_b64_tr_b16 v[76:77], v47 offset:16384\n\t"
      "ds_read_b64_tr_b16 v[78:79], v47 offset:18432\n\t"
      "ds_read_b64_tr_b16 v[80:81], v44 offset:20480\n\t"
      "ds_read_b64_tr_b16 v[82:83], v44 offset:22528\n\t"
      "ds_read_b64_tr_b16 v[84:85], v45 offset:20480\n\t"
      "ds_read_b64_tr_b16 v[86:87], v45 offset:22528\n\t"
      "ds_read_b64_tr_b16 v[88:89], v46 offset:20480\n\t"
      "ds_read_b64_tr_b16 v[90:91], v46 offset:22528\n\t"
      "ds_read_b64_tr_b16 v[92:93], v47 offset:20480\n\t"
      "ds_read_b64_tr_b16 v[94:95], v47 offset:22528\n\t"
      "s_mov_b32 s44, 0x40000\n\t"
      "v_add_u32 v57, 0x10000, %[vchk]\n\t"
      "v_mov_b32 v52, 0x3f803f80\n\t"
      "v_mov_b32 v53, 0x3f803f80\n\t"
      "v_mov_b32 v54, 0x3f803f80\n\t"
      "v_mov_b32 v55, 0x3f803f80\n\t"
      "s_waitcnt lgkmcnt(0)\n\t"
      "v_mfma_f32_16x16x32_bf16 v[128:131], a[0:3], v[64:67], 0\n\t"
      "ds_read_b64_tr_b16 v[96:97], v44 offset:24576\n\t"
      "v_mfma_f32_16x16x32_bf16 v[144:147], a[4:7], v[64:67], 0\n\t"
      "s_add_u32 m0, %[m0b], 0\n\t"
      "s_add_u32 s36, %[xlo], 131072\n\t"
      "v_mfma_f32_16x16x32_bf16 v[160:163], a[8:11], v[64:67], 0\n\t"
      "ds_read_b64_tr_b16 v[98:99], v44 offset:26624\n\t"
      "ds_read_b64_tr_b16 v[100:101], v45 offset:24576\n\t"
      "v_mfma_f32_16x16x32_bf16 v[176:179], v[52:55], v[64:67], 0\n\t"
      "s_addc_u32 s37, %[xhi], 0\n\t"
      "global_load_lds_dwordx4 %[voff], s[36:37] offset:2048 sc1\n\t"
      "v_mfma_f32_16x16x32_bf16 v[180:183], v[64:67], v[64:67], 0\n\t"
      "ds_read_b64_tr_b16 v[102:103], v45 offset:26624\n\t"
      "ds_read_b64_tr_b16 v[104:105], v46 offset:24576\n\t"
      "v_mfma_f32_16x16x32_bf16 v[132:135], a[0:3], v[68:71], 0\n\t"
      "ds_read_b64_tr_b16 v[106:107], v46 offset:26624\n\t"
      "ds_read_b64_tr_b16 v[108:109], v47 offset:24576\n\t"
      "v_mfma_f32_16x16x32_bf16 v[148:151], a[4:7], v[68:71], 0\n\t"
      "ds_read_b64_tr_b16 v[110:111], v47 offset:26624\n\t"
      "v_mfma_f32_16x16x32_bf16 v[164:167], a[8:11], v[68:71], 0\n\t"
      "global_load_lds_dwordx4 %[voff], s[36:37] offset:3072 sc1\n\t"
      "v_mfma_f32_16x16x32_bf16 v[136:139], a[0:3], v[72:75], 0\n\t"
      "ds_read_b64_tr_b16 v[112:113], v44 offset:28672\n\t"
      "ds_read_b64_tr_b16 v[114:115], v44 offset:30720\n\t"
      "v_mfma_f32_16x16x32_bf16 v[152:155], a[4:7], v[72:75], 0\n\t"
      "ds_read_b64_tr_b16 v[116:117], v45 offset:28672\n\t"
      "v_mfma_f32_16x16x32_bf16 v[168:171], a[8:11], v[72:75], 0\n\t"
      "s_add_u32 m0, %[m0b], 16384\n\t"
      "s_add_u32 s36, %[xlo], 147456\n\t"
      "v_mfma_f32_16x16x32_bf16 v[140:143], a[0:3], v[76:79], 0\n\t"
      "ds_read_b64_tr_b16 v[118:119], v45 offset:30720\n\t"
      "ds_read_b64_tr_b16 v[120:121], v46 offset:28672\n\t"
      "v_mfma_f32_16x16x32_bf16 v[156:159], a[4:7], v[76:79], 0\n\t"
      "s_addc_u32 s37, %[xhi], 0\n\t"
      "global_load_lds_dwordx4 %[voff], s[36:37] offset:0 sc1\n\t"
      "v_mfma_f32_16x16x32_bf16 v[172:175], a[8:11], v[76:79], 0\n\t"
      "ds_read_b64_tr_b16 v[122:123], v46 offset:30720\n\t"
      "ds_read_b64_tr_b16 v[124:125], v47 offset:28672\n\t"
      "v_mfma_f32_16x16x32_bf16 v[128:131], a[12:15], v[80:83], v[128:131]\n\t"
      "ds_read_b64_tr_b16 v[126:127], v47 offset:30720\n\t"
      "v_mfma_f32_16x16x32_bf16 v[144:147], a[16:19], v[80:83], v[144:147]\n\t"
      "global_load_lds_dwordx4 %[voff], s[36:37] offset:1024 sc1\n\t"
      "v_mfma_f32_16x16x32_bf16 v[160:163], a[20:23], v[80:83], v[160:163]\n\t"
      "v_mfma_f32_16x16x32_bf16 v[176:179], v[52:55], v[80:83], v[176:179]\n\t"
      "ds_read_b128 v[240:243], v57 offset:32768\n\t"
      "v_mfma_f32_16x16x32_bf16 v[180:183], v[80:83], v[80:83], v[180:183]\n\t"
      "ds_read_b128 v[244:247], v57 offset:33792\n\t"
      "ds_read_b128 v[248:251], v57 offset:34816\n\t"
      "v_mfma_f32_16x16x32_bf16 v[132:135], a[12:15], v[84:87], v[132:135]\n\t"
      "ds_read_b128 v[252:255], v57 offset:35840\n\t"
      "v_mfma_f32_16x16x32_bf16 v[148:151], a[16:19], v[84:87], v[148:151]\n\t"
      "v_mfma_f32_16x16x32_bf16 v[164:167], a[20:23], v[84:87], v[164:167]\n\t"
      "v_mfma_f32_16x16x32_bf16 v[136:139], a[12:15], v[88:91], v[136:139]\n\t"
      "s_waitcnt lgkmcnt(0)\n\t"
      "v_max3_u32 v56, v240, v241, v242\n\t"
      "v_mfma_f32_16x16x32_bf16 v[152:155], a[16:19], v[88:91], v[152:155]\n\t"
      "v_max3_u32 v56, v56, v243, v244\n\t"
      "v_max3_u32 v56, v56, v245, v246\n\t"
      "v_mfma_f32_16x16x32_bf16 v[168:171], a[20:23], v[88:91], v[168:171]\n\t"
      "v_max3_u32 v56, v56, v247, v248\n\t"
      "v_max3_u32 v56, v56, v249, v250\n\t"
      "v_mfma_f32_16x16x32_bf16 v[140:143], a[12:15], v[92:95], v[140:143]\n\t"
      "v_max3_u32 v56, v56, v251, v252\n\t"
      "v_max3_u32 v56, v56, v253, v254\n\t"
      "v_mfma_f32_16x16x32_bf16 v[156:159], a[16:19], v[92:95], v[156:159]\n\t"
      "v_max_u32 v56, v56, v255\n\t"
      "v_cmp_eq_u32 vcc, -1, v56\n\t"
      "v_mfma_f32_16x16x32_bf16 v[172:175], a[20:23], v[92:95], v[172:175]\n\t"
      "s_cbranch_vccnz L_g1_slow6_%=\n\t"
      "L_g1_back6_%=:\n\t"
      "s_waitcnt lgkmcnt(0)\n\t"
      "s_barrier\n\t"
      "v_mfma_f32_16x16x32_bf16 v[128:131], a[24:27], v[96:99], v[128:131]\n\t"
      "ds_read_b64_tr_b16 v[64:65], v44 offset:32768\n\t"
      "v_mfma_f32_16x16x32_bf16 v[144:147], a[28:31], v[96:99], v[144:147]\n\t"
      "s_add_u32 m0, %[m0b], 16384\n\t"
      "s_add_u32 s36, %[xlo], 147456\n\t"
      "v_mfma_f32_16x16x32_bf16 v[160:163], a[32:35], v[96:99], v[160:163]\n\t"
      "ds_read_b64_tr_b16 v[66:67], v44 offset:34816\n\t"
      "v_mfma_f32_16x16x32_bf16 v[176:179], v[52:55], v[96:99], v[176:179]\n\t"
      "s_addc_u32 s37, %[xhi], 0\n\t"
      "global_load_lds_dwordx4 %[voff], s[36:37] offset:2048 sc1\n\t"
      "v_mfma_f32_16x16x32_bf16 v[180:183], v[96:99], v[96:99], v[180:183]\n\t"
      "ds_read_b64_tr_b16 v[68:69], v45 offset:32768\n\t"
      "ds_read_b64_tr_b16 v[70:71], v45 offset:34816\n\t"
      "v_mfma_f32_16x16x32_bf16 v[132:135], a[24:27], v[100:103], v[132:135]\n\t"
      "ds_read_b64_tr_b16 v[72:73], v46 offset:32768\n\t"
      "v_mfma_f32_16x16x32_bf16 v[148:151], a[28:31], v[100:103], v[148:151]\n\t"
      "global_load_lds_dwordx4 %[voff], s[36:37] offset:3072 sc1\n\t"
      "v_mfma_f32_16x16x32_bf16 v[164:167], a[32:35], v[100:103], v[164:167]\n\t"
      "ds_read_b64_tr_b16 v[74:75], v46 offset:34816\n\t"
      "ds_read_b64_tr_b16 v[76:77], v47 offset:32768\n\t"
      "v_mfma_f32_16x16x32_bf16 v[136:139], a[24:27], v[104:107], v[136:139]\n\t"
      "s_add_u32 s40, %[dlo], 4096\n\t"
      "s_addc_u32 s41, %[dhi], 0\n\t"
      "v_mfma_f32_16x16x32_bf16 v[152:155], a[28:31], v[104:107], v[152:155]\n\t"
      "ds_read_b64_tr_b16 v[78:79], v47 offset:34816\n\t"
      "ds_read_b64_tr_b16 v[80:81], v44 offset:36864\n\t"
      "v_mfma_f32_16x16x32_bf16 v[168:171], a[32:35], v[104:107], v[168:171]\n\t"
      "global_load_dwordx4 a[0:3], %[l16], s[40:41] offset:-4096 sc1\n\t"
      "v_mfma_f32_16x16x32_bf16 v[140:143], a[24:27], v[108:111], v[140:143]\n\t"
      "ds_read_b64_tr_b16 v[82:83], v44 offset:38912\n\t"
      "ds_read_b64_tr_b16 v[84:85], v45 offset:36864\n\t"
      "v_mfma_f32_16x16x32_bf16 v[156:159], a[28:31], v[108:111], v[156:159]\n\t"
      "global_load_dwordx4 a[4:7], %[l16], s[40:41] offset:-3072 sc1\n\t"
      "v_mfma_f32_16x16x32_bf16 v[172:175], a[32:35], v[108:111], v[172:175]\n\t"
      "ds_read_b64_tr_b16 v[86:87], v45 offset:38912\n\t"
      "ds_read_b64_tr_b16 v[88:89], v46 offset:36864\n\t"
      "v_mfma_f32_16x16x32_bf16 v[128:131], a[36:39], v[112:115], v[128:131]\n\t"
      "global_load_dwordx4 a[8:11], %[l16], s[40:41] offset:-2048 sc1\n\t"
      "v_mfma_f32_16x16x32_bf16 v[144:147], a[40:43], v[112:115], v[144:147]\n\t"
      "ds_read_b64_tr_b16 v[90:91], v46 offset:38912\n\t"
      "ds_read_b64_tr_b16 v[92:93], v47 offset:36864\n\t"
      "v_mfma_f32_16x16x32_bf16 v[160:163], a[44:47], v[112:115], v[160:163]\n\t"
      "global_load_dwordx4 a[12:15], %[l16], s[40:41] offset:-1024 sc1\n\t"
      "v_mfma_f32_16x16x32_bf16 v[176:179], v[52:55], v[112:115], v[176:179]\n\t"
      "ds_read_b64_tr_b16 v[94:95], v47 offset:38912\n\t"
      "v_mfma_f32_16x16x32_bf16 v[180:183], v[112:115], v[112:115], v[180:183]\n\t"
      "global_load_dwordx4 a[16:19], %[l16], s[40:41] offset:0 sc1\n\t"
      "v_mfma_f32_16x16x32_bf16 v[132:135], a[36:39], v[116:119], v[132:135]\n\t"
      "global_load_dwordx4 a[20:23], %[l16], s[40:41] offset:1024 sc1\n\t"
      "v_mfma_f32_16x16x32_bf16 v[148:151], a[40:43], v[116:119], v[148:151]\n\t"
      "v_mfma_f32_16x16x32_bf16 v[164:167], a[44:47], v[116:119], v[164:167]\n\t"
      "v_mfma_f32_16x16x32_bf16 v[136:139], a[36:39], v[120:123], v[136:139]\n\t"
      "v_mfma_f32_16x16x32_bf16 v[152:155], a[40:43], v[120:123], v[152:155]\n\t"
      "v_mfma_f32_16x16x32_bf16 v[168:171], a[44:47], v[120:123], v[168:171]\n\t"
      "v_mfma_f32_16x16x32_bf16 v[140:143], a[36:39], v[124:127], v[140:143]\n\t"
      "v_mfma_f32_16x16x32_bf16 v[156:159], a[40:43], v[124:127], v[156:159]\n\t"
      "v_mfma_f32_16x16x32_bf16 v[172:175], a[44:47], v[124:127], v[172:175]\n\t"
      "s_waitcnt lgkmcnt(0)\n\t"
      "v_mfma_f32_16x16x32_bf16 v[128:131], a[48:51], v[64:67], v[128:131]\n\t"
      "ds_read_b64_tr_b16 v[96:97], v44 offset:40960\n\t"
      "v_mfma_f32_16x16x32_bf16 v[144:147], a[52:55], v[64:67], v[144:147]\n\t"
      "global_load_dwordx4 a[24:27], %[l16], s[40:41] offset:2048 sc1\n\t"
      "v_mfma_f32_16x16x32_bf16 v[160:163], a[56:59], v[64:67], v[160:163]\n\t"
      "ds_read_b64_tr_b16 v[98:99], v44 offset:43008\n\t"
      "ds_read_b64_tr_b16 v[100:101], v45 offset:40960\n\t"
      "v_mfma_f32_16x16x32_bf16 v[176:179], v[52:55], v[64:67], v[176:179]\n\t"
      "global_load_dwordx4 a[28:31], %[l16], s[40:41] offset:3072 sc1\n\t"
      "v_mfma_f32_16x16x32_bf16 v[180:183], v[64:67], v[64:67], v[180:183]\n\t"
      "ds_read_b64_tr_b16 v[102:103], v45 offset:43008\n\t"
      "ds_read_b64_tr_b16 v[104:105], v46 offset:40960\n\t"
      "v_mfma_f32_16x16x32_bf16 v[132:135], a[48:51], v[68:71], v[132:135]\n\t"
      "ds_read_b64_tr_b16 v[106:107], v46 offset:43008\n\t"
      "v_mfma_f32_16x16x32_bf16 v[148:151], a[52:55], v[68:71], v[148:151]\n\t"
      "s_add_u32 s40, %[dlo], 12288\n\t"
      "s_addc_u32 s41, %[dhi], 0\n\t"
      "v_mfma_f32_16x16x32_bf16 v[164:167], a[56:59], v[68:71], v[164:167]\n\t"
      "ds_read_b64_tr_b16 v[108:109], v47 offset:40960\n\t"
      "ds_read_b64_tr_b16 v[110:111], v47 offset:43008\n\t"
      "v_mfma_f32_16x16x32_bf16 v[136:139], a[48:51], v[72:75], v[136:139]\n\t"
      "ds_read_b64_tr_b16 v[112:113], v44 offset:45056\n\t"
      "v_mfma_f32_16x16x32_bf16 v[152:155], a[52:55], v[72:75], v[152:155]\n\t"
      "global_load_dwordx4 a[32:35], %[l16], s[40:41] offset:-4096 sc1\n\t"
      "v_mfma_f32_16x16x32_bf16 v[168:171], a[56:59], v[72:75], v[168:171]\n\t"
      "ds_read_b64_tr_b16 v[114:115], v44 offset:47104\n\t"
      "ds_read_b64_tr_b16 v[116:117], v45 offset:45056\n\t"
      "v_mfma_f32_16x16x32_bf16 v[140:143], a[48:51], v[76:79], v[140:143]\n\t"
      "ds_read_b64_tr_b16 v[118:119], v45 offset:47104\n\t"
      "v_mfma_f32_16x16x32_bf16 v[156:159], a[52:55], v[76:79], v[156:159]\n\t"
      "global_load_dwordx4 a[36:39], %[l16], s[40:41] offset:-3072 sc1\n\t"
      "v_mfma_f32_16x16x32_bf16 v[172:175], a[56:59], v[76:79], v[172:175]\n\t"
      "ds_read_b64_tr_b16 v[120:121], v46 offset:45056\n\t"
      "ds_read_b64_tr_b16 v[122:123], v46 offset:47104\n\t"
      "v_mfma_f32_16x16x32_bf16 v[128:131], a[60:63], v[80:83], v[128:131]\n\t"
      "ds_read_b64_tr_b16 v[124:125], v47 offset:45056\n\t"
      "v_mfma_f32_16x16x32_bf16 v[144:147], a[64:67], v[80:83], v[144:147]\n\t"
      "global_load_dwordx4 a[40:43], %[l16], s[40:41] offset:-2048 sc1\n\t"
      "v_mfma_f32_16x16x32_bf16 v[160:163], a[68:71], v[80:83], v[160:163]\n\t"
      "ds_read_b64_tr_b16 v[126:127], v47 offset:47104\n\t"
      "v_mfma_f32_16x16x32_bf16 v[176:179], v[52:55], v[80:83], v[176:179]\n\t"
      "ds_read_b128 v[240:243], v57 offset:49152\n\t"
      "v_mfma_f32_16x16x32_bf16 v[180:183], v[80:83], v[80:83], v[180:183]\n\t"
      "ds_read_b128 v[244:247], v57 offset:50176\n\t"
      "ds_read_b128 v[248:251], v57 offset:51200\n\t"
      "v_mfma_f32_16x16x32_bf16 v[132:135], a[60:63], v[84:87], v[132:135]\n\t"
      "ds_read_b128 v[252:255], v57 offset:52224\n\t"
      "v_mfma_f32_16x16x32_bf16 v[148:151], a[64:67], v[84:87], v[148:151]\n\t"
      "global_load_dwordx4 a[44:47], %[l16], s[40:41] offset:-1024 sc1\n\t"
      "v_mfma_f32_16x16x32_bf16 v[164:167], a[68:71], v[84:87], v[164:167]\n\t"
      "v_mfma_f32_16x16x32_bf16 v[136:139], a[60:63], v[88:91], v[136:139]\n\t"
      "s_waitcnt lgkmcnt(0)\n\t"
      "v_max3_u32 v56, v240, v241, v242\n\t"
      "v_mfma_f32_16x16x32_bf16 v[152:155], a[64:67], v[88:91], v[152:155]\n\t"
      "v_max3_u32 v56, v56, v243, v244\n\t"
      "v_max3_u32 v56, v56, v245, v246\n\t"
      "v_mfma_f32_16x16x32_bf16 v[168:171], a[68:71], v[88:91], v[168:171]\n\t"
      "v_max3_u32 v56, v56, v247, v248\n\t"
      "v_max3_u32 v56, v56, v249, v250\n\t"
      "v_mfma_f32_16x16x32_bf16 v[140:143], a[60:63], v[92:95], v[140:143]\n\t"
      "v_max3_u32 v56, v56, v251, v252\n\t"
      "v_max3_u32 v56, v56, v253, v254\n\t"
      "v_mfma_f32_16x16x32_bf16 v[156:159], a[64:67], v[92:95], v[156:159]\n\t"
      "v_max_u32 v56, v56, v255\n\t"
      "v_cmp_eq_u32 vcc, -1, v56\n\t"
      "v_mfma_f32_16x16x32_bf16 v[172:175], a[68:71], v[92:95], v[172:175]\n\t"
      "s_cbranch_vccnz L_g1_slow7_%=\n\t"
      "L_g1_back7_%=:\n\t"
      "s_waitcnt lgkmcnt(0)\n\t"
      "s_barrier\n\t"
      "v_mfma_f32_16x16x32_bf16 v[128:131], a[72:75], v[96:99], v[128:131]\n\t"
      "ds_read_b64_tr_b16 v[64:65], v44 offset:49152\n\t"
      "v_mfma_f32_16x16x32_bf16 v[144:147], a[76:79], v[96:99], v[144:147]\n\t"
      "global_load_dwordx4 a[48:51], %[l16], s[40:41] offset:0 sc1\n\t"
      "v_mfma_f32_16x16x32_bf16 v[160:163], a[80:83], v[96:99], v[160:163]\n\t"
      "ds_read_b64_tr_b16 v[66:67], v44 offset:51200\n\t"
      "ds_read_b64_tr_b16 v[68:69], v45 offset:49152\n\t"
      "v_mfma_f32_16x16x32_bf16 v[176:179], v[52:55], v[96:99], v[176:179]\n\t"
      "global_load_dwordx4 a[52:55], %[l16], s[40:41] offset:1024 sc1\n\t"
      "v_mfma_f32_16x16x32_bf16 v[180:183], v[96:99], v[96:99], v[180:183]\n\t"
      "ds_read_b64_tr_b16 v[70:71], v45 offset:51200\n\t"
      "ds_read_b64_tr_b16 v[72:73], v46 offset:49152\n\t"
      "v_mfma_f32_16x16x32_bf16 v[132:135], a[72:75], v[100:103], v[132:135]\n\t"
      "ds_read_b64_tr_b16 v[74:75], v46 offset:51200\n\t"
      "v_mfma_f32_16x16x32_bf16 v[148:151], a[76:79], v[100:103], v[148:151]\n\t"
      "global_load_dwordx4 a[56:59], %[l16], s[40:41] offset:2048 sc1\n\t"
      "v_mfma_f32_16x16x32_bf16 v[164:167], a[80:83], v[100:103], v[164:167]\n\t"
      "ds_read_b64_tr_b16 v[76:77], v47 offset:49152\n\t"
      "ds_read_b64_tr_b16 v[78:79], v47 offset:51200\n\t"
      "v_mfma_f32_16x16x32_bf16 v[136:139], a[72:75], v[104:107], v[136:139]\n\t"
      "ds_read_b64_tr_b16 v[80:81], v44 offset:53248\n\t"
      "v_mfma_f32_16x16x32_bf16 v[152:155], a[76:79], v[104:107], v[152:155]\n\t"
      "global_load_dwordx4 a[60:63], %[l16], s[40:41] offset:3072 sc1\n\t"
      "v_mfma_f32_16x16x32_bf16 v[168:171], a[80:83], v[104:107], v[168:171]\n\t"
      "ds_read_b64_tr_b16 v[82:83], v44 offset:55296\n\t"
      "ds_read_b64_tr_b16 v[84:85], v45 offset:53248\n\t"
      "v_mfma_f32_16x16x32_bf16 v[140:143], a[72:75], v[108:111], v[140:143]\n\t"
      "ds_read_b64_tr_b16 v[86:87], v45 offset:55296\n\t"
      "v_mfma_f32_16x16x32_bf16 v[156:159], a[76:79], v[108:111], v[156:159]\n\t"
      "s_add_u32 s40, %[dlo], 20480\n\t"
      "s_addc_u32 s41, %[dhi], 0\n\t"
      "v_mfma_f32_16x16x32_bf16 v[172:175], a[80:83], v[108:111], v[172:175]\n\t"
      "ds_read_b64_tr_b16 v[88:89], v46 offset:53248\n\t"
      "ds_read_b64_tr_b16 v[90:91], v46 offset:55296\n\t"
      "v_mfma_f32_16x16x32_bf16 v[128:131], a[84:87], v[112:115], v[128:131]\n\t"
      "ds_read_b64_tr_b16 v[92:93], v47 offset:53248\n\t"
      "v_mfma_f32_16x16x32_bf16 v[144:147], a[88:91], v[112:115], v[144:147]\n\t"
      "global_load_dwordx4 a[64:67], %[l16], s[40:41] offset:-4096 sc1\n\t"
      "v_mfma_f32_16x16x32_bf16 v[160:163], a[92:95], v[112:115], v[160:163]\n\t"
      "ds_read_b64_tr_b16 v[94:95], v47 offset:55296\n\t"
      "v_mfma_f32_16x16x32_bf16 v[176:179], v[52:55], v[112:115], v[176:179]\n\t"
      "global_load_dwordx4 a[68:71], %[l16], s[40:41] offset:-3072 sc1\n\t"
      "v_mfma_f32_16x16x32_bf16 v[180:183], v[112:115], v[112:115], v[180:183]\n\t"
      "v_mfma_f32_16x16x32_bf16 v[132:135], a[84:87], v[116:119], v[132:135]\n\t"
      "v_mfma_f32_16x16x32_bf16 v[148:151], a[88:91], v[116:119], v[148:151]\n\t"
      "v_mfma_f32_16x16x32_bf16 v[164:167], a[92:95], v[116:119], v[164:167]\n\t"
      "v_mfma_f32_16x16x32_bf16 v[136:139], a[84:87], v[120:123], v[136:139]\n\t"
      "v_mfma_f32_16x16x32_bf16 v[152:155], a[88:91], v[120:123], v[152:155]\n\t"
      "v_mfma_f32_16x16x32_bf16 v[168:171], a[92:95], v[120:123], v[168:171]\n\t"
      "v_mfma_f32_16x16x32_bf16 v[140:143], a[84:87], v[124:127], v[140:143]\n\t"
      "v_mfma_f32_16x16x32_bf16 v[156:159], a[88:91], v[124:127], v[156:159]\n\t"
      "v_mfma_f32_16x16x32_bf16 v[172:175], a[92:95], v[124:127], v[172:175]\n\t"
      "s_waitcnt lgkmcnt(0)\n\t"
      "v_mfma_f32_16x16x32_bf16 v[128:131], a[96:99], v[64:67], v[128:131]\n\t"
      "ds_read_b64_tr_b16 v[96:97], v44 offset:57344\n\t"
      "v_mfma_f32_16x16x32_bf16 v[144:147], a[100:103], v[64:67], v[144:147]\n\t"
      "global_load_dwordx4 a[72:75], %[l16], s[40:41] offset:-2048 sc1\n\t"
      "v_mfma_f32_16x16x32_bf16 v[160:163], a[104:107], v[64:67], v[160:163]\n\t"
      "ds_read_b64_tr_b16 v[98:99], v44 offset:59392\n\t"
      "ds_read_b64_tr_b16 v[100:101], v45 offset:57344\n\t"
      "v_mfma_f32_16x16x32_bf16 v[176:179], v[52:55], v[64:67], v[176:179]\n\t"
      "ds_read_b64_tr_b16 v[102:103], v45 offset:59392\n\t"
      "v_mfma_f32_16x16x32_bf16 v[180:183], v[64:67], v[64:67], v[180:183]\n\t"
      "global_load_dwordx4 a[76:79], %[l16], s[40:41] offset:-1024 sc1\n\t"
      "v_mfma_f32_16x16x32_bf16 v[132:135], a[96:99], v[68:71], v[132:135]\n\t"
      "ds_read_b64_tr_b16 v[104:105], v46 offset:57344\n\t"
      "ds_read_b64_tr_b16 v[106:107], v46 offset:59392\n\t"
      "v_mfma_f32_16x16x32_bf16 v[148:151], a[100:103], v[68:71], v[148:151]\n\t"
      "ds_read_b64_tr_b16 v[108:109], v47 offset:57344\n\t"
      "v_mfma_f32_16x16x32_bf16 v[164:167], a[104:107], v[68:71], v[164:167]\n\t"
      "global_load_dwordx4 a[80:83], %[l16], s[40:41] offset:0 sc1\n\t"
      "v_mfma_f32_16x16x32_bf16 v[136:139], a[96:99], v[72:75], v[136:139]\n\t"
      "ds_read_b64_tr_b16 v[110:111], v47 offset:59392\n\t"
      "ds_read_b64_tr_b16 v[112:113], v44 offset:61440\n\t"
      "v_mfma_f32_16x16x32_bf16 v[152:155], a[100:103], v[72:75], v[152:155]\n\t"
      "ds_read_b64_tr_b16 v[114:115], v44 offset:63488\n\t"
      "ds_read_b64_tr_b16 v[116:117], v45 offset:61440\n\t"
      "v_mfma_f32_16x16x32_bf16 v[168:171], a[104:107], v[72:75], v[168:171]\n\t"
      "global_load_dwordx4 a[84:87], %[l16], s[40:41] offset:1024 sc1\n\t"
      "v_mfma_f32_16x16x32_bf16 v[140:143], a[96:99], v[76:79], v[140:143]\n\t"
      "ds_read_b64_tr_b16 v[118:119], v45 offset:63488\n\t"
      "ds_read_b64_tr_b16 v[120:121], v46 offset:61440\n\t"
      "v_mfma_f32_16x16x32_bf16 v[156:159], a[100:103], v[76:79], v[156:159]\n\t"
      "ds_read_b64_tr_b16 v[122:123], v46 offset:63488\n\t"
      "v_mfma_f32_16x16x32_bf16 v[172:175], a[104:107], v[76:79], v[172:175]\n\t"
      "global_load_dwordx4 a[88:91], %[l16], s[40:41] offset:2048 sc1\n\t"
      "v_mfma_f32_16x16x32_bf16 v[128:131], a[108:111], v[80:83], v[128:131]\n\t"
      "ds_read_b64_tr_b16 v[124:125], v47 offset:61440\n\t"
      "ds_read_b64_tr_b16 v[126:127], v47 offset:63488\n\t"
      "v_mfma_f32_16x16x32_bf16 v[144:147], a[112:115], v[80:83], v[144:147]\n\t"
      "global_load_dwordx4 a[92:95], %[l16], s[40:41] offset:3072 sc1\n\t"
      "v_mfma_f32_16x16x32_bf16 v[160:163], a[116:119], v[80:83], v[160:163]\n\t"
      "v_mfma_f32_16x16x32_bf16 v[176:179], v[52:55], v[80:83], v[176:179]\n\t"
      "s_waitcnt vmcnt(28)\n\t"
      "ds_read_b128 v[240:243], %[vchk] offset:0\n\t"
      "v_mfma_f32_16x16x32_bf16 v[180:183], v[80:83], v[80:83], v[180:183]\n\t"
      "ds_read_b128 v[244:247], %[vchk] offset:1024\n\t"
      "ds_read_b128 v[248:251], %[vchk] offset:2048\n\t"
      "v_mfma_f32_16x16x32_bf16 v[132:135], a[108:111], v[84:87], v[132:135]\n\t"
      "ds_read_b128 v[252:255], %[vchk] offset:3072\n\t"
      "v_mfma_f32_16x16x32_bf16 v[148:151], a[112:115], v[84:87], v[148:151]\n\t"
      "v_mfma_f32_16x16x32_bf16 v[164:167], a[116:119], v[84:87], v[164:167]\n\t"
      "v_mfma_f32_16x16x32_bf16 v[136:139], a[108:111], v[88:91], v[136:139]\n\t"
      "s_waitcnt lgkmcnt(0)\n\t"
      "v_max3_u32 v56, v240, v241, v242\n\t"
      "v_mfma_f32_16x16x32_bf16 v[152:155], a[112:115], v[88:91], v[152:155]\n\t"
      "v_max3_u32 v56, v56, v243, v244\n\t"
      "v_max3_u32 v56, v56, v245, v246\n\t"
      "v_mfma_f32_16x16x32_bf16 v[168:171], a[116:119], v[88:91], v[168:171]\n\t"
      "v_max3_u32 v56, v56, v247, v248\n\t"
      "v_max3_u32 v56, v56, v249, v250\n\t"
      "v_mfma_f32_16x16x32_bf16 v[140:143], a[108:111], v[92:95], v[140:143]\n\t"
      "v_max3_u32 v56, v56, v251, v252\n\t"
      "v_max3_u32 v56, v56, v253, v254\n\t"
      "v_mfma_f32_16x16x32_bf16 v[156:159], a[112:115], v[92:95], v[156:159]\n\t"
      "v_max_u32 v56, v56, v255\n\t"
      "v_cmp_eq_u32 vcc, -1, v56\n\t"
      "v_mfma_f32_16x16x32_bf16 v[172:175], a[116:119], v[92:95], v[172:175]\n\t"
      "s_cbranch_vccnz L_g1_slow8_%=\n\t"
      "L_g1_back8_%=:\n\t"
      "s_waitcnt lgkmcnt(0)\n\t"
      "s_barrier\n\t"
      "v_mfma_f32_16x16x32_bf16 v[128:131], a[120:123], v[96:99], v[128:131]\n\t"
      "ds_read_b64_tr_b16 v[64:65], v40 offset:0\n\t"
      "v_mfma_f32_16x16x32_bf16 v[144:147], a[124:127], v[96:99], v[144:147]\n\t"
      "s_add_u32 s40, %[dlo], 28672\n\t"
      "s_addc_u32 s41, %[dhi], 0\n\t"
      "v_mfma_f32_16x16x32_bf16 v[160:163], a[128:131], v[96:99], v[160:163]\n\t"
      "ds_read_b64_tr_b16 v[66:67], v40 offset:2048\n\t"
      "ds_read_b64_tr_b16 v[68:69], v41 offset:0\n\t"
      "v_mfma_f32_16x16x32_bf16 v[176:179], v[52:55], v[96:99], v[176:179]\n\t"
      "global_load_dwordx4 a[96:99], %[l16], s[40:41] offset:-4096 sc1\n\t"
      "v_mfma_f32_16x16x32_bf16 v[180:183], v[96:99], v[96:99], v[180:183]\n\t"
      "ds_read_b64_tr_b16 v[70:71], v41 offset:2048\n\t"
      "ds_read_b64_tr_b16 v[72:73], v42 offset:0\n\t"
      "v_mfma_f32_16x16x32_bf16 v[132:135], a[120:123], v[100:103], v[132:135]\n\t"
      "global_load_dwordx4 a[100:103], %[l16], s[40:41] offset:-3072 sc1\n\t"
      "v_mfma_f32_16x16x32_bf16 v[148:151], a[124:127], v[100:103], v[148:151]\n\t"
      "ds_read_b64_tr_b16 v[74:75], v42 offset:2048\n\t"
      "v_mfma_f32_16x16x32_bf16 v[164:167], a[128:131], v[100:103], v[164:167]\n\t"
      "global_load_dwordx4 a[104:107], %[l16], s[40:41] offset:-2048 sc1\n\t"
      "v_mfma_f32_16x16x32_bf16 v[136:139], a[120:123], v[104:107], v[136:139]\n\t"
      "ds_read_b64_tr_b16 v[76:77], v43 offset:0\n\t"
      "ds_read_b64_tr_b16 v[78:79], v43 offset:2048\n\t"
      "v_mfma_f32_16x16x32_bf16 v[152:155], a[124:127], v[104:107], v[152:155]\n\t"
      "ds_read_b64_tr_b16 v[80:81], v40 offset:4096\n\t"
      "v_mfma_f32_16x16x32_bf16 v[168:171], a[128:131], v[104:107], v[168:171]\n\t"
      "global_load_dwordx4 a[108:111], %[l16], s[40:41] offset:-1024 sc1\n\t"
      "v_mfma_f32_16x16x32_bf16 v[140:143], a[120:123], v[108:111], v[140:143]\n\t"
      "ds_read_b64_tr_b16 v[82:83], v40 offset:6144\n\t"
      "ds_read_b64_tr_b16 v[84:85], v41 offset:4096\n\t"
      "v_mfma_f32_16x16x32_bf16 v[156:159], a[124:127], v[108:111], v[156:159]\n\t"
      "global_load_dwordx4 a[112:115], %[l16], s[40:41] offset:0 sc1\n\t"
      "v_mfma_f32_16x16x32_bf16 v[172:175], a[128:131], v[108:111], v[172:175]\n\t"
      "ds_read_b64_tr_b16 v[86:87], v41 offset:6144\n\t"
      "v_mfma_f32_16x16x32_bf16 v[128:131], a[132:135], v[112:115], v[128:131]\n\t"
      "global_load_dwordx4 a[116:119], %[l16], s[40:41] offset:1024 sc1\n\t"
      "v_mfma_f32_16x16x32_bf16 v[144:147], a[136:139], v[112:115], v[144:147]\n\t"
      "ds_read_b64_tr_b16 v[88:89], v42 offset:4096\n\t"
      "ds_read_b64_tr_b16 v[90:91], v42 offset:6144\n\t"
      "v_mfma_f32_16x16x32_bf16 v[160:163], a[140:143], v[112:115], v[160:163]\n\t"
      "s_add_u32 s38, %[hlo], 4096\n\t"
      "s_addc_u32 s39, %[hhi], 0\n\t"
      "v_mfma_f32_16x16x32_bf16 v[176:179], v[52:55], v[112:115], v[176:179]\n\t"
      "ds_read_b64_tr_b16 v[92:93], v43 offset:4096\n\t"
      "ds_read_b64_tr_b16 v[94:95], v43 offset:6144\n\t"
      "v_mfma_f32_16x16x32_bf16 v[180:183], v[112:115], v[112:115], v[180:183]\n\t"
      "global_load_dwordx4 v[208:211], %[l16], s[38:39] offset:-4096 sc1\n\t"
      "v_mfma_f32_16x16x32_bf16 v[132:135], a[132:135], v[116:119], v[132:135]\n\t"
      "global_load_dwordx4 v[212:215], %[l16], s[38:39] offset:-3072 sc1\n\t"
      "v_mfma_f32_16x16x32_bf16 v[148:151], a[136:139], v[116:119], v[148:151]\n\t"
      "v_mfma_f32_16x16x32_bf16 v[164:167], a[140:143], v[116:119], v[164:167]\n\t"
      "v_mfma_f32_16x16x32_bf16 v[136:139], a[132:135], v[120:123], v[136:139]\n\t"
      "v_mfma_f32_16x16x32_bf16 v[152:155], a[136:139], v[120:123], v[152:155]\n\t"
      "v_mfma_f32_16x16x32_bf16 v[168:171], a[140:143], v[120:123], v[168:171]\n\t"
      "v_mfma_f32_16x16x32_bf16 v[140:143], a[132:135], v[124:127], v[140:143]\n\t"
      "v_mfma_f32_16x16x32_bf16 v[156:159], a[136:139], v[124:127], v[156:159]\n\t"
      "v_mfma_f32_16x16x32_bf16 v[172:175], a[140:143], v[124:127], v[172:175]\n\t"
      "s_waitcnt lgkmcnt(0)\n\t"
      "v_mfma_f32_16x16x32_bf16 v[128:131], a[144:147], v[64:67], v[128:131]\n\t"
      "ds_read_b64_tr_b16 v[96:97], v40 offset:8192\n\t"
      "v_mfma_f32_16x16x32_bf16 v[144:147], a[148:151], v[64:67], v[144:147]\n\t"
      "global_load_dwordx4 a[120:123], %[l16], s[40:41] offset:2048 sc1\n\t"
      "v_mfma_f32_16x16x32_bf16 v[160:163], a[152:155], v[64:67], v[160:163]\n\t"
      "ds_read_b64_tr_b16 v[98:99], v40 offset:10240\n\t"
      "ds_read_b64_tr_b16 v[100:101], v41 offset:8192\n\t"
      "v_mfma_f32_16x16x32_bf16 v[176:179], v[52:55], v[64:67], v[176:179]\n\t"
      "global_load_dwordx4 a[124:127], %[l16], s[40:41] offset:3072 sc1\n\t"
      "v_mfma_f32_16x16x32_bf16 v[180:183], v[64:67], v[64:67], v[180:183]\n\t"
      "ds_read_b64_tr_b16 v[102:103], v41 offset:10240\n\t"
      "ds_read_b64_tr_b16 v[104:105], v42 offset:8192\n\t"
      "v_mfma_f32_16x16x32_bf16 v[132:135], a[144:147], v[68:71], v[132:135]\n\t"
      "s_add_u32 s40, %[dlo], 36864\n\t"
      "s_addc_u32 s41, %[dhi], 0\n\t"
      "v_mfma_f32_16x16x32_bf16 v[148:151], a[148:151], v[68:71], v[148:151]\n\t"
      "ds_read_b64_tr_b16 v[106:107], v42 offset:10240\n\t"
      "ds_read_b64_tr_b16 v[108:109], v43 offset:8192\n\t"
      "v_mfma_f32_16x16x32_bf16 v[164:167], a[152:155], v[68:71], v[164:167]\n\t"
      "global_load_dwordx4 a[128:131], %[l16], s[40:41] offset:-4096 sc1\n\t"
      "v_mfma_f32_16x16x32_bf16 v[136:139], a[144:147], v[72:75], v[136:139]\n\t"
      "ds_read_b64_tr_b16 v[110:111], v43 offset:10240\n\t"
      "ds_read_b64_tr_b16 v[112:113], v40 offset:12288\n\t"
      "v_mfma_f32_16x16x32_bf16 v[152:155], a[148:151], v[72:75], v[152:155]\n\t"
      "global_load_dwordx4 a[132:135], %[l16], s[40:41] offset:-3072 sc1\n\t"
      "v_mfma_f32_16x16x32_bf16 v[168:171], a[152:155], v[72:75], v[168:171]\n\t"
      "ds_read_b64_tr_b16 v[114:115], v40 offset:14336\n\t"
      "ds_read_b64_tr_b16 v[116:117], v41 offset:12288\n\t"
      "v_mfma_f32_16x16x32_bf16 v[140:143], a[144:147], v[76:79], v[140:143]\n\t"
      "ds_read_b64_tr_b16 v[118:119], v41 offset:14336\n\t"
      "v_mfma_f32_16x16x32_bf16 v[156:159], a[148:151], v[76:79], v[156:159]\n\t"
      "global_load_dwordx4 a[136:139], %[l16], s[40:41] offset:-2048 sc1\n\t"
      "v_mfma_f32_16x16x32_bf16 v[172:175], a[152:155], v[76:79], v[172:175]\n\t"
      "ds_read_b64_tr_b16 v[120:121], v42 offset:12288\n\t"
      "ds_read_b64_tr_b16 v[122:123], v42 offset:14336\n\t"
      "v_mfma_f32_16x16x32_bf16 v[128:131], a[156:159], v[80:83], v[128:131]\n\t"
      "global_load_dwordx4 a[140:143], %[l16], s[40:41] offset:-1024 sc1\n\t"
      "v_mfma_f32_16x16x32_bf16 v[144:147], a[160:163], v[80:83], v[144:147]\n\t"
      "ds_read_b64_tr_b16 v[124:125], v43 offset:12288\n\t"
      "ds_read_b64_tr_b16 v[126:127], v43 offset:14336\n\t"
      "v_mfma_f32_16x16x32_bf16 v[160:163], a[164:167], v[80:83], v[160:163]\n\t"
      "global_load_dwordx4 v[216:219], %[l16], s[38:39] offset:0 sc1\n\t"
      "v_mfma_f32_16x16x32_bf16 v[176:179], v[52:55], v[80:83], v[176:179]\n\t"
      "s_waitcnt vmcnt(39)\n\t"
      "ds_read_b128 v[240:243], %[vchk] offset:16384\n\t"
      "v_mfma_f32_16x16x32_bf16 v[180:183], v[80:83], v[80:83], v[180:183]\n\t"
      "ds_read_b128 v[244:247], %[vchk] offset:17408\n\t"
      "ds_read_b128 v[248:251], %[vchk] offset:18432\n\t"
      "v_mfma_f32_16x16x32_bf16 v[132:135], a[156:159], v[84:87], v[132:135]\n\t"
      "ds_read_b128 v[252:255], %[vchk] offset:19456\n\t"
      "v_mfma_f32_16x16x32_bf16 v[148:151], a[160:163], v[84:87], v[148:151]\n\t"
      "global_load_dwordx4 v[220:223], %[l16], s[38:39] offset:1024 sc1\n\t"
      "v_mfma_f32_16x16x32_bf16 v[164:167], a[164:167], v[84:87], v[164:167]\n\t"
      "v_mfma_f32_16x16x32_bf16 v[136:139], a[156:159], v[88:91], v[136:139]\n\t"
      "s_waitcnt lgkmcnt(0)\n\t"
      "v_max3_u32 v56, v240, v241, v242\n\t"
      "v_mfma_f32_16x16x32_bf16 v[152:155], a[160:163], v[88:91], v[152:155]\n\t"
      "v_max3_u32 v56, v56, v243, v244\n\t"
      "v_max3_u32 v56, v56, v245, v246\n\t"
      "v_mfma_f32_16x16x32_bf16 v[168:171], a[164:167], v[88:91], v[168:171]\n\t"
      "v_max3_u32 v56, v56, v247, v248\n\t"
      "v_max3_u32 v56, v56, v249, v250\n\t"
      "v_mfma_f32_16x16x32_bf16 v[140:143], a[156:159], v[92:95], v[140:143]\n\t"
      "v_max3_u32 v56, v56, v251, v252\n\t"
      "v_max3_u32 v56, v56, v253, v254\n\t"
      "v_mfma_f32_16x16x32_bf16 v[156:159], a[160:163], v[92:95], v[156:159]\n\t"
      "v_max_u32 v56, v56, v255\n\t"
      "v_cmp_eq_u32 vcc, -1, v56\n\t"
      "v_mfma_f32_16x16x32_bf16 v[172:175], a[164:167], v[92:95], v[172:175]\n\t"
      "s_cbranch_vccnz L_g1_slow9_%=\n\t"
      "L_g1_back9_%=:\n\t"
      "s_waitcnt lgkmcnt(0)\n\t"
      "s_barrier\n\t"
      "v_mfma_f32_16x16x32_bf16 v[128:131], a[168:171], v[96:99], v[128:131]\n\t"
      "ds_read_b64_tr_b16 v[64:65], v40 offset:16384\n\t"
      "v_mfma_f32_16x16x32_bf16 v[144:147], a[172:175], v[96:99], v[144:147]\n\t"
      "global_load_dwordx4 a[144:147], %[l16], s[40:41] offset:0 sc1\n\t"
      "v_mfma_f32_16x16x32_bf16 v[160:163], a[176:179], v[96:99], v[160:163]\n\t"
      "ds_read_b64_tr_b16 v[66:67], v40 offset:18432\n\t"
      "ds_read_b64_tr_b16 v[68:69], v41 offset:16384\n\t"
      "v_mfma_f32_16x16x32_bf16 v[176:179], v[52:55], v[96:99], v[176:179]\n\t"
      "global_load_dwordx4 a[148:151], %[l16], s[40:41] offset:1024 sc1\n\t"
      "v_mfma_f32_16x16x32_bf16 v[180:183], v[96:99], v[96:99], v[180:183]\n\t"
      "ds_read_b64_tr_b16 v[70:71], v41 offset:18432\n\t"
      "ds_read_b64_tr_b16 v[72:73], v42 offset:16384\n\t"
      "v_mfma_f32_16x16x32_bf16 v[132:135], a[168:171], v[100:103], v[132:135]\n\t"
      "global_load_dwordx4 a[152:155], %[l16], s[40:41] offset:2048 sc1\n\t"
      "v_mfma_f32_16x16x32_bf16 v[148:151], a[172:175], v[100:103], v[148:151]\n\t"
      "ds_read_b64_tr_b16 v[74:75], v42 offset:18432\n\t"
      "v_mfma_f32_16x16x32_bf16 v[164:167], a[176:179], v[100:103], v[164:167]\n\t"
      "global_load_dwordx4 a[156:159], %[l16], s[40:41] offset:3072 sc1\n\t"
      "v_mfma_f32_16x16x32_bf16 v[136:139], a[168:171], v[104:107], v[136:139]\n\t"
      "ds_read_b64_tr_b16 v[76:77], v43 offset:16384\n\t"
      "ds_read_b64_tr_b16 v[78:79], v43 offset:18432\n\t"
      "v_mfma_f32_16x16x32_bf16 v[152:155], a[172:175], v[104:107], v[152:155]\n\t"
      "ds_read_b64_tr_b16 v[80:81], v40 offset:20480\n\t"
      "v_mfma_f32_16x16x32_bf16 v[168:171], a[176:179], v[104:107], v[168:171]\n\t"
      "s_add_u32 s40, %[dlo], 45056\n\t"
      "s_addc_u32 s41, %[dhi], 0\n\t"
      "v_mfma_f32_16x16x32_bf16 v[140:143], a[168:171], v[108:111], v[140:143]\n\t"
      "ds_read_b64_tr_b16 v[82:83], v40 offset:22528\n\t"
      "ds_read_b64_tr_b16 v[84:85], v41 offset:20480\n\t"
      "v_mfma_f32_16x16x32_bf16 v[156:159], a[172:175], v[108:111], v[156:159]\n\t"
      "global_load_dwordx4 a[160:163], %[l16], s[40:41] offset:-4096 sc1\n\t"
      "v_mfma_f32_16x16x32_bf16 v[172:175], a[176:179], v[108:111], v[172:175]\n\t"
      "ds_read_b64_tr_b16 v[86:87], v41 offset:22528\n\t"
      "v_mfma_f32_16x16x32_bf16 v[128:131], a[180:183], v[112:115], v[128:131]\n\t"
      "global_load_dwordx4 a[164:167], %[l16], s[40:41] offset:-3072 sc1\n\t"
      "v_mfma_f32_16x16x32_bf16 v[144:147], a[184:187], v[112:115], v[144:147]\n\t"
      "ds_read_b64_tr_b16 v[88:89], v42 offset:20480\n\t"
      "ds_read_b64_tr_b16 v[90:91], v42 offset:22528\n\t"
      "v_mfma_f32_16x16x32_bf16 v[160:163], a[188:191], v[112:115], v[160:163]\n\t"
      "s_add_u32 s38, %[hlo], 12288\n\t"
      "s_addc_u32 s39, %[hhi], 0\n\t"
      "v_mfma_f32_16x16x32_bf16 v[176:179], v[52:55], v[112:115], v[176:179]\n\t"
      "ds_read_b64_tr_b16 v[92:93], v43 offset:20480\n\t"
      "ds_read_b64_tr_b16 v[94:95], v43 offset:22528\n\t"
      "v_mfma_f32_16x16x32_bf16 v[180:183], v[112:115], v[112:115], v[180:183]\n\t"
      "global_load_dwordx4 v[224:227], %[l16], s[38:39] offset:-4096 sc1\n\t"
      "v_mfma_f32_16x16x32_bf16 v[132:135], a[180:183], v[116:119], v[132:135]\n\t"
      "global_load_dwordx4 v[228:231], %[l16], s[38:39] offset:-3072 sc1\n\t"
      "v_mfma_f32_16x16x32_bf16 v[148:151], a[184:187], v[116:119], v[148:151]\n\t"
      "v_mfma_f32_16x16x32_bf16 v[164:167], a[188:191], v[116:119], v[164:167]\n\t"
      "v_mfma_f32_16x16x32_bf16 v[136:139], a[180:183], v[120:123], v[136:139]\n\t"
      "v_mfma_f32_16x16x32_bf16 v[152:155], a[184:187], v[120:123], v[152:155]\n\t"
      "v_mfma_f32_16x16x32_bf16 v[168:171], a[188:191], v[120:123], v[168:171]\n\t"
      "v_mfma_f32_16x16x32_bf16 v[140:143], a[180:183], v[124:127], v[140:143]\n\t"
      "v_mfma_f32_16x16x32_bf16 v[156:159], a[184:187], v[124:127], v[156:159]\n\t"
      "v_mfma_f32_16x16x32_bf16 v[172:175], a[188:191], v[124:127], v[172:175]\n\t"
      "s_waitcnt lgkmcnt(0)\n\t"
      "v_mfma_f32_16x16x32_bf16 v[128:131], a[192:195], v[64:67], v[128:131]\n\t"
      "ds_read_b64_tr_b16 v[96:97], v40 offset:24576\n\t"
      "v_mfma_f32_16x16x32_bf16 v[144:147], a[196:199], v[64:67], v[144:147]\n\t"
      "global_load_dwordx4 a[168:171], %[l16], s[40:41] offset:-2048 sc1\n\t"
      "v_mfma_f32_16x16x32_bf16 v[160:163], a[200:203], v[64:67], v[160:163]\n\t"
      "ds_read_b64_tr_b16 v[98:99], v40 offset:26624\n\t"
      "ds_read_b64_tr_b16 v[100:101], v41 offset:24576\n\t"
      "v_mfma_f32_16x16x32_bf16 v[176:179], v[52:55], v[64:67], v[176:179]\n\t"
      "global_load_dwordx4 a[172:175], %[l16], s[40:41] offset:-1024 sc1\n\t"
      "v_mfma_f32_16x16x32_bf16 v[180:183], v[64:67], v[64:67], v[180:183]\n\t"
      "ds_read_b64_tr_b16 v[102:103], v41 offset:26624\n\t"
      "ds_read_b64_tr_b16 v[104:105], v42 offset:24576\n\t"
      "v_mfma_f32_16x16x32_bf16 v[132:135], a[192:195], v[68:71], v[132:135]\n\t"
      "global_load_dwordx4 a[176:179], %[l16], s[40:41] offset:0 sc1\n\t"
      "v_mfma_f32_16x16x32_bf16 v[148:151], a[196:199], v[68:71], v[148:151]\n\t"
      "ds_read_b64_tr_b16 v[106:107], v42 offset:26624\n\t"
      "ds_read_b64_tr_b16 v[108:109], v43 offset:24576\n\t"
      "v_mfma_f32_16x16x32_bf16 v[164:167], a[200:203], v[68:71], v[164:167]\n\t"
      "global_load_dwordx4 a[180:183], %[l16], s[40:41] offset:1024 sc1\n\t"
      "v_mfma_f32_16x16x32_bf16 v[136:139], a[192:195], v[72:75], v[136:139]\n\t"
      "ds_read_b64_tr_b16 v[110:111], v43 offset:26624\n\t"
      "ds_read_b64_tr_b16 v[112:113], v40 offset:28672\n\t"
      "v_mfma_f32_16x16x32_bf16 v[152:155], a[196:199], v[72:75], v[152:155]\n\t"
      "global_load_dwordx4 a[184:187], %[l16], s[40:41] offset:2048 sc1\n\t"
      "v_mfma_f32_16x16x32_bf16 v[168:171], a[200:203], v[72:75], v[168:171]\n\t"
      "ds_read_b64_tr_b16 v[114:115], v40 offset:30720\n\t"
      "ds_read_b64_tr_b16 v[116:117], v41 offset:28672\n\t"
      "v_mfma_f32_16x16x32_bf16 v[140:143], a[192:195], v[76:79], v[140:143]\n\t"
      "ds_read_b64_tr_b16 v[118:119], v41 offset:30720\n\t"
      "v_mfma_f32_16x16x32_bf16 v[156:159], a[196:199], v[76:79], v[156:159]\n\t"
      "global_load_dwordx4 a[188:191], %[l16], s[40:41] offset:3072 sc1\n\t"
      "v_mfma_f32_16x16x32_bf16 v[172:175], a[200:203], v[76:79], v[172:175]\n\t"
      "ds_read_b64_tr_b16 v[120:121], v42 offset:28672\n\t"
      "ds_read_b64_tr_b16 v[122:123], v42 offset:30720\n\t"
      "v_mfma_f32_16x16x32_bf16 v[128:131], a[204:207], v[80:83], v[128:131]\n\t"
      "s_add_u32 s38, %[hlo], 16384\n\t"
      "s_addc_u32 s39, %[hhi], 0\n\t"
      "v_mfma_f32_16x16x32_bf16 v[144:147], a[208:211], v[80:83], v[144:147]\n\t"
      "ds_read_b64_tr_b16 v[124:125], v43 offset:28672\n\t"
      "ds_read_b64_tr_b16 v[126:127], v43 offset:30720\n\t"
      "v_mfma_f32_16x16x32_bf16 v[160:163], a[212:215], v[80:83], v[160:163]\n\t"
      "global_load_dwordx4 v[232:235], %[l16], s[38:39] offset:-4096 sc1\n\t"
      "v_mfma_f32_16x16x32_bf16 v[176:179], v[52:55], v[80:83], v[176:179]\n\t"
      "global_load_dwordx4 v[236:239], %[l16], s[38:39] offset:-3072 sc1\n\t"
      "v_mfma_f32_16x16x32_bf16 v[180:183], v[80:83], v[80:83], v[180:183]\n\t"
      "v_mfma_f32_16x16x32_bf16 v[132:135], a[204:207], v[84:87], v[132:135]\n\t"
      "v_mfma_f32_16x16x32_bf16 v[148:151], a[208:211], v[84:87], v[148:151]\n\t"
      "v_mfma_f32_16x16x32_bf16 v[164:167], a[212:215], v[84:87], v[164:167]\n\t"
      "v_mfma_f32_16x16x32_bf16 v[136:139], a[204:207], v[88:91], v[136:139]\n\t"
      "v_mfma_f32_16x16x32_bf16 v[152:155], a[208:211], v[88:91], v[152:155]\n\t"
      "v_mfma_f32_16x16x32_bf16 v[168:171], a[212:215], v[88:91], v[168:171]\n\t"
      "v_mfma_f32_16x16x32_bf16 v[140:143], a[204:207], v[92:95], v[140:143]\n\t"
      "v_mfma_f32_16x16x32_bf16 v[156:159], a[208:211], v[92:95], v[156:159]\n\t"
      "v_mfma_f32_16x16x32_bf16 v[172:175], a[212:215], v[92:95], v[172:175]\n\t"
      "s_waitcnt lgkmcnt(0)\n\t"
      "v_mfma_f32_16x16x32_bf16 v[128:131], a[216:219], v[96:99], v[128:131]\n\t"
      "s_add_u32 s40, %[dlo], 53248\n\t"
      "s_addc_u32 s41, %[dhi], 0\n\t"
      "v_mfma_f32_16x16x32_bf16 v[144:147], a[220:223], v[96:99], v[144:147]\n\t"
      "global_load_dwordx4 a[192:195], %[l16], s[40:41] offset:-4096 sc1\n\t"
      "v_mfma_f32_16x16x32_bf16 v[160:163], a[224:227], v[96:99], v[160:163]\n\t"
      "global_load_dwordx4 a[196:199], %[l16], s[40:41] offset:-3072 sc1\n\t"
      "v_mfma_f32_16x16x32_bf16 v[176:179], v[52:55], v[96:99], v[176:179]\n\t"
      "global_load_dwordx4 a[200:203], %[l16], s[40:41] offset:-2048 sc1\n\t"
      "v_mfma_f32_16x16x32_bf16 v[180:183], v[96:99], v[96:99], v[180:183]\n\t"
      "global_load_dwordx4 v[64:67], %[l16], s[38:39] offset:0 sc1\n\t"
      "v_mfma_f32_16x16x32_bf16 v[132:135], a[216:219], v[100:103], v[132:135]\n\t"
      "global_load_dwordx4 v[68:71], %[l16], s[38:39] offset:1024 sc1\n\t"
      "v_mfma_f32_16x16x32_bf16 v[148:151], a[220:223], v[100:103], v[148:151]\n\t"
      "s_add_u32 s38, %[hlo], 24576\n\t"
      "s_addc_u32 s39, %[hhi], 0\n\t"
      "v_mfma_f32_16x16x32_bf16 v[164:167], a[224:227], v[100:103], v[164:167]\n\t"
      "global_load_dwordx4 v[72:75], %[l16], s[38:39] offset:-4096 sc1\n\t"
      "v_mfma_f32_16x16x32_bf16 v[136:139], a[216:219], v[104:107], v[136:139]\n\t"
      "global_load_dwordx4 v[76:79], %[l16], s[38:39] offset:-3072 sc1\n\t"
      "v_mfma_f32_16x16x32_bf16 v[152:155], a[220:223], v[104:107], v[152:155]\n\t"
      "v_mfma_f32_16x16x32_bf16 v[168:171], a[224:227], v[104:107], v[168:171]\n\t"
      "v_mfma_f32_16x16x32_bf16 v[140:143], a[216:219], v[108:111], v[140:143]\n\t"
      "v_mfma_f32_16x16x32_bf16 v[156:159], a[220:223], v[108:111], v[156:159]\n\t"
      "v_mfma_f32_16x16x32_bf16 v[172:175], a[224:227], v[108:111], v[172:175]\n\t"
      "v_mfma_f32_16x16x32_bf16 v[128:131], a[228:231], v[112:115], v[128:131]\n\t"
      "v_mfma_f32_16x16x32_bf16 v[144:147], a[232:235], v[112:115], v[144:147]\n\t"
      "v_mfma_f32_16x16x32_bf16 v[160:163], a[236:239], v[112:115], v[160:163]\n\t"
      "v_mfma_f32_16x16x32_bf16 v[176:179], v[52:55], v[112:115], v[176:179]\n\t"
      "v_mfma_f32_16x16x32_bf16 v[180:183], v[112:115], v[112:115], v[180:183]\n\t"
      "v_mfma_f32_16x16x32_bf16 v[132:135], a[228:231], v[116:119], v[132:135]\n\t"
      "v_mfma_f32_16x16x32_bf16 v[148:151], a[232:235], v[116:119], v[148:151]\n\t"
      "v_mfma_f32_16x16x32_bf16 v[164:167], a[236:239], v[116:119], v[164:167]\n\t"
      "v_mfma_f32_16x16x32_bf16 v[136:139], a[228:231], v[120:123], v[136:139]\n\t"
      "v_mfma_f32_16x16x32_bf16 v[152:155], a[232:235], v[120:123], v[152:155]\n\t"
      "v_mfma_f32_16x16x32_bf16 v[168:171], a[236:239], v[120:123], v[168:171]\n\t"
      "v_mfma_f32_16x16x32_bf16 v[140:143], a[228:231], v[124:127], v[140:143]\n\t"
      "v_mfma_f32_16x16x32_bf16 v[156:159], a[232:235], v[124:127], v[156:159]\n\t"
      "v_mfma_f32_16x16x32_bf16 v[172:175], a[236:239], v[124:127], v[172:175]\n\t"
      "s_mov_b32 m0, s45\n\t"
      "s_nop 7\n\t"
      "s_branch L_g1_end%=\n\t"
      "L_g1_slow6_%=:\n\t"
      "s_add_u32 m0, %[m0b], 98304\n\t"
      "s_add_u32 s42, %[xlo], 98304\n\t"
      "s_addc_u32 s43, %[xhi], 0\n\t"
      "global_load_lds_dwordx4 %[voff], s[42:43] offset:0 sc1\n\t"
      "global_load_lds_dwordx4 %[voff], s[42:43] offset:1024 sc1\n\t"
      "global_load_lds_dwordx4 %[voff], s[42:43] offset:2048 sc1\n\t"
      "global_load_lds_dwordx4 %[voff], s[42:43] offset:3072 sc1\n\t"
      "s_waitcnt vmcnt(0)\n\t"
      "ds_read_b128 v[240:243], v57 offset:32768\n\t"
      "ds_read_b128 v[244:247], v57 offset:33792\n\t"
      "ds_read_b128 v[248:251], v57 offset:34816\n\t"
      "ds_read_b128 v[252:255], v57 offset:35840\n\t"
      "s_waitcnt lgkmcnt(0)\n\t"
      "v_max3_u32 v56, v240, v241, v242\n\t"
      "v_max3_u32 v56, v56, v243, v244\n\t"
      "v_max3_u32 v56, v56, v245, v246\n\t"
      "v_max3_u32 v56, v56, v247, v248\n\t"
      "v_max3_u32 v56, v56, v249, v250\n\t"
      "v_max3_u32 v56, v56, v251, v252\n\t"
      "v_max3_u32 v56, v56, v253, v254\n\t"
      "v_max_u32 v56, v56, v255\n\t"
      "v_cmp_eq_u32 vcc, -1, v56\n\t"
      "s_sub_u32 s44, s44, 1\n\t"
      "s_cbranch_vccz L_g1_back6_%=\n\t"
      "s_cmp_lg_u32 s44, 0\n\t"
      "s_cbranch_scc1 L_g1_slow6_%=\n\t"
      "s_branch L_g1_back6_%=\n\t"
      "L_g1_slow7_%=:\n\t"
      "s_add_u32 m0, %[m0b], 114688\n\t"
      "s_add_u32 s42, %[xlo], 114688\n\t"
      "s_addc_u32 s43, %[xhi], 0\n\t"
      "global_load_lds_dwordx4 %[voff], s[42:43] offset:0 sc1\n\t"
      "global_load_lds_dwordx4 %[voff], s[42:43] offset:1024 sc1\n\t"
      "global_load_lds_dwordx4 %[voff], s[42:43] offset:2048 sc1\n\t"
      "global_load_lds_dwordx4 %[voff], s[42:43] offset:3072 sc1\n\t"
      "s_waitcnt vmcnt(0)\n\t"
      "ds_read_b128 v[240:243], v57 offset:49152\n\t"
      "ds_read_b128 v[244:247], v57 offset:50176\n\t"
      "ds_read_b128 v[248:251], v57 offset:51200\n\t"
      "ds_read_b128 v[252:255], v57 offset:52224\n\t"
      "s_waitcnt lgkmcnt(0)\n\t"
      "v_max3_u32 v56, v240, v241, v242\n\t"
      "v_max3_u32 v56, v56, v243, v244\n\t"
      "v_max3_u32 v56, v56, v245, v246\n\t"
      "v_max3_u32 v56, v56, v247, v248\n\t"
      "v_max3_u32 v56, v56, v249, v250\n\t"
      "v_max3_u32 v56, v56, v251, v252\n\t"
      "v_max3_u32 v56, v56, v253, v254\n\t"
      "v_max_u32 v56, v56, v255\n\t"
      "v_cmp_eq_u32 vcc, -1, v56\n\t"
      "s_sub_u32 s44, s44, 1\n\t"
      "s_cbranch_vccz L_g1_back7_%=\n\t"
      "s_cmp_lg_u32 s44, 0\n\t"
      "s_cbranch_scc1 L_g1_slow7_%=\n\t"
      "s_branch L_g1_back7_%=\n\t"
      "L_g1_slow8_%=:\n\t"
      "s_add_u32 m0, %[m0b], 0\n\t"
      "s_add_u32 s42, %[xlo], 131072\n\t"
      "s_addc_u32 s43, %[xhi], 0\n\t"
      "global_load_lds_dwordx4 %[voff], s[42:43] offset:0 sc1\n\t"
      "global_load_lds_dwordx4 %[voff], s[42:43] offset:1024 sc1\n\t"
      "global_load_lds_dwordx4 %[voff], s[42:43] offset:2048 sc1\n\t"
      "global_load_lds_dwordx4 %[voff], s[42:43] offset:3072 sc1\n\t"
      "s_waitcnt vmcnt(0)\n\t"
      "ds_read_b128 v[240:243], %[vchk] offset:0\n\t"
      "ds_read_b128 v[244:247], %[vchk] offset:1024\n\t"
      "ds_read_b128 v[248:251], %[vchk] offset:2048\n\t"
      "ds_read_b128 v[252:255], %[vchk] offset:3072\n\t"
      "s_waitcnt lgkmcnt(0)\n\t"
      "v_max3_u32 v56, v240, v241, v242\n\t"
      "v_max3_u32 v56, v56, v243, v244\n\t"
      "v_max3_u32 v56, v56, v245, v246\n\t"
      "v_max3_u32 v56, v56, v247, v248\n\t"
      "v_max3_u32 v56, v56, v249, v250\n\t"
      "v_max3_u32 v56, v56, v251, v252\n\t"
      "v_max3_u32 v56, v56, v253, v254\n\t"
      "v_max_u32 v56, v56, v255\n\t"
      "v_cmp_eq_u32 vcc, -1, v56\n\t"
      "s_sub_u32 s44, s44, 1\n\t"
      "s_cbranch_vccz L_g1_back8_%=\n\t"
      "s_cmp_lg_u32 s44, 0\n\t"
      "s_cbranch_scc1 L_g1_slow8_%=\n\t"
      "s_branch L_g1_back8_%=\n\t"
      "L_g1_slow9_%=:\n\t"
      "s_add_u32 m0, %[m0b], 16384\n\t"
      "s_add_u32 s42, %[xlo], 147456\n\t"
      "s_addc_u32 s43, %[xhi], 0\n\t"
      "global_load_lds_dwordx4 %[voff], s[42:43] offset:0 sc1\n\t"
      "global_load_lds_dwordx4 %[voff], s[42:43] offset:1024 sc1\n\t"
      "global_load_lds_dwordx4 %[voff], s[42:43] offset:2048 sc1\n\t"
      "global_load_lds_dwordx4 %[voff], s[42:43] offset:3072 sc1\n\t"
      "s_waitcnt vmcnt(0)\n\t"
      "ds_read_b128 v[240:243], %[vchk] offset:16384\n\t"
      "ds_read_b128 v[244:247], %[vchk] offset:17408\n\t"
      "ds_read_b128 v[248:251], %[vchk] offset:18432\n\t"
      "ds_read_b128 v[252:255], %[vchk] offset:19456\n\t"
      "s_waitcnt lgkmcnt(0)\n\t"
      "v_max3_u32 v56, v240, v241, v242\n\t"
      "v_max3_u32 v56, v56, v243, v244\n\t"
      "v_max3_u32 v56, v56, v245, v246\n\t"
      "v_max3_u32 v56, v56, v247, v248\n\t"
      "v_max3_u32 v56, v56, v249, v250\n\t"
      "v_max3_u32 v56, v56, v251, v252\n\t"
      "v_max3_u32 v56, v56, v253, v254\n\t"
      "v_max_u32 v56, v56, v255\n\t"
      "v_cmp_eq_u32 vcc, -1, v56\n\t"
      "s_sub_u32 s44, s44, 1\n\t"
      "s_cbranch_vccz L_g1_back9_%=\n\t"
      "s_cmp_lg_u32 s44, 0\n\t"
      "s_cbranch_scc1 L_g1_slow9_%=\n\t"
      "s_branch L_g1_back9_%=\n\t"
      "L_g1_end%=:\n\t"
      : "=&{v[128:159]}"(accA), "=&{v[160:175]}"(accB), "=&{v[176:183]}"(accS), "=&{v[64:95]}"(h0), "=&{v[96:127]}"(h1), "=&{v[208:239]}"(h2), "=&{v[184:187]}"(hc)
      : G1_IN : G1_CL,  "v48", "v49", "v50", "v51", "v52", "v53", "v54", "v55", "v56", "v57", "v58", "v59", "v60", "v61", "v62", "v63", "v240", "v241", "v242", "v243", "v244", "v245", "v246", "v247", "v248", "v249", "v250", "v251", "v252", "v253", "v254", "v255", "a0", "a1", "a2", "a3", "a4", "a5", "a6", "a7", "a8", "a9", "a10", "a11", "a12", "a13", "a14", "a15", "a16", "a17", "a18", "a19", "a20", "a21", "a22", "a23", "a24", "a25", "a26", "a27", "a28", "a29", "a30", "a31", "a32", "a33", "a34", "a35", "a36", "a37", "a38", "a39", "a40", "a41", "a42", "a43", "a44", "a45", "a46", "a47", "a48", "a49", "a50", "a51", "a52", "a53", "a54", "a55", "a56", "a57", "a58", "a59", "a60", "a61", "a62", "a63", "a64", "a65", "a66", "a67", "a68", "a69", "a70", "a71", "a72", "a73", "a74", "a75", "a76", "a77", "a78", "a79", "a80", "a81", "a82", "a83", "a84", "a85", "a86", "a87", "a88", "a89", "a90", "a91", "a92", "a93", "a94", "a95", "a96", "a97", "a98", "a99", "a100", "a101", "a102", "a103", "a104", "a105", "a106", "a107", "a108", "a109", "a110", "a111", "a112", "a113", "a114", "a115", "a116", "a117", "a118", "a119", "a120", "a121", "a122", "a123", "a124", "a125", "a126", "a127", "a128", "a129", "a130", "a131", "a132", "a133", "a134", "a135", "a136", "a137", "a138", "a139", "a140", "a141", "a142", "a143", "a144", "a145", "a146", "a147", "a148", "a149", "a150", "a151", "a152", "a153", "a154", "a155", "a156", "a157", "a158", "a159", "a160", "a161", "a162", "a163", "a164", "a165", "a166", "a167", "a168", "a169", "a170", "a171", "a172", "a173", "a174", "a175", "a176", "a177", "a178", "a179", "a180", "a181", "a182", "a183", "a184", "a185", "a186", "a187", "a188", "a189", "a190", "a191", "a192", "a193", "a194", "a195", "a196", "a197", "a198", "a199", "a200", "a201", "a202", "a203", "a204", "a205", "a206", "a207", "a208", "a209", "a210", "a211", "a212", "a213", "a214", "a215", "a216", "a217", "a218", "a219", "a220", "a221", "a222", "a223", "a224", "a225", "a226", "a227", "a228", "a229", "a230", "a231", "a232", "a233", "a234", "a235", "a236", "a237", "a238", "a239");

  g1_epi(1);
  u32x4 xg;
#pragma unroll
  for (int i = 0; i < 3; ++i) {
    const int e = lane + 64 * i < 160 ? lane + 64 * i : 159;
    const int k = kpos(bn2 * 80 + (e >> 1));
    xg[i] = WS_X1T + (unsigned)(((bm2 & ~1) + (w >> 1)) * D_ + k) * 128 + (((2 * bm2 + (w & 1)) ^ (k >> 1)) & 3) * 32 + (e & 1) * 16 - i * 1024;
  }
  xg[3] = 0;
  asm volatile(
      "s_mov_b32 s44, 0x40000\n\t"
      "s_mov_b32 s45, m0\n\t"
      "s_waitcnt vmcnt(4)\n\t"
      "v_add3_u32 v46, v208, v209, %[t0]\n\t"
      "v_add3_u32 v46, v46, v210, v211\n\t"
      "v_add3_u32 v46, v46, v212, v213\n\t"
      "v_add3_u32 v46, v46, v214, v215\n\t"
      "v_add3_u32 v46, v46, v216, v217\n\t"
      "v_add3_u32 v46, v46, v218, v219\n\t"
      "v_add3_u32 v46, v46, v220, v221\n\t"
      "v_add3_u32 v46, v46, v222, v223\n\t"
      "v_add3_u32 v46, v46, v224, v225\n\t"
      "v_add3_u32 v46, v46, v226, v227\n\t"
      "v_add3_u32 v46, v46, v228, v229\n\t"
      "v_add3_u32 v46, v46, v230, v231\n\t"
      "v_cmp_ne_u32 vcc, v46, v184\n\t"
      "s_cbranch_vccnz L_g2_slow0_%=\n\t"
      "L_g2_back0_%=:\n\t"
      "v_mfma_f32_16x16x32_bf16 v[128:131], a[0:3], v[208:211], 0\n\t"
      "s_add_u32 s40, %[dlo], 53248\n\t"
      "s_addc_u32 s41, %[dhi], 0\n\t"
      "global_load_dwordx4 a[204:207], %[l16], s[40:41] offset:-1024 sc1\n\t"
      "v_mfma_f32_16x16x32_bf16 v[136:139], a[4:7], v[208:211], 0\n\t"
      "v_mfma_f32_16x16x32_bf16 v[144:147], a[8:11], v[208:211], 0\n\t"
      "v_mfma_f32_16x16x32_bf16 v[152:155], a[12:15], v[208:211], 0\n\t"
      "v_mfma_f32_16x16x32_bf16 v[160:163], a[16:19], v[208:211], 0\n\t"
      "global_load_dwordx4 a[208:211], %[l16], s[40:41] offset:0 sc1\n\t"
      "v_mfma_f32_16x16x32_bf16 v[132:135], a[0:3], v[212:215], 0\n\t"
      "v_mfma_f32_16x16x32_bf16 v[140:143], a[4:7], v[212:215], 0\n\t"
      "v_mfma_f32_16x16x32_bf16 v[148:151], a[8:11], v[212:215], 0\n\t"
      "v_mfma_f32_16x16x32_bf16 v[156:159], a[12:15], v[212:215], 0\n\t"
      "global_load_dwordx4 a[212:215], %[l16], s[40:41] offset:1024 sc1\n\t"
      "v_mfma_f32_16x16x32_bf16 v[164:167], a[16:19], v[212:215], 0\n\t"
      "v_mfma_f32_16x16x32_bf16 v[128:131], a[20:23], v[216:219], v[128:131]\n\t"
      "v_mfma_f32_16x16x32_bf16 v[136:139], a[24:27], v[216:219], v[136:139]\n\t"
      "v_mfma_f32_16x16x32_bf16 v[144:147], a[28:31], v[216:219], v[144:147]\n\t"
      "global_load_dwordx4 a[216:219], %[l16], s[40:41] offset:2048 sc1\n\t"
      "v_mfma_f32_16x16x32_bf16 v[152:155], a[32:35], v[216:219], v[152:155]\n\t"
      "v_mfma_f32_16x16x32_bf16 v[160:163], a[36:39], v[216:219], v[160:163]\n\t"
      "v_mfma_f32_16x16x32_bf16 v[132:135], a[20:23], v[220:223], v[132:135]\n\t"
      "v_mfma_f32_16x16x32_bf16 v[140:143], a[24:27], v[220:223], v[140:143]\n\t"
      "global_load_dwordx4 a[220:223], %[l16], s[40:41] offset:3072 sc1\n\t"
      "v_add3_u32 v46, v232, v233, %[t0]\n\t"
      "v_mfma_f32_16x16x32_bf16 v[148:151], a[28:31], v[220:223], v[148:151]\n\t"
      "v_add3_u32 v46, v46, v234, v235\n\t"
      "v_mfma_f32_16x16x32_bf16 v[156:159], a[32:35], v[220:223], v[156:159]\n\t"
      "v_add3_u32 v46, v46, v236, v237\n\t"
      "v_mfma_f32_16x16x32_bf16 v[164:167], a[36:39], v[220:223], v[164:167]\n\t"
      "v_add3_u32 v46, v46, v238, v239\n\t"
      "v_mfma_f32_16x16x32_bf16 v[128:131], a[40:43], v[224:227], v[128:131]\n\t"
      "s_add_u32 s40, %[dlo], 61440\n\t"
      "s_addc_u32 s41, %[dhi], 0\n\t"
      "global_load_dwordx4 a[224:227], %[l16], s[40:41] offset:-4096 sc1\n\t"
      "v_add3_u32 v46, v46, v64, v65\n\t"
      "v_mfma_f32_16x16x32_bf16 v[136:139], a[44:47], v[224:227], v[136:139]\n\t"
      "v_add3_u32 v46, v46, v66, v67\n\t"
      "v_mfma_f32_16x16x32_bf16 v[144:147], a[48:51], v[224:227], v[144:147]\n\t"
      "v_add3_u32 v46, v46, v68, v69\n\t"
      "v_mfma_f32_16x16x32_bf16 v[152:155], a[52:55], v[224:227], v[152:155]\n\t"
      "v_add3_u32 v46, v46, v70, v71\n\t"
      "v_mfma_f32_16x16x32_bf16 v[160:163], a[56:59], v[224:227], v[160:163]\n\t"
      "global_load_dwordx4 a[228:231], %[l16], s[40:41] offset:-3072 sc1\n\t"
      "v_add3_u32 v46, v46, v72, v73\n\t"
      "v_mfma_f32_16x16x32_bf16 v[132:135], a[40:43], v[228:231], v[132:135]\n\t"
      "v_add3_u32 v46, v46, v74, v75\n\t"
      "v_mfma_f32_16x16x32_bf16 v[140:143], a[44:47], v[228:231], v[140:143]\n\t"
      "v_add3_u32 v46, v46, v76, v77\n\t"
      "v_mfma_f32_16x16x32_bf16 v[148:151], a[48:51], v[228:231], v[148:151]\n\t"
      "v_add3_u32 v46, v46, v78, v79\n\t"
      "v_mfma_f32_16x16x32_bf16 v[156:159], a[52:55], v[228:231], v[156:159]\n\t"
      "global_load_dwordx4 a[232:235], %[l16], s[40:41] offset:-2048 sc1\n\t"
      "v_cmp_ne_u32 vcc, v46, v185\n\t"
      "v_mfma_f32_16x16x32_bf16 v[164:167], a[56:59], v[228:231], v[164:167]\n\t"
      "s_cbranch_vccnz L_g2_slow1_%=\n\t"
      "L_g2_back1_%=:\n\t"
      "v_mfma_f32_16x16x32_bf16 v[128:131], a[60:63], v[232:235], v[128:131]\n\t"
      "global_load_dwordx4 a[236:239], %[l16], s[40:41] offset:-1024 sc1\n\t"
      "v_mfma_f32_16x16x32_bf16 v[136:139], a[64:67], v[232:235], v[136:139]\n\t"
      "v_mfma_f32_16x16x32_bf16 v[144:147], a[68:71], v[232:235], v[144:147]\n\t"
      "v_mfma_f32_16x16x32_bf16 v[152:155], a[72:75], v[232:235], v[152:155]\n\t"
      "v_mfma_f32_16x16x32_bf16 v[160:163], a[76:79], v[232:235], v[160:163]\n\t"
      "v_mfma_f32_16x16x32_bf16 v[132:135], a[60:63], v[236:239], v[132:135]\n\t"
      "v_mfma_f32_16x16x32_bf16 v[140:143], a[64:67], v[236:239], v[140:143]\n\t"
      "v_mfma_f32_16x16x32_bf16 v[148:151], a[68:71], v[236:239], v[148:151]\n\t"
      "v_mfma_f32_16x16x32_bf16 v[156:159], a[72:75], v[236:239], v[156:159]\n\t"
      "v_mfma_f32_16x16x32_bf16 v[164:167], a[76:79], v[236:239], v[164:167]\n\t"
      "v_mfma_f32_16x16x32_bf16 v[128:131], a[80:83], v[64:67], v[128:131]\n\t"
      "v_mfma_f32_16x16x32_bf16 v[136:139], a[84:87], v[64:67], v[136:139]\n\t"
      "v_mfma_f32_16x16x32_bf16 v[144:147], a[88:91], v[64:67], v[144:147]\n\t"
      "v_mfma_f32_16x16x32_bf16 v[152:155], a[92:95], v[64:67], v[152:155]\n\t"
      "v_mfma_f32_16x16x32_bf16 v[160:163], a[96:99], v[64:67], v[160:163]\n\t"
      "v_mfma_f32_16x16x32_bf16 v[132:135], a[80:83], v[68:71], v[132:135]\n\t"
      "v_mfma_f32_16x16x32_bf16 v[140:143], a[84:87], v[68:71], v[140:143]\n\t"
      "v_add3_u32 v46, v80, v81, %[t0]\n\t"
      "v_mfma_f32_16x16x32_bf16 v[148:151], a[88:91], v[68:71], v[148:151]\n\t"
      "v_add3_u32 v46, v46, v82, v83\n\t"
      "v_mfma_f32_16x16x32_bf16 v[156:159], a[92:95], v[68:71], v[156:159]\n\t"
      "v_add3_u32 v46, v46, v84, v85\n\t"
      "v_mfma_f32_16x16x32_bf16 v[164:167], a[96:99], v[68:71], v[164:167]\n\t"
      "v_add3_u32 v46, v46, v86, v87\n\t"
      "v_mfma_f32_16x16x32_bf16 v[128:131], a[100:103], v[72:75], v[128:131]\n\t"
      "v_add3_u32 v46, v46, v88, v89\n\t"
      "v_mfma_f32_16x16x32_bf16 v[136:139], a[104:107], v[72:75], v[136:139]\n\t"
      "v_add3_u32 v46, v46, v90, v91\n\t"
      "v_mfma_f32_16x16x32_bf16 v[144:147], a[108:111], v[72:75], v[144:147]\n\t"
      "v_add3_u32 v46, v46, v92, v93\n\t"
      "v_mfma_f32_16x16x32_bf16 v[152:155], a[112:115], v[72:75], v[152:155]\n\t"
      "v_add3_u32 v46, v46, v94, v95\n\t"
      "v_mfma_f32_16x16x32_bf16 v[160:163], a[116:119], v[72:75], v[160:163]\n\t"
      "v_add3_u32 v46, v46, v96, v97\n\t"
      "v_mfma_f32_16x16x32_bf16 v[132:135], a[100:103], v[76:79], v[132:135]\n\t"
      "v_add3_u32 v46, v46, v98, v99\n\t"
      "v_mfma_f32_16x16x32_bf16 v[140:143], a[104:107], v[76:79], v[140:143]\n\t"
      "v_add3_u32 v46, v46, v100, v101\n\t"
      "v_mfma_f32_16x16x32_bf16 v[148:151], a[108:111], v[76:79], v[148:151]\n\t"
      "v_add3_u32 v46, v46, v102, v103\n\t"
      "v_mfma_f32_16x16x32_bf16 v[156:159], a[112:115], v[76:79], v[156:159]\n\t"
      "v_cmp_ne_u32 vcc, v46, v186\n\t"
      "v_mfma_f32_16x16x32_bf16 v[164:167], a[116:119], v[76:79], v[164:167]\n\t"
      "s_cbranch_vccnz L_g2_slow2_%=\n\t"
      "L_g2_back2_%=:\n\t"
      "v_mfma_f32_16x16x32_bf16 v[128:131], a[120:123], v[80:83], v[128:131]\n\t"
      "s_add_u32 s38, %[hlo], 6144\n\t"
      "s_addc_u32 s39, %[hhi], 0\n\t"
      "global_load_dwordx4 v[48:51], %[l16], s[38:39] offset:-4096 sc1\n\t"
      "v_mfma_f32_16x16x32_bf16 v[136:139], a[124:127], v[80:83], v[136:139]\n\t"
      "v_mfma_f32_16x16x32_bf16 v[144:147], a[128:131], v[80:83], v[144:147]\n\t"
      "v_mfma_f32_16x16x32_bf16 v[152:155], a[132:135], v[80:83], v[152:155]\n\t"
      "v_mfma_f32_16x16x32_bf16 v[160:163], a[136:139], v[80:83], v[160:163]\n\t"
      "global_load_dwordx4 v[52:55], %[l16], s[38:39] offset:-3072 sc1\n\t"
      "v_mfma_f32_16x16x32_bf16 v[132:135], a[120:123], v[84:87], v[132:135]\n\t"
      "v_mfma_f32_16x16x32_bf16 v[140:143], a[124:127], v[84:87], v[140:143]\n\t"
      "v_mfma_f32_16x16x32_bf16 v[148:151], a[128:131], v[84:87], v[148:151]\n\t"
      "v_mfma_f32_16x16x32_bf16 v[156:159], a[132:135], v[84:87], v[156:159]\n\t"
      "global_load_dwordx4 v[56:59], %[l16], s[38:39] offset:0 sc1\n\t"
      "v_mfma_f32_16x16x32_bf16 v[164:167], a[136:139], v[84:87], v[164:167]\n\t"
      "v_mfma_f32_16x16x32_bf16 v[128:131], a[140:143], v[88:91], v[128:131]\n\t"
      "v_mfma_f32_16x16x32_bf16 v[136:139], a[144:147], v[88:91], v[136:139]\n\t"
      "v_mfma_f32_16x16x32_bf16 v[144:147], a[148:151], v[88:91], v[144:147]\n\t"
      "global_load_dwordx4 v[60:63], %[l16], s[38:39] offset:1024 sc1\n\t"
      "v_mfma_f32_16x16x32_bf16 v[152:155], a[152:155], v[88:91], v[152:155]\n\t"
      "v_mfma_f32_16x16x32_bf16 v[160:163], a[156:159], v[88:91], v[160:163]\n\t"
      "v_mfma_f32_16x16x32_bf16 v[132:135], a[140:143], v[92:95], v[132:135]\n\t"
      "s_waitcnt vmcnt(4)\n\t"
      "v_mfma_f32_16x16x32_bf16 v[140:143], a[144:147], v[92:95], v[140:143]\n\t"
      "s_add_u32 s38, %[hlo], 14336\n\t"
      "s_addc_u32 s39, %[hhi], 0\n\t"
      "global_load_dwordx4 v[240:243], %[l16], s[38:39] offset:-4096 sc1\n\t"
      "v_add3_u32 v46, v104, v105, %[t0]\n\t"
      "v_mfma_f32_16x16x32_bf16 v[148:151], a[148:151], v[92:95], v[148:151]\n\t"
      "v_add3_u32 v46, v46, v106, v107\n\t"
      "v_mfma_f32_16x16x32_bf16 v[156:159], a[152:155], v[92:95], v[156:159]\n\t"
      "v_add3_u32 v46, v46, v108, v109\n\t"
      "v_mfma_f32_16x16x32_bf16 v[164:167], a[156:159], v[92:95], v[164:167]\n\t"
      "v_add3_u32 v46, v46, v110, v111\n\t"
      "v_mfma_f32_16x16x32_bf16 v[128:131], a[160:163], v[96:99], v[128:131]\n\t"
      "global_load_dwordx4 v[244:247], %[l16], s[38:39] offset:-3072 sc1\n\t"
      "v_add3_u32 v46, v46, v112, v113\n\t"
      "v_mfma_f32_16x16x32_bf16 v[136:139], a[164:167], v[96:99], v[136:139]\n\t"
      "v_add3_u32 v46, v46, v114, v115\n\t"
      "v_mfma_f32_16x16x32_bf16 v[144:147], a[168:171], v[96:99], v[144:147]\n\t"
      "v_add3_u32 v46, v46, v116, v117\n\t"
      "v_mfma_f32_16x16x32_bf16 v[152:155], a[172:175], v[96:99], v[152:155]\n\t"
      "v_add3_u32 v46, v46, v118, v119\n\t"
      "v_mfma_f32_16x16x32_bf16 v[160:163], a[176:179], v[96:99], v[160:163]\n\t"
      "global_load_dword v32, %[cko], %[wsb] offset:256 sc1\n\t"
      "v_add3_u32 v46, v46, v120, v121\n\t"
      "v_mfma_f32_16x16x32_bf16 v[132:135], a[160:163], v[100:103], v[132:135]\n\t"
      "v_add3_u32 v46, v46, v122, v123\n\t"
      "v_mfma_f32_16x16x32_bf16 v[140:143], a[164:167], v[100:103], v[140:143]\n\t"
      "v_add3_u32 v46, v46, v124, v125\n\t"
      "v_mfma_f32_16x16x32_bf16 v[148:151], a[168:171], v[100:103], v[148:151]\n\t"
      "v_add3_u32 v46, v46, v126, v127\n\t"
      "v_mfma_f32_16x16x32_bf16 v[156:159], a[172:175], v[100:103], v[156:159]\n\t"
      "s_add_u32 s38, %[hlo], 18432\n\t"
      "s_addc_u32 s39, %[hhi], 0\n\t"
      "global_load_dwordx4 v[248:251], %[l16], s[38:39] offset:-4096 sc1\n\t"
      "v_cmp_ne_u32 vcc, v46, v187\n\t"
      "v_mfma_f32_16x16x32_bf16 v[164:167], a[176:179], v[100:103], v[164:167]\n\t"
      "s_cbranch_vccnz L_g2_slow3_%=\n\t"
      "L_g2_back3_%=:\n\t"
      "v_mfma_f32_16x16x32_bf16 v[128:131], a[180:183], v[104:107], v[128:131]\n\t"
      "global_load_dwordx4 v[252:255], %[l16], s[38:39] offset:-3072 sc1\n\t"
      "v_mfma_f32_16x16x32_bf16 v[136:139], a[184:187], v[104:107], v[136:139]\n\t"
      "v_mfma_f32_16x16x32_bf16 v[144:147], a[188:191], v[104:107], v[144:147]\n\t"
      "v_mfma_f32_16x16x32_bf16 v[152:155], a[192:195], v[104:107], v[152:155]\n\t"
      "v_mfma_f32_16x16x32_bf16 v[160:163], a[196:199], v[104:107], v[160:163]\n\t"
      "global_load_dwordx4 v[208:211], %[l16], s[38:39] offset:0 sc1\n\t"
      "v_mfma_f32_16x16x32_bf16 v[132:135], a[180:183], v[108:111], v[132:135]\n\t"
      "v_mfma_f32_16x16x32_bf16 v[140:143], a[184:187], v[108:111], v[140:143]\n\t"
      "v_mfma_f32_16x16x32_bf16 v[148:151], a[188:191], v[108:111], v[148:151]\n\t"
      "v_mfma_f32_16x16x32_bf16 v[156:159], a[192:195], v[108:111], v[156:159]\n\t"
      "global_load_dwordx4 v[212:215], %[l16], s[38:39] offset:1024 sc1\n\t"
      "v_mfma_f32_16x16x32_bf16 v[164:167], a[196:199], v[108:111], v[164:167]\n\t"
      "v_mfma_f32_16x16x32_bf16 v[128:131], a[200:203], v[112:115], v[128:131]\n\t"
      "v_mfma_f32_16x16x32_bf16 v[136:139], a[204:207], v[112:115], v[136:139]\n\t"
      "v_mfma_f32_16x16x32_bf16 v[144:147], a[208:211], v[112:115], v[144:147]\n\t"
      "s_add_u32 s38, %[hlo], 26624\n\t"
      "s_addc_u32 s39, %[hhi], 0\n\t"
      "global_load_dwordx4 v[216:219], %[l16], s[38:39] offset:-4096 sc1\n\t"
      "v_mfma_f32_16x16x32_bf16 v[152:155], a[212:215], v[112:115], v[152:155]\n\t"
      "v_mfma_f32_16x16x32_bf16 v[160:163], a[216:219], v[112:115], v[160:163]\n\t"
      "v_mfma_f32_16x16x32_bf16 v[132:135], a[200:203], v[116:119], v[132:135]\n\t"
      "s_waitcnt vmcnt(5)\n\t"
      "v_mfma_f32_16x16x32_bf16 v[140:143], a[204:207], v[116:119], v[140:143]\n\t"
      "global_load_dwordx4 v[220:223], %[l16], s[38:39] offset:-3072 sc1\n\t"
      "v_add3_u32 v46, v48, v49, %[t0]\n\t"
      "v_mfma_f32_16x16x32_bf16 v[148:151], a[208:211], v[116:119], v[148:151]\n\t"
      "v_add3_u32 v46, v46, v50, v51\n\t"
      "v_mfma_f32_16x16x32_bf16 v[156:159], a[212:215], v[116:119], v[156:159]\n\t"
      "v_add3_u32 v46, v46, v52, v53\n\t"
      "v_mfma_f32_16x16x32_bf16 v[164:167], a[216:219], v[116:119], v[164:167]\n\t"
      "v_add3_u32 v46, v46, v54, v55\n\t"
      "v_mfma_f32_16x16x32_bf16 v[128:131], a[220:223], v[120:123], v[128:131]\n\t"
      "global_load_dword v33, %[cko], %[wsb] offset:768 sc1\n\t"
      "v_add3_u32 v46, v46, v56, v57\n\t"
      "v_mfma_f32_16x16x32_bf16 v[136:139], a[224:227], v[120:123], v[136:139]\n\t"
      "v_add3_u32 v46, v46, v58, v59\n\t"
      "v_mfma_f32_16x16x32_bf16 v[144:147], a[228:231], v[120:123], v[144:147]\n\t"
      "v_add3_u32 v46, v46, v60, v61\n\t"
      "v_mfma_f32_16x16x32_bf16 v[152:155], a[232:235], v[120:123], v[152:155]\n\t"
      "v_add3_u32 v46, v46, v62, v63\n\t"
      "v_mfma_f32_16x16x32_bf16 v[160:163], a[236:239], v[120:123], v[160:163]\n\t"
      "s_add_u32 s38, %[hlo], 30720\n\t"
      "s_addc_u32 s39, %[hhi], 0\n\t"
      "global_load_dwordx4 v[224:227], %[l16], s[38:39] offset:-4096 sc1\n\t"
      "v_add3_u32 v46, v46, v240, v241\n\t"
      "v_mfma_f32_16x16x32_bf16 v[132:135], a[220:223], v[124:127], v[132:135]\n\t"
      "v_add3_u32 v46, v46, v242, v243\n\t"
      "v_mfma_f32_16x16x32_bf16 v[140:143], a[224:227], v[124:127], v[140:143]\n\t"
      "v_add3_u32 v46, v46, v244, v245\n\t"
      "v_mfma_f32_16x16x32_bf16 v[148:151], a[228:231], v[124:127], v[148:151]\n\t"
      "v_add3_u32 v46, v46, v246, v247\n\t"
      "v_mfma_f32_16x16x32_bf16 v[156:159], a[232:235], v[124:127], v[156:159]\n\t"
      "global_load_dwordx4 v[228:231], %[l16], s[38:39] offset:-3072 sc1\n\t"
      "v_cmp_ne_u32 vcc, v46, v32\n\t"
      "v_mfma_f32_16x16x32_bf16 v[164:167], a[236:239], v[124:127], v[164:167]\n\t"
      "s_cbranch_vccnz L_g2_slow4_%=\n\t"
      "L_g2_back4_%=:\n\t"
      "v_mfma_f32_16x16x32_bf16 v[168:171], a[0:3], v[48:51], 0\n\t"
      "global_load_dwordx4 v[232:235], %[l16], s[38:39] offset:0 sc1\n\t"
      "v_mfma_f32_16x16x32_bf16 v[176:179], a[4:7], v[48:51], 0\n\t"
      "v_mfma_f32_16x16x32_bf16 v[184:187], a[8:11], v[48:51], 0\n\t"
      "v_mfma_f32_16x16x32_bf16 v[192:195], a[12:15], v[48:51], 0\n\t"
      "v_mfma_f32_16x16x32_bf16 v[200:203], a[16:19], v[48:51], 0\n\t"
      "global_load_dwordx4 v[236:239], %[l16], s[38:39] offset:1024 sc1\n\t"
      "v_mfma_f32_16x16x32_bf16 v[172:175], a[0:3], v[52:55], 0\n\t"
      "ds_write_b128 %[exw], v[128:131] offset:0\n\t"
      "v_mfma_f32_16x16x32_bf16 v[180:183], a[4:7], v[52:55], 0\n\t"
      "v_mfma_f32_16x16x32_bf16 v[188:191], a[8:11], v[52:55], 0\n\t"
      "v_mfma_f32_16x16x32_bf16 v[196:199], a[12:15], v[52:55], 0\n\t"
      "s_add_u32 s38, %[hlo], 38912\n\t"
      "s_addc_u32 s39, %[hhi], 0\n\t"
      "global_load_dwordx4 v[64:67], %[l16], s[38:39] offset:-4096 sc1\n\t"
      "ds_write_b128 %[exw], v[132:135] offset:20480\n\t"
      "v_mfma_f32_16x16x32_bf16 v[204:207], a[16:19], v[52:55], 0\n\t"
      "v_mfma_f32_16x16x32_bf16 v[168:171], a[20:23], v[56:59], v[168:171]\n\t"
      "v_mfma_f32_16x16x32_bf16 v[176:179], a[24:27], v[56:59], v[176:179]\n\t"
      "ds_write_b128 %[exw], v[136:139] offset:1024\n\t"
      "v_mfma_f32_16x16x32_bf16 v[184:187], a[28:31], v[56:59], v[184:187]\n\t"
      "global_load_dwordx4 v[68:71], %[l16], s[38:39] offset:-3072 sc1\n\t"
      "v_mfma_f32_16x16x32_bf16 v[192:195], a[32:35], v[56:59], v[192:195]\n\t"
      "v_mfma_f32_16x16x32_bf16 v[200:203], a[36:39], v[56:59], v[200:203]\n\t"
      "ds_write_b128 %[exw], v[140:143] offset:21504\n\t"
      "v_mfma_f32_16x16x32_bf16 v[172:175], a[20:23], v[60:63], v[172:175]\n\t"
      "s_waitcnt vmcnt(6)\n\t"
      "v_mfma_f32_16x16x32_bf16 v[180:183], a[24:27], v[60:63], v[180:183]\n\t"
      "global_load_dword v34, %[cko], %[wsb] offset:1280 sc1\n\t"
      "v_add3_u32 v46, v248, v249, %[t0]\n\t"
      "v_mfma_f32_16x16x32_bf16 v[188:191], a[28:31], v[60:63], v[188:191]\n\t"
      "v_add3_u32 v46, v46, v250, v251\n\t"
      "v_mfma_f32_16x16x32_bf16 v[196:199], a[32:35], v[60:63], v[196:199]\n\t"
      "v_add3_u32 v46, v46, v252, v253\n\t"
      "v_mfma_f32_16x16x32_bf16 v[204:207], a[36:39], v[60:63], v[204:207]\n\t"
      "v_add3_u32 v46, v46, v254, v255\n\t"
      "v_mfma_f32_16x16x32_bf16 v[168:171], a[40:43], v[240:243], v[168:171]\n\t"
      "s_add_u32 s38, %[hlo], 43008\n\t"
      "s_addc_u32 s39, %[hhi], 0\n\t"
      "global_load_dwordx4 v[72:75], %[l16], s[38:39] offset:-4096 sc1\n\t"
      "v_add3_u32 v46, v46, v208, v209\n\t"
      "v_mfma_f32_16x16x32_bf16 v[176:179], a[44:47], v[240:243], v[176:179]\n\t"
      "v_add3_u32 v46, v46, v210, v211\n\t"
      "v_mfma_f32_16x16x32_bf16 v[184:187], a[48:51], v[240:243], v[184:187]\n\t"
      "v_add3_u32 v46, v46, v212, v213\n\t"
      "v_mfma_f32_16x16x32_bf16 v[192:195], a[52:55], v[240:243], v[192:195]\n\t"
      "v_add3_u32 v46, v46, v214, v215\n\t"
      "v_mfma_f32_16x16x32_bf16 v[200:203], a[56:59], v[240:243], v[200:203]\n\t"
      "global_load_dwordx4 v[76:79], %[l16], s[38:39] offset:-3072 sc1\n\t"
      "v_add3_u32 v46, v46, v216, v217\n\t"
      "v_mfma_f32_16x16x32_bf16 v[172:175], a[40:43], v[244:247], v[172:175]\n\t"
      "v_add3_u32 v46, v46, v218, v219\n\t"
      "v_mfma_f32_16x16x32_bf16 v[180:183], a[44:47], v[244:247], v[180:183]\n\t"
      "v_add3_u32 v46, v46, v220, v221\n\t"
      "v_mfma_f32_16x16x32_bf16 v[188:191], a[48:51], v[244:247], v[188:191]\n\t"
      "v_add3_u32 v46, v46, v222, v223\n\t"
      "v_mfma_f32_16x16x32_bf16 v[196:199], a[52:55], v[244:247], v[196:199]\n\t"
      "global_load_dwordx4 v[80:83], %[l16], s[38:39] offset:0 sc1\n\t"
      "v_cmp_ne_u32 vcc, v46, v33\n\t"
      "v_mfma_f32_16x16x32_bf16 v[204:207], a[56:59], v[244:247], v[204:207]\n\t"
      "s_cbranch_vccnz L_g2_slow5_%=\n\t"
      "L_g2_back5_%=:\n\t"
      "v_mfma_f32_16x16x32_bf16 v[168:171], a[60:63], v[248:251], v[168:171]\n\t"
      "global_load_dwordx4 v[84:87], %[l16], s[38:39] offset:1024 sc1\n\t"
      "v_mfma_f32_16x16x32_bf16 v[176:179], a[64:67], v[248:251], v[176:179]\n\t"
      "v_mfma_f32_16x16x32_bf16 v[184:187], a[68:71], v[248:251], v[184:187]\n\t"
      "v_mfma_f32_16x16x32_bf16 v[192:195], a[72:75], v[248:251], v[192:195]\n\t"
      "v_mfma_f32_16x16x32_bf16 v[200:203], a[76:79], v[248:251], v[200:203]\n\t"
      "s_add_u32 s38, %[hlo], 51200\n\t"
      "s_addc_u32 s39, %[hhi], 0\n\t"
      "global_load_dwordx4 v[88:91], %[l16], s[38:39] offset:-4096 sc1\n\t"
      "v_mfma_f32_16x16x32_bf16 v[172:175], a[60:63], v[252:255], v[172:175]\n\t"
      "ds_write_b128 %[exw], v[144:147] offset:2048\n\t"
      "v_mfma_f32_16x16x32_bf16 v[180:183], a[64:67], v[252:255], v[180:183]\n\t"
      "v_mfma_f32_16x16x32_bf16 v[188:191], a[68:71], v[252:255], v[188:191]\n\t"
      "v_mfma_f32_16x16x32_bf16 v[196:199], a[72:75], v[252:255], v[196:199]\n\t"
      "global_load_dwordx4 v[92:95], %[l16], s[38:39] offset:-3072 sc1\n\t"
      "ds_write_b128 %[exw], v[148:151] offset:22528\n\t"
      "v_mfma_f32_16x16x32_bf16 v[204:207], a[76:79], v[252:255], v[204:207]\n\t"
      "v_mfma_f32_16x16x32_bf16 v[168:171], a[80:83], v[208:211], v[168:171]\n\t"
      "v_mfma_f32_16x16x32_bf16 v[176:179], a[84:87], v[208:211], v[176:179]\n\t"
      "ds_write_b128 %[exw], v[152:155] offset:3072\n\t"
      "v_mfma_f32_16x16x32_bf16 v[184:187], a[88:91], v[208:211], v[184:187]\n\t"
      "global_load_dword v35, %[cko], %[wsb] offset:1792 sc1\n\t"
      "v_mfma_f32_16x16x32_bf16 v[192:195], a[92:95], v[208:211], v[192:195]\n\t"
      "v_mfma_f32_16x16x32_bf16 v[200:203], a[96:99], v[208:211], v[200:203]\n\t"
      "ds_write_b128 %[exw], v[156:159] offset:23552\n\t"
      "v_mfma_f32_16x16x32_bf16 v[172:175], a[80:83], v[212:215], v[172:175]\n\t"
      "s_waitcnt vmcnt(7)\n\t"
      "v_mfma_f32_16x16x32_bf16 v[180:183], a[84:87], v[212:215], v[180:183]\n\t"
      "s_mov_b32 m0, %[xrm]\n\t"
      "s_nop 0\n\t"
      "global_load_lds_dwordx4 v40, %[wsb] sc1\n\t"
      "v_add3_u32 v46, v224, v225, %[t0]\n\t"
      "v_mfma_f32_16x16x32_bf16 v[188:191], a[88:91], v[212:215], v[188:191]\n\t"
      "v_add3_u32 v46, v46, v226, v227\n\t"
      "v_mfma_f32_16x16x32_bf16 v[196:199], a[92:95], v[212:215], v[196:199]\n\t"
      "v_add3_u32 v46, v46, v228, v229\n\t"
      "v_mfma_f32_16x16x32_bf16 v[204:207], a[96:99], v[212:215], v[204:207]\n\t"
      "v_add3_u32 v46, v46, v230, v231\n\t"
      "v_mfma_f32_16x16x32_bf16 v[168:171], a[100:103], v[216:219], v[168:171]\n\t"
      "v_add3_u32 v46, v46, v232, v233\n\t"
      "v_mfma_f32_16x16x32_bf16 v[176:179], a[104:107], v[216:219], v[176:179]\n\t"
      "v_add3_u32 v46, v46, v234, v235\n\t"
      "v_mfma_f32_16x16x32_bf16 v[184:187], a[108:111], v[216:219], v[184:187]\n\t"
      "v_add3_u32 v46, v46, v236, v237\n\t"
      "v_mfma_f32_16x16x32_bf16 v[192:195], a[112:115], v[216:219], v[192:195]\n\t"
      "v_add3_u32 v46, v46, v238, v239\n\t"
      "v_mfma_f32_16x16x32_bf16 v[200:203], a[116:119], v[216:219], v[200:203]\n\t"
      "v_add3_u32 v46, v46, v64, v65\n\t"
      "v_mfma_f32_16x16x32_bf16 v[172:175], a[100:103], v[220:223], v[172:175]\n\t"
      "v_add3_u32 v46, v46, v66, v67\n\t"
      "v_mfma_f32_16x16x32_bf16 v[180:183], a[104:107], v[220:223], v[180:183]\n\t"
      "v_add3_u32 v46, v46, v68, v69\n\t"
      "v_mfma_f32_16x16x32_bf16 v[188:191], a[108:111], v[220:223], v[188:191]\n\t"
      "v_add3_u32 v46, v46, v70, v71\n\t"
      "v_mfma_f32_16x16x32_bf16 v[196:199], a[112:115], v[220:223], v[196:199]\n\t"
      "v_cmp_ne_u32 vcc, v46, v34\n\t"
      "v_mfma_f32_16x16x32_bf16 v[204:207], a[116:119], v[220:223], v[204:207]\n\t"
      "s_cbranch_vccnz L_g2_slow6_%=\n\t"
      "L_g2_back6_%=:\n\t"
      "v_mfma_f32_16x16x32_bf16 v[168:171], a[120:123], v[224:227], v[168:171]\n\t"
      "v_mfma_f32_16x16x32_bf16 v[176:179], a[124:127], v[224:227], v[176:179]\n\t"
      "v_mfma_f32_16x16x32_bf16 v[184:187], a[128:131], v[224:227], v[184:187]\n\t"
      "v_mfma_f32_16x16x32_bf16 v[192:195], a[132:135], v[224:227], v[192:195]\n\t"
      "v_mfma_f32_16x16x32_bf16 v[200:203], a[136:139], v[224:227], v[200:203]\n\t"
      "global_load_lds_dwordx4 v41, %[wsb] offset:1024 sc1\n\t"
      "v_mfma_f32_16x16x32_bf16 v[172:175], a[120:123], v[228:231], v[172:175]\n\t"
      "ds_write_b128 %[exw], v[160:163] offset:4096\n\t"
      "v_mfma_f32_16x16x32_bf16 v[180:183], a[124:127], v[228:231], v[180:183]\n\t"
      "v_mfma_f32_16x16x32_bf16 v[188:191], a[128:131], v[228:231], v[188:191]\n\t"
      "v_mfma_f32_16x16x32_bf16 v[196:199], a[132:135], v[228:231], v[196:199]\n\t"
      "ds_write_b128 %[exw], v[164:167] offset:24576\n\t"
      "v_mfma_f32_16x16x32_bf16 v[204:207], a[136:139], v[228:231], v[204:207]\n\t"
      "v_mfma_f32_16x16x32_bf16 v[168:171], a[140:143], v[232:235], v[168:171]\n\t"
      "v_mfma_f32_16x16x32_bf16 v[176:179], a[144:147], v[232:235], v[176:179]\n\t"
      "v_mfma_f32_16x16x32_bf16 v[184:187], a[148:151], v[232:235], v[184:187]\n\t"
      "v_mfma_f32_16x16x32_bf16 v[192:195], a[152:155], v[232:235], v[192:195]\n\t"
      "v_mfma_f32_16x16x32_bf16 v[200:203], a[156:159], v[232:235], v[200:203]\n\t"
      "v_mfma_f32_16x16x32_bf16 v[172:175], a[140:143], v[236:239], v[172:175]\n\t"
      "s_waitcnt vmcnt(2)\n\t"
      "v_mfma_f32_16x16x32_bf16 v[180:183], a[144:147], v[236:239], v[180:183]\n\t"
      "v_add3_u32 v46, v72, v73, %[t0]\n\t"
      "v_mfma_f32_16x16x32_bf16 v[188:191], a[148:151], v[236:239], v[188:191]\n\t"
      "v_add3_u32 v46, v46, v74, v75\n\t"
      "v_mfma_f32_16x16x32_bf16 v[196:199], a[152:155], v[236:239], v[196:199]\n\t"
      "v_add3_u32 v46, v46, v76, v77\n\t"
      "v_mfma_f32_16x16x32_bf16 v[204:207], a[156:159], v[236:239], v[204:207]\n\t"
      "v_add3_u32 v46, v46, v78, v79\n\t"
      "v_mfma_f32_16x16x32_bf16 v[168:171], a[160:163], v[64:67], v[168:171]\n\t"
      "global_load_lds_dwordx4 v42, %[wsb] offset:2048 sc1\n\t"
      "v_add3_u32 v46, v46, v80, v81\n\t"
      "v_mfma_f32_16x16x32_bf16 v[176:179], a[164:167], v[64:67], v[176:179]\n\t"
      "v_add3_u32 v46, v46, v82, v83\n\t"
      "v_mfma_f32_16x16x32_bf16 v[184:187], a[168:171], v[64:67], v[184:187]\n\t"
      "v_add3_u32 v46, v46, v84, v85\n\t"
      "v_mfma_f32_16x16x32_bf16 v[192:195], a[172:175], v[64:67], v[192:195]\n\t"
      "v_add3_u32 v46, v46, v86, v87\n\t"
      "v_mfma_f32_16x16x32_bf16 v[200:203], a[176:179], v[64:67], v[200:203]\n\t"
      "v_add3_u32 v46, v46, v88, v89\n\t"
      "v_mfma_f32_16x16x32_bf16 v[172:175], a[160:163], v[68:71], v[172:175]\n\t"
      "v_add3_u32 v46, v46, v90, v91\n\t"
      "v_mfma_f32_16x16x32_bf16 v[180:183], a[164:167], v[68:71], v[180:183]\n\t"
      "v_add3_u32 v46, v46, v92, v93\n\t"
      "v_mfma_f32_16x16x32_bf16 v[188:191], a[168:171], v[68:71], v[188:191]\n\t"
      "v_add3_u32 v46, v46, v94, v95\n\t"
      "v_mfma_f32_16x16x32_bf16 v[196:199], a[172:175], v[68:71], v[196:199]\n\t"
      "v_cmp_ne_u32 vcc, v46, v35\n\t"
      "v_mfma_f32_16x16x32_bf16 v[204:207], a[176:179], v[68:71], v[204:207]\n\t"
      "s_cbranch_vccnz L_g2_slow7_%=\n\t"
      "L_g2_back7_%=:\n\t"
      "v_mfma_f32_16x16x32_bf16 v[168:171], a[180:183], v[72:75], v[168:171]\n\t"
      "v_add_u32 v46, 0xa000, %[exw]\n\t"
      "v_mfma_f32_16x16x32_bf16 v[168:171], a[200:203], v[80:83], v[168:171]\n\t"
      "v_mfma_f32_16x16x32_bf16 v[168:171], a[220:223], v[88:91], v[168:171]\n\t"
      "v_mfma_f32_16x16x32_bf16 v[172:175], a[180:183], v[76:79], v[172:175]\n\t"
      "v_mfma_f32_16x16x32_bf16 v[172:175], a[200:203], v[84:87], v[172:175]\n\t"
      "v_mfma_f32_16x16x32_bf16 v[172:175], a[220:223], v[92:95], v[172:175]\n\t"
      "v_mfma_f32_16x16x32_bf16 v[176:179], a[184:187], v[72:75], v[176:179]\n\t"
      "ds_write_b128 v46, v[168:171] offset:0\n\t"
      "v_mfma_f32_16x16x32_bf16 v[176:179], a[204:207], v[80:83], v[176:179]\n\t"
      "v_mfma_f32_16x16x32_bf16 v[176:179], a[224:227], v[88:91], v[176:179]\n\t"
      "v_mfma_f32_16x16x32_bf16 v[180:183], a[184:187], v[76:79], v[180:183]\n\t"
      "ds_write_b128 v46, v[172:175] offset:20480\n\t"
      "v_mfma_f32_16x16x32_bf16 v[180:183], a[204:207], v[84:87], v[180:183]\n\t"
      "v_mfma_f32_16x16x32_bf16 v[180:183], a[224:227], v[92:95], v[180:183]\n\t"
      "v_mfma_f32_16x16x32_bf16 v[184:187], a[188:191], v[72:75], v[184:187]\n\t"
      "ds_write_b128 v46, v[176:179] offset:1024\n\t"
      "v_mfma_f32_16x16x32_bf16 v[184:187], a[208:211], v[80:83], v[184:187]\n\t"
      "v_mfma_f32_16x16x32_bf16 v[184:187], a[228:231], v[88:91], v[184:187]\n\t"
      "v_mfma_f32_16x16x32_bf16 v[188:191], a[188:191], v[76:79], v[188:191]\n\t"
      "ds_write_b128 v46, v[180:183] offset:21504\n\t"
      "v_mfma_f32_16x16x32_bf16 v[188:191], a[208:211], v[84:87], v[188:191]\n\t"
      "v_mfma_f32_16x16x32_bf16 v[188:191], a[228:231], v[92:95], v[188:191]\n\t"
      "v_mfma_f32_16x16x32_bf16 v[192:195], a[192:195], v[72:75], v[192:195]\n\t"
      "ds_write_b128 v46, v[184:187] offset:2048\n\t"
      "v_mfma_f32_16x16x32_bf16 v[192:195], a[212:215], v[80:83], v[192:195]\n\t"
      "v_mfma_f32_16x16x32_bf16 v[192:195], a[232:235], v[88:91], v[192:195]\n\t"
      "v_mfma_f32_16x16x32_bf16 v[196:199], a[192:195], v[76:79], v[196:199]\n\t"
      "ds_write_b128 v46, v[188:191] offset:22528\n\t"
      "v_mfma_f32_16x16x32_bf16 v[196:199], a[212:215], v[84:87], v[196:199]\n\t"
      "v_mfma_f32_16x16x32_bf16 v[196:199], a[232:235], v[92:95], v[196:199]\n\t"
      "v_mfma_f32_16x16x32_bf16 v[200:203], a[196:199], v[72:75], v[200:203]\n\t"
      "ds_write_b128 v46, v[192:195] offset:3072\n\t"
      "v_mfma_f32_16x16x32_bf16 v[200:203], a[216:219], v[80:83], v[200:203]\n\t"
      "v_mfma_f32_16x16x32_bf16 v[200:203], a[236:239], v[88:91], v[200:203]\n\t"
      "v_mfma_f32_16x16x32_bf16 v[204:207], a[196:199], v[76:79], v[204:207]\n\t"
      "ds_write_b128 v46, v[196:199] offset:23552\n\t"
      "v_mfma_f32_16x16x32_bf16 v[204:207], a[216:219], v[84:87], v[204:207]\n\t"
      "v_mfma_f32_16x16x32_bf16 v[204:207], a[236:239], v[92:95], v[204:207]\n\t"
      "ds_write_b128 v46, v[200:203] offset:4096\n\t"
      "s_mov_b32 m0, s45\n\t"
      "s_waitcnt vmcnt(0)\n\t"
      "s_nop 5\n\t"
      "ds_write_b128 v46, v[204:207] offset:24576\n\t"
      "s_branch L_g2_end%=\n\t"
      "L_g2_slow0_%=:\n\t"
      "s_mov_b64 s[42:43], s[38:39]\n\t"
      "s_add_u32 s38, %[hlo], 4096\n\t"
      "s_addc_u32 s39, %[hhi], 0\n\t"
      "global_load_dwordx4 v[208:211], %[l16], s[38:39] offset:-4096 sc1\n\t"
      "global_load_dwordx4 v[212:215], %[l16], s[38:39] offset:-3072 sc1\n\t"
      "global_load_dwordx4 v[216:219], %[l16], s[38:39] offset:0 sc1\n\t"
      "global_load_dwordx4 v[220:223], %[l16], s[38:39] offset:1024 sc1\n\t"
      "s_add_u32 s38, %[hlo], 12288\n\t"
      "s_addc_u32 s39, %[hhi], 0\n\t"
      "global_load_dwordx4 v[224:227], %[l16], s[38:39] offset:-4096 sc1\n\t"
      "global_load_dwordx4 v[228:231], %[l16], s[38:39] offset:-3072 sc1\n\t"
      "global_load_dword v184, %[cko], %[wsb] offset:0 sc1\n\t"
      "s_mov_b64 s[38:39], s[42:43]\n\t"
      "s_waitcnt vmcnt(0)\n\t"
      "v_add3_u32 v46, v208, v209, %[t0]\n\t"
      "v_add3_u32 v46, v46, v210, v211\n\t"
      "v_add3_u32 v46, v46, v212, v213\n\t"
      "v_add3_u32 v46, v46, v214, v215\n\t"
      "v_add3_u32 v46, v46, v216, v217\n\t"
      "v_add3_u32 v46, v46, v218, v219\n\t"
      "v_add3_u32 v46, v46, v220, v221\n\t"
      "v_add3_u32 v46, v46, v222, v223\n\t"
      "v_add3_u32 v46, v46, v224, v225\n\t"
      "v_add3_u32 v46, v46, v226, v227\n\t"
      "v_add3_u32 v46, v46, v228, v229\n\t"
      "v_add3_u32 v46, v46, v230, v231\n\t"
      "v_cmp_ne_u32 vcc, v46, v184\n\t"
      "s_sub_u32 s44, s44, 1\n\t"
      "s_cbranch_vccz L_g2_back0_%=\n\t"
      "s_cmp_lg_u32 s44, 0\n\t"
      "s_cbranch_scc1 L_g2_slow0_%=\n\t"
      "s_branch L_g2_back0_%=\n\t"
      "L_g2_slow1_%=:\n\t"
      "s_mov_b64 s[42:43], s[38:39]\n\t"
      "s_add_u32 s38, %[hlo], 16384\n\t"
      "s_addc_u32 s39, %[hhi], 0\n\t"
      "global_load_dwordx4 v[232:235], %[l16], s[38:39] offset:-4096 sc1\n\t"
      "global_load_dwordx4 v[236:239], %[l16], s[38:39] offset:-3072 sc1\n\t"
      "global_load_dwordx4 v[64:67], %[l16], s[38:39] offset:0 sc1\n\t"
      "global_load_dwordx4 v[68:71], %[l16], s[38:39] offset:1024 sc1\n\t"
      "s_add_u32 s38, %[hlo], 24576\n\t"
      "s_addc_u32 s39, %[hhi], 0\n\t"
      "global_load_dwordx4 v[72:75], %[l16], s[38:39] offset:-4096 sc1\n\t"
      "global_load_dwordx4 v[76:79], %[l16], s[38:39] offset:-3072 sc1\n\t"
      "global_load_dword v185, %[cko], %[wsb] offset:512 sc1\n\t"
      "s_mov_b64 s[38:39], s[42:43]\n\t"
      "s_waitcnt vmcnt(0)\n\t"
      "v_add3_u32 v46, v232, v233, %[t0]\n\t"
      "v_add3_u32 v46, v46, v234, v235\n\t"
      "v_add3_u32 v46, v46, v236, v237\n\t"
      "v_add3_u32 v46, v46, v238, v239\n\t"
      "v_add3_u32 v46, v46, v64, v65\n\t"
      "v_add3_u32 v46, v46, v66, v67\n\t"
      "v_add3_u32 v46, v46, v68, v69\n\t"
      "v_add3_u32 v46, v46, v70, v71\n\t"
      "v_add3_u32 v46, v46, v72, v73\n\t"
      "v_add3_u32 v46, v46, v74, v75\n\t"
      "v_add3_u32 v46, v46, v76, v77\n\t"
      "v_add3_u32 v46, v46, v78, v79\n\t"
      "v_cmp_ne_u32 vcc, v46, v185\n\t"
      "s_sub_u32 s44, s44, 1\n\t"
      "s_cbranch_vccz L_g2_back1_%=\n\t"
      "s_cmp_lg_u32 s44, 0\n\t"
      "s_cbranch_scc1 L_g2_slow1_%=\n\t"
      "s_branch L_g2_back1_%=\n\t"
      "L_g2_slow2_%=:\n\t"
      "s_mov_b64 s[42:43], s[38:39]\n\t"
      "s_add_u32 s38, %[hlo], 28672\n\t"
      "s_addc_u32 s39, %[hhi], 0\n\t"
      "global_load_dwordx4 v[80:83], %[l16], s[38:39] offset:-4096 sc1\n\t"
      "global_load_dwordx4 v[84:87], %[l16], s[38:39] offset:-3072 sc1\n\t"
      "global_load_dwordx4 v[88:91], %[l16], s[38:39] offset:0 sc1\n\t"
      "global_load_dwordx4 v[92:95], %[l16], s[38:39] offset:1024 sc1\n\t"
      "s_add_u32 s38, %[hlo], 36864\n\t"
      "s_addc_u32 s39, %[hhi], 0\n\t"
      "global_load_dwordx4 v[96:99], %[l16], s[38:39] offset:-4096 sc1\n\t"
      "global_load_dwordx4 v[100:103], %[l16], s[38:39] offset:-3072 sc1\n\t"
      "global_load_dword v186, %[cko], %[wsb] offset:1024 sc1\n\t"
      "s_mov_b64 s[38:39], s[42:43]\n\t"
      "s_waitcnt vmcnt(0)\n\t"
      "v_add3_u32 v46, v80, v81, %[t0]\n\t"
      "v_add3_u32 v46, v46, v82, v83\n\t"
      "v_add3_u32 v46, v46, v84, v85\n\t"
      "v_add3_u32 v46, v46, v86, v87\n\t"
      "v_add3_u32 v46, v46, v88, v89\n\t"
      "v_add3_u32 v46, v46, v90, v91\n\t"
      "v_add3_u32 v46, v46, v92, v93\n\t"
      "v_add3_u32 v46, v46, v94, v95\n\t"
      "v_add3_u32 v46, v46, v96, v97\n\t"
      "v_add3_u32 v46, v46, v98, v99\n\t"
      "v_add3_u32 v46, v46, v100, v101\n\t"
      "v_add3_u32 v46, v46, v102, v103\n\t"
      "v_cmp_ne_u32 vcc, v46, v186\n\t"
      "s_sub_u32 s44, s44, 1\n\t"
      "s_cbranch_vccz L_g2_back2_%=\n\t"
      "s_cmp_lg_u32 s44, 0\n\t"
      "s_cbranch_scc1 L_g2_slow2_%=\n\t"
      "s_branch L_g2_back2_%=\n\t"
      "L_g2_slow3_%=:\n\t"
      "s_mov_b64 s[42:43], s[38:39]\n\t"
      "s_add_u32 s38, %[hlo], 40960\n\t"
      "s_addc_u32 s39, %[hhi], 0\n\t"
      "global_load_dwordx4 v[104:107], %[l16], s[38:39] offset:-4096 sc1\n\t"
      "global_load_dwordx4 v[108:111], %[l16], s[38:39] offset:-3072 sc1\n\t"
      "global_load_dwordx4 v[112:115], %[l16], s[38:39] offset:0 sc1\n\t"
      "global_load_dwordx4 v[116:119], %[l16], s[38:39] offset:1024 sc1\n\t"
      "s_add_u32 s38, %[hlo], 49152\n\t"
      "s_addc_u32 s39, %[hhi], 0\n\t"
      "global_load_dwordx4 v[120:123], %[l16], s[38:39] offset:-4096 sc1\n\t"
      "global_load_dwordx4 v[124:127], %[l16], s[38:39] offset:-3072 sc1\n\t"
      "global_load_dword v187, %[cko], %[wsb] offset:1536 sc1\n\t"
      "s_mov_b64 s[38:39], s[42:43]\n\t"
      "s_waitcnt vmcnt(0)\n\t"
      "v_add3_u32 v46, v104, v105, %[t0]\n\t"
      "v_add3_u32 v46, v46, v106, v107\n\t"
      "v_add3_u32 v46, v46, v108, v109\n\t"
      "v_add3_u32 v46, v46, v110, v111\n\t"
      "v_add3_u32 v46, v46, v112, v113\n\t"
      "v_add3_u32 v46, v46, v114, v115\n\t"
      "v_add3_u32 v46, v46, v116, v117\n\t"
      "v_add3_u32 v46, v46, v118, v119\n\t"
      "v_add3_u32 v46, v46, v120, v121\n\t"
      "v_add3_u32 v46, v46, v122, v123\n\t"
      "v_add3_u32 v46, v46, v124, v125\n\t"
      "v_add3_u32 v46, v46, v126, v127\n\t"
      "v_cmp_ne_u32 vcc, v46, v187\n\t"
      "s_sub_u32 s44, s44, 1\n\t"
      "s_cbranch_vccz L_g2_back3_%=\n\t"
      "s_cmp_lg_u32 s44, 0\n\t"
      "s_cbranch_scc1 L_g2_slow3_%=\n\t"
      "s_branch L_g2_back3_%=\n\t"
      "L_g2_slow4_%=:\n\t"
      "s_mov_b64 s[42:43], s[38:39]\n\t"
      "s_add_u32 s38, %[hlo], 6144\n\t"
      "s_addc_u32 s39, %[hhi], 0\n\t"
      "global_load_dwordx4 v[48:51], %[l16], s[38:39] offset:-4096 sc1\n\t"
      "global_load_dwordx4 v[52:55], %[l16], s[38:39] offset:-3072 sc1\n\t"
      "global_load_dwordx4 v[56:59], %[l16], s[38:39] offset:0 sc1\n\t"
      "global_load_dwordx4 v[60:63], %[l16], s[38:39] offset:1024 sc1\n\t"
      "s_add_u32 s38, %[hlo], 14336\n\t"
      "s_addc_u32 s39, %[hhi], 0\n\t"
      "global_load_dwordx4 v[240:243], %[l16], s[38:39] offset:-4096 sc1\n\t"
      "global_load_dwordx4 v[244:247], %[l16], s[38:39] offset:-3072 sc1\n\t"
      "global_load_dword v32, %[cko], %[wsb] offset:256 sc1\n\t"
      "s_mov_b64 s[38:39], s[42:43]\n\t"
      "s_waitcnt vmcnt(0)\n\t"
      "v_add3_u32 v46, v48, v49, %[t0]\n\t"
      "v_add3_u32 v46, v46, v50, v51\n\t"
      "v_add3_u32 v46, v46, v52, v53\n\t"
      "v_add3_u32 v46, v46, v54, v55\n\t"
      "v_add3_u32 v46, v46, v56, v57\n\t"
      "v_add3_u32 v46, v46, v58, v59\n\t"
      "v_add3_u32 v46, v46, v60, v61\n\t"
      "v_add3_u32 v46, v46, v62, v63\n\t"
      "v_add3_u32 v46, v46, v240, v241\n\t"
      "v_add3_u32 v46, v46, v242, v243\n\t"
      "v_add3_u32 v46, v46, v244, v245\n\t"
      "v_add3_u32 v46, v46, v246, v247\n\t"
      "v_cmp_ne_u32 vcc, v46, v32\n\t"
      "s_sub_u32 s44, s44, 1\n\t"
      "s_cbranch_vccz L_g2_back4_%=\n\t"
      "s_cmp_lg_u32 s44, 0\n\t"
      "s_cbranch_scc1 L_g2_slow4_%=\n\t"
      "s_branch L_g2_back4_%=\n\t"
      "L_g2_slow5_%=:\n\t"
      "s_mov_b64 s[42:43], s[38:39]\n\t"
      "s_add_u32 s38, %[hlo], 18432\n\t"
      "s_addc_u32 s39, %[hhi], 0\n\t"
      "global_load_dwordx4 v[248:251], %[l16], s[38:39] offset:-4096 sc1\n\t"
      "global_load_dwordx4 v[252:255], %[l16], s[38:39] offset:-3072 sc1\n\t"
      "global_load_dwordx4 v[208:211], %[l16], s[38:39] offset:0 sc1\n\t"
      "global_load_dwordx4 v[212:215], %[l16], s[38:39] offset:1024 sc1\n\t"
      "s_add_u32 s38, %[hlo], 26624\n\t"
      "s_addc_u32 s39, %[hhi], 0\n\t"
      "global_load_dwordx4 v[216:219], %[l16], s[38:39] offset:-4096 sc1\n\t"
      "global_load_dwordx4 v[220:223], %[l16], s[38:39] offset:-3072 sc1\n\t"
      "global_load_dword v33, %[cko], %[wsb] offset:768 sc1\n\t"
      "s_mov_b64 s[38:39], s[42:43]\n\t"
      "s_waitcnt vmcnt(0)\n\t"
      "v_add3_u32 v46, v248, v249, %[t0]\n\t"
      "v_add3_u32 v46, v46, v250, v251\n\t"
      "v_add3_u32 v46, v46, v252, v253\n\t"
      "v_add3_u32 v46, v46, v254, v255\n\t"
      "v_add3_u32 v46, v46, v208, v209\n\t"
      "v_add3_u32 v46, v46, v210, v211\n\t"
      "v_add3_u32 v46, v46, v212, v213\n\t"
      "v_add3_u32 v46, v46, v214, v215\n\t"
      "v_add3_u32 v46, v46, v216, v217\n\t"
      "v_add3_u32 v46, v46, v218, v219\n\t"
      "v_add3_u32 v46, v46, v220, v221\n\t"
      "v_add3_u32 v46, v46, v222, v223\n\t"
      "v_cmp_ne_u32 vcc, v46, v33\n\t"
      "s_sub_u32 s44, s44, 1\n\t"
      "s_cbranch_vccz L_g2_back5_%=\n\t"
      "s_cmp_lg_u32 s44, 0\n\t"
      "s_cbranch_scc1 L_g2_slow5_%=\n\t"
      "s_branch L_g2_back5_%=\n\t"
      "L_g2_slow6_%=:\n\t"
      "s_mov_b64 s[42:43], s[38:39]\n\t"
      "s_add_u32 s38, %[hlo], 30720\n\t"
      "s_addc_u32 s39, %[hhi], 0\n\t"
      "global_load_dwordx4 v[224:227], %[l16], s[38:39] offset:-4096 sc1\n\t"
      "global_load_dwordx4 v[228:231], %[l16], s[38:39] offset:-3072 sc1\n\t"
      "global_load_dwordx4 v[232:235], %[l16], s[38:39] offset:0 sc1\n\t"
      "global_load_dwordx4 v[236:239], %[l16], s[38:39] offset:1024 sc1\n\t"
      "s_add_u32 s38, %[hlo], 38912\n\t"
      "s_addc_u32 s39, %[hhi], 0\n\t"
      "global_load_dwordx4 v[64:67], %[l16], s[38:39] offset:-4096 sc1\n\t"
      "global_load_dwordx4 v[68:71], %[l16], s[38:39] offset:-3072 sc1\n\t"
      "global_load_dword v34, %[cko], %[wsb] offset:1280 sc1\n\t"
      "s_mov_b64 s[38:39], s[42:43]\n\t"
      "s_waitcnt vmcnt(0)\n\t"
      "v_add3_u32 v46, v224, v225, %[t0]\n\t"
      "v_add3_u32 v46, v46, v226, v227\n\t"
      "v_add3_u32 v46, v46, v228, v229\n\t"
      "v_add3_u32 v46, v46, v230, v231\n\t"
      "v_add3_u32 v46, v46, v232, v233\n\t"
      "v_add3_u32 v46, v46, v234, v235\n\t"
      "v_add3_u32 v46, v46, v236, v237\n\t"
      "v_add3_u32 v46, v46, v238, v239\n\t"
      "v_add3_u32 v46, v46, v64, v65\n\t"
      "v_add3_u32 v46, v46, v66, v67\n\t"
      "v_add3_u32 v46, v46, v68, v69\n\t"
      "v_add3_u32 v46, v46, v70, v71\n\t"
      "v_cmp_ne_u32 vcc, v46, v34\n\t"
      "s_sub_u32 s44, s44, 1\n\t"
      "s_cbranch_vccz L_g2_back6_%=\n\t"
      "s_cmp_lg_u32 s44, 0\n\t"
      "s_cbranch_scc1 L_g2_slow6_%=\n\t"
      "s_branch L_g2_back6_%=\n\t"
      "L_g2_slow7_%=:\n\t"
      "s_mov_b64 s[42:43], s[38:39]\n\t"
      "s_add_u32 s38, %[hlo], 43008\n\t"
      "s_addc_u32 s39, %[hhi], 0\n\t"
      "global_load_dwordx4 v[72:75], %[l16], s[38:39] offset:-4096 sc1\n\t"
      "global_load_dwordx4 v[76:79], %[l16], s[38:39] offset:-3072 sc1\n\t"
      "global_load_dwordx4 v[80:83], %[l16], s[38:39] offset:0 sc1\n\t"
      "global_load_dwordx4 v[84:87], %[l16], s[38:39] offset:1024 sc1\n\t"
      "s_add_u32 s38, %[hlo], 51200\n\t"
      "s_addc_u32 s39, %[hhi], 0\n\t"
      "global_load_dwordx4 v[88:91], %[l16], s[38:39] offset:-4096 sc1\n\t"
      "global_load_dwordx4 v[92:95], %[l16], s[38:39] offset:-3072 sc1\n\t"
      "global_load_dword v35, %[cko], %[wsb] offset:1792 sc1\n\t"
      "s_mov_b64 s[38:39], s[42:43]\n\t"
      "s_waitcnt vmcnt(0)\n\t"
      "v_add3_u32 v46, v72, v73, %[t0]\n\t"
      "v_add3_u32 v46, v46, v74, v75\n\t"
      "v_add3_u32 v46, v46, v76, v77\n\t"
      "v_add3_u32 v46, v46, v78, v79\n\t"
      "v_add3_u32 v46, v46, v80, v81\n\t"
      "v_add3_u32 v46, v46, v82, v83\n\t"
      "v_add3_u32 v46, v46, v84, v85\n\t"
      "v_add3_u32 v46, v46, v86, v87\n\t"
      "v_add3_u32 v46, v46, v88, v89\n\t"
      "v_add3_u32 v46, v46, v90, v91\n\t"
      "v_add3_u32 v46, v46, v92, v93\n\t"
      "v_add3_u32 v46, v46, v94, v95\n\t"
      "v_cmp_ne_u32 vcc, v46, v35\n\t"
      "s_sub_u32 s44, s44, 1\n\t"
      "s_cbranch_vccz L_g2_back7_%=\n\t"
      "s_cmp_lg_u32 s44, 0\n\t"
      "s_cbranch_scc1 L_g2_slow7_%=\n\t"
      "s_branch L_g2_back7_%=\n\t"
      "L_g2_end%=:\n\t"
      : "+{v[64:95]}"(h0), "+{v[96:127]}"(h1), "+{v[208:239]}"(h2), "+{v[184:187]}"(hc)
      : [l16] "v"(lane * 16), [exw] "v"(lbase + w * 5120 + lane * 16), [hlo] "s"((unsigned)hb), [hhi] "s"((unsigned)(hb >> 32)), [dlo] "s"((unsigned)wb),
        [dhi] "s"((unsigned)(wb >> 32)), [cko] "v"(hco), [wsb] "s"(ws), [t0] "s"(T[0]), [xrm] "s"(lbase + L_XR + w * 3072), "{v[40:43]}"(xg)
      : "memory", "scc", "vcc", "s38", "s39", "s40", "s41", "s42", "s43", "s44", "s45", "s46", "s47",  "v32", "v33", "v34", "v35", "v46", "v47", "v48", "v49", "v50", "v51", "v52", "v53", "v54", "v55", "v56", "v57", "v58", "v59", "v60", "v61", "v62", "v63", "v128", "v129", "v130", "v131", "v132", "v133", "v134", "v135", "v136", "v137", "v138", "v139", "v140", "v141", "v142", "v143", "v144", "v145", "v146", "v147", "v148", "v149", "v150", "v151", "v152", "v153", "v154", "v155", "v156", "v157", "v158", "v159", "v160", "v161", "v162", "v163", "v164", "v165", "v166", "v167", "v168", "v169", "v170", "v171", "v172", "v173", "v174", "v175", "v176", "v177", "v178", "v179", "v180", "v181", "v182", "v183", "v188", "v189", "v190", "v191", "v192", "v193", "v194", "v195", "v196", "v197", "v198", "v199", "v200", "v201", "v202", "v203", "v204", "v205", "v206", "v207", "v240", "v241", "v242", "v243", "v244", "v245", "v246", "v247", "v248", "v249", "v250", "v251", "v252", "v253", "v254", "v255", "a0", "a1", "a2", "a3", "a4", "a5", "a6", "a7", "a8", "a9", "a10", "a11", "a12", "a13", "a14", "a15", "a16", "a17", "a18", "a19", "a20", "a21", "a22", "a23", "a24", "a25", "a26", "a27", "a28", "a29", "a30", "a31", "a32", "a33", "a34", "a35", "a36", "a37", "a38", "a39", "a40", "a41", "a42", "a43", "a44", "a45", "a46", "a47", "a48", "a49", "a50", "a51", "a52", "a53", "a54", "a55", "a56", "a57", "a58", "a59", "a60", "a61", "a62", "a63", "a64", "a65", "a66", "a67", "a68", "a69", "a70", "a71", "a72", "a73", "a74", "a75", "a76", "a77", "a78", "a79", "a80", "a81", "a82", "a83", "a84", "a85", "a86", "a87", "a88", "a89", "a90", "a91", "a92", "a93", "a94", "a95", "a96", "a97", "a98", "a99", "a100", "a101", "a102", "a103", "a104", "a105", "a106", "a107", "a108", "a109", "a110", "a111", "a112", "a113", "a114", "a115", "a116", "a117", "a118", "a119", "a120", "a121", "a122", "a123", "a124", "a125", "a126", "a127", "a128", "a129", "a130", "a131", "a132", "a133", "a134", "a135", "a136", "a137", "a138", "a139", "a140", "a141", "a142", "a143", "a144", "a145", "a146", "a147", "a148", "a149", "a150", "a151", "a152", "a153", "a154", "a155", "a156", "a157", "a158", "a159", "a160", "a161", "a162", "a163", "a164", "a165", "a166", "a167", "a168", "a169", "a170", "a171", "a172", "a173", "a174", "a175", "a176", "a177", "a178", "a179", "a180", "a181", "a182", "a183", "a184", "a185", "a186", "a187", "a188", "a189", "a190", "a191", "a192", "a193", "a194", "a195", "a196", "a197", "a198", "a199", "a200", "a201", "a202", "a203", "a204", "a205", "a206", "a207", "a208", "a209", "a210", "a211", "a212", "a213", "a214", "a215", "a216", "a217", "a218", "a219", "a220", "a221", "a222", "a223", "a224", "a225", "a226", "a227", "a228", "a229", "a230", "a231", "a232", "a233", "a234", "a235", "a236", "a237", "a238", "a239");

  {
    u32x2 xv[5];
    const unsigned xa = lbase + L_XR + w * 3072 + (4 * fq + (ip >> 2)) * 32 + (lane & 3) * 8;
#pragma unroll
    for (int n = 0; n < 5; ++n) DSRTR(xv[n], xa, n * 512);
    f32x4 gb[5];
#pragma unroll
    for (int n = 0; n < 5; ++n) gb[n] = LDSF4(lds + L_GB + (4 * n + fq) * 16);
    const int row = (bm2 >> 1) * 128 + (w >> 1) * 64 + (bm2 & 1) * 32 + (w & 1) * 16 + ip;
    GAS u16* op = out + (size_t)row * D_ + bn2 * 80 + fq * 4;
    asm volatile("" : "+v"(gb[0]), "+v"(gb[1]), "+v"(gb[2]), "+v"(gb[3]), "+v"(gb[4]));
    asm volatile("s_waitcnt lgkmcnt(0)\n\ts_barrier" : "+v"(xv[0]), "+v"(xv[1]), "+v"(xv[2]), "+v"(xv[3]), "+v"(xv[4])::"memory");
    f32x4 pp[20];
#pragma unroll
    for (int n = 0; n < 5; ++n)
#pragma unroll
      for (int j = 0; j < 4; ++j) pp[4 * n + j] = LDSF4(lds + ((w * 20 + j * 5 + n) * 64 + lane) * 16);
    SCHEDB();
#pragma unroll
    for (int n = 0; n < 5; ++n) {
      const f32x4 a = (pp[4 * n] + pp[4 * n + 1]) + (pp[4 * n + 2] + pp[4 * n + 3]);
      const f32x2 m01 = rb2((f32x2){gb[n][0], gb[n][1]} * rb2((f32x2){a[0], a[1]}));
      const f32x2 m23 = rb2((f32x2){gb[n][2], gb[n][3]} * rb2((f32x2){a[2], a[3]}));
      const f32x2 o01 = up2(xv[n][0]) + m01, o23 = up2(xv[n][1]) + m23;
      const u32x2 ov = {pk2(o01[0], o01[1]), pk2(o23[0], o23[1])};
      *(GAS u32x2*)(op + n * 16) = ov;
    }
  }
  { const u64 tg_ = t0_ + 6000 + ((-1 < 0 || b == -1) ? 4 * (tk_ - t0_) : 0); while (wall_clock64() < tg_) __builtin_amdgcn_s_sleep(2); }
}

extern "C" void kernel_launch(void* const* d_in, const int* in_sizes, int n_in, void* d_out, int out_size,
                              void* d_ws, size_t ws_size, hipStream_t stream) {
  mega<<<256, 256, 0, stream>>>((const u16*)d_in[0], (const u16*)d_in[1], (const u16*)d_in[2], (const u16*)d_in[3],
                                (const u16*)d_in[4], (const u16*)d_in[5], (const u16*)d_in[7], (const u16*)d_in[8],
                                (const u16*)d_in[9], (const u16*)d_in[10], (const u16*)d_in[11], (char*)d_ws, (u16*)d_out);
}
```

```cpp
#include <hip/hip_runtime.h>
#include <stdint.h>

typedef unsigned short u16;
typedef __attribute__((ext_vector_type(4))) unsigned int u32x4;
typedef __attribute__((ext_vector_type(8))) unsigned int u32x8;
typedef __attribute__((ext_vector_type(16))) unsigned int u32x16;
static constexpr int D_ = 640, F_ = 1536;

static constexpr unsigned WS_FW = 20480;
static constexpr unsigned WS_FD = WS_FW + 61440;
static constexpr unsigned WS_STAT = 147456;
static constexpr unsigned WS_PART = 262144;
static constexpr unsigned WS_MOD = 786432;
static constexpr unsigned WS_KVT = 1u << 20;
static constexpr unsigned WS_QT = 4u << 20;
static constexpr unsigned WS_X1T = 7u << 20;
static constexpr unsigned WS_CK = 2686976;
static constexpr unsigned WS_WP = 10u << 20;
static constexpr unsigned WS_WD = 14u << 20;
static constexpr unsigned WS_HH = 16u << 20;
static constexpr unsigned WS_HC = 22u << 20;
static constexpr unsigned WS_DUMMY = WS_HC + (768u << 10);

static constexpr int KCP = 4160, KCH = 4 * KCP;
static constexpr int XR_CH = 7680;
static constexpr int L_XI = 3 * KCH;
static constexpr int L_EX = 72960;
static constexpr int L_KV = L_EX, KVW = 6400;
static constexpr int L_KT = L_KV + 4 * KVW;
static constexpr int L_QT = L_KT + 84 * 144;
static constexpr int L_WR = L_QT + 10240;
static constexpr int L_GW = L_WR + 16384;
static constexpr int L_GS = L_GW + 20480;
static constexpr int L_SC = L_GS + 320;
static constexpr int L_WD1 = 122880;
static_assert(L_KT == 98560 && L_QT == 110656 && L_WR == 120896 && L_GW == 137280, "");

struct __attribute__((aligned(256))) Tab { unsigned s[1024][64]; };
static constexpr Tab make_tab() {
  Tab t = {};
  for (int i = 0; i < 1024; ++i) {
    const int b = i >> 2, w = i & 3, xcd = b & 7, idx = b >> 3, L = xcd * 32 + idx, side = L & 1;
    const int ct = L & 7, rt = L >> 3, srow = xcd * 256 + idx * 8;
    const int chA = 128 + L, chH = L >> 1, rH = 512 * (side + (3 - 2 * side) * (w >> 1)) + 256 * (w & 1);
    const int id = idx < 29 ? idx : 29, gj = xcd * 30 + id, j_ks = 5 * (xcd >> 1) + (id * 4 + w) / 24, j_b = 24 * (xcd & 1) + (id * 4 + w) % 24;
    const int d_ks = gj / 5, d_cb = 4 * (gj % 5) + w;
    const unsigned dummy = WS_DUMMY + (i & 63) * 2048;
    const int bm = (xcd >> 2) * 8 + (idx >> 2), bn = (xcd & 3) * 4 + (idx & 3), bm2 = (xcd >> 1) * 8 + (idx >> 2), bn2 = (xcd & 1) * 4 + (idx & 3);
    t.s[i][0] = WS_KVT + (1984 - 64 * rt) * 2 + ct * 80 * 4096;
    t.s[i][1] = WS_KVT + WS_CK + ((31 - rt) / 4 * D_ + ct * 80) * 16 + (31 - rt) % 4 * 4;
    t.s[i][2] = idx < 30 ? WS_MOD + (gj >> 2) * 2048 + (gj & 3) * 8 : dummy;
    t.s[i][3] = 32 * j_ks * (F_ * 2) + (j_b - w) * 64;
    t.s[i][4] = WS_QT + rt * 128 + ct * 80 * 4096;
    t.s[i][5] = WS_QT + WS_CK + (rt / 4 * D_ + ct * 80) * 32 + rt % 4 * 8;
    t.s[i][6] = srow * 1280;
    t.s[i][7] = WS_STAT + (srow >> 9) * 4096 + ((srow >> 3) & 63) * 16;
    t.s[i][8] = WS_MOD + (1920 + 640 * (w >> 1) + 32 * j_ks + 16 * (w & 1)) * 32;
    t.s[i][9] = idx < 30 ? WS_PART + j_ks * 24576 + j_b * 256 : dummy;
    t.s[i][10] = WS_KVT + WS_CK + chA * 16;
    t.s[i][11] = WS_KVT + WS_CK + chH * 16;
    t.s[i][12] = idx < 30 ? WS_WP + (4 * j_b + 0) / 3 * 61440 + (j_ks * 3 + (4 * j_b + 0) % 3) * 1024 : dummy;
    t.s[i][13] = idx < 30 ? WS_WP + (4 * j_b + 1) / 3 * 61440 + (j_ks * 3 + (4 * j_b + 1) % 3) * 1024 : dummy;
    t.s[i][14] = idx < 30 ? WS_WP + (4 * j_b + 2) / 3 * 61440 + (j_ks * 3 + (4 * j_b + 2) % 3) * 1024 : dummy;
    t.s[i][15] = idx < 30 ? WS_WP + (4 * j_b + 3) / 3 * 61440 + (j_ks * 3 + (4 * j_b + 3) % 3) * 1024 : dummy;
    t.s[i][16] = WS_FW + xcd * 7680 + (3 * (bn * 4 + w) / 4 < 46 ? 3 * (bn * 4 + w) / 4 : 46) * 160;
    t.s[i][17] = WS_X1T + WS_CK + chH / 8 * 1024 + chH % 8 * 4 + (rH >> 6) * 32;
    t.s[i][18] = WS_X1T + ((rH >> 6) * D_ + chH) * 128;
    t.s[i][19] = idx < 30 ? WS_FW + (j_b * 20 + j_ks) * 8 : dummy;
    t.s[i][20] = WS_QT + chH * 4096 + rH * 2;
    t.s[i][21] = WS_X1T + WS_CK + chA / 8 * 1024 + chA % 8 * 4;
    t.s[i][22] = WS_STAT + (rH >> 9) * 4096 + ((rH >> 3) & 63) * 16;
    t.s[i][23] = WS_MOD + chH * 32;
    t.s[i][24] = WS_QT + WS_CK + chA * 32;
    t.s[i][25] = WS_QT + WS_CK + ((rH >> 8) * D_ + chH) * 32;
    t.s[i][26] = idx < 30 ? WS_WD + ((2 * d_cb / 5 * 48 + d_ks) * 5 + 2 * d_cb % 5) * 1024 : dummy;
    t.s[i][27] = idx < 30 ? WS_WD + (((2 * d_cb + 1) / 5 * 48 + d_ks) * 5 + (2 * d_cb + 1) % 5) * 1024 : dummy;
    t.s[i][28] = idx < 30 ? WS_FD + (d_cb * 48 + d_ks) * 8 : dummy;
    t.s[i][29] = WS_X1T + chA * 128;
    t.s[i][30] = WS_X1T + WS_CK + bm * 64;
    t.s[i][31] = ((bm2 >> 1) * 128 + (w >> 1) * 64 + (bm2 & 1) * 32 + (w & 1) * 16) * (D_ * 2) + bn2 * 160;
    t.s[i][32] = chA * 4096;
    t.s[i][33] = chH * 4096;
    t.s[i][34] = ((1792 - rH) >> 5) * 96;
    t.s[i][35] = (96 * (d_ks / 3) + 16 * (d_ks % 3 >> 1) + 2 * (d_ks % 3 & 1)) * (D_ * 2) + d_cb * 64;
    t.s[i][36] = (d_ks % 3 == 1 ? 30 : 18) * (D_ * 2);
    t.s[i][37] = ((chA >> 1) & 3) * 32;
    t.s[i][38] = ((chH >> 1) & 3) * 32;
    t.s[i][39] = side;
    t.s[i][48] = w;
    t.s[i][49] = WS_HH + (((2 * bm + (w >> 1)) * 4 + (w & 1)) * 48 + 3 * bn) * 1024;
    t.s[i][50] = WS_HC + (((2 * bm + (w >> 1)) * 4 + (bn >> 2)) * 8 + (bn & 3)) * 512 + (w & 1) * 4;
    t.s[i][51] = WS_HC + (bm2 * 4 + w) * 4096;
    t.s[i][52] = WS_FD + xcd * 7680 + (((5 * bn2) >> 1) * 48 + 12 * w) * 8;
    t.s[i][53] = WS_PART + (bn * 192 + 48 * w) * 4;
    t.s[i][54] = WS_MOD + (3200 + bn2 * 80) * 32;
    t.s[i][55] = WS_X1T + (((bm2 & ~1) + (w >> 1)) * D_ + bn2 * 80) * 128;
    t.s[i][56] = ((2 * bm2 + (w & 1)) & 3) * 32;
    t.s[i][57] = 98304 + ((unsigned)w << 31);
    t.s[i][58] = (4 + 2 * (w & 1) + ((bn & 3) >> 1) - (bn & 3)) * 512 + ((bn & 1) - (w & 1)) * 4 + ((unsigned)w << 31);
    t.s[i][59] = WS_X1T + bm * 163840;
    t.s[i][60] = WS_WD + (bn2 * 48 + 12 * w) * 5120;
    t.s[i][61] = w * 4096;
    t.s[i][62] = WS_WP + (bn * 4 + w) * 61440;
    t.s[i][63] = WS_HH + (bm2 * 192 + 12 * w) * 1024;
  }
  return t;
}
static __device__ const Tab TAB = make_tab();

__global__ __launch_bounds__(256, 1) void mega(const u16* q, const u16* k, const u16* v, const u16* shift_k, const u16* ssm_k, const u16* ssm_D, const u16* w_gate,
                                               const u16* w_up, const u16* w_down, const u16* c, const u16* w_mod, char* ws, u16* out) {
  __shared__ __attribute__((aligned(16))) char lds[163840];
  int w = (int)(size_t)lds, tid = threadIdx.x, v1;
  u32x16 s4, s60, s76;
  u32x8 s20, s52, s92;
  u32x4 s28, s36, v232;
  asm volatile(
      "s_load_dwordx16 s[4:19], s[0:1], 0x0\n"
      "s_load_dwordx8 s[20:27], s[0:1], 0x40\n"
      "s_load_dwordx2 s[28:29], s[0:1], 0x60\n"
      "v_readfirstlane_b32 s3, v0\n"
      "v_and_b32 v2, 63, v0\n"
      "s_lshl_b32 s42, s2, 4\n"
      "s_and_b32 s43, s2, 7\n"
      "v_mul_u32_u24 v3, 205, v2\n"
      "s_and_b32 s52, s2, 0xc0\n"
      "s_lshr_b32 s3, s3, 6\n"
      "v_lshlrev_b32 v8, 4, v2\n"
      "s_cmp_lg_u32 s3, 3\n"
      "s_cbranch_scc1 L_pf%=\n"
      "s_and_b32 s47, s2, 0xf8\n"
      "s_lshl_b32 s47, s47, 10\n"
      "s_cmp_ge_u32 s47, 38976\n"
      "s_cbranch_scc1 L_pf%=\n"
      "s_getpc_b64 s[54:55]\n"
      "v_lshl_add_u32 v9, v8, 3, s47\n"
      "s_and_b32 s54, s54, 0xffffff00\n"
      "s_mov_b32 m0, 160512\n"
      "v_min_u32 v9, 38912, v9\n"
      "global_load_lds_dword v9, s[54:55]\n"
      "L_pf%=:\n"
      "s_lshl_b32 s47, s43, 8\n"
      "v_add_u32 v9, 13120, v3\n"
      "v_add_u32 v10, 26240, v3\n"
      "s_add_u32 s52, s52, s47\n"
      "s_lshl_b32 s47, s3, 4\n"
      "v_lshrrev_b32 v3, 11, v3\n"
      "v_lshrrev_b32 v9, 11, v9\n"
      "s_add_u32 s52, s52, s47\n"
      "s_movk_i32 s47, 1120\n"
      "v_lshrrev_b32 v10, 11, v10\n"
      "s_mulk_i32 s52, 1280\n"
      "s_bfe_u32 s54, s2, 0x30003\n"
      "v_mad_u32_u24 v232, v3, s47, v8\n"
      "v_mad_u32_u24 v233, v9, s47, v8\n"
      "v_mad_u32_u24 v234, v10, s47, v8\n"
      "s_mul_i32 s47, s54, 160\n"
      "s_mul_i32 s53, s3, 6400\n"
      "s_add_u32 s52, s52, s47\n"
      "v_subrev_u32 v233, 6656, v233\n"
      "v_subrev_u32 v234, 5632, v234\n"
      "s_sub_u32 s47, s52, 3840\n"
      "s_add_u32 s53, s53, 72960\n"
      "v_add_u32 v3, s47, v232\n"
      "s_add_u32 s47, s52, 3840\n"
      "s_add_u32 s55, s52, 7680\n"
      "s_mov_b32 s38, 2621440\n"
      "s_mov_b32 s39, 0x20000\n"
      "s_mov_b32 m0, s53\n"
      "s_mulk_i32 s54, 640\n"
      "s_waitcnt lgkmcnt(0)\n"
      "s_mov_b32 s36, s6\n"
      "s_and_b32 s37, s7, 0xffff\n"
      "buffer_load_dwordx4 v3, s[36:39], 0 offen lds\n"
      "s_add_u32 m0, s53, 1024\n"
      "s_load_dwordx2 s[30:31], s[26:27], 0x0\n"
      "buffer_load_dwordx4 v233, s[36:39], s47 offen lds\n"
      "s_add_u32 m0, s53, 2048\n"
      "v_add_u32 v9, s54, v8\n"
      "buffer_load_dwordx4 v234, s[36:39], s47 offen lds\n"
      "s_add_u32 m0, s53, 3072\n"
      "s_mov_b32 s36, s8\n"
      "s_and_b32 s37, s9, 0xffff\n"
      "buffer_load_dwordx4 v232, s[36:39], s52 offen lds\n"
      "s_add_u32 m0, s53, 4096\n"
      "s_lshl_b32 s47, s3, 2\n"
      "buffer_load_dwordx4 v233, s[36:39], s55 offen lds\n"
      "s_add_u32 m0, s53, 5120\n"
      "s_mov_b32 exec_hi, 0\n"
      "buffer_load_dwordx4 v234, s[36:39], s55 offen lds\n"
      "s_add_u32 m0, s53, 5632\n"
      "s_mov_b32 exec_hi, 0xff\n"
      "global_load_lds_dwordx4 v9, s[10:11]\n"
      "s_mov_b32 exec_hi, -1\n"
      "s_add_u32 s42, s42, s47\n"
      "s_lshl_b32 s42, s42, 6\n"
      "s_add_u32 s56, s40, s42\n"
      "s_addc_u32 s57, s41, 0\n"
      "s_load_dwordx16 s[60:75], s[56:57], 0x0\n"
      "s_load_dwordx16 s[76:91], s[56:57], 0x40\n"
      "s_load_dwordx8 s[92:99], s[56:57], 0x80\n"
      "s_lshr_b32 s42, s2, 3\n"
      "s_lshl_b32 s47, s43, 5\n"
      "s_add_u32 s47, s47, s42\n"
      "s_and_b32 s54, s47, -4\n"
      "s_lshl_b32 s47, s47, 1\n"
      "s_and_b32 s47, s47, -4\n"
      "s_load_dword s53, s[14:15], s54\n"
      "s_load_dword s58, s[14:15], s47 offset:0x100\n"
      "s_load_dword s59, s[14:15], s47 offset:0x300\n"
      "s_lshl_b32 s47, s43, 5\n"
      "s_add_u32 s47, s47, s42\n"
      "s_min_u32 s54, s3, 2\n"
      "s_lshl_b32 s54, s54, 10\n"
      "s_lshr_b32 s10, s47, 1\n"
      "s_lshl_b32 s47, s47, 12\n"
      "s_lshl_b32 s10, s10, 12\n"
      "s_add_u32 s47, s47, s54\n"
      "s_add_u32 s54, s54, s10\n"
      "s_load_dwordx2 s[6:7], s[12:13], s47 offset:0x80400\n"
      "s_add_u32 s47, s47, 0x100000\n"
      "s_load_dwordx2 s[10:11], s[12:13], s54 offset:0x400\n"
      "s_load_dwordx2 s[8:9], s[12:13], s47 offset:0x80400\n"
      "s_min_u32 s42, s42, 29\n"
      "s_mul_i32 s43, s43, 30\n"
      "v_lshrrev_b32 v3, 3, v2\n"
      "s_add_u32 s42, s42, s43\n"
      "v_xor_b32 v9, v3, v2\n"
      "s_and_b32 s43, s42, 3\n"
      "v_and_b32 v11, 1, v2\n"
      "s_lshl_b32 s43, s43, 2\n"
      "v_and_or_b32 v9, v9, 6, v11\n"
      "s_add_u32 s43, s43, s3\n"
      "v_mul_u32_u24 v3, 7680, v3\n"
      "s_lshr_b32 s42, s42, 2\n"
      "s_mul_i32 s47, s43, 307200\n"
      "s_lshl_b32 s42, s42, 7\n"
      "v_lshl_add_u32 v3, v9, 4, v3\n"
      "s_add_u32 s42, s42, s47\n"
      "s_mul_i32 s47, s3, 5120\n"
      "s_add_u32 s24, s24, s42\n"
      "s_addc_u32 s25, s25, 0\n"
      "s_add_u32 m0, s47, 137280\n"
      "s_mulk_i32 s43, 80\n"
      "global_load_lds_dwordx4 v3, s[24:25]\n"
      "s_add_u32 m0, m0, 1024\n"
      "v_add_u32 v3, 61440, v3\n"
      "global_load_lds_dwordx4 v3, s[24:25]\n"
      "s_add_u32 m0, m0, 1024\n"
      "v_add_u32 v3, 61440, v3\n"
      "global_load_lds_dwordx4 v3, s[24:25]\n"
      "s_add_u32 m0, m0, 1024\n"
      "v_add_u32 v3, 61440, v3\n"
      "global_load_lds_dwordx4 v3, s[24:25]\n"
      "s_add_u32 m0, m0, 1024\n"
      "v_add_u32 v3, 61440, v3\n"
      "global_load_lds_dwordx4 v3, s[24:25]\n"
      "v_min_u32 v9, 39, v2\n"
      "v_lshl_add_u32 v9, v9, 1, s43\n"
      "global_load_ushort v235, v9, s[22:23]\n"
      : "+{s3}"(w), "+{v0}"(tid), "=&{s[4:19]}"(s4), "=&{s[20:27]}"(s20), "=&{s[28:31]}"(s28), "=&{s[36:39]}"(s36), "=&{s[52:59]}"(s52), "=&{s[60:75]}"(s60), "=&{s[76:91]}"(s76),
        "=&{s[92:99]}"(s92), "=&{v[232:235]}"(v232), "=&{v1}"(v1)
      : "{s[0:1]}"(__builtin_amdgcn_kernarg_segment_ptr()), "{s2}"(blockIdx.x), "{s[40:41]}"(&TAB)
      : "memory", "scc", "s42", "s43", "s47", "v2", "v3", "v8", "v9", "v10", "v11");
#define STATE "+{s3}"(w), "+{v0}"(tid), "+{s[4:19]}"(s4), "+{s[20:27]}"(s20), "+{s[28:31]}"(s28), "+{s[36:39]}"(s36), "+{s[52:59]}"(s52), "+{s[60:75]}"(s60), "+{s[76:91]}"(s76), \
              "+{s[92:99]}"(s92), "+{v[232:235]}"(v232), "+{v1}"(v1)
  u32x4 t[18], s32, v24, v52;
  u32x16 v8;
  {
    const int lane = tid & 63, ip = lane & 15, fq = lane >> 4, q4 = ip >> 2, p4 = lane & 3;
    const int ck = 40 * w + (lane < 40 ? lane : 39);
    const bool dg = fq == q4;
    const unsigned msk = dg ? ((ip & 1) ? 0xFFFF0000u : 0xFFFFu) : 0u, mk0 = (ip & 2) ? 0u : msk, mk1 = (ip & 2) ? msk : 0u;
    t[0][0] = dg && p4 == 0 ? 0x3F803F80u : 0u;
    t[0][1] = dg && p4 == 1 ? 0x3F803F80u : 0u;
    t[0][2] = dg && p4 == 2 ? 0x3F803F80u : 0u;
    t[0][3] = dg && p4 == 3 ? 0x3F803F80u : 0u;
    t[1][0] = (mk0 & 0x0D0C0D0Cu) ^ 0x0C0C0C0Cu;
    t[1][1] = (mk1 & 0x0D0C0D0Cu) ^ 0x0C0C0C0Cu;
    t[1][2] = (mk0 & 0x0F0E0F0Eu) ^ 0x0C0C0C0Cu;
    t[1][3] = (mk1 & 0x0F0E0F0Eu) ^ 0x0C0C0C0Cu;
    t[2][0] = L_KV + w * KVW + ip * 160 + fq * 8;
    t[2][1] = L_KV + w * KVW + 5632 + ip * 8;
    t[2][2] = mk0;
    t[2][3] = mk1;
    t[3][0] = L_KT + ip * 144 + (60 - 16 * w - 4 * fq) * 2;
    t[3][1] = L_KT + (20 * w + (lane >> 3)) * 144 + (lane & 7) * 16;
    t[3][2] = (20 * w + (lane >> 3)) * 4096 + (lane & 7) * 16;
    t[3][3] = 8 * (lane & 7) + (lane >> 3) < 20 ? (20 * w + 8 * (lane & 7) + (lane >> 3)) * 16 : 0x80000000u;
    t[4][0] = L_GS + ((ck & ~31) + ((ck >> 2) & 3) * 8 + ((ck >> 4) & 1) * 4 + (ck & 3)) * 2;
    t[4][1] = L_GW + (4 * fq + q4) * 128 + p4 * 8 + ((w ^ (2 * fq + (q4 >> 1))) & 3) * 32;
    t[4][2] = L_GS + fq * 16;
    t[4][3] = lane < 16 ? (16 * w + lane) * 32 : 0x80000000u;
    t[5][0] = (lane & 12) == 4 ? 1 << 20 : 0;
    t[5][1] = lane & 8 ? 0u : ~0u;
    t[5][2] = (8 * w + fq) * (F_ * 2) + (ip ^ (2 * fq)) * 16;
    t[5][3] = (2 * w + (lane >> 5)) * 1280 + (lane & 31) * 16;
  }
  asm volatile(
      "s_mov_b32 s48, 0xaaaaaaaa\n"
      "s_mov_b32 s49, s48\n"
      "s_mov_b32 s50, 0xcccccccc\n"
      "s_mov_b32 s51, s50\n"
      "s_mov_b32 s32, s26\n"
      "s_and_b32 s33, s27, 0xffff\n"
      "s_mov_b32 s34, 24117248\n"
      "s_mov_b32 s35, 0x20000\n"
      "s_sub_u32 s18, s18, 2048\n"
      "s_subb_u32 s19, s19, 0\n"
      "s_add_u32 s22, s20, 60416\n"
      "s_addc_u32 s23, s21, 0\n"
      "v_lshlrev_b32 v54, 4, v0\n"
      "v_mov_b32 v66, 0\n"
      "v_mov_b32 v67, 0\n"
      "v_mov_b32 v72, 0\n"
      "v_mov_b32 v73, 0\n"
      "v_mov_b32 v76, 0\n"
      "v_mov_b32 v77, 0\n"
      "v_mov_b32 v82, 0\n"
      "v_mov_b32 v83, 0\n"
      "v_mov_b32 v88, 0\n"
      "v_mov_b32 v89, 0\n"
      "v_mov_b32 v92, 0\n"
      "v_mov_b32 v93, 0\n"
      "v_mov_b32 v98, 0\n"
      "v_mov_b32 v99, 0\n"
      "v_mov_b32 v104, 0\n"
      "v_mov_b32 v105, 0\n"
      "v_mov_b32 v108, 0\n"
      "v_mov_b32 v109, 0\n"
      "v_mov_b32 v114, 0\n"
      "v_mov_b32 v115, 0\n"
      "v_mov_b32 v120, 0\n"
      "v_mov_b32 v121, 0\n"
      "v_mov_b32 v124, 0\n"
      "v_mov_b32 v125, 0\n"
      "v_mov_b32 v34, 0\n"
      "v_mov_b32 v35, 0\n"
      "s_waitcnt lgkmcnt(0)\n"
      "s_add_u32 s30, s30, 1\n"
      "s_addc_u32 s31, s31, 0\n"
      "s_mul_i32 s24, s30, 13\n"
      "v_mov_b32 v52, s30\n"
      "v_mov_b32 v53, s31\n"
      "s_add_u32 s42, s60, 0x8000\n"
      "s_add_u32 s43, s60, 0x10000\n"
      "s_mov_b32 s36, s12\n"
      "s_and_b32 s37, s13, 0xffff\n"
      "s_add_u32 s47, s92, 0x100000\n"
      "s_sub_u32 s54, s92, s93\n"
      "s_lshl_b32 s45, s3, 12\n"
      "v_and_b32 v39, s54, v209\n"
      "v_add_u32 v210, s63, v210\n"
      "v_add_u32 v208, v39, v208\n"
      "v_add_u32 v39, 11264, v210\n"
      "v_and_b32 v32, 0x3f803f80, v198\n"
      "v_and_b32 v33, 0x3f803f80, v199\n"
      "v_and_b32 v36, 0x3f803f80, v198\n"
      "v_and_b32 v37, 0x3f803f80, v199\n"
      "s_waitcnt vmcnt(6)\n"
      "ds_read_b64 v[244:245], v197 offset:0\n"
      "ds_read_b64 v[246:247], v197 offset:128\n"
      "ds_read_b64 v[248:249], v197 offset:256\n"
      "ds_read_b64 v[250:251], v197 offset:384\n"
      "ds_read_b64 v[252:253], v197 offset:512\n"
      "ds_read_b64 a[0:1], v196 offset:0\n"
      "ds_read_b64 a[2:3], v196 offset:32\n"
      "ds_read_b64 a[4:5], v196 offset:64\n"
      "ds_read_b64 a[6:7], v196 offset:96\n"
      "ds_read_b64 a[8:9], v196 offset:128\n"
      "ds_read_b64 a[10:11], v196 offset:160\n"
      "ds_read_b64 a[12:13], v196 offset:192\n"
      "ds_read_b64 a[14:15], v196 offset:224\n"
      "ds_read_b64 a[16:17], v196 offset:256\n"
      "ds_read_b64 a[18:19], v196 offset:288\n"
      "ds_read_b64 a[20:21], v196 offset:320\n"
      "ds_read_b64 a[22:23], v196 offset:352\n"
      "ds_read_b64 a[24:25], v196 offset:384\n"
      "ds_read_b64 a[26:27], v196 offset:416\n"
      "ds_read_b64 a[28:29], v196 offset:448\n"
      "ds_read_b64 a[30:31], v196 offset:480\n"
      "ds_read_b64 a[32:33], v196 offset:512\n"
      "ds_read_b64 a[34:35], v196 offset:544\n"
      "ds_read_b64 a[36:37], v196 offset:576\n"
      "ds_read_b64 a[38:39], v196 offset:608\n"
      "ds_read_b64 a[40:41], v196 offset:3072\n"
      "ds_read_b64 a[42:43], v196 offset:3104\n"
      "ds_read_b64 a[44:45], v196 offset:3136\n"
      "ds_read_b64 a[46:47], v196 offset:3168\n"
      "ds_read_b64 a[48:49], v196 offset:3200\n"
      "s_waitcnt lgkmcnt(14)\n"
      "v_perm_b32 v64, v245, v245, v242\n"
      "v_perm_b32 v65, v245, v245, v243\n"
      "v_perm_b32 v80, v245, v245, v240\n"
      "v_perm_b32 v81, v245, v245, v241\n"
      "v_perm_b32 v68, v247, v247, v242\n"
      "v_perm_b32 v69, v247, v247, v243\n"
      "v_perm_b32 v84, v247, v247, v240\n"
      "v_perm_b32 v85, v247, v247, v241\n"
      "v_perm_b32 v70, v249, v249, v242\n"
      "v_perm_b32 v71, v249, v249, v243\n"
      "v_perm_b32 v86, v249, v249, v240\n"
      "v_perm_b32 v87, v249, v249, v241\n"
      "v_perm_b32 v74, v251, v251, v242\n"
      "v_perm_b32 v75, v251, v251, v243\n"
      "v_perm_b32 v90, v251, v251, v240\n"
      "v_perm_b32 v91, v251, v251, v241\n"
      "v_perm_b32 v78, v253, v253, v242\n"
      "v_perm_b32 v79, v253, v253, v243\n"
      "v_perm_b32 v94, v253, v253, v240\n"
      "v_perm_b32 v95, v253, v253, v241\n"
      "v_mfma_f32_16x16x32_bf16 v[128:131], a[0:3], v[64:67], 0\n"
      "v_perm_b32 v96, v244, v244, v242\n"
      "v_perm_b32 v97, v244, v244, v243\n"
      "v_mfma_f32_16x16x32_bf16 v[132:135], a[10:13], v[80:83], 0\n"
      "buffer_load_dwordx4 v[8:11], v54, s[36:39], s92 offen\n"
      "v_perm_b32 v112, v244, v244, v240\n"
      "v_perm_b32 v113, v244, v244, v241\n"
      "v_mfma_f32_16x16x32_bf16 v[140:143], a[0:3], v[66:69], 0\n"
      "v_perm_b32 v100, v246, v246, v242\n"
      "v_perm_b32 v101, v246, v246, v243\n"
      "v_mfma_f32_16x16x32_bf16 v[144:147], a[10:13], v[82:85], 0\n"
      "buffer_load_dwordx4 v[14:17], v54, s[36:39], s47 offen\n"
      "v_perm_b32 v116, v246, v246, v240\n"
      "v_perm_b32 v117, v246, v246, v241\n"
      "v_mfma_f32_16x16x32_bf16 v[152:155], a[4:7], v[70:73], 0\n"
      "v_perm_b32 v102, v248, v248, v242\n"
      "v_perm_b32 v103, v248, v248, v243\n"
      "v_mfma_f32_16x16x32_bf16 v[156:159], a[14:17], v[86:89], 0\n"
      "buffer_load_dwordx4 v[20:23], v54, s[36:39], s93 offen\n"
      "v_perm_b32 v118, v248, v248, v240\n"
      "v_perm_b32 v119, v248, v248, v241\n"
      "v_mfma_f32_16x16x32_bf16 v[164:167], a[4:7], v[72:75], 0\n"
      "v_perm_b32 v106, v250, v250, v242\n"
      "v_perm_b32 v107, v250, v250, v243\n"
      "v_mfma_f32_16x16x32_bf16 v[168:171], a[14:17], v[88:91], 0\n"
      "buffer_load_dwordx2 v[26:27], v208, s[36:39], s93 offen\n"
      "v_perm_b32 v122, v250, v250, v240\n"
      "v_perm_b32 v123, v250, v250, v241\n"
      "v_mfma_f32_16x16x32_bf16 v[176:179], a[6:9], v[76:79], 0\n"
      "v_perm_b32 v110, v252, v252, v242\n"
      "v_perm_b32 v111, v252, v252, v243\n"
      "v_mfma_f32_16x16x32_bf16 v[180:183], a[16:19], v[92:95], 0\n"
      "v_perm_b32 v126, v252, v252, v240\n"
      "v_perm_b32 v127, v252, v252, v241\n"
      "s_waitcnt lgkmcnt(0)\n"
      "v_cvt_pk_bf16_f32 v136, v128, v132\n"
      "v_cvt_pk_bf16_f32 v137, v129, v133\n"
      "v_cvt_pk_bf16_f32 v138, v130, v134\n"
      "v_cvt_pk_bf16_f32 v139, v131, v135\n"
      "v_cvt_pk_bf16_f32 v148, v140, v144\n"
      "v_cvt_pk_bf16_f32 v149, v141, v145\n"
      "v_mfma_f32_16x16x32_bf16 v[128:131], v[192:195], v[136:139], 0\n"
      "v_cvt_pk_bf16_f32 v150, v142, v146\n"
      "v_cvt_pk_bf16_f32 v151, v143, v147\n"
      "v_mfma_f32_16x16x32_bf16 v[132:135], a[20:23], v[96:99], 0\n"
      "v_cvt_pk_bf16_f32 v160, v152, v156\n"
      "v_cvt_pk_bf16_f32 v161, v153, v157\n"
      "v_mfma_f32_16x16x32_bf16 v[140:143], v[192:195], v[148:151], 0\n"
      "v_cvt_pk_bf16_f32 v162, v154, v158\n"
      "v_cvt_pk_bf16_f32 v163, v155, v159\n"
      "v_mfma_f32_16x16x32_bf16 v[144:147], a[20:23], v[98:101], 0\n"
      "v_cvt_pk_bf16_f32 v172, v164, v168\n"
      "v_cvt_pk_bf16_f32 v173, v165, v169\n"
      "v_mfma_f32_16x16x32_bf16 v[152:155], v[192:195], v[160:163], 0\n"
      "v_cvt_pk_bf16_f32 v174, v166, v170\n"
      "v_cvt_pk_bf16_f32 v175, v167, v171\n"
      "v_mfma_f32_16x16x32_bf16 v[156:159], a[24:27], v[102:105], 0\n"
      "v_cvt_pk_bf16_f32 v184, v176, v180\n"
      "v_cvt_pk_bf16_f32 v185, v177, v181\n"
      "v_mfma_f32_16x16x32_bf16 v[164:167], v[192:195], v[172:175], 0\n"
      "v_cvt_pk_bf16_f32 v186, v178, v182\n"
      "v_cvt_pk_bf16_f32 v187, v179, v183\n"
      "v_mfma_f32_16x16x32_bf16 v[168:171], a[24:27], v[104:107], 0\n"
      "v_cvt_pk_bf16_f32 v136, v128, v132\n"
      "v_cvt_pk_bf16_f32 v137, v129, v133\n"
      "v_mfma_f32_16x16x32_bf16 v[176:179], v[192:195], v[184:187], 0\n"
      "v_cvt_pk_bf16_f32 v138, v130, v134\n"
      "v_cvt_pk_bf16_f32 v139, v131, v135\n"
      "v_mfma_f32_16x16x32_bf16 v[180:183], a[26:29], v[108:111], 0\n"
      "v_cvt_pk_bf16_f32 v148, v140, v144\n"
      "v_cvt_pk_bf16_f32 v149, v141, v145\n"
      "v_mfma_f32_16x16x32_bf16 v[128:131], v[192:195], v[136:139], 0\n"
      "v_cvt_pk_bf16_f32 v150, v142, v146\n"
      "v_cvt_pk_bf16_f32 v151, v143, v147\n"
      "v_mfma_f32_16x16x32_bf16 v[132:135], a[30:33], v[112:115], 0\n"
      "v_cvt_pk_bf16_f32 v160, v152, v156\n"
      "v_cvt_pk_bf16_f32 v161, v153, v157\n"
      "v_mfma_f32_16x16x32_bf16 v[140:143], v[192:195], v[148:151], 0\n"
      "v_cvt_pk_bf16_f32 v162, v154, v158\n"
      "v_cvt_pk_bf16_f32 v163, v155, v159\n"
      "v_mfma_f32_16x16x32_bf16 v[144:147], a[30:33], v[114:117], 0\n"
      "v_cvt_pk_bf16_f32 v172, v164, v168\n"
      "v_cvt_pk_bf16_f32 v173, v165, v169\n"
      "v_mfma_f32_16x16x32_bf16 v[152:155], v[192:195], v[160:163], 0\n"
      "v_cvt_pk_bf16_f32 v174, v166, v170\n"
      "v_cvt_pk_bf16_f32 v175, v167, v171\n"
      "v_mfma_f32_16x16x32_bf16 v[156:159], a[34:37], v[118:121], 0\n"
      "v_cvt_pk_bf16_f32 v184, v176, v180\n"
      "v_cvt_pk_bf16_f32 v185, v177, v181\n"
      "v_mfma_f32_16x16x32_bf16 v[164:167], v[192:195], v[172:175], 0\n"
      "v_cvt_pk_bf16_f32 v186, v178, v182\n"
      "v_cvt_pk_bf16_f32 v187, v179, v183\n"
      "v_mfma_f32_16x16x32_bf16 v[168:171], a[34:37], v[120:123], 0\n"
      "v_cvt_pk_bf16_f32 v136, v128, v132\n"
      "v_cvt_pk_bf16_f32 v137, v129, v133\n"
      "v_mfma_f32_16x16x32_bf16 v[176:179], v[192:195], v[184:187], 0\n"
      "v_cvt_pk_bf16_f32 v138, v130, v134\n"
      "v_cvt_pk_bf16_f32 v139, v131, v135\n"
      "v_mfma_f32_16x16x32_bf16 v[180:183], a[36:39], v[124:127], 0\n"
      "v_cvt_pk_bf16_f32 v148, v140, v144\n"
      "v_cvt_pk_bf16_f32 v149, v141, v145\n"
      "v_mfma_f32_16x16x32_bf16 v[128:131], v[192:195], v[136:139], 0\n"
      "v_cvt_pk_bf16_f32 v150, v142, v146\n"
      "v_cvt_pk_bf16_f32 v151, v143, v147\n"
      "v_mfma_f32_16x16x32_bf16 v[132:135], a[40:43], v[32:35], 0\n"
      "v_cvt_pk_bf16_f32 v160, v152, v156\n"
      "v_cvt_pk_bf16_f32 v161, v153, v157\n"
      "v_mfma_f32_16x16x32_bf16 v[140:143], v[192:195], v[148:151], 0\n"
      "v_cvt_pk_bf16_f32 v162, v154, v158\n"
      "v_cvt_pk_bf16_f32 v163, v155, v159\n"
      "v_mfma_f32_16x16x32_bf16 v[144:147], a[40:43], v[34:37], 0\n"
      "v_cvt_pk_bf16_f32 v172, v164, v168\n"
      "v_cvt_pk_bf16_f32 v173, v165, v169\n"
      "v_mfma_f32_16x16x32_bf16 v[152:155], v[192:195], v[160:163], 0\n"
      "v_cvt_pk_bf16_f32 v174, v166, v170\n"
      "v_cvt_pk_bf16_f32 v175, v167, v171\n"
      "v_mfma_f32_16x16x32_bf16 v[156:159], a[44:47], v[32:35], 0\n"
      "s_add_u32 m0, s45, 120896\n"
      "s_nop 0\n"
      "global_load_lds_dwordx4 v210, s[16:17]\n"
      "v_cvt_pk_bf16_f32 v184, v176, v180\n"
      "v_cvt_pk_bf16_f32 v185, v177, v181\n"
      "v_mfma_f32_16x16x32_bf16 v[164:167], v[192:195], v[172:175], 0\n"
      "v_cvt_pk_bf16_f32 v186, v178, v182\n"
      "v_cvt_pk_bf16_f32 v187, v179, v183\n"
      "v_mfma_f32_16x16x32_bf16 v[168:171], a[44:47], v[34:37], 0\n"
      "global_load_lds_dwordx4 v39, s[16:17] offset:1024\n"
      "v_cvt_pk_bf16_f32 v128, 0, v128\n"
      "v_cvt_pk_bf16_f32 v129, 0, v129\n"
      "v_mfma_f32_16x16x32_bf16 v[176:179], v[192:195], v[184:187], 0\n"
      "v_cvt_pk_bf16_f32 v130, 0, v130\n"
      "v_cvt_pk_bf16_f32 v131, 0, v131\n"
      "v_mfma_f32_16x16x32_bf16 v[180:183], a[46:49], v[34:37], 0\n"
      "global_load_lds_dwordx4 v210, s[18:19] offset:2048\n"
      "v_mul_f32 v128, v128, v132\n"
      "v_mul_f32 v129, v129, v133\n"
      "v_mul_f32 v130, v130, v134\n"
      "v_mul_f32 v131, v131, v135\n"
      "v_cvt_pk_bf16_f32 v136, v131, v130\n"
      "v_cvt_pk_bf16_f32 v137, v129, v128\n"
      "v_cvt_pk_bf16_f32 v140, 0, v140\n"
      "v_cvt_pk_bf16_f32 v141, 0, v141\n"
      "v_cvt_pk_bf16_f32 v142, 0, v142\n"
      "v_cvt_pk_bf16_f32 v143, 0, v143\n"
      "global_load_lds_dwordx4 v39, s[18:19] offset:3072\n"
      "ds_write_b64 v200, v[136:137] offset:0\n"
      "v_mul_f32 v140, v140, v144\n"
      "v_mul_f32 v141, v141, v145\n"
      "v_mul_f32 v142, v142, v146\n"
      "v_mul_f32 v143, v143, v147\n"
      "v_cvt_pk_bf16_f32 v148, v143, v142\n"
      "v_cvt_pk_bf16_f32 v149, v141, v140\n"
      "v_cvt_pk_bf16_f32 v152, 0, v152\n"
      "v_cvt_pk_bf16_f32 v153, 0, v153\n"
      "v_cvt_pk_bf16_f32 v154, 0, v154\n"
      "v_cvt_pk_bf16_f32 v155, 0, v155\n"
      "ds_write_b64 v200, v[148:149] offset:2304\n"
      "v_mul_f32 v152, v152, v156\n"
      "v_mul_f32 v153, v153, v157\n"
      "v_mul_f32 v154, v154, v158\n"
      "v_mul_f32 v155, v155, v159\n"
      "v_cvt_pk_bf16_f32 v160, v155, v154\n"
      "v_cvt_pk_bf16_f32 v161, v153, v152\n"
      "v_cvt_pk_bf16_f32 v164, 0, v164\n"
      "v_cvt_pk_bf16_f32 v165, 0, v165\n"
      "v_cvt_pk_bf16_f32 v166, 0, v166\n"
      "v_cvt_pk_bf16_f32 v167, 0, v167\n"
      "ds_write_b64 v200, v[160:161] offset:4608\n"
      "v_mul_f32 v164, v164, v168\n"
      "v_mul_f32 v165, v165, v169\n"
      "v_mul_f32 v166, v166, v170\n"
      "v_mul_f32 v167, v167, v171\n"
      "v_cvt_pk_bf16_f32 v172, v167, v166\n"
      "v_cvt_pk_bf16_f32 v173, v165, v164\n"
      "v_cvt_pk_bf16_f32 v176, 0, v176\n"
      "v_cvt_pk_bf16_f32 v177, 0, v177\n"
      "v_cvt_pk_bf16_f32 v178, 0, v178\n"
      "v_cvt_pk_bf16_f32 v179, 0, v179\n"
      "ds_write_b64 v200, v[172:173] offset:6912\n"
      "v_mul_f32 v176, v176, v180\n"
      "v_mul_f32 v177, v177, v181\n"
      "v_mul_f32 v178, v178, v182\n"
      "v_mul_f32 v179, v179, v183\n"
      "v_cvt_pk_bf16_f32 v184, v179, v178\n"
      "v_cvt_pk_bf16_f32 v185, v177, v176\n"
      "ds_write_b64 v200, v[184:185] offset:9216\n"
      "s_waitcnt lgkmcnt(0)\n"
      "s_barrier\n"
      "ds_read_b128 v[64:67], v201 offset:0\n"
      "ds_read_b128 v[68:71], v201 offset:1152\n"
      "ds_read_b128 v[72:75], v201 offset:2304\n"
      "s_waitcnt lgkmcnt(2)\n"
      "buffer_store_dwordx4 v[64:67], v202, s[32:35], s60 offen offset:0 sc1\n"
      "v_add3_u32 v76, v64, v65, v66\n"
      "v_add_u32 v76, v76, v67\n"
      "s_waitcnt lgkmcnt(1)\n"
      "buffer_store_dwordx4 v[68:71], v202, s[32:35], s42 offen offset:0 sc1\n"
      "v_add3_u32 v77, v68, v69, v70\n"
      "v_add_u32 v77, v77, v71\n"
      "s_waitcnt lgkmcnt(0)\n"
      "s_mov_b32 exec_hi, 0\n"
      "buffer_store_dwordx4 v[72:75], v202, s[32:35], s43 offen offset:0 sc1\n"
      "s_mov_b32 exec_hi, -1\n"
      "v_add3_u32 v78, v72, v73, v74\n"
      "v_add_u32 v78, v78, v75\n"
      "v_add_u32_dpp v76, v76, v76 quad_perm:[1,0,3,2] row_mask:0xf bank_mask:0xf\n"
      "v_add_u32_dpp v77, v77, v77 quad_perm:[1,0,3,2] row_mask:0xf bank_mask:0xf\n"
      "v_add_u32_dpp v78, v78, v78 quad_perm:[1,0,3,2] row_mask:0xf bank_mask:0xf\n"
      "v_add_u32_dpp v76, v76, v76 quad_perm:[2,3,0,1] row_mask:0xf bank_mask:0xf\n"
      "v_add_u32_dpp v77, v77, v77 quad_perm:[2,3,0,1] row_mask:0xf bank_mask:0xf\n"
      "v_add_u32_dpp v78, v78, v78 quad_perm:[2,3,0,1] row_mask:0xf bank_mask:0xf\n"
      "v_add_u32_dpp v76, v76, v76 row_half_mirror row_mask:0xf bank_mask:0xf\n"
      "v_add_u32_dpp v77, v77, v77 row_half_mirror row_mask:0xf bank_mask:0xf\n"
      "v_add_u32_dpp v78, v78, v78 row_half_mirror row_mask:0xf bank_mask:0xf\n"
      "v_cndmask_b32 v76, v76, v77, s[48:49]\n"
      "v_cndmask_b32 v76, v76, v78, s[50:51]\n"
      "v_sub_u32 v76, s30, v76\n"
      "buffer_store_dword v76, v203, s[32:35], s61 offen offset:0 sc1\n"
      "s_waitcnt vmcnt(12)\n"
      "v_lshlrev_b32 v38, 16, v235\n"
      "v_mul_f32 v84, 0xbfb8aa3b, v38\n"
      "v_exp_f32 v84, v84\n"
      "s_nop 0\n"
      "v_add_f32 v84, 1.0, v84\n"
      "v_rcp_f32 v84, v84\n"
      "s_nop 0\n"
      "v_mul_f32 v38, v38, v84\n"
      "v_cvt_pk_bf16_f32 v38, v38, v38\n"
      "ds_write_b16 v204, v38\n"
      "s_waitcnt lgkmcnt(0)\n"
      "s_barrier\n"
      "ds_read_b128 v[128:131], v206 offset:0\n"
      "ds_read_b64_tr_b16 v[132:133], v205 offset:0\n"
      "ds_read_b64_tr_b16 v[134:135], v205 offset:2048\n"
      "ds_read_b128 v[136:139], v206 offset:64\n"
      "ds_read_b64_tr_b16 v[140:141], v205 offset:4096\n"
      "ds_read_b64_tr_b16 v[142:143], v205 offset:6144\n"
      "ds_read_b128 v[144:147], v206 offset:128\n"
      "ds_read_b64_tr_b16 v[148:149], v205 offset:8192\n"
      "ds_read_b64_tr_b16 v[150:151], v205 offset:10240\n"
      "ds_read_b128 v[152:155], v206 offset:192\n"
      "ds_read_b64_tr_b16 v[156:157], v205 offset:12288\n"
      "ds_read_b64_tr_b16 v[158:159], v205 offset:14336\n"
      "ds_read_b128 v[160:163], v206 offset:256\n"
      "ds_read_b64_tr_b16 v[164:165], v205 offset:16384\n"
      "ds_read_b64_tr_b16 v[166:167], v205 offset:18432\n"
      "s_waitcnt lgkmcnt(12)\n"
      "v_mfma_f32_16x16x32_bf16 v[168:171], v[128:131], v[132:135], 0\n"
      "s_waitcnt lgkmcnt(9)\n"
      "v_mfma_f32_16x16x32_bf16 v[168:171], v[136:139], v[140:143], v[168:171]\n"
      "s_waitcnt lgkmcnt(6)\n"
      "v_mfma_f32_16x16x32_bf16 v[168:171], v[144:147], v[148:151], v[168:171]\n"
      "s_waitcnt lgkmcnt(3)\n"
      "v_mfma_f32_16x16x32_bf16 v[168:171], v[152:155], v[156:159], v[168:171]\n"
      "s_waitcnt lgkmcnt(0)\n"
      "v_mfma_f32_16x16x32_bf16 v[168:171], v[160:163], v[164:167], v[168:171]\n"
      "s_nop 7\n"
      "v_mov_b32 v169, s30\n"
      "buffer_store_dwordx2 v[168:169], v207, s[32:35], s62 offen offset:0 sc1\n"
      : STATE, "=&{s[32:35]}"(s32), "=&{v[8:23]}"(v8), "=&{v[24:27]}"(v24), "=&{v[52:55]}"(v52), "+{v[192:195]}"(t[0]), "+{v[240:243]}"(t[1]), "+{v[196:199]}"(t[2]), "+{v[200:203]}"(t[3]), "+{v[204:207]}"(t[4]), "+{v[208:211]}"(t[5])
      : "{s2}"(blockIdx.x)
      : "memory", "scc", "vcc", "s40", "s41", "s42", "s43", "s44", "s45", "s46", "s47", "s48", "s49", "s50", "s51", "v2", "v3", "v4", "v5", "v6", "v7", "v28", "v29", "v30", "v31", "v32", "v33", "v34", "v35", "v36", "v37", "v38", "v39", "v40", "v41", "v42", "v43", "v44", "v45", "v46", "v47", "v48", "v49", "v50", "v51", "v56", "v57", "v58", "v59", "v60", "v61", "v62", "v63", "v64", "v65", "v66", "v67", "v68", "v69", "v70", "v71", "v72", "v73", "v74", "v75", "v76", "v77", "v78", "v79", "v80", "v81", "v82", "v83", "v84", "v85", "v86", "v87", "v88", "v89", "v90", "v91", "v92", "v93", "v94", "v95", "v96", "v97", "v98", "v99", "v100", "v101", "v102", "v103", "v104", "v105", "v106", "v107", "v108", "v109", "v110", "v111", "v112", "v113", "v114", "v115", "v116", "v117", "v118", "v119", "v120", "v121", "v122", "v123", "v124", "v125", "v126", "v127", "v128", "v129", "v130", "v131", "v132", "v133", "v134", "v135", "v136", "v137", "v138", "v139", "v140", "v141", "v142", "v143", "v144", "v145", "v146", "v147", "v148", "v149", "v150", "v151", "v152", "v153", "v154", "v155", "v156", "v157", "v158", "v159", "v160", "v161", "v162", "v163", "v164", "v165", "v166", "v167", "v168", "v169", "v170", "v171", "v172", "v173", "v174", "v175", "v176", "v177", "v178", "v179", "v180", "v181", "v182", "v183", "v184", "v185", "v186", "v187", "v188", "v189", "v190", "v191", "v212", "v213", "v214", "v215", "v216", "v217", "v218", "v219", "v220", "v221", "v222", "v223", "v224", "v225", "v226", "v227", "v228", "v229", "v230", "v231", "v236", "v237", "v238", "v239", "v244", "v245", "v246", "v247", "v248", "v249", "v250", "v251", "v252", "v253", "v254", "v255", "a0", "a1", "a2", "a3", "a4", "a5", "a6", "a7", "a8", "a9", "a10", "a11", "a12", "a13", "a14", "a15", "a16", "a17", "a18", "a19", "a20", "a21", "a22", "a23", "a24", "a25", "a26", "a27", "a28", "a29", "a30", "a31", "a32", "a33", "a34", "a35", "a36", "a37", "a38", "a39", "a40", "a41", "a42", "a43", "a44", "a45", "a46", "a47", "a48", "a49", "a50", "a51", "a52", "a53", "a54", "a55", "a56", "a57", "a58", "a59", "a60", "a61", "a62", "a63", "a64", "a65", "a66", "a67", "a68", "a69", "a70", "a71", "a72", "a73", "a74", "a75", "a76", "a77", "a78", "a79", "a80", "a81", "a82", "a83", "a84", "a85", "a86", "a87", "a88", "a89", "a90", "a91", "a92", "a93", "a94", "a95", "a96", "a97", "a98", "a99", "a100", "a101", "a102", "a103", "a104", "a105", "a106", "a107", "a108", "a109", "a110", "a111", "a112", "a113", "a114", "a115", "a116", "a117", "a118", "a119", "a120", "a121", "a122", "a123", "a124", "a125", "a126", "a127", "a128", "a129", "a130", "a131", "a132", "a133", "a134", "a135", "a136", "a137", "a138", "a139", "a140", "a141", "a142", "a143", "a144", "a145", "a146", "a147", "a148", "a149", "a150", "a151", "a152", "a153", "a154", "a155", "a156", "a157", "a158", "a159", "a160", "a161", "a162", "a163", "a164", "a165", "a166", "a167", "a168", "a169", "a170", "a171", "a172", "a173", "a174", "a175", "a176", "a177", "a178", "a179", "a180", "a181", "a182", "a183", "a184", "a185", "a186", "a187", "a188", "a189", "a190", "a191", "a192", "a193", "a194", "a195", "a196", "a197", "a198", "a199", "a200", "a201", "a202", "a203", "a204", "a205", "a206", "a207", "a208", "a209", "a210", "a211", "a212", "a213", "a214", "a215", "a216", "a217", "a218", "a219", "a220", "a221", "a222", "a223", "a224", "a225", "a226", "a227", "a228", "a229", "a230", "a231", "a232", "a233", "a234", "a235", "a236", "a237", "a238", "a239", "a240", "a241", "a242", "a243", "a244", "a245", "a246", "a247", "a248", "a249", "a250", "a251", "a252", "a253", "a254", "a255");
  {
    const int lane = tid & 63, ip = lane & 15, fq = lane >> 4, q4 = ip >> 2, p4 = lane & 3;
    const int fo = (ip & 3) * KCP + 2 * ((ip & 12) + 8 * (fq & 1) + 256 * (fq >> 1)), go = L_XI + (15 - ip + 8 * (fq >> 1)) * 96 + 16 * (fq & 1);
    const int exw = L_EX + ip * 128 + (fq ^ (ip & 7)) * 16, z7 = ((tid < 84 ? tid : 83) * 37) >> 8, rowA = 8 * tid;
    t[6][0] = (2 * w + (lane >> 5)) * 1280 + (lane & 31) * 8;
    t[6][1] = L_WR + (fq >> 1) * 4096 + (fq & 1) * 1024 + q4 * 256 + (((2 * w + 1) ^ q4) & 7) * 32 + p4 * 8;
    t[6][2] = (tid >> 5) * D_ * 16 + ((tid >> 3) & 3) * 4;
    t[6][3] = 48 - 16 * p4;
    t[7][0] = (ip < 8 ? ip : 8 + p4) * KCP + 56;
    t[7][1] = z7 * (KCP - 56) + (tid < 84 ? tid : 83) * 8;
    t[7][2] = L_XI + 64 * 96 + (tid < 90 ? tid : 89) * 16;
    t[7][3] = L_WR + (fq >> 1) * 4096 + (fq & 1) * 1024 + q4 * 256 + (((2 * w) ^ q4) & 7) * 32 + p4 * 8;
    t[8][0] = L_SC + w * 32 + (lane >> 2) * 2;
    t[8][1] = L_SC + fq * 8;
    t[8][2] = L_SC + 64 + (ip >> 1) * 4;
    t[8][3] = (fq & 1) * 12288 + ((fq >> 1) * 32 + ip) * 4;
    t[9][0] = L_QT + (8 * fq + q4) * 160 + p4 * 8 + w * 32;
    t[9][1] = L_QT + (8 * fq + q4) * 160 + p4 * 8 + 128 + (w & 1) * 5120;
    t[9][2] = (16 * w + ip) * 4096 + fq * 16;
    t[9][3] = w < 2 ? (64 + ip) * 4096 + fq * 16 + w * 64 : 0x80000000u;
    t[10][0] = ((fq & 1 ? w * 4 + 2048 : fq * 2 + 512 * w) + ip * 32) | ((unsigned)(fq & (fq >> 1 | w >> 1)) << 31);
    t[10][1] = lane == 0 ? w * 1024 : 0x80000000u;
    t[10][2] = L_XI + (tid >> 2) * 96 + (tid & 3) * 16;
    t[10][3] = ((lane >> 3) ^ (lane >> 4)) & 1;
    t[11][0] = fo + (w >> 1) * KCH + (w & 1) * 256;
    t[11][1] = go + (w >> 1) * XR_CH + (w & 1) * 384;
    t[11][2] = fo + 2 * KCH + w * 128;
    t[11][3] = go + 2 * XR_CH + w * 192;
    t[12][0] = exw + w * 8192;
    t[12][1] = L_EX + 32768 + w * 1024 + (lane >> 3) * 128 + ((lane & 7) ^ (lane >> 3)) * 16;
    t[12][2] = L_XI + ((2040 - rowA) >> 5) * 96 + ((2040 - rowA) & 31) * 2;
    t[12][3] = L_XI + 2 * XR_CH + ((252 - 4 * lane) >> 5) * 96 + ((252 - 4 * lane) & 31) * 2;
    t[13][0] = (lane < 20 ? lane : 19) * 16;
    t[13][1] = ip == 0 ? fq * 32 : 0x80000000u;
    t[13][2] = fq * D_ * 128 + q4 * 32 + p4 * 8;
    t[13][3] = lane < 8 ? lane * 7680 : 0x80000000u;
    t[14][0] = exw + 32768 + w * 4096;
    t[14][1] = (lane & 1) * 2048 + (lane >> 1) * 16;
    t[14][2] = ((lane & 12) == 4 ? 256 * 32 : 0) + (p4 < 3 ? p4 : 0) * 20480;
    t[14][3] = (exw + w * 8192) ^ 64;
    t[15][0] = (tid >> 5) * D_ * 32 + ((tid >> 2) & 7) * 4;
    t[15][1] = (lane >> 3) * 4;
    t[15][2] = (exw + 32768 + w * 4096) ^ 64;
    t[15][3] = (4 * fq + ((lane >> 2) & 1)) * (D_ * 2) + p4 * 16;
    t[16][0] = L_WD1 + w * 9216 + (4 * fq + q4) * 64 + p4 * 8;
    t[16][1] = (rowA >> 6) * D_ * 128 + ((lane >> 1) & 3) * 32 + (lane & 1) * 16;
    t[16][2] = (lane & 7) == 0 ? (rowA >> 6) * 32 : 0x80000000u;
    t[16][3] = L_EX + w * 2048 + (lane >> 2) * 128 + ((2 * p4) ^ ((lane >> 2) & 7)) * 16;
    t[17][0] = lane * 8;
    t[17][1] = lane & 4 ? 0x80000000u : (4 * w + (lane & 7)) * 1024 + (lane >> 3) * 4;
    t[17][2] = (4 * fq + q4) * 128 + ((w ^ (2 * fq + (q4 >> 1))) & 3) * 32 + p4 * 8;
    t[17][3] = lane * 16;
  }
  asm volatile(
      "v_mov_b32 v34, 0\n"
      "v_mov_b32 v35, 0\n"
      "v_mov_b32 v236, 0\n"
      "v_mov_b32 v237, 0\n"
      "v_mov_b32 v238, 0\n"
      "v_mov_b32 v239, 0\n"
      "s_mov_b32 s44, 0x40000\n"
      "s_mov_b32 s46, 0\n"
      "s_lshl_b32 s42, s3, 12\n"
      "s_add_u32 s42, s42, 147456\n"
      "v_add_u32 v55, s42, v7\n"
      "ds_write_b64 v217, v[236:237]\n"
      "ds_write_b128 v218, v[236:239] offset:0\n"
      "ds_write_b128 v218, v[236:239] offset:7680\n"
      "ds_write_b128 v218, v[236:239] offset:15360\n"
      "s_sub_u32 s43, s92, s93\n"
      "s_lshr_b32 s43, s43, 7\n"
      "v_and_b32 v39, s43, v209\n"
      "v_add_u32 v47, s94, v47\n"
      "v_add_u32 v58, v39, v58\n"
      "v_xor_b32 v50, s98, v50\n"
      "v_xor_b32 v29, s97, v29\n"
      "v_mad_u32_u24 v63, v231, s96, v63\n"
      "v_add_u32 v63, s95, v63\n"
      "v_add_u32 v5, s90, v5\n"
      "s_lshl_b32 s42, s99, 4\n"
      "s_lshr_b32 s58, s58, s42\n"
      "s_lshr_b32 s59, s59, s42\n"
      "s_lshr_b32 s42, s93, 8\n"
      "s_and_b32 s42, s42, 16\n"
      "s_lshr_b32 s25, s53, s42\n"
      "s_lshl_b32 s14, s58, 16\n"
      "s_lshl_b32 s15, s59, 16\n"
      "s_lshl_b32 s25, s25, 16\n"
      "s_mul_i32 s42, s3, 2560\n"
      "s_add_u32 m0, s42, 110656\n"
      "s_mov_b32 s36, s4\n"
      "s_and_b32 s37, s5, 0xffff\n"
      "buffer_load_dwordx4 v232, s[36:39], s52 offen lds\n"
      "s_add_u32 m0, m0, 1024\n"
      "s_nop 0\n"
      "buffer_load_dwordx4 v233, s[36:39], s55 offen lds\n"
      "s_add_u32 m0, m0, 1024\n"
      "s_mov_b32 exec_hi, 0\n"
      "buffer_load_dwordx4 v234, s[36:39], s55 offen lds\n"
      "s_mov_b32 exec_hi, -1\n"
      "buffer_load_dwordx4 v[112:115], v211, s[36:39], s66 offen\n"
      "buffer_load_dwordx4 v[116:119], v211, s[36:39], s66 offen offset:512\n"
      "buffer_load_dwordx2 v[120:121], v212, s[36:39], s66 offen offset:1024\n"
      "buffer_load_dwordx2 v[244:245], v4, s[32:35], s68 offen sc1\n"
      "v_mov_b32 v12, s6\n"
      "v_mov_b32 v13, s7\n"
      "v_mov_b32 v18, s8\n"
      "v_mov_b32 v19, s9\n"
      "v_mov_b32 v24, s10\n"
      "v_mov_b32 v25, s11\n"
      "s_waitcnt vmcnt(16)\n"
      "v_lshlrev_b64 v[26:27], v215, v[26:27]\n"
      "v_mov_b32_dpp v12, v8 wave_shl:1 row_mask:0xf bank_mask:0xf\n"
      "v_mov_b32_dpp v13, v9 wave_shl:1 row_mask:0xf bank_mask:0xf\n"
      "v_mov_b32_dpp v18, v14 wave_shl:1 row_mask:0xf bank_mask:0xf\n"
      "v_mov_b32_dpp v19, v15 wave_shl:1 row_mask:0xf bank_mask:0xf\n"
      "v_mov_b32_dpp v24, v20 wave_shl:1 row_mask:0xf bank_mask:0xf\n"
      "v_mov_b32_dpp v25, v21 wave_shl:1 row_mask:0xf bank_mask:0xf\n"
      "ds_write_b128 v54, v[10:13] offset:12544\n"
      "v_alignbit_b32 v88, v9, v8, 16\n"
      "v_alignbit_b32 v89, v10, v9, 16\n"
      "v_alignbit_b32 v90, v11, v10, 16\n"
      "v_alignbit_b32 v91, v12, v11, 16\n"
      "ds_write_b128 v54, v[88:91] offset:64\n"
      "v_alignbit_b32 v92, v10, v9, 16\n"
      "v_alignbit_b32 v93, v11, v10, 16\n"
      "v_alignbit_b32 v94, v12, v11, 16\n"
      "v_alignbit_b32 v95, v13, v12, 16\n"
      "ds_write_b128 v54, v[92:95] offset:8384\n"
      "v_mov_b32 v96, v9\n"
      "v_mov_b32 v97, v10\n"
      "v_mov_b32 v98, v11\n"
      "v_mov_b32 v99, v12\n"
      "ds_write_b128 v54, v[96:99] offset:4224\n"
      "ds_write_b128 v54, v[16:19] offset:29184\n"
      "v_alignbit_b32 v100, v15, v14, 16\n"
      "v_alignbit_b32 v101, v16, v15, 16\n"
      "v_alignbit_b32 v102, v17, v16, 16\n"
      "v_alignbit_b32 v103, v18, v17, 16\n"
      "ds_write_b128 v54, v[100:103] offset:16704\n"
      "v_alignbit_b32 v104, v16, v15, 16\n"
      "v_alignbit_b32 v105, v17, v16, 16\n"
      "v_alignbit_b32 v106, v18, v17, 16\n"
      "v_alignbit_b32 v107, v19, v18, 16\n"
      "ds_write_b128 v54, v[104:107] offset:25024\n"
      "v_mov_b32 v108, v15\n"
      "v_mov_b32 v109, v16\n"
      "v_mov_b32 v110, v17\n"
      "v_mov_b32 v111, v18\n"
      "ds_write_b128 v54, v[108:111] offset:20864\n"
      "ds_write_b128 v54, v[22:25] offset:45824\n"
      "v_alignbit_b32 v88, v21, v20, 16\n"
      "v_alignbit_b32 v89, v22, v21, 16\n"
      "v_alignbit_b32 v90, v23, v22, 16\n"
      "v_alignbit_b32 v91, v24, v23, 16\n"
      "ds_write_b128 v54, v[88:91] offset:33344\n"
      "v_alignbit_b32 v92, v22, v21, 16\n"
      "v_alignbit_b32 v93, v23, v22, 16\n"
      "v_alignbit_b32 v94, v24, v23, 16\n"
      "v_alignbit_b32 v95, v25, v24, 16\n"
      "ds_write_b128 v54, v[92:95] offset:41664\n"
      "v_mov_b32 v96, v21\n"
      "v_mov_b32 v97, v22\n"
      "v_mov_b32 v98, v23\n"
      "v_mov_b32 v99, v24\n"
      "ds_write_b128 v54, v[96:99] offset:37504\n"
      "ds_write_b64 v216, v[26:27]\n"
      "s_add_u32 s42, s92, 0x100000\n"
      "s_add_u32 s43, s93, 0x100000\n"
      "buffer_load_dwordx4 v[64:67], v54, s[32:35], s42 offen sc1\n"
      "s_add_u32 s42, s42, 0x100000\n"
      "buffer_load_dwordx4 v[72:75], v54, s[32:35], s43 offen sc1\n"
      "buffer_load_dwordx4 v[68:71], v54, s[32:35], s42 offen sc1\n"
      "buffer_load_dword v76, v214, s[32:35], s70 offen sc1\n"
      "s_add_u32 s42, s70, 4096\n"
      "buffer_load_dword v77, v214, s[32:35], s42 offen sc1\n"
      "buffer_load_dword v78, v214, s[32:35], s71 offen sc1\n"
      "s_lshr_b32 s42, s3, 1\n"
      "s_mul_i32 s42, s42, 0x3f800000\n"
      "s_waitcnt vmcnt(6)\n"
      "v_cmp_ne_u32 vcc, s30, v245\n"
      "s_cbranch_vccnz L_psm_%=\n"
      "L_pbm_%=:\n"
      "v_add_f32_dpp v244, v244, v244 quad_perm:[1,0,3,2] row_mask:0xf bank_mask:0xf\n"
      "s_nop 1\n"
      "v_add_f32_dpp v244, v244, v244 quad_perm:[2,3,0,1] row_mask:0xf bank_mask:0xf\n"
      "v_cvt_pk_bf16_f32 v244, 0, v244\n"
      "v_add_f32 v244, s42, v244\n"
      "v_cvt_pk_bf16_f32 v244, v244, v244\n"
      "ds_write_b16 v220, v244\n"
      "s_mov_b32 vcc_lo, 0x33333333\n"
      "s_mov_b32 vcc_hi, 0x33333333\n"
      "s_waitcnt lgkmcnt(0)\n"
      "s_barrier\n"
      "ds_read_b64_tr_b16 v[144:145], v219 offset:0\n"
      "ds_read_b64_tr_b16 v[146:147], v219 offset:8192\n"
      "ds_read_b64_tr_b16 v[148:149], v219 offset:2048\n"
      "ds_read_b64_tr_b16 v[150:151], v219 offset:10240\n"
      "ds_read_b64_tr_b16 v[152:153], v213 offset:0\n"
      "ds_read_b64_tr_b16 v[154:155], v213 offset:8192\n"
      "ds_read_b64_tr_b16 v[156:157], v213 offset:2048\n"
      "ds_read_b64_tr_b16 v[158:159], v213 offset:10240\n"
      "ds_read2_b64 v[248:251], v221 offset0:0 offset1:4\n"
      "ds_read2_b32 v[38:39], v222 offset0:0 offset1:8\n"
      "s_mov_b32 s42, 0xf0f0f0f0\n"
      "s_mov_b32 s43, s42\n"
      "v_mov_b32 v244, 0x3f803f80\n"
      "v_cndmask_b32 v244, v244, 0, s[42:43]\n"
      "v_mov_b32 v245, v244\n"
      "v_mov_b32 v246, v244\n"
      "v_mov_b32 v247, v244\n"
      "s_waitcnt lgkmcnt(2)\n"
      "v_cndmask_b32_dpp v128, v148, v144, vcc quad_perm:[0,1,0,1] row_mask:0xf bank_mask:0xf\n"
      "v_cndmask_b32_dpp v129, v149, v145, vcc quad_perm:[0,1,0,1] row_mask:0xf bank_mask:0xf\n"
      "v_cndmask_b32_dpp v130, v150, v146, vcc quad_perm:[0,1,0,1] row_mask:0xf bank_mask:0xf\n"
      "v_cndmask_b32_dpp v131, v151, v147, vcc quad_perm:[0,1,0,1] row_mask:0xf bank_mask:0xf\n"
      "v_cndmask_b32_dpp v136, v156, v152, vcc quad_perm:[0,1,0,1] row_mask:0xf bank_mask:0xf\n"
      "v_cndmask_b32_dpp v137, v157, v153, vcc quad_perm:[0,1,0,1] row_mask:0xf bank_mask:0xf\n"
      "v_cndmask_b32_dpp v138, v158, v154, vcc quad_perm:[0,1,0,1] row_mask:0xf bank_mask:0xf\n"
      "v_cndmask_b32_dpp v139, v159, v155, vcc quad_perm:[0,1,0,1] row_mask:0xf bank_mask:0xf\n"
      "s_not_b64 vcc, vcc\n"
      "v_cndmask_b32_dpp v132, v144, v148, vcc quad_perm:[2,3,2,3] row_mask:0xf bank_mask:0xf\n"
      "v_cndmask_b32_dpp v133, v145, v149, vcc quad_perm:[2,3,2,3] row_mask:0xf bank_mask:0xf\n"
      "v_cndmask_b32_dpp v134, v146, v150, vcc quad_perm:[2,3,2,3] row_mask:0xf bank_mask:0xf\n"
      "v_cndmask_b32_dpp v135, v147, v151, vcc quad_perm:[2,3,2,3] row_mask:0xf bank_mask:0xf\n"
      "v_cndmask_b32_dpp v140, v152, v156, vcc quad_perm:[2,3,2,3] row_mask:0xf bank_mask:0xf\n"
      "v_cndmask_b32_dpp v141, v153, v157, vcc quad_perm:[2,3,2,3] row_mask:0xf bank_mask:0xf\n"
      "v_cndmask_b32_dpp v142, v154, v158, vcc quad_perm:[2,3,2,3] row_mask:0xf bank_mask:0xf\n"
      "v_cndmask_b32_dpp v143, v155, v159, vcc quad_perm:[2,3,2,3] row_mask:0xf bank_mask:0xf\n"
      "s_waitcnt lgkmcnt(0)\n"
      "v_and_b32 v32, v38, v198\n"
      "v_and_b32 v33, v38, v199\n"
      "v_and_b32 v36, v39, v198\n"
      "v_and_b32 v37, v39, v199\n"
      "v_mfma_f32_16x16x32_bf16 v[144:147], v[32:35], v[128:131], 0\n"
      "v_cndmask_b32 v248, 0, v248, s[42:43]\n"
      "v_mfma_f32_16x16x32_bf16 v[148:151], v[34:37], v[128:131], 0\n"
      "v_mfma_f32_16x16x32_bf16 v[152:155], v[32:35], v[132:135], 0\n"
      "v_cndmask_b32 v249, 0, v249, s[42:43]\n"
      "v_mfma_f32_16x16x32_bf16 v[156:159], v[34:37], v[132:135], 0\n"
      "v_mfma_f32_16x16x32_bf16 v[160:163], v[32:35], v[136:139], 0\n"
      "v_cndmask_b32 v250, 0, v250, s[42:43]\n"
      "v_mfma_f32_16x16x32_bf16 v[164:167], v[34:37], v[136:139], 0\n"
      "v_mfma_f32_16x16x32_bf16 v[168:171], v[32:35], v[140:143], 0\n"
      "v_cndmask_b32 v251, 0, v251, s[42:43]\n"
      "v_mfma_f32_16x16x32_bf16 v[172:175], v[34:37], v[140:143], 0\n"
      "s_mov_b32 s42, 0\n"
      "s_mov_b32 s43, -1\n"
      "v_mfma_f32_16x16x32_bf16 v[176:179], v[248:251], v[128:131], 0\n"
      "v_cvt_pk_bf16_f32 v144, v144, v145\n"
      "v_cvt_pk_bf16_f32 v145, v146, v147\n"
      "v_cvt_pk_bf16_f32 v146, v148, v149\n"
      "v_cvt_pk_bf16_f32 v147, v150, v151\n"
      "v_mfma_f32_16x16x32_bf16 v[180:183], v[248:251], v[132:135], 0\n"
      "v_cvt_pk_bf16_f32 v152, v152, v153\n"
      "v_cvt_pk_bf16_f32 v153, v154, v155\n"
      "v_cvt_pk_bf16_f32 v154, v156, v157\n"
      "v_cvt_pk_bf16_f32 v155, v158, v159\n"
      "v_mfma_f32_16x16x32_bf16 v[184:187], v[248:251], v[136:139], 0\n"
      "v_cvt_pk_bf16_f32 v160, v160, v161\n"
      "v_cvt_pk_bf16_f32 v161, v162, v163\n"
      "v_cvt_pk_bf16_f32 v162, v164, v165\n"
      "v_cvt_pk_bf16_f32 v163, v166, v167\n"
      "v_mfma_f32_16x16x32_bf16 v[188:191], v[248:251], v[140:143], 0\n"
      "v_cvt_pk_bf16_f32 v168, v168, v169\n"
      "v_cvt_pk_bf16_f32 v169, v170, v171\n"
      "v_cvt_pk_bf16_f32 v170, v172, v173\n"
      "v_cvt_pk_bf16_f32 v171, v174, v175\n"
      "v_mfma_f32_16x16x32_bf16 v[176:179], v[244:247], v[144:147], v[176:179]\n"
      "v_mfma_f32_16x16x32_bf16 v[180:183], v[244:247], v[152:155], v[180:183]\n"
      "v_mfma_f32_16x16x32_bf16 v[184:187], v[244:247], v[160:163], v[184:187]\n"
      "v_mfma_f32_16x16x32_bf16 v[188:191], v[244:247], v[168:171], v[188:191]\n"
      "buffer_store_dwordx4 v[144:147], v7, s[32:35], s72 offen offset:0 sc1\n"
      "buffer_store_dwordx4 v[152:155], v7, s[32:35], s73 offen offset:0 sc1\n"
      "buffer_store_dwordx4 v[160:163], v7, s[32:35], s74 offen offset:0 sc1\n"
      "buffer_store_dwordx4 v[168:171], v7, s[32:35], s75 offen offset:0 sc1\n"
      "s_mov_b32 s42, 0\n"
      "s_mov_b32 s43, -1\n"
      "s_nop 0\n"
      "v_cndmask_b32 v176, v176, v184, s[42:43]\n"
      "v_cndmask_b32 v180, v180, v188, s[42:43]\n"
      "buffer_store_dword v176, v223, s[32:35], s69 offen offset:0 sc1\n"
      "buffer_store_dword v180, v223, s[32:35], s69 offen offset:64 sc1\n"
      "s_barrier\n"
      "ds_read_b64_tr_b16 v[96:97], v224 offset:0\n"
      "ds_read_b64_tr_b16 v[98:99], v224 offset:640\n"
      "ds_read_b64_tr_b16 v[100:101], v224 offset:5120\n"
      "ds_read_b64_tr_b16 v[102:103], v224 offset:5760\n"
      "ds_read_b64_tr_b16 v[104:105], v225 offset:0\n"
      "ds_read_b64_tr_b16 v[106:107], v225 offset:640\n"
      "v_mov_b32 v122, 0\n"
      "v_mov_b32 v123, 0\n"
      "v_mov_b32 v124, 0x3f803f80\n"
      "v_dot2c_f32_bf16 v122, v112, v124\n"
      "v_dot2c_f32_bf16 v123, v112, v112\n"
      "v_dot2c_f32_bf16 v122, v113, v124\n"
      "v_dot2c_f32_bf16 v123, v113, v113\n"
      "v_dot2c_f32_bf16 v122, v114, v124\n"
      "v_dot2c_f32_bf16 v123, v114, v114\n"
      "v_dot2c_f32_bf16 v122, v115, v124\n"
      "v_dot2c_f32_bf16 v123, v115, v115\n"
      "v_dot2c_f32_bf16 v122, v116, v124\n"
      "v_dot2c_f32_bf16 v123, v116, v116\n"
      "v_dot2c_f32_bf16 v122, v117, v124\n"
      "v_dot2c_f32_bf16 v123, v117, v117\n"
      "v_dot2c_f32_bf16 v122, v118, v124\n"
      "v_dot2c_f32_bf16 v123, v118, v118\n"
      "v_dot2c_f32_bf16 v122, v119, v124\n"
      "v_dot2c_f32_bf16 v123, v119, v119\n"
      "v_dot2c_f32_bf16 v122, v120, v124\n"
      "v_dot2c_f32_bf16 v123, v120, v120\n"
      "v_dot2c_f32_bf16 v122, v121, v124\n"
      "v_dot2c_f32_bf16 v123, v121, v121\n"
      "s_waitcnt lgkmcnt(0)\n"
      "v_add3_u32 v108, v96, v97, v98\n"
      "v_add_u32 v108, v108, v99\n"
      "v_add3_u32 v109, v100, v101, v102\n"
      "v_add_f32_dpp v122, v122, v122 quad_perm:[1,0,3,2] row_mask:0xf bank_mask:0xf\n"
      "v_add_u32 v109, v109, v103\n"
      "v_add_f32_dpp v123, v123, v123 quad_perm:[1,0,3,2] row_mask:0xf bank_mask:0xf\n"
      "v_add3_u32 v110, v104, v105, v106\n"
      "v_add_f32_dpp v122, v122, v122 quad_perm:[2,3,0,1] row_mask:0xf bank_mask:0xf\n"
      "v_add_u32 v110, v110, v107\n"
      "v_add_f32_dpp v123, v123, v123 quad_perm:[2,3,0,1] row_mask:0xf bank_mask:0xf\n"
      "v_mov_b32 v111, v110\n"
      "v_add_f32_dpp v122, v122, v122 row_half_mirror row_mask:0xf bank_mask:0xf\n"
      "v_permlane32_swap_b32 v108, v109\n"
      "v_add_f32_dpp v123, v123, v123 row_half_mirror row_mask:0xf bank_mask:0xf\n"
      "v_permlane32_swap_b32 v110, v111\n"
      "v_add_f32_dpp v122, v122, v122 row_mirror row_mask:0xf bank_mask:0xf\n"
      "v_add_u32 v108, v108, v109\n"
      "v_add_f32_dpp v123, v123, v123 row_mirror row_mask:0xf bank_mask:0xf\n"
      "v_add_u32 v110, v110, v111\n"
      "v_mov_b32 v124, v122\n"
      "s_nop 0\n"
      "v_permlane16_swap_b32 v108, v110\n"
      "v_mov_b32 v125, v123\n"
      "v_add_u32 v108, v108, v110\n"
      "v_permlane16_swap_b32 v122, v124\n"
      "v_sub_u32 v108, s30, v108\n"
      "v_permlane16_swap_b32 v123, v125\n"
      "v_add_f32 v122, v122, v124\n"
      "v_add_f32 v123, v123, v125\n"
      "v_mul_f32 v122, 0x3acccccd, v122\n"
      "v_mul_f32 v123, 0x3acccccd, v123\n"
      "v_fma_f32 v123, -v122, v122, v123\n"
      "v_max_f32 v123, 0, v123\n"
      "v_cvt_pk_bf16_f32 v123, 0, v123\n"
      "v_add_f32 v123, 0x3727c5ac, v123\n"
      "v_cvt_pk_bf16_f32 v123, 0, v123\n"
      "v_sqrt_f32 v123, v123\n"
      "v_readlane_b32 s42, v122, 32\n"
      "v_cvt_pk_bf16_f32 v123, 0, v123\n"
      "v_rcp_f32 v124, v123\n"
      "v_mov_b32 v127, s30\n"
      "v_cvt_pk_bf16_f32 v126, v122, s42\n"
      "v_readlane_b32 s42, v124, 32\n"
      "s_nop 1\n"
      "v_mov_b32 v125, s42\n"
      "s_waitcnt vmcnt(6)\n"
      "v_add3_u32 v79, v64, v65, v66\n"
      "v_add3_u32 v79, v79, v67, v68\n"
      "v_add3_u32 v79, v79, v69, v70\n"
      "v_add3_u32 v79, v79, v71, v72\n"
      "v_add3_u32 v79, v79, v73, v74\n"
      "v_add_u32 v79, v79, v75\n"
      "v_add3_u32 v76, v76, v77, v78\n"
      "s_mul_i32 s47, s30, 3\n"
      "v_add_u32_dpp v79, v79, v79 quad_perm:[1,0,3,2] row_mask:0xf bank_mask:0xf\n"
      "s_nop 1\n"
      "v_add_u32_dpp v79, v79, v79 quad_perm:[2,3,0,1] row_mask:0xf bank_mask:0xf\n"
      "s_nop 1\n"
      "v_add_u32_dpp v79, v79, v79 row_half_mirror row_mask:0xf bank_mask:0xf\n"
      "v_add_u32 v79, v79, v76\n"
      "v_cmp_ne_u32 vcc, s47, v79\n"
      "s_cbranch_vccnz L_psx_%=\n"
      "L_pbx_%=:\n"
      "ds_write_b128 v230, v[64:67] offset:0\n"
      "ds_write_b128 v230, v[68:71] offset:7680\n"
      "ds_write_b128 v230, v[72:75] offset:15360\n"
      "buffer_store_dwordx4 v[96:99], v226, s[32:35], s64 offen offset:0 sc1\n"
      "buffer_store_dwordx4 v[100:103], v226, s[32:35], s64 offen offset:64 sc1\n"
      "buffer_store_dwordx4 v[104:107], v227, s[32:35], s64 offen offset:0 sc1\n"
      "buffer_store_dword v108, v228, s[32:35], s65 offen offset:0 sc1\n"
      "buffer_store_dwordx4 v[124:127], v229, s[32:35], s67 offen offset:0 sc1\n"
      "s_mul_i32 s42, s3, 9216\n"
      "s_add_u32 m0, s42, 122880\n"
      "s_add_u32 s53, s92, 0x400000\n"
      "s_add_u32 s54, s92, 0x500000\n"
      "s_add_u32 s55, s84, 8192\n"
      "s_waitcnt lgkmcnt(0)\n"
      "s_barrier\n"
      "global_load_lds_dwordx4 v63, s[20:21]\n"
      "global_load_lds_dwordx4 v63, s[22:23] offset:1024\n"
      "s_cmp_lg_u32 s99, 0\n"
      "s_cbranch_scc1 L_cv_b%=\n"
      "ds_read_b128 v[160:163], v43 offset:4608\n"
      "ds_read_b64 v[112:113], v42 offset:0\n"
      "ds_read_b64 v[114:115], v42 offset:8\n"
      "ds_read_b64 v[148:149], v42 offset:3072\n"
      "ds_read_b64 v[150:151], v42 offset:3080\n"
      "ds_read_b128 v[172:175], v43 offset:0\n"
      "ds_read_b128 v[168:171], v43 offset:1536\n"
      "ds_read_b64 v[124:125], v42 offset:1024\n"
      "ds_read_b64 v[126:127], v42 offset:1032\n"
      "ds_read_b128 v[164:167], v43 offset:3072\n"
      "ds_read_b64 v[136:137], v42 offset:2048\n"
      "ds_read_b64 v[138:139], v42 offset:2056\n"
      "ds_read_b64 v[116:117], v42 offset:32\n"
      "ds_read_b64 v[118:119], v42 offset:40\n"
      "ds_read_b64 v[128:129], v42 offset:1056\n"
      "ds_read_b64 v[130:131], v42 offset:1064\n"
      "ds_read_b64 v[140:141], v42 offset:2080\n"
      "ds_read_b64 v[142:143], v42 offset:2088\n"
      "ds_read_b64 v[152:153], v42 offset:3104\n"
      "ds_read_b64 v[154:155], v42 offset:3112\n"
      "s_waitcnt lgkmcnt(8)\n"
      "v_mfma_f32_16x16x32_bf16 v[96:99], v[112:115], v[160:163], 0\n"
      "ds_read_b128 v[176:179], v43 offset:4640\n"
      "ds_read_b128 v[180:183], v43 offset:3104\n"
      "v_mfma_f32_16x16x32_bf16 v[104:107], v[148:151], v[160:163], 0\n"
      "ds_read_b128 v[184:187], v43 offset:1568\n"
      "v_mfma_f32_16x16x32_bf16 v[104:107], v[112:115], v[172:175], v[104:107]\n"
      "ds_read_b128 v[188:191], v43 offset:32\n"
      "v_mfma_f32_16x16x32_bf16 v[104:107], v[124:127], v[168:171], v[104:107]\n"
      "ds_read_b64 v[120:121], v42 offset:64\n"
      "ds_read_b64 v[122:123], v42 offset:72\n"
      "v_mfma_f32_16x16x32_bf16 v[104:107], v[136:139], v[164:167], v[104:107]\n"
      "ds_read_b64 v[132:133], v42 offset:1088\n"
      "s_waitcnt lgkmcnt(7)\n"
      "v_mfma_f32_16x16x32_bf16 v[100:103], v[116:119], v[160:163], 0\n"
      "ds_read_b64 v[134:135], v42 offset:1096\n"
      "v_mfma_f32_16x16x32_bf16 v[108:111], v[152:155], v[160:163], 0\n"
      "ds_read_b64 v[144:145], v42 offset:2112\n"
      "v_mfma_f32_16x16x32_bf16 v[108:111], v[116:119], v[172:175], v[108:111]\n"
      "ds_read_b64 v[146:147], v42 offset:2120\n"
      "v_mfma_f32_16x16x32_bf16 v[108:111], v[128:131], v[168:171], v[108:111]\n"
      "ds_read_b64 v[156:157], v42 offset:3136\n"
      "v_mfma_f32_16x16x32_bf16 v[108:111], v[140:143], v[164:167], v[108:111]\n"
      "ds_read_b64 v[158:159], v42 offset:3144\n"
      "s_waitcnt lgkmcnt(8)\n"
      "v_mfma_f32_16x16x32_bf16 v[96:99], v[116:119], v[176:179], v[96:99]\n"
      "ds_read_b128 v[160:163], v43 offset:4704\n"
      "ds_read_b128 v[164:167], v43 offset:3168\n"
      "v_mfma_f32_16x16x32_bf16 v[104:107], v[152:155], v[176:179], v[104:107]\n"
      "ds_read_b128 v[168:171], v43 offset:1632\n"
      "v_mfma_f32_16x16x32_bf16 v[104:107], v[116:119], v[188:191], v[104:107]\n"
      "ds_read_b128 v[172:175], v43 offset:96\n"
      "v_mfma_f32_16x16x32_bf16 v[104:107], v[128:131], v[184:187], v[104:107]\n"
      "ds_read_b64 v[112:113], v42 offset:96\n"
      "ds_read_b64 v[114:115], v42 offset:104\n"
      "v_mfma_f32_16x16x32_bf16 v[104:107], v[140:143], v[180:183], v[104:107]\n"
      "ds_read_b64 v[124:125], v42 offset:1120\n"
      "s_waitcnt lgkmcnt(7)\n"
      "v_mfma_f32_16x16x32_bf16 v[100:103], v[120:123], v[176:179], v[100:103]\n"
      "ds_read_b64 v[126:127], v42 offset:1128\n"
      "v_mfma_f32_16x16x32_bf16 v[108:111], v[156:159], v[176:179], v[108:111]\n"
      "ds_read_b64 v[136:137], v42 offset:2144\n"
      "v_mfma_f32_16x16x32_bf16 v[108:111], v[120:123], v[188:191], v[108:111]\n"
      "ds_read_b64 v[138:139], v42 offset:2152\n"
      "v_mfma_f32_16x16x32_bf16 v[108:111], v[132:135], v[184:187], v[108:111]\n"
      "ds_read_b64 v[148:149], v42 offset:3168\n"
      "v_mfma_f32_16x16x32_bf16 v[108:111], v[144:147], v[180:183], v[108:111]\n"
      "ds_read_b64 v[150:151], v42 offset:3176\n"
      "s_waitcnt lgkmcnt(8)\n"
      "v_mfma_f32_16x16x32_bf16 v[96:99], v[120:123], v[160:163], v[96:99]\n"
      "ds_read_b128 v[176:179], v43 offset:4736\n"
      "ds_read_b128 v[180:183], v43 offset:3200\n"
      "v_mfma_f32_16x16x32_bf16 v[104:107], v[156:159], v[160:163], v[104:107]\n"
      "ds_read_b128 v[184:187], v43 offset:1664\n"
      "v_mfma_f32_16x16x32_bf16 v[104:107], v[120:123], v[172:175], v[104:107]\n"
      "ds_read_b128 v[188:191], v43 offset:128\n"
      "v_mfma_f32_16x16x32_bf16 v[104:107], v[132:135], v[168:171], v[104:107]\n"
      "ds_read_b64 v[116:117], v42 offset:128\n"
      "ds_read_b64 v[118:119], v42 offset:136\n"
      "v_mfma_f32_16x16x32_bf16 v[104:107], v[144:147], v[164:167], v[104:107]\n"
      "ds_read_b64 v[128:129], v42 offset:1152\n"
      "s_waitcnt lgkmcnt(7)\n"
      "v_mfma_f32_16x16x32_bf16 v[100:103], v[112:115], v[160:163], v[100:103]\n"
      "ds_read_b64 v[130:131], v42 offset:1160\n"
      "v_mfma_f32_16x16x32_bf16 v[108:111], v[148:151], v[160:163], v[108:111]\n"
      "ds_read_b64 v[140:141], v42 offset:2176\n"
      "v_mfma_f32_16x16x32_bf16 v[108:111], v[112:115], v[172:175], v[108:111]\n"
      "ds_read_b64 v[142:143], v42 offset:2184\n"
      "v_mfma_f32_16x16x32_bf16 v[108:111], v[124:127], v[168:171], v[108:111]\n"
      "ds_read_b64 v[152:153], v42 offset:3200\n"
      "v_mfma_f32_16x16x32_bf16 v[108:111], v[136:139], v[164:167], v[108:111]\n"
      "ds_read_b64 v[154:155], v42 offset:3208\n"
      "s_waitcnt lgkmcnt(8)\n"
      "v_mfma_f32_16x16x32_bf16 v[96:99], v[112:115], v[176:179], v[96:99]\n"
      "ds_read_b128 v[160:163], v41 offset:4608\n"
      "ds_read_b64 v[120:121], v40 offset:0\n"
      "v_mfma_f32_16x16x32_bf16 v[104:107], v[148:151], v[176:179], v[104:107]\n"
      "ds_read_b64 v[122:123], v40 offset:8\n"
      "ds_read_b128 v[164:167], v41 offset:3072\n"
      "v_mfma_f32_16x16x32_bf16 v[104:107], v[112:115], v[188:191], v[104:107]\n"
      "ds_read_b64 v[132:133], v40 offset:1024\n"
      "ds_read_b64 v[134:135], v40 offset:1032\n"
      "v_mfma_f32_16x16x32_bf16 v[104:107], v[124:127], v[184:187], v[104:107]\n"
      "ds_read_b128 v[168:171], v41 offset:1536\n"
      "ds_read_b64 v[144:145], v40 offset:2048\n"
      "v_mfma_f32_16x16x32_bf16 v[104:107], v[136:139], v[180:183], v[104:107]\n"
      "ds_read_b64 v[146:147], v40 offset:2056\n"
      "ds_read_b128 v[172:175], v41 offset:0\n"
      "s_waitcnt lgkmcnt(10)\n"
      "v_mfma_f32_16x16x32_bf16 v[100:103], v[116:119], v[176:179], v[100:103]\n"
      "ds_read_b64 v[156:157], v40 offset:3072\n"
      "v_mfma_f32_16x16x32_bf16 v[108:111], v[152:155], v[176:179], v[108:111]\n"
      "ds_read_b64 v[158:159], v40 offset:3080\n"
      "v_mfma_f32_16x16x32_bf16 v[108:111], v[116:119], v[188:191], v[108:111]\n"
      "v_mfma_f32_16x16x32_bf16 v[108:111], v[128:131], v[184:187], v[108:111]\n"
      "v_mfma_f32_16x16x32_bf16 v[108:111], v[140:143], v[180:183], v[108:111]\n"
      "s_waitcnt lgkmcnt(0)\n"
      "s_branch L_cv_e%=\n"
      "L_cv_b%=:\n"
      "ds_read_b128 v[164:167], v43 offset:3072\n"
      "ds_read_b64 v[112:113], v42 offset:0\n"
      "ds_read_b64 v[114:115], v42 offset:8\n"
      "ds_read_b64 v[124:125], v42 offset:1024\n"
      "ds_read_b64 v[126:127], v42 offset:1032\n"
      "ds_read_b128 v[160:163], v43 offset:4608\n"
      "ds_read_b64 v[136:137], v42 offset:2048\n"
      "ds_read_b64 v[138:139], v42 offset:2056\n"
      "ds_read_b128 v[168:171], v43 offset:1536\n"
      "ds_read_b64 v[116:117], v42 offset:32\n"
      "ds_read_b64 v[118:119], v42 offset:40\n"
      "ds_read_b64 v[128:129], v42 offset:1056\n"
      "ds_read_b64 v[130:131], v42 offset:1064\n"
      "ds_read_b64 v[140:141], v42 offset:2080\n"
      "ds_read_b64 v[142:143], v42 offset:2088\n"
      "s_waitcnt lgkmcnt(6)\n"
      "v_mfma_f32_16x16x32_bf16 v[96:99], v[112:115], v[164:167], 0\n"
      "ds_read_b128 v[176:179], v43 offset:4640\n"
      "v_mfma_f32_16x16x32_bf16 v[104:107], v[124:127], v[164:167], 0\n"
      "ds_read_b128 v[180:183], v43 offset:3104\n"
      "v_mfma_f32_16x16x32_bf16 v[96:99], v[124:127], v[160:163], v[96:99]\n"
      "ds_read_b128 v[184:187], v43 offset:1568\n"
      "v_mfma_f32_16x16x32_bf16 v[104:107], v[136:139], v[160:163], v[104:107]\n"
      "ds_read_b64 v[120:121], v42 offset:64\n"
      "v_mfma_f32_16x16x32_bf16 v[104:107], v[112:115], v[168:171], v[104:107]\n"
      "ds_read_b64 v[122:123], v42 offset:72\n"
      "s_waitcnt lgkmcnt(5)\n"
      "v_mfma_f32_16x16x32_bf16 v[100:103], v[116:119], v[164:167], 0\n"
      "ds_read_b64 v[132:133], v42 offset:1088\n"
      "v_mfma_f32_16x16x32_bf16 v[108:111], v[128:131], v[164:167], 0\n"
      "ds_read_b64 v[134:135], v42 offset:1096\n"
      "v_mfma_f32_16x16x32_bf16 v[100:103], v[128:131], v[160:163], v[100:103]\n"
      "ds_read_b64 v[144:145], v42 offset:2112\n"
      "v_mfma_f32_16x16x32_bf16 v[108:111], v[140:143], v[160:163], v[108:111]\n"
      "ds_read_b64 v[146:147], v42 offset:2120\n"
      "v_mfma_f32_16x16x32_bf16 v[108:111], v[116:119], v[168:171], v[108:111]\n"
      "s_waitcnt lgkmcnt(6)\n"
      "v_mfma_f32_16x16x32_bf16 v[96:99], v[116:119], v[180:183], v[96:99]\n"
      "ds_read_b128 v[160:163], v43 offset:4704\n"
      "v_mfma_f32_16x16x32_bf16 v[104:107], v[128:131], v[180:183], v[104:107]\n"
      "ds_read_b128 v[164:167], v43 offset:3168\n"
      "v_mfma_f32_16x16x32_bf16 v[96:99], v[128:131], v[176:179], v[96:99]\n"
      "ds_read_b128 v[168:171], v43 offset:1632\n"
      "v_mfma_f32_16x16x32_bf16 v[104:107], v[140:143], v[176:179], v[104:107]\n"
      "ds_read_b64 v[112:113], v42 offset:96\n"
      "v_mfma_f32_16x16x32_bf16 v[104:107], v[116:119], v[184:187], v[104:107]\n"
      "ds_read_b64 v[114:115], v42 offset:104\n"
      "s_waitcnt lgkmcnt(5)\n"
      "v_mfma_f32_16x16x32_bf16 v[100:103], v[120:123], v[180:183], v[100:103]\n"
      "ds_read_b64 v[124:125], v42 offset:1120\n"
      "v_mfma_f32_16x16x32_bf16 v[108:111], v[132:135], v[180:183], v[108:111]\n"
      "ds_read_b64 v[126:127], v42 offset:1128\n"
      "v_mfma_f32_16x16x32_bf16 v[100:103], v[132:135], v[176:179], v[100:103]\n"
      "ds_read_b64 v[136:137], v42 offset:2144\n"
      "v_mfma_f32_16x16x32_bf16 v[108:111], v[144:147], v[176:179], v[108:111]\n"
      "ds_read_b64 v[138:139], v42 offset:2152\n"
      "v_mfma_f32_16x16x32_bf16 v[108:111], v[120:123], v[184:187], v[108:111]\n"
      "s_waitcnt lgkmcnt(6)\n"
      "v_mfma_f32_16x16x32_bf16 v[96:99], v[120:123], v[164:167], v[96:99]\n"
      "ds_read_b128 v[176:179], v43 offset:4736\n"
      "v_mfma_f32_16x16x32_bf16 v[104:107], v[132:135], v[164:167], v[104:107]\n"
      "ds_read_b128 v[180:183], v43 offset:3200\n"
      "v_mfma_f32_16x16x32_bf16 v[96:99], v[132:135], v[160:163], v[96:99]\n"
      "ds_read_b128 v[184:187], v43 offset:1664\n"
      "v_mfma_f32_16x16x32_bf16 v[104:107], v[144:147], v[160:163], v[104:107]\n"
      "ds_read_b64 v[116:117], v42 offset:128\n"
      "v_mfma_f32_16x16x32_bf16 v[104:107], v[120:123], v[168:171], v[104:107]\n"
      "ds_read_b64 v[118:119], v42 offset:136\n"
      "s_waitcnt lgkmcnt(5)\n"
      "v_mfma_f32_16x16x32_bf16 v[100:103], v[112:115], v[164:167], v[100:103]\n"
      "ds_read_b64 v[128:129], v42 offset:1152\n"
      "v_mfma_f32_16x16x32_bf16 v[108:111], v[124:127], v[164:167], v[108:111]\n"
      "ds_read_b64 v[130:131], v42 offset:1160\n"
      "v_mfma_f32_16x16x32_bf16 v[100:103], v[124:127], v[160:163], v[100:103]\n"
      "ds_read_b64 v[140:141], v42 offset:2176\n"
      "v_mfma_f32_16x16x32_bf16 v[108:111], v[136:139], v[160:163], v[108:111]\n"
      "ds_read_b64 v[142:143], v42 offset:2184\n"
      "v_mfma_f32_16x16x32_bf16 v[108:111], v[112:115], v[168:171], v[108:111]\n"
      "s_waitcnt lgkmcnt(6)\n"
      "v_mfma_f32_16x16x32_bf16 v[96:99], v[112:115], v[180:183], v[96:99]\n"
      "ds_read_b128 v[160:163], v41 offset:4608\n"
      "ds_read_b64 v[120:121], v40 offset:0\n"
      "v_mfma_f32_16x16x32_bf16 v[104:107], v[124:127], v[180:183], v[104:107]\n"
      "ds_read_b64 v[122:123], v40 offset:8\n"
      "ds_read_b128 v[164:167], v41 offset:3072\n"
      "v_mfma_f32_16x16x32_bf16 v[96:99], v[124:127], v[176:179], v[96:99]\n"
      "ds_read_b64 v[132:133], v40 offset:1024\n"
      "ds_read_b64 v[134:135], v40 offset:1032\n"
      "v_mfma_f32_16x16x32_bf16 v[104:107], v[136:139], v[176:179], v[104:107]\n"
      "ds_read_b128 v[168:171], v41 offset:1536\n"
      "ds_read_b64 v[144:145], v40 offset:2048\n"
      "v_mfma_f32_16x16x32_bf16 v[104:107], v[112:115], v[184:187], v[104:107]\n"
      "ds_read_b64 v[146:147], v40 offset:2056\n"
      "ds_read_b128 v[172:175], v41 offset:0\n"
      "s_waitcnt lgkmcnt(10)\n"
      "v_mfma_f32_16x16x32_bf16 v[100:103], v[116:119], v[180:183], v[100:103]\n"
      "ds_read_b64 v[156:157], v40 offset:3072\n"
      "v_mfma_f32_16x16x32_bf16 v[108:111], v[128:131], v[180:183], v[108:111]\n"
      "ds_read_b64 v[158:159], v40 offset:3080\n"
      "v_mfma_f32_16x16x32_bf16 v[100:103], v[128:131], v[176:179], v[100:103]\n"
      "v_mfma_f32_16x16x32_bf16 v[108:111], v[140:143], v[176:179], v[108:111]\n"
      "v_mfma_f32_16x16x32_bf16 v[108:111], v[116:119], v[184:187], v[108:111]\n"
      "s_waitcnt lgkmcnt(0)\n"
      "L_cv_e%=:\n"
      "ds_read_b64 v[112:113], v40 offset:32\n"
      "ds_read_b64 v[114:115], v40 offset:40\n"
      "v_mfma_f32_16x16x32_bf16 v[64:67], v[120:123], v[160:163], 0\n"
      "ds_read_b64 v[124:125], v40 offset:1056\n"
      "v_mfma_f32_16x16x32_bf16 v[72:75], v[120:123], v[164:167], 0\n"
      "ds_read_b64 v[126:127], v40 offset:1064\n"
      "ds_read_b64 v[136:137], v40 offset:2080\n"
      "v_mfma_f32_16x16x32_bf16 v[80:83], v[120:123], v[168:171], 0\n"
      "ds_read_b64 v[138:139], v40 offset:2088\n"
      "v_mfma_f32_16x16x32_bf16 v[88:91], v[120:123], v[172:175], 0\n"
      "ds_read_b64 v[148:149], v40 offset:3104\n"
      "v_mfma_f32_16x16x32_bf16 v[72:75], v[132:135], v[160:163], v[72:75]\n"
      "ds_read_b64 v[150:151], v40 offset:3112\n"
      "v_mfma_f32_16x16x32_bf16 v[80:83], v[132:135], v[164:167], v[80:83]\n"
      "ds_read_b128 v[176:179], v41 offset:4640\n"
      "v_mfma_f32_16x16x32_bf16 v[88:91], v[132:135], v[168:171], v[88:91]\n"
      "ds_read_b128 v[180:183], v41 offset:3104\n"
      "v_mfma_f32_16x16x32_bf16 v[80:83], v[144:147], v[160:163], v[80:83]\n"
      "ds_read_b128 v[184:187], v41 offset:1568\n"
      "v_mfma_f32_16x16x32_bf16 v[88:91], v[144:147], v[164:167], v[88:91]\n"
      "ds_read_b128 v[188:191], v41 offset:32\n"
      "v_mfma_f32_16x16x32_bf16 v[88:91], v[156:159], v[160:163], v[88:91]\n"
      "ds_read_b64 v[116:117], v40 offset:64\n"
      "s_waitcnt lgkmcnt(5)\n"
      "v_mfma_f32_16x16x32_bf16 v[68:71], v[112:115], v[160:163], 0\n"
      "ds_write_b128 v56, v[96:99]\n"
      "ds_read_b64 v[118:119], v40 offset:72\n"
      "ds_read_b64 v[128:129], v40 offset:1088\n"
      "ds_read_b64 v[130:131], v40 offset:1096\n"
      "v_mfma_f32_16x16x32_bf16 v[76:79], v[112:115], v[164:167], 0\n"
      "ds_read_b64 v[140:141], v40 offset:2112\n"
      "v_mfma_f32_16x16x32_bf16 v[84:87], v[112:115], v[168:171], 0\n"
      "ds_read_b64 v[142:143], v40 offset:2120\n"
      "s_load_dwordx16 s[60:75], s[56:57], 0xc0\n"
      "v_mfma_f32_16x16x32_bf16 v[92:95], v[112:115], v[172:175], 0\n"
      "ds_read_b64 v[152:153], v40 offset:3136\n"
      "v_mfma_f32_16x16x32_bf16 v[76:79], v[124:127], v[160:163], v[76:79]\n"
      "ds_write_b128 v56, v[104:107] offset:2048\n"
      "ds_read_b64 v[154:155], v40 offset:3144\n"
      "v_mfma_f32_16x16x32_bf16 v[84:87], v[124:127], v[164:167], v[84:87]\n"
      "v_mfma_f32_16x16x32_bf16 v[92:95], v[124:127], v[168:171], v[92:95]\n"
      "v_mfma_f32_16x16x32_bf16 v[84:87], v[136:139], v[160:163], v[84:87]\n"
      "v_mfma_f32_16x16x32_bf16 v[92:95], v[136:139], v[164:167], v[92:95]\n"
      "ds_write_b128 v62, v[100:103]\n"
      "v_mfma_f32_16x16x32_bf16 v[92:95], v[148:151], v[160:163], v[92:95]\n"
      "s_waitcnt lgkmcnt(11)\n"
      "v_mfma_f32_16x16x32_bf16 v[64:67], v[112:115], v[176:179], v[64:67]\n"
      "ds_read_b128 v[160:163], v41 offset:4704\n"
      "v_mfma_f32_16x16x32_bf16 v[72:75], v[112:115], v[180:183], v[72:75]\n"
      "ds_read_b128 v[164:167], v41 offset:3168\n"
      "v_mfma_f32_16x16x32_bf16 v[80:83], v[112:115], v[184:187], v[80:83]\n"
      "ds_write_b128 v62, v[108:111] offset:2048\n"
      "ds_read_b128 v[168:171], v41 offset:1632\n"
      "ds_read_b128 v[172:175], v41 offset:96\n"
      "ds_read_b64 v[120:121], v40 offset:96\n"
      "v_mfma_f32_16x16x32_bf16 v[88:91], v[112:115], v[188:191], v[88:91]\n"
      "ds_read_b64 v[122:123], v40 offset:104\n"
      "v_mfma_f32_16x16x32_bf16 v[72:75], v[124:127], v[176:179], v[72:75]\n"
      "ds_read_b64 v[132:133], v40 offset:1120\n"
      "v_mfma_f32_16x16x32_bf16 v[80:83], v[124:127], v[180:183], v[80:83]\n"
      "ds_read_b64 v[134:135], v40 offset:1128\n"
      "v_mfma_f32_16x16x32_bf16 v[88:91], v[124:127], v[184:187], v[88:91]\n"
      "ds_read_b64 v[144:145], v40 offset:2144\n"
      "v_mfma_f32_16x16x32_bf16 v[80:83], v[136:139], v[176:179], v[80:83]\n"
      "ds_read_b64 v[146:147], v40 offset:2152\n"
      "v_mfma_f32_16x16x32_bf16 v[88:91], v[136:139], v[180:183], v[88:91]\n"
      "ds_read_b64 v[156:157], v40 offset:3168\n"
      "v_mfma_f32_16x16x32_bf16 v[88:91], v[148:151], v[176:179], v[88:91]\n"
      "ds_read_b64 v[158:159], v40 offset:3176\n"
      "s_waitcnt lgkmcnt(14)\n"
      "v_mfma_f32_16x16x32_bf16 v[68:71], v[116:119], v[176:179], v[68:71]\n"
      "v_mfma_f32_16x16x32_bf16 v[76:79], v[116:119], v[180:183], v[76:79]\n"
      "v_mfma_f32_16x16x32_bf16 v[84:87], v[116:119], v[184:187], v[84:87]\n"
      "v_mfma_f32_16x16x32_bf16 v[92:95], v[116:119], v[188:191], v[92:95]\n"
      "v_mfma_f32_16x16x32_bf16 v[76:79], v[128:131], v[176:179], v[76:79]\n"
      "s_waitcnt lgkmcnt(10)\n"
      "s_barrier\n"
      "v_mfma_f32_16x16x32_bf16 v[84:87], v[128:131], v[180:183], v[84:87]\n"
      "ds_read_b128 v[96:99], v45 offset:0\n"
      "v_mfma_f32_16x16x32_bf16 v[92:95], v[128:131], v[184:187], v[92:95]\n"
      "ds_read_b128 v[100:103], v45 offset:4096\n"
      "v_mfma_f32_16x16x32_bf16 v[84:87], v[140:143], v[176:179], v[84:87]\n"
      "ds_read_b128 v[104:107], v45 offset:8192\n"
      "v_mfma_f32_16x16x32_bf16 v[92:95], v[140:143], v[180:183], v[92:95]\n"
      "ds_read_b128 v[108:111], v45 offset:12288\n"
      "v_mfma_f32_16x16x32_bf16 v[92:95], v[152:155], v[176:179], v[92:95]\n"
      "ds_read_b64 v[202:203], v47\n"
      "s_waitcnt lgkmcnt(13)\n"
      "v_mfma_f32_16x16x32_bf16 v[64:67], v[116:119], v[160:163], v[64:67]\n"
      "ds_read_b128 v[176:179], v41 offset:4736\n"
      "s_waitcnt vmcnt(2)\n"
      "v_mfma_f32_16x16x32_bf16 v[72:75], v[116:119], v[164:167], v[72:75]\n"
      "buffer_store_dwordx2 v[52:53], v51, s[32:35], s79 offen sc1\n"
      "v_mfma_f32_16x16x32_bf16 v[80:83], v[116:119], v[168:171], v[80:83]\n"
      "ds_read_b128 v[180:183], v41 offset:3200\n"
      "v_mfma_f32_16x16x32_bf16 v[88:91], v[116:119], v[172:175], v[88:91]\n"
      "ds_read_b128 v[184:187], v41 offset:1664\n"
      "v_mfma_f32_16x16x32_bf16 v[72:75], v[128:131], v[160:163], v[72:75]\n"
      "buffer_load_dwordx2 v[200:201], v4, s[32:35], s80 offen offset:0 sc1\n"
      "s_waitcnt lgkmcnt(3)\n"
      "v_mfma_f32_16x16x32_bf16 v[80:83], v[128:131], v[164:167], v[80:83]\n"
      "ds_read_b128 v[188:191], v41 offset:128\n"
      "v_add_f32 v96, v96, v100\n"
      "v_mfma_f32_16x16x32_bf16 v[88:91], v[128:131], v[168:171], v[88:91]\n"
      "buffer_load_dword v250, v61, s[32:35], s85 offen offset:0 sc1\n"
      "v_add_f32 v97, v97, v101\n"
      "v_mfma_f32_16x16x32_bf16 v[80:83], v[140:143], v[160:163], v[80:83]\n"
      "ds_read_b64 v[112:113], v40 offset:128\n"
      "v_add_f32 v98, v98, v102\n"
      "v_mfma_f32_16x16x32_bf16 v[88:91], v[140:143], v[164:167], v[88:91]\n"
      "buffer_load_dwordx4 v[204:207], v58, s[32:35], s83 offen offset:0 sc1\n"
      "v_add_f32 v99, v99, v103\n"
      "v_mfma_f32_16x16x32_bf16 v[88:91], v[152:155], v[160:163], v[88:91]\n"
      "ds_read_b64 v[114:115], v40 offset:136\n"
      "v_mfma_f32_16x16x32_bf16 v[68:71], v[120:123], v[160:163], v[68:71]\n"
      "ds_read_b64 v[124:125], v40 offset:1152\n"
      "v_add_f32 v104, v104, v108\n"
      "v_mfma_f32_16x16x32_bf16 v[76:79], v[120:123], v[164:167], v[76:79]\n"
      "buffer_load_dwordx4 v[36:39], v58, s[32:35], s83 offen offset:16 sc1\n"
      "v_add_f32 v105, v105, v109\n"
      "v_mfma_f32_16x16x32_bf16 v[84:87], v[120:123], v[168:171], v[84:87]\n"
      "ds_read_b64 v[126:127], v40 offset:1160\n"
      "v_add_f32 v106, v106, v110\n"
      "v_mfma_f32_16x16x32_bf16 v[92:95], v[120:123], v[172:175], v[92:95]\n"
      "ds_read_b64 v[136:137], v40 offset:2176\n"
      "v_add_f32 v107, v107, v111\n"
      "v_mfma_f32_16x16x32_bf16 v[76:79], v[132:135], v[160:163], v[76:79]\n"
      "buffer_load_dwordx4 v[224:227], v57, s[32:35], s82 offen offset:0 sc1\n"
      "v_add_f32 v96, v96, v104\n"
      "v_mfma_f32_16x16x32_bf16 v[84:87], v[132:135], v[164:167], v[84:87]\n"
      "ds_read_b64 v[138:139], v40 offset:2184\n"
      "v_add_f32 v97, v97, v105\n"
      "v_mfma_f32_16x16x32_bf16 v[92:95], v[132:135], v[168:171], v[92:95]\n"
      "ds_read_b64 v[148:149], v40 offset:3200\n"
      "v_add_f32 v98, v98, v106\n"
      "v_mfma_f32_16x16x32_bf16 v[84:87], v[144:147], v[160:163], v[84:87]\n"
      "buffer_load_dwordx4 v[228:231], v57, s[32:35], s82 offen offset:1024 sc1\n"
      "v_add_f32 v99, v99, v107\n"
      "v_mfma_f32_16x16x32_bf16 v[92:95], v[144:147], v[164:167], v[92:95]\n"
      "ds_read_b64 v[150:151], v40 offset:3208\n"
      "v_cvt_pk_bf16_f32 v96, 0, v96\n"
      "v_mfma_f32_16x16x32_bf16 v[92:95], v[156:159], v[160:163], v[92:95]\n"
      "ds_read_b128 v[232:235], v46\n"
      "s_waitcnt lgkmcnt(9)\n"
      "v_mfma_f32_16x16x32_bf16 v[64:67], v[120:123], v[176:179], v[64:67]\n"
      "buffer_load_dwordx4 v[192:195], v54, s[32:35], s53 offen offset:0 sc1\n"
      "s_waitcnt vmcnt(8)\n"
      "v_mfma_f32_16x16x32_bf16 v[72:75], v[120:123], v[180:183], v[72:75]\n"
      "ds_read_b128 v[160:163], v41 offset:4800\n"
      "v_cvt_pk_bf16_f32 v97, 0, v97\n"
      "v_mfma_f32_16x16x32_bf16 v[80:83], v[120:123], v[184:187], v[80:83]\n"
      "ds_read_b64_tr_b16 v[240:241], v28 offset:0\n"
      "v_cvt_pk_bf16_f32 v98, 0, v98\n"
      "v_mfma_f32_16x16x32_bf16 v[88:91], v[120:123], v[188:191], v[88:91]\n"
      "buffer_load_dwordx4 v[196:199], v54, s[32:35], s54 offen offset:0 sc1\n"
      "v_cvt_pk_bf16_f32 v99, 0, v99\n"
      "v_mfma_f32_16x16x32_bf16 v[72:75], v[132:135], v[176:179], v[72:75]\n"
      "ds_read_b64_tr_b16 v[242:243], v28 offset:1024\n"
      "v_and_b32 v100, 0xffff0000, v203\n"
      "v_mfma_f32_16x16x32_bf16 v[80:83], v[132:135], v[180:183], v[80:83]\n"
      "ds_read_b64_tr_b16 v[244:245], v28 offset:32\n"
      "v_lshlrev_b32 v101, 16, v203\n"
      "v_mfma_f32_16x16x32_bf16 v[88:91], v[132:135], v[184:187], v[88:91]\n"
      "buffer_load_dword v248, v60, s[32:35], s84 offen offset:0 sc1\n"
      "v_and_b32 v102, 0xffff0000, v202\n"
      "v_mfma_f32_16x16x32_bf16 v[80:83], v[144:147], v[176:179], v[80:83]\n"
      "ds_read_b64_tr_b16 v[246:247], v28 offset:1056\n"
      "v_lshlrev_b32 v103, 16, v202\n"
      "v_mfma_f32_16x16x32_bf16 v[88:91], v[144:147], v[180:183], v[88:91]\n"
      "buffer_load_dword v249, v60, s[32:35], s55 offen offset:0 sc1\n"
      "v_mul_f32 v100, s25, v100\n"
      "v_mfma_f32_16x16x32_bf16 v[88:91], v[156:159], v[176:179], v[88:91]\n"
      "ds_read_b128 v[164:167], v41 offset:3264\n"
      "s_waitcnt lgkmcnt(7)\n"
      "v_mfma_f32_16x16x32_bf16 v[68:71], v[112:115], v[176:179], v[68:71]\n"
      "buffer_load_dwordx4 v[208:211], v55, s[32:35], 0 offen offset:0 sc1\n"
      "v_mul_f32 v101, s25, v101\n"
      "v_mfma_f32_16x16x32_bf16 v[76:79], v[112:115], v[180:183], v[76:79]\n"
      "ds_read_b128 v[168:171], v41 offset:1728\n"
      "v_mul_f32 v102, s25, v102\n"
      "v_mfma_f32_16x16x32_bf16 v[84:87], v[112:115], v[184:187], v[84:87]\n"
      "ds_read_b128 v[172:175], v41 offset:192\n"
      "v_mul_f32 v103, s25, v103\n"
      "v_mfma_f32_16x16x32_bf16 v[92:95], v[112:115], v[188:191], v[92:95]\n"
      "buffer_load_dwordx4 v[212:215], v55, s[32:35], 0 offen offset:1024 sc1\n"
      "v_cvt_pk_bf16_f32 v100, 0, v100\n"
      "v_mfma_f32_16x16x32_bf16 v[76:79], v[124:127], v[176:179], v[76:79]\n"
      "ds_read_b64 v[116:117], v40 offset:160\n"
      "v_cvt_pk_bf16_f32 v101, 0, v101\n"
      "v_mfma_f32_16x16x32_bf16 v[84:87], v[124:127], v[180:183], v[84:87]\n"
      "ds_read_b64 v[118:119], v40 offset:168\n"
      "v_cvt_pk_bf16_f32 v102, 0, v102\n"
      "v_mfma_f32_16x16x32_bf16 v[92:95], v[124:127], v[184:187], v[92:95]\n"
      "buffer_load_dwordx4 v[216:219], v55, s[32:35], 0 offen offset:2048 sc1\n"
      "ds_read_b64 v[128:129], v40 offset:1184\n"
      "v_cvt_pk_bf16_f32 v103, 0, v103\n"
      "v_mfma_f32_16x16x32_bf16 v[84:87], v[136:139], v[176:179], v[84:87]\n"
      "ds_read_b64 v[130:131], v40 offset:1192\n"
      "v_add_f32 v96, v96, v100\n"
      "v_mfma_f32_16x16x32_bf16 v[92:95], v[136:139], v[180:183], v[92:95]\n"
      "ds_read_b64 v[140:141], v40 offset:2208\n"
      "v_add_f32 v97, v97, v101\n"
      "v_mfma_f32_16x16x32_bf16 v[92:95], v[148:151], v[176:179], v[92:95]\n"
      "buffer_load_dwordx4 v[220:223], v55, s[32:35], 0 offen offset:3072 sc1\n"
      "s_waitcnt lgkmcnt(5)\n"
      "v_mfma_f32_16x16x32_bf16 v[64:67], v[112:115], v[160:163], v[64:67]\n"
      "ds_read_b64 v[142:143], v40 offset:2216\n"
      "v_mfma_f32_16x16x32_bf16 v[72:75], v[112:115], v[164:167], v[72:75]\n"
      "ds_read_b64 v[152:153], v40 offset:3232\n"
      "v_add_f32 v98, v98, v102\n"
      "v_mfma_f32_16x16x32_bf16 v[80:83], v[112:115], v[168:171], v[80:83]\n"
      "buffer_store_dwordx4 v[240:243], v7, s[32:35], s86 offen sc1\n"
      "v_add_f32 v99, v99, v103\n"
      "v_mfma_f32_16x16x32_bf16 v[88:91], v[112:115], v[172:175], v[88:91]\n"
      "ds_read_b64 v[154:155], v40 offset:3240\n"
      "v_cvt_pk_bf16_f32 v96, 0, v96\n"
      "v_mfma_f32_16x16x32_bf16 v[72:75], v[124:127], v[160:163], v[72:75]\n"
      "ds_read_b128 v[176:179], v41 offset:4832\n"
      "v_cvt_pk_bf16_f32 v97, 0, v97\n"
      "v_mfma_f32_16x16x32_bf16 v[80:83], v[124:127], v[164:167], v[80:83]\n"
      "buffer_store_dwordx4 v[244:247], v7, s[32:35], s87 offen sc1\n"
      "v_cvt_pk_bf16_f32 v98, 0, v98\n"
      "v_mfma_f32_16x16x32_bf16 v[88:91], v[124:127], v[168:171], v[88:91]\n"
      "ds_read_b128 v[180:183], v41 offset:3296\n"
      "v_cvt_pk_bf16_f32 v99, 0, v99\n"
      "v_mfma_f32_16x16x32_bf16 v[80:83], v[136:139], v[160:163], v[80:83]\n"
      "ds_read_b128 v[184:187], v41 offset:1760\n"
      "v_mfma_f32_16x16x32_bf16 v[88:91], v[136:139], v[164:167], v[88:91]\n"
      "ds_read_b128 v[188:191], v41 offset:224\n"
      "v_mfma_f32_16x16x32_bf16 v[88:91], v[148:151], v[160:163], v[88:91]\n"
      "ds_read_b64 v[120:121], v40 offset:192\n"
      "s_waitcnt lgkmcnt(5)\n"
      "v_mfma_f32_16x16x32_bf16 v[68:71], v[116:119], v[160:163], v[68:71]\n"
      "ds_read_b64 v[122:123], v40 offset:200\n"
      "v_mfma_f32_16x16x32_bf16 v[76:79], v[116:119], v[164:167], v[76:79]\n"
      "ds_read_b64 v[132:133], v40 offset:1216\n"
      "v_mfma_f32_16x16x32_bf16 v[84:87], v[116:119], v[168:171], v[84:87]\n"
      "ds_read_b64 v[134:135], v40 offset:1224\n"
      "v_mfma_f32_16x16x32_bf16 v[92:95], v[116:119], v[172:175], v[92:95]\n"
      "ds_read_b64 v[144:145], v40 offset:2240\n"
      "v_mfma_f32_16x16x32_bf16 v[76:79], v[128:131], v[160:163], v[76:79]\n"
      "ds_read_b64 v[146:147], v40 offset:2248\n"
      "v_mfma_f32_16x16x32_bf16 v[84:87], v[128:131], v[164:167], v[84:87]\n"
      "ds_read_b64 v[156:157], v40 offset:3264\n"
      "v_mfma_f32_16x16x32_bf16 v[92:95], v[128:131], v[168:171], v[92:95]\n"
      "ds_read_b64 v[158:159], v40 offset:3272\n"
      "v_mfma_f32_16x16x32_bf16 v[84:87], v[140:143], v[160:163], v[84:87]\n"
      "ds_read_b128 v[236:239], v46 offset:7680\n"
      "v_mfma_f32_16x16x32_bf16 v[92:95], v[140:143], v[164:167], v[92:95]\n"
      "v_mfma_f32_16x16x32_bf16 v[92:95], v[152:155], v[160:163], v[92:95]\n"
      "s_waitcnt lgkmcnt(9)\n"
      "v_mfma_f32_16x16x32_bf16 v[64:67], v[116:119], v[176:179], v[64:67]\n"
      "ds_read_b128 v[160:163], v41 offset:4896\n"
      "v_mfma_f32_16x16x32_bf16 v[72:75], v[116:119], v[180:183], v[72:75]\n"
      "ds_read_b128 v[164:167], v41 offset:3360\n"
      "v_mfma_f32_16x16x32_bf16 v[80:83], v[116:119], v[184:187], v[80:83]\n"
      "ds_read_b128 v[168:171], v41 offset:1824\n"
      "v_mfma_f32_16x16x32_bf16 v[88:91], v[116:119], v[188:191], v[88:91]\n"
      "ds_read_b128 v[172:175], v41 offset:288\n"
      "v_mfma_f32_16x16x32_bf16 v[72:75], v[128:131], v[176:179], v[72:75]\n"
      "ds_read_b64 v[112:113], v40 offset:224\n"
      "v_mfma_f32_16x16x32_bf16 v[80:83], v[128:131], v[180:183], v[80:83]\n"
      "ds_read_b64 v[114:115], v40 offset:232\n"
      "v_mfma_f32_16x16x32_bf16 v[88:91], v[128:131], v[184:187], v[88:91]\n"
      "ds_read_b64 v[124:125], v40 offset:1248\n"
      "v_mfma_f32_16x16x32_bf16 v[80:83], v[140:143], v[176:179], v[80:83]\n"
      "ds_read_b64 v[126:127], v40 offset:1256\n"
      "v_mfma_f32_16x16x32_bf16 v[88:91], v[140:143], v[180:183], v[88:91]\n"
      "ds_read_b64 v[136:137], v40 offset:2272\n"
      "v_mfma_f32_16x16x32_bf16 v[88:91], v[152:155], v[176:179], v[88:91]\n"
      "ds_read_b64 v[138:139], v40 offset:2280\n"
      "s_waitcnt lgkmcnt(11)\n"
      "v_mfma_f32_16x16x32_bf16 v[68:71], v[120:123], v[176:179], v[68:71]\n"
      "ds_read_b64 v[148:149], v40 offset:3296\n"
      "v_mfma_f32_16x16x32_bf16 v[76:79], v[120:123], v[180:183], v[76:79]\n"
      "ds_read_b64 v[150:151], v40 offset:3304\n"
      "v_mfma_f32_16x16x32_bf16 v[84:87], v[120:123], v[184:187], v[84:87]\n"
      "v_mfma_f32_16x16x32_bf16 v[92:95], v[120:123], v[188:191], v[92:95]\n"
      "v_mfma_f32_16x16x32_bf16 v[76:79], v[132:135], v[176:179], v[76:79]\n"
      "v_mfma_f32_16x16x32_bf16 v[84:87], v[132:135], v[180:183], v[84:87]\n"
      "v_mfma_f32_16x16x32_bf16 v[92:95], v[132:135], v[184:187], v[92:95]\n"
      "s_waitcnt vmcnt(2)\n"
      "v_add_u32 v251, v200, v201\n"
      "v_mfma_f32_16x16x32_bf16 v[84:87], v[144:147], v[176:179], v[84:87]\n"
      "v_add3_u32 v34, v192, v193, v194\n"
      "v_add3_u32 v34, v34, v195, v196\n"
      "v_mfma_f32_16x16x32_bf16 v[92:95], v[144:147], v[180:183], v[92:95]\n"
      "v_add_u32_dpp v251, v251, v251 row_half_mirror row_mask:0xf bank_mask:0xf\n"
      "v_add3_u32 v34, v34, v197, v198\n"
      "v_add3_u32 v251, v251, v34, v199\n"
      "v_mfma_f32_16x16x32_bf16 v[92:95], v[156:159], v[176:179], v[92:95]\n"
      "v_add3_u32 v34, v248, v249, v250\n"
      "v_add3_u32 v34, v34, v205, v207\n"
      "v_add_u32_dpp v251, v251, v251 quad_perm:[1,0,3,2] row_mask:0xf bank_mask:0xf\n"
      "s_waitcnt lgkmcnt(8)\n"
      "v_mfma_f32_16x16x32_bf16 v[64:67], v[120:123], v[160:163], v[64:67]\n"
      "ds_read_b128 v[176:179], v41 offset:4928\n"
      "v_add3_u32 v34, v34, v37, v39\n"
      "v_add3_u32 v34, v34, v211, v215\n"
      "v_add_u32_dpp v251, v251, v251 quad_perm:[2,3,0,1] row_mask:0xf bank_mask:0xf\n"
      "v_mfma_f32_16x16x32_bf16 v[72:75], v[120:123], v[164:167], v[72:75]\n"
      "ds_read_b128 v[180:183], v41 offset:3392\n"
      "v_add3_u32 v34, v34, v219, v223\n"
      "v_add3_u32 v34, v34, v227, v231\n"
      "v_add_u32 v251, v251, v34\n"
      "v_mfma_f32_16x16x32_bf16 v[80:83], v[120:123], v[168:171], v[80:83]\n"
      "ds_read_b128 v[184:187], v41 offset:1856\n"
      "v_cmp_ne_u32 vcc, s24, v251\n"
      "v_mfma_f32_16x16x32_bf16 v[88:91], v[120:123], v[172:175], v[88:91]\n"
      "ds_read_b128 v[188:191], v41 offset:320\n"
      "v_mfma_f32_16x16x32_bf16 v[72:75], v[132:135], v[160:163], v[72:75]\n"
      "ds_read_b64 v[116:117], v40 offset:256\n"
      "s_cbranch_vccnz L_cv_s%=\n"
      "L_cv_r%=:\n"
      "v_add_f32 v204, v204, v206\n"
      "v_add_f32 v36, v36, v38\n"
      "v_lshlrev_b32 v100, 16, v200\n"
      "v_mfma_f32_16x16x32_bf16 v[80:83], v[132:135], v[164:167], v[80:83]\n"
      "ds_read_b64 v[118:119], v40 offset:264\n"
      "v_add_f32 v204, v204, v36\n"
      "v_and_b32 v101, 0xffff0000, v200\n"
      "v_cvt_pk_bf16_f32 v204, 0, v204\n"
      "v_mfma_f32_16x16x32_bf16 v[88:91], v[132:135], v[168:171], v[88:91]\n"
      "ds_read_b64 v[128:129], v40 offset:1280\n"
      "v_lshlrev_b32 v102, 16, v201\n"
      "v_add_f32 v205, 1.0, v204\n"
      "v_and_b32 v103, 0xffff0000, v201\n"
      "v_mfma_f32_16x16x32_bf16 v[80:83], v[144:147], v[160:163], v[80:83]\n"
      "ds_read_b64 v[130:131], v40 offset:1288\n"
      "v_cvt_pk_bf16_f32 v205, 0, v205\n"
      "v_lshlrev_b32 v104, 16, v226\n"
      "v_readlane_b32 s40, v204, 8\n"
      "v_mfma_f32_16x16x32_bf16 v[88:91], v[144:147], v[164:167], v[88:91]\n"
      "ds_read_b64 v[140:141], v40 offset:2304\n"
      "v_and_b32 v105, 0xffff0000, v226\n"
      "v_readlane_b32 s41, v205, 9\n"
      "v_lshlrev_b32 v106, 16, v230\n"
      "v_mfma_f32_16x16x32_bf16 v[88:91], v[156:159], v[160:163], v[88:91]\n"
      "ds_read_b64 v[142:143], v40 offset:2312\n"
      "v_readlane_b32 s42, v204, 10\n"
      "v_and_b32 v107, 0xffff0000, v230\n"
      "v_sub_f32 v104, v100, v104\n"
      "s_waitcnt lgkmcnt(10)\n"
      "v_mfma_f32_16x16x32_bf16 v[68:71], v[112:115], v[160:163], v[68:71]\n"
      "ds_read_b64 v[152:153], v40 offset:3328\n"
      "v_sub_f32 v105, v101, v105\n"
      "v_sub_f32 v106, v102, v106\n"
      "v_mfma_f32_16x16x32_bf16 v[76:79], v[112:115], v[164:167], v[76:79]\n"
      "ds_read_b64 v[154:155], v40 offset:3336\n"
      "v_sub_f32 v107, v103, v107\n"
      "v_cvt_pk_bf16_f32 v104, 0, v104\n"
      "v_cvt_pk_bf16_f32 v105, 0, v105\n"
      "v_mfma_f32_16x16x32_bf16 v[84:87], v[112:115], v[168:171], v[84:87]\n"
      "v_cvt_pk_bf16_f32 v106, 0, v106\n"
      "v_cvt_pk_bf16_f32 v107, 0, v107\n"
      "v_mul_f32 v104, v104, v224\n"
      "v_mfma_f32_16x16x32_bf16 v[92:95], v[112:115], v[172:175], v[92:95]\n"
      "v_mul_f32 v105, v105, v225\n"
      "v_mul_f32 v106, v106, v228\n"
      "v_mul_f32 v107, v107, v229\n"
      "v_mfma_f32_16x16x32_bf16 v[76:79], v[124:127], v[160:163], v[76:79]\n"
      "v_cvt_pk_bf16_f32 v104, 0, v104\n"
      "v_cvt_pk_bf16_f32 v105, 0, v105\n"
      "v_cvt_pk_bf16_f32 v106, 0, v106\n"
      "v_mfma_f32_16x16x32_bf16 v[84:87], v[124:127], v[164:167], v[84:87]\n"
      "v_cvt_pk_bf16_f32 v107, 0, v107\n"
      "v_mul_f32 v104, s41, v104\n"
      "v_mul_f32 v105, s41, v105\n"
      "v_mfma_f32_16x16x32_bf16 v[92:95], v[124:127], v[168:171], v[92:95]\n"
      "v_mul_f32 v106, s41, v106\n"
      "v_mul_f32 v107, s41, v107\n"
      "v_mfma_f32_16x16x32_bf16 v[84:87], v[136:139], v[160:163], v[84:87]\n"
      "v_cvt_pk_bf16_f32 v104, 0, v104\n"
      "v_cvt_pk_bf16_f32 v105, 0, v105\n"
      "v_cvt_pk_bf16_f32 v106, 0, v106\n"
      "v_mfma_f32_16x16x32_bf16 v[92:95], v[136:139], v[164:167], v[92:95]\n"
      "v_cvt_pk_bf16_f32 v107, 0, v107\n"
      "v_add_f32 v104, s40, v104\n"
      "v_add_f32 v105, s40, v105\n"
      "v_mfma_f32_16x16x32_bf16 v[92:95], v[148:151], v[160:163], v[92:95]\n"
      "v_add_f32 v106, s40, v106\n"
      "v_add_f32 v107, s40, v107\n"
      "v_cvt_pk_bf16_f32 v104, 0, v104\n"
      "s_waitcnt lgkmcnt(0)\n"
      "v_mfma_f32_16x16x32_bf16 v[64:67], v[112:115], v[176:179], v[64:67]\n"
      "buffer_load_dwordx4 v[240:243], v48, s[32:35], s76 offen offset:0 sc1\n"
      "v_cvt_pk_bf16_f32 v105, 0, v105\n"
      "v_cvt_pk_bf16_f32 v106, 0, v106\n"
      "v_cvt_pk_bf16_f32 v107, 0, v107\n"
      "v_mfma_f32_16x16x32_bf16 v[68:71], v[116:119], v[176:179], v[68:71]\n"
      "v_mul_f32 v104, v104, v96\n"
      "v_mul_f32 v105, v105, v97\n"
      "v_mul_f32 v106, v106, v98\n"
      "v_mfma_f32_16x16x32_bf16 v[72:75], v[112:115], v[180:183], v[72:75]\n"
      "v_mul_f32 v107, v107, v99\n"
      "v_cvt_pk_bf16_f32 v104, 0, v104\n"
      "v_mfma_f32_16x16x32_bf16 v[72:75], v[124:127], v[176:179], v[72:75]\n"
      "ds_write_b128 v44, v[64:67] offset:0\n"
      "v_cvt_pk_bf16_f32 v105, 0, v105\n"
      "v_cvt_pk_bf16_f32 v106, 0, v106\n"
      "v_cvt_pk_bf16_f32 v107, 0, v107\n"
      "v_mul_f32 v104, s42, v104\n"
      "v_mfma_f32_16x16x32_bf16 v[76:79], v[116:119], v[180:183], v[76:79]\n"
      "ds_write_b128 v59, v[68:71] offset:0\n"
      "v_mul_f32 v105, s42, v105\n"
      "v_mul_f32 v106, s42, v106\n"
      "v_mul_f32 v107, s42, v107\n"
      "v_cvt_pk_bf16_f32 v104, 0, v104\n"
      "v_mfma_f32_16x16x32_bf16 v[76:79], v[128:131], v[176:179], v[76:79]\n"
      "v_cvt_pk_bf16_f32 v105, 0, v105\n"
      "v_cvt_pk_bf16_f32 v106, 0, v106\n"
      "v_mfma_f32_16x16x32_bf16 v[80:83], v[112:115], v[184:187], v[80:83]\n"
      "ds_write_b128 v44, v[72:75] offset:2048\n"
      "v_cvt_pk_bf16_f32 v107, 0, v107\n"
      "v_add_f32 v104, v104, v100\n"
      "v_add_f32 v105, v105, v101\n"
      "v_add_f32 v106, v106, v102\n"
      "v_mfma_f32_16x16x32_bf16 v[80:83], v[124:127], v[180:183], v[80:83]\n"
      "v_add_f32 v107, v107, v103\n"
      "v_cvt_pk_bf16_f32 v104, v104, v105\n"
      "v_mfma_f32_16x16x32_bf16 v[80:83], v[136:139], v[176:179], v[80:83]\n"
      "ds_write_b128 v59, v[76:79] offset:2048\n"
      "v_cvt_pk_bf16_f32 v105, v106, v107\n"
      "v_add_u32 v106, v104, v105\n"
      "s_add_u32 s8, s26, s74\n"
      "s_addc_u32 s9, s27, 0\n"
      "v_mfma_f32_16x16x32_bf16 v[84:87], v[116:119], v[184:187], v[84:87]\n"
      "buffer_store_dwordx2 v[104:105], v50, s[32:35], s78 offen sc1\n"
      "v_add_u32_dpp v106, v106, v106 quad_perm:[1,0,3,2] row_mask:0xf bank_mask:0xf\n"
      "v_mfma_f32_16x16x32_bf16 v[84:87], v[128:131], v[180:183], v[84:87]\n"
      "s_add_u32 s4, s26, s71\n"
      "s_addc_u32 s5, s27, 0\n"
      "v_mfma_f32_16x16x32_bf16 v[84:87], v[140:143], v[176:179], v[84:87]\n"
      "ds_write_b128 v44, v[80:83] offset:4096\n"
      "v_add_u32_dpp v106, v106, v106 quad_perm:[2,3,0,1] row_mask:0xf bank_mask:0xf\n"
      "s_add_u32 s6, s26, s72\n"
      "s_addc_u32 s7, s27, 0\n"
      "v_mfma_f32_16x16x32_bf16 v[88:91], v[112:115], v[188:191], v[88:91]\n"
      "s_add_u32 s10, s26, s75\n"
      "s_addc_u32 s11, s27, 0\n"
      "v_mfma_f32_16x16x32_bf16 v[88:91], v[124:127], v[184:187], v[88:91]\n"
      "v_add_u32_dpp v106, v106, v106 row_half_mirror row_mask:0xf bank_mask:0xf\n"
      "v_mfma_f32_16x16x32_bf16 v[88:91], v[136:139], v[180:183], v[88:91]\n"
      "ds_write_b128 v59, v[84:87] offset:4096\n"
      "s_add_u32 s12, s10, 49152\n"
      "s_addc_u32 s13, s11, 0\n"
      "s_lshl_b32 s71, s30, 2\n"
      "v_mfma_f32_16x16x32_bf16 v[88:91], v[148:151], v[176:179], v[88:91]\n"
      "v_add_u32_dpp v106, v106, v106 row_mirror row_mask:0xf bank_mask:0xf\n"
      "v_sub_u32 v106, v52, v106\n"
      "v_mfma_f32_16x16x32_bf16 v[92:95], v[116:119], v[188:191], v[92:95]\n"
      "buffer_store_dword v106, v49, s[32:35], s77 offen sc1\n"
      "v_mfma_f32_16x16x32_bf16 v[92:95], v[128:131], v[184:187], v[92:95]\n"
      "v_mfma_f32_16x16x32_bf16 v[92:95], v[140:143], v[180:183], v[92:95]\n"
      "ds_write_b128 v44, v[88:91] offset:6144\n"
      "v_mfma_f32_16x16x32_bf16 v[92:95], v[152:155], v[176:179], v[92:95]\n"
      "s_nop 7\n"
      "ds_write_b128 v59, v[92:95] offset:6144\n"
      "v_readlane_b32 s40, v204, 0\n"
      "v_readlane_b32 s42, v205, 1\n"
      "v_xor_b32 v33, 16, v31\n"
      "v_readlane_b32 s58, v204, 2\n"
      "v_readlane_b32 s48, v204, 4\n"
      "v_readlane_b32 s50, v205, 5\n"
      "v_readlane_b32 s52, v204, 6\n"
      "v_add_u32 v40, s73, v7\n"
      "v_lshlrev_b32 v96, 16, v210\n"
      "v_and_b32 v97, 0xffff0000, v210\n"
      "v_lshlrev_b32 v98, 16, v214\n"
      "v_and_b32 v99, 0xffff0000, v214\n"
      "v_lshlrev_b32 v100, 16, v218\n"
      "v_and_b32 v101, 0xffff0000, v218\n"
      "v_lshlrev_b32 v102, 16, v222\n"
      "v_and_b32 v103, 0xffff0000, v222\n"
      "s_waitcnt lgkmcnt(0)\n"
      "s_barrier\n"
      "ds_read_b128 v[64:67], v31 offset:0\n"
      "ds_read_b128 v[68:71], v33 offset:0\n"
      "ds_read_b128 v[72:75], v31 offset:8192\n"
      "ds_read_b128 v[76:79], v33 offset:8192\n"
      "ds_read_b128 v[80:83], v31 offset:16384\n"
      "ds_read_b128 v[84:87], v33 offset:16384\n"
      "ds_read_b128 v[88:91], v31 offset:24576\n"
      "ds_read_b128 v[92:95], v33 offset:24576\n"
      "v_lshlrev_b32 v112, 16, v192\n"
      "v_and_b32 v113, 0xffff0000, v192\n"
      "v_lshlrev_b32 v114, 16, v193\n"
      "v_and_b32 v115, 0xffff0000, v193\n"
      "v_lshlrev_b32 v116, 16, v194\n"
      "v_and_b32 v117, 0xffff0000, v194\n"
      "v_lshlrev_b32 v118, 16, v195\n"
      "v_and_b32 v119, 0xffff0000, v195\n"
      "v_pk_add_f32 v[104:105], v[112:113], v[96:97] neg_lo:[0,1] neg_hi:[0,1]\n"
      "v_pk_add_f32 v[106:107], v[114:115], v[98:99] neg_lo:[0,1] neg_hi:[0,1]\n"
      "v_pk_add_f32 v[108:109], v[116:117], v[100:101] neg_lo:[0,1] neg_hi:[0,1]\n"
      "v_pk_add_f32 v[110:111], v[118:119], v[102:103] neg_lo:[0,1] neg_hi:[0,1]\n"
      "v_cvt_pk_bf16_f32 v104, 0, v104\n"
      "v_cvt_pk_bf16_f32 v105, 0, v105\n"
      "v_cvt_pk_bf16_f32 v106, 0, v106\n"
      "v_cvt_pk_bf16_f32 v107, 0, v107\n"
      "v_cvt_pk_bf16_f32 v108, 0, v108\n"
      "v_cvt_pk_bf16_f32 v109, 0, v109\n"
      "v_cvt_pk_bf16_f32 v110, 0, v110\n"
      "v_cvt_pk_bf16_f32 v111, 0, v111\n"
      "v_pk_mul_f32 v[104:105], v[104:105], v[208:209]\n"
      "v_pk_mul_f32 v[106:107], v[106:107], v[212:213]\n"
      "v_pk_mul_f32 v[108:109], v[108:109], v[216:217]\n"
      "v_pk_mul_f32 v[110:111], v[110:111], v[220:221]\n"
      "v_cvt_pk_bf16_f32 v104, 0, v104\n"
      "v_cvt_pk_bf16_f32 v105, 0, v105\n"
      "v_cvt_pk_bf16_f32 v106, 0, v106\n"
      "v_cvt_pk_bf16_f32 v107, 0, v107\n"
      "v_cvt_pk_bf16_f32 v108, 0, v108\n"
      "v_cvt_pk_bf16_f32 v109, 0, v109\n"
      "s_waitcnt vmcnt(2)\n"
      "v_add3_u32 v240, v240, v241, v242\n"
      "s_add_u32 s47, s30, s31\n"
      "v_add_u32 v240, v240, v243\n"
      "s_lshl_b32 s47, s47, 1\n"
      "v_cmp_ne_u32 vcc, s47, v240\n"
      "v_cvt_pk_bf16_f32 v110, 0, v110\n"
      "v_cvt_pk_bf16_f32 v111, 0, v111\n"
      "v_pk_mul_f32 v[104:105], v[104:105], s[42:43] op_sel_hi:[1,0]\n"
      "v_pk_mul_f32 v[106:107], v[106:107], s[42:43] op_sel_hi:[1,0]\n"
      "v_pk_mul_f32 v[108:109], v[108:109], s[42:43] op_sel_hi:[1,0]\n"
      "v_pk_mul_f32 v[110:111], v[110:111], s[42:43] op_sel_hi:[1,0]\n"
      "s_cbranch_vccnz L_es%=\n"
      "L_eb%=:\n"
      "v_add_u32 v34, 4096, v7\n"
      "global_load_dwordx4 a[0:3], v34, s[8:9] offset:-4096 sc1\n"
      "v_cvt_pk_bf16_f32 v104, 0, v104\n"
      "v_cvt_pk_bf16_f32 v105, 0, v105\n"
      "v_cvt_pk_bf16_f32 v106, 0, v106\n"
      "v_cvt_pk_bf16_f32 v107, 0, v107\n"
      "v_cvt_pk_bf16_f32 v108, 0, v108\n"
      "v_cvt_pk_bf16_f32 v109, 0, v109\n"
      "v_cvt_pk_bf16_f32 v110, 0, v110\n"
      "v_cvt_pk_bf16_f32 v111, 0, v111\n"
      "v_pk_add_f32 v[104:105], v[104:105], s[40:41] op_sel_hi:[1,0]\n"
      "global_load_dwordx4 a[4:7], v34, s[8:9] offset:-3072 sc1\n"
      "v_pk_add_f32 v[106:107], v[106:107], s[40:41] op_sel_hi:[1,0]\n"
      "v_pk_add_f32 v[108:109], v[108:109], s[40:41] op_sel_hi:[1,0]\n"
      "v_pk_add_f32 v[110:111], v[110:111], s[40:41] op_sel_hi:[1,0]\n"
      "v_cvt_pk_bf16_f32 v104, 0, v104\n"
      "v_cvt_pk_bf16_f32 v105, 0, v105\n"
      "v_cvt_pk_bf16_f32 v106, 0, v106\n"
      "v_cvt_pk_bf16_f32 v107, 0, v107\n"
      "v_cvt_pk_bf16_f32 v108, 0, v108\n"
      "v_cvt_pk_bf16_f32 v109, 0, v109\n"
      "global_load_dwordx4 a[8:11], v34, s[8:9] offset:-2048 sc1\n"
      "v_cvt_pk_bf16_f32 v110, 0, v110\n"
      "v_cvt_pk_bf16_f32 v111, 0, v111\n"
      "v_and_b32 v120, 0xffff0000, v235\n"
      "v_lshlrev_b32 v121, 16, v235\n"
      "v_and_b32 v122, 0xffff0000, v234\n"
      "v_lshlrev_b32 v123, 16, v234\n"
      "v_and_b32 v124, 0xffff0000, v233\n"
      "v_lshlrev_b32 v125, 16, v233\n"
      "v_and_b32 v126, 0xffff0000, v232\n"
      "global_load_dwordx4 a[12:15], v34, s[8:9] offset:-1024 sc1\n"
      "v_lshlrev_b32 v127, 16, v232\n"
      "v_pk_mul_f32 v[120:121], v[120:121], s[14:15] op_sel_hi:[1,0]\n"
      "v_pk_mul_f32 v[122:123], v[122:123], s[14:15] op_sel_hi:[1,0]\n"
      "v_pk_mul_f32 v[124:125], v[124:125], s[14:15] op_sel_hi:[1,0]\n"
      "v_pk_mul_f32 v[126:127], v[126:127], s[14:15] op_sel_hi:[1,0]\n"
      "v_cvt_pk_bf16_f32 v120, 0, v120\n"
      "v_cvt_pk_bf16_f32 v121, 0, v121\n"
      "v_cvt_pk_bf16_f32 v122, 0, v122\n"
      "v_cvt_pk_bf16_f32 v123, 0, v123\n"
      "global_load_dwordx4 a[16:19], v34, s[8:9] offset:0 sc1\n"
      "v_cvt_pk_bf16_f32 v124, 0, v124\n"
      "v_cvt_pk_bf16_f32 v125, 0, v125\n"
      "v_cvt_pk_bf16_f32 v126, 0, v126\n"
      "v_cvt_pk_bf16_f32 v127, 0, v127\n"
      "s_add_u32 s43, s89, 32768\n"
      "s_add_u32 s45, s81, 32768\n"
      "s_waitcnt lgkmcnt(0)\n"
      "v_pk_add_f32 v[64:65], v[64:65], v[72:73]\n"
      "v_pk_add_f32 v[66:67], v[66:67], v[74:75]\n"
      "global_load_dwordx4 a[20:23], v34, s[8:9] offset:1024 sc1\n"
      "v_pk_add_f32 v[68:69], v[68:69], v[76:77]\n"
      "v_pk_add_f32 v[70:71], v[70:71], v[78:79]\n"
      "v_cvt_pk_bf16_f32 v64, 0, v64\n"
      "v_cvt_pk_bf16_f32 v65, 0, v65\n"
      "v_cvt_pk_bf16_f32 v66, 0, v66\n"
      "v_cvt_pk_bf16_f32 v67, 0, v67\n"
      "v_cvt_pk_bf16_f32 v68, 0, v68\n"
      "v_cvt_pk_bf16_f32 v69, 0, v69\n"
      "v_cvt_pk_bf16_f32 v70, 0, v70\n"
      "global_load_dwordx4 a[24:27], v34, s[8:9] offset:2048 sc1\n"
      "v_cvt_pk_bf16_f32 v71, 0, v71\n"
      "v_pk_add_f32 v[120:121], v[120:121], v[64:65]\n"
      "v_pk_add_f32 v[122:123], v[122:123], v[66:67]\n"
      "v_pk_add_f32 v[124:125], v[124:125], v[68:69]\n"
      "v_pk_add_f32 v[126:127], v[126:127], v[70:71]\n"
      "v_cvt_pk_bf16_f32 v120, 0, v120\n"
      "v_cvt_pk_bf16_f32 v121, 0, v121\n"
      "v_cvt_pk_bf16_f32 v122, 0, v122\n"
      "v_cvt_pk_bf16_f32 v123, 0, v123\n"
      "global_load_dwordx4 a[28:31], v34, s[8:9] offset:3072 sc1\n"
      "v_cvt_pk_bf16_f32 v124, 0, v124\n"
      "v_cvt_pk_bf16_f32 v125, 0, v125\n"
      "v_cvt_pk_bf16_f32 v126, 0, v126\n"
      "v_cvt_pk_bf16_f32 v127, 0, v127\n"
      "v_pk_mul_f32 v[104:105], v[104:105], v[120:121]\n"
      "v_pk_mul_f32 v[106:107], v[106:107], v[122:123]\n"
      "v_pk_mul_f32 v[108:109], v[108:109], v[124:125]\n"
      "v_pk_mul_f32 v[110:111], v[110:111], v[126:127]\n"
      "v_cvt_pk_bf16_f32 v104, 0, v104\n"
      "v_add_u32 v34, 12288, v7\n"
      "global_load_dwordx4 a[32:35], v34, s[8:9] offset:-4096 sc1\n"
      "v_cvt_pk_bf16_f32 v105, 0, v105\n"
      "v_cvt_pk_bf16_f32 v106, 0, v106\n"
      "v_cvt_pk_bf16_f32 v107, 0, v107\n"
      "v_cvt_pk_bf16_f32 v108, 0, v108\n"
      "v_cvt_pk_bf16_f32 v109, 0, v109\n"
      "v_cvt_pk_bf16_f32 v110, 0, v110\n"
      "v_cvt_pk_bf16_f32 v111, 0, v111\n"
      "v_pk_mul_f32 v[104:105], v[104:105], s[58:59] op_sel_hi:[1,0]\n"
      "v_pk_mul_f32 v[106:107], v[106:107], s[58:59] op_sel_hi:[1,0]\n"
      "global_load_dwordx4 a[36:39], v34, s[8:9] offset:-3072 sc1\n"
      "v_pk_mul_f32 v[108:109], v[108:109], s[58:59] op_sel_hi:[1,0]\n"
      "v_pk_mul_f32 v[110:111], v[110:111], s[58:59] op_sel_hi:[1,0]\n"
      "v_cvt_pk_bf16_f32 v104, 0, v104\n"
      "v_cvt_pk_bf16_f32 v105, 0, v105\n"
      "v_cvt_pk_bf16_f32 v106, 0, v106\n"
      "v_cvt_pk_bf16_f32 v107, 0, v107\n"
      "v_cvt_pk_bf16_f32 v108, 0, v108\n"
      "v_cvt_pk_bf16_f32 v109, 0, v109\n"
      "v_cvt_pk_bf16_f32 v110, 0, v110\n"
      "v_cvt_pk_bf16_f32 v111, 0, v111\n"
      "v_pk_add_f32 v[104:105], v[104:105], v[112:113]\n"
      "v_pk_add_f32 v[106:107], v[106:107], v[114:115]\n"
      "v_pk_add_f32 v[108:109], v[108:109], v[116:117]\n"
      "v_pk_add_f32 v[110:111], v[110:111], v[118:119]\n"
      "v_cvt_pk_bf16_f32 v128, v104, v105\n"
      "v_cvt_pk_bf16_f32 v129, v106, v107\n"
      "v_cvt_pk_bf16_f32 v130, v108, v109\n"
      "v_cvt_pk_bf16_f32 v131, v110, v111\n"
      "buffer_store_dwordx4 v[128:131], v29, s[32:35], s89 offen sc1\n"
      "v_add3_u32 v35, v128, v129, v130\n"
      "v_add_u32 v35, v35, v131\n"
      "v_lshlrev_b32 v112, 16, v196\n"
      "v_and_b32 v113, 0xffff0000, v196\n"
      "v_add_u32_dpp v35, v35, v35 quad_perm:[1,0,3,2] row_mask:0xf bank_mask:0xf\n"
      "v_lshlrev_b32 v114, 16, v197\n"
      "v_and_b32 v115, 0xffff0000, v197\n"
      "v_add_u32_dpp v35, v35, v35 quad_perm:[2,3,0,1] row_mask:0xf bank_mask:0xf\n"
      "v_lshlrev_b32 v116, 16, v198\n"
      "v_and_b32 v117, 0xffff0000, v198\n"
      "v_add_u32_dpp v35, v35, v35 row_half_mirror row_mask:0xf bank_mask:0xf\n"
      "v_lshlrev_b32 v118, 16, v199\n"
      "v_and_b32 v119, 0xffff0000, v199\n"
      "v_sub_u32 v35, s30, v35\n"
      "v_pk_add_f32 v[104:105], v[112:113], v[96:97] neg_lo:[0,1] neg_hi:[0,1]\n"
      "v_pk_add_f32 v[106:107], v[114:115], v[98:99] neg_lo:[0,1] neg_hi:[0,1]\n"
      "buffer_store_dword v35, v30, s[32:35], s81 offen sc1\n"
      "buffer_store_dwordx2 v[52:53], v51, s[32:35], s88 offen sc1\n"
      "v_pk_add_f32 v[108:109], v[116:117], v[100:101] neg_lo:[0,1] neg_hi:[0,1]\n"
      "v_pk_add_f32 v[110:111], v[118:119], v[102:103] neg_lo:[0,1] neg_hi:[0,1]\n"
      "v_cvt_pk_bf16_f32 v104, 0, v104\n"
      "v_cvt_pk_bf16_f32 v105, 0, v105\n"
      "global_load_dwordx4 a[40:43], v34, s[8:9] offset:-2048 sc1\n"
      "v_cvt_pk_bf16_f32 v106, 0, v106\n"
      "v_cvt_pk_bf16_f32 v107, 0, v107\n"
      "v_cvt_pk_bf16_f32 v108, 0, v108\n"
      "v_cvt_pk_bf16_f32 v109, 0, v109\n"
      "global_load_dwordx4 a[44:47], v34, s[8:9] offset:-1024 sc1\n"
      "v_cvt_pk_bf16_f32 v110, 0, v110\n"
      "v_cvt_pk_bf16_f32 v111, 0, v111\n"
      "v_pk_mul_f32 v[104:105], v[104:105], v[208:209]\n"
      "v_pk_mul_f32 v[106:107], v[106:107], v[212:213]\n"
      "global_load_dwordx4 a[48:51], v34, s[8:9] offset:0 sc1\n"
      "v_pk_mul_f32 v[108:109], v[108:109], v[216:217]\n"
      "v_pk_mul_f32 v[110:111], v[110:111], v[220:221]\n"
      "v_cvt_pk_bf16_f32 v104, 0, v104\n"
      "v_cvt_pk_bf16_f32 v105, 0, v105\n"
      "global_load_dwordx4 a[52:55], v34, s[8:9] offset:1024 sc1\n"
      "v_cvt_pk_bf16_f32 v106, 0, v106\n"
      "v_cvt_pk_bf16_f32 v107, 0, v107\n"
      "v_cvt_pk_bf16_f32 v108, 0, v108\n"
      "v_cvt_pk_bf16_f32 v109, 0, v109\n"
      "global_load_dwordx4 a[56:59], v34, s[8:9] offset:2048 sc1\n"
      "v_cvt_pk_bf16_f32 v110, 0, v110\n"
      "v_cvt_pk_bf16_f32 v111, 0, v111\n"
      "v_pk_mul_f32 v[104:105], v[104:105], s[50:51] op_sel_hi:[1,0]\n"
      "v_pk_mul_f32 v[106:107], v[106:107], s[50:51] op_sel_hi:[1,0]\n"
      "global_load_dwordx4 a[60:63], v34, s[8:9] offset:3072 sc1\n"
      "v_pk_mul_f32 v[108:109], v[108:109], s[50:51] op_sel_hi:[1,0]\n"
      "v_pk_mul_f32 v[110:111], v[110:111], s[50:51] op_sel_hi:[1,0]\n"
      "v_cvt_pk_bf16_f32 v104, 0, v104\n"
      "v_cvt_pk_bf16_f32 v105, 0, v105\n"
      "v_add_u32 v34, 20480, v7\n"
      "global_load_dwordx4 a[64:67], v34, s[8:9] offset:-4096 sc1\n"
      "v_cvt_pk_bf16_f32 v106, 0, v106\n"
      "v_cvt_pk_bf16_f32 v107, 0, v107\n"
      "v_cvt_pk_bf16_f32 v108, 0, v108\n"
      "v_cvt_pk_bf16_f32 v109, 0, v109\n"
      "global_load_dwordx4 a[68:71], v34, s[8:9] offset:-3072 sc1\n"
      "v_cvt_pk_bf16_f32 v110, 0, v110\n"
      "v_cvt_pk_bf16_f32 v111, 0, v111\n"
      "v_pk_add_f32 v[104:105], v[104:105], s[48:49] op_sel_hi:[1,0]\n"
      "v_pk_add_f32 v[106:107], v[106:107], s[48:49] op_sel_hi:[1,0]\n"
      "global_load_dwordx4 a[72:75], v34, s[8:9] offset:-2048 sc1\n"
      "v_pk_add_f32 v[108:109], v[108:109], s[48:49] op_sel_hi:[1,0]\n"
      "v_pk_add_f32 v[110:111], v[110:111], s[48:49] op_sel_hi:[1,0]\n"
      "v_cvt_pk_bf16_f32 v104, 0, v104\n"
      "v_cvt_pk_bf16_f32 v105, 0, v105\n"
      "global_load_dwordx4 a[76:79], v34, s[8:9] offset:-1024 sc1\n"
      "v_cvt_pk_bf16_f32 v106, 0, v106\n"
      "v_cvt_pk_bf16_f32 v107, 0, v107\n"
      "v_cvt_pk_bf16_f32 v108, 0, v108\n"
      "v_cvt_pk_bf16_f32 v109, 0, v109\n"
      "global_load_dwordx4 a[80:83], v34, s[8:9] offset:0 sc1\n"
      "v_cvt_pk_bf16_f32 v110, 0, v110\n"
      "v_cvt_pk_bf16_f32 v111, 0, v111\n"
      "v_and_b32 v120, 0xffff0000, v239\n"
      "v_lshlrev_b32 v121, 16, v239\n"
      "global_load_dwordx4 a[84:87], v34, s[8:9] offset:1024 sc1\n"
      "v_and_b32 v122, 0xffff0000, v238\n"
      "v_lshlrev_b32 v123, 16, v238\n"
      "v_and_b32 v124, 0xffff0000, v237\n"
      "v_lshlrev_b32 v125, 16, v237\n"
      "global_load_dwordx4 a[88:91], v34, s[8:9] offset:2048 sc1\n"
      "v_and_b32 v126, 0xffff0000, v236\n"
      "v_lshlrev_b32 v127, 16, v236\n"
      "v_pk_mul_f32 v[120:121], v[120:121], s[14:15] op_sel:[0,1] op_sel_hi:[1,1]\n"
      "v_pk_mul_f32 v[122:123], v[122:123], s[14:15] op_sel:[0,1] op_sel_hi:[1,1]\n"
      "global_load_dwordx4 a[92:95], v34, s[8:9] offset:3072 sc1\n"
      "v_pk_mul_f32 v[124:125], v[124:125], s[14:15] op_sel:[0,1] op_sel_hi:[1,1]\n"
      "v_pk_mul_f32 v[126:127], v[126:127], s[14:15] op_sel:[0,1] op_sel_hi:[1,1]\n"
      "v_cvt_pk_bf16_f32 v120, 0, v120\n"
      "v_cvt_pk_bf16_f32 v121, 0, v121\n"
      "s_add_u32 m0, s73, 0\n"
      "v_add_u32 v43, 0, v40\n"
      "global_load_lds_dwordx4 v43, s[4:5] offset:0 sc1\n"
      "v_cvt_pk_bf16_f32 v122, 0, v122\n"
      "v_cvt_pk_bf16_f32 v123, 0, v123\n"
      "v_cvt_pk_bf16_f32 v124, 0, v124\n"
      "v_cvt_pk_bf16_f32 v125, 0, v125\n"
      "global_load_lds_dwordx4 v43, s[4:5] offset:1024 sc1\n"
      "v_cvt_pk_bf16_f32 v126, 0, v126\n"
      "v_cvt_pk_bf16_f32 v127, 0, v127\n"
      "v_pk_add_f32 v[80:81], v[80:81], v[88:89]\n"
      "v_pk_add_f32 v[82:83], v[82:83], v[90:91]\n"
      "global_load_lds_dwordx4 v43, s[4:5] offset:2048 sc1\n"
      "v_pk_add_f32 v[84:85], v[84:85], v[92:93]\n"
      "v_pk_add_f32 v[86:87], v[86:87], v[94:95]\n"
      "v_cvt_pk_bf16_f32 v80, 0, v80\n"
      "v_cvt_pk_bf16_f32 v81, 0, v81\n"
      "global_load_lds_dwordx4 v43, s[4:5] offset:3072 sc1\n"
      "v_cvt_pk_bf16_f32 v82, 0, v82\n"
      "v_cvt_pk_bf16_f32 v83, 0, v83\n"
      "v_cvt_pk_bf16_f32 v84, 0, v84\n"
      "v_cvt_pk_bf16_f32 v85, 0, v85\n"
      "s_mov_b32 s47, 0\n"
      "buffer_load_dword v32, v5, s[32:35], s47 offen sc1\n"
      "v_cvt_pk_bf16_f32 v86, 0, v86\n"
      "v_cvt_pk_bf16_f32 v87, 0, v87\n"
      "v_pk_add_f32 v[120:121], v[120:121], v[80:81]\n"
      "v_pk_add_f32 v[122:123], v[122:123], v[82:83]\n"
      "s_add_u32 m0, s73, 16384\n"
      "v_add_u32 v43, 81920, v40\n"
      "global_load_lds_dwordx4 v43, s[4:5] offset:0 sc1\n"
      "v_pk_add_f32 v[124:125], v[124:125], v[84:85]\n"
      "v_pk_add_f32 v[126:127], v[126:127], v[86:87]\n"
      "v_cvt_pk_bf16_f32 v120, 0, v120\n"
      "v_cvt_pk_bf16_f32 v121, 0, v121\n"
      "global_load_lds_dwordx4 v43, s[4:5] offset:1024 sc1\n"
      "v_cvt_pk_bf16_f32 v122, 0, v122\n"
      "v_cvt_pk_bf16_f32 v123, 0, v123\n"
      "v_cvt_pk_bf16_f32 v124, 0, v124\n"
      "v_cvt_pk_bf16_f32 v125, 0, v125\n"
      "global_load_lds_dwordx4 v43, s[4:5] offset:2048 sc1\n"
      "v_cvt_pk_bf16_f32 v126, 0, v126\n"
      "v_cvt_pk_bf16_f32 v127, 0, v127\n"
      "v_pk_mul_f32 v[104:105], v[104:105], v[120:121]\n"
      "v_pk_mul_f32 v[106:107], v[106:107], v[122:123]\n"
      "global_load_lds_dwordx4 v43, s[4:5] offset:3072 sc1\n"
      "v_pk_mul_f32 v[108:109], v[108:109], v[124:125]\n"
      "v_pk_mul_f32 v[110:111], v[110:111], v[126:127]\n"
      "v_cvt_pk_bf16_f32 v104, 0, v104\n"
      "v_cvt_pk_bf16_f32 v105, 0, v105\n"
      "s_mov_b32 s47, 32\n"
      "buffer_load_dword v4, v5, s[32:35], s47 offen sc1\n"
      "v_cvt_pk_bf16_f32 v106, 0, v106\n"
      "v_cvt_pk_bf16_f32 v107, 0, v107\n"
      "v_cvt_pk_bf16_f32 v108, 0, v108\n"
      "v_cvt_pk_bf16_f32 v109, 0, v109\n"
      "v_cvt_pk_bf16_f32 v110, 0, v110\n"
      "v_cvt_pk_bf16_f32 v111, 0, v111\n"
      "v_pk_mul_f32 v[104:105], v[104:105], s[52:53] op_sel_hi:[1,0]\n"
      "v_pk_mul_f32 v[106:107], v[106:107], s[52:53] op_sel_hi:[1,0]\n"
      "v_pk_mul_f32 v[108:109], v[108:109], s[52:53] op_sel_hi:[1,0]\n"
      "v_pk_mul_f32 v[110:111], v[110:111], s[52:53] op_sel_hi:[1,0]\n"
      "v_cvt_pk_bf16_f32 v104, 0, v104\n"
      "v_cvt_pk_bf16_f32 v105, 0, v105\n"
      "v_cvt_pk_bf16_f32 v106, 0, v106\n"
      "v_cvt_pk_bf16_f32 v107, 0, v107\n"
      "v_cvt_pk_bf16_f32 v108, 0, v108\n"
      "v_cvt_pk_bf16_f32 v109, 0, v109\n"
      "v_cvt_pk_bf16_f32 v110, 0, v110\n"
      "v_cvt_pk_bf16_f32 v111, 0, v111\n"
      "v_pk_add_f32 v[104:105], v[104:105], v[112:113]\n"
      "v_pk_add_f32 v[106:107], v[106:107], v[114:115]\n"
      "v_pk_add_f32 v[108:109], v[108:109], v[116:117]\n"
      "v_pk_add_f32 v[110:111], v[110:111], v[118:119]\n"
      "v_cvt_pk_bf16_f32 v132, v104, v105\n"
      "v_cvt_pk_bf16_f32 v133, v106, v107\n"
      "v_cvt_pk_bf16_f32 v134, v108, v109\n"
      "v_cvt_pk_bf16_f32 v135, v110, v111\n"
      "buffer_store_dwordx4 v[132:135], v29, s[32:35], s43 offen sc1\n"
      "v_add3_u32 v35, v132, v133, v134\n"
      "v_add_u32 v35, v35, v135\n"
      "s_nop 1\n"
      "v_add_u32_dpp v35, v35, v35 quad_perm:[1,0,3,2] row_mask:0xf bank_mask:0xf\n"
      "s_nop 1\n"
      "v_add_u32_dpp v35, v35, v35 quad_perm:[2,3,0,1] row_mask:0xf bank_mask:0xf\n"
      "s_nop 1\n"
      "v_add_u32_dpp v35, v35, v35 row_half_mirror row_mask:0xf bank_mask:0xf\n"
      "v_sub_u32 v35, s30, v35\n"
      "buffer_store_dword v35, v30, s[32:35], s45 offen sc1\n"
      "v_xor_b32 v33, 32, v6\n"
      "v_xor_b32 v34, 64, v6\n"
      "v_xor_b32 v35, 96, v6\n"
      "v_mov_b32 v60, 0x3f803f80\n"
      "v_mov_b32 v61, 0x3f803f80\n"
      "v_mov_b32 v62, 0x3f803f80\n"
      "v_mov_b32 v63, 0x3f803f80\n"
      "v_add_u32 v45, 28672, v7\n"
      "s_waitcnt vmcnt(7)\n"
      "ds_read_b128 v[96:99], v40 offset:0\n"
      "ds_read_b128 v[100:103], v40 offset:1024\n"
      "ds_read_b128 v[104:107], v40 offset:2048\n"
      "ds_read_b128 v[108:111], v40 offset:3072\n"
      "s_waitcnt lgkmcnt(3)\n"
      "v_add3_u32 v32, v32, v96, v97\n"
      "v_add3_u32 v32, v32, v98, v99\n"
      "s_waitcnt lgkmcnt(2)\n"
      "v_add3_u32 v32, v32, v100, v101\n"
      "v_add3_u32 v32, v32, v102, v103\n"
      "s_waitcnt lgkmcnt(1)\n"
      "v_add3_u32 v32, v32, v104, v105\n"
      "v_add3_u32 v32, v32, v106, v107\n"
      "s_waitcnt lgkmcnt(0)\n"
      "v_add3_u32 v32, v32, v108, v109\n"
      "v_add3_u32 v32, v32, v110, v111\n"
      "s_nop 1\n"
      "v_add_u32_dpp v32, v32, v32 quad_perm:[1,0,3,2] row_mask:0xf bank_mask:0xf\n"
      "s_nop 1\n"
      "v_add_u32_dpp v32, v32, v32 quad_perm:[2,3,0,1] row_mask:0xf bank_mask:0xf\n"
      "s_nop 1\n"
      "v_add_u32_dpp v32, v32, v32 row_half_mirror row_mask:0xf bank_mask:0xf\n"
      "v_cmp_ne_u32 vcc, s71, v32\n"
      "s_cbranch_vccnz L_sx0_%=\n"
      "L_bx0_%=:\n"
      "s_barrier\n"
      "ds_read_b64_tr_b16 v[64:65], v6 offset:0\n"
      "ds_read_b64_tr_b16 v[66:67], v6 offset:2048\n"
      "ds_read_b64_tr_b16 v[68:69], v33 offset:0\n"
      "ds_read_b64_tr_b16 v[70:71], v33 offset:2048\n"
      "ds_read_b64_tr_b16 v[72:73], v34 offset:0\n"
      "ds_read_b64_tr_b16 v[74:75], v34 offset:2048\n"
      "ds_read_b64_tr_b16 v[76:77], v35 offset:0\n"
      "ds_read_b64_tr_b16 v[78:79], v35 offset:2048\n"
      "ds_read_b64_tr_b16 v[80:81], v6 offset:4096\n"
      "ds_read_b64_tr_b16 v[82:83], v6 offset:6144\n"
      "ds_read_b64_tr_b16 v[84:85], v33 offset:4096\n"
      "ds_read_b64_tr_b16 v[86:87], v33 offset:6144\n"
      "ds_read_b64_tr_b16 v[88:89], v34 offset:4096\n"
      "ds_read_b64_tr_b16 v[90:91], v34 offset:6144\n"
      "ds_read_b64_tr_b16 v[92:93], v35 offset:4096\n"
      "ds_read_b64_tr_b16 v[94:95], v35 offset:6144\n"
      "s_waitcnt lgkmcnt(0)\n"
      "v_mfma_f32_16x16x32_bf16 v[128:131], a[0:3], v[64:67], 0\n"
      "global_load_dwordx4 a[96:99], v45, s[8:9] offset:-4096 sc1\n"
      "s_add_u32 m0, s73, 32768\n"
      "v_add_u32 v43, 16384, v40\n"
      "v_mfma_f32_16x16x32_bf16 v[144:147], a[4:7], v[64:67], 0\n"
      "ds_read_b64_tr_b16 v[96:97], v6 offset:8192\n"
      "s_waitcnt vmcnt(3)\n"
      "v_mfma_f32_16x16x32_bf16 v[160:163], a[8:11], v[64:67], 0\n"
      "global_load_lds_dwordx4 v43, s[4:5] offset:0 sc1\n"
      "s_lshl_b32 s42, s60, 5\n"
      "v_mfma_f32_16x16x32_bf16 v[16:19], v[60:63], v[64:67], 0\n"
      "ds_read_b128 v[248:251], v40 offset:16384\n"
      "v_xor_b32 v36, s42, v6\n"
      "v_mfma_f32_16x16x32_bf16 v[20:23], v[64:67], v[64:67], 0\n"
      "ds_read_b128 v[252:255], v40 offset:17408\n"
      "v_xor_b32 v37, 64, v36\n"
      "v_mfma_f32_16x16x32_bf16 v[132:135], a[0:3], v[68:71], 0\n"
      "ds_read_b64_tr_b16 v[98:99], v6 offset:10240\n"
      "v_xor_b32 v38, 32, v36\n"
      "v_mfma_f32_16x16x32_bf16 v[148:151], a[4:7], v[68:71], 0\n"
      "global_load_lds_dwordx4 v43, s[4:5] offset:1024 sc1\n"
      "ds_read_b64_tr_b16 v[100:101], v33 offset:8192\n"
      "v_xor_b32 v39, 0x60, v36\n"
      "v_mfma_f32_16x16x32_bf16 v[164:167], a[8:11], v[68:71], 0\n"
      "ds_read_b64_tr_b16 v[102:103], v33 offset:10240\n"
      "v_lshrrev_b32 v54, 8, v7\n"
      "v_mfma_f32_16x16x32_bf16 v[136:139], a[0:3], v[72:75], 0\n"
      "ds_read_b64_tr_b16 v[104:105], v34 offset:8192\n"
      "s_waitcnt lgkmcnt(4)\n"
      "v_mfma_f32_16x16x32_bf16 v[152:155], a[4:7], v[72:75], 0\n"
      "ds_read_b64_tr_b16 v[106:107], v34 offset:10240\n"
      "v_add3_u32 v4, v4, v248, v249\n"
      "v_add3_u32 v4, v4, v250, v251\n"
      "v_mfma_f32_16x16x32_bf16 v[168:171], a[8:11], v[72:75], 0\n"
      "global_load_lds_dwordx4 v43, s[4:5] offset:2048 sc1\n"
      "v_add3_u32 v4, v4, v252, v253\n"
      "v_mfma_f32_16x16x32_bf16 v[140:143], a[0:3], v[76:79], 0\n"
      "ds_read_b64_tr_b16 v[108:109], v35 offset:8192\n"
      "v_add3_u32 v4, v4, v254, v255\n"
      "v_mfma_f32_16x16x32_bf16 v[156:159], a[4:7], v[76:79], 0\n"
      "ds_read_b64_tr_b16 v[110:111], v35 offset:10240\n"
      "v_bfe_u32 v2, v7, 4, 4\n"
      "v_mfma_f32_16x16x32_bf16 v[172:175], a[8:11], v[76:79], 0\n"
      "ds_read_b128 v[248:251], v40 offset:18432\n"
      "ds_read_b128 v[252:255], v40 offset:19456\n"
      "ds_read_b64_tr_b16 v[112:113], v6 offset:12288\n"
      "v_min_u32 v54, 2, v54\n"
      "v_mfma_f32_16x16x32_bf16 v[128:131], a[12:15], v[80:83], v[128:131]\n"
      "global_load_lds_dwordx4 v43, s[4:5] offset:3072 sc1\n"
      "ds_read_b64_tr_b16 v[114:115], v6 offset:14336\n"
      "s_mov_b32 s47, 16384\n"
      "v_mfma_f32_16x16x32_bf16 v[144:147], a[16:19], v[80:83], v[144:147]\n"
      "ds_read_b64_tr_b16 v[116:117], v33 offset:12288\n"
      "v_min_u32 v2, 11, v2\n"
      "v_mfma_f32_16x16x32_bf16 v[160:163], a[20:23], v[80:83], v[160:163]\n"
      "buffer_load_dword v55, v5, s[32:35], s47 offen sc1\n"
      "s_add_u32 m0, s73, 49152\n"
      "v_add_u32 v43, 32768, v40\n"
      "v_mfma_f32_16x16x32_bf16 v[16:19], v[60:63], v[80:83], v[16:19]\n"
      "ds_read_b64_tr_b16 v[118:119], v33 offset:14336\n"
      "s_waitcnt lgkmcnt(4)\n"
      "v_mfma_f32_16x16x32_bf16 v[20:23], v[80:83], v[80:83], v[20:23]\n"
      "global_load_lds_dwordx4 v43, s[4:5] offset:0 sc1\n"
      "ds_read_b64_tr_b16 v[120:121], v34 offset:12288\n"
      "v_add3_u32 v4, v4, v248, v249\n"
      "v_add3_u32 v4, v4, v250, v251\n"
      "v_add3_u32 v4, v4, v252, v253\n"
      "v_add3_u32 v4, v4, v254, v255\n"
      "v_mfma_f32_16x16x32_bf16 v[132:135], a[12:15], v[84:87], v[132:135]\n"
      "ds_read_b64_tr_b16 v[122:123], v34 offset:14336\n"
      "v_mad_u32_u24 v54, v54, 48, v2\n"
      "v_mfma_f32_16x16x32_bf16 v[148:151], a[16:19], v[84:87], v[148:151]\n"
      "ds_read_b64_tr_b16 v[124:125], v35 offset:12288\n"
      "v_add_u32_dpp v4, v4, v4 quad_perm:[1,0,3,2] row_mask:0xf bank_mask:0xf\n"
      "v_mfma_f32_16x16x32_bf16 v[164:167], a[20:23], v[84:87], v[164:167]\n"
      "ds_read_b64_tr_b16 v[126:127], v35 offset:14336\n"
      "v_lshl_add_u32 v54, v54, 3, s64\n"
      "v_mfma_f32_16x16x32_bf16 v[136:139], a[12:15], v[88:91], v[136:139]\n"
      "global_load_lds_dwordx4 v43, s[4:5] offset:1024 sc1\n"
      "v_add_u32_dpp v4, v4, v4 quad_perm:[2,3,0,1] row_mask:0xf bank_mask:0xf\n"
      "v_mfma_f32_16x16x32_bf16 v[152:155], a[16:19], v[88:91], v[152:155]\n"
      "v_mfma_f32_16x16x32_bf16 v[168:171], a[20:23], v[88:91], v[168:171]\n"
      "v_add_u32_dpp v4, v4, v4 row_half_mirror row_mask:0xf bank_mask:0xf\n"
      "v_cmp_ne_u32 vcc, s71, v4\n"
      "v_mfma_f32_16x16x32_bf16 v[140:143], a[12:15], v[92:95], v[140:143]\n"
      "v_mfma_f32_16x16x32_bf16 v[156:159], a[16:19], v[92:95], v[156:159]\n"
      "global_load_lds_dwordx4 v43, s[4:5] offset:2048 sc1\n"
      "s_cbranch_vccnz L_sx1_%=\n"
      "L_bx1_%=:\n"
      "v_mfma_f32_16x16x32_bf16 v[172:175], a[20:23], v[92:95], v[172:175]\n"
      "s_waitcnt lgkmcnt(0)\n"
      "s_barrier\n"
      "v_mfma_f32_16x16x32_bf16 v[128:131], a[24:27], v[96:99], v[128:131]\n"
      "ds_read_b64_tr_b16 v[64:65], v36 offset:16384\n"
      "v_and_b32 v3, 0x1f0, v7\n"
      "v_mfma_f32_16x16x32_bf16 v[144:147], a[28:31], v[96:99], v[144:147]\n"
      "ds_read_b64_tr_b16 v[66:67], v36 offset:18432\n"
      "v_min_u32 v3, 0x170, v3\n"
      "v_mfma_f32_16x16x32_bf16 v[160:163], a[32:35], v[96:99], v[160:163]\n"
      "global_load_dwordx2 v[58:59], v54, s[26:27] sc1\n"
      "v_lshrrev_b32 v2, 9, v7\n"
      "v_mfma_f32_16x16x32_bf16 v[16:19], v[60:63], v[96:99], v[16:19]\n"
      "ds_read_b64_tr_b16 v[68:69], v37 offset:16384\n"
      "v_mul_u32_u24 v2, 0x3c000, v2\n"
      "v_mfma_f32_16x16x32_bf16 v[20:23], v[96:99], v[96:99], v[20:23]\n"
      "global_load_lds_dwordx4 v43, s[4:5] offset:3072 sc1\n"
      "v_add3_u32 v53, v2, v3, s65\n"
      "v_mfma_f32_16x16x32_bf16 v[132:135], a[24:27], v[100:103], v[132:135]\n"
      "ds_read_b64_tr_b16 v[70:71], v37 offset:18432\n"
      "v_add_u32 v2, 0x40, v3\n"
      "v_mfma_f32_16x16x32_bf16 v[148:151], a[28:31], v[100:103], v[148:151]\n"
      "ds_read_b64_tr_b16 v[72:73], v38 offset:16384\n"
      "v_lshrrev_b32 v2, 8, v2\n"
      "v_mfma_f32_16x16x32_bf16 v[164:167], a[32:35], v[100:103], v[164:167]\n"
      "ds_read_b64_tr_b16 v[74:75], v38 offset:18432\n"
      "v_mul_u32_u24 v2, 0x2f40, v2\n"
      "v_mfma_f32_16x16x32_bf16 v[136:139], a[24:27], v[104:107], v[136:139]\n"
      "global_load_dwordx4 a[100:103], v45, s[8:9] offset:-3072 sc1\n"
      "v_add_u32 v53, v53, v2\n"
      "v_mfma_f32_16x16x32_bf16 v[152:155], a[28:31], v[104:107], v[152:155]\n"
      "ds_read_b64_tr_b16 v[76:77], v39 offset:16384\n"
      "s_mul_i32 s40, s60, 384\n"
      "v_mfma_f32_16x16x32_bf16 v[168:171], a[32:35], v[104:107], v[168:171]\n"
      "ds_read_b64_tr_b16 v[78:79], v39 offset:18432\n"
      "s_add_u32 s40, s40, 160768\n"
      "v_mfma_f32_16x16x32_bf16 v[140:143], a[24:27], v[108:111], v[140:143]\n"
      "ds_read_b64_tr_b16 v[80:81], v36 offset:20480\n"
      "v_add_u32 v57, s40, v3\n"
      "v_mfma_f32_16x16x32_bf16 v[156:159], a[28:31], v[108:111], v[156:159]\n"
      "global_load_dwordx4 a[104:107], v45, s[8:9] offset:-2048 sc1\n"
      "v_lshrrev_b32 v2, 4, v7\n"
      "v_mfma_f32_16x16x32_bf16 v[172:175], a[32:35], v[108:111], v[172:175]\n"
      "ds_read_b64_tr_b16 v[82:83], v36 offset:22528\n"
      "v_and_b32 v2, 0x30, v2\n"
      "v_mfma_f32_16x16x32_bf16 v[128:131], a[36:39], v[112:115], v[128:131]\n"
      "ds_read_b64_tr_b16 v[8:9], v6 offset:16384\n"
      "v_add_u32 v52, s40, v2\n"
      "v_mfma_f32_16x16x32_bf16 v[144:147], a[40:43], v[112:115], v[144:147]\n"
      "ds_read_b64_tr_b16 v[84:85], v37 offset:20480\n"
      "s_mul_i32 s40, s60, 9216\n"
      "v_mfma_f32_16x16x32_bf16 v[160:163], a[44:47], v[112:115], v[160:163]\n"
      "global_load_dwordx4 a[108:111], v45, s[8:9] offset:-1024 sc1\n"
      "s_add_u32 s40, s40, 122880\n"
      "v_mfma_f32_16x16x32_bf16 v[16:19], v[60:63], v[112:115], v[16:19]\n"
      "ds_read_b64_tr_b16 v[86:87], v37 offset:22528\n"
      "ds_read_b64_tr_b16 v[10:11], v6 offset:18432\n"
      "s_lshl_b32 s41, s60, 13\n"
      "v_mfma_f32_16x16x32_bf16 v[20:23], v[112:115], v[112:115], v[20:23]\n"
      "ds_read_b64_tr_b16 v[88:89], v38 offset:20480\n"
      "s_add_u32 s41, s41, 65536\n"
      "v_mfma_f32_16x16x32_bf16 v[132:135], a[36:39], v[116:119], v[132:135]\n"
      "ds_read_b64_tr_b16 v[90:91], v38 offset:22528\n"
      "ds_read_b64_tr_b16 v[12:13], v6 offset:20480\n"
      "s_mov_b32 s47, 32768\n"
      "v_mfma_f32_16x16x32_bf16 v[148:151], a[40:43], v[116:119], v[148:151]\n"
      "buffer_load_dword v56, v5, s[32:35], s47 offen sc1\n"
      "ds_read_b64_tr_b16 v[92:93], v39 offset:20480\n"
      "s_add_u32 m0, s73, 0\n"
      "v_add_u32 v43, 49152, v40\n"
      "v_mfma_f32_16x16x32_bf16 v[164:167], a[44:47], v[116:119], v[164:167]\n"
      "ds_read_b64_tr_b16 v[94:95], v39 offset:22528\n"
      "v_mfma_f32_16x16x32_bf16 v[136:139], a[36:39], v[120:123], v[136:139]\n"
      "global_load_lds_dwordx4 v43, s[4:5] offset:0 sc1\n"
      "ds_read_b64_tr_b16 v[14:15], v6 offset:22528\n"
      "v_mfma_f32_16x16x32_bf16 v[152:155], a[40:43], v[120:123], v[152:155]\n"
      "v_mfma_f32_16x16x32_bf16 v[168:171], a[44:47], v[120:123], v[168:171]\n"
      "v_mfma_f32_16x16x32_bf16 v[140:143], a[36:39], v[124:127], v[140:143]\n"
      "v_mfma_f32_16x16x32_bf16 v[156:159], a[40:43], v[124:127], v[156:159]\n"
      "global_load_dwordx4 a[112:115], v45, s[8:9] offset:0 sc1\n"
      "v_mfma_f32_16x16x32_bf16 v[172:175], a[44:47], v[124:127], v[172:175]\n"
      "s_waitcnt lgkmcnt(0)\n"
      "v_mfma_f32_16x16x32_bf16 v[24:27], v[60:63], v[8:11], 0\n"
      "ds_read_b64_tr_b16 v[96:97], v36 offset:24576\n"
      "s_lshl_b32 s42, s60, 10\n"
      "v_mfma_f32_16x16x32_bf16 v[28:31], v[8:11], v[8:11], 0\n"
      "ds_read_b64_tr_b16 v[98:99], v36 offset:26624\n"
      "s_waitcnt vmcnt(11)\n"
      "v_mfma_f32_16x16x32_bf16 v[24:27], v[60:63], v[12:15], v[24:27]\n"
      "global_load_lds_dwordx4 v43, s[4:5] offset:1024 sc1\n"
      "s_add_u32 s43, s42, 98304\n"
      "v_mfma_f32_16x16x32_bf16 v[28:31], v[12:15], v[12:15], v[28:31]\n"
      "ds_read_b128 v[248:251], v40 offset:32768\n"
      "v_add_u32 v47, s43, v7\n"
      "v_mfma_f32_16x16x32_bf16 v[176:179], a[0:3], v[64:67], 0\n"
      "ds_read_b128 v[252:255], v40 offset:33792\n"
      "v_bfe_u32 v2, v7, 8, 1\n"
      "v_mfma_f32_16x16x32_bf16 v[184:187], a[4:7], v[64:67], 0\n"
      "global_load_dwordx4 v[224:227], v53, s[26:27] sc1\n"
      "v_add_u32 v53, 0x6000, v53\n"
      "v_mfma_f32_16x16x32_bf16 v[192:195], a[8:11], v[64:67], 0\n"
      "global_load_dwordx4 v[228:231], v53, s[26:27] sc1\n"
      "ds_read_b64_tr_b16 v[100:101], v37 offset:24576\n"
      "v_add_u32 v53, 0x6000, v53\n"
      "v_mfma_f32_16x16x32_bf16 v[180:183], a[0:3], v[68:71], 0\n"
      "global_load_dwordx4 v[232:235], v53, s[26:27] sc1\n"
      "ds_read_b64_tr_b16 v[102:103], v37 offset:26624\n"
      "v_add_u32 v53, 0x6000, v53\n"
      "v_mfma_f32_16x16x32_bf16 v[188:191], a[4:7], v[68:71], 0\n"
      "global_load_dwordx4 v[236:239], v53, s[26:27] sc1\n"
      "ds_read_b64_tr_b16 v[104:105], v38 offset:24576\n"
      "v_add_u32 v53, 0x6000, v53\n"
      "v_mfma_f32_16x16x32_bf16 v[196:199], a[8:11], v[68:71], 0\n"
      "global_load_dwordx4 v[240:243], v53, s[26:27] sc1\n"
      "s_waitcnt lgkmcnt(3)\n"
      "v_add3_u32 v55, v55, v248, v249\n"
      "v_add3_u32 v55, v55, v250, v251\n"
      "v_add_u32 v53, 0x6000, v53\n"
      "v_mfma_f32_16x16x32_bf16 v[200:203], a[0:3], v[72:75], 0\n"
      "global_load_dwordx4 v[244:247], v53, s[26:27] sc1\n"
      "ds_read_b64_tr_b16 v[106:107], v38 offset:26624\n"
      "v_add3_u32 v55, v55, v252, v253\n"
      "v_mfma_f32_16x16x32_bf16 v[208:211], a[4:7], v[72:75], 0\n"
      "ds_read_b64_tr_b16 v[108:109], v39 offset:24576\n"
      "v_add3_u32 v55, v55, v254, v255\n"
      "v_mfma_f32_16x16x32_bf16 v[216:219], a[8:11], v[72:75], 0\n"
      "ds_read_b64_tr_b16 v[110:111], v39 offset:26624\n"
      "v_xor_b32 v2, s60, v2\n"
      "v_mfma_f32_16x16x32_bf16 v[204:207], a[0:3], v[76:79], 0\n"
      "ds_read_b128 v[248:251], v40 offset:34816\n"
      "ds_read_b128 v[252:255], v40 offset:35840\n"
      "ds_read_b64_tr_b16 v[112:113], v36 offset:28672\n"
      "v_lshl_add_u32 v46, v2, 2, v47\n"
      "v_mfma_f32_16x16x32_bf16 v[212:215], a[4:7], v[76:79], 0\n"
      "global_load_dwordx4 a[116:119], v45, s[8:9] offset:1024 sc1\n"
      "ds_read_b64_tr_b16 v[114:115], v36 offset:30720\n"
      "v_add_u32 v48, s61, v7\n"
      "v_mfma_f32_16x16x32_bf16 v[220:223], a[8:11], v[76:79], 0\n"
      "ds_read_b64_tr_b16 v[8:9], v6 offset:24576\n"
      "v_lshrrev_b32 v42, 1, v7\n"
      "v_mfma_f32_16x16x32_bf16 v[176:179], a[12:15], v[80:83], v[176:179]\n"
      "ds_read_b64_tr_b16 v[116:117], v37 offset:28672\n"
      "v_add_u32 v49, s62, v42\n"
      "v_mfma_f32_16x16x32_bf16 v[184:187], a[16:19], v[80:83], v[184:187]\n"
      "ds_read_b64_tr_b16 v[118:119], v37 offset:30720\n"
      "ds_read_b64_tr_b16 v[10:11], v6 offset:26624\n"
      "s_waitcnt lgkmcnt(6)\n"
      "v_mfma_f32_16x16x32_bf16 v[192:195], a[20:23], v[80:83], v[192:195]\n"
      "global_load_lds_dwordx4 v43, s[4:5] offset:2048 sc1\n"
      "ds_read_b64_tr_b16 v[120:121], v38 offset:28672\n"
      "v_add3_u32 v55, v55, v248, v249\n"
      "v_add3_u32 v55, v55, v250, v251\n"
      "v_add3_u32 v55, v55, v252, v253\n"
      "v_add3_u32 v55, v55, v254, v255\n"
      "v_mfma_f32_16x16x32_bf16 v[180:183], a[12:15], v[84:87], v[180:183]\n"
      "ds_read_b64_tr_b16 v[122:123], v38 offset:30720\n"
      "ds_read_b64_tr_b16 v[12:13], v6 offset:28672\n"
      "s_lshl_b32 s43, s60, 7\n"
      "v_mfma_f32_16x16x32_bf16 v[188:191], a[16:19], v[84:87], v[188:191]\n"
      "ds_read_b64_tr_b16 v[124:125], v39 offset:28672\n"
      "v_add_u32_dpp v55, v55, v55 quad_perm:[1,0,3,2] row_mask:0xf bank_mask:0xf\n"
      "v_mfma_f32_16x16x32_bf16 v[196:199], a[20:23], v[84:87], v[196:199]\n"
      "ds_read_b64_tr_b16 v[126:127], v39 offset:30720\n"
      "s_add_u32 s43, s43, 162304\n"
      "v_mfma_f32_16x16x32_bf16 v[200:203], a[12:15], v[88:91], v[200:203]\n"
      "global_load_lds_dwordx4 v43, s[4:5] offset:3072 sc1\n"
      "ds_read_b64_tr_b16 v[14:15], v6 offset:30720\n"
      "v_add_u32_dpp v55, v55, v55 quad_perm:[2,3,0,1] row_mask:0xf bank_mask:0xf\n"
      "v_mfma_f32_16x16x32_bf16 v[208:211], a[16:19], v[88:91], v[208:211]\n"
      "s_mov_b32 s47, 49152\n"
      "v_and_b32 v2, 0x78, v42\n"
      "v_mfma_f32_16x16x32_bf16 v[216:219], a[20:23], v[88:91], v[216:219]\n"
      "v_add_u32_dpp v55, v55, v55 row_half_mirror row_mask:0xf bank_mask:0xf\n"
      "v_cmp_ne_u32 vcc, s71, v55\n"
      "v_mfma_f32_16x16x32_bf16 v[204:207], a[12:15], v[92:95], v[204:207]\n"
      "v_add_u32 v51, s43, v2\n"
      "s_mov_b32 s48, 0xaaaaaaaa\n"
      "v_mfma_f32_16x16x32_bf16 v[212:215], a[16:19], v[92:95], v[212:215]\n"
      "global_load_dwordx4 a[120:123], v45, s[8:9] offset:2048 sc1\n"
      "s_cbranch_vccnz L_sx2_%=\n"
      "L_bx2_%=:\n"
      "s_mov_b32 s49, s48\n"
      "v_mfma_f32_16x16x32_bf16 v[220:223], a[20:23], v[92:95], v[220:223]\n"
      "s_waitcnt lgkmcnt(0)\n"
      "s_barrier\n"
      "v_mfma_f32_16x16x32_bf16 v[24:27], v[60:63], v[8:11], v[24:27]\n"
      "ds_read_b64_tr_b16 v[64:65], v6 offset:32768\n"
      "v_add_u32 v44, 4096, v7\n"
      "v_mfma_f32_16x16x32_bf16 v[28:31], v[8:11], v[8:11], v[28:31]\n"
      "ds_read_b64_tr_b16 v[66:67], v6 offset:34816\n"
      "s_mov_b32 s50, 0xcccccccc\n"
      "v_mfma_f32_16x16x32_bf16 v[24:27], v[60:63], v[12:15], v[24:27]\n"
      "buffer_load_dword v32, v5, s[32:35], s47 offen sc1\n"
      "s_mov_b32 s51, s50\n"
      "v_mfma_f32_16x16x32_bf16 v[28:31], v[12:15], v[12:15], v[28:31]\n"
      "ds_read_b64_tr_b16 v[68:69], v33 offset:32768\n"
      "s_waitcnt vmcnt(19)\n"
      "v_cmp_ne_u64 vcc, s[30:31], v[58:59]\n"
      "v_mfma_f32_16x16x32_bf16 v[176:179], a[24:27], v[96:99], v[176:179]\n"
      "global_load_dwordx4 a[124:127], v45, s[8:9] offset:3072 sc1\n"
      "v_add_u32 v2, 110592, v42\n"
      "v_mfma_f32_16x16x32_bf16 v[184:187], a[28:31], v[96:99], v[184:187]\n"
      "ds_read_b64_tr_b16 v[70:71], v33 offset:34816\n"
      "s_cbranch_vccnz L_sfd_%=\n"
      "L_bfd_%=:\n"
      "s_mov_b32 s74, 0x00f0000f\n"
      "v_mfma_f32_16x16x32_bf16 v[192:195], a[32:35], v[96:99], v[192:195]\n"
      "ds_read_b64_tr_b16 v[72:73], v34 offset:32768\n"
      "v_add_u32 v45, 36864, v7\n"
      "s_add_u32 m0, s73, 16384\n"
      "v_add_u32 v43, 65536, v40\n"
      "v_mfma_f32_16x16x32_bf16 v[180:183], a[24:27], v[100:103], v[180:183]\n"
      "ds_read_b64_tr_b16 v[74:75], v34 offset:34816\n"
      "s_mov_b32 s75, 0xf0000f00\n"
      "v_mfma_f32_16x16x32_bf16 v[188:191], a[28:31], v[100:103], v[188:191]\n"
      "global_load_dwordx4 a[128:131], v45, s[8:9] offset:-4096 sc1\n"
      "v_cndmask_b32 v50, v2, v51, s[74:75]\n"
      "v_mfma_f32_16x16x32_bf16 v[196:199], a[32:35], v[100:103], v[196:199]\n"
      "ds_read_b64_tr_b16 v[76:77], v35 offset:32768\n"
      "v_mfma_f32_16x16x32_bf16 v[200:203], a[24:27], v[104:107], v[200:203]\n"
      "ds_read_b64_tr_b16 v[78:79], v35 offset:34816\n"
      "v_mfma_f32_16x16x32_bf16 v[208:211], a[28:31], v[104:107], v[208:211]\n"
      "ds_read_b64_tr_b16 v[80:81], v6 offset:36864\n"
      "v_mfma_f32_16x16x32_bf16 v[216:219], a[32:35], v[104:107], v[216:219]\n"
      "global_load_dwordx4 a[132:135], v45, s[8:9] offset:-3072 sc1\n"
      "v_mfma_f32_16x16x32_bf16 v[204:207], a[24:27], v[108:111], v[204:207]\n"
      "ds_read_b64_tr_b16 v[82:83], v6 offset:38912\n"
      "v_mfma_f32_16x16x32_bf16 v[212:215], a[28:31], v[108:111], v[212:215]\n"
      "ds_read_b64_tr_b16 v[84:85], v33 offset:36864\n"
      "v_mfma_f32_16x16x32_bf16 v[220:223], a[32:35], v[108:111], v[220:223]\n"
      "ds_read_b64_tr_b16 v[86:87], v33 offset:38912\n"
      "v_mfma_f32_16x16x32_bf16 v[176:179], a[36:39], v[112:115], v[176:179]\n"
      "global_load_dwordx4 a[136:139], v45, s[8:9] offset:-2048 sc1\n"
      "v_mfma_f32_16x16x32_bf16 v[184:187], a[40:43], v[112:115], v[184:187]\n"
      "ds_read_b64_tr_b16 v[88:89], v34 offset:36864\n"
      "v_mfma_f32_16x16x32_bf16 v[192:195], a[44:47], v[112:115], v[192:195]\n"
      "ds_read_b64_tr_b16 v[90:91], v34 offset:38912\n"
      "v_mfma_f32_16x16x32_bf16 v[180:183], a[36:39], v[116:119], v[180:183]\n"
      "ds_read_b64_tr_b16 v[92:93], v35 offset:36864\n"
      "v_mfma_f32_16x16x32_bf16 v[188:191], a[40:43], v[116:119], v[188:191]\n"
      "global_load_dwordx4 a[140:143], v45, s[8:9] offset:-1024 sc1\n"
      "v_mfma_f32_16x16x32_bf16 v[196:199], a[44:47], v[116:119], v[196:199]\n"
      "ds_read_b64_tr_b16 v[94:95], v35 offset:38912\n"
      "v_mfma_f32_16x16x32_bf16 v[200:203], a[36:39], v[120:123], v[200:203]\n"
      "v_mfma_f32_16x16x32_bf16 v[208:211], a[40:43], v[120:123], v[208:211]\n"
      "v_mfma_f32_16x16x32_bf16 v[216:219], a[44:47], v[120:123], v[216:219]\n"
      "global_load_dwordx4 a[144:147], v45, s[8:9] offset:0 sc1\n"
      "v_mfma_f32_16x16x32_bf16 v[204:207], a[36:39], v[124:127], v[204:207]\n"
      "v_mfma_f32_16x16x32_bf16 v[212:215], a[40:43], v[124:127], v[212:215]\n"
      "v_mfma_f32_16x16x32_bf16 v[220:223], a[44:47], v[124:127], v[220:223]\n"
      "s_waitcnt lgkmcnt(0)\n"
      "v_mfma_f32_16x16x32_bf16 v[128:131], a[48:51], v[64:67], v[128:131]\n"
      "global_load_dwordx4 a[148:151], v45, s[8:9] offset:1024 sc1\n"
      "v_mfma_f32_16x16x32_bf16 v[144:147], a[52:55], v[64:67], v[144:147]\n"
      "ds_read_b64_tr_b16 v[96:97], v6 offset:40960\n"
      "s_waitcnt vmcnt(21)\n"
      "v_mfma_f32_16x16x32_bf16 v[160:163], a[56:59], v[64:67], v[160:163]\n"
      "ds_read_b128 v[248:251], v40 offset:49152\n"
      "v_mfma_f32_16x16x32_bf16 v[16:19], v[60:63], v[64:67], v[16:19]\n"
      "ds_read_b128 v[252:255], v40 offset:50176\n"
      "v_mfma_f32_16x16x32_bf16 v[20:23], v[64:67], v[64:67], v[20:23]\n"
      "global_load_dwordx4 a[152:155], v45, s[8:9] offset:2048 sc1\n"
      "s_waitcnt vmcnt(13)\n"
      "v_mfma_f32_16x16x32_bf16 v[132:135], a[48:51], v[68:71], v[132:135]\n"
      "ds_read_b64_tr_b16 v[98:99], v6 offset:43008\n"
      "v_add_f32 v224, v224, v228\n"
      "v_mfma_f32_16x16x32_bf16 v[148:151], a[52:55], v[68:71], v[148:151]\n"
      "ds_read_b64_tr_b16 v[100:101], v33 offset:40960\n"
      "v_add_f32 v225, v225, v229\n"
      "v_mfma_f32_16x16x32_bf16 v[164:167], a[56:59], v[68:71], v[164:167]\n"
      "ds_read_b64_tr_b16 v[102:103], v33 offset:43008\n"
      "s_waitcnt lgkmcnt(3)\n"
      "v_mfma_f32_16x16x32_bf16 v[136:139], a[48:51], v[72:75], v[136:139]\n"
      "global_load_dwordx4 a[156:159], v45, s[8:9] offset:3072 sc1\n"
      "ds_read_b64_tr_b16 v[104:105], v34 offset:40960\n"
      "v_add3_u32 v56, v56, v248, v249\n"
      "v_mfma_f32_16x16x32_bf16 v[152:155], a[52:55], v[72:75], v[152:155]\n"
      "ds_read_b64_tr_b16 v[106:107], v34 offset:43008\n"
      "v_add3_u32 v56, v56, v250, v251\n"
      "v_add_f32 v226, v226, v230\n"
      "v_mfma_f32_16x16x32_bf16 v[168:171], a[56:59], v[72:75], v[168:171]\n"
      "ds_read_b64_tr_b16 v[108:109], v35 offset:40960\n"
      "v_add_u32 v45, 45056, v7\n"
      "v_add3_u32 v56, v56, v252, v253\n"
      "v_add_f32 v227, v227, v231\n"
      "v_mfma_f32_16x16x32_bf16 v[140:143], a[48:51], v[76:79], v[140:143]\n"
      "ds_read_b64_tr_b16 v[110:111], v35 offset:43008\n"
      "v_add3_u32 v56, v56, v254, v255\n"
      "v_add_f32 v232, v232, v236\n"
      "v_mfma_f32_16x16x32_bf16 v[156:159], a[52:55], v[76:79], v[156:159]\n"
      "global_load_lds_dwordx4 v43, s[4:5] offset:0 sc1\n"
      "v_add_f32 v233, v233, v237\n"
      "v_mfma_f32_16x16x32_bf16 v[172:175], a[56:59], v[76:79], v[172:175]\n"
      "ds_read_b128 v[248:251], v40 offset:51200\n"
      "ds_read_b128 v[252:255], v40 offset:52224\n"
      "ds_read_b64_tr_b16 v[112:113], v6 offset:45056\n"
      "v_add_f32 v234, v234, v238\n"
      "v_mfma_f32_16x16x32_bf16 v[128:131], a[60:63], v[80:83], v[128:131]\n"
      "ds_read_b64_tr_b16 v[114:115], v6 offset:47104\n"
      "v_add_f32 v235, v235, v239\n"
      "v_mfma_f32_16x16x32_bf16 v[144:147], a[64:67], v[80:83], v[144:147]\n"
      "ds_read_b64_tr_b16 v[116:117], v33 offset:45056\n"
      "v_add_f32 v240, v240, v244\n"
      "v_mfma_f32_16x16x32_bf16 v[160:163], a[68:71], v[80:83], v[160:163]\n"
      "global_load_dwordx4 a[160:163], v45, s[8:9] offset:-4096 sc1\n"
      "v_add_f32 v241, v241, v245\n"
      "v_mfma_f32_16x16x32_bf16 v[16:19], v[60:63], v[80:83], v[16:19]\n"
      "ds_read_b64_tr_b16 v[118:119], v33 offset:47104\n"
      "s_waitcnt lgkmcnt(4)\n"
      "v_mfma_f32_16x16x32_bf16 v[20:23], v[80:83], v[80:83], v[20:23]\n"
      "ds_read_b64_tr_b16 v[120:121], v34 offset:45056\n"
      "v_add3_u32 v56, v56, v248, v249\n"
      "v_add3_u32 v56, v56, v250, v251\n"
      "v_add3_u32 v56, v56, v252, v253\n"
      "v_add3_u32 v56, v56, v254, v255\n"
      "v_add_f32 v242, v242, v246\n"
      "v_mfma_f32_16x16x32_bf16 v[132:135], a[60:63], v[84:87], v[132:135]\n"
      "ds_read_b64_tr_b16 v[122:123], v34 offset:47104\n"
      "v_add_f32 v243, v243, v247\n"
      "v_mfma_f32_16x16x32_bf16 v[148:151], a[64:67], v[84:87], v[148:151]\n"
      "global_load_lds_dwordx4 v43, s[4:5] offset:1024 sc1\n"
      "ds_read_b64_tr_b16 v[124:125], v35 offset:45056\n"
      "v_add_u32_dpp v56, v56, v56 quad_perm:[1,0,3,2] row_mask:0xf bank_mask:0xf\n"
      "v_add_f32 v224, v224, v232\n"
      "v_mfma_f32_16x16x32_bf16 v[164:167], a[68:71], v[84:87], v[164:167]\n"
      "ds_read_b64_tr_b16 v[126:127], v35 offset:47104\n"
      "v_add_f32 v225, v225, v233\n"
      "v_mfma_f32_16x16x32_bf16 v[136:139], a[60:63], v[88:91], v[136:139]\n"
      "v_add_u32_dpp v56, v56, v56 quad_perm:[2,3,0,1] row_mask:0xf bank_mask:0xf\n"
      "v_add_f32 v226, v226, v234\n"
      "v_mfma_f32_16x16x32_bf16 v[152:155], a[64:67], v[88:91], v[152:155]\n"
      "v_add_f32 v227, v227, v235\n"
      "v_add_f32 v224, v224, v240\n"
      "v_mfma_f32_16x16x32_bf16 v[168:171], a[68:71], v[88:91], v[168:171]\n"
      "global_load_dwordx4 a[164:167], v45, s[8:9] offset:-3072 sc1\n"
      "v_add_u32_dpp v56, v56, v56 row_half_mirror row_mask:0xf bank_mask:0xf\n"
      "v_cmp_ne_u32 vcc, s71, v56\n"
      "v_mfma_f32_16x16x32_bf16 v[140:143], a[60:63], v[92:95], v[140:143]\n"
      "v_add_f32 v225, v225, v241\n"
      "v_add_f32 v226, v226, v242\n"
      "v_mfma_f32_16x16x32_bf16 v[156:159], a[64:67], v[92:95], v[156:159]\n"
      "s_cbranch_vccnz L_sx3_%=\n"
      "L_bx3_%=:\n"
      "v_add_f32 v227, v227, v243\n"
      "v_add_u32 v53, 0x6000, v53\n"
      "v_mfma_f32_16x16x32_bf16 v[172:175], a[68:71], v[92:95], v[172:175]\n"
      "s_waitcnt lgkmcnt(0)\n"
      "s_barrier\n"
      "v_mfma_f32_16x16x32_bf16 v[128:131], a[72:75], v[96:99], v[128:131]\n"
      "global_load_dwordx4 v[228:231], v53, s[26:27] sc1\n"
      "v_mfma_f32_16x16x32_bf16 v[144:147], a[76:79], v[96:99], v[144:147]\n"
      "ds_read_b64_tr_b16 v[64:65], v6 offset:49152\n"
      "v_mfma_f32_16x16x32_bf16 v[160:163], a[80:83], v[96:99], v[160:163]\n"
      "ds_read_b64_tr_b16 v[66:67], v6 offset:51200\n"
      "v_add_u32 v53, 0x6000, v53\n"
      "v_mfma_f32_16x16x32_bf16 v[16:19], v[60:63], v[96:99], v[16:19]\n"
      "ds_read_b64_tr_b16 v[68:69], v33 offset:49152\n"
      "v_mfma_f32_16x16x32_bf16 v[20:23], v[96:99], v[96:99], v[20:23]\n"
      "global_load_dwordx4 v[232:235], v53, s[26:27] sc1\n"
      "v_mfma_f32_16x16x32_bf16 v[132:135], a[72:75], v[100:103], v[132:135]\n"
      "ds_read_b64_tr_b16 v[70:71], v33 offset:51200\n"
      "v_mfma_f32_16x16x32_bf16 v[148:151], a[76:79], v[100:103], v[148:151]\n"
      "ds_read_b64_tr_b16 v[72:73], v34 offset:49152\n"
      "v_add_u32 v53, 0x6000, v53\n"
      "v_mfma_f32_16x16x32_bf16 v[164:167], a[80:83], v[100:103], v[164:167]\n"
      "ds_read_b64_tr_b16 v[74:75], v34 offset:51200\n"
      "v_mfma_f32_16x16x32_bf16 v[136:139], a[72:75], v[104:107], v[136:139]\n"
      "global_load_dwordx4 v[236:239], v53, s[26:27] sc1\n"
      "v_add_u32 v53, 0x6000, v53\n"
      "v_mfma_f32_16x16x32_bf16 v[152:155], a[76:79], v[104:107], v[152:155]\n"
      "global_load_dwordx4 v[240:243], v53, s[26:27] sc1\n"
      "v_mfma_f32_16x16x32_bf16 v[168:171], a[80:83], v[104:107], v[168:171]\n"
      "ds_read_b64_tr_b16 v[76:77], v35 offset:49152\n"
      "v_mfma_f32_16x16x32_bf16 v[140:143], a[72:75], v[108:111], v[140:143]\n"
      "ds_read_b64_tr_b16 v[78:79], v35 offset:51200\n"
      "v_mfma_f32_16x16x32_bf16 v[156:159], a[76:79], v[108:111], v[156:159]\n"
      "ds_read_b64_tr_b16 v[80:81], v6 offset:53248\n"
      "v_mfma_f32_16x16x32_bf16 v[172:175], a[80:83], v[108:111], v[172:175]\n"
      "global_load_lds_dwordx4 v43, s[4:5] offset:2048 sc1\n"
      "v_mfma_f32_16x16x32_bf16 v[128:131], a[84:87], v[112:115], v[128:131]\n"
      "ds_read_b64_tr_b16 v[82:83], v6 offset:55296\n"
      "v_mfma_f32_16x16x32_bf16 v[144:147], a[88:91], v[112:115], v[144:147]\n"
      "ds_read_b64_tr_b16 v[84:85], v33 offset:53248\n"
      "v_mfma_f32_16x16x32_bf16 v[160:163], a[92:95], v[112:115], v[160:163]\n"
      "ds_read_b64_tr_b16 v[86:87], v33 offset:55296\n"
      "v_mfma_f32_16x16x32_bf16 v[16:19], v[60:63], v[112:115], v[16:19]\n"
      "global_load_lds_dwordx4 v43, s[4:5] offset:3072 sc1\n"
      "s_mov_b32 s47, 65536\n"
      "v_mfma_f32_16x16x32_bf16 v[20:23], v[112:115], v[112:115], v[20:23]\n"
      "ds_read_b64_tr_b16 v[88:89], v34 offset:53248\n"
      "v_mfma_f32_16x16x32_bf16 v[132:135], a[84:87], v[116:119], v[132:135]\n"
      "ds_read_b64_tr_b16 v[90:91], v34 offset:55296\n"
      "v_mfma_f32_16x16x32_bf16 v[148:151], a[88:91], v[116:119], v[148:151]\n"
      "ds_read_b64_tr_b16 v[92:93], v35 offset:53248\n"
      "v_mfma_f32_16x16x32_bf16 v[164:167], a[92:95], v[116:119], v[164:167]\n"
      "global_load_dwordx4 a[168:171], v45, s[8:9] offset:-2048 sc1\n"
      "v_mfma_f32_16x16x32_bf16 v[136:139], a[84:87], v[120:123], v[136:139]\n"
      "ds_read_b64_tr_b16 v[94:95], v35 offset:55296\n"
      "v_mfma_f32_16x16x32_bf16 v[152:155], a[88:91], v[120:123], v[152:155]\n"
      "v_mfma_f32_16x16x32_bf16 v[168:171], a[92:95], v[120:123], v[168:171]\n"
      "v_mfma_f32_16x16x32_bf16 v[140:143], a[84:87], v[124:127], v[140:143]\n"
      "buffer_load_dword v4, v5, s[32:35], s47 offen sc1\n"
      "s_add_u32 m0, s73, 32768\n"
      "v_add_u32 v43, 98304, v40\n"
      "v_mfma_f32_16x16x32_bf16 v[156:159], a[88:91], v[124:127], v[156:159]\n"
      "v_mfma_f32_16x16x32_bf16 v[172:175], a[92:95], v[124:127], v[172:175]\n"
      "global_load_dwordx4 a[172:175], v45, s[8:9] offset:-1024 sc1\n"
      "s_waitcnt vmcnt(26)\n"
      "s_waitcnt lgkmcnt(0)\n"
      "v_mfma_f32_16x16x32_bf16 v[128:131], a[96:99], v[64:67], v[128:131]\n"
      "ds_read_b64_tr_b16 v[96:97], v6 offset:57344\n"
      "v_mfma_f32_16x16x32_bf16 v[144:147], a[100:103], v[64:67], v[144:147]\n"
      "ds_read_b64_tr_b16 v[98:99], v6 offset:59392\n"
      "s_waitcnt vmcnt(22)\n"
      "v_mfma_f32_16x16x32_bf16 v[160:163], a[104:107], v[64:67], v[160:163]\n"
      "ds_read_b128 v[248:251], v40 offset:0\n"
      "v_mfma_f32_16x16x32_bf16 v[16:19], v[60:63], v[64:67], v[16:19]\n"
      "global_load_dwordx4 a[176:179], v45, s[8:9] offset:0 sc1\n"
      "v_mfma_f32_16x16x32_bf16 v[20:23], v[64:67], v[64:67], v[20:23]\n"
      "ds_read_b128 v[252:255], v40 offset:1024\n"
      "v_mfma_f32_16x16x32_bf16 v[132:135], a[96:99], v[68:71], v[132:135]\n"
      "ds_read_b64_tr_b16 v[100:101], v33 offset:57344\n"
      "v_mfma_f32_16x16x32_bf16 v[148:151], a[100:103], v[68:71], v[148:151]\n"
      "ds_read_b64_tr_b16 v[102:103], v33 offset:59392\n"
      "v_mfma_f32_16x16x32_bf16 v[164:167], a[104:107], v[68:71], v[164:167]\n"
      "global_load_dwordx4 a[180:183], v45, s[8:9] offset:1024 sc1\n"
      "v_mfma_f32_16x16x32_bf16 v[136:139], a[96:99], v[72:75], v[136:139]\n"
      "ds_read_b64_tr_b16 v[104:105], v34 offset:57344\n"
      "s_waitcnt lgkmcnt(3)\n"
      "v_mfma_f32_16x16x32_bf16 v[152:155], a[100:103], v[72:75], v[152:155]\n"
      "ds_read_b64_tr_b16 v[106:107], v34 offset:59392\n"
      "v_add3_u32 v32, v32, v248, v249\n"
      "v_add3_u32 v32, v32, v250, v251\n"
      "v_mfma_f32_16x16x32_bf16 v[168:171], a[104:107], v[72:75], v[168:171]\n"
      "ds_read_b64_tr_b16 v[108:109], v35 offset:57344\n"
      "v_add3_u32 v32, v32, v252, v253\n"
      "v_mfma_f32_16x16x32_bf16 v[140:143], a[96:99], v[76:79], v[140:143]\n"
      "global_load_dwordx4 a[184:187], v45, s[8:9] offset:2048 sc1\n"
      "v_add3_u32 v32, v32, v254, v255\n"
      "v_mfma_f32_16x16x32_bf16 v[156:159], a[100:103], v[76:79], v[156:159]\n"
      "ds_read_b64_tr_b16 v[110:111], v35 offset:59392\n"
      "v_mfma_f32_16x16x32_bf16 v[172:175], a[104:107], v[76:79], v[172:175]\n"
      "ds_read_b128 v[248:251], v40 offset:2048\n"
      "ds_read_b128 v[252:255], v40 offset:3072\n"
      "ds_read_b64_tr_b16 v[112:113], v6 offset:61440\n"
      "v_mfma_f32_16x16x32_bf16 v[128:131], a[108:111], v[80:83], v[128:131]\n"
      "ds_read_b64_tr_b16 v[114:115], v6 offset:63488\n"
      "v_mfma_f32_16x16x32_bf16 v[144:147], a[112:115], v[80:83], v[144:147]\n"
      "global_load_dwordx4 a[188:191], v45, s[8:9] offset:3072 sc1\n"
      "v_mfma_f32_16x16x32_bf16 v[160:163], a[116:119], v[80:83], v[160:163]\n"
      "ds_read_b64_tr_b16 v[116:117], v33 offset:61440\n"
      "v_mfma_f32_16x16x32_bf16 v[16:19], v[60:63], v[80:83], v[16:19]\n"
      "ds_read_b64_tr_b16 v[118:119], v33 offset:63488\n"
      "v_add_u32 v45, 53248, v7\n"
      "v_mfma_f32_16x16x32_bf16 v[20:23], v[80:83], v[80:83], v[20:23]\n"
      "ds_read_b64_tr_b16 v[120:121], v34 offset:61440\n"
      "s_waitcnt lgkmcnt(5)\n"
      "v_add3_u32 v32, v32, v248, v249\n"
      "v_add3_u32 v32, v32, v250, v251\n"
      "v_add3_u32 v32, v32, v252, v253\n"
      "v_add3_u32 v32, v32, v254, v255\n"
      "v_mfma_f32_16x16x32_bf16 v[132:135], a[108:111], v[84:87], v[132:135]\n"
      "global_load_dwordx4 a[192:195], v45, s[8:9] offset:-4096 sc1\n"
      "ds_read_b64_tr_b16 v[122:123], v34 offset:63488\n"
      "v_mfma_f32_16x16x32_bf16 v[148:151], a[112:115], v[84:87], v[148:151]\n"
      "ds_read_b64_tr_b16 v[124:125], v35 offset:61440\n"
      "v_add_u32_dpp v32, v32, v32 quad_perm:[1,0,3,2] row_mask:0xf bank_mask:0xf\n"
      "v_mfma_f32_16x16x32_bf16 v[164:167], a[116:119], v[84:87], v[164:167]\n"
      "ds_read_b64_tr_b16 v[126:127], v35 offset:63488\n"
      "v_mfma_f32_16x16x32_bf16 v[136:139], a[108:111], v[88:91], v[136:139]\n"
      "v_add_u32_dpp v32, v32, v32 quad_perm:[2,3,0,1] row_mask:0xf bank_mask:0xf\n"
      "v_mfma_f32_16x16x32_bf16 v[152:155], a[112:115], v[88:91], v[152:155]\n"
      "global_load_dwordx4 a[196:199], v45, s[8:9] offset:-3072 sc1\n"
      "v_mfma_f32_16x16x32_bf16 v[168:171], a[116:119], v[88:91], v[168:171]\n"
      "v_add_u32_dpp v32, v32, v32 row_half_mirror row_mask:0xf bank_mask:0xf\n"
      "v_cmp_ne_u32 vcc, s71, v32\n"
      "v_mfma_f32_16x16x32_bf16 v[140:143], a[108:111], v[92:95], v[140:143]\n"
      "v_mfma_f32_16x16x32_bf16 v[156:159], a[112:115], v[92:95], v[156:159]\n"
      "s_cbranch_vccnz L_sx4_%=\n"
      "L_bx4_%=:\n"
      "v_mfma_f32_16x16x32_bf16 v[172:175], a[116:119], v[92:95], v[172:175]\n"
      "global_load_dwordx4 a[200:203], v45, s[8:9] offset:-2048 sc1\n"
      "s_waitcnt vmcnt(24)\n"
      "s_waitcnt lgkmcnt(0)\n"
      "s_barrier\n"
      "v_mfma_f32_16x16x32_bf16 v[128:131], a[120:123], v[96:99], v[128:131]\n"
      "ds_read_b64_tr_b16 v[64:65], v6 offset:0\n"
      "v_mfma_f32_16x16x32_bf16 v[144:147], a[124:127], v[96:99], v[144:147]\n"
      "ds_read_b64_tr_b16 v[66:67], v6 offset:2048\n"
      "v_mfma_f32_16x16x32_bf16 v[160:163], a[128:131], v[96:99], v[160:163]\n"
      "ds_read_b64_tr_b16 v[68:69], v33 offset:0\n"
      "v_mfma_f32_16x16x32_bf16 v[16:19], v[60:63], v[96:99], v[16:19]\n"
      "global_load_dwordx4 a[204:207], v45, s[8:9] offset:-1024 sc1\n"
      "s_waitcnt vmcnt(13)\n"
      "v_mfma_f32_16x16x32_bf16 v[20:23], v[96:99], v[96:99], v[20:23]\n"
      "ds_read_b64_tr_b16 v[70:71], v33 offset:2048\n"
      "v_add_f32 v228, v228, v232\n"
      "v_mfma_f32_16x16x32_bf16 v[132:135], a[120:123], v[100:103], v[132:135]\n"
      "ds_read_b64_tr_b16 v[72:73], v34 offset:0\n"
      "v_add_f32 v229, v229, v233\n"
      "v_mfma_f32_16x16x32_bf16 v[148:151], a[124:127], v[100:103], v[148:151]\n"
      "ds_read_b64_tr_b16 v[74:75], v34 offset:2048\n"
      "v_add_f32 v230, v230, v234\n"
      "v_mfma_f32_16x16x32_bf16 v[164:167], a[128:131], v[100:103], v[164:167]\n"
      "global_load_lds_dwordx4 v43, s[4:5] offset:0 sc1\n"
      "v_add_f32 v231, v231, v235\n"
      "v_mfma_f32_16x16x32_bf16 v[136:139], a[120:123], v[104:107], v[136:139]\n"
      "ds_read_b64_tr_b16 v[76:77], v35 offset:0\n"
      "v_add_f32 v236, v236, v240\n"
      "v_mfma_f32_16x16x32_bf16 v[152:155], a[124:127], v[104:107], v[152:155]\n"
      "ds_read_b64_tr_b16 v[78:79], v35 offset:2048\n"
      "v_add_f32 v237, v237, v241\n"
      "v_mfma_f32_16x16x32_bf16 v[168:171], a[128:131], v[104:107], v[168:171]\n"
      "ds_read_b64_tr_b16 v[80:81], v6 offset:4096\n"
      "v_add_f32 v238, v238, v242\n"
      "v_mfma_f32_16x16x32_bf16 v[140:143], a[120:123], v[108:111], v[140:143]\n"
      "global_load_dwordx4 a[208:211], v45, s[8:9] offset:0 sc1\n"
      "v_add_f32 v239, v239, v243\n"
      "v_mfma_f32_16x16x32_bf16 v[156:159], a[124:127], v[108:111], v[156:159]\n"
      "ds_read_b64_tr_b16 v[82:83], v6 offset:6144\n"
      "v_add_f32 v224, v224, v228\n"
      "v_mfma_f32_16x16x32_bf16 v[172:175], a[128:131], v[108:111], v[172:175]\n"
      "ds_read_b64_tr_b16 v[84:85], v33 offset:4096\n"
      "v_add_f32 v225, v225, v229\n"
      "v_mfma_f32_16x16x32_bf16 v[128:131], a[132:135], v[112:115], v[128:131]\n"
      "ds_read_b64_tr_b16 v[86:87], v33 offset:6144\n"
      "v_add_f32 v226, v226, v230\n"
      "v_mfma_f32_16x16x32_bf16 v[144:147], a[136:139], v[112:115], v[144:147]\n"
      "global_load_lds_dwordx4 v43, s[4:5] offset:1024 sc1\n"
      "v_add_f32 v227, v227, v231\n"
      "v_mfma_f32_16x16x32_bf16 v[160:163], a[140:143], v[112:115], v[160:163]\n"
      "ds_read_b64_tr_b16 v[88:89], v34 offset:4096\n"
      "v_add_f32 v224, v224, v236\n"
      "v_mfma_f32_16x16x32_bf16 v[16:19], v[60:63], v[112:115], v[16:19]\n"
      "ds_read_b64_tr_b16 v[90:91], v34 offset:6144\n"
      "v_add_f32 v225, v225, v237\n"
      "v_mfma_f32_16x16x32_bf16 v[20:23], v[112:115], v[112:115], v[20:23]\n"
      "ds_read_b64_tr_b16 v[92:93], v35 offset:4096\n"
      "v_add_f32 v226, v226, v238\n"
      "v_mfma_f32_16x16x32_bf16 v[132:135], a[132:135], v[116:119], v[132:135]\n"
      "global_load_dwordx4 a[212:215], v45, s[8:9] offset:1024 sc1\n"
      "v_add_f32 v227, v227, v239\n"
      "v_mfma_f32_16x16x32_bf16 v[148:151], a[136:139], v[116:119], v[148:151]\n"
      "ds_read_b64_tr_b16 v[94:95], v35 offset:6144\n"
      "v_mov_b32 v228, v224\n"
      "v_mfma_f32_16x16x32_bf16 v[164:167], a[140:143], v[116:119], v[164:167]\n"
      "v_mov_b32 v229, v225\n"
      "v_mov_b32 v230, v226\n"
      "v_mfma_f32_16x16x32_bf16 v[136:139], a[132:135], v[120:123], v[136:139]\n"
      "v_mov_b32 v231, v227\n"
      "s_nop 0\n"
      "v_mfma_f32_16x16x32_bf16 v[152:155], a[136:139], v[120:123], v[152:155]\n"
      "global_load_lds_dwordx4 v43, s[4:5] offset:2048 sc1\n"
      "v_permlane32_swap_b32 v224, v228\n"
      "v_mfma_f32_16x16x32_bf16 v[168:171], a[140:143], v[120:123], v[168:171]\n"
      "v_permlane32_swap_b32 v225, v229\n"
      "v_permlane32_swap_b32 v226, v230\n"
      "v_mfma_f32_16x16x32_bf16 v[140:143], a[132:135], v[124:127], v[140:143]\n"
      "v_permlane32_swap_b32 v227, v231\n"
      "v_add_f32 v224, v224, v228\n"
      "v_mfma_f32_16x16x32_bf16 v[156:159], a[136:139], v[124:127], v[156:159]\n"
      "v_add_f32 v225, v225, v229\n"
      "v_add_f32 v226, v226, v230\n"
      "v_mfma_f32_16x16x32_bf16 v[172:175], a[140:143], v[124:127], v[172:175]\n"
      "global_load_lds_dwordx4 v43, s[4:5] offset:3072 sc1\n"
      "s_waitcnt lgkmcnt(0)\n"
      "v_mfma_f32_16x16x32_bf16 v[128:131], a[144:147], v[64:67], v[128:131]\n"
      "ds_read_b64_tr_b16 v[96:97], v6 offset:8192\n"
      "s_mov_b32 s47, 16416\n"
      "v_mfma_f32_16x16x32_bf16 v[144:147], a[148:151], v[64:67], v[144:147]\n"
      "ds_read_b64_tr_b16 v[98:99], v6 offset:10240\n"
      "s_waitcnt vmcnt(15)\n"
      "v_mfma_f32_16x16x32_bf16 v[160:163], a[152:155], v[64:67], v[160:163]\n"
      "ds_read_b128 v[248:251], v40 offset:16384\n"
      "v_add_f32 v227, v227, v231\n"
      "v_mfma_f32_16x16x32_bf16 v[16:19], v[60:63], v[64:67], v[16:19]\n"
      "global_load_dwordx4 a[216:219], v45, s[8:9] offset:2048 sc1\n"
      "v_mfma_f32_16x16x32_bf16 v[20:23], v[64:67], v[64:67], v[20:23]\n"
      "ds_read_b128 v[252:255], v40 offset:17408\n"
      "v_mfma_f32_16x16x32_bf16 v[132:135], a[144:147], v[68:71], v[132:135]\n"
      "ds_read_b64_tr_b16 v[100:101], v33 offset:8192\n"
      "v_mfma_f32_16x16x32_bf16 v[148:151], a[148:151], v[68:71], v[148:151]\n"
      "buffer_load_dword v55, v5, s[32:35], s47 offen sc1\n"
      "s_add_u32 m0, s40, 0\n"
      "v_add_u32 v43, 40960, v7\n"
      "v_mfma_f32_16x16x32_bf16 v[164:167], a[152:155], v[68:71], v[164:167]\n"
      "ds_read_b64_tr_b16 v[102:103], v33 offset:10240\n"
      "v_mfma_f32_16x16x32_bf16 v[136:139], a[144:147], v[72:75], v[136:139]\n"
      "global_load_dwordx4 a[220:223], v45, s[8:9] offset:3072 sc1\n"
      "ds_read_b64_tr_b16 v[104:105], v34 offset:8192\n"
      "s_waitcnt lgkmcnt(3)\n"
      "v_mfma_f32_16x16x32_bf16 v[152:155], a[148:151], v[72:75], v[152:155]\n"
      "ds_read_b64_tr_b16 v[106:107], v34 offset:10240\n"
      "v_add3_u32 v4, v4, v248, v249\n"
      "v_add3_u32 v4, v4, v250, v251\n"
      "v_mfma_f32_16x16x32_bf16 v[168:171], a[152:155], v[72:75], v[168:171]\n"
      "ds_read_b64_tr_b16 v[108:109], v35 offset:8192\n"
      "v_add_u32 v45, 61440, v7\n"
      "v_add3_u32 v4, v4, v252, v253\n"
      "v_mfma_f32_16x16x32_bf16 v[140:143], a[144:147], v[76:79], v[140:143]\n"
      "ds_write_b128 v57, v[224:227]\n"
      "ds_read_b64_tr_b16 v[110:111], v35 offset:10240\n"
      "ds_read_b64_tr_b16 v[112:113], v6 offset:12288\n"
      "ds_read_b64_tr_b16 v[114:115], v6 offset:14336\n"
      "v_add3_u32 v4, v4, v254, v255\n"
      "v_mfma_f32_16x16x32_bf16 v[156:159], a[148:151], v[76:79], v[156:159]\n"
      "global_load_dwordx4 a[224:227], v45, s[8:9] offset:-4096 sc1\n"
      "v_mfma_f32_16x16x32_bf16 v[172:175], a[152:155], v[76:79], v[172:175]\n"
      "ds_read_b128 v[248:251], v40 offset:18432\n"
      "ds_read_b128 v[252:255], v40 offset:19456\n"
      "v_mfma_f32_16x16x32_bf16 v[128:131], a[156:159], v[80:83], v[128:131]\n"
      "ds_read_b64_tr_b16 v[116:117], v33 offset:12288\n"
      "v_mfma_f32_16x16x32_bf16 v[144:147], a[160:163], v[80:83], v[144:147]\n"
      "ds_read_b64_tr_b16 v[118:119], v33 offset:14336\n"
      "v_mfma_f32_16x16x32_bf16 v[160:163], a[164:167], v[80:83], v[160:163]\n"
      "global_load_dwordx4 a[228:231], v45, s[8:9] offset:-3072 sc1\n"
      "v_mfma_f32_16x16x32_bf16 v[16:19], v[60:63], v[80:83], v[16:19]\n"
      "ds_read_b64_tr_b16 v[120:121], v34 offset:12288\n"
      "s_waitcnt lgkmcnt(3)\n"
      "v_mfma_f32_16x16x32_bf16 v[20:23], v[80:83], v[80:83], v[20:23]\n"
      "ds_read_b64_tr_b16 v[122:123], v34 offset:14336\n"
      "v_add3_u32 v4, v4, v248, v249\n"
      "v_add3_u32 v4, v4, v250, v251\n"
      "v_add3_u32 v4, v4, v252, v253\n"
      "v_add3_u32 v4, v4, v254, v255\n"
      "v_mfma_f32_16x16x32_bf16 v[132:135], a[156:159], v[84:87], v[132:135]\n"
      "ds_read_b64_tr_b16 v[124:125], v35 offset:12288\n"
      "v_mfma_f32_16x16x32_bf16 v[148:151], a[160:163], v[84:87], v[148:151]\n"
      "global_load_dwordx4 a[232:235], v45, s[8:9] offset:-2048 sc1\n"
      "v_add_u32_dpp v4, v4, v4 quad_perm:[1,0,3,2] row_mask:0xf bank_mask:0xf\n"
      "v_mfma_f32_16x16x32_bf16 v[164:167], a[164:167], v[84:87], v[164:167]\n"
      "ds_read_b64_tr_b16 v[126:127], v35 offset:14336\n"
      "v_mfma_f32_16x16x32_bf16 v[136:139], a[156:159], v[88:91], v[136:139]\n"
      "ds_read_b128 v[224:227], v52 offset:0\n"
      "v_add_u32_dpp v4, v4, v4 quad_perm:[2,3,0,1] row_mask:0xf bank_mask:0xf\n"
      "v_mfma_f32_16x16x32_bf16 v[152:155], a[160:163], v[88:91], v[152:155]\n"
      "ds_read_b128 v[228:231], v52 offset:64\n"
      "v_mfma_f32_16x16x32_bf16 v[168:171], a[164:167], v[88:91], v[168:171]\n"
      "global_load_dwordx4 a[236:239], v45, s[8:9] offset:-1024 sc1\n"
      "v_add_u32_dpp v4, v4, v4 row_half_mirror row_mask:0xf bank_mask:0xf\n"
      "v_cmp_ne_u32 vcc, s71, v4\n"
      "v_mfma_f32_16x16x32_bf16 v[140:143], a[156:159], v[92:95], v[140:143]\n"
      "ds_read_b128 v[232:235], v52 offset:128\n"
      "v_mfma_f32_16x16x32_bf16 v[156:159], a[160:163], v[92:95], v[156:159]\n"
      "s_cbranch_vccnz L_sx5_%=\n"
      "L_bx5_%=:\n"
      "v_mfma_f32_16x16x32_bf16 v[172:175], a[164:167], v[92:95], v[172:175]\n"
      "s_waitcnt vmcnt(17)\n"
      "s_waitcnt lgkmcnt(3)\n"
      "s_barrier\n"
      "v_mfma_f32_16x16x32_bf16 v[128:131], a[168:171], v[96:99], v[128:131]\n"
      "global_load_lds_dwordx4 v43, s[6:7] offset:0 sc1\n"
      "v_mfma_f32_16x16x32_bf16 v[144:147], a[172:175], v[96:99], v[144:147]\n"
      "ds_read_b64_tr_b16 v[64:65], v6 offset:16384\n"
      "v_mfma_f32_16x16x32_bf16 v[160:163], a[176:179], v[96:99], v[160:163]\n"
      "ds_read_b64_tr_b16 v[66:67], v6 offset:18432\n"
      "v_mfma_f32_16x16x32_bf16 v[16:19], v[60:63], v[96:99], v[16:19]\n"
      "ds_read_b64_tr_b16 v[68:69], v33 offset:16384\n"
      "v_mfma_f32_16x16x32_bf16 v[20:23], v[96:99], v[96:99], v[20:23]\n"
      "global_load_lds_dwordx4 v43, s[6:7] offset:1024 sc1\n"
      "v_mfma_f32_16x16x32_bf16 v[132:135], a[168:171], v[100:103], v[132:135]\n"
      "ds_read_b64_tr_b16 v[70:71], v33 offset:18432\n"
      "v_mfma_f32_16x16x32_bf16 v[148:151], a[172:175], v[100:103], v[148:151]\n"
      "ds_read_b64_tr_b16 v[72:73], v34 offset:16384\n"
      "v_mfma_f32_16x16x32_bf16 v[164:167], a[176:179], v[100:103], v[164:167]\n"
      "ds_read_b64_tr_b16 v[74:75], v34 offset:18432\n"
      "v_mfma_f32_16x16x32_bf16 v[136:139], a[168:171], v[104:107], v[136:139]\n"
      "global_load_lds_dwordx4 v43, s[6:7] offset:2048 sc1\n"
      "v_mfma_f32_16x16x32_bf16 v[152:155], a[172:175], v[104:107], v[152:155]\n"
      "ds_read_b64_tr_b16 v[76:77], v35 offset:16384\n"
      "v_mfma_f32_16x16x32_bf16 v[168:171], a[176:179], v[104:107], v[168:171]\n"
      "ds_read_b64_tr_b16 v[78:79], v35 offset:18432\n"
      "v_mfma_f32_16x16x32_bf16 v[140:143], a[168:171], v[108:111], v[140:143]\n"
      "ds_read_b64_tr_b16 v[80:81], v6 offset:20480\n"
      "v_mfma_f32_16x16x32_bf16 v[156:159], a[172:175], v[108:111], v[156:159]\n"
      "global_load_lds_dwordx4 v43, s[6:7] offset:3072 sc1\n"
      "v_mfma_f32_16x16x32_bf16 v[172:175], a[176:179], v[108:111], v[172:175]\n"
      "ds_read_b64_tr_b16 v[82:83], v6 offset:22528\n"
      "v_mfma_f32_16x16x32_bf16 v[128:131], a[180:183], v[112:115], v[128:131]\n"
      "ds_read_b64_tr_b16 v[84:85], v33 offset:20480\n"
      "s_add_u32 m0, s73, 49152\n"
      "v_add_u32 v43, 114688, v40\n"
      "v_mfma_f32_16x16x32_bf16 v[144:147], a[184:187], v[112:115], v[144:147]\n"
      "ds_read_b64_tr_b16 v[86:87], v33 offset:22528\n"
      "v_mfma_f32_16x16x32_bf16 v[160:163], a[188:191], v[112:115], v[160:163]\n"
      "global_load_lds_dwordx4 v43, s[4:5] offset:0 sc1\n"
      "v_mfma_f32_16x16x32_bf16 v[16:19], v[60:63], v[112:115], v[16:19]\n"
      "ds_read_b64_tr_b16 v[88:89], v34 offset:20480\n"
      "v_mfma_f32_16x16x32_bf16 v[20:23], v[112:115], v[112:115], v[20:23]\n"
      "ds_read_b64_tr_b16 v[90:91], v34 offset:22528\n"
      "v_mfma_f32_16x16x32_bf16 v[132:135], a[180:183], v[116:119], v[132:135]\n"
      "ds_read_b64_tr_b16 v[92:93], v35 offset:20480\n"
      "v_mfma_f32_16x16x32_bf16 v[148:151], a[184:187], v[116:119], v[148:151]\n"
      "global_load_lds_dwordx4 v43, s[4:5] offset:1024 sc1\n"
      "v_mfma_f32_16x16x32_bf16 v[164:167], a[188:191], v[116:119], v[164:167]\n"
      "ds_read_b64_tr_b16 v[94:95], v35 offset:22528\n"
      "v_mfma_f32_16x16x32_bf16 v[136:139], a[180:183], v[120:123], v[136:139]\n"
      "ds_read_b128 v[236:239], v52 offset:192\n"
      "v_mfma_f32_16x16x32_bf16 v[152:155], a[184:187], v[120:123], v[152:155]\n"
      "ds_read_b128 v[240:243], v52 offset:256\n"
      "v_mfma_f32_16x16x32_bf16 v[168:171], a[188:191], v[120:123], v[168:171]\n"
      "global_load_lds_dwordx4 v43, s[4:5] offset:2048 sc1\n"
      "v_mfma_f32_16x16x32_bf16 v[140:143], a[180:183], v[124:127], v[140:143]\n"
      "ds_read_b128 v[244:247], v52 offset:320\n"
      "v_mfma_f32_16x16x32_bf16 v[156:159], a[184:187], v[124:127], v[156:159]\n"
      "v_mfma_f32_16x16x32_bf16 v[172:175], a[188:191], v[124:127], v[172:175]\n"
      "s_waitcnt vmcnt(16)\n"
      "s_waitcnt lgkmcnt(3)\n"
      "v_mfma_f32_16x16x32_bf16 v[128:131], a[192:195], v[64:67], v[128:131]\n"
      "global_load_lds_dwordx4 v43, s[4:5] offset:3072 sc1\n"
      "s_mov_b32 s47, 32800\n"
      "v_mfma_f32_16x16x32_bf16 v[144:147], a[196:199], v[64:67], v[144:147]\n"
      "ds_read_b64_tr_b16 v[96:97], v6 offset:24576\n"
      "s_waitcnt vmcnt(13)\n"
      "v_mfma_f32_16x16x32_bf16 v[160:163], a[200:203], v[64:67], v[160:163]\n"
      "ds_read_b128 v[248:251], v40 offset:32768\n"
      "v_mfma_f32_16x16x32_bf16 v[16:19], v[60:63], v[64:67], v[16:19]\n"
      "ds_read_b128 v[252:255], v40 offset:33792\n"
      "v_mfma_f32_16x16x32_bf16 v[20:23], v[64:67], v[64:67], v[20:23]\n"
      "buffer_load_dword v56, v5, s[32:35], s47 offen sc1\n"
      "s_add_u32 m0, s40, 4096\n"
      "v_add_u32 v43, 45056, v7\n"
      "v_mfma_f32_16x16x32_bf16 v[132:135], a[192:195], v[68:71], v[132:135]\n"
      "ds_read_b64_tr_b16 v[98:99], v6 offset:26624\n"
      "v_mfma_f32_16x16x32_bf16 v[148:151], a[196:199], v[68:71], v[148:151]\n"
      "global_load_lds_dwordx4 v43, s[6:7] offset:0 sc1\n"
      "ds_read_b64_tr_b16 v[100:101], v33 offset:24576\n"
      "v_mfma_f32_16x16x32_bf16 v[164:167], a[200:203], v[68:71], v[164:167]\n"
      "ds_read_b64_tr_b16 v[102:103], v33 offset:26624\n"
      "s_waitcnt lgkmcnt(3)\n"
      "v_mfma_f32_16x16x32_bf16 v[136:139], a[192:195], v[72:75], v[136:139]\n"
      "ds_read_b64_tr_b16 v[104:105], v34 offset:24576\n"
      "v_add3_u32 v55, v55, v248, v249\n"
      "v_mfma_f32_16x16x32_bf16 v[152:155], a[196:199], v[72:75], v[152:155]\n"
      "ds_read_b64_tr_b16 v[106:107], v34 offset:26624\n"
      "v_add3_u32 v55, v55, v250, v251\n"
      "v_mfma_f32_16x16x32_bf16 v[168:171], a[200:203], v[72:75], v[168:171]\n"
      "global_load_lds_dwordx4 v43, s[6:7] offset:1024 sc1\n"
      "v_add3_u32 v55, v55, v252, v253\n"
      "v_mfma_f32_16x16x32_bf16 v[140:143], a[192:195], v[76:79], v[140:143]\n"
      "ds_read_b64_tr_b16 v[108:109], v35 offset:24576\n"
      "v_add3_u32 v55, v55, v254, v255\n"
      "v_mfma_f32_16x16x32_bf16 v[156:159], a[196:199], v[76:79], v[156:159]\n"
      "ds_read_b64_tr_b16 v[110:111], v35 offset:26624\n"
      "v_mfma_f32_16x16x32_bf16 v[172:175], a[200:203], v[76:79], v[172:175]\n"
      "ds_read_b128 v[248:251], v40 offset:34816\n"
      "ds_read_b128 v[252:255], v40 offset:35840\n"
      "ds_read_b64_tr_b16 v[112:113], v6 offset:28672\n"
      "v_mfma_f32_16x16x32_bf16 v[128:131], a[204:207], v[80:83], v[128:131]\n"
      "global_load_lds_dwordx4 v43, s[6:7] offset:2048 sc1\n"
      "ds_read_b64_tr_b16 v[114:115], v6 offset:30720\n"
      "v_mfma_f32_16x16x32_bf16 v[144:147], a[208:211], v[80:83], v[144:147]\n"
      "ds_read_b64_tr_b16 v[116:117], v33 offset:28672\n"
      "v_mfma_f32_16x16x32_bf16 v[160:163], a[212:215], v[80:83], v[160:163]\n"
      "ds_read_b64_tr_b16 v[118:119], v33 offset:30720\n"
      "v_mfma_f32_16x16x32_bf16 v[16:19], v[60:63], v[80:83], v[16:19]\n"
      "ds_read_b64_tr_b16 v[120:121], v34 offset:28672\n"
      "s_waitcnt lgkmcnt(5)\n"
      "v_mfma_f32_16x16x32_bf16 v[20:23], v[80:83], v[80:83], v[20:23]\n"
      "global_load_lds_dwordx4 v43, s[6:7] offset:3072 sc1\n"
      "v_add3_u32 v55, v55, v248, v249\n"
      "v_add3_u32 v55, v55, v250, v251\n"
      "v_add3_u32 v55, v55, v252, v253\n"
      "v_add3_u32 v55, v55, v254, v255\n"
      "v_mfma_f32_16x16x32_bf16 v[132:135], a[204:207], v[84:87], v[132:135]\n"
      "ds_read_b64_tr_b16 v[122:123], v34 offset:30720\n"
      "s_add_u32 m0, s41, 0\n"
      "v_add_u32 v43, 50176, v7\n"
      "v_mfma_f32_16x16x32_bf16 v[148:151], a[208:211], v[84:87], v[148:151]\n"
      "global_load_lds_dwordx4 v43, s[6:7] offset:0 sc1\n"
      "global_load_lds_dwordx4 v43, s[6:7] offset:1024 sc1\n"
      "global_load_lds_dwordx4 v43, s[6:7] offset:2048 sc1\n"
      "global_load_lds_dwordx4 v43, s[6:7] offset:3072 sc1\n"
      "ds_read_b64_tr_b16 v[124:125], v35 offset:28672\n"
      "v_add_u32_dpp v55, v55, v55 quad_perm:[1,0,3,2] row_mask:0xf bank_mask:0xf\n"
      "s_add_u32 m0, s73, 0\n"
      "v_add_u32 v43, 131072, v40\n"
      "v_mfma_f32_16x16x32_bf16 v[164:167], a[212:215], v[84:87], v[164:167]\n"
      "global_load_lds_dwordx4 v43, s[4:5] offset:0 sc1\n"
      "v_mfma_f32_16x16x32_bf16 v[136:139], a[204:207], v[88:91], v[136:139]\n"
      "ds_read_b64_tr_b16 v[126:127], v35 offset:30720\n"
      "v_add_u32_dpp v55, v55, v55 quad_perm:[2,3,0,1] row_mask:0xf bank_mask:0xf\n"
      "v_mfma_f32_16x16x32_bf16 v[152:155], a[208:211], v[88:91], v[152:155]\n"
      "v_mfma_f32_16x16x32_bf16 v[168:171], a[212:215], v[88:91], v[168:171]\n"
      "v_add_u32_dpp v55, v55, v55 row_half_mirror row_mask:0xf bank_mask:0xf\n"
      "v_cmp_ne_u32 vcc, s71, v55\n"
      "v_mfma_f32_16x16x32_bf16 v[140:143], a[204:207], v[92:95], v[140:143]\n"
      "global_load_lds_dwordx4 v43, s[4:5] offset:1024 sc1\n"
      "v_mfma_f32_16x16x32_bf16 v[156:159], a[208:211], v[92:95], v[156:159]\n"
      "s_cbranch_vccnz L_sx6_%=\n"
      "L_bx6_%=:\n"
      "v_mfma_f32_16x16x32_bf16 v[172:175], a[212:215], v[92:95], v[172:175]\n"
      "s_waitcnt vmcnt(19)\n"
      "s_waitcnt lgkmcnt(0)\n"
      "s_barrier\n"
      "v_mfma_f32_16x16x32_bf16 v[16:19], v[60:63], v[96:99], v[16:19]\n"
      "ds_read_b64_tr_b16 v[64:65], v36 offset:32768\n"
      "v_mfma_f32_16x16x32_bf16 v[20:23], v[96:99], v[96:99], v[20:23]\n"
      "global_load_lds_dwordx4 v43, s[4:5] offset:2048 sc1\n"
      "v_mfma_f32_16x16x32_bf16 v[16:19], v[60:63], v[112:115], v[16:19]\n"
      "ds_read_b64_tr_b16 v[66:67], v36 offset:34816\n"
      "v_mfma_f32_16x16x32_bf16 v[20:23], v[112:115], v[112:115], v[20:23]\n"
      "ds_read_b64_tr_b16 v[68:69], v37 offset:32768\n"
      "v_mfma_f32_16x16x32_bf16 v[128:131], a[216:219], v[96:99], v[128:131]\n"
      "ds_read_b64_tr_b16 v[70:71], v37 offset:34816\n"
      "v_mfma_f32_16x16x32_bf16 v[144:147], a[220:223], v[96:99], v[144:147]\n"
      "global_load_lds_dwordx4 v43, s[4:5] offset:3072 sc1\n"
      "s_mov_b32 s47, 49184\n"
      "v_mfma_f32_16x16x32_bf16 v[160:163], a[224:227], v[96:99], v[160:163]\n"
      "ds_read_b64_tr_b16 v[72:73], v38 offset:32768\n"
      "v_cndmask_b32 v58, v20, v21, s[48:49]\n"
      "v_mfma_f32_16x16x32_bf16 v[132:135], a[216:219], v[100:103], v[132:135]\n"
      "buffer_load_dword v32, v5, s[32:35], s47 offen sc1\n"
      "s_add_u32 m0, s41, 4096\n"
      "v_add_u32 v43, 54272, v7\n"
      "v_mfma_f32_16x16x32_bf16 v[148:151], a[220:223], v[100:103], v[148:151]\n"
      "ds_read_b64_tr_b16 v[74:75], v38 offset:34816\n"
      "v_cndmask_b32 v59, v22, v23, s[48:49]\n"
      "v_mfma_f32_16x16x32_bf16 v[164:167], a[224:227], v[100:103], v[164:167]\n"
      "global_load_lds_dwordx4 v43, s[6:7] offset:0 sc1\n"
      "v_cndmask_b32 v58, v58, v59, s[50:51]\n"
      "v_mfma_f32_16x16x32_bf16 v[136:139], a[216:219], v[104:107], v[136:139]\n"
      "ds_read_b64_tr_b16 v[76:77], v39 offset:32768\n"
      "v_mul_f32 v16, 0x3acccccd, v16\n"
      "v_mfma_f32_16x16x32_bf16 v[152:155], a[220:223], v[104:107], v[152:155]\n"
      "ds_read_b64_tr_b16 v[78:79], v39 offset:34816\n"
      "v_mul_f32 v58, 0x3acccccd, v58\n"
      "v_mfma_f32_16x16x32_bf16 v[168:171], a[224:227], v[104:107], v[168:171]\n"
      "ds_read_b64_tr_b16 v[80:81], v36 offset:36864\n"
      "v_fma_f32 v58, -v16, v16, v58\n"
      "v_mfma_f32_16x16x32_bf16 v[140:143], a[216:219], v[108:111], v[140:143]\n"
      "global_load_lds_dwordx4 v43, s[6:7] offset:1024 sc1\n"
      "v_max_f32 v58, 0, v58\n"
      "v_mfma_f32_16x16x32_bf16 v[156:159], a[220:223], v[108:111], v[156:159]\n"
      "ds_read_b64_tr_b16 v[82:83], v36 offset:38912\n"
      "v_cvt_pk_bf16_f32 v58, 0, v58\n"
      "v_mfma_f32_16x16x32_bf16 v[172:175], a[224:227], v[108:111], v[172:175]\n"
      "ds_read_b64_tr_b16 v[8:9], v6 offset:32768\n"
      "v_add_f32 v58, 0x3727c5ac, v58\n"
      "v_mfma_f32_16x16x32_bf16 v[128:131], a[228:231], v[112:115], v[128:131]\n"
      "ds_read_b64_tr_b16 v[84:85], v37 offset:36864\n"
      "v_cvt_pk_bf16_f32 v58, 0, v58\n"
      "v_mfma_f32_16x16x32_bf16 v[144:147], a[232:235], v[112:115], v[144:147]\n"
      "global_load_lds_dwordx4 v43, s[6:7] offset:2048 sc1\n"
      "ds_read_b64_tr_b16 v[86:87], v37 offset:38912\n"
      "ds_read_b64_tr_b16 v[10:11], v6 offset:34816\n"
      "v_mfma_f32_16x16x32_bf16 v[160:163], a[236:239], v[112:115], v[160:163]\n"
      "global_load_lds_dwordx4 v43, s[6:7] offset:3072 sc1\n"
      "ds_read_b64_tr_b16 v[88:89], v38 offset:36864\n"
      "s_add_u32 m0, s40, 8192\n"
      "v_add_u32 v43, 49152, v7\n"
      "v_mfma_f32_16x16x32_bf16 v[132:135], a[228:231], v[116:119], v[132:135]\n"
      "global_load_lds_dwordx4 v43, s[6:7] offset:0 sc1\n"
      "ds_read_b64_tr_b16 v[90:91], v38 offset:38912\n"
      "ds_read_b64_tr_b16 v[12:13], v6 offset:36864\n"
      "s_add_u32 m0, s73, 16384\n"
      "v_add_u32 v43, 147456, v40\n"
      "v_mfma_f32_16x16x32_bf16 v[148:151], a[232:235], v[116:119], v[148:151]\n"
      "global_load_lds_dwordx4 v43, s[4:5] offset:0 sc1\n"
      "global_load_dwordx4 a[240:243], v44, s[6:7] offset:-4096 sc1\n"
      "ds_read_b64_tr_b16 v[92:93], v39 offset:36864\n"
      "v_sqrt_f32 v58, v58\n"
      "v_sub_f32 v16, 0, v16\n"
      "v_mfma_f32_16x16x32_bf16 v[164:167], a[236:239], v[116:119], v[164:167]\n"
      "ds_read_b64_tr_b16 v[94:95], v39 offset:38912\n"
      "v_cvt_pk_bf16_f32 v58, 0, v58\n"
      "v_mfma_f32_16x16x32_bf16 v[136:139], a[228:231], v[120:123], v[136:139]\n"
      "ds_read_b64_tr_b16 v[14:15], v6 offset:38912\n"
      "v_rcp_f32 v17, v58\n"
      "v_cvt_pk_bf16_f32 v16, 0, v16\n"
      "v_mfma_f32_16x16x32_bf16 v[152:155], a[232:235], v[120:123], v[152:155]\n"
      "ds_write_b64 v50, v[16:17] offset:0\n"
      "v_mfma_f32_16x16x32_bf16 v[168:171], a[236:239], v[120:123], v[168:171]\n"
      "global_load_lds_dwordx4 v43, s[4:5] offset:1024 sc1\n"
      "global_load_dwordx4 a[244:247], v44, s[6:7] offset:-3072 sc1\n"
      "v_mfma_f32_16x16x32_bf16 v[140:143], a[228:231], v[124:127], v[140:143]\n"
      "v_mfma_f32_16x16x32_bf16 v[156:159], a[232:235], v[124:127], v[156:159]\n"
      "v_mfma_f32_16x16x32_bf16 v[172:175], a[236:239], v[124:127], v[172:175]\n"
      "s_waitcnt lgkmcnt(0)\n"
      "s_barrier\n"
      "v_mfma_f32_16x16x32_bf16 v[24:27], v[60:63], v[8:11], v[24:27]\n"
      "global_load_lds_dwordx4 v43, s[4:5] offset:2048 sc1\n"
      "global_load_dwordx4 a[248:251], v44, s[6:7] offset:-2048 sc1\n"
      "ds_read_b64 v[16:17], v51\n"
      "v_xor_b32 v58, 128, v51\n"
      "v_mfma_f32_16x16x32_bf16 v[28:31], v[8:11], v[8:11], v[28:31]\n"
      "ds_read_b64 v[18:19], v58\n"
      "v_xor_b32 v58, 256, v51\n"
      "v_mfma_f32_16x16x32_bf16 v[24:27], v[60:63], v[12:15], v[24:27]\n"
      "ds_read_b64 v[20:21], v58\n"
      "s_waitcnt vmcnt(24)\n"
      "v_xor_b32 v58, 384, v51\n"
      "v_mfma_f32_16x16x32_bf16 v[28:31], v[12:15], v[12:15], v[28:31]\n"
      "ds_read_b64 v[22:23], v58\n"
      "v_mfma_f32_16x16x32_bf16 v[176:179], a[48:51], v[64:67], v[176:179]\n"
      "global_load_lds_dwordx4 v43, s[4:5] offset:3072 sc1\n"
      "global_load_dwordx4 a[252:255], v44, s[6:7] offset:-1024 sc1\n"
      "ds_read_b128 v[248:251], v40 offset:49152\n"
      "ds_read_b128 v[252:255], v40 offset:50176\n"
      "ds_read_b64_tr_b16 v[96:97], v36 offset:40960\n"
      "s_mov_b32 s47, 65568\n"
      "v_mfma_f32_16x16x32_bf16 v[184:187], a[52:55], v[64:67], v[184:187]\n"
      "ds_read_b64_tr_b16 v[98:99], v36 offset:43008\n"
      "v_mfma_f32_16x16x32_bf16 v[192:195], a[56:59], v[64:67], v[192:195]\n"
      "buffer_load_dword v4, v5, s[32:35], s47 offen sc1\n"
      "ds_read_b64_tr_b16 v[100:101], v37 offset:40960\n"
      "v_mfma_f32_16x16x32_bf16 v[180:183], a[48:51], v[68:71], v[180:183]\n"
      "ds_read_b64_tr_b16 v[102:103], v37 offset:43008\n"
      "s_waitcnt lgkmcnt(6)\n"
      "v_mfma_f32_16x16x32_bf16 v[188:191], a[52:55], v[68:71], v[188:191]\n"
      "global_load_dwordx4 a[0:3], v44, s[6:7] offset:0 sc1\n"
      "ds_read_b64_tr_b16 v[104:105], v38 offset:40960\n"
      "v_fmac_f32 v128, v16, v224\n"
      "v_fmac_f32 v129, v16, v225\n"
      "v_mfma_f32_16x16x32_bf16 v[196:199], a[56:59], v[68:71], v[196:199]\n"
      "ds_read_b64_tr_b16 v[106:107], v38 offset:43008\n"
      "s_waitcnt lgkmcnt(6)\n"
      "v_add3_u32 v56, v56, v248, v249\n"
      "v_add3_u32 v56, v56, v250, v251\n"
      "v_fmac_f32 v130, v16, v226\n"
      "v_fmac_f32 v131, v16, v227\n"
      "v_mfma_f32_16x16x32_bf16 v[200:203], a[48:51], v[72:75], v[200:203]\n"
      "ds_read_b64_tr_b16 v[108:109], v39 offset:40960\n"
      "v_add3_u32 v56, v56, v252, v253\n"
      "v_fma_f32 v128, v128, v17, v236\n"
      "v_fma_f32 v129, v129, v17, v237\n"
      "v_mfma_f32_16x16x32_bf16 v[208:211], a[52:55], v[72:75], v[208:211]\n"
      "ds_read_b64_tr_b16 v[110:111], v39 offset:43008\n"
      "v_add3_u32 v56, v56, v254, v255\n"
      "v_fma_f32 v130, v130, v17, v238\n"
      "v_fma_f32 v131, v131, v17, v239\n"
      "v_mfma_f32_16x16x32_bf16 v[216:219], a[56:59], v[72:75], v[216:219]\n"
      "global_load_dwordx4 a[4:7], v44, s[6:7] offset:1024 sc1\n"
      "v_mul_f32 v128, v128, v130\n"
      "v_mul_f32 v129, v129, v131\n"
      "v_cvt_pk_bf16_f32 v128, v128, v129\n"
      "v_mfma_f32_16x16x32_bf16 v[204:207], a[48:51], v[76:79], v[204:207]\n"
      "ds_read_b128 v[248:251], v40 offset:51200\n"
      "ds_read_b128 v[252:255], v40 offset:52224\n"
      "ds_write_b32 v46, v128 offset:0\n"
      "ds_read_b64_tr_b16 v[112:113], v36 offset:45056\n"
      "v_fmac_f32 v144, v16, v228\n"
      "v_mfma_f32_16x16x32_bf16 v[212:215], a[52:55], v[76:79], v[212:215]\n"
      "ds_read_b64_tr_b16 v[114:115], v36 offset:47104\n"
      "v_fmac_f32 v145, v16, v229\n"
      "v_mfma_f32_16x16x32_bf16 v[220:223], a[56:59], v[76:79], v[220:223]\n"
      "ds_read_b64_tr_b16 v[8:9], v6 offset:40960\n"
      "v_fmac_f32 v146, v16, v230\n"
      "v_fmac_f32 v147, v16, v231\n"
      "v_mfma_f32_16x16x32_bf16 v[176:179], a[60:63], v[80:83], v[176:179]\n"
      "global_load_dwordx4 a[8:11], v44, s[6:7] offset:2048 sc1\n"
      "ds_read_b64_tr_b16 v[116:117], v37 offset:45056\n"
      "v_fma_f32 v144, v144, v17, v240\n"
      "v_fma_f32 v145, v145, v17, v241\n"
      "v_mfma_f32_16x16x32_bf16 v[184:187], a[64:67], v[80:83], v[184:187]\n"
      "ds_read_b64_tr_b16 v[118:119], v37 offset:47104\n"
      "ds_read_b64_tr_b16 v[10:11], v6 offset:43008\n"
      "v_fma_f32 v146, v146, v17, v242\n"
      "v_fma_f32 v147, v147, v17, v243\n"
      "v_mfma_f32_16x16x32_bf16 v[192:195], a[68:71], v[80:83], v[192:195]\n"
      "ds_read_b64_tr_b16 v[120:121], v38 offset:45056\n"
      "s_waitcnt lgkmcnt(8)\n"
      "v_add3_u32 v56, v56, v248, v249\n"
      "v_add3_u32 v56, v56, v250, v251\n"
      "v_add3_u32 v56, v56, v252, v253\n"
      "v_add3_u32 v56, v56, v254, v255\n"
      "v_mul_f32 v144, v144, v146\n"
      "v_mul_f32 v145, v145, v147\n"
      "v_cvt_pk_bf16_f32 v144, v144, v145\n"
      "v_mfma_f32_16x16x32_bf16 v[180:183], a[60:63], v[84:87], v[180:183]\n"
      "ds_write_b32 v46, v144 offset:4096\n"
      "ds_read_b64_tr_b16 v[122:123], v38 offset:47104\n"
      "ds_read_b64_tr_b16 v[12:13], v6 offset:45056\n"
      "v_fmac_f32 v160, v16, v232\n"
      "v_mfma_f32_16x16x32_bf16 v[188:191], a[64:67], v[84:87], v[188:191]\n"
      "global_load_dwordx4 a[12:15], v44, s[6:7] offset:3072 sc1\n"
      "ds_read_b64_tr_b16 v[124:125], v39 offset:45056\n"
      "v_add_u32_dpp v56, v56, v56 quad_perm:[1,0,3,2] row_mask:0xf bank_mask:0xf\n"
      "v_fmac_f32 v161, v16, v233\n"
      "v_mfma_f32_16x16x32_bf16 v[196:199], a[68:71], v[84:87], v[196:199]\n"
      "ds_read_b64_tr_b16 v[126:127], v39 offset:47104\n"
      "v_fmac_f32 v162, v16, v234\n"
      "v_fmac_f32 v163, v16, v235\n"
      "v_mfma_f32_16x16x32_bf16 v[200:203], a[60:63], v[88:91], v[200:203]\n"
      "ds_read_b64_tr_b16 v[14:15], v6 offset:47104\n"
      "v_add_u32 v44, 12288, v7\n"
      "v_add_u32_dpp v56, v56, v56 quad_perm:[2,3,0,1] row_mask:0xf bank_mask:0xf\n"
      "v_fma_f32 v160, v160, v17, v244\n"
      "v_fma_f32 v161, v161, v17, v245\n"
      "v_mfma_f32_16x16x32_bf16 v[208:211], a[64:67], v[88:91], v[208:211]\n"
      "v_fma_f32 v162, v162, v17, v246\n"
      "v_fma_f32 v163, v163, v17, v247\n"
      "v_mfma_f32_16x16x32_bf16 v[216:219], a[68:71], v[88:91], v[216:219]\n"
      "global_load_dwordx4 a[16:19], v44, s[6:7] offset:-4096 sc1\n"
      "v_add_u32_dpp v56, v56, v56 row_half_mirror row_mask:0xf bank_mask:0xf\n"
      "v_cmp_ne_u32 vcc, s71, v56\n"
      "v_mul_f32 v160, v160, v162\n"
      "v_mul_f32 v161, v161, v163\n"
      "v_cvt_pk_bf16_f32 v160, v160, v161\n"
      "v_mfma_f32_16x16x32_bf16 v[204:207], a[60:63], v[92:95], v[204:207]\n"
      "ds_write_b32 v46, v160 offset:8192\n"
      "v_xor_b32 v59, 1024, v46\n"
      "v_fmac_f32 v132, v18, v224\n"
      "v_mfma_f32_16x16x32_bf16 v[212:215], a[64:67], v[92:95], v[212:215]\n"
      "s_cbranch_vccnz L_sx7_%=\n"
      "L_bx7_%=:\n"
      "v_fmac_f32 v133, v18, v225\n"
      "v_mfma_f32_16x16x32_bf16 v[220:223], a[68:71], v[92:95], v[220:223]\n"
      "v_fmac_f32 v134, v18, v226\n"
      "s_waitcnt lgkmcnt(1)\n"
      "s_barrier\n"
      "v_mfma_f32_16x16x32_bf16 v[24:27], v[60:63], v[8:11], v[24:27]\n"
      "global_load_dwordx4 a[20:23], v44, s[6:7] offset:-3072 sc1\n"
      "v_fmac_f32 v135, v18, v227\n"
      "v_fma_f32 v132, v132, v19, v236\n"
      "v_mfma_f32_16x16x32_bf16 v[28:31], v[8:11], v[8:11], v[28:31]\n"
      "ds_read_b64_tr_b16 v[64:65], v36 offset:49152\n"
      "v_fma_f32 v133, v133, v19, v237\n"
      "v_fma_f32 v134, v134, v19, v238\n"
      "v_mfma_f32_16x16x32_bf16 v[24:27], v[60:63], v[12:15], v[24:27]\n"
      "ds_read_b64_tr_b16 v[66:67], v36 offset:51200\n"
      "v_fma_f32 v135, v135, v19, v239\n"
      "v_mul_f32 v132, v132, v134\n"
      "v_mfma_f32_16x16x32_bf16 v[28:31], v[12:15], v[12:15], v[28:31]\n"
      "ds_read_b64_tr_b16 v[68:69], v37 offset:49152\n"
      "s_waitcnt vmcnt(20)\n"
      "v_mul_f32 v133, v133, v135\n"
      "v_cvt_pk_bf16_f32 v132, v132, v133\n"
      "v_mfma_f32_16x16x32_bf16 v[176:179], a[72:75], v[96:99], v[176:179]\n"
      "global_load_dwordx4 a[24:27], v44, s[6:7] offset:-2048 sc1\n"
      "ds_write_b32 v59, v132 offset:0\n"
      "ds_read_b128 v[248:251], v40 offset:0\n"
      "v_fmac_f32 v148, v18, v228\n"
      "v_mfma_f32_16x16x32_bf16 v[184:187], a[76:79], v[96:99], v[184:187]\n"
      "ds_read_b128 v[252:255], v40 offset:1024\n"
      "v_fmac_f32 v149, v18, v229\n"
      "v_fmac_f32 v150, v18, v230\n"
      "v_mfma_f32_16x16x32_bf16 v[192:195], a[80:83], v[96:99], v[192:195]\n"
      "ds_read_b64_tr_b16 v[70:71], v37 offset:51200\n"
      "v_fmac_f32 v151, v18, v231\n"
      "v_fma_f32 v148, v148, v19, v240\n"
      "v_mfma_f32_16x16x32_bf16 v[180:183], a[72:75], v[100:103], v[180:183]\n"
      "ds_read_b64_tr_b16 v[72:73], v38 offset:49152\n"
      "v_fma_f32 v149, v149, v19, v241\n"
      "v_fma_f32 v150, v150, v19, v242\n"
      "v_mfma_f32_16x16x32_bf16 v[188:191], a[76:79], v[100:103], v[188:191]\n"
      "global_load_dwordx4 a[28:31], v44, s[6:7] offset:-1024 sc1\n"
      "v_fma_f32 v151, v151, v19, v243\n"
      "v_mul_f32 v148, v148, v150\n"
      "v_mfma_f32_16x16x32_bf16 v[196:199], a[80:83], v[100:103], v[196:199]\n"
      "ds_read_b64_tr_b16 v[74:75], v38 offset:51200\n"
      "v_mul_f32 v149, v149, v151\n"
      "v_cvt_pk_bf16_f32 v148, v148, v149\n"
      "v_mfma_f32_16x16x32_bf16 v[200:203], a[72:75], v[104:107], v[200:203]\n"
      "ds_write_b32 v59, v148 offset:4096\n"
      "s_waitcnt lgkmcnt(4)\n"
      "v_add3_u32 v32, v32, v248, v249\n"
      "v_add3_u32 v32, v32, v250, v251\n"
      "v_fmac_f32 v164, v18, v232\n"
      "v_mfma_f32_16x16x32_bf16 v[208:211], a[76:79], v[104:107], v[208:211]\n"
      "ds_read_b64_tr_b16 v[76:77], v39 offset:49152\n"
      "v_add3_u32 v32, v32, v252, v253\n"
      "v_fmac_f32 v165, v18, v233\n"
      "v_fmac_f32 v166, v18, v234\n"
      "v_mfma_f32_16x16x32_bf16 v[216:219], a[80:83], v[104:107], v[216:219]\n"
      "global_load_dwordx4 a[32:35], v44, s[6:7] offset:0 sc1\n"
      "ds_read_b64_tr_b16 v[78:79], v39 offset:51200\n"
      "v_add3_u32 v32, v32, v254, v255\n"
      "v_fmac_f32 v167, v18, v235\n"
      "v_fma_f32 v164, v164, v19, v244\n"
      "v_mfma_f32_16x16x32_bf16 v[204:207], a[72:75], v[108:111], v[204:207]\n"
      "ds_read_b64_tr_b16 v[80:81], v36 offset:53248\n"
      "v_fma_f32 v165, v165, v19, v245\n"
      "v_fma_f32 v166, v166, v19, v246\n"
      "v_mfma_f32_16x16x32_bf16 v[212:215], a[76:79], v[108:111], v[212:215]\n"
      "ds_read_b128 v[248:251], v40 offset:2048\n"
      "ds_read_b128 v[252:255], v40 offset:3072\n"
      "ds_read_b64_tr_b16 v[82:83], v36 offset:55296\n"
      "v_fma_f32 v167, v167, v19, v247\n"
      "v_mul_f32 v164, v164, v166\n"
      "v_mfma_f32_16x16x32_bf16 v[220:223], a[80:83], v[108:111], v[220:223]\n"
      "ds_read_b64_tr_b16 v[8:9], v6 offset:49152\n"
      "v_mul_f32 v165, v165, v167\n"
      "v_cvt_pk_bf16_f32 v164, v164, v165\n"
      "v_mfma_f32_16x16x32_bf16 v[176:179], a[84:87], v[112:115], v[176:179]\n"
      "global_load_dwordx4 a[36:39], v44, s[6:7] offset:1024 sc1\n"
      "ds_write_b32 v59, v164 offset:8192\n"
      "ds_read_b64_tr_b16 v[84:85], v37 offset:53248\n"
      "v_xor_b32 v59, 2048, v46\n"
      "v_mfma_f32_16x16x32_bf16 v[184:187], a[88:91], v[112:115], v[184:187]\n"
      "ds_read_b64_tr_b16 v[86:87], v37 offset:55296\n"
      "ds_read_b64_tr_b16 v[10:11], v6 offset:51200\n"
      "v_fmac_f32 v136, v20, v224\n"
      "v_fmac_f32 v137, v20, v225\n"
      "v_mfma_f32_16x16x32_bf16 v[192:195], a[92:95], v[112:115], v[192:195]\n"
      "ds_read_b64_tr_b16 v[88:89], v38 offset:53248\n"
      "v_fmac_f32 v138, v20, v226\n"
      "v_fmac_f32 v139, v20, v227\n"
      "v_mfma_f32_16x16x32_bf16 v[180:183], a[84:87], v[116:119], v[180:183]\n"
      "ds_read_b64_tr_b16 v[90:91], v38 offset:55296\n"
      "ds_read_b64_tr_b16 v[12:13], v6 offset:53248\n"
      "s_waitcnt lgkmcnt(9)\n"
      "v_add3_u32 v32, v32, v248, v249\n"
      "v_add3_u32 v32, v32, v250, v251\n"
      "v_add3_u32 v32, v32, v252, v253\n"
      "v_add3_u32 v32, v32, v254, v255\n"
      "v_fma_f32 v136, v136, v21, v236\n"
      "v_fma_f32 v137, v137, v21, v237\n"
      "v_mfma_f32_16x16x32_bf16 v[188:191], a[88:91], v[116:119], v[188:191]\n"
      "global_load_dwordx4 a[40:43], v44, s[6:7] offset:2048 sc1\n"
      "ds_read_b64_tr_b16 v[92:93], v39 offset:53248\n"
      "v_fma_f32 v138, v138, v21, v238\n"
      "v_fma_f32 v139, v139, v21, v239\n"
      "v_mfma_f32_16x16x32_bf16 v[196:199], a[92:95], v[116:119], v[196:199]\n"
      "ds_read_b64_tr_b16 v[94:95], v39 offset:55296\n"
      "v_add_u32_dpp v32, v32, v32 quad_perm:[1,0,3,2] row_mask:0xf bank_mask:0xf\n"
      "v_mul_f32 v136, v136, v138\n"
      "v_mul_f32 v137, v137, v139\n"
      "v_cvt_pk_bf16_f32 v136, v136, v137\n"
      "v_mfma_f32_16x16x32_bf16 v[200:203], a[84:87], v[120:123], v[200:203]\n"
      "ds_write_b32 v59, v136 offset:0\n"
      "ds_read_b64_tr_b16 v[14:15], v6 offset:55296\n"
      "v_fmac_f32 v152, v20, v228\n"
      "v_mfma_f32_16x16x32_bf16 v[208:211], a[88:91], v[120:123], v[208:211]\n"
      "v_add_u32_dpp v32, v32, v32 quad_perm:[2,3,0,1] row_mask:0xf bank_mask:0xf\n"
      "v_fmac_f32 v153, v20, v229\n"
      "v_mfma_f32_16x16x32_bf16 v[216:219], a[92:95], v[120:123], v[216:219]\n"
      "global_load_dwordx4 a[44:47], v44, s[6:7] offset:3072 sc1\n"
      "v_fmac_f32 v154, v20, v230\n"
      "v_fmac_f32 v155, v20, v231\n"
      "v_mfma_f32_16x16x32_bf16 v[204:207], a[84:87], v[124:127], v[204:207]\n"
      "v_add_u32_dpp v32, v32, v32 row_half_mirror row_mask:0xf bank_mask:0xf\n"
      "v_cmp_ne_u32 vcc, s71, v32\n"
      "v_fma_f32 v152, v152, v21, v240\n"
      "v_fma_f32 v153, v153, v21, v241\n"
      "v_mfma_f32_16x16x32_bf16 v[212:215], a[88:91], v[124:127], v[212:215]\n"
      "v_add_u32 v44, 20480, v7\n"
      "v_fma_f32 v154, v154, v21, v242\n"
      "v_fma_f32 v155, v155, v21, v243\n"
      "v_mfma_f32_16x16x32_bf16 v[220:223], a[92:95], v[124:127], v[220:223]\n"
      "s_cbranch_vccnz L_sx8_%=\n"
      "L_bx8_%=:\n"
      "v_mul_f32 v152, v152, v154\n"
      "v_mul_f32 v153, v153, v155\n"
      "v_cvt_pk_bf16_f32 v152, v152, v153\n"
      "s_waitcnt lgkmcnt(0)\n"
      "v_mfma_f32_16x16x32_bf16 v[24:27], v[60:63], v[8:11], v[24:27]\n"
      "global_load_dwordx4 a[48:51], v44, s[6:7] offset:-4096 sc1\n"
      "ds_write_b32 v59, v152 offset:4096\n"
      "ds_read_b64_tr_b16 v[96:97], v36 offset:57344\n"
      "v_fmac_f32 v168, v20, v232\n"
      "v_mfma_f32_16x16x32_bf16 v[28:31], v[8:11], v[8:11], v[28:31]\n"
      "ds_read_b64_tr_b16 v[98:99], v36 offset:59392\n"
      "v_fmac_f32 v169, v20, v233\n"
      "v_mfma_f32_16x16x32_bf16 v[24:27], v[60:63], v[12:15], v[24:27]\n"
      "ds_read_b64_tr_b16 v[100:101], v37 offset:57344\n"
      "s_waitcnt vmcnt(13)\n"
      "v_fmac_f32 v170, v20, v234\n"
      "v_fmac_f32 v171, v20, v235\n"
      "v_mfma_f32_16x16x32_bf16 v[28:31], v[12:15], v[12:15], v[28:31]\n"
      "ds_read_b128 v[248:251], v40 offset:16384\n"
      "v_fma_f32 v168, v168, v21, v244\n"
      "v_fma_f32 v169, v169, v21, v245\n"
      "v_mfma_f32_16x16x32_bf16 v[176:179], a[96:99], v[64:67], v[176:179]\n"
      "global_load_dwordx4 a[52:55], v44, s[6:7] offset:-3072 sc1\n"
      "ds_read_b128 v[252:255], v40 offset:17408\n"
      "v_fma_f32 v170, v170, v21, v246\n"
      "v_fma_f32 v171, v171, v21, v247\n"
      "v_mfma_f32_16x16x32_bf16 v[184:187], a[100:103], v[64:67], v[184:187]\n"
      "ds_read_b64_tr_b16 v[102:103], v37 offset:59392\n"
      "v_mul_f32 v168, v168, v170\n"
      "v_mul_f32 v169, v169, v171\n"
      "v_cvt_pk_bf16_f32 v168, v168, v169\n"
      "v_mfma_f32_16x16x32_bf16 v[192:195], a[104:107], v[64:67], v[192:195]\n"
      "ds_write_b32 v59, v168 offset:8192\n"
      "ds_read_b64_tr_b16 v[104:105], v38 offset:57344\n"
      "v_xor_b32 v59, 3072, v46\n"
      "v_mfma_f32_16x16x32_bf16 v[180:183], a[96:99], v[68:71], v[180:183]\n"
      "ds_read_b64_tr_b16 v[106:107], v38 offset:59392\n"
      "v_fmac_f32 v140, v22, v224\n"
      "v_mfma_f32_16x16x32_bf16 v[188:191], a[100:103], v[68:71], v[188:191]\n"
      "global_load_dwordx4 a[56:59], v44, s[6:7] offset:-2048 sc1\n"
      "v_fmac_f32 v141, v22, v225\n"
      "v_fmac_f32 v142, v22, v226\n"
      "v_mfma_f32_16x16x32_bf16 v[196:199], a[104:107], v[68:71], v[196:199]\n"
      "ds_read_b64_tr_b16 v[108:109], v39 offset:57344\n"
      "s_waitcnt lgkmcnt(5)\n"
      "v_add3_u32 v4, v4, v248, v249\n"
      "v_add3_u32 v4, v4, v250, v251\n"
      "v_fmac_f32 v143, v22, v227\n"
      "v_fma_f32 v140, v140, v23, v236\n"
      "v_mfma_f32_16x16x32_bf16 v[200:203], a[96:99], v[72:75], v[200:203]\n"
      "ds_read_b64_tr_b16 v[110:111], v39 offset:59392\n"
      "v_add3_u32 v4, v4, v252, v253\n"
      "v_fma_f32 v141, v141, v23, v237\n"
      "v_fma_f32 v142, v142, v23, v238\n"
      "v_mfma_f32_16x16x32_bf16 v[208:211], a[100:103], v[72:75], v[208:211]\n"
      "ds_read_b64_tr_b16 v[112:113], v36 offset:61440\n"
      "v_add3_u32 v4, v4, v254, v255\n"
      "v_fma_f32 v143, v143, v23, v239\n"
      "v_mul_f32 v140, v140, v142\n"
      "v_mfma_f32_16x16x32_bf16 v[216:219], a[104:107], v[72:75], v[216:219]\n"
      "global_load_dwordx4 a[60:63], v44, s[6:7] offset:-1024 sc1\n"
      "v_mul_f32 v141, v141, v143\n"
      "v_cvt_pk_bf16_f32 v140, v140, v141\n"
      "v_mfma_f32_16x16x32_bf16 v[204:207], a[96:99], v[76:79], v[204:207]\n"
      "ds_read_b128 v[248:251], v40 offset:18432\n"
      "ds_read_b128 v[252:255], v40 offset:19456\n"
      "ds_write_b32 v59, v140 offset:0\n"
      "ds_read_b64_tr_b16 v[114:115], v36 offset:63488\n"
      "v_fmac_f32 v156, v22, v228\n"
      "v_mfma_f32_16x16x32_bf16 v[212:215], a[100:103], v[76:79], v[212:215]\n"
      "ds_read_b64_tr_b16 v[8:9], v6 offset:57344\n"
      "v_fmac_f32 v157, v22, v229\n"
      "v_fmac_f32 v158, v22, v230\n"
      "v_mfma_f32_16x16x32_bf16 v[220:223], a[104:107], v[76:79], v[220:223]\n"
      "ds_read_b64_tr_b16 v[116:117], v37 offset:61440\n"
      "v_fmac_f32 v159, v22, v231\n"
      "v_fma_f32 v156, v156, v23, v240\n"
      "v_mfma_f32_16x16x32_bf16 v[176:179], a[108:111], v[80:83], v[176:179]\n"
      "global_load_dwordx4 a[64:67], v44, s[6:7] offset:0 sc1\n"
      "v_fma_f32 v157, v157, v23, v241\n"
      "v_fma_f32 v158, v158, v23, v242\n"
      "v_mfma_f32_16x16x32_bf16 v[184:187], a[112:115], v[80:83], v[184:187]\n"
      "ds_read_b64_tr_b16 v[118:119], v37 offset:63488\n"
      "ds_read_b64_tr_b16 v[10:11], v6 offset:59392\n"
      "v_fma_f32 v159, v159, v23, v243\n"
      "v_mul_f32 v156, v156, v158\n"
      "v_mfma_f32_16x16x32_bf16 v[192:195], a[116:119], v[80:83], v[192:195]\n"
      "ds_read_b64_tr_b16 v[120:121], v38 offset:61440\n"
      "s_waitcnt lgkmcnt(7)\n"
      "v_add3_u32 v4, v4, v248, v249\n"
      "v_add3_u32 v4, v4, v250, v251\n"
      "v_add3_u32 v4, v4, v252, v253\n"
      "v_add3_u32 v4, v4, v254, v255\n"
      "v_mul_f32 v157, v157, v159\n"
      "v_cvt_pk_bf16_f32 v156, v156, v157\n"
      "v_mfma_f32_16x16x32_bf16 v[180:183], a[108:111], v[84:87], v[180:183]\n"
      "ds_write_b32 v59, v156 offset:4096\n"
      "ds_read_b64_tr_b16 v[122:123], v38 offset:63488\n"
      "ds_read_b64_tr_b16 v[12:13], v6 offset:61440\n"
      "v_fmac_f32 v172, v22, v232\n"
      "v_mfma_f32_16x16x32_bf16 v[188:191], a[112:115], v[84:87], v[188:191]\n"
      "global_load_dwordx4 a[68:71], v44, s[6:7] offset:1024 sc1\n"
      "ds_read_b64_tr_b16 v[124:125], v39 offset:61440\n"
      "v_add_u32_dpp v4, v4, v4 quad_perm:[1,0,3,2] row_mask:0xf bank_mask:0xf\n"
      "v_fmac_f32 v173, v22, v233\n"
      "v_fmac_f32 v174, v22, v234\n"
      "v_mfma_f32_16x16x32_bf16 v[196:199], a[116:119], v[84:87], v[196:199]\n"
      "ds_read_b64_tr_b16 v[126:127], v39 offset:63488\n"
      "v_fmac_f32 v175, v22, v235\n"
      "v_fma_f32 v172, v172, v23, v244\n"
      "v_mfma_f32_16x16x32_bf16 v[200:203], a[108:111], v[88:91], v[200:203]\n"
      "ds_read_b64_tr_b16 v[14:15], v6 offset:63488\n"
      "v_add_u32_dpp v4, v4, v4 quad_perm:[2,3,0,1] row_mask:0xf bank_mask:0xf\n"
      "v_fma_f32 v173, v173, v23, v245\n"
      "v_fma_f32 v174, v174, v23, v246\n"
      "v_mfma_f32_16x16x32_bf16 v[208:211], a[112:115], v[88:91], v[208:211]\n"
      "v_fma_f32 v175, v175, v23, v247\n"
      "v_mul_f32 v172, v172, v174\n"
      "v_mfma_f32_16x16x32_bf16 v[216:219], a[116:119], v[88:91], v[216:219]\n"
      "global_load_dwordx4 a[72:75], v44, s[6:7] offset:2048 sc1\n"
      "v_add_u32_dpp v4, v4, v4 row_half_mirror row_mask:0xf bank_mask:0xf\n"
      "v_cmp_ne_u32 vcc, s71, v4\n"
      "v_mul_f32 v173, v173, v175\n"
      "v_cvt_pk_bf16_f32 v172, v172, v173\n"
      "v_mfma_f32_16x16x32_bf16 v[204:207], a[108:111], v[92:95], v[204:207]\n"
      "ds_write_b32 v59, v172 offset:8192\n"
      "v_mfma_f32_16x16x32_bf16 v[212:215], a[112:115], v[92:95], v[212:215]\n"
      "s_cbranch_vccnz L_sx9_%=\n"
      "L_bx9_%=:\n"
      "v_mfma_f32_16x16x32_bf16 v[220:223], a[116:119], v[92:95], v[220:223]\n"
      "s_waitcnt lgkmcnt(0)\n"
      "s_barrier\n"
      "v_mfma_f32_16x16x32_bf16 v[24:27], v[60:63], v[8:11], v[24:27]\n"
      "global_load_dwordx4 a[76:79], v44, s[6:7] offset:3072 sc1\n"
      "ds_read_b128 v[160:163], v47 offset:0\n"
      "v_add_u32 v55, s63, v42\n"
      "v_mfma_f32_16x16x32_bf16 v[28:31], v[8:11], v[8:11], v[28:31]\n"
      "ds_read_b128 v[164:167], v47 offset:4096\n"
      "v_add_u32 v57, s40, v7\n"
      "v_mfma_f32_16x16x32_bf16 v[24:27], v[60:63], v[12:15], v[24:27]\n"
      "ds_read_b128 v[168:171], v47 offset:8192\n"
      "v_add_u32 v44, 28672, v7\n"
      "v_mfma_f32_16x16x32_bf16 v[28:31], v[12:15], v[12:15], v[28:31]\n"
      "ds_read_b64_tr_b16 v[64:65], v36 offset:0\n"
      "s_add_u32 s43, s73, s42\n"
      "v_mfma_f32_16x16x32_bf16 v[176:179], a[120:123], v[96:99], v[176:179]\n"
      "global_load_dwordx4 a[80:83], v44, s[6:7] offset:-4096 sc1\n"
      "v_add_u32 v56, s43, v7\n"
      "v_mfma_f32_16x16x32_bf16 v[184:187], a[124:127], v[96:99], v[184:187]\n"
      "ds_read_b64_tr_b16 v[66:67], v36 offset:2048\n"
      "s_waitcnt lgkmcnt(2)\n"
      "v_mfma_f32_16x16x32_bf16 v[192:195], a[128:131], v[96:99], v[192:195]\n"
      "ds_read_b64_tr_b16 v[68:69], v37 offset:0\n"
      "ds_read_b64_tr_b16 v[128:129], v6 offset:0\n"
      "v_and_b32 v2, 0x60, v42\n"
      "v_mfma_f32_16x16x32_bf16 v[180:183], a[120:123], v[100:103], v[180:183]\n"
      "buffer_store_dwordx4 v[160:163], v48, s[32:35], 0 offen offset:0 sc1\n"
      "ds_read_b64_tr_b16 v[70:71], v37 offset:2048\n"
      "ds_read_b64_tr_b16 v[130:131], v6 offset:2048\n"
      "v_add3_u32 v58, v160, v161, s30\n"
      "v_mfma_f32_16x16x32_bf16 v[188:191], a[124:127], v[100:103], v[188:191]\n"
      "global_load_dwordx4 a[84:87], v44, s[6:7] offset:-3072 sc1\n"
      "ds_read_b64_tr_b16 v[72:73], v36 offset:4096\n"
      "ds_read_b64_tr_b16 v[132:133], v6 offset:4096\n"
      "v_add3_u32 v58, v58, v162, v163\n"
      "v_add3_u32 v58, v58, v164, v165\n"
      "v_mfma_f32_16x16x32_bf16 v[196:199], a[128:131], v[100:103], v[196:199]\n"
      "buffer_store_dwordx4 v[164:167], v48, s[32:35], 0 offen offset:1024 sc1\n"
      "ds_read_b64_tr_b16 v[134:135], v6 offset:6144\n"
      "v_add3_u32 v58, v58, v166, v167\n"
      "v_mfma_f32_16x16x32_bf16 v[200:203], a[120:123], v[104:107], v[200:203]\n"
      "ds_read_b64_tr_b16 v[74:75], v36 offset:6144\n"
      "ds_read_b64_tr_b16 v[136:137], v6 offset:8192\n"
      "v_add3_u32 v58, v58, v168, v169\n"
      "v_add3_u32 v58, v58, v170, v171\n"
      "v_mfma_f32_16x16x32_bf16 v[208:211], a[124:127], v[104:107], v[208:211]\n"
      "buffer_store_dwordx4 v[168:171], v48, s[32:35], 0 offen offset:2048 sc1\n"
      "ds_read_b64_tr_b16 v[76:77], v37 offset:4096\n"
      "ds_read_b64_tr_b16 v[138:139], v6 offset:10240\n"
      "v_xor_b32 v2, s68, v2\n"
      "v_mfma_f32_16x16x32_bf16 v[216:219], a[128:131], v[104:107], v[216:219]\n"
      "buffer_store_dword v58, v49, s[32:35], 0 offen sc1\n"
      "global_load_dwordx4 a[88:91], v44, s[6:7] offset:-2048 sc1\n"
      "ds_read_b64_tr_b16 v[78:79], v37 offset:6144\n"
      "ds_read_b64_tr_b16 v[140:141], v6 offset:12288\n"
      "v_and_b32 v3, 0x3e0, v7\n"
      "v_mfma_f32_16x16x32_bf16 v[204:207], a[120:123], v[108:111], v[204:207]\n"
      "ds_read_b64_tr_b16 v[80:81], v36 offset:8192\n"
      "ds_read_b64_tr_b16 v[142:143], v6 offset:14336\n"
      "v_lshl_add_u32 v2, v3, 2, v2\n"
      "v_mfma_f32_16x16x32_bf16 v[212:215], a[124:127], v[108:111], v[212:215]\n"
      "ds_read_b64_tr_b16 v[82:83], v36 offset:10240\n"
      "v_and_b32 v3, 16, v7\n"
      "v_mfma_f32_16x16x32_bf16 v[220:223], a[128:131], v[108:111], v[220:223]\n"
      "ds_read_b64_tr_b16 v[144:145], v6 offset:16384\n"
      "v_add3_u32 v4, v2, v3, s67\n"
      "v_mfma_f32_16x16x32_bf16 v[176:179], a[132:135], v[112:115], v[176:179]\n"
      "global_load_dwordx4 a[92:95], v44, s[6:7] offset:-1024 sc1\n"
      "ds_read_b64_tr_b16 v[84:85], v37 offset:8192\n"
      "ds_read_b64_tr_b16 v[146:147], v6 offset:18432\n"
      "s_mul_i32 s43, s60, 3072\n"
      "v_mfma_f32_16x16x32_bf16 v[184:187], a[136:139], v[112:115], v[184:187]\n"
      "ds_read_b64_tr_b16 v[86:87], v37 offset:10240\n"
      "ds_read_b64_tr_b16 v[148:149], v6 offset:20480\n"
      "s_add_u32 s43, s43, 110592\n"
      "v_mfma_f32_16x16x32_bf16 v[192:195], a[140:143], v[112:115], v[192:195]\n"
      "ds_read_b64_tr_b16 v[88:89], v36 offset:12288\n"
      "ds_read_b64_tr_b16 v[150:151], v6 offset:22528\n"
      "v_min_u32 v5, 0x270, v7\n"
      "v_mfma_f32_16x16x32_bf16 v[180:183], a[132:135], v[116:119], v[180:183]\n"
      "ds_read_b64_tr_b16 v[90:91], v36 offset:14336\n"
      "ds_read_b64_tr_b16 v[152:153], v6 offset:24576\n"
      "v_lshl_add_u32 v53, v5, 2, s66\n"
      "v_mfma_f32_16x16x32_bf16 v[188:191], a[136:139], v[116:119], v[188:191]\n"
      "global_load_dwordx4 a[96:99], v44, s[6:7] offset:0 sc1\n"
      "ds_read_b64_tr_b16 v[92:93], v37 offset:12288\n"
      "ds_read_b64_tr_b16 v[154:155], v6 offset:26624\n"
      "v_lshrrev_b32 v5, 1, v5\n"
      "v_mfma_f32_16x16x32_bf16 v[196:199], a[140:143], v[116:119], v[196:199]\n"
      "ds_read_b64_tr_b16 v[156:157], v6 offset:28672\n"
      "v_add_u32 v5, 163328, v5\n"
      "v_mfma_f32_16x16x32_bf16 v[200:203], a[132:135], v[120:123], v[200:203]\n"
      "ds_read_b64_tr_b16 v[94:95], v37 offset:14336\n"
      "s_lshl_b32 s71, s71, 1\n"
      "v_mfma_f32_16x16x32_bf16 v[208:211], a[136:139], v[120:123], v[208:211]\n"
      "ds_read_b64_tr_b16 v[158:159], v6 offset:30720\n"
      "v_mfma_f32_16x16x32_bf16 v[216:219], a[140:143], v[120:123], v[216:219]\n"
      "global_load_dwordx4 a[100:103], v44, s[6:7] offset:1024 sc1\n"
      "v_mfma_f32_16x16x32_bf16 v[204:207], a[132:135], v[124:127], v[204:207]\n"
      "v_mfma_f32_16x16x32_bf16 v[212:215], a[136:139], v[124:127], v[212:215]\n"
      "v_mfma_f32_16x16x32_bf16 v[220:223], a[140:143], v[124:127], v[220:223]\n"
      "s_waitcnt lgkmcnt(0)\n"
      "v_mfma_f32_16x16x32_bf16 v[176:179], a[144:147], v[64:67], v[176:179]\n"
      "global_load_dwordx4 a[104:107], v44, s[6:7] offset:2048 sc1\n"
      "v_subrev_u32 v46, s42, v46\n"
      "v_mfma_f32_16x16x32_bf16 v[184:187], a[148:151], v[64:67], v[184:187]\n"
      "ds_read_b64_tr_b16 v[96:97], v36 offset:16384\n"
      "v_and_b32 v51, 0xfffffe7f, v51\n"
      "v_mfma_f32_16x16x32_bf16 v[192:195], a[152:155], v[64:67], v[192:195]\n"
      "ds_read_b64_tr_b16 v[98:99], v36 offset:18432\n"
      "v_mfma_f32_16x16x32_bf16 v[24:27], v[60:63], v[128:131], v[24:27]\n"
      "ds_read_b64_tr_b16 v[100:101], v37 offset:16384\n"
      "v_mfma_f32_16x16x32_bf16 v[28:31], v[128:131], v[128:131], v[28:31]\n"
      "global_load_dwordx4 a[108:111], v44, s[6:7] offset:3072 sc1\n"
      "v_mfma_f32_16x16x32_bf16 v[180:183], a[144:147], v[68:71], v[180:183]\n"
      "ds_read_b64_tr_b16 v[102:103], v37 offset:18432\n"
      "v_mfma_f32_16x16x32_bf16 v[188:191], a[148:151], v[68:71], v[188:191]\n"
      "ds_read_b64_tr_b16 v[104:105], v36 offset:20480\n"
      "v_add_u32 v44, 36864, v7\n"
      "v_mfma_f32_16x16x32_bf16 v[196:199], a[152:155], v[68:71], v[196:199]\n"
      "ds_read_b64_tr_b16 v[106:107], v36 offset:22528\n"
      "v_mfma_f32_16x16x32_bf16 v[24:27], v[60:63], v[132:135], v[24:27]\n"
      "global_load_dwordx4 a[112:115], v44, s[6:7] offset:-4096 sc1\n"
      "v_mfma_f32_16x16x32_bf16 v[28:31], v[132:135], v[132:135], v[28:31]\n"
      "ds_read_b64_tr_b16 v[108:109], v37 offset:20480\n"
      "v_mfma_f32_16x16x32_bf16 v[176:179], a[156:159], v[72:75], v[176:179]\n"
      "ds_read_b64_tr_b16 v[110:111], v37 offset:22528\n"
      "v_mfma_f32_16x16x32_bf16 v[184:187], a[160:163], v[72:75], v[184:187]\n"
      "ds_read_b64_tr_b16 v[112:113], v36 offset:24576\n"
      "v_mfma_f32_16x16x32_bf16 v[192:195], a[164:167], v[72:75], v[192:195]\n"
      "global_load_dwordx4 a[116:119], v44, s[6:7] offset:-3072 sc1\n"
      "v_mfma_f32_16x16x32_bf16 v[24:27], v[60:63], v[136:139], v[24:27]\n"
      "ds_read_b64_tr_b16 v[114:115], v36 offset:26624\n"
      "v_mfma_f32_16x16x32_bf16 v[28:31], v[136:139], v[136:139], v[28:31]\n"
      "ds_read_b64_tr_b16 v[116:117], v37 offset:24576\n"
      "v_mfma_f32_16x16x32_bf16 v[180:183], a[156:159], v[76:79], v[180:183]\n"
      "ds_read_b64_tr_b16 v[118:119], v37 offset:26624\n"
      "v_mfma_f32_16x16x32_bf16 v[188:191], a[160:163], v[76:79], v[188:191]\n"
      "global_load_dwordx4 a[120:123], v44, s[6:7] offset:-2048 sc1\n"
      "v_mfma_f32_16x16x32_bf16 v[196:199], a[164:167], v[76:79], v[196:199]\n"
      "ds_read_b64_tr_b16 v[120:121], v36 offset:28672\n"
      "v_mfma_f32_16x16x32_bf16 v[24:27], v[60:63], v[140:143], v[24:27]\n"
      "ds_read_b64_tr_b16 v[122:123], v36 offset:30720\n"
      "v_mfma_f32_16x16x32_bf16 v[28:31], v[140:143], v[140:143], v[28:31]\n"
      "ds_read_b64_tr_b16 v[124:125], v37 offset:28672\n"
      "v_mfma_f32_16x16x32_bf16 v[176:179], a[168:171], v[80:83], v[176:179]\n"
      "global_load_dwordx4 a[124:127], v44, s[6:7] offset:-1024 sc1\n"
      "v_mfma_f32_16x16x32_bf16 v[184:187], a[172:175], v[80:83], v[184:187]\n"
      "ds_read_b64_tr_b16 v[126:127], v37 offset:30720\n"
      "v_mfma_f32_16x16x32_bf16 v[192:195], a[176:179], v[80:83], v[192:195]\n"
      "v_mfma_f32_16x16x32_bf16 v[24:27], v[60:63], v[144:147], v[24:27]\n"
      "v_mfma_f32_16x16x32_bf16 v[28:31], v[144:147], v[144:147], v[28:31]\n"
      "global_load_dwordx4 a[128:131], v44, s[6:7] offset:0 sc1\n"
      "v_mfma_f32_16x16x32_bf16 v[180:183], a[168:171], v[84:87], v[180:183]\n"
      "v_mfma_f32_16x16x32_bf16 v[188:191], a[172:175], v[84:87], v[188:191]\n"
      "v_mfma_f32_16x16x32_bf16 v[196:199], a[176:179], v[84:87], v[196:199]\n"
      "v_mfma_f32_16x16x32_bf16 v[24:27], v[60:63], v[148:151], v[24:27]\n"
      "global_load_dwordx4 a[132:135], v44, s[6:7] offset:1024 sc1\n"
      "v_mfma_f32_16x16x32_bf16 v[28:31], v[148:151], v[148:151], v[28:31]\n"
      "v_mfma_f32_16x16x32_bf16 v[176:179], a[180:183], v[88:91], v[176:179]\n"
      "v_mfma_f32_16x16x32_bf16 v[184:187], a[184:187], v[88:91], v[184:187]\n"
      "v_mfma_f32_16x16x32_bf16 v[192:195], a[188:191], v[88:91], v[192:195]\n"
      "global_load_dwordx4 a[136:139], v44, s[6:7] offset:2048 sc1\n"
      "v_mfma_f32_16x16x32_bf16 v[24:27], v[60:63], v[152:155], v[24:27]\n"
      "v_mfma_f32_16x16x32_bf16 v[28:31], v[152:155], v[152:155], v[28:31]\n"
      "v_add_u32 v45, 4096, v7\n"
      "v_mfma_f32_16x16x32_bf16 v[180:183], a[180:183], v[92:95], v[180:183]\n"
      "v_mfma_f32_16x16x32_bf16 v[188:191], a[184:187], v[92:95], v[188:191]\n"
      "global_load_dwordx4 a[140:143], v44, s[6:7] offset:3072 sc1\n"
      "v_mfma_f32_16x16x32_bf16 v[196:199], a[188:191], v[92:95], v[196:199]\n"
      "v_mfma_f32_16x16x32_bf16 v[24:27], v[60:63], v[156:159], v[24:27]\n"
      "v_mfma_f32_16x16x32_bf16 v[28:31], v[156:159], v[156:159], v[28:31]\n"
      "s_waitcnt lgkmcnt(0)\n"
      "v_mfma_f32_16x16x32_bf16 v[176:179], a[192:195], v[96:99], v[176:179]\n"
      "global_load_dwordx4 v[16:19], v45, s[10:11] offset:-4096 sc1\n"
      "v_mfma_f32_16x16x32_bf16 v[184:187], a[196:199], v[96:99], v[184:187]\n"
      "ds_read_b64_tr_b16 v[64:65], v38 offset:0\n"
      "v_mfma_f32_16x16x32_bf16 v[192:195], a[200:203], v[96:99], v[192:195]\n"
      "ds_read_b64_tr_b16 v[66:67], v38 offset:2048\n"
      "v_mfma_f32_16x16x32_bf16 v[180:183], a[192:195], v[100:103], v[180:183]\n"
      "global_load_dwordx4 v[20:23], v45, s[12:13] offset:-4096 sc1\n"
      "v_mfma_f32_16x16x32_bf16 v[188:191], a[196:199], v[100:103], v[188:191]\n"
      "ds_read_b64_tr_b16 v[68:69], v39 offset:0\n"
      "v_cndmask_b32 v58, v28, v29, s[48:49]\n"
      "v_mfma_f32_16x16x32_bf16 v[196:199], a[200:203], v[100:103], v[196:199]\n"
      "ds_read_b64_tr_b16 v[70:71], v39 offset:2048\n"
      "v_cndmask_b32 v59, v30, v31, s[48:49]\n"
      "v_mfma_f32_16x16x32_bf16 v[176:179], a[204:207], v[104:107], v[176:179]\n"
      "global_load_dwordx4 v[8:11], v45, s[10:11] offset:-3072 sc1\n"
      "v_cndmask_b32 v58, v58, v59, s[50:51]\n"
      "v_mfma_f32_16x16x32_bf16 v[184:187], a[208:211], v[104:107], v[184:187]\n"
      "ds_read_b64_tr_b16 v[72:73], v38 offset:4096\n"
      "v_mul_f32 v24, 0x3acccccd, v24\n"
      "v_mfma_f32_16x16x32_bf16 v[192:195], a[212:215], v[104:107], v[192:195]\n"
      "ds_read_b64_tr_b16 v[74:75], v38 offset:6144\n"
      "v_mul_f32 v58, 0x3acccccd, v58\n"
      "v_mfma_f32_16x16x32_bf16 v[180:183], a[204:207], v[108:111], v[180:183]\n"
      "global_load_dwordx4 v[12:15], v45, s[12:13] offset:-3072 sc1\n"
      "ds_read_b64_tr_b16 v[76:77], v39 offset:4096\n"
      "v_fma_f32 v58, -v24, v24, v58\n"
      "v_mfma_f32_16x16x32_bf16 v[188:191], a[208:211], v[108:111], v[188:191]\n"
      "ds_read_b64_tr_b16 v[78:79], v39 offset:6144\n"
      "v_max_f32 v58, 0, v58\n"
      "v_mfma_f32_16x16x32_bf16 v[196:199], a[212:215], v[108:111], v[196:199]\n"
      "ds_read_b64_tr_b16 v[80:81], v38 offset:8192\n"
      "v_cvt_pk_bf16_f32 v58, 0, v58\n"
      "v_mfma_f32_16x16x32_bf16 v[176:179], a[216:219], v[112:115], v[176:179]\n"
      "global_load_dwordx4 v[160:163], v45, s[10:11] offset:-2048 sc1\n"
      "ds_read_b64_tr_b16 v[82:83], v38 offset:10240\n"
      "v_add_f32 v58, 0x3727c5ac, v58\n"
      "v_mfma_f32_16x16x32_bf16 v[184:187], a[220:223], v[112:115], v[184:187]\n"
      "ds_read_b64_tr_b16 v[84:85], v39 offset:8192\n"
      "v_cvt_pk_bf16_f32 v58, 0, v58\n"
      "v_mfma_f32_16x16x32_bf16 v[192:195], a[224:227], v[112:115], v[192:195]\n"
      "ds_read_b64_tr_b16 v[86:87], v39 offset:10240\n"
      "v_mfma_f32_16x16x32_bf16 v[180:183], a[216:219], v[116:119], v[180:183]\n"
      "global_load_dwordx4 v[164:167], v45, s[12:13] offset:-2048 sc1\n"
      "ds_read_b64_tr_b16 v[88:89], v38 offset:12288\n"
      "v_mfma_f32_16x16x32_bf16 v[188:191], a[220:223], v[116:119], v[188:191]\n"
      "global_load_dwordx2 v[32:33], v55, s[26:27] offset:0 sc1\n"
      "ds_read_b64_tr_b16 v[90:91], v38 offset:14336\n"
      "v_sqrt_f32 v58, v58\n"
      "v_sub_f32 v24, 0, v24\n"
      "v_mfma_f32_16x16x32_bf16 v[196:199], a[224:227], v[116:119], v[196:199]\n"
      "ds_read_b64_tr_b16 v[92:93], v39 offset:12288\n"
      "v_cvt_pk_bf16_f32 v58, 0, v58\n"
      "v_mfma_f32_16x16x32_bf16 v[176:179], a[228:231], v[120:123], v[176:179]\n"
      "ds_read_b64_tr_b16 v[94:95], v39 offset:14336\n"
      "v_rcp_f32 v25, v58\n"
      "v_cvt_pk_bf16_f32 v24, 0, v24\n"
      "v_mfma_f32_16x16x32_bf16 v[184:187], a[232:235], v[120:123], v[184:187]\n"
      "global_load_dwordx4 v[168:171], v45, s[10:11] offset:-1024 sc1\n"
      "v_mfma_f32_16x16x32_bf16 v[192:195], a[236:239], v[120:123], v[192:195]\n"
      "ds_write_b64 v50, v[24:25] offset:512\n"
      "v_mfma_f32_16x16x32_bf16 v[180:183], a[228:231], v[124:127], v[180:183]\n"
      "v_mfma_f32_16x16x32_bf16 v[188:191], a[232:235], v[124:127], v[188:191]\n"
      "v_mfma_f32_16x16x32_bf16 v[196:199], a[236:239], v[124:127], v[196:199]\n"
      "global_load_dwordx4 v[172:175], v45, s[12:13] offset:-1024 sc1\n"
      "s_waitcnt lgkmcnt(0)\n"
      "s_barrier\n"
      "v_mfma_f32_16x16x32_bf16 v[200:203], a[144:147], v[64:67], v[200:203]\n"
      "ds_read_b64 v[24:25], v51 offset:512\n"
      "v_mfma_f32_16x16x32_bf16 v[208:211], a[148:151], v[64:67], v[208:211]\n"
      "ds_read_b64 v[26:27], v51 offset:768\n"
      "v_mfma_f32_16x16x32_bf16 v[216:219], a[152:155], v[64:67], v[216:219]\n"
      "ds_read_b64 v[28:29], v51 offset:640\n"
      "v_mfma_f32_16x16x32_bf16 v[204:207], a[144:147], v[68:71], v[204:207]\n"
      "global_load_dwordx4 v[128:131], v45, s[10:11] offset:0 sc1\n"
      "ds_read_b64 v[30:31], v51 offset:896\n"
      "v_mfma_f32_16x16x32_bf16 v[212:215], a[148:151], v[68:71], v[212:215]\n"
      "ds_read_b64_tr_b16 v[96:97], v38 offset:16384\n"
      "ds_read_b64_tr_b16 v[98:99], v38 offset:18432\n"
      "v_mfma_f32_16x16x32_bf16 v[220:223], a[152:155], v[68:71], v[220:223]\n"
      "ds_read_b64_tr_b16 v[100:101], v39 offset:16384\n"
      "v_mfma_f32_16x16x32_bf16 v[200:203], a[156:159], v[72:75], v[200:203]\n"
      "ds_read_b64_tr_b16 v[102:103], v39 offset:18432\n"
      "v_mfma_f32_16x16x32_bf16 v[208:211], a[160:163], v[72:75], v[208:211]\n"
      "global_load_dwordx4 v[132:135], v45, s[12:13] offset:0 sc1\n"
      "ds_read_b64_tr_b16 v[104:105], v38 offset:20480\n"
      "s_waitcnt lgkmcnt(5)\n"
      "v_mfma_f32_16x16x32_bf16 v[216:219], a[164:167], v[72:75], v[216:219]\n"
      "ds_read_b64_tr_b16 v[106:107], v38 offset:22528\n"
      "v_fmac_f32 v176, v24, v224\n"
      "v_mfma_f32_16x16x32_bf16 v[204:207], a[156:159], v[76:79], v[204:207]\n"
      "ds_read_b64_tr_b16 v[108:109], v39 offset:20480\n"
      "v_fmac_f32 v177, v24, v225\n"
      "v_fmac_f32 v178, v24, v226\n"
      "v_mfma_f32_16x16x32_bf16 v[212:215], a[160:163], v[76:79], v[212:215]\n"
      "ds_read_b64_tr_b16 v[110:111], v39 offset:22528\n"
      "v_fmac_f32 v179, v24, v227\n"
      "v_fma_f32 v176, v176, v25, v236\n"
      "v_mfma_f32_16x16x32_bf16 v[220:223], a[164:167], v[76:79], v[220:223]\n"
      "global_load_dwordx4 v[136:139], v45, s[10:11] offset:1024 sc1\n"
      "ds_read_b64_tr_b16 v[112:113], v38 offset:24576\n"
      "v_fma_f32 v177, v177, v25, v237\n"
      "v_fma_f32 v178, v178, v25, v238\n"
      "v_mfma_f32_16x16x32_bf16 v[200:203], a[168:171], v[80:83], v[200:203]\n"
      "ds_read_b64_tr_b16 v[114:115], v38 offset:26624\n"
      "v_fma_f32 v179, v179, v25, v239\n"
      "v_mul_f32 v176, v176, v178\n"
      "v_mfma_f32_16x16x32_bf16 v[208:211], a[172:175], v[80:83], v[208:211]\n"
      "ds_read_b64_tr_b16 v[116:117], v39 offset:24576\n"
      "v_mul_f32 v177, v177, v179\n"
      "v_cvt_pk_bf16_f32 v176, v176, v177\n"
      "v_mfma_f32_16x16x32_bf16 v[216:219], a[176:179], v[80:83], v[216:219]\n"
      "ds_write_b32 v46, v176 offset:0\n"
      "ds_read_b64_tr_b16 v[118:119], v39 offset:26624\n"
      "v_fmac_f32 v184, v24, v228\n"
      "v_mfma_f32_16x16x32_bf16 v[204:207], a[168:171], v[84:87], v[204:207]\n"
      "global_load_dwordx4 v[140:143], v45, s[12:13] offset:1024 sc1\n"
      "ds_read_b64_tr_b16 v[120:121], v38 offset:28672\n"
      "v_fmac_f32 v185, v24, v229\n"
      "v_fmac_f32 v186, v24, v230\n"
      "v_mfma_f32_16x16x32_bf16 v[212:215], a[172:175], v[84:87], v[212:215]\n"
      "ds_read_b64_tr_b16 v[122:123], v38 offset:30720\n"
      "v_fmac_f32 v187, v24, v231\n"
      "v_fma_f32 v184, v184, v25, v240\n"
      "v_mfma_f32_16x16x32_bf16 v[220:223], a[176:179], v[84:87], v[220:223]\n"
      "global_load_dwordx2 v[34:35], v55, s[26:27] offset:512 sc1\n"
      "ds_read_b64_tr_b16 v[124:125], v39 offset:28672\n"
      "v_add_u32 v45, 10240, v7\n"
      "v_fma_f32 v185, v185, v25, v241\n"
      "v_fma_f32 v186, v186, v25, v242\n"
      "v_mfma_f32_16x16x32_bf16 v[200:203], a[180:183], v[88:91], v[200:203]\n"
      "ds_read_b64_tr_b16 v[126:127], v39 offset:30720\n"
      "v_fma_f32 v187, v187, v25, v243\n"
      "v_mul_f32 v184, v184, v186\n"
      "v_mfma_f32_16x16x32_bf16 v[208:211], a[184:187], v[88:91], v[208:211]\n"
      "global_load_dwordx4 v[144:147], v45, s[10:11] offset:-4096 sc1\n"
      "v_mul_f32 v185, v185, v187\n"
      "v_cvt_pk_bf16_f32 v184, v184, v185\n"
      "v_mfma_f32_16x16x32_bf16 v[216:219], a[188:191], v[88:91], v[216:219]\n"
      "ds_write_b32 v46, v184 offset:4096\n"
      "ds_read_b128 a[144:147], v57 offset:0\n"
      "v_fmac_f32 v192, v24, v232\n"
      "v_mfma_f32_16x16x32_bf16 v[204:207], a[180:183], v[92:95], v[204:207]\n"
      "ds_read_b128 a[148:151], v57 offset:1024\n"
      "v_fmac_f32 v193, v24, v233\n"
      "v_fmac_f32 v194, v24, v234\n"
      "v_mfma_f32_16x16x32_bf16 v[212:215], a[184:187], v[92:95], v[212:215]\n"
      "global_load_dwordx4 v[148:151], v45, s[12:13] offset:-4096 sc1\n"
      "v_fmac_f32 v195, v24, v235\n"
      "v_fma_f32 v192, v192, v25, v244\n"
      "v_mfma_f32_16x16x32_bf16 v[220:223], a[188:191], v[92:95], v[220:223]\n"
      "ds_read_b128 a[152:155], v57 offset:2048\n"
      "v_fma_f32 v193, v193, v25, v245\n"
      "v_fma_f32 v194, v194, v25, v246\n"
      "s_waitcnt lgkmcnt(4)\n"
      "v_mfma_f32_16x16x32_bf16 v[200:203], a[192:195], v[96:99], v[200:203]\n"
      "ds_read_b128 a[156:159], v57 offset:3072\n"
      "v_fma_f32 v195, v195, v25, v247\n"
      "v_mul_f32 v192, v192, v194\n"
      "v_mfma_f32_16x16x32_bf16 v[208:211], a[196:199], v[96:99], v[208:211]\n"
      "global_load_dwordx4 v[152:155], v45, s[10:11] offset:-3072 sc1\n"
      "v_mul_f32 v193, v193, v195\n"
      "v_cvt_pk_bf16_f32 v192, v192, v193\n"
      "v_mfma_f32_16x16x32_bf16 v[216:219], a[200:203], v[96:99], v[216:219]\n"
      "ds_write_b32 v46, v192 offset:8192\n"
      "ds_read_b128 a[160:163], v57 offset:4096\n"
      "v_fmac_f32 v180, v26, v224\n"
      "v_mfma_f32_16x16x32_bf16 v[204:207], a[192:195], v[100:103], v[204:207]\n"
      "ds_read_b128 a[164:167], v57 offset:5120\n"
      "v_fmac_f32 v181, v26, v225\n"
      "v_mfma_f32_16x16x32_bf16 v[212:215], a[196:199], v[100:103], v[212:215]\n"
      "global_load_dwordx4 v[156:159], v45, s[12:13] offset:-3072 sc1\n"
      "v_fmac_f32 v182, v26, v226\n"
      "v_fmac_f32 v183, v26, v227\n"
      "v_mfma_f32_16x16x32_bf16 v[220:223], a[200:203], v[100:103], v[220:223]\n"
      "ds_read_b128 a[168:171], v57 offset:6144\n"
      "v_fma_f32 v180, v180, v27, v236\n"
      "v_fma_f32 v181, v181, v27, v237\n"
      "v_mfma_f32_16x16x32_bf16 v[200:203], a[204:207], v[104:107], v[200:203]\n"
      "ds_read_b128 a[172:175], v57 offset:7168\n"
      "v_fma_f32 v182, v182, v27, v238\n"
      "v_fma_f32 v183, v183, v27, v239\n"
      "v_mfma_f32_16x16x32_bf16 v[208:211], a[208:211], v[104:107], v[208:211]\n"
      "global_load_dwordx4 v[64:67], v45, s[10:11] offset:-2048 sc1\n"
      "v_mul_f32 v180, v180, v182\n"
      "v_mul_f32 v181, v181, v183\n"
      "v_cvt_pk_bf16_f32 v180, v180, v181\n"
      "v_mfma_f32_16x16x32_bf16 v[216:219], a[212:215], v[104:107], v[216:219]\n"
      "ds_write_b32 v46, v180 offset:2048\n"
      "ds_read_b128 a[176:179], v57 offset:8192\n"
      "v_fmac_f32 v188, v26, v228\n"
      "v_mfma_f32_16x16x32_bf16 v[204:207], a[204:207], v[108:111], v[204:207]\n"
      "v_add_u32 v44, 61440, v7\n"
      "v_fmac_f32 v189, v26, v229\n"
      "v_mfma_f32_16x16x32_bf16 v[212:215], a[208:211], v[108:111], v[212:215]\n"
      "global_load_dwordx4 v[68:71], v45, s[12:13] offset:-2048 sc1\n"
      "v_fmac_f32 v190, v26, v230\n"
      "v_fmac_f32 v191, v26, v231\n"
      "v_mfma_f32_16x16x32_bf16 v[220:223], a[212:215], v[108:111], v[220:223]\n"
      "global_load_dwordx2 v[60:61], v55, s[26:27] offset:1024 sc1\n"
      "s_waitcnt vmcnt(14)\n"
      "v_fma_f32 v188, v188, v27, v240\n"
      "v_fma_f32 v189, v189, v27, v241\n"
      "v_mfma_f32_16x16x32_bf16 v[200:203], a[216:219], v[112:115], v[200:203]\n"
      "v_add3_u32 v2, v16, v17, s30\n"
      "v_add3_u32 v2, v2, v18, v19\n"
      "v_fma_f32 v190, v190, v27, v242\n"
      "v_fma_f32 v191, v191, v27, v243\n"
      "v_mfma_f32_16x16x32_bf16 v[208:211], a[220:223], v[112:115], v[208:211]\n"
      "global_load_dwordx4 v[72:75], v45, s[10:11] offset:-1024 sc1\n"
      "v_add3_u32 v2, v2, v8, v9\n"
      "v_add3_u32 v2, v2, v10, v11\n"
      "v_mul_f32 v188, v188, v190\n"
      "v_mul_f32 v189, v189, v191\n"
      "v_cvt_pk_bf16_f32 v188, v188, v189\n"
      "v_mfma_f32_16x16x32_bf16 v[216:219], a[224:227], v[112:115], v[216:219]\n"
      "ds_write_b32 v46, v188 offset:6144\n"
      "v_add3_u32 v2, v2, v160, v161\n"
      "v_add3_u32 v2, v2, v162, v163\n"
      "v_mfma_f32_16x16x32_bf16 v[204:207], a[216:219], v[116:119], v[204:207]\n"
      "v_xor_b32 v2, v2, v32\n"
      "v_fmac_f32 v196, v26, v232\n"
      "v_fmac_f32 v197, v26, v233\n"
      "v_mfma_f32_16x16x32_bf16 v[212:215], a[220:223], v[116:119], v[212:215]\n"
      "global_load_dwordx4 v[76:79], v45, s[12:13] offset:-1024 sc1\n"
      "v_add3_u32 v3, v20, v21, s30\n"
      "v_add3_u32 v3, v3, v22, v23\n"
      "v_fmac_f32 v198, v26, v234\n"
      "v_fmac_f32 v199, v26, v235\n"
      "v_mfma_f32_16x16x32_bf16 v[220:223], a[224:227], v[116:119], v[220:223]\n"
      "v_add3_u32 v3, v3, v12, v13\n"
      "v_fma_f32 v196, v196, v27, v244\n"
      "v_fma_f32 v197, v197, v27, v245\n"
      "v_mfma_f32_16x16x32_bf16 v[200:203], a[228:231], v[120:123], v[200:203]\n"
      "v_add3_u32 v3, v3, v14, v15\n"
      "v_add3_u32 v3, v3, v164, v165\n"
      "v_fma_f32 v198, v198, v27, v246\n"
      "v_fma_f32 v199, v199, v27, v247\n"
      "v_mfma_f32_16x16x32_bf16 v[208:211], a[232:235], v[120:123], v[208:211]\n"
      "global_load_dwordx4 v[80:83], v45, s[10:11] offset:0 sc1\n"
      "v_add3_u32 v3, v3, v166, v167\n"
      "v_xor_b32 v3, v3, v33\n"
      "v_mul_f32 v196, v196, v198\n"
      "v_mul_f32 v197, v197, v199\n"
      "v_mfma_f32_16x16x32_bf16 v[216:219], a[236:239], v[120:123], v[216:219]\n"
      "global_load_dwordx4 a[212:215], v44, s[6:7] offset:-3072 sc1\n"
      "v_or_b32 v2, v2, v3\n"
      "v_cvt_pk_bf16_f32 v196, v196, v197\n"
      "v_mfma_f32_16x16x32_bf16 v[204:207], a[228:231], v[124:127], v[204:207]\n"
      "ds_write_b32 v46, v196 offset:10240\n"
      "v_cmp_ne_u32 vcc, 0, v2\n"
      "v_add_u32 v57, s41, v7\n"
      "v_mfma_f32_16x16x32_bf16 v[212:215], a[232:235], v[124:127], v[212:215]\n"
      "global_load_dwordx4 v[84:87], v45, s[12:13] offset:0 sc1\n"
      "v_mfma_f32_16x16x32_bf16 v[220:223], a[236:239], v[124:127], v[220:223]\n"
      "ds_read_b128 a[180:183], v57 offset:0\n"
      "s_cbranch_vccnz L_shA0_%=\n"
      "L_bhA0_%=:\n"
      "s_waitcnt lgkmcnt(1)\n"
      "s_barrier\n"
      "v_mfma_f32_16x16x32_bf16 v[96:99], a[240:243], v[16:19], 0\n"
      "global_load_dwordx4 a[216:219], v44, s[6:7] offset:-2048 sc1\n"
      "global_load_dwordx4 a[220:223], v44, s[6:7] offset:-1024 sc1\n"
      "ds_read_b128 v[176:179], v47 offset:0\n"
      "v_mfma_f32_16x16x32_bf16 v[104:107], a[244:247], v[16:19], 0\n"
      "global_load_dwordx4 v[88:91], v45, s[10:11] offset:1024 sc1\n"
      "ds_read_b128 v[180:183], v47 offset:4096\n"
      "v_mfma_f32_16x16x32_bf16 v[112:115], a[248:251], v[16:19], 0\n"
      "ds_read_b128 v[184:187], v47 offset:8192\n"
      "v_add_u32 v43, s69, v48\n"
      "v_mfma_f32_16x16x32_bf16 v[120:123], a[252:255], v[16:19], 0\n"
      "ds_read_b128 a[184:187], v57 offset:1024\n"
      "v_add_u32 v59, s70, v49\n"
      "v_fmac_f32 v200, v28, v224\n"
      "v_fmac_f32 v201, v28, v225\n"
      "v_fmac_f32 v202, v28, v226\n"
      "v_fmac_f32 v203, v28, v227\n"
      "v_mfma_f32_16x16x32_bf16 v[248:251], a[0:3], v[16:19], 0\n"
      "global_load_dwordx4 v[92:95], v45, s[12:13] offset:1024 sc1\n"
      "v_fma_f32 v200, v200, v29, v236\n"
      "v_fma_f32 v201, v201, v29, v237\n"
      "v_fma_f32 v202, v202, v29, v238\n"
      "v_mfma_f32_16x16x32_bf16 v[100:103], a[240:243], v[20:23], 0\n"
      "global_load_dwordx2 v[62:63], v55, s[26:27] offset:1536 sc1\n"
      "v_fma_f32 v203, v203, v29, v239\n"
      "v_mul_f32 v200, v200, v202\n"
      "v_mul_f32 v201, v201, v203\n"
      "v_cvt_pk_bf16_f32 v200, v200, v201\n"
      "v_mfma_f32_16x16x32_bf16 v[108:111], a[244:247], v[20:23], 0\n"
      "ds_write_b32 v46, v200 offset:1024\n"
      "s_waitcnt lgkmcnt(2)\n"
      "v_fmac_f32 v208, v28, v228\n"
      "v_fmac_f32 v209, v28, v229\n"
      "v_mfma_f32_16x16x32_bf16 v[116:119], a[248:251], v[20:23], 0\n"
      "buffer_store_dwordx4 v[176:179], v43, s[32:35], 0 offen offset:0 sc1\n"
      "v_add3_u32 v58, v176, v177, s30\n"
      "v_fmac_f32 v210, v28, v230\n"
      "v_fmac_f32 v211, v28, v231\n"
      "v_fma_f32 v208, v208, v29, v240\n"
      "v_mfma_f32_16x16x32_bf16 v[124:127], a[252:255], v[20:23], 0\n"
      "ds_read_b128 a[188:191], v57 offset:2048\n"
      "v_add3_u32 v58, v58, v178, v179\n"
      "v_add3_u32 v58, v58, v180, v181\n"
      "v_fma_f32 v209, v209, v29, v241\n"
      "v_fma_f32 v210, v210, v29, v242\n"
      "v_fma_f32 v211, v211, v29, v243\n"
      "v_mul_f32 v208, v208, v210\n"
      "v_mul_f32 v209, v209, v211\n"
      "v_cvt_pk_bf16_f32 v208, v208, v209\n"
      "v_mfma_f32_16x16x32_bf16 v[252:255], a[0:3], v[20:23], 0\n"
      "buffer_store_dwordx4 v[180:183], v43, s[32:35], 0 offen offset:1024 sc1\n"
      "ds_write_b32 v46, v208 offset:5120\n"
      "ds_read_b128 a[192:195], v57 offset:3072\n"
      "v_add3_u32 v58, v58, v182, v183\n"
      "v_mfma_f32_16x16x32_bf16 v[96:99], a[4:7], v[8:11], v[96:99]\n"
      "ds_read_b128 a[196:199], v57 offset:4096\n"
      "v_add3_u32 v58, v58, v184, v185\n"
      "v_add3_u32 v58, v58, v186, v187\n"
      "v_fmac_f32 v216, v28, v232\n"
      "v_fmac_f32 v217, v28, v233\n"
      "v_fmac_f32 v218, v28, v234\n"
      "v_fmac_f32 v219, v28, v235\n"
      "v_mfma_f32_16x16x32_bf16 v[104:107], a[8:11], v[8:11], v[104:107]\n"
      "buffer_store_dwordx4 v[184:187], v43, s[32:35], 0 offen offset:2048 sc1\n"
      "v_fma_f32 v216, v216, v29, v244\n"
      "v_fma_f32 v217, v217, v29, v245\n"
      "v_fma_f32 v218, v218, v29, v246\n"
      "v_mfma_f32_16x16x32_bf16 v[112:115], a[12:15], v[8:11], v[112:115]\n"
      "buffer_store_dword v58, v59, s[32:35], 0 offen sc1\n"
      "v_fma_f32 v219, v219, v29, v247\n"
      "v_mul_f32 v216, v216, v218\n"
      "v_mul_f32 v217, v217, v219\n"
      "v_cvt_pk_bf16_f32 v216, v216, v217\n"
      "v_mfma_f32_16x16x32_bf16 v[120:123], a[16:19], v[8:11], v[120:123]\n"
      "ds_write_b32 v46, v216 offset:9216\n"
      "v_fmac_f32 v204, v30, v224\n"
      "v_fmac_f32 v205, v30, v225\n"
      "v_mfma_f32_16x16x32_bf16 v[248:251], a[20:23], v[8:11], v[248:251]\n"
      "ds_read_b128 a[200:203], v57 offset:5120\n"
      "v_fmac_f32 v206, v30, v226\n"
      "v_fmac_f32 v207, v30, v227\n"
      "v_fma_f32 v204, v204, v31, v236\n"
      "v_mfma_f32_16x16x32_bf16 v[100:103], a[4:7], v[12:15], v[100:103]\n"
      "ds_read_b128 a[204:207], v57 offset:6144\n"
      "v_fma_f32 v205, v205, v31, v237\n"
      "v_fma_f32 v206, v206, v31, v238\n"
      "v_fma_f32 v207, v207, v31, v239\n"
      "v_mul_f32 v204, v204, v206\n"
      "v_mul_f32 v205, v205, v207\n"
      "v_cvt_pk_bf16_f32 v204, v204, v205\n"
      "v_mfma_f32_16x16x32_bf16 v[108:111], a[8:11], v[12:15], v[108:111]\n"
      "ds_write_b32 v46, v204 offset:3072\n"
      "ds_read_b128 a[208:211], v57 offset:7168\n"
      "s_waitcnt vmcnt(21)\n"
      "v_mfma_f32_16x16x32_bf16 v[116:119], a[12:15], v[12:15], v[116:119]\n"
      "v_fmac_f32 v212, v30, v228\n"
      "v_fmac_f32 v213, v30, v229\n"
      "v_fmac_f32 v214, v30, v230\n"
      "v_mfma_f32_16x16x32_bf16 v[124:127], a[16:19], v[12:15], v[124:127]\n"
      "v_add3_u32 v2, v168, v169, s30\n"
      "v_add3_u32 v2, v2, v170, v171\n"
      "v_fmac_f32 v215, v30, v231\n"
      "v_fma_f32 v212, v212, v31, v240\n"
      "v_fma_f32 v213, v213, v31, v241\n"
      "v_fma_f32 v214, v214, v31, v242\n"
      "v_mfma_f32_16x16x32_bf16 v[252:255], a[20:23], v[12:15], v[252:255]\n"
      "v_add3_u32 v2, v2, v128, v129\n"
      "v_add3_u32 v2, v2, v130, v131\n"
      "v_fma_f32 v215, v215, v31, v243\n"
      "v_mul_f32 v212, v212, v214\n"
      "v_mul_f32 v213, v213, v215\n"
      "v_cvt_pk_bf16_f32 v212, v212, v213\n"
      "v_mfma_f32_16x16x32_bf16 v[96:99], a[24:27], v[160:163], v[96:99]\n"
      "ds_write_b32 v46, v212 offset:7168\n"
      "v_add3_u32 v2, v2, v136, v137\n"
      "v_fmac_f32 v220, v30, v232\n"
      "v_mfma_f32_16x16x32_bf16 v[104:107], a[28:31], v[160:163], v[104:107]\n"
      "v_add3_u32 v2, v2, v138, v139\n"
      "v_xor_b32 v2, v2, v34\n"
      "v_fmac_f32 v221, v30, v233\n"
      "v_fmac_f32 v222, v30, v234\n"
      "v_fmac_f32 v223, v30, v235\n"
      "v_fma_f32 v220, v220, v31, v244\n"
      "v_mfma_f32_16x16x32_bf16 v[112:115], a[32:35], v[160:163], v[112:115]\n"
      "v_add3_u32 v3, v172, v173, s30\n"
      "v_add3_u32 v3, v3, v174, v175\n"
      "v_fma_f32 v221, v221, v31, v245\n"
      "v_fma_f32 v222, v222, v31, v246\n"
      "v_fma_f32 v223, v223, v31, v247\n"
      "v_mfma_f32_16x16x32_bf16 v[120:123], a[36:39], v[160:163], v[120:123]\n"
      "v_add3_u32 v3, v3, v132, v133\n"
      "v_mul_f32 v220, v220, v222\n"
      "v_mul_f32 v221, v221, v223\n"
      "v_cvt_pk_bf16_f32 v220, v220, v221\n"
      "v_mfma_f32_16x16x32_bf16 v[248:251], a[40:43], v[160:163], v[248:251]\n"
      "ds_write_b32 v46, v220 offset:11264\n"
      "v_add3_u32 v3, v3, v134, v135\n"
      "v_add3_u32 v3, v3, v140, v141\n"
      "v_mfma_f32_16x16x32_bf16 v[100:103], a[24:27], v[164:167], v[100:103]\n"
      "v_add3_u32 v3, v3, v142, v143\n"
      "v_xor_b32 v3, v3, v35\n"
      "v_mfma_f32_16x16x32_bf16 v[108:111], a[28:31], v[164:167], v[108:111]\n"
      "v_or_b32 v2, v2, v3\n"
      "s_waitcnt lgkmcnt(0)\n"
      "s_barrier\n"
      "v_mfma_f32_16x16x32_bf16 v[116:119], a[32:35], v[164:167], v[116:119]\n"
      "ds_read_b128 v[200:203], v47 offset:0\n"
      "v_cmp_ne_u32 vcc, 0, v2\n"
      "v_mfma_f32_16x16x32_bf16 v[124:127], a[36:39], v[164:167], v[124:127]\n"
      "ds_read_b128 v[204:207], v47 offset:4096\n"
      "v_mfma_f32_16x16x32_bf16 v[252:255], a[40:43], v[164:167], v[252:255]\n"
      "ds_read_b128 v[208:211], v47 offset:8192\n"
      "s_cbranch_vccnz L_shA1_%=\n"
      "L_bhA1_%=:\n"
      "v_xor_b32 v43, 0x80000000, v43\n"
      "v_mfma_f32_16x16x32_bf16 v[96:99], a[44:47], v[168:171], v[96:99]\n"
      "v_xor_b32 v59, 0x80000000, v59\n"
      "v_mfma_f32_16x16x32_bf16 v[104:107], a[48:51], v[168:171], v[104:107]\n"
      "v_mfma_f32_16x16x32_bf16 v[112:115], a[52:55], v[168:171], v[112:115]\n"
      "v_mfma_f32_16x16x32_bf16 v[120:123], a[56:59], v[168:171], v[120:123]\n"
      "s_waitcnt lgkmcnt(0)\n"
      "v_mfma_f32_16x16x32_bf16 v[248:251], a[60:63], v[168:171], v[248:251]\n"
      "buffer_store_dwordx4 v[200:203], v43, s[32:35], 0 offen offset:0 sc1\n"
      "v_add3_u32 v58, v200, v201, s30\n"
      "v_mfma_f32_16x16x32_bf16 v[100:103], a[44:47], v[172:175], v[100:103]\n"
      "v_add3_u32 v58, v58, v202, v203\n"
      "v_add3_u32 v58, v58, v204, v205\n"
      "v_mfma_f32_16x16x32_bf16 v[108:111], a[48:51], v[172:175], v[108:111]\n"
      "buffer_store_dwordx4 v[204:207], v43, s[32:35], 0 offen offset:1024 sc1\n"
      "v_add3_u32 v58, v58, v206, v207\n"
      "v_mfma_f32_16x16x32_bf16 v[116:119], a[52:55], v[172:175], v[116:119]\n"
      "v_add3_u32 v58, v58, v208, v209\n"
      "v_add3_u32 v58, v58, v210, v211\n"
      "v_mfma_f32_16x16x32_bf16 v[124:127], a[56:59], v[172:175], v[124:127]\n"
      "buffer_store_dwordx4 v[208:211], v43, s[32:35], 0 offen offset:2048 sc1\n"
      "v_mfma_f32_16x16x32_bf16 v[252:255], a[60:63], v[172:175], v[252:255]\n"
      "buffer_store_dword v58, v59, s[32:35], 0 offen sc1\n"
      "v_mfma_f32_16x16x32_bf16 v[96:99], a[64:67], v[128:131], v[96:99]\n"
      "v_mfma_f32_16x16x32_bf16 v[104:107], a[68:71], v[128:131], v[104:107]\n"
      "v_mfma_f32_16x16x32_bf16 v[112:115], a[72:75], v[128:131], v[112:115]\n"
      "v_mfma_f32_16x16x32_bf16 v[120:123], a[76:79], v[128:131], v[120:123]\n"
      "v_mfma_f32_16x16x32_bf16 v[248:251], a[80:83], v[128:131], v[248:251]\n"
      "v_mfma_f32_16x16x32_bf16 v[100:103], a[64:67], v[132:135], v[100:103]\n"
      "s_waitcnt vmcnt(18)\n"
      "v_mfma_f32_16x16x32_bf16 v[108:111], a[68:71], v[132:135], v[108:111]\n"
      "v_add3_u32 v2, v144, v145, s30\n"
      "v_add3_u32 v2, v2, v146, v147\n"
      "v_mfma_f32_16x16x32_bf16 v[116:119], a[72:75], v[132:135], v[116:119]\n"
      "v_add3_u32 v2, v2, v152, v153\n"
      "v_add3_u32 v2, v2, v154, v155\n"
      "v_mfma_f32_16x16x32_bf16 v[124:127], a[76:79], v[132:135], v[124:127]\n"
      "v_add_u32 v45, 102400, v7\n"
      "v_add3_u32 v2, v2, v64, v65\n"
      "v_mfma_f32_16x16x32_bf16 v[252:255], a[80:83], v[132:135], v[252:255]\n"
      "v_add3_u32 v2, v2, v66, v67\n"
      "v_xor_b32 v2, v2, v60\n"
      "v_mfma_f32_16x16x32_bf16 v[96:99], a[84:87], v[136:139], v[96:99]\n"
      "global_load_dwordx4 v[16:19], v45, s[10:11] offset:-4096 sc1\n"
      "v_add3_u32 v3, v148, v149, s30\n"
      "v_mfma_f32_16x16x32_bf16 v[104:107], a[88:91], v[136:139], v[104:107]\n"
      "v_add3_u32 v3, v3, v150, v151\n"
      "v_add3_u32 v3, v3, v156, v157\n"
      "v_mfma_f32_16x16x32_bf16 v[112:115], a[92:95], v[136:139], v[112:115]\n"
      "v_add3_u32 v3, v3, v158, v159\n"
      "v_add3_u32 v3, v3, v68, v69\n"
      "v_mfma_f32_16x16x32_bf16 v[120:123], a[96:99], v[136:139], v[120:123]\n"
      "v_add3_u32 v3, v3, v70, v71\n"
      "v_xor_b32 v3, v3, v61\n"
      "v_mfma_f32_16x16x32_bf16 v[248:251], a[100:103], v[136:139], v[248:251]\n"
      "global_load_dwordx4 v[20:23], v45, s[10:11] offset:-3072 sc1\n"
      "v_or_b32 v2, v2, v3\n"
      "v_mfma_f32_16x16x32_bf16 v[100:103], a[84:87], v[140:143], v[100:103]\n"
      "v_cmp_ne_u32 vcc, 0, v2\n"
      "v_mfma_f32_16x16x32_bf16 v[108:111], a[88:91], v[140:143], v[108:111]\n"
      "v_mfma_f32_16x16x32_bf16 v[116:119], a[92:95], v[140:143], v[116:119]\n"
      "s_cbranch_vccnz L_shA2_%=\n"
      "L_bhA2_%=:\n"
      "v_mfma_f32_16x16x32_bf16 v[124:127], a[96:99], v[140:143], v[124:127]\n"
      "global_load_dwordx4 v[8:11], v45, s[10:11] offset:-2048 sc1\n"
      "v_mfma_f32_16x16x32_bf16 v[252:255], a[100:103], v[140:143], v[252:255]\n"
      "v_mfma_f32_16x16x32_bf16 v[96:99], a[104:107], v[144:147], v[96:99]\n"
      "v_mfma_f32_16x16x32_bf16 v[104:107], a[108:111], v[144:147], v[104:107]\n"
      "global_load_dwordx4 v[12:15], v45, s[10:11] offset:-1024 sc1\n"
      "v_mfma_f32_16x16x32_bf16 v[112:115], a[112:115], v[144:147], v[112:115]\n"
      "v_mfma_f32_16x16x32_bf16 v[120:123], a[116:119], v[144:147], v[120:123]\n"
      "v_mfma_f32_16x16x32_bf16 v[248:251], a[120:123], v[144:147], v[248:251]\n"
      "global_load_dwordx4 v[160:163], v45, s[10:11] offset:0 sc1\n"
      "v_mfma_f32_16x16x32_bf16 v[100:103], a[104:107], v[148:151], v[100:103]\n"
      "v_mfma_f32_16x16x32_bf16 v[108:111], a[108:111], v[148:151], v[108:111]\n"
      "v_mfma_f32_16x16x32_bf16 v[116:119], a[112:115], v[148:151], v[116:119]\n"
      "global_load_dwordx4 v[164:167], v45, s[10:11] offset:1024 sc1\n"
      "v_mfma_f32_16x16x32_bf16 v[124:127], a[116:119], v[148:151], v[124:127]\n"
      "global_load_dwordx2 v[36:37], v55, s[26:27] offset:2048 sc1\n"
      "v_mfma_f32_16x16x32_bf16 v[252:255], a[120:123], v[148:151], v[252:255]\n"
      "v_add_u32 v45, 108544, v7\n"
      "v_mfma_f32_16x16x32_bf16 v[96:99], a[124:127], v[152:155], v[96:99]\n"
      "v_mfma_f32_16x16x32_bf16 v[104:107], a[128:131], v[152:155], v[104:107]\n"
      "global_load_dwordx4 v[168:171], v45, s[10:11] offset:-4096 sc1\n"
      "v_mfma_f32_16x16x32_bf16 v[112:115], a[132:135], v[152:155], v[112:115]\n"
      "v_mfma_f32_16x16x32_bf16 v[120:123], a[136:139], v[152:155], v[120:123]\n"
      "v_mfma_f32_16x16x32_bf16 v[248:251], a[140:143], v[152:155], v[248:251]\n"
      "v_mfma_f32_16x16x32_bf16 v[100:103], a[124:127], v[156:159], v[100:103]\n"
      "global_load_dwordx4 v[172:175], v45, s[10:11] offset:-3072 sc1\n"
      "s_waitcnt vmcnt(17)\n"
      "v_mfma_f32_16x16x32_bf16 v[108:111], a[128:131], v[156:159], v[108:111]\n"
      "v_add3_u32 v2, v72, v73, s30\n"
      "v_add3_u32 v2, v2, v74, v75\n"
      "v_mfma_f32_16x16x32_bf16 v[116:119], a[132:135], v[156:159], v[116:119]\n"
      "v_add3_u32 v2, v2, v80, v81\n"
      "v_add3_u32 v2, v2, v82, v83\n"
      "v_mfma_f32_16x16x32_bf16 v[124:127], a[136:139], v[156:159], v[124:127]\n"
      "v_add3_u32 v2, v2, v88, v89\n"
      "v_add3_u32 v2, v2, v90, v91\n"
      "v_mfma_f32_16x16x32_bf16 v[252:255], a[140:143], v[156:159], v[252:255]\n"
      "global_load_dwordx4 v[128:131], v45, s[10:11] offset:-2048 sc1\n"
      "v_xor_b32 v2, v2, v62\n"
      "v_mfma_f32_16x16x32_bf16 v[96:99], a[144:147], v[64:67], v[96:99]\n"
      "v_add3_u32 v3, v76, v77, s30\n"
      "v_add3_u32 v3, v3, v78, v79\n"
      "v_mfma_f32_16x16x32_bf16 v[104:107], a[148:151], v[64:67], v[104:107]\n"
      "v_add3_u32 v3, v3, v84, v85\n"
      "v_add3_u32 v3, v3, v86, v87\n"
      "v_mfma_f32_16x16x32_bf16 v[112:115], a[152:155], v[64:67], v[112:115]\n"
      "v_add3_u32 v3, v3, v92, v93\n"
      "v_add3_u32 v3, v3, v94, v95\n"
      "v_mfma_f32_16x16x32_bf16 v[120:123], a[156:159], v[64:67], v[120:123]\n"
      "global_load_dwordx4 v[132:135], v45, s[10:11] offset:-1024 sc1\n"
      "v_xor_b32 v3, v3, v63\n"
      "v_mfma_f32_16x16x32_bf16 v[248:251], a[160:163], v[64:67], v[248:251]\n"
      "v_or_b32 v2, v2, v3\n"
      "v_cmp_ne_u32 vcc, 0, v2\n"
      "v_mfma_f32_16x16x32_bf16 v[100:103], a[144:147], v[68:71], v[100:103]\n"
      "v_mfma_f32_16x16x32_bf16 v[108:111], a[148:151], v[68:71], v[108:111]\n"
      "s_cbranch_vccnz L_shA3_%=\n"
      "L_bhA3_%=:\n"
      "v_mfma_f32_16x16x32_bf16 v[116:119], a[152:155], v[68:71], v[116:119]\n"
      "global_load_dwordx4 v[136:139], v45, s[10:11] offset:0 sc1\n"
      "v_mfma_f32_16x16x32_bf16 v[124:127], a[156:159], v[68:71], v[124:127]\n"
      "v_mfma_f32_16x16x32_bf16 v[252:255], a[160:163], v[68:71], v[252:255]\n"
      "v_mfma_f32_16x16x32_bf16 v[96:99], a[164:167], v[72:75], v[96:99]\n"
      "global_load_dwordx4 v[140:143], v45, s[10:11] offset:1024 sc1\n"
      "v_mfma_f32_16x16x32_bf16 v[104:107], a[168:171], v[72:75], v[104:107]\n"
      "global_load_dwordx2 v[38:39], v55, s[26:27] offset:2560 sc1\n"
      "v_mfma_f32_16x16x32_bf16 v[112:115], a[172:175], v[72:75], v[112:115]\n"
      "v_add_u32 v45, 102400, v7\n"
      "v_mfma_f32_16x16x32_bf16 v[120:123], a[176:179], v[72:75], v[120:123]\n"
      "v_mfma_f32_16x16x32_bf16 v[248:251], a[180:183], v[72:75], v[248:251]\n"
      "global_load_dwordx4 v[224:227], v45, s[12:13] offset:-4096 sc1\n"
      "v_mfma_f32_16x16x32_bf16 v[100:103], a[164:167], v[76:79], v[100:103]\n"
      "v_mfma_f32_16x16x32_bf16 v[108:111], a[168:171], v[76:79], v[108:111]\n"
      "v_mfma_f32_16x16x32_bf16 v[116:119], a[172:175], v[76:79], v[116:119]\n"
      "v_mfma_f32_16x16x32_bf16 v[124:127], a[176:179], v[76:79], v[124:127]\n"
      "global_load_dwordx4 v[228:231], v45, s[12:13] offset:-3072 sc1\n"
      "v_mfma_f32_16x16x32_bf16 v[252:255], a[180:183], v[76:79], v[252:255]\n"
      "v_mfma_f32_16x16x32_bf16 v[96:99], a[184:187], v[80:83], v[96:99]\n"
      "v_mfma_f32_16x16x32_bf16 v[104:107], a[188:191], v[80:83], v[104:107]\n"
      "v_mfma_f32_16x16x32_bf16 v[112:115], a[192:195], v[80:83], v[112:115]\n"
      "global_load_dwordx4 v[232:235], v45, s[12:13] offset:-2048 sc1\n"
      "v_mfma_f32_16x16x32_bf16 v[120:123], a[196:199], v[80:83], v[120:123]\n"
      "v_mfma_f32_16x16x32_bf16 v[248:251], a[200:203], v[80:83], v[248:251]\n"
      "v_mfma_f32_16x16x32_bf16 v[100:103], a[184:187], v[84:87], v[100:103]\n"
      "s_waitcnt vmcnt(10)\n"
      "v_add3_u32 v2, v16, v17, s30\n"
      "v_mfma_f32_16x16x32_bf16 v[108:111], a[188:191], v[84:87], v[108:111]\n"
      "global_load_dwordx4 v[236:239], v45, s[12:13] offset:-1024 sc1\n"
      "v_add3_u32 v2, v2, v18, v19\n"
      "v_mfma_f32_16x16x32_bf16 v[116:119], a[192:195], v[84:87], v[116:119]\n"
      "v_add3_u32 v2, v2, v20, v21\n"
      "v_add3_u32 v2, v2, v22, v23\n"
      "v_mfma_f32_16x16x32_bf16 v[124:127], a[196:199], v[84:87], v[124:127]\n"
      "v_add3_u32 v2, v2, v8, v9\n"
      "v_add3_u32 v2, v2, v10, v11\n"
      "v_mfma_f32_16x16x32_bf16 v[252:255], a[200:203], v[84:87], v[252:255]\n"
      "v_xor_b32 v2, v2, v36\n"
      "v_add3_u32 v3, v12, v13, s30\n"
      "v_mfma_f32_16x16x32_bf16 v[96:99], a[204:207], v[88:91], v[96:99]\n"
      "global_load_dwordx4 v[240:243], v45, s[12:13] offset:0 sc1\n"
      "v_add3_u32 v3, v3, v14, v15\n"
      "v_mfma_f32_16x16x32_bf16 v[104:107], a[208:211], v[88:91], v[104:107]\n"
      "v_add3_u32 v3, v3, v160, v161\n"
      "v_add3_u32 v3, v3, v162, v163\n"
      "v_mfma_f32_16x16x32_bf16 v[112:115], a[212:215], v[88:91], v[112:115]\n"
      "v_add3_u32 v3, v3, v164, v165\n"
      "v_add3_u32 v3, v3, v166, v167\n"
      "v_mfma_f32_16x16x32_bf16 v[120:123], a[216:219], v[88:91], v[120:123]\n"
      "v_xor_b32 v3, v3, v37\n"
      "v_or_b32 v2, v2, v3\n"
      "v_mfma_f32_16x16x32_bf16 v[248:251], a[220:223], v[88:91], v[248:251]\n"
      "global_load_dwordx4 v[244:247], v45, s[12:13] offset:1024 sc1\n"
      "v_cmp_ne_u32 vcc, 0, v2\n"
      "v_mfma_f32_16x16x32_bf16 v[100:103], a[204:207], v[92:95], v[100:103]\n"
      "v_mfma_f32_16x16x32_bf16 v[108:111], a[208:211], v[92:95], v[108:111]\n"
      "s_cbranch_vccnz L_sh20_%=\n"
      "L_bh20_%=:\n"
      "v_mfma_f32_16x16x32_bf16 v[116:119], a[212:215], v[92:95], v[116:119]\n"
      "v_mfma_f32_16x16x32_bf16 v[124:127], a[216:219], v[92:95], v[124:127]\n"
      "global_load_dwordx2 v[24:25], v55, s[26:27] offset:3072 sc1\n"
      "v_add_u32 v45, 108544, v7\n"
      "v_mfma_f32_16x16x32_bf16 v[252:255], a[220:223], v[92:95], v[252:255]\n"
      "v_mfma_f32_16x16x32_bf16 v[176:179], a[240:243], v[16:19], 0\n"
      "global_load_dwordx4 v[144:147], v45, s[12:13] offset:-4096 sc1\n"
      "v_mfma_f32_16x16x32_bf16 v[180:183], a[244:247], v[16:19], 0\n"
      "v_mfma_f32_16x16x32_bf16 v[184:187], a[248:251], v[16:19], 0\n"
      "v_mfma_f32_16x16x32_bf16 v[188:191], a[252:255], v[16:19], 0\n"
      "v_mfma_f32_16x16x32_bf16 v[192:195], a[0:3], v[16:19], 0\n"
      "global_load_dwordx4 v[148:151], v45, s[12:13] offset:-3072 sc1\n"
      "v_mfma_f32_16x16x32_bf16 v[176:179], a[4:7], v[20:23], v[176:179]\n"
      "ds_write_b128 v56, v[96:99] offset:0\n"
      "v_mfma_f32_16x16x32_bf16 v[180:183], a[8:11], v[20:23], v[180:183]\n"
      "v_mfma_f32_16x16x32_bf16 v[184:187], a[12:15], v[20:23], v[184:187]\n"
      "v_mfma_f32_16x16x32_bf16 v[188:191], a[16:19], v[20:23], v[188:191]\n"
      "global_load_dwordx4 v[152:155], v45, s[12:13] offset:-2048 sc1\n"
      "v_mfma_f32_16x16x32_bf16 v[192:195], a[20:23], v[20:23], v[192:195]\n"
      "ds_write_b128 v56, v[100:103] offset:20480\n"
      "v_mfma_f32_16x16x32_bf16 v[176:179], a[24:27], v[8:11], v[176:179]\n"
      "v_mfma_f32_16x16x32_bf16 v[180:183], a[28:31], v[8:11], v[180:183]\n"
      "v_mfma_f32_16x16x32_bf16 v[184:187], a[32:35], v[8:11], v[184:187]\n"
      "global_load_dwordx4 v[156:159], v45, s[12:13] offset:-1024 sc1\n"
      "v_mfma_f32_16x16x32_bf16 v[188:191], a[36:39], v[8:11], v[188:191]\n"
      "ds_write_b128 v56, v[104:107] offset:1024\n"
      "v_mfma_f32_16x16x32_bf16 v[192:195], a[40:43], v[8:11], v[192:195]\n"
      "v_mfma_f32_16x16x32_bf16 v[176:179], a[44:47], v[12:15], v[176:179]\n"
      "s_waitcnt vmcnt(11)\n"
      "v_add3_u32 v2, v168, v169, s30\n"
      "v_mfma_f32_16x16x32_bf16 v[180:183], a[48:51], v[12:15], v[180:183]\n"
      "global_load_dwordx4 v[64:67], v45, s[12:13] offset:0 sc1\n"
      "v_add3_u32 v2, v2, v170, v171\n"
      "v_mfma_f32_16x16x32_bf16 v[184:187], a[52:55], v[12:15], v[184:187]\n"
      "ds_write_b128 v56, v[108:111] offset:21504\n"
      "v_add3_u32 v2, v2, v172, v173\n"
      "v_add3_u32 v2, v2, v174, v175\n"
      "v_add3_u32 v2, v2, v128, v129\n"
      "v_add3_u32 v2, v2, v130, v131\n"
      "v_mfma_f32_16x16x32_bf16 v[188:191], a[56:59], v[12:15], v[188:191]\n"
      "v_xor_b32 v2, v2, v38\n"
      "v_add3_u32 v3, v132, v133, s30\n"
      "v_mfma_f32_16x16x32_bf16 v[192:195], a[60:63], v[12:15], v[192:195]\n"
      "v_add3_u32 v3, v3, v134, v135\n"
      "v_add3_u32 v3, v3, v136, v137\n"
      "v_mfma_f32_16x16x32_bf16 v[176:179], a[64:67], v[160:163], v[176:179]\n"
      "global_load_dwordx4 v[68:71], v45, s[12:13] offset:1024 sc1\n"
      "v_add3_u32 v3, v3, v138, v139\n"
      "v_mfma_f32_16x16x32_bf16 v[180:183], a[68:71], v[160:163], v[180:183]\n"
      "ds_write_b128 v56, v[112:115] offset:2048\n"
      "v_add3_u32 v3, v3, v140, v141\n"
      "v_add3_u32 v3, v3, v142, v143\n"
      "v_xor_b32 v3, v3, v39\n"
      "v_or_b32 v2, v2, v3\n"
      "v_mfma_f32_16x16x32_bf16 v[184:187], a[72:75], v[160:163], v[184:187]\n"
      "v_cmp_ne_u32 vcc, 0, v2\n"
      "v_mfma_f32_16x16x32_bf16 v[188:191], a[76:79], v[160:163], v[188:191]\n"
      "v_mfma_f32_16x16x32_bf16 v[192:195], a[80:83], v[160:163], v[192:195]\n"
      "global_load_dwordx2 v[26:27], v55, s[26:27] offset:3584 sc1\n"
      "v_mfma_f32_16x16x32_bf16 v[176:179], a[84:87], v[164:167], v[176:179]\n"
      "ds_write_b128 v56, v[116:119] offset:22528\n"
      "s_cbranch_vccnz L_sh21_%=\n"
      "L_bh21_%=:\n"
      "v_mfma_f32_16x16x32_bf16 v[180:183], a[88:91], v[164:167], v[180:183]\n"
      "global_load_dwordx4 v[72:75], v53, s[26:27] offset:0 sc1\n"
      "v_mfma_f32_16x16x32_bf16 v[184:187], a[92:95], v[164:167], v[184:187]\n"
      "v_mfma_f32_16x16x32_bf16 v[188:191], a[96:99], v[164:167], v[188:191]\n"
      "v_mfma_f32_16x16x32_bf16 v[192:195], a[100:103], v[164:167], v[192:195]\n"
      "ds_write_b128 v56, v[120:123] offset:3072\n"
      "v_mfma_f32_16x16x32_bf16 v[176:179], a[104:107], v[168:171], v[176:179]\n"
      "global_load_dwordx4 v[76:79], v53, s[26:27] offset:16 sc1\n"
      "s_mov_b32 m0, s43\n"
      "v_mfma_f32_16x16x32_bf16 v[180:183], a[108:111], v[168:171], v[180:183]\n"
      "v_mfma_f32_16x16x32_bf16 v[184:187], a[112:115], v[168:171], v[184:187]\n"
      "v_mfma_f32_16x16x32_bf16 v[188:191], a[116:119], v[168:171], v[188:191]\n"
      "ds_write_b128 v56, v[124:127] offset:23552\n"
      "v_mfma_f32_16x16x32_bf16 v[192:195], a[120:123], v[168:171], v[192:195]\n"
      "global_load_lds_dwordx4 v4, s[26:27] sc1\n"
      "v_mfma_f32_16x16x32_bf16 v[176:179], a[124:127], v[172:175], v[176:179]\n"
      "v_mfma_f32_16x16x32_bf16 v[180:183], a[128:131], v[172:175], v[180:183]\n"
      "v_mfma_f32_16x16x32_bf16 v[184:187], a[132:135], v[172:175], v[184:187]\n"
      "ds_write_b128 v56, v[248:251] offset:4096\n"
      "v_mfma_f32_16x16x32_bf16 v[188:191], a[136:139], v[172:175], v[188:191]\n"
      "global_load_dwordx4 v[80:83], v53, s[26:27] offset:32 sc1\n"
      "v_mfma_f32_16x16x32_bf16 v[192:195], a[140:143], v[172:175], v[192:195]\n"
      "v_mfma_f32_16x16x32_bf16 v[176:179], a[144:147], v[128:131], v[176:179]\n"
      "v_mfma_f32_16x16x32_bf16 v[180:183], a[148:151], v[128:131], v[180:183]\n"
      "ds_write_b128 v56, v[252:255] offset:24576\n"
      "v_mfma_f32_16x16x32_bf16 v[184:187], a[152:155], v[128:131], v[184:187]\n"
      "global_load_dwordx4 v[84:87], v53, s[26:27] offset:48 sc1\n"
      "v_mfma_f32_16x16x32_bf16 v[188:191], a[156:159], v[128:131], v[188:191]\n"
      "v_add_u32 v4, 0xc00, v4\n"
      "v_mfma_f32_16x16x32_bf16 v[192:195], a[160:163], v[128:131], v[192:195]\n"
      "v_mfma_f32_16x16x32_bf16 v[176:179], a[164:167], v[132:135], v[176:179]\n"
      "s_waitcnt vmcnt(12)\n"
      "v_add3_u32 v2, v224, v225, s30\n"
      "v_mfma_f32_16x16x32_bf16 v[180:183], a[168:171], v[132:135], v[180:183]\n"
      "global_load_lds_dwordx4 v4, s[26:27] offset:1024 sc1\n"
      "v_add3_u32 v2, v2, v226, v227\n"
      "v_mfma_f32_16x16x32_bf16 v[184:187], a[172:175], v[132:135], v[184:187]\n"
      "v_add3_u32 v2, v2, v228, v229\n"
      "v_add3_u32 v2, v2, v230, v231\n"
      "v_mfma_f32_16x16x32_bf16 v[188:191], a[176:179], v[132:135], v[188:191]\n"
      "v_add3_u32 v2, v2, v232, v233\n"
      "v_add3_u32 v2, v2, v234, v235\n"
      "v_mfma_f32_16x16x32_bf16 v[192:195], a[180:183], v[132:135], v[192:195]\n"
      "v_xor_b32 v2, v2, v24\n"
      "v_add3_u32 v3, v236, v237, s30\n"
      "v_mfma_f32_16x16x32_bf16 v[176:179], a[184:187], v[136:139], v[176:179]\n"
      "v_add3_u32 v3, v3, v238, v239\n"
      "v_add3_u32 v3, v3, v240, v241\n"
      "v_mfma_f32_16x16x32_bf16 v[180:183], a[188:191], v[136:139], v[180:183]\n"
      "v_add3_u32 v3, v3, v242, v243\n"
      "v_add3_u32 v3, v3, v244, v245\n"
      "v_mfma_f32_16x16x32_bf16 v[184:187], a[192:195], v[136:139], v[184:187]\n"
      "v_add3_u32 v3, v3, v246, v247\n"
      "v_xor_b32 v3, v3, v25\n"
      "v_mfma_f32_16x16x32_bf16 v[188:191], a[196:199], v[136:139], v[188:191]\n"
      "v_or_b32 v2, v2, v3\n"
      "v_cmp_ne_u32 vcc, 0, v2\n"
      "v_mfma_f32_16x16x32_bf16 v[192:195], a[200:203], v[136:139], v[192:195]\n"
      "v_mfma_f32_16x16x32_bf16 v[176:179], a[204:207], v[140:143], v[176:179]\n"
      "s_cbranch_vccnz L_sh30_%=\n"
      "L_bh30_%=:\n"
      "v_add_u32 v4, 0xc00, v4\n"
      "v_mfma_f32_16x16x32_bf16 v[180:183], a[208:211], v[140:143], v[180:183]\n"
      "global_load_lds_dwordx4 v4, s[26:27] offset:2048 sc1\n"
      "v_mfma_f32_16x16x32_bf16 v[184:187], a[212:215], v[140:143], v[184:187]\n"
      "v_mfma_f32_16x16x32_bf16 v[188:191], a[216:219], v[140:143], v[188:191]\n"
      "v_mfma_f32_16x16x32_bf16 v[192:195], a[220:223], v[140:143], v[192:195]\n"
      "v_mfma_f32_16x16x32_bf16 v[200:203], a[240:243], v[224:227], 0\n"
      "v_mfma_f32_16x16x32_bf16 v[204:207], a[244:247], v[224:227], 0\n"
      "v_mfma_f32_16x16x32_bf16 v[208:211], a[248:251], v[224:227], 0\n"
      "v_mfma_f32_16x16x32_bf16 v[212:215], a[252:255], v[224:227], 0\n"
      "v_mfma_f32_16x16x32_bf16 v[216:219], a[0:3], v[224:227], 0\n"
      "ds_write_b128 v56, v[176:179] offset:40960\n"
      "v_mfma_f32_16x16x32_bf16 v[200:203], a[4:7], v[228:231], v[200:203]\n"
      "v_mfma_f32_16x16x32_bf16 v[204:207], a[8:11], v[228:231], v[204:207]\n"
      "v_mfma_f32_16x16x32_bf16 v[208:211], a[12:15], v[228:231], v[208:211]\n"
      "v_mfma_f32_16x16x32_bf16 v[212:215], a[16:19], v[228:231], v[212:215]\n"
      "ds_write_b128 v56, v[180:183] offset:41984\n"
      "v_mfma_f32_16x16x32_bf16 v[216:219], a[20:23], v[228:231], v[216:219]\n"
      "v_mfma_f32_16x16x32_bf16 v[200:203], a[24:27], v[232:235], v[200:203]\n"
      "v_mfma_f32_16x16x32_bf16 v[204:207], a[28:31], v[232:235], v[204:207]\n"
      "v_mfma_f32_16x16x32_bf16 v[208:211], a[32:35], v[232:235], v[208:211]\n"
      "ds_write_b128 v56, v[184:187] offset:43008\n"
      "v_mfma_f32_16x16x32_bf16 v[212:215], a[36:39], v[232:235], v[212:215]\n"
      "v_mfma_f32_16x16x32_bf16 v[216:219], a[40:43], v[232:235], v[216:219]\n"
      "v_mfma_f32_16x16x32_bf16 v[200:203], a[44:47], v[236:239], v[200:203]\n"
      "s_waitcnt vmcnt(7)\n"
      "v_add3_u32 v2, v144, v145, s30\n"
      "v_mfma_f32_16x16x32_bf16 v[204:207], a[48:51], v[236:239], v[204:207]\n"
      "ds_write_b128 v56, v[188:191] offset:44032\n"
      "v_add3_u32 v2, v2, v146, v147\n"
      "v_add3_u32 v2, v2, v148, v149\n"
      "v_add3_u32 v2, v2, v150, v151\n"
      "v_add3_u32 v2, v2, v152, v153\n"
      "v_mfma_f32_16x16x32_bf16 v[208:211], a[52:55], v[236:239], v[208:211]\n"
      "v_add3_u32 v2, v2, v154, v155\n"
      "v_xor_b32 v2, v2, v26\n"
      "v_mfma_f32_16x16x32_bf16 v[212:215], a[56:59], v[236:239], v[212:215]\n"
      "v_add3_u32 v3, v156, v157, s30\n"
      "v_add3_u32 v3, v3, v158, v159\n"
      "v_mfma_f32_16x16x32_bf16 v[216:219], a[60:63], v[236:239], v[216:219]\n"
      "v_add3_u32 v3, v3, v64, v65\n"
      "v_add3_u32 v3, v3, v66, v67\n"
      "v_mfma_f32_16x16x32_bf16 v[200:203], a[64:67], v[240:243], v[200:203]\n"
      "ds_write_b128 v56, v[192:195] offset:45056\n"
      "v_add3_u32 v3, v3, v68, v69\n"
      "v_add3_u32 v3, v3, v70, v71\n"
      "v_xor_b32 v3, v3, v27\n"
      "v_or_b32 v2, v2, v3\n"
      "v_mfma_f32_16x16x32_bf16 v[204:207], a[68:71], v[240:243], v[204:207]\n"
      "v_cmp_ne_u32 vcc, 0, v2\n"
      "v_add_u32 v56, 0xf000, v56\n"
      "v_mfma_f32_16x16x32_bf16 v[208:211], a[72:75], v[240:243], v[208:211]\n"
      "v_mfma_f32_16x16x32_bf16 v[212:215], a[76:79], v[240:243], v[212:215]\n"
      "s_cbranch_vccnz L_sh31_%=\n"
      "L_bh31_%=:\n"
      "v_mfma_f32_16x16x32_bf16 v[216:219], a[80:83], v[240:243], v[216:219]\n"
      "v_mfma_f32_16x16x32_bf16 v[200:203], a[84:87], v[244:247], v[200:203]\n"
      "v_mfma_f32_16x16x32_bf16 v[204:207], a[88:91], v[244:247], v[204:207]\n"
      "v_mfma_f32_16x16x32_bf16 v[208:211], a[92:95], v[244:247], v[208:211]\n"
      "v_mfma_f32_16x16x32_bf16 v[212:215], a[96:99], v[244:247], v[212:215]\n"
      "v_mfma_f32_16x16x32_bf16 v[216:219], a[100:103], v[244:247], v[216:219]\n"
      "v_mfma_f32_16x16x32_bf16 v[200:203], a[104:107], v[144:147], v[200:203]\n"
      "s_waitcnt vmcnt(2)\n"
      "v_add3_u32 v88, v73, v75, v77\n"
      "v_mfma_f32_16x16x32_bf16 v[204:207], a[108:111], v[144:147], v[204:207]\n"
      "v_add3_u32 v88, v88, v79, v81\n"
      "v_add3_u32 v88, v88, v83, v85\n"
      "v_mfma_f32_16x16x32_bf16 v[208:211], a[112:115], v[144:147], v[208:211]\n"
      "v_add_u32 v88, v88, v87\n"
      "v_cmp_ne_u32 vcc, s71, v88\n"
      "v_mfma_f32_16x16x32_bf16 v[212:215], a[116:119], v[144:147], v[212:215]\n"
      "v_mfma_f32_16x16x32_bf16 v[216:219], a[120:123], v[144:147], v[216:219]\n"
      "s_cbranch_vccnz L_sgb_%=\n"
      "L_bgb_%=:\n"
      "v_add_f32 v72, v72, v74\n"
      "v_mfma_f32_16x16x32_bf16 v[200:203], a[124:127], v[148:151], v[200:203]\n"
      "v_add_f32 v76, v76, v78\n"
      "v_add_f32 v80, v80, v82\n"
      "v_mfma_f32_16x16x32_bf16 v[204:207], a[128:131], v[148:151], v[204:207]\n"
      "v_add_f32 v84, v84, v86\n"
      "v_add_f32 v72, v72, v76\n"
      "v_mfma_f32_16x16x32_bf16 v[208:211], a[132:135], v[148:151], v[208:211]\n"
      "v_add_f32 v73, v80, v84\n"
      "v_cvt_pk_bf16_f32 v72, 0, v72\n"
      "v_mfma_f32_16x16x32_bf16 v[212:215], a[136:139], v[148:151], v[212:215]\n"
      "v_cvt_pk_bf16_f32 v73, 0, v73\n"
      "v_mfma_f32_16x16x32_bf16 v[216:219], a[140:143], v[148:151], v[216:219]\n"
      "ds_write_b64 v5, v[72:73]\n"
      "v_mfma_f32_16x16x32_bf16 v[200:203], a[144:147], v[152:155], v[200:203]\n"
      "v_mfma_f32_16x16x32_bf16 v[204:207], a[148:151], v[152:155], v[204:207]\n"
      "v_mfma_f32_16x16x32_bf16 v[208:211], a[152:155], v[152:155], v[208:211]\n"
      "v_mfma_f32_16x16x32_bf16 v[212:215], a[156:159], v[152:155], v[212:215]\n"
      "v_mfma_f32_16x16x32_bf16 v[216:219], a[160:163], v[152:155], v[216:219]\n"
      "v_mfma_f32_16x16x32_bf16 v[200:203], a[164:167], v[156:159], v[200:203]\n"
      "v_mfma_f32_16x16x32_bf16 v[200:203], a[184:187], v[64:67], v[200:203]\n"
      "v_mfma_f32_16x16x32_bf16 v[200:203], a[204:207], v[68:71], v[200:203]\n"
      "v_mfma_f32_16x16x32_bf16 v[204:207], a[168:171], v[156:159], v[204:207]\n"
      "v_mfma_f32_16x16x32_bf16 v[204:207], a[188:191], v[64:67], v[204:207]\n"
      "v_mfma_f32_16x16x32_bf16 v[204:207], a[208:211], v[68:71], v[204:207]\n"
      "ds_write_b128 v56, v[200:203] offset:0\n"
      "v_mfma_f32_16x16x32_bf16 v[208:211], a[172:175], v[156:159], v[208:211]\n"
      "v_mfma_f32_16x16x32_bf16 v[208:211], a[192:195], v[64:67], v[208:211]\n"
      "v_mfma_f32_16x16x32_bf16 v[208:211], a[212:215], v[68:71], v[208:211]\n"
      "ds_write_b128 v56, v[204:207] offset:1024\n"
      "v_mfma_f32_16x16x32_bf16 v[212:215], a[176:179], v[156:159], v[212:215]\n"
      "v_mfma_f32_16x16x32_bf16 v[212:215], a[196:199], v[64:67], v[212:215]\n"
      "v_mfma_f32_16x16x32_bf16 v[212:215], a[216:219], v[68:71], v[212:215]\n"
      "ds_write_b128 v56, v[208:211] offset:2048\n"
      "v_mfma_f32_16x16x32_bf16 v[216:219], a[180:183], v[156:159], v[216:219]\n"
      "v_mfma_f32_16x16x32_bf16 v[216:219], a[200:203], v[64:67], v[216:219]\n"
      "v_mfma_f32_16x16x32_bf16 v[216:219], a[220:223], v[68:71], v[216:219]\n"
      "ds_write_b128 v56, v[212:215] offset:3072\n"
      "s_waitcnt vmcnt(0)\n"
      "s_nop 5\n"
      "ds_write_b128 v56, v[216:219] offset:4096\n"
      "v_lshrrev_b32 v2, 1, v7\n"
      "v_lshrrev_b32 v3, 4, v7\n"
      "v_add_u32 v8, s43, v2\n"
      "v_and_b32 v9, 0x30, v3\n"
      "s_mul_i32 s40, s60, 20480\n"
      "v_and_b32 v3, 15, v3\n"
      "ds_read_b64_tr_b16 v[12:13], v8 offset:0\n"
      "ds_read_b64_tr_b16 v[14:15], v8 offset:512\n"
      "ds_read_b64_tr_b16 v[16:17], v8 offset:1024\n"
      "ds_read_b64_tr_b16 v[18:19], v8 offset:1536\n"
      "ds_read_b64_tr_b16 v[20:21], v8 offset:2048\n"
      "v_add_u32 v10, 163328, v9\n"
      "v_add_u32 v8, s40, v7\n"
      "v_mul_u32_u24 v3, 1280, v3\n"
      "v_lshrrev_b32 v9, 1, v9\n"
      "ds_read_b128 v[24:27], v10 offset:0\n"
      "ds_read_b128 v[28:31], v10 offset:64\n"
      "ds_read_b128 v[32:35], v10 offset:128\n"
      "ds_read_b128 v[36:39], v10 offset:192\n"
      "ds_read_b128 v[40:43], v10 offset:256\n"
      "v_add3_u32 v3, v3, v9, s91\n"
      "s_waitcnt lgkmcnt(0)\n"
      "s_barrier\n"
      "ds_read_b128 v[64:67], v8 offset:0\n"
      "ds_read_b128 v[68:71], v8 offset:5120\n"
      "ds_read_b128 v[72:75], v8 offset:10240\n"
      "ds_read_b128 v[76:79], v8 offset:15360\n"
      "ds_read_b128 v[80:83], v8 offset:1024\n"
      "ds_read_b128 v[84:87], v8 offset:6144\n"
      "ds_read_b128 v[88:91], v8 offset:11264\n"
      "ds_read_b128 v[92:95], v8 offset:16384\n"
      "ds_read_b128 v[96:99], v8 offset:2048\n"
      "ds_read_b128 v[100:103], v8 offset:7168\n"
      "ds_read_b128 v[104:107], v8 offset:12288\n"
      "ds_read_b128 v[108:111], v8 offset:17408\n"
      "ds_read_b128 v[112:115], v8 offset:3072\n"
      "ds_read_b128 v[116:119], v8 offset:8192\n"
      "ds_read_b128 v[120:123], v8 offset:13312\n"
      "ds_read_b128 v[124:127], v8 offset:18432\n"
      "ds_read_b128 v[128:131], v8 offset:4096\n"
      "ds_read_b128 v[132:135], v8 offset:9216\n"
      "ds_read_b128 v[136:139], v8 offset:14336\n"
      "ds_read_b128 v[140:143], v8 offset:19456\n"
      "v_lshlrev_b32 v44, 16, v12\n"
      "v_and_b32 v45, 0xffff0000, v12\n"
      "v_lshlrev_b32 v46, 16, v13\n"
      "v_and_b32 v47, 0xffff0000, v13\n"
      "v_lshlrev_b32 v48, 16, v14\n"
      "v_and_b32 v49, 0xffff0000, v14\n"
      "v_lshlrev_b32 v50, 16, v15\n"
      "v_and_b32 v51, 0xffff0000, v15\n"
      "v_lshlrev_b32 v52, 16, v16\n"
      "v_and_b32 v53, 0xffff0000, v16\n"
      "v_lshlrev_b32 v54, 16, v17\n"
      "v_and_b32 v55, 0xffff0000, v17\n"
      "v_lshlrev_b32 v56, 16, v18\n"
      "v_and_b32 v57, 0xffff0000, v18\n"
      "v_lshlrev_b32 v58, 16, v19\n"
      "v_and_b32 v59, 0xffff0000, v19\n"
      "v_lshlrev_b32 v60, 16, v20\n"
      "v_and_b32 v61, 0xffff0000, v20\n"
      "v_lshlrev_b32 v62, 16, v21\n"
      "v_and_b32 v63, 0xffff0000, v21\n"
      "s_waitcnt lgkmcnt(14)\n"
      "v_pk_add_f32 v[64:65], v[64:65], v[68:69]\n"
      "v_pk_add_f32 v[66:67], v[66:67], v[70:71]\n"
      "v_pk_add_f32 v[72:73], v[72:73], v[76:77]\n"
      "v_pk_add_f32 v[74:75], v[74:75], v[78:79]\n"
      "v_pk_add_f32 v[64:65], v[64:65], v[72:73]\n"
      "v_pk_add_f32 v[66:67], v[66:67], v[74:75]\n"
      "v_cvt_pk_bf16_f32 v64, 0, v64\n"
      "v_cvt_pk_bf16_f32 v65, 0, v65\n"
      "v_cvt_pk_bf16_f32 v66, 0, v66\n"
      "v_cvt_pk_bf16_f32 v67, 0, v67\n"
      "v_pk_mul_f32 v[64:65], v[64:65], v[24:25]\n"
      "v_pk_mul_f32 v[66:67], v[66:67], v[26:27]\n"
      "v_cvt_pk_bf16_f32 v64, 0, v64\n"
      "v_cvt_pk_bf16_f32 v65, 0, v65\n"
      "v_cvt_pk_bf16_f32 v66, 0, v66\n"
      "v_cvt_pk_bf16_f32 v67, 0, v67\n"
      "v_pk_add_f32 v[64:65], v[64:65], v[44:45]\n"
      "v_pk_add_f32 v[66:67], v[66:67], v[46:47]\n"
      "v_cvt_pk_bf16_f32 v64, v64, v65\n"
      "v_cvt_pk_bf16_f32 v65, v66, v67\n"
      "global_store_dwordx2 v3, v[64:65], s[28:29] offset:0\n"
      "s_waitcnt lgkmcnt(12)\n"
      "v_pk_add_f32 v[80:81], v[80:81], v[84:85]\n"
      "v_pk_add_f32 v[82:83], v[82:83], v[86:87]\n"
      "v_pk_add_f32 v[88:89], v[88:89], v[92:93]\n"
      "v_pk_add_f32 v[90:91], v[90:91], v[94:95]\n"
      "v_pk_add_f32 v[80:81], v[80:81], v[88:89]\n"
      "v_pk_add_f32 v[82:83], v[82:83], v[90:91]\n"
      "v_cvt_pk_bf16_f32 v80, 0, v80\n"
      "v_cvt_pk_bf16_f32 v81, 0, v81\n"
      "v_cvt_pk_bf16_f32 v82, 0, v82\n"
      "v_cvt_pk_bf16_f32 v83, 0, v83\n"
      "v_pk_mul_f32 v[80:81], v[80:81], v[28:29]\n"
      "v_pk_mul_f32 v[82:83], v[82:83], v[30:31]\n"
      "v_cvt_pk_bf16_f32 v80, 0, v80\n"
      "v_cvt_pk_bf16_f32 v81, 0, v81\n"
      "v_cvt_pk_bf16_f32 v82, 0, v82\n"
      "v_cvt_pk_bf16_f32 v83, 0, v83\n"
      "v_pk_add_f32 v[80:81], v[80:81], v[48:49]\n"
      "v_pk_add_f32 v[82:83], v[82:83], v[50:51]\n"
      "v_cvt_pk_bf16_f32 v80, v80, v81\n"
      "v_cvt_pk_bf16_f32 v81, v82, v83\n"
      "global_store_dwordx2 v3, v[80:81], s[28:29] offset:32\n"
      "s_waitcnt lgkmcnt(8)\n"
      "v_pk_add_f32 v[96:97], v[96:97], v[100:101]\n"
      "v_pk_add_f32 v[98:99], v[98:99], v[102:103]\n"
      "v_pk_add_f32 v[104:105], v[104:105], v[108:109]\n"
      "v_pk_add_f32 v[106:107], v[106:107], v[110:111]\n"
      "v_pk_add_f32 v[96:97], v[96:97], v[104:105]\n"
      "v_pk_add_f32 v[98:99], v[98:99], v[106:107]\n"
      "v_cvt_pk_bf16_f32 v96, 0, v96\n"
      "v_cvt_pk_bf16_f32 v97, 0, v97\n"
      "v_cvt_pk_bf16_f32 v98, 0, v98\n"
      "v_cvt_pk_bf16_f32 v99, 0, v99\n"
      "v_pk_mul_f32 v[96:97], v[96:97], v[32:33]\n"
      "v_pk_mul_f32 v[98:99], v[98:99], v[34:35]\n"
      "v_cvt_pk_bf16_f32 v96, 0, v96\n"
      "v_cvt_pk_bf16_f32 v97, 0, v97\n"
      "v_cvt_pk_bf16_f32 v98, 0, v98\n"
      "v_cvt_pk_bf16_f32 v99, 0, v99\n"
      "v_pk_add_f32 v[96:97], v[96:97], v[52:53]\n"
      "v_pk_add_f32 v[98:99], v[98:99], v[54:55]\n"
      "v_cvt_pk_bf16_f32 v96, v96, v97\n"
      "v_cvt_pk_bf16_f32 v97, v98, v99\n"
      "global_store_dwordx2 v3, v[96:97], s[28:29] offset:64\n"
      "s_waitcnt lgkmcnt(4)\n"
      "v_pk_add_f32 v[112:113], v[112:113], v[116:117]\n"
      "v_pk_add_f32 v[114:115], v[114:115], v[118:119]\n"
      "v_pk_add_f32 v[120:121], v[120:121], v[124:125]\n"
      "v_pk_add_f32 v[122:123], v[122:123], v[126:127]\n"
      "v_pk_add_f32 v[112:113], v[112:113], v[120:121]\n"
      "v_pk_add_f32 v[114:115], v[114:115], v[122:123]\n"
      "v_cvt_pk_bf16_f32 v112, 0, v112\n"
      "v_cvt_pk_bf16_f32 v113, 0, v113\n"
      "v_cvt_pk_bf16_f32 v114, 0, v114\n"
      "v_cvt_pk_bf16_f32 v115, 0, v115\n"
      "v_pk_mul_f32 v[112:113], v[112:113], v[36:37]\n"
      "v_pk_mul_f32 v[114:115], v[114:115], v[38:39]\n"
      "v_cvt_pk_bf16_f32 v112, 0, v112\n"
      "v_cvt_pk_bf16_f32 v113, 0, v113\n"
      "v_cvt_pk_bf16_f32 v114, 0, v114\n"
      "v_cvt_pk_bf16_f32 v115, 0, v115\n"
      "v_pk_add_f32 v[112:113], v[112:113], v[56:57]\n"
      "v_pk_add_f32 v[114:115], v[114:115], v[58:59]\n"
      "v_cvt_pk_bf16_f32 v112, v112, v113\n"
      "v_cvt_pk_bf16_f32 v113, v114, v115\n"
      "global_store_dwordx2 v3, v[112:113], s[28:29] offset:96\n"
      "s_waitcnt lgkmcnt(0)\n"
      "v_pk_add_f32 v[128:129], v[128:129], v[132:133]\n"
      "v_pk_add_f32 v[130:131], v[130:131], v[134:135]\n"
      "v_pk_add_f32 v[136:137], v[136:137], v[140:141]\n"
      "v_pk_add_f32 v[138:139], v[138:139], v[142:143]\n"
      "v_pk_add_f32 v[128:129], v[128:129], v[136:137]\n"
      "v_pk_add_f32 v[130:131], v[130:131], v[138:139]\n"
      "v_cvt_pk_bf16_f32 v128, 0, v128\n"
      "v_cvt_pk_bf16_f32 v129, 0, v129\n"
      "v_cvt_pk_bf16_f32 v130, 0, v130\n"
      "v_cvt_pk_bf16_f32 v131, 0, v131\n"
      "v_pk_mul_f32 v[128:129], v[128:129], v[40:41]\n"
      "v_pk_mul_f32 v[130:131], v[130:131], v[42:43]\n"
      "v_cvt_pk_bf16_f32 v128, 0, v128\n"
      "v_cvt_pk_bf16_f32 v129, 0, v129\n"
      "v_cvt_pk_bf16_f32 v130, 0, v130\n"
      "v_cvt_pk_bf16_f32 v131, 0, v131\n"
      "v_pk_add_f32 v[128:129], v[128:129], v[60:61]\n"
      "v_pk_add_f32 v[130:131], v[130:131], v[62:63]\n"
      "v_cvt_pk_bf16_f32 v128, v128, v129\n"
      "v_cvt_pk_bf16_f32 v129, v130, v131\n"
      "global_store_dwordx2 v3, v[128:129], s[28:29] offset:128\n"
      "s_or_b32 s40, s2, s60\n"
      "s_cmp_lg_u32 s40, 0\n"
      "s_cbranch_scc1 L_fin%=\n"
      "v_mov_b32 v8, s30\n"
      "v_mov_b32 v9, s31\n"
      "v_mov_b32 v10, 0\n"
      "s_mov_b64 exec, 1\n"
      "global_store_dwordx2 v10, v[8:9], s[26:27] sc1\n"
      "s_mov_b64 exec, -1\n"
      "L_fin%=:\n"
      "s_endpgm\n"
      "L_psm_%=:\n"
      "buffer_load_dwordx2 v[244:245], v4, s[32:35], s68 offen sc1\n"
      "s_waitcnt vmcnt(0)\n"
      "v_cmp_ne_u32 vcc, s30, v245\n"
      "s_sub_u32 s44, s44, 1\n"
      "s_cbranch_vccz L_pbm_%=\n"
      "s_cmp_lg_u32 s44, 0\n"
      "s_cbranch_scc1 L_psm_%=\n"
      "s_branch L_pbm_%=\n"
      "L_psx_%=:\n"
      "s_add_u32 s42, s92, 0x100000\n"
      "s_add_u32 s43, s93, 0x100000\n"
      "buffer_load_dwordx4 v[64:67], v54, s[32:35], s42 offen sc1\n"
      "s_add_u32 s42, s42, 0x100000\n"
      "buffer_load_dwordx4 v[72:75], v54, s[32:35], s43 offen sc1\n"
      "buffer_load_dwordx4 v[68:71], v54, s[32:35], s42 offen sc1\n"
      "buffer_load_dword v76, v214, s[32:35], s70 offen sc1\n"
      "s_add_u32 s42, s70, 4096\n"
      "buffer_load_dword v77, v214, s[32:35], s42 offen sc1\n"
      "buffer_load_dword v78, v214, s[32:35], s71 offen sc1\n"
      "s_waitcnt vmcnt(0)\n"
      "v_add3_u32 v79, v64, v65, v66\n"
      "v_add3_u32 v79, v79, v67, v68\n"
      "v_add3_u32 v79, v79, v69, v70\n"
      "v_add3_u32 v79, v79, v71, v72\n"
      "v_add3_u32 v79, v79, v73, v74\n"
      "v_add_u32 v79, v79, v75\n"
      "v_add3_u32 v76, v76, v77, v78\n"
      "s_mul_i32 s47, s30, 3\n"
      "v_add_u32_dpp v79, v79, v79 quad_perm:[1,0,3,2] row_mask:0xf bank_mask:0xf\n"
      "s_nop 1\n"
      "v_add_u32_dpp v79, v79, v79 quad_perm:[2,3,0,1] row_mask:0xf bank_mask:0xf\n"
      "s_nop 1\n"
      "v_add_u32_dpp v79, v79, v79 row_half_mirror row_mask:0xf bank_mask:0xf\n"
      "v_add_u32 v79, v79, v76\n"
      "v_cmp_ne_u32 vcc, s47, v79\n"
      "s_sub_u32 s44, s44, 1\n"
      "s_cbranch_vccz L_pbx_%=\n"
      "s_cmp_lg_u32 s44, 0\n"
      "s_cbranch_scc1 L_psx_%=\n"
      "s_branch L_pbx_%=\n"
      "L_cv_s%=:\n"
      "buffer_load_dwordx2 v[200:201], v4, s[32:35], s80 offen offset:0 sc1\n"
      "buffer_load_dword v250, v61, s[32:35], s85 offen offset:0 sc1\n"
      "buffer_load_dwordx4 v[204:207], v58, s[32:35], s83 offen offset:0 sc1\n"
      "buffer_load_dwordx4 v[36:39], v58, s[32:35], s83 offen offset:16 sc1\n"
      "buffer_load_dwordx4 v[224:227], v57, s[32:35], s82 offen offset:0 sc1\n"
      "buffer_load_dwordx4 v[228:231], v57, s[32:35], s82 offen offset:1024 sc1\n"
      "buffer_load_dwordx4 v[192:195], v54, s[32:35], s53 offen offset:0 sc1\n"
      "buffer_load_dwordx4 v[196:199], v54, s[32:35], s54 offen offset:0 sc1\n"
      "buffer_load_dword v248, v60, s[32:35], s84 offen offset:0 sc1\n"
      "buffer_load_dword v249, v60, s[32:35], s55 offen offset:0 sc1\n"
      "buffer_load_dwordx4 v[208:211], v55, s[32:35], 0 offen offset:0 sc1\n"
      "buffer_load_dwordx4 v[212:215], v55, s[32:35], 0 offen offset:1024 sc1\n"
      "buffer_load_dwordx4 v[216:219], v55, s[32:35], 0 offen offset:2048 sc1\n"
      "buffer_load_dwordx4 v[220:223], v55, s[32:35], 0 offen offset:3072 sc1\n"
      "s_waitcnt vmcnt(0)\n"
      "v_add_u32 v251, v200, v201\n"
      "v_add3_u32 v34, v192, v193, v194\n"
      "v_add3_u32 v34, v34, v195, v196\n"
      "v_add_u32_dpp v251, v251, v251 row_half_mirror row_mask:0xf bank_mask:0xf\n"
      "v_add3_u32 v34, v34, v197, v198\n"
      "v_add3_u32 v251, v251, v34, v199\n"
      "v_add3_u32 v34, v248, v249, v250\n"
      "v_add3_u32 v34, v34, v205, v207\n"
      "v_add_u32_dpp v251, v251, v251 quad_perm:[1,0,3,2] row_mask:0xf bank_mask:0xf\n"
      "v_add3_u32 v34, v34, v37, v39\n"
      "v_add3_u32 v34, v34, v211, v215\n"
      "v_add_u32_dpp v251, v251, v251 quad_perm:[2,3,0,1] row_mask:0xf bank_mask:0xf\n"
      "v_add3_u32 v34, v34, v219, v223\n"
      "v_add3_u32 v34, v34, v227, v231\n"
      "v_add_u32 v251, v251, v34\n"
      "v_cmp_ne_u32 vcc, s24, v251\n"
      "s_sub_u32 s44, s44, 1\n"
      "s_cbranch_vccz L_cv_r%=\n"
      "s_cmp_lg_u32 s44, 0\n"
      "s_cbranch_scc1 L_cv_s%=\n"
      "s_branch L_cv_r%=\n"
      "L_es%=:\n"
      "buffer_load_dwordx4 v[240:243], v48, s[32:35], s76 offen sc1\n"
      "s_waitcnt vmcnt(0)\n"
      "v_add3_u32 v240, v240, v241, v242\n"
      "s_add_u32 s47, s30, s31\n"
      "v_add_u32 v240, v240, v243\n"
      "s_lshl_b32 s47, s47, 1\n"
      "v_cmp_ne_u32 vcc, s47, v240\n"
      "s_sub_u32 s44, s44, 1\n"
      "s_cbranch_vccz L_eb%=\n"
      "s_cmp_lg_u32 s44, 0\n"
      "s_cbranch_scc1 L_es%=\n"
      "s_branch L_eb%=\n"
      "L_sx1_%=:\n"
      "s_mov_b32 s45, m0\n"
      "s_mov_b32 s58, s47\n"
      "s_add_u32 m0, s73, 16384\n"
      "v_add_u32 v248, 81920, v40\n"
      "global_load_lds_dwordx4 v248, s[4:5] offset:0 sc1\n"
      "global_load_lds_dwordx4 v248, s[4:5] offset:1024 sc1\n"
      "global_load_lds_dwordx4 v248, s[4:5] offset:2048 sc1\n"
      "global_load_lds_dwordx4 v248, s[4:5] offset:3072 sc1\n"
      "s_mov_b32 s47, 32\n"
      "buffer_load_dword v4, v5, s[32:35], s47 offen sc1\n"
      "s_waitcnt vmcnt(0)\n"
      "s_mov_b32 m0, s45\n"
      "s_mov_b32 s47, s58\n"
      "ds_read_b128 v[248:251], v40 offset:16384\n"
      "ds_read_b128 v[252:255], v40 offset:17408\n"
      "s_waitcnt lgkmcnt(0)\n"
      "v_add3_u32 v4, v4, v248, v249\n"
      "v_add3_u32 v4, v4, v250, v251\n"
      "v_add3_u32 v4, v4, v252, v253\n"
      "v_add3_u32 v4, v4, v254, v255\n"
      "ds_read_b128 v[248:251], v40 offset:18432\n"
      "ds_read_b128 v[252:255], v40 offset:19456\n"
      "s_waitcnt lgkmcnt(0)\n"
      "v_add3_u32 v4, v4, v248, v249\n"
      "v_add3_u32 v4, v4, v250, v251\n"
      "v_add3_u32 v4, v4, v252, v253\n"
      "v_add3_u32 v4, v4, v254, v255\n"
      "s_nop 1\n"
      "v_add_u32_dpp v4, v4, v4 quad_perm:[1,0,3,2] row_mask:0xf bank_mask:0xf\n"
      "s_nop 1\n"
      "v_add_u32_dpp v4, v4, v4 quad_perm:[2,3,0,1] row_mask:0xf bank_mask:0xf\n"
      "s_nop 1\n"
      "v_add_u32_dpp v4, v4, v4 row_half_mirror row_mask:0xf bank_mask:0xf\n"
      "v_cmp_ne_u32 vcc, s71, v4\n"
      "s_sub_u32 s44, s44, 1\n"
      "s_cbranch_vccz L_bx1_%=\n"
      "s_cmp_lg_u32 s44, 0\n"
      "s_cbranch_scc1 L_sx1_%=\n"
      "s_branch L_bx1_%=\n"
      "L_sx2_%=:\n"
      "s_mov_b32 s45, m0\n"
      "s_mov_b32 s58, s47\n"
      "s_add_u32 m0, s73, 32768\n"
      "v_add_u32 v248, 16384, v40\n"
      "global_load_lds_dwordx4 v248, s[4:5] offset:0 sc1\n"
      "global_load_lds_dwordx4 v248, s[4:5] offset:1024 sc1\n"
      "global_load_lds_dwordx4 v248, s[4:5] offset:2048 sc1\n"
      "global_load_lds_dwordx4 v248, s[4:5] offset:3072 sc1\n"
      "s_mov_b32 s47, 16384\n"
      "buffer_load_dword v55, v5, s[32:35], s47 offen sc1\n"
      "s_waitcnt vmcnt(0)\n"
      "s_mov_b32 m0, s45\n"
      "s_mov_b32 s47, s58\n"
      "ds_read_b128 v[248:251], v40 offset:32768\n"
      "ds_read_b128 v[252:255], v40 offset:33792\n"
      "s_waitcnt lgkmcnt(0)\n"
      "v_add3_u32 v55, v55, v248, v249\n"
      "v_add3_u32 v55, v55, v250, v251\n"
      "v_add3_u32 v55, v55, v252, v253\n"
      "v_add3_u32 v55, v55, v254, v255\n"
      "ds_read_b128 v[248:251], v40 offset:34816\n"
      "ds_read_b128 v[252:255], v40 offset:35840\n"
      "s_waitcnt lgkmcnt(0)\n"
      "v_add3_u32 v55, v55, v248, v249\n"
      "v_add3_u32 v55, v55, v250, v251\n"
      "v_add3_u32 v55, v55, v252, v253\n"
      "v_add3_u32 v55, v55, v254, v255\n"
      "s_nop 1\n"
      "v_add_u32_dpp v55, v55, v55 quad_perm:[1,0,3,2] row_mask:0xf bank_mask:0xf\n"
      "s_nop 1\n"
      "v_add_u32_dpp v55, v55, v55 quad_perm:[2,3,0,1] row_mask:0xf bank_mask:0xf\n"
      "s_nop 1\n"
      "v_add_u32_dpp v55, v55, v55 row_half_mirror row_mask:0xf bank_mask:0xf\n"
      "v_cmp_ne_u32 vcc, s71, v55\n"
      "s_sub_u32 s44, s44, 1\n"
      "s_cbranch_vccz L_bx2_%=\n"
      "s_cmp_lg_u32 s44, 0\n"
      "s_cbranch_scc1 L_sx2_%=\n"
      "s_branch L_bx2_%=\n"
      "L_sx3_%=:\n"
      "s_mov_b32 s45, m0\n"
      "s_mov_b32 s58, s47\n"
      "s_add_u32 m0, s73, 49152\n"
      "v_add_u32 v248, 32768, v40\n"
      "global_load_lds_dwordx4 v248, s[4:5] offset:0 sc1\n"
      "global_load_lds_dwordx4 v248, s[4:5] offset:1024 sc1\n"
      "global_load_lds_dwordx4 v248, s[4:5] offset:2048 sc1\n"
      "global_load_lds_dwordx4 v248, s[4:5] offset:3072 sc1\n"
      "s_mov_b32 s47, 32768\n"
      "buffer_load_dword v56, v5, s[32:35], s47 offen sc1\n"
      "s_waitcnt vmcnt(0)\n"
      "s_mov_b32 m0, s45\n"
      "s_mov_b32 s47, s58\n"
      "ds_read_b128 v[248:251], v40 offset:49152\n"
      "ds_read_b128 v[252:255], v40 offset:50176\n"
      "s_waitcnt lgkmcnt(0)\n"
      "v_add3_u32 v56, v56, v248, v249\n"
      "v_add3_u32 v56, v56, v250, v251\n"
      "v_add3_u32 v56, v56, v252, v253\n"
      "v_add3_u32 v56, v56, v254, v255\n"
      "ds_read_b128 v[248:251], v40 offset:51200\n"
      "ds_read_b128 v[252:255], v40 offset:52224\n"
      "s_waitcnt lgkmcnt(0)\n"
      "v_add3_u32 v56, v56, v248, v249\n"
      "v_add3_u32 v56, v56, v250, v251\n"
      "v_add3_u32 v56, v56, v252, v253\n"
      "v_add3_u32 v56, v56, v254, v255\n"
      "s_nop 1\n"
      "v_add_u32_dpp v56, v56, v56 quad_perm:[1,0,3,2] row_mask:0xf bank_mask:0xf\n"
      "s_nop 1\n"
      "v_add_u32_dpp v56, v56, v56 quad_perm:[2,3,0,1] row_mask:0xf bank_mask:0xf\n"
      "s_nop 1\n"
      "v_add_u32_dpp v56, v56, v56 row_half_mirror row_mask:0xf bank_mask:0xf\n"
      "v_cmp_ne_u32 vcc, s71, v56\n"
      "s_sub_u32 s44, s44, 1\n"
      "s_cbranch_vccz L_bx3_%=\n"
      "s_cmp_lg_u32 s44, 0\n"
      "s_cbranch_scc1 L_sx3_%=\n"
      "s_branch L_bx3_%=\n"
      "L_sx4_%=:\n"
      "s_mov_b32 s45, m0\n"
      "s_mov_b32 s58, s47\n"
      "s_add_u32 m0, s73, 0\n"
      "v_add_u32 v248, 49152, v40\n"
      "global_load_lds_dwordx4 v248, s[4:5] offset:0 sc1\n"
      "global_load_lds_dwordx4 v248, s[4:5] offset:1024 sc1\n"
      "global_load_lds_dwordx4 v248, s[4:5] offset:2048 sc1\n"
      "global_load_lds_dwordx4 v248, s[4:5] offset:3072 sc1\n"
      "s_mov_b32 s47, 49152\n"
      "buffer_load_dword v32, v5, s[32:35], s47 offen sc1\n"
      "s_waitcnt vmcnt(0)\n"
      "s_mov_b32 m0, s45\n"
      "s_mov_b32 s47, s58\n"
      "ds_read_b128 v[248:251], v40 offset:0\n"
      "ds_read_b128 v[252:255], v40 offset:1024\n"
      "s_waitcnt lgkmcnt(0)\n"
      "v_add3_u32 v32, v32, v248, v249\n"
      "v_add3_u32 v32, v32, v250, v251\n"
      "v_add3_u32 v32, v32, v252, v253\n"
      "v_add3_u32 v32, v32, v254, v255\n"
      "ds_read_b128 v[248:251], v40 offset:2048\n"
      "ds_read_b128 v[252:255], v40 offset:3072\n"
      "s_waitcnt lgkmcnt(0)\n"
      "v_add3_u32 v32, v32, v248, v249\n"
      "v_add3_u32 v32, v32, v250, v251\n"
      "v_add3_u32 v32, v32, v252, v253\n"
      "v_add3_u32 v32, v32, v254, v255\n"
      "s_nop 1\n"
      "v_add_u32_dpp v32, v32, v32 quad_perm:[1,0,3,2] row_mask:0xf bank_mask:0xf\n"
      "s_nop 1\n"
      "v_add_u32_dpp v32, v32, v32 quad_perm:[2,3,0,1] row_mask:0xf bank_mask:0xf\n"
      "s_nop 1\n"
      "v_add_u32_dpp v32, v32, v32 row_half_mirror row_mask:0xf bank_mask:0xf\n"
      "v_cmp_ne_u32 vcc, s71, v32\n"
      "s_sub_u32 s44, s44, 1\n"
      "s_cbranch_vccz L_bx4_%=\n"
      "s_cmp_lg_u32 s44, 0\n"
      "s_cbranch_scc1 L_sx4_%=\n"
      "s_branch L_bx4_%=\n"
      "L_sx5_%=:\n"
      "s_mov_b32 s45, m0\n"
      "s_mov_b32 s58, s47\n"
      "s_add_u32 m0, s73, 16384\n"
      "v_add_u32 v248, 65536, v40\n"
      "global_load_lds_dwordx4 v248, s[4:5] offset:0 sc1\n"
      "global_load_lds_dwordx4 v248, s[4:5] offset:1024 sc1\n"
      "global_load_lds_dwordx4 v248, s[4:5] offset:2048 sc1\n"
      "global_load_lds_dwordx4 v248, s[4:5] offset:3072 sc1\n"
      "s_mov_b32 s47, 65536\n"
      "buffer_load_dword v4, v5, s[32:35], s47 offen sc1\n"
      "s_waitcnt vmcnt(0)\n"
      "s_mov_b32 m0, s45\n"
      "s_mov_b32 s47, s58\n"
      "ds_read_b128 v[248:251], v40 offset:16384\n"
      "ds_read_b128 v[252:255], v40 offset:17408\n"
      "s_waitcnt lgkmcnt(0)\n"
      "v_add3_u32 v4, v4, v248, v249\n"
      "v_add3_u32 v4, v4, v250, v251\n"
      "v_add3_u32 v4, v4, v252, v253\n"
      "v_add3_u32 v4, v4, v254, v255\n"
      "ds_read_b128 v[248:251], v40 offset:18432\n"
      "ds_read_b128 v[252:255], v40 offset:19456\n"
      "s_waitcnt lgkmcnt(0)\n"
      "v_add3_u32 v4, v4, v248, v249\n"
      "v_add3_u32 v4, v4, v250, v251\n"
      "v_add3_u32 v4, v4, v252, v253\n"
      "v_add3_u32 v4, v4, v254, v255\n"
      "s_nop 1\n"
      "v_add_u32_dpp v4, v4, v4 quad_perm:[1,0,3,2] row_mask:0xf bank_mask:0xf\n"
      "s_nop 1\n"
      "v_add_u32_dpp v4, v4, v4 quad_perm:[2,3,0,1] row_mask:0xf bank_mask:0xf\n"
      "s_nop 1\n"
      "v_add_u32_dpp v4, v4, v4 row_half_mirror row_mask:0xf bank_mask:0xf\n"
      "v_cmp_ne_u32 vcc, s71, v4\n"
      "s_sub_u32 s44, s44, 1\n"
      "s_cbranch_vccz L_bx5_%=\n"
      "s_cmp_lg_u32 s44, 0\n"
      "s_cbranch_scc1 L_sx5_%=\n"
      "s_branch L_bx5_%=\n"
      "L_sx6_%=:\n"
      "s_mov_b32 s45, m0\n"
      "s_mov_b32 s58, s47\n"
      "s_add_u32 m0, s73, 32768\n"
      "v_add_u32 v248, 98304, v40\n"
      "global_load_lds_dwordx4 v248, s[4:5] offset:0 sc1\n"
      "global_load_lds_dwordx4 v248, s[4:5] offset:1024 sc1\n"
      "global_load_lds_dwordx4 v248, s[4:5] offset:2048 sc1\n"
      "global_load_lds_dwordx4 v248, s[4:5] offset:3072 sc1\n"
      "s_mov_b32 s47, 16416\n"
      "buffer_load_dword v55, v5, s[32:35], s47 offen sc1\n"
      "s_waitcnt vmcnt(0)\n"
      "s_mov_b32 m0, s45\n"
      "s_mov_b32 s47, s58\n"
      "ds_read_b128 v[248:251], v40 offset:32768\n"
      "ds_read_b128 v[252:255], v40 offset:33792\n"
      "s_waitcnt lgkmcnt(0)\n"
      "v_add3_u32 v55, v55, v248, v249\n"
      "v_add3_u32 v55, v55, v250, v251\n"
      "v_add3_u32 v55, v55, v252, v253\n"
      "v_add3_u32 v55, v55, v254, v255\n"
      "ds_read_b128 v[248:251], v40 offset:34816\n"
      "ds_read_b128 v[252:255], v40 offset:35840\n"
      "s_waitcnt lgkmcnt(0)\n"
      "v_add3_u32 v55, v55, v248, v249\n"
      "v_add3_u32 v55, v55, v250, v251\n"
      "v_add3_u32 v55, v55, v252, v253\n"
      "v_add3_u32 v55, v55, v254, v255\n"
      "s_nop 1\n"
      "v_add_u32_dpp v55, v55, v55 quad_perm:[1,0,3,2] row_mask:0xf bank_mask:0xf\n"
      "s_nop 1\n"
      "v_add_u32_dpp v55, v55, v55 quad_perm:[2,3,0,1] row_mask:0xf bank_mask:0xf\n"
      "s_nop 1\n"
      "v_add_u32_dpp v55, v55, v55 row_half_mirror row_mask:0xf bank_mask:0xf\n"
      "v_cmp_ne_u32 vcc, s71, v55\n"
      "s_sub_u32 s44, s44, 1\n"
      "s_cbranch_vccz L_bx6_%=\n"
      "s_cmp_lg_u32 s44, 0\n"
      "s_cbranch_scc1 L_sx6_%=\n"
      "s_branch L_bx6_%=\n"
      "L_sx7_%=:\n"
      "s_mov_b32 s45, m0\n"
      "s_mov_b32 s58, s47\n"
      "s_add_u32 m0, s73, 49152\n"
      "v_add_u32 v248, 114688, v40\n"
      "global_load_lds_dwordx4 v248, s[4:5] offset:0 sc1\n"
      "global_load_lds_dwordx4 v248, s[4:5] offset:1024 sc1\n"
      "global_load_lds_dwordx4 v248, s[4:5] offset:2048 sc1\n"
      "global_load_lds_dwordx4 v248, s[4:5] offset:3072 sc1\n"
      "s_mov_b32 s47, 32800\n"
      "buffer_load_dword v56, v5, s[32:35], s47 offen sc1\n"
      "s_waitcnt vmcnt(0)\n"
      "s_mov_b32 m0, s45\n"
      "s_mov_b32 s47, s58\n"
      "ds_read_b128 v[248:251], v40 offset:49152\n"
      "ds_read_b128 v[252:255], v40 offset:50176\n"
      "s_waitcnt lgkmcnt(0)\n"
      "v_add3_u32 v56, v56, v248, v249\n"
      "v_add3_u32 v56, v56, v250, v251\n"
      "v_add3_u32 v56, v56, v252, v253\n"
      "v_add3_u32 v56, v56, v254, v255\n"
      "ds_read_b128 v[248:251], v40 offset:51200\n"
      "ds_read_b128 v[252:255], v40 offset:52224\n"
      "s_waitcnt lgkmcnt(0)\n"
      "v_add3_u32 v56, v56, v248, v249\n"
      "v_add3_u32 v56, v56, v250, v251\n"
      "v_add3_u32 v56, v56, v252, v253\n"
      "v_add3_u32 v56, v56, v254, v255\n"
      "s_nop 1\n"
      "v_add_u32_dpp v56, v56, v56 quad_perm:[1,0,3,2] row_mask:0xf bank_mask:0xf\n"
      "s_nop 1\n"
      "v_add_u32_dpp v56, v56, v56 quad_perm:[2,3,0,1] row_mask:0xf bank_mask:0xf\n"
      "s_nop 1\n"
      "v_add_u32_dpp v56, v56, v56 row_half_mirror row_mask:0xf bank_mask:0xf\n"
      "v_cmp_ne_u32 vcc, s71, v56\n"
      "s_sub_u32 s44, s44, 1\n"
      "s_cbranch_vccz L_bx7_%=\n"
      "s_cmp_lg_u32 s44, 0\n"
      "s_cbranch_scc1 L_sx7_%=\n"
      "s_branch L_bx7_%=\n"
      "L_sx8_%=:\n"
      "s_mov_b32 s45, m0\n"
      "s_mov_b32 s58, s47\n"
      "s_add_u32 m0, s73, 0\n"
      "v_add_u32 v248, 131072, v40\n"
      "global_load_lds_dwordx4 v248, s[4:5] offset:0 sc1\n"
      "global_load_lds_dwordx4 v248, s[4:5] offset:1024 sc1\n"
      "global_load_lds_dwordx4 v248, s[4:5] offset:2048 sc1\n"
      "global_load_lds_dwordx4 v248, s[4:5] offset:3072 sc1\n"
      "s_mov_b32 s47, 49184\n"
      "buffer_load_dword v32, v5, s[32:35], s47 offen sc1\n"
      "s_waitcnt vmcnt(0)\n"
      "s_mov_b32 m0, s45\n"
      "s_mov_b32 s47, s58\n"
      "ds_read_b128 v[248:251], v40 offset:0\n"
      "ds_read_b128 v[252:255], v40 offset:1024\n"
      "s_waitcnt lgkmcnt(0)\n"
      "v_add3_u32 v32, v32, v248, v249\n"
      "v_add3_u32 v32, v32, v250, v251\n"
      "v_add3_u32 v32, v32, v252, v253\n"
      "v_add3_u32 v32, v32, v254, v255\n"
      "ds_read_b128 v[248:251], v40 offset:2048\n"
      "ds_read_b128 v[252:255], v40 offset:3072\n"
      "s_waitcnt lgkmcnt(0)\n"
      "v_add3_u32 v32, v32, v248, v249\n"
      "v_add3_u32 v32, v32, v250, v251\n"
      "v_add3_u32 v32, v32, v252, v253\n"
      "v_add3_u32 v32, v32, v254, v255\n"
      "s_nop 1\n"
      "v_add_u32_dpp v32, v32, v32 quad_perm:[1,0,3,2] row_mask:0xf bank_mask:0xf\n"
      "s_nop 1\n"
      "v_add_u32_dpp v32, v32, v32 quad_perm:[2,3,0,1] row_mask:0xf bank_mask:0xf\n"
      "s_nop 1\n"
      "v_add_u32_dpp v32, v32, v32 row_half_mirror row_mask:0xf bank_mask:0xf\n"
      "v_cmp_ne_u32 vcc, s71, v32\n"
      "s_sub_u32 s44, s44, 1\n"
      "s_cbranch_vccz L_bx8_%=\n"
      "s_cmp_lg_u32 s44, 0\n"
      "s_cbranch_scc1 L_sx8_%=\n"
      "s_branch L_bx8_%=\n"
      "L_sx9_%=:\n"
      "s_mov_b32 s45, m0\n"
      "s_mov_b32 s58, s47\n"
      "s_add_u32 m0, s73, 16384\n"
      "v_add_u32 v248, 147456, v40\n"
      "global_load_lds_dwordx4 v248, s[4:5] offset:0 sc1\n"
      "global_load_lds_dwordx4 v248, s[4:5] offset:1024 sc1\n"
      "global_load_lds_dwordx4 v248, s[4:5] offset:2048 sc1\n"
      "global_load_lds_dwordx4 v248, s[4:5] offset:3072 sc1\n"
      "s_mov_b32 s47, 65568\n"
      "buffer_load_dword v4, v5, s[32:35], s47 offen sc1\n"
      "s_waitcnt vmcnt(0)\n"
      "s_mov_b32 m0, s45\n"
      "s_mov_b32 s47, s58\n"
      "ds_read_b128 v[248:251], v40 offset:16384\n"
      "ds_read_b128 v[252:255], v40 offset:17408\n"
      "s_waitcnt lgkmcnt(0)\n"
      "v_add3_u32 v4, v4, v248, v249\n"
      "v_add3_u32 v4, v4, v250, v251\n"
      "v_add3_u32 v4, v4, v252, v253\n"
      "v_add3_u32 v4, v4, v254, v255\n"
      "ds_read_b128 v[248:251], v40 offset:18432\n"
      "ds_read_b128 v[252:255], v40 offset:19456\n"
      "s_waitcnt lgkmcnt(0)\n"
      "v_add3_u32 v4, v4, v248, v249\n"
      "v_add3_u32 v4, v4, v250, v251\n"
      "v_add3_u32 v4, v4, v252, v253\n"
      "v_add3_u32 v4, v4, v254, v255\n"
      "s_nop 1\n"
      "v_add_u32_dpp v4, v4, v4 quad_perm:[1,0,3,2] row_mask:0xf bank_mask:0xf\n"
      "s_nop 1\n"
      "v_add_u32_dpp v4, v4, v4 quad_perm:[2,3,0,1] row_mask:0xf bank_mask:0xf\n"
      "s_nop 1\n"
      "v_add_u32_dpp v4, v4, v4 row_half_mirror row_mask:0xf bank_mask:0xf\n"
      "v_cmp_ne_u32 vcc, s71, v4\n"
      "s_sub_u32 s44, s44, 1\n"
      "s_cbranch_vccz L_bx9_%=\n"
      "s_cmp_lg_u32 s44, 0\n"
      "s_cbranch_scc1 L_sx9_%=\n"
      "s_branch L_bx9_%=\n"
      "L_sx0_%=:\n"
      "s_mov_b32 s45, m0\n"
      "s_mov_b32 s58, s47\n"
      "s_add_u32 m0, s73, 0\n"
      "v_add_u32 v248, 0, v40\n"
      "global_load_lds_dwordx4 v248, s[4:5] offset:0 sc1\n"
      "global_load_lds_dwordx4 v248, s[4:5] offset:1024 sc1\n"
      "global_load_lds_dwordx4 v248, s[4:5] offset:2048 sc1\n"
      "global_load_lds_dwordx4 v248, s[4:5] offset:3072 sc1\n"
      "s_mov_b32 s47, 0\n"
      "buffer_load_dword v32, v5, s[32:35], s47 offen sc1\n"
      "s_waitcnt vmcnt(0)\n"
      "s_mov_b32 m0, s45\n"
      "s_mov_b32 s47, s58\n"
      "ds_read_b128 v[248:251], v40 offset:0\n"
      "ds_read_b128 v[252:255], v40 offset:1024\n"
      "s_waitcnt lgkmcnt(0)\n"
      "v_add3_u32 v32, v32, v248, v249\n"
      "v_add3_u32 v32, v32, v250, v251\n"
      "v_add3_u32 v32, v32, v252, v253\n"
      "v_add3_u32 v32, v32, v254, v255\n"
      "ds_read_b128 v[248:251], v40 offset:2048\n"
      "ds_read_b128 v[252:255], v40 offset:3072\n"
      "s_waitcnt lgkmcnt(0)\n"
      "v_add3_u32 v32, v32, v248, v249\n"
      "v_add3_u32 v32, v32, v250, v251\n"
      "v_add3_u32 v32, v32, v252, v253\n"
      "v_add3_u32 v32, v32, v254, v255\n"
      "s_nop 1\n"
      "v_add_u32_dpp v32, v32, v32 quad_perm:[1,0,3,2] row_mask:0xf bank_mask:0xf\n"
      "s_nop 1\n"
      "v_add_u32_dpp v32, v32, v32 quad_perm:[2,3,0,1] row_mask:0xf bank_mask:0xf\n"
      "s_nop 1\n"
      "v_add_u32_dpp v32, v32, v32 row_half_mirror row_mask:0xf bank_mask:0xf\n"
      "v_cmp_ne_u32 vcc, s71, v32\n"
      "s_sub_u32 s44, s44, 1\n"
      "s_cbranch_vccz L_bx0_%=\n"
      "s_cmp_lg_u32 s44, 0\n"
      "s_cbranch_scc1 L_sx0_%=\n"
      "s_branch L_bx0_%=\n"
      "L_sfd_%=:\n"
      "global_load_dwordx2 v[58:59], v54, s[26:27] sc1\n"
      "s_waitcnt vmcnt(0)\n"
      "v_cmp_ne_u64 vcc, s[30:31], v[58:59]\n"
      "s_sub_u32 s44, s44, 1\n"
      "s_cbranch_vccz L_bfd_%=\n"
      "s_cmp_lg_u32 s44, 0\n"
      "s_cbranch_scc1 L_sfd_%=\n"
      "s_branch L_bfd_%=\n"
      "L_shA0_%=:\n"
      "v_add_u32 v2, 4096, v7\n"
      "global_load_dwordx4 v[16:19], v2, s[10:11] offset:-4096 sc1\n"
      "global_load_dwordx4 v[8:11], v2, s[10:11] offset:-3072 sc1\n"
      "global_load_dwordx4 v[160:163], v2, s[10:11] offset:-2048 sc1\n"
      "v_add_u32 v2, 4096, v7\n"
      "global_load_dwordx4 v[20:23], v2, s[12:13] offset:-4096 sc1\n"
      "global_load_dwordx4 v[12:15], v2, s[12:13] offset:-3072 sc1\n"
      "global_load_dwordx4 v[164:167], v2, s[12:13] offset:-2048 sc1\n"
      "global_load_dwordx2 v[32:33], v55, s[26:27] offset:0 sc1\n"
      "s_waitcnt vmcnt(0)\n"
      "v_add3_u32 v2, v16, v17, s30\n"
      "v_add3_u32 v2, v2, v18, v19\n"
      "v_add3_u32 v2, v2, v8, v9\n"
      "v_add3_u32 v2, v2, v10, v11\n"
      "v_add3_u32 v2, v2, v160, v161\n"
      "v_add3_u32 v2, v2, v162, v163\n"
      "v_xor_b32 v2, v2, v32\n"
      "v_add3_u32 v3, v20, v21, s30\n"
      "v_add3_u32 v3, v3, v22, v23\n"
      "v_add3_u32 v3, v3, v12, v13\n"
      "v_add3_u32 v3, v3, v14, v15\n"
      "v_add3_u32 v3, v3, v164, v165\n"
      "v_add3_u32 v3, v3, v166, v167\n"
      "v_xor_b32 v3, v3, v33\n"
      "v_or_b32 v2, v2, v3\n"
      "v_cmp_ne_u32 vcc, 0, v2\n"
      "s_sub_u32 s44, s44, 1\n"
      "s_cbranch_vccz L_bhA0_%=\n"
      "s_cmp_lg_u32 s44, 0\n"
      "s_cbranch_scc1 L_shA0_%=\n"
      "s_branch L_bhA0_%=\n"
      "L_shA1_%=:\n"
      "v_add_u32 v2, 4096, v7\n"
      "global_load_dwordx4 v[168:171], v2, s[10:11] offset:-1024 sc1\n"
      "global_load_dwordx4 v[128:131], v2, s[10:11] offset:0 sc1\n"
      "global_load_dwordx4 v[136:139], v2, s[10:11] offset:1024 sc1\n"
      "v_add_u32 v2, 4096, v7\n"
      "global_load_dwordx4 v[172:175], v2, s[12:13] offset:-1024 sc1\n"
      "global_load_dwordx4 v[132:135], v2, s[12:13] offset:0 sc1\n"
      "global_load_dwordx4 v[140:143], v2, s[12:13] offset:1024 sc1\n"
      "global_load_dwordx2 v[34:35], v55, s[26:27] offset:512 sc1\n"
      "s_waitcnt vmcnt(0)\n"
      "v_add3_u32 v2, v168, v169, s30\n"
      "v_add3_u32 v2, v2, v170, v171\n"
      "v_add3_u32 v2, v2, v128, v129\n"
      "v_add3_u32 v2, v2, v130, v131\n"
      "v_add3_u32 v2, v2, v136, v137\n"
      "v_add3_u32 v2, v2, v138, v139\n"
      "v_xor_b32 v2, v2, v34\n"
      "v_add3_u32 v3, v172, v173, s30\n"
      "v_add3_u32 v3, v3, v174, v175\n"
      "v_add3_u32 v3, v3, v132, v133\n"
      "v_add3_u32 v3, v3, v134, v135\n"
      "v_add3_u32 v3, v3, v140, v141\n"
      "v_add3_u32 v3, v3, v142, v143\n"
      "v_xor_b32 v3, v3, v35\n"
      "v_or_b32 v2, v2, v3\n"
      "v_cmp_ne_u32 vcc, 0, v2\n"
      "s_sub_u32 s44, s44, 1\n"
      "s_cbranch_vccz L_bhA1_%=\n"
      "s_cmp_lg_u32 s44, 0\n"
      "s_cbranch_scc1 L_shA1_%=\n"
      "s_branch L_bhA1_%=\n"
      "L_shA2_%=:\n"
      "v_add_u32 v2, 10240, v7\n"
      "global_load_dwordx4 v[144:147], v2, s[10:11] offset:-4096 sc1\n"
      "global_load_dwordx4 v[152:155], v2, s[10:11] offset:-3072 sc1\n"
      "global_load_dwordx4 v[64:67], v2, s[10:11] offset:-2048 sc1\n"
      "v_add_u32 v2, 10240, v7\n"
      "global_load_dwordx4 v[148:151], v2, s[12:13] offset:-4096 sc1\n"
      "global_load_dwordx4 v[156:159], v2, s[12:13] offset:-3072 sc1\n"
      "global_load_dwordx4 v[68:71], v2, s[12:13] offset:-2048 sc1\n"
      "global_load_dwordx2 v[60:61], v55, s[26:27] offset:1024 sc1\n"
      "s_waitcnt vmcnt(0)\n"
      "v_add3_u32 v2, v144, v145, s30\n"
      "v_add3_u32 v2, v2, v146, v147\n"
      "v_add3_u32 v2, v2, v152, v153\n"
      "v_add3_u32 v2, v2, v154, v155\n"
      "v_add3_u32 v2, v2, v64, v65\n"
      "v_add3_u32 v2, v2, v66, v67\n"
      "v_xor_b32 v2, v2, v60\n"
      "v_add3_u32 v3, v148, v149, s30\n"
      "v_add3_u32 v3, v3, v150, v151\n"
      "v_add3_u32 v3, v3, v156, v157\n"
      "v_add3_u32 v3, v3, v158, v159\n"
      "v_add3_u32 v3, v3, v68, v69\n"
      "v_add3_u32 v3, v3, v70, v71\n"
      "v_xor_b32 v3, v3, v61\n"
      "v_or_b32 v2, v2, v3\n"
      "v_cmp_ne_u32 vcc, 0, v2\n"
      "s_sub_u32 s44, s44, 1\n"
      "s_cbranch_vccz L_bhA2_%=\n"
      "s_cmp_lg_u32 s44, 0\n"
      "s_cbranch_scc1 L_shA2_%=\n"
      "s_branch L_bhA2_%=\n"
      "L_shA3_%=:\n"
      "v_add_u32 v2, 10240, v7\n"
      "global_load_dwordx4 v[72:75], v2, s[10:11] offset:-1024 sc1\n"
      "global_load_dwordx4 v[80:83], v2, s[10:11] offset:0 sc1\n"
      "global_load_dwordx4 v[88:91], v2, s[10:11] offset:1024 sc1\n"
      "v_add_u32 v2, 10240, v7\n"
      "global_load_dwordx4 v[76:79], v2, s[12:13] offset:-1024 sc1\n"
      "global_load_dwordx4 v[84:87], v2, s[12:13] offset:0 sc1\n"
      "global_load_dwordx4 v[92:95], v2, s[12:13] offset:1024 sc1\n"
      "global_load_dwordx2 v[62:63], v55, s[26:27] offset:1536 sc1\n"
      "s_waitcnt vmcnt(0)\n"
      "v_add3_u32 v2, v72, v73, s30\n"
      "v_add3_u32 v2, v2, v74, v75\n"
      "v_add3_u32 v2, v2, v80, v81\n"
      "v_add3_u32 v2, v2, v82, v83\n"
      "v_add3_u32 v2, v2, v88, v89\n"
      "v_add3_u32 v2, v2, v90, v91\n"
      "v_xor_b32 v2, v2, v62\n"
      "v_add3_u32 v3, v76, v77, s30\n"
      "v_add3_u32 v3, v3, v78, v79\n"
      "v_add3_u32 v3, v3, v84, v85\n"
      "v_add3_u32 v3, v3, v86, v87\n"
      "v_add3_u32 v3, v3, v92, v93\n"
      "v_add3_u32 v3, v3, v94, v95\n"
      "v_xor_b32 v3, v3, v63\n"
      "v_or_b32 v2, v2, v3\n"
      "v_cmp_ne_u32 vcc, 0, v2\n"
      "s_sub_u32 s44, s44, 1\n"
      "s_cbranch_vccz L_bhA3_%=\n"
      "s_cmp_lg_u32 s44, 0\n"
      "s_cbranch_scc1 L_shA3_%=\n"
      "s_branch L_bhA3_%=\n"
      "L_sh20_%=:\n"
      "v_add_u32 v2, 102400, v7\n"
      "global_load_dwordx4 v[16:19], v2, s[10:11] offset:-4096 sc1\n"
      "global_load_dwordx4 v[20:23], v2, s[10:11] offset:-3072 sc1\n"
      "global_load_dwordx4 v[8:11], v2, s[10:11] offset:-2048 sc1\n"
      "v_add_u32 v2, 102400, v7\n"
      "global_load_dwordx4 v[12:15], v2, s[10:11] offset:-1024 sc1\n"
      "global_load_dwordx4 v[160:163], v2, s[10:11] offset:0 sc1\n"
      "global_load_dwordx4 v[164:167], v2, s[10:11] offset:1024 sc1\n"
      "global_load_dwordx2 v[36:37], v55, s[26:27] offset:2048 sc1\n"
      "s_waitcnt vmcnt(0)\n"
      "v_add3_u32 v2, v16, v17, s30\n"
      "v_add3_u32 v2, v2, v18, v19\n"
      "v_add3_u32 v2, v2, v20, v21\n"
      "v_add3_u32 v2, v2, v22, v23\n"
      "v_add3_u32 v2, v2, v8, v9\n"
      "v_add3_u32 v2, v2, v10, v11\n"
      "v_xor_b32 v2, v2, v36\n"
      "v_add3_u32 v3, v12, v13, s30\n"
      "v_add3_u32 v3, v3, v14, v15\n"
      "v_add3_u32 v3, v3, v160, v161\n"
      "v_add3_u32 v3, v3, v162, v163\n"
      "v_add3_u32 v3, v3, v164, v165\n"
      "v_add3_u32 v3, v3, v166, v167\n"
      "v_xor_b32 v3, v3, v37\n"
      "v_or_b32 v2, v2, v3\n"
      "v_cmp_ne_u32 vcc, 0, v2\n"
      "s_sub_u32 s44, s44, 1\n"
      "s_cbranch_vccz L_bh20_%=\n"
      "s_cmp_lg_u32 s44, 0\n"
      "s_cbranch_scc1 L_sh20_%=\n"
      "s_branch L_bh20_%=\n"
      "L_sh21_%=:\n"
      "v_add_u32 v2, 108544, v7\n"
      "global_load_dwordx4 v[168:171], v2, s[10:11] offset:-4096 sc1\n"
      "global_load_dwordx4 v[172:175], v2, s[10:11] offset:-3072 sc1\n"
      "global_load_dwordx4 v[128:131], v2, s[10:11] offset:-2048 sc1\n"
      "v_add_u32 v2, 108544, v7\n"
      "global_load_dwordx4 v[132:135], v2, s[10:11] offset:-1024 sc1\n"
      "global_load_dwordx4 v[136:139], v2, s[10:11] offset:0 sc1\n"
      "global_load_dwordx4 v[140:143], v2, s[10:11] offset:1024 sc1\n"
      "global_load_dwordx2 v[38:39], v55, s[26:27] offset:2560 sc1\n"
      "s_waitcnt vmcnt(0)\n"
      "v_add3_u32 v2, v168, v169, s30\n"
      "v_add3_u32 v2, v2, v170, v171\n"
      "v_add3_u32 v2, v2, v172, v173\n"
      "v_add3_u32 v2, v2, v174, v175\n"
      "v_add3_u32 v2, v2, v128, v129\n"
      "v_add3_u32 v2, v2, v130, v131\n"
      "v_xor_b32 v2, v2, v38\n"
      "v_add3_u32 v3, v132, v133, s30\n"
      "v_add3_u32 v3, v3, v134, v135\n"
      "v_add3_u32 v3, v3, v136, v137\n"
      "v_add3_u32 v3, v3, v138, v139\n"
      "v_add3_u32 v3, v3, v140, v141\n"
      "v_add3_u32 v3, v3, v142, v143\n"
      "v_xor_b32 v3, v3, v39\n"
      "v_or_b32 v2, v2, v3\n"
      "v_cmp_ne_u32 vcc, 0, v2\n"
      "s_sub_u32 s44, s44, 1\n"
      "s_cbranch_vccz L_bh21_%=\n"
      "s_cmp_lg_u32 s44, 0\n"
      "s_cbranch_scc1 L_sh21_%=\n"
      "s_branch L_bh21_%=\n"
      "L_sh30_%=:\n"
      "v_add_u32 v2, 102400, v7\n"
      "global_load_dwordx4 v[224:227], v2, s[12:13] offset:-4096 sc1\n"
      "global_load_dwordx4 v[228:231], v2, s[12:13] offset:-3072 sc1\n"
      "global_load_dwordx4 v[232:235], v2, s[12:13] offset:-2048 sc1\n"
      "v_add_u32 v2, 102400, v7\n"
      "global_load_dwordx4 v[236:239], v2, s[12:13] offset:-1024 sc1\n"
      "global_load_dwordx4 v[240:243], v2, s[12:13] offset:0 sc1\n"
      "global_load_dwordx4 v[244:247], v2, s[12:13] offset:1024 sc1\n"
      "global_load_dwordx2 v[24:25], v55, s[26:27] offset:3072 sc1\n"
      "s_waitcnt vmcnt(0)\n"
      "v_add3_u32 v2, v224, v225, s30\n"
      "v_add3_u32 v2, v2, v226, v227\n"
      "v_add3_u32 v2, v2, v228, v229\n"
      "v_add3_u32 v2, v2, v230, v231\n"
      "v_add3_u32 v2, v2, v232, v233\n"
      "v_add3_u32 v2, v2, v234, v235\n"
      "v_xor_b32 v2, v2, v24\n"
      "v_add3_u32 v3, v236, v237, s30\n"
      "v_add3_u32 v3, v3, v238, v239\n"
      "v_add3_u32 v3, v3, v240, v241\n"
      "v_add3_u32 v3, v3, v242, v243\n"
      "v_add3_u32 v3, v3, v244, v245\n"
      "v_add3_u32 v3, v3, v246, v247\n"
      "v_xor_b32 v3, v3, v25\n"
      "v_or_b32 v2, v2, v3\n"
      "v_cmp_ne_u32 vcc, 0, v2\n"
      "s_sub_u32 s44, s44, 1\n"
      "s_cbranch_vccz L_bh30_%=\n"
      "s_cmp_lg_u32 s44, 0\n"
      "s_cbranch_scc1 L_sh30_%=\n"
      "s_branch L_bh30_%=\n"
      "L_sh31_%=:\n"
      "v_add_u32 v2, 108544, v7\n"
      "global_load_dwordx4 v[144:147], v2, s[12:13] offset:-4096 sc1\n"
      "global_load_dwordx4 v[148:151], v2, s[12:13] offset:-3072 sc1\n"
      "global_load_dwordx4 v[152:155], v2, s[12:13] offset:-2048 sc1\n"
      "v_add_u32 v2, 108544, v7\n"
      "global_load_dwordx4 v[156:159], v2, s[12:13] offset:-1024 sc1\n"
      "global_load_dwordx4 v[64:67], v2, s[12:13] offset:0 sc1\n"
      "global_load_dwordx4 v[68:71], v2, s[12:13] offset:1024 sc1\n"
      "global_load_dwordx2 v[26:27], v55, s[26:27] offset:3584 sc1\n"
      "s_waitcnt vmcnt(0)\n"
      "v_add3_u32 v2, v144, v145, s30\n"
      "v_add3_u32 v2, v2, v146, v147\n"
      "v_add3_u32 v2, v2, v148, v149\n"
      "v_add3_u32 v2, v2, v150, v151\n"
      "v_add3_u32 v2, v2, v152, v153\n"
      "v_add3_u32 v2, v2, v154, v155\n"
      "v_xor_b32 v2, v2, v26\n"
      "v_add3_u32 v3, v156, v157, s30\n"
      "v_add3_u32 v3, v3, v158, v159\n"
      "v_add3_u32 v3, v3, v64, v65\n"
      "v_add3_u32 v3, v3, v66, v67\n"
      "v_add3_u32 v3, v3, v68, v69\n"
      "v_add3_u32 v3, v3, v70, v71\n"
      "v_xor_b32 v3, v3, v27\n"
      "v_or_b32 v2, v2, v3\n"
      "v_cmp_ne_u32 vcc, 0, v2\n"
      "s_sub_u32 s44, s44, 1\n"
      "s_cbranch_vccz L_bh31_%=\n"
      "s_cmp_lg_u32 s44, 0\n"
      "s_cbranch_scc1 L_sh31_%=\n"
      "s_branch L_bh31_%=\n"
      "L_sgb_%=:\n"
      "global_load_dwordx4 v[72:75], v53, s[26:27] offset:0 sc1\n"
      "global_load_dwordx4 v[76:79], v53, s[26:27] offset:16 sc1\n"
      "global_load_dwordx4 v[80:83], v53, s[26:27] offset:32 sc1\n"
      "global_load_dwordx4 v[84:87], v53, s[26:27] offset:48 sc1\n"
      "s_waitcnt vmcnt(0)\n"
      "v_add3_u32 v88, v73, v75, v77\n"
      "v_add3_u32 v88, v88, v79, v81\n"
      "v_add3_u32 v88, v88, v83, v85\n"
      "v_add_u32 v88, v88, v87\n"
      "v_cmp_ne_u32 vcc, s71, v88\n"
      "s_sub_u32 s44, s44, 1\n"
      "s_cbranch_vccz L_bgb_%=\n"
      "s_cmp_lg_u32 s44, 0\n"
      "s_cbranch_scc1 L_sgb_%=\n"
      "s_branch L_bgb_%=\n"
      : STATE, "+{s[32:35]}"(s32), "+{v[8:23]}"(v8), "+{v[24:27]}"(v24), "+{v[52:55]}"(v52), "+{v[192:195]}"(t[0]), "+{v[240:243]}"(t[1]), "+{v[196:199]}"(t[2]), "+{v[200:203]}"(t[3]), "+{v[204:207]}"(t[4]), "+{v[208:211]}"(t[5]), "+{v[212:215]}"(t[6]), "+{v[216:219]}"(t[7]), "+{v[220:223]}"(t[8]), "+{v[224:227]}"(t[9]), "+{v[228:231]}"(t[10]), "+{v[40:43]}"(t[11]), "+{v[44:47]}"(t[12]), "+{v[48:51]}"(t[13]), "+{v[56:59]}"(t[14]), "+{v[60:63]}"(t[15]), "+{v[28:31]}"(t[16]), "+{v[4:7]}"(t[17])
      : "{s2}"(blockIdx.x)
      : "memory", "scc", "vcc", "s40", "s41", "s42", "s43", "s44", "s45", "s46", "s47", "s48", "s49", "s50", "s51", "v2", "v3", "v32", "v33", "v34", "v35", "v36", "v37", "v38", "v39", "v64", "v65", "v66", "v67", "v68", "v69", "v70", "v71", "v72", "v73", "v74", "v75", "v76", "v77", "v78", "v79", "v80", "v81", "v82", "v83", "v84", "v85", "v86", "v87", "v88", "v89", "v90", "v91", "v92", "v93", "v94", "v95", "v96", "v97", "v98", "v99", "v100", "v101", "v102", "v103", "v104", "v105", "v106", "v107", "v108", "v109", "v110", "v111", "v112", "v113", "v114", "v115", "v116", "v117", "v118", "v119", "v120", "v121", "v122", "v123", "v124", "v125", "v126", "v127", "v128", "v129", "v130", "v131", "v132", "v133", "v134", "v135", "v136", "v137", "v138", "v139", "v140", "v141", "v142", "v143", "v144", "v145", "v146", "v147", "v148", "v149", "v150", "v151", "v152", "v153", "v154", "v155", "v156", "v157", "v158", "v159", "v160", "v161", "v162", "v163", "v164", "v165", "v166", "v167", "v168", "v169", "v170", "v171", "v172", "v173", "v174", "v175", "v176", "v177", "v178", "v179", "v180", "v181", "v182", "v183", "v184", "v185", "v186", "v187", "v188", "v189", "v190", "v191", "v236", "v237", "v238", "v239", "v244", "v245", "v246", "v247", "v248", "v249", "v250", "v251", "v252", "v253", "v254", "v255", "a0", "a1", "a2", "a3", "a4", "a5", "a6", "a7", "a8", "a9", "a10", "a11", "a12", "a13", "a14", "a15", "a16", "a17", "a18", "a19", "a20", "a21", "a22", "a23", "a24", "a25", "a26", "a27", "a28", "a29", "a30", "a31", "a32", "a33", "a34", "a35", "a36", "a37", "a38", "a39", "a40", "a41", "a42", "a43", "a44", "a45", "a46", "a47", "a48", "a49", "a50", "a51", "a52", "a53", "a54", "a55", "a56", "a57", "a58", "a59", "a60", "a61", "a62", "a63", "a64", "a65", "a66", "a67", "a68", "a69", "a70", "a71", "a72", "a73", "a74", "a75", "a76", "a77", "a78", "a79", "a80", "a81", "a82", "a83", "a84", "a85", "a86", "a87", "a88", "a89", "a90", "a91", "a92", "a93", "a94", "a95", "a96", "a97", "a98", "a99", "a100", "a101", "a102", "a103", "a104", "a105", "a106", "a107", "a108", "a109", "a110", "a111", "a112", "a113", "a114", "a115", "a116", "a117", "a118", "a119", "a120", "a121", "a122", "a123", "a124", "a125", "a126", "a127", "a128", "a129", "a130", "a131", "a132", "a133", "a134", "a135", "a136", "a137", "a138", "a139", "a140", "a141", "a142", "a143", "a144", "a145", "a146", "a147", "a148", "a149", "a150", "a151", "a152", "a153", "a154", "a155", "a156", "a157", "a158", "a159", "a160", "a161", "a162", "a163", "a164", "a165", "a166", "a167", "a168", "a169", "a170", "a171", "a172", "a173", "a174", "a175", "a176", "a177", "a178", "a179", "a180", "a181", "a182", "a183", "a184", "a185", "a186", "a187", "a188", "a189", "a190", "a191", "a192", "a193", "a194", "a195", "a196", "a197", "a198", "a199", "a200", "a201", "a202", "a203", "a204", "a205", "a206", "a207", "a208", "a209", "a210", "a211", "a212", "a213", "a214", "a215", "a216", "a217", "a218", "a219", "a220", "a221", "a222", "a223", "a224", "a225", "a226", "a227", "a228", "a229", "a230", "a231", "a232", "a233", "a234", "a235", "a236", "a237", "a238", "a239", "a240", "a241", "a242", "a243", "a244", "a245", "a246", "a247", "a248", "a249", "a250", "a251", "a252", "a253", "a254", "a255");
}

extern "C" void kernel_launch(void* const* d_in, const int* in_sizes, int n_in, void* d_out, int out_size,
                              void* d_ws, size_t ws_size, hipStream_t stream) {
  mega<<<256, 256, 0, stream>>>((const u16*)d_in[0], (const u16*)d_in[1], (const u16*)d_in[2], (const u16*)d_in[3],
                                (const u16*)d_in[4], (const u16*)d_in[5], (const u16*)d_in[7], (const u16*)d_in[8],
                                (const u16*)d_in[9], (const u16*)d_in[10], (const u16*)d_in[11], (char*)d_ws, (u16*)d_out);
}
```

```cpp
#include <hip/hip_runtime.h>
#include <stdint.h>

typedef unsigned short u16;
typedef __attribute__((ext_vector_type(4))) unsigned int u32x4;
typedef __attribute__((ext_vector_type(8))) unsigned int u32x8;
typedef __attribute__((ext_vector_type(16))) unsigned int u32x16;
static constexpr int D_ = 640, F_ = 1536, RS_ = 4224;

static constexpr unsigned WS_FW = 20480;
static constexpr unsigned WS_FD = WS_FW + 61440;
static constexpr unsigned WS_STAT = 147456;
static constexpr unsigned WS_PART = 262144;
static constexpr unsigned WS_MOD = 786432;
static constexpr unsigned WS_KVT = 1u << 20;
static constexpr unsigned WS_QT = 4u << 20;
static constexpr unsigned WS_X1T = 7u << 20;
static constexpr unsigned WS_CK = 2752512;
static constexpr unsigned WS_WP = 10u << 20;
static constexpr unsigned WS_WD = 14u << 20;
static constexpr unsigned WS_HH = 16u << 20;
static constexpr unsigned WS_HC = 22u << 20;
static constexpr unsigned WS_ST2 = WS_HC + (512u << 10);
static constexpr unsigned WS_X1R = 23u << 20;
static constexpr unsigned WS_DUMMY = WS_HC + (768u << 10);

static constexpr int KCP = 4160, KCH = 4 * KCP;
static constexpr int XR_CH = 7680;
static constexpr int L_XI = 3 * KCH;
static constexpr int L_EX = 72960;
static constexpr int L_KV = L_EX, KVW = 6400;
static constexpr int L_KT = L_KV + 4 * KVW;
static constexpr int L_QT = L_KT + 84 * 144;
static constexpr int L_WR = L_QT + 10240;
static constexpr int L_GW = L_WR + 16384;
static constexpr int L_GS = L_GW + 20480;
static constexpr int L_SC = L_GS + 320;
static constexpr int L_XR = 110592, L_RS = 162304;
static constexpr int L_WD1 = 122880;
static_assert(L_KT == 98560 && L_QT == 110656 && L_WR == 120896 && L_GW == 137280, "");

struct __attribute__((aligned(256))) Tab { unsigned s[1024][64]; };
static constexpr Tab make_tab() {
  Tab t = {};
  for (int i = 0; i < 1024; ++i) {
    const int b = i >> 2, w = i & 3, xcd = b & 7, idx = b >> 3, L = xcd * 32 + idx, side = L & 1;
    const int ct = L & 7, rt = L >> 3, srow = xcd * 256 + idx * 8;
    const int chA = 128 + L, chH = L >> 1, rH = 512 * (side + (3 - 2 * side) * (w >> 1)) + 256 * (w & 1);
    const int id = idx < 29 ? idx : 29, gj = xcd * 30 + id, j_ks = 5 * (xcd >> 1) + (id * 4 + w) / 24, j_b = 24 * (xcd & 1) + (id * 4 + w) % 24;
    const int d_ks = gj / 5, d_cb = 4 * (gj % 5) + w;
    const unsigned dummy = WS_DUMMY + (i & 63) * 1024;
    const int bm = (xcd >> 2) * 8 + (idx >> 2), bn = (xcd & 3) * 4 + (idx & 3), bm2 = (xcd >> 1) * 8 + (idx >> 2), bn2 = (xcd & 1) * 4 + (idx & 3);
    t.s[i][0] = WS_KVT + (1984 - 64 * rt) * 2 + ct * 80 * RS_;
    t.s[i][1] = WS_KVT + WS_CK + (31 - rt) * RS_ + ct * 512;
    t.s[i][2] = idx < 30 ? WS_MOD + (gj >> 2) * 2048 + (gj & 3) * 64 : dummy;
    t.s[i][3] = 32 * j_ks * (F_ * 2) + (j_b - w) * 64;
    t.s[i][4] = WS_QT + rt * 128 + ct * 80 * RS_;
    t.s[i][5] = WS_QT + WS_CK + (rt * 40 + ct * 5) * 128;
    t.s[i][6] = srow * 1280;
    t.s[i][7] = WS_STAT + (srow >> 9) * 4096 + ((srow >> 3) & 63) * 16;
    t.s[i][8] = WS_MOD + (1920 + 640 * (w >> 1) + 32 * j_ks + 16 * (w & 1)) * 32;
    t.s[i][9] = idx < 30 ? WS_PART + j_ks * 24576 + j_b * 512 : dummy;
    t.s[i][10] = WS_KVT + WS_CK + chA / 20 * 128 + chA % 20 * 4;
    t.s[i][11] = WS_KVT + WS_CK + chH / 20 * 128 + chH % 20 * 4;
    t.s[i][12] = idx < 30 ? WS_WP + (4 * j_b + 0) / 3 * 61440 + (j_ks * 3 + (4 * j_b + 0) % 3) * 1024 : dummy;
    t.s[i][13] = idx < 30 ? WS_WP + (4 * j_b + 1) / 3 * 61440 + (j_ks * 3 + (4 * j_b + 1) % 3) * 1024 : dummy;
    t.s[i][14] = idx < 30 ? WS_WP + (4 * j_b + 2) / 3 * 61440 + (j_ks * 3 + (4 * j_b + 2) % 3) * 1024 : dummy;
    t.s[i][15] = idx < 30 ? WS_WP + (4 * j_b + 3) / 3 * 61440 + (j_ks * 3 + (4 * j_b + 3) % 3) * 1024 : dummy;
    t.s[i][16] = WS_FW + (3 * (bn * 4 + w) / 4 < 46 ? 3 * (bn * 4 + w) / 4 : 46) * 1280;
    t.s[i][17] = WS_X1T + WS_CK + chH / 2 * 512 + chH % 2 * 64 + (rH >> 9) * 128 + ((rH >> 6) & 7) * 8;
    t.s[i][18] = WS_X1T + ((rH >> 6) * D_ + chH) * 128;
    t.s[i][19] = idx < 30 ? WS_FW + (j_b * 20 + j_ks) * 64 : dummy;
    t.s[i][20] = WS_QT + chH * RS_ + rH * 2;
    t.s[i][21] = WS_X1T + WS_CK + chA / 2 * 512 + chA % 2 * 64;
    t.s[i][22] = WS_STAT + (rH >> 9) * 4096 + ((rH >> 3) & 63) * 16;
    t.s[i][23] = WS_MOD + chH / 8 * 256 + chH % 8 * 8;
    t.s[i][24] = WS_QT + WS_CK + chA / 16 * 128 + chA % 16 * 4;
    t.s[i][25] = WS_QT + WS_CK + (rH >> 6) * 5120 + chH / 16 * 128 + chH % 16 * 4;
    t.s[i][26] = idx < 30 ? WS_WD + ((2 * d_cb / 5 * 48 + d_ks) * 5 + 2 * d_cb % 5) * 1024 : dummy;
    t.s[i][27] = idx < 30 ? WS_WD + (((2 * d_cb + 1) / 5 * 48 + d_ks) * 5 + (2 * d_cb + 1) % 5) * 1024 : dummy;
    t.s[i][28] = idx < 30 ? WS_FD + (d_cb * 48 + d_ks) * 64 : dummy;
    t.s[i][29] = WS_X1T + chA * 128;
    t.s[i][30] = WS_X1T + WS_CK + bm / 4 * 128 + bm % 4 * 16 + 4;
    t.s[i][31] = WS_WP + (bn * 4 + w) * 61440;
    t.s[i][32] = chA * 4096;
    t.s[i][33] = chH * 4096;
    t.s[i][34] = ((1792 - rH) >> 5) * 96;
    t.s[i][35] = (96 * (d_ks / 3) + 16 * (d_ks % 3 >> 1) + 2 * (d_ks % 3 & 1)) * (D_ * 2) + d_cb * 64;
    t.s[i][36] = (d_ks % 3 == 1 ? 30 : 18) * (D_ * 2) + ((chA / 8 * 256 + chA % 8 * 8 - chH / 8 * 256 - chH % 8 * 8) << 16);
    t.s[i][37] = ((chA >> 1) & 3) * 32;
    t.s[i][38] = WS_KVT + WS_CK + (chA + 256) / 20 * 128 + (chA + 256) % 20 * 4;
    t.s[i][39] = side;
    t.s[i][40] = WS_ST2 + (bm2 * 8 + bn2) * 1024 + w * 256;
    t.s[i][41] = WS_ST2 + (2 * bm + (w >> 1)) * 8192 + (w & 1) * 512;
    t.s[i][42] = L_XR + (w < 3 ? 20 * w : 56) * 128;
    t.s[i][43] = L_RS + (w & 1) * 512 + (w >> 1) * 256;
    t.s[i][48] = w;
    t.s[i][49] = WS_HH + (((2 * bm + (w >> 1)) * 4 + (w & 1)) * 48 + 3 * bn) * 1024;
    t.s[i][50] = WS_HC + (((2 * bm + (w >> 1)) * 4 + (bn >> 2)) * 8 + (bn & 3)) * 512 + (w & 1) * 256;
    t.s[i][51] = WS_HC + (bm2 * 4 + w) * 4096;
    t.s[i][52] = WS_FD + (((5 * bn2) >> 1) * 48 + 12 * w) * 64;
    t.s[i][53] = WS_PART + (bn * 4 + w) * 384;
    t.s[i][54] = WS_MOD + (3200 + bn2 * 80 + (w < 3 ? 16 * w : 48)) * 32;
    t.s[i][55] = WS_X1R + (bm2 * D_ + bn2 * 80 + (w < 3 ? 20 * w : 56)) * 128;
    t.s[i][56] = WS_X1T + WS_CK + (bn2 * 80 + (w < 3 ? 20 * w : 56)) * 256 + bm2 / 8 * 128 + bm2 % 8 * 8 + bm2 % 2 * 4;
    t.s[i][57] = WS_HH + (((2 * bm + (w >> 1)) * 4 + 2 + (w & 1)) * 48 + 3 * bn) * 1024;
    t.s[i][58] = WS_HC + (((2 * bm + (w >> 1)) * 4 + (bn >> 2)) * 8 + 4) * 512 + (4 * (w & 1) + (bn & 3)) * 256;
    t.s[i][59] = WS_X1T + bm * 163840;
    t.s[i][60] = WS_WD + (bn2 * 48 + 12 * w) * 5120;
    t.s[i][61] = w * 4096;
    t.s[i][62] = ((bm2 >> 1) * 128 + (bm2 & 1) * 32 + (w & 1) * 16) * (D_ * 2) + bn2 * 160 + (w >> 1) * 64;
    t.s[i][63] = WS_HH + (bm2 * 192 + 12 * w) * 1024;
  }
  return t;
}
static __device__ const Tab TAB = make_tab();

__global__ __launch_bounds__(256, 1) void mega(const u16* q, const u16* k, const u16* v, const u16* shift_k, const u16* ssm_k, const u16* ssm_D, const u16* w_gate,
                                               const u16* w_up, const u16* w_down, const u16* c, const u16* w_mod, char* ws, u16* out) {
  __shared__ __attribute__((aligned(16))) char lds[163840];
  int w = (int)(size_t)lds, tid = threadIdx.x, v1;
  u32x16 s4, s60, s76;
  u32x8 s20, s52, s92;
  u32x4 s28, s36, v232;
  asm volatile(
      "s_load_dwordx16 s[4:19], s[0:1], 0x0\n"
      "s_load_dwordx8 s[20:27], s[0:1], 0x40\n"
      "s_load_dwordx2 s[28:29], s[0:1], 0x60\n"
      "v_readfirstlane_b32 s3, v0\n"
      "v_and_b32 v2, 63, v0\n"
      "s_lshl_b32 s42, s2, 4\n"
      "s_and_b32 s43, s2, 7\n"
      "v_mul_u32_u24 v3, 205, v2\n"
      "s_and_b32 s52, s2, 0xc0\n"
      "s_lshr_b32 s3, s3, 6\n"
      "v_lshlrev_b32 v8, 4, v2\n"
      "s_cmp_lg_u32 s3, 3\n"
      "s_cbranch_scc1 L_pf%=\n"
      "s_and_b32 s47, s2, 0xf8\n"
      "s_lshl_b32 s47, s47, 10\n"
      "s_cmp_ge_u32 s47, 39296\n"
      "s_cbranch_scc1 L_pf%=\n"
      "s_getpc_b64 s[54:55]\n"
      "v_lshl_add_u32 v9, v8, 3, s47\n"
      "s_and_b32 s54, s54, 0xffffff00\n"
      "s_mov_b32 m0, 160512\n"
      "v_min_u32 v9, 39232, v9\n"
      "global_load_lds_dword v9, s[54:55]\n"
      "L_pf%=:\n"
      "s_lshl_b32 s47, s43, 8\n"
      "v_add_u32 v9, 13120, v3\n"
      "v_add_u32 v10, 26240, v3\n"
      "s_add_u32 s52, s52, s47\n"
      "s_lshl_b32 s47, s3, 4\n"
      "v_lshrrev_b32 v3, 11, v3\n"
      "v_lshrrev_b32 v9, 11, v9\n"
      "s_add_u32 s52, s52, s47\n"
      "s_movk_i32 s47, 1120\n"
      "v_lshrrev_b32 v10, 11, v10\n"
      "s_mulk_i32 s52, 1280\n"
      "s_bfe_u32 s54, s2, 0x30003\n"
      "v_mad_u32_u24 v232, v3, s47, v8\n"
      "v_mad_u32_u24 v233, v9, s47, v8\n"
      "v_mad_u32_u24 v234, v10, s47, v8\n"
      "s_mul_i32 s47, s54, 160\n"
      "s_mul_i32 s53, s3, 6400\n"
      "s_add_u32 s52, s52, s47\n"
      "v_subrev_u32 v233, 6656, v233\n"
      "v_subrev_u32 v234, 5632, v234\n"
      "s_sub_u32 s47, s52, 3840\n"
      "s_add_u32 s53, s53, 72960\n"
      "v_add_u32 v3, s47, v232\n"
      "s_add_u32 s47, s52, 3840\n"
      "s_add_u32 s55, s52, 7680\n"
      "s_mov_b32 s38, 2621440\n"
      "s_mov_b32 s39, 0x20000\n"
      "s_mulk_i32 s54, 640\n"
      "s_waitcnt lgkmcnt(0)\n"
      "s_mov_b32 m0, s53\n"
      "s_mov_b32 s36, s6\n"
      "s_and_b32 s37, s7, 0xffff\n"
      "buffer_load_dwordx4 v3, s[36:39], 0 offen lds\n"
      "s_add_u32 m0, s53, 1024\n"
      "s_nop 0\n"
      "buffer_load_dwordx4 v233, s[36:39], s47 offen lds\n"
      "s_add_u32 m0, s53, 2048\n"
      "v_add_u32 v9, s54, v8\n"
      "buffer_load_dwordx4 v234, s[36:39], s47 offen lds\n"
      "s_add_u32 m0, s53, 3072\n"
      "s_mov_b32 s36, s8\n"
      "s_and_b32 s37, s9, 0xffff\n"
      "buffer_load_dwordx4 v232, s[36:39], s52 offen lds\n"
      "s_add_u32 m0, s53, 4096\n"
      "s_nop 0\n"
      "buffer_load_dwordx4 v233, s[36:39], s55 offen lds\n"
      "s_add_u32 m0, s53, 5120\n"
      "s_mov_b32 exec_hi, 0\n"
      "buffer_load_dwordx4 v234, s[36:39], s55 offen lds\n"
      "s_add_u32 m0, s53, 5632\n"
      "s_mov_b32 exec_hi, 0xff\n"
      "global_load_lds_dwordx4 v9, s[10:11]\n"
      "s_mov_b32 exec_hi, -1\n"
      "s_load_dwordx2 s[30:31], s[26:27], 0x0\n"
      "s_lshl_b32 s51, s3, 2\n"
      "s_add_u32 s42, s42, s51\n"
      "s_lshl_b32 s42, s42, 6\n"
      "s_add_u32 s56, s40, s42\n"
      "s_addc_u32 s57, s41, 0\n"
      "s_load_dwordx16 s[60:75], s[56:57], 0x0\n"
      "s_load_dwordx16 s[76:91], s[56:57], 0x40\n"
      "s_load_dwordx8 s[92:99], s[56:57], 0x80\n"
      : "+{s3}"(w), "+{v0}"(tid), "=&{s[4:19]}"(s4), "=&{s[20:27]}"(s20), "=&{s[28:31]}"(s28), "=&{s[36:39]}"(s36), "=&{s[52:59]}"(s52), "=&{s[60:75]}"(s60), "=&{s[76:91]}"(s76),
        "=&{s[92:99]}"(s92), "=&{v[232:235]}"(v232), "=&{v1}"(v1)
      : "{s[0:1]}"(__builtin_amdgcn_kernarg_segment_ptr()), "{s2}"(blockIdx.x), "{s[40:41]}"(&TAB)
      : "memory", "scc", "s42", "s43", "s47", "s48", "s49", "s50", "s51", "v2", "v3", "v4", "v5", "v8", "v9", "v10", "v11");
#define STATE "+{s3}"(w), "+{v0}"(tid), "+{s[4:19]}"(s4), "+{s[20:27]}"(s20), "+{s[28:31]}"(s28), "+{s[36:39]}"(s36), "+{s[52:59]}"(s52), "+{s[60:75]}"(s60), "+{s[76:91]}"(s76), \
              "+{s[92:99]}"(s92), "+{v[232:235]}"(v232), "+{v1}"(v1)
  u32x4 t[18], s32, v24, v52;
  u32x16 v8;
  {
    const int lane = tid & 63, ip = lane & 15, fq = lane >> 4, q4 = ip >> 2, p4 = lane & 3;
    const int ck = 40 * w + (lane < 40 ? lane : 39);
    const bool dg = fq == q4;
    const unsigned msk = dg ? ((ip & 1) ? 0xFFFF0000u : 0xFFFFu) : 0u, mk0 = (ip & 2) ? 0u : msk, mk1 = (ip & 2) ? msk : 0u;
    t[0][0] = dg && p4 == 0 ? 0x3F803F80u : 0u;
    t[0][1] = dg && p4 == 1 ? 0x3F803F80u : 0u;
    t[0][2] = dg && p4 == 2 ? 0x3F803F80u : 0u;
    t[0][3] = dg && p4 == 3 ? 0x3F803F80u : 0u;
    t[1][0] = (mk0 & 0x0D0C0D0Cu) ^ 0x0C0C0C0Cu;
    t[1][1] = (mk1 & 0x0D0C0D0Cu) ^ 0x0C0C0C0Cu;
    t[1][2] = (mk0 & 0x0F0E0F0Eu) ^ 0x0C0C0C0Cu;
    t[1][3] = (mk1 & 0x0F0E0F0Eu) ^ 0x0C0C0C0Cu;
    t[2][0] = L_KV + w * KVW + ip * 160 + fq * 8;
    t[2][1] = L_KV + w * KVW + 5632 + ip * 8;
    t[2][2] = mk0;
    t[2][3] = mk1;
    t[3][0] = L_KT + ip * 144 + (60 - 16 * w - 4 * fq) * 2;
    t[3][1] = L_KT + (20 * w + (lane >> 3)) * 144 + (lane & 7) * 16;
    t[3][2] = (20 * w + (lane >> 3)) * RS_ + (lane & 7) * 16;
    t[3][3] = lane & 4 ? 0x80000000u : w * 128 + (8 * (lane & 7) + (lane >> 3)) * 4;
    t[4][0] = L_GS + ((ck & ~31) + ((ck >> 2) & 3) * 8 + ((ck >> 4) & 1) * 4 + (ck & 3)) * 2;
    t[4][1] = L_GW + (4 * fq + q4) * 128 + p4 * 8 + ((w ^ (2 * fq + (q4 >> 1))) & 3) * 32;
    t[4][2] = L_GS + fq * 16;
    t[4][3] = lane < 16 ? (2 * w + (lane >> 3)) * 256 + (lane & 7) * 8 : 0x80000000u;
    t[5][0] = (lane & 12) == 4 ? 1 << 20 : 0;
    t[5][1] = lane & 8 ? 0u : ~0u;
    t[5][2] = (8 * w + fq) * (F_ * 2) + (ip ^ (2 * fq)) * 16;
    t[5][3] = (2 * w + (lane >> 5)) * 1280 + (lane & 31) * 16;
  }
  asm volatile(
      "v_and_b32 v2, 63, v0\n"
      "s_and_b32 s43, s2, 7\n"
      "v_mul_u32_u24 v3, 205, v2\n"
      "v_lshlrev_b32 v8, 4, v2\n"
      "s_lshr_b32 s48, s2, 3\n"
      "s_min_u32 s48, s48, 29\n"
      "s_mul_i32 s49, s43, 30\n"
      "v_lshrrev_b32 v4, 3, v2\n"
      "s_add_u32 s48, s48, s49\n"
      "v_xor_b32 v5, v4, v2\n"
      "s_and_b32 s49, s48, 3\n"
      "v_and_b32 v11, 1, v2\n"
      "s_lshl_b32 s49, s49, 2\n"
      "v_and_or_b32 v5, v5, 6, v11\n"
      "s_add_u32 s49, s49, s3\n"
      "v_mul_u32_u24 v4, 7680, v4\n"
      "s_lshr_b32 s48, s48, 2\n"
      "s_mul_i32 s50, s49, 307200\n"
      "s_lshl_b32 s48, s48, 7\n"
      "v_lshl_add_u32 v4, v5, 4, v4\n"
      "s_add_u32 s48, s48, s50\n"
      "s_mul_i32 s50, s3, 5120\n"
      "s_mulk_i32 s49, 80\n"
      "v_min_u32 v5, 39, v2\n"
      "v_add_u32 v4, s48, v4\n"
      "v_lshl_add_u32 v5, v5, 1, s49\n"
      "s_add_u32 m0, s50, 137280\n"
      "global_load_ushort v235, v5, s[22:23]\n"
      "global_load_lds_dwordx4 v4, s[24:25]\n"
      "s_add_u32 m0, m0, 1024\n"
      "v_add_u32 v4, 61440, v4\n"
      "global_load_lds_dwordx4 v4, s[24:25]\n"
      "s_add_u32 m0, m0, 1024\n"
      "v_add_u32 v4, 61440, v4\n"
      "global_load_lds_dwordx4 v4, s[24:25]\n"
      "s_add_u32 m0, m0, 1024\n"
      "v_add_u32 v4, 61440, v4\n"
      "global_load_lds_dwordx4 v4, s[24:25]\n"
      "s_add_u32 m0, m0, 1024\n"
      "v_add_u32 v4, 61440, v4\n"
      "global_load_lds_dwordx4 v4, s[24:25]\n"
      "s_cmp_lg_u32 s3, 3\n"
      "s_cbranch_scc1 L_tc%=\n"
      "v_min_u32 v9, 25, v2\n"
      "s_mov_b32 m0, 160512\n"
      "v_lshlrev_b32 v9, 20, v9\n"
      "v_add_u32 v9, 0xfff80, v9\n"
      "v_min_u32 v10, 2, v2\n"
      "v_lshlrev_b32 v10, 20, v10\n"
      "v_min_u32 v11, 0x100000, v10\n"
      "global_load_lds_dword v9, s[26:27] \n"
      "global_load_lds_dword v10, s[28:29]\n"
      "global_load_lds_dword v10, s[12:13]\n"
      "global_load_lds_dword v11, s[16:17]\n"
      "global_load_lds_dword v11, s[18:19]\n"
      "global_load_lds_dword v10, s[4:5]\n"
      "global_load_lds_dword v11, s[20:21]\n"
      "L_tc%=:\n"
      "s_lshr_b32 s42, s2, 3\n"
      "s_lshl_b32 s47, s43, 5\n"
      "s_add_u32 s42, s47, s42\n"
      "s_and_b32 s54, s42, -4\n"
      "s_lshl_b32 s47, s42, 1\n"
      "s_and_b32 s47, s47, -4\n"
      "s_load_dword s53, s[14:15], s54\n"
      "s_load_dword s58, s[14:15], s47 offset:0x100\n"
      "s_load_dword s59, s[14:15], s47 offset:0x300\n"
      "s_min_u32 s54, s3, 2\n"
      "s_lshl_b32 s54, s54, 10\n"
      "s_lshr_b32 s10, s42, 1\n"
      "s_lshl_b32 s47, s42, 12\n"
      "s_lshl_b32 s10, s10, 12\n"
      "s_add_u32 s47, s47, s54\n"
      "s_add_u32 s54, s54, s10\n"
      "s_load_dwordx2 s[6:7], s[12:13], s47 offset:0x80400\n"
      "s_add_u32 s47, s47, 0x100000\n"
      "s_load_dwordx2 s[10:11], s[12:13], s54 offset:0x400\n"
      "s_load_dwordx2 s[8:9], s[12:13], s47 offset:0x80400\n"
      : STATE, "=&{s[32:35]}"(s32), "=&{v[8:23]}"(v8), "=&{v[24:27]}"(v24), "=&{v[52:55]}"(v52), "+{v[192:195]}"(t[0]), "+{v[240:243]}"(t[1]), "+{v[196:199]}"(t[2]), "+{v[200:203]}"(t[3]), "+{v[204:207]}"(t[4]), "+{v[208:211]}"(t[5])
      : "{s2}"(blockIdx.x)
      : "memory", "scc", "vcc", "s40", "s41", "s42", "s43", "s44", "s45", "s46", "s47", "s48", "s49", "s50", "s51", "v2", "v3", "v4", "v5", "v6", "v7", "v28", "v29", "v30", "v31", "v32", "v33", "v34", "v35", "v36", "v37", "v38", "v39", "v40", "v41", "v42", "v43", "v44", "v45", "v46", "v47", "v48", "v49", "v50", "v51", "v56", "v57", "v58", "v59", "v60", "v61", "v62", "v63", "v64", "v65", "v66", "v67", "v68", "v69", "v70", "v71", "v72", "v73", "v74", "v75", "v76", "v77", "v78", "v79", "v80", "v81", "v82", "v83", "v84", "v85", "v86", "v87", "v88", "v89", "v90", "v91", "v92", "v93", "v94", "v95", "v96", "v97", "v98", "v99", "v100", "v101", "v102", "v103", "v104", "v105", "v106", "v107", "v108", "v109", "v110", "v111", "v112", "v113", "v114", "v115", "v116", "v117", "v118", "v119", "v120", "v121", "v122", "v123", "v124", "v125", "v126", "v127", "v128", "v129", "v130", "v131", "v132", "v133", "v134", "v135", "v136", "v137", "v138", "v139", "v140", "v141", "v142", "v143", "v144", "v145", "v146", "v147", "v148", "v149", "v150", "v151", "v152", "v153", "v154", "v155", "v156", "v157", "v158", "v159", "v160", "v161", "v162", "v163", "v164", "v165", "v166", "v167", "v168", "v169", "v170", "v171", "v172", "v173", "v174", "v175", "v176", "v177", "v178", "v179", "v180", "v181", "v182", "v183", "v184", "v185", "v186", "v187", "v188", "v189", "v190", "v191", "v212", "v213", "v214", "v215", "v216", "v217", "v218", "v219", "v220", "v221", "v222", "v223", "v224", "v225", "v226", "v227", "v228", "v229", "v230", "v231", "v236", "v237", "v238", "v239", "v244", "v245", "v246", "v247", "v248", "v249", "v250", "v251", "v252", "v253", "v254", "v255", "a0", "a1", "a2", "a3", "a4", "a5", "a6", "a7", "a8", "a9", "a10", "a11", "a12", "a13", "a14", "a15", "a16", "a17", "a18", "a19", "a20", "a21", "a22", "a23", "a24", "a25", "a26", "a27", "a28", "a29", "a30", "a31", "a32", "a33", "a34", "a35", "a36", "a37", "a38", "a39", "a40", "a41", "a42", "a43", "a44", "a45", "a46", "a47", "a48", "a49", "a50", "a51", "a52", "a53", "a54", "a55", "a56", "a57", "a58", "a59", "a60", "a61", "a62", "a63", "a64", "a65", "a66", "a67", "a68", "a69", "a70", "a71", "a72", "a73", "a74", "a75", "a76", "a77", "a78", "a79", "a80", "a81", "a82", "a83", "a84", "a85", "a86", "a87", "a88", "a89", "a90", "a91", "a92", "a93", "a94", "a95", "a96", "a97", "a98", "a99", "a100", "a101", "a102", "a103", "a104", "a105", "a106", "a107", "a108", "a109", "a110", "a111", "a112", "a113", "a114", "a115", "a116", "a117", "a118", "a119", "a120", "a121", "a122", "a123", "a124", "a125", "a126", "a127", "a128", "a129", "a130", "a131", "a132", "a133", "a134", "a135", "a136", "a137", "a138", "a139", "a140", "a141", "a142", "a143", "a144", "a145", "a146", "a147", "a148", "a149", "a150", "a151", "a152", "a153", "a154", "a155", "a156", "a157", "a158", "a159", "a160", "a161", "a162", "a163", "a164", "a165", "a166", "a167", "a168", "a169", "a170", "a171", "a172", "a173", "a174", "a175", "a176", "a177", "a178", "a179", "a180", "a181", "a182", "a183", "a184", "a185", "a186", "a187", "a188", "a189", "a190", "a191", "a192", "a193", "a194", "a195", "a196", "a197", "a198", "a199", "a200", "a201", "a202", "a203", "a204", "a205", "a206", "a207", "a208", "a209", "a210", "a211", "a212", "a213", "a214", "a215", "a216", "a217", "a218", "a219", "a220", "a221", "a222", "a223", "a224", "a225", "a226", "a227", "a228", "a229", "a230", "a231", "a232", "a233", "a234", "a235", "a236", "a237", "a238", "a239", "a240", "a241", "a242", "a243", "a244", "a245", "a246", "a247", "a248", "a249", "a250", "a251", "a252", "a253", "a254", "a255");
  {
    const int lane = tid & 63, ip = lane & 15, fq = lane >> 4, q4 = ip >> 2, p4 = lane & 3;
    const int fo = (ip & 3) * KCP + 2 * ((ip & 12) + 8 * (fq & 1) + 256 * (fq >> 1)), go = L_XI + (15 - ip + 8 * (fq >> 1)) * 96 + 16 * (fq & 1);
    const int exw = L_EX + ip * 128 + (fq ^ (ip & 7)) * 16, z7 = ((tid < 84 ? tid : 83) * 37) >> 8, rowA = 8 * tid;
    t[6][0] = p4 * 64 + (lane >> 5) * 256 + ((lane >> 2) & 7) * 8;
    t[6][1] = L_WR + (fq >> 1) * 4096 + (fq & 1) * 1024 + q4 * 256 + (((2 * w + 1) ^ q4) & 7) * 32 + p4 * 8;
    t[6][2] = lane == 0 ? w * 1024 : 0x80000000u;
    t[6][3] = ((fq & 1 ? w * 64 + 512 : fq * 32 + 128 * w) + ip * 4) | ((unsigned)(fq & (fq >> 1 | w >> 1)) << 31);
    t[7][0] = (ip < 8 ? ip : 8 + p4) * KCP + 56;
    t[7][1] = z7 * (KCP - 56) + (tid < 84 ? tid : 83) * 8;
    t[7][2] = L_XI + 64 * 96 + (tid < 90 ? tid : 89) * 16;
    t[7][3] = L_WR + (fq >> 1) * 4096 + (fq & 1) * 1024 + q4 * 256 + (((2 * w) ^ q4) & 7) * 32 + p4 * 8;
    t[8][0] = L_SC + w * 32 + (lane >> 2) * 2;
    t[8][1] = (2 * (tid >> 4) + ((tid >> 2) & 1)) * (D_ * 128) + (2 * ((tid >> 3) & 1) + ((tid >> 1) & 1)) * 32 + (tid & 1) * 16;
    t[8][2] = (2 * (lane >> 5) + (q4 >> 1)) * (D_ * 128) + (2 * (fq & 1) + (q4 & 1)) * 32 + p4 * 8;
    t[8][3] = ((lane >> 3) ^ (lane >> 4)) & 1;
    t[9][0] = L_QT + (8 * fq + q4) * 160 + p4 * 8 + w * 32;
    t[9][1] = L_QT + (8 * fq + q4) * 160 + p4 * 8 + 128 + (w & 1) * 5120;
    t[9][2] = (16 * w + ip) * RS_ + fq * 16;
    t[9][3] = w < 2 ? (64 + ip) * RS_ + fq * 16 + w * 64 : 0x80000000u;
    t[10][0] = L_SC + (ip & 4 ? 0 : 64) + fq * 8;
    t[10][1] = (tid >> 3) * RS_;
    t[10][2] = L_XI + (tid >> 2) * 96 + (tid & 3) * 16;
    t[10][3] = (fq >> 1) * 256 + (fq & 1) * 64 + ip * 4;
    t[11][0] = fo + (w >> 1) * KCH + (w & 1) * 256;
    t[11][1] = go + (w >> 1) * XR_CH + (w & 1) * 384;
    t[11][2] = fo + 2 * KCH + w * 128;
    t[11][3] = go + 2 * XR_CH + w * 192;
    t[12][0] = exw + w * 8192;
    t[12][1] = L_EX + 32768 + w * 1024 + (lane >> 3) * 128 + ((lane & 7) ^ (lane >> 3)) * 16;
    t[12][2] = L_XI + ((2040 - rowA) >> 5) * 96 + ((2040 - rowA) & 31) * 2;
    t[12][3] = L_XI + 2 * XR_CH + ((252 - 4 * lane) >> 5) * 96 + ((252 - 4 * lane) & 31) * 2;
    t[13][0] = lane < 8 ? lane * 8 : 0x80000000u;
    t[13][1] = (ip >> 1) == 4 * (fq & 1) ? fq * 8 + (ip & 1) * 4 : 0x80000000u;
    t[13][2] = fq * D_ * 128 + q4 * 32 + p4 * 8;
    t[13][3] = (lane < 40 ? lane : 39) * 64;
    t[14][0] = exw + 32768 + w * 4096;
    t[14][1] = (lane & 1) * 2048 + (lane >> 1) * 16;
    t[14][2] = p4 * 64 + (fq == 1 ? 256 * 32 : 0) + (q4 < 3 ? q4 : 4) * 20480;
    t[14][3] = (exw + w * 8192) ^ 64;
    t[15][0] = (tid >> 3) * 5120 + ((tid >> 2) & 1) * 64;
    t[15][1] = fq * 5120 + ((lane >> 3) & 1) * 64;
    t[15][2] = (exw + 32768 + w * 4096) ^ 64;
    t[15][3] = (4 * fq + ((lane >> 2) & 1)) * (D_ * 2) + p4 * 16;
    t[16][0] = L_WD1 + w * 9216 + (4 * fq + q4) * 64 + p4 * 8;
    t[16][1] = (rowA >> 6) * D_ * 128 + ((lane >> 1) & 3) * 32 + (lane & 1) * 16;
    t[16][2] = ((lane >> 1) & 3) == 2 * ((lane >> 3) & 1) ? w * 128 + (lane >> 3) * 8 + (lane & 1) * 4 : 0x80000000u;
    t[16][3] = L_EX + w * 2048 + (lane >> 2) * 128 + ((2 * p4) ^ ((lane >> 2) & 7)) * 16;
    t[17][0] = lane * 8;
    t[17][1] = lane & 4 ? 0x80000000u : (16 * w + 4 * (lane & 7) + fq) * 512 + ((lane >> 3) & 1) * 64;
    t[17][2] = (4 * fq + q4) * 128 + ((2 * fq + (q4 >> 1)) & 3) * 32 + p4 * 8;
    t[17][3] = lane * 16;
  }
  asm volatile(
      "s_mov_b32 s32, s26\n"
      "s_and_b32 s33, s27, 0xffff\n"
      "s_mov_b32 s34, 27262976\n"
      "s_mov_b32 s35, 0x20000\n"
      "v_lshlrev_b32 v54, 4, v0\n"
      "s_sub_u32 s18, s18, 2048\n"
      "s_subb_u32 s19, s19, 0\n"
      "s_add_u32 s22, s20, 60416\n"
      "s_addc_u32 s23, s21, 0\n"
      "v_mov_b32 v66, 0\n"
      "v_mov_b32 v67, 0\n"
      "v_mov_b32 v72, 0\n"
      "v_mov_b32 v73, 0\n"
      "v_mov_b32 v76, 0\n"
      "v_mov_b32 v77, 0\n"
      "v_mov_b32 v82, 0\n"
      "v_mov_b32 v83, 0\n"
      "v_mov_b32 v88, 0\n"
      "v_mov_b32 v89, 0\n"
      "v_mov_b32 v92, 0\n"
      "v_mov_b32 v93, 0\n"
      "v_mov_b32 v98, 0\n"
      "v_mov_b32 v99, 0\n"
      "v_mov_b32 v104, 0\n"
      "v_mov_b32 v105, 0\n"
      "v_mov_b32 v108, 0\n"
      "v_mov_b32 v109, 0\n"
      "v_mov_b32 v114, 0\n"
      "v_mov_b32 v115, 0\n"
      "v_mov_b32 v120, 0\n"
      "v_mov_b32 v121, 0\n"
      "v_mov_b32 v124, 0\n"
      "v_mov_b32 v125, 0\n"
      "v_mov_b32 v34, 0\n"
      "v_mov_b32 v35, 0\n"
      "v_and_b32 v32, 0x3f803f80, v198\n"
      "v_and_b32 v33, 0x3f803f80, v199\n"
      "v_and_b32 v36, 0x3f803f80, v198\n"
      "v_and_b32 v37, 0x3f803f80, v199\n"
      "s_mov_b32 s48, 0xaaaaaaaa\n"
      "s_mov_b32 s49, s48\n"
      "s_mov_b32 s50, 0xcccccccc\n"
      "s_mov_b32 s51, s50\n"
      "s_waitcnt lgkmcnt(0)\n"
      "s_add_u32 s30, s30, 1\n"
      "s_addc_u32 s31, s31, 0\n"
      "s_mul_i32 s24, s30, 10\n"
      "v_mov_b32 v52, s30\n"
      "v_mov_b32 v53, s31\n"
      "v_mov_b32 v34, 0\n"
      "v_mov_b32 v35, 0\n"
      "v_mov_b32 v228, 0\n"
      "v_mov_b32 v229, 0\n"
      "v_mov_b32 v230, 0\n"
      "v_mov_b32 v231, 0\n"
      "s_mov_b32 s44, 0x40000\n"
      "s_mov_b32 s46, 0\n"
      "s_lshl_b32 s42, s3, 12\n"
      "s_add_u32 s42, s42, 147456\n"
      "v_add_u32 v55, s42, v7\n"
      "ds_write_b64 v217, v[228:229]\n"
      "ds_write_b128 v218, v[228:231] offset:0\n"
      "ds_write_b128 v218, v[228:231] offset:7680\n"
      "ds_write_b128 v218, v[228:231] offset:15360\n"
      "s_lshr_b32 s43, s96, 16\n"
      "s_and_b32 s96, s96, 0xffff\n"
      "s_lshr_b32 s42, s93, 8\n"
      "s_and_b32 s42, s42, 0x60\n"
      "v_add_u32 v47, s94, v47\n"
      "s_mov_b32 exec_hi, 0\n"
      "v_add_u32 v58, s43, v58\n"
      "s_mov_b32 exec_hi, -1\n"
      "v_xor_b32 v50, s42, v50\n"
      "v_xor_b32 v29, s97, v29\n"
      "v_mad_u32_u24 v63, v223, s96, v63\n"
      "v_add_u32 v63, s95, v63\n"
      "v_add_u32 v5, s90, v5\n"
      "s_lshl_b32 s42, s99, 4\n"
      "s_lshr_b32 s58, s58, s42\n"
      "s_lshr_b32 s59, s59, s42\n"
      "s_lshr_b32 s42, s93, 8\n"
      "s_and_b32 s42, s42, 16\n"
      "s_lshr_b32 s25, s53, s42\n"
      "s_lshl_b32 s14, s58, 16\n"
      "s_lshl_b32 s15, s59, 16\n"
      "s_lshl_b32 s25, s25, 16\n"
      "s_mov_b32 s36, s12\n"
      "s_and_b32 s37, s13, 0xffff\n"
      "s_add_u32 s40, s60, 33792\n"
      "s_add_u32 s41, s60, 67584\n"
      "s_add_u32 s47, s92, 0x100000\n"
      "s_sub_u32 s54, s92, s93\n"
      "s_lshl_b32 s45, s3, 12\n"
      "v_and_b32 v39, s54, v209\n"
      "v_add_u32 v210, s63, v210\n"
      "v_add_u32 v208, v39, v208\n"
      "v_add_u32 v39, 11264, v210\n"
      "s_waitcnt vmcnt(6)\n"
      "ds_read_b64 v[244:245], v197 offset:0\n"
      "ds_read_b64 v[246:247], v197 offset:128\n"
      "ds_read_b64 v[248:249], v197 offset:256\n"
      "ds_read_b64 v[250:251], v197 offset:384\n"
      "ds_read_b64 v[252:253], v197 offset:512\n"
      "ds_read_b64 a[0:1], v196 offset:0\n"
      "ds_read_b64 a[2:3], v196 offset:32\n"
      "ds_read_b64 a[4:5], v196 offset:64\n"
      "ds_read_b64 a[6:7], v196 offset:96\n"
      "ds_read_b64 a[8:9], v196 offset:128\n"
      "ds_read_b64 a[10:11], v196 offset:160\n"
      "ds_read_b64 a[12:13], v196 offset:192\n"
      "ds_read_b64 a[14:15], v196 offset:224\n"
      "ds_read_b64 a[16:17], v196 offset:256\n"
      "ds_read_b64 a[18:19], v196 offset:288\n"
      "s_waitcnt lgkmcnt(10)\n"
      "ds_read_b64 a[20:21], v196 offset:320\n"
      "ds_read_b64 a[22:23], v196 offset:352\n"
      "ds_read_b64 a[24:25], v196 offset:384\n"
      "v_perm_b32 v64, v245, v245, v242\n"
      "v_perm_b32 v65, v245, v245, v243\n"
      "v_perm_b32 v80, v245, v245, v240\n"
      "v_perm_b32 v81, v245, v245, v241\n"
      "ds_read_b64 a[26:27], v196 offset:416\n"
      "ds_read_b64 a[28:29], v196 offset:448\n"
      "ds_read_b64 a[30:31], v196 offset:480\n"
      "v_perm_b32 v68, v247, v247, v242\n"
      "v_perm_b32 v69, v247, v247, v243\n"
      "v_perm_b32 v84, v247, v247, v240\n"
      "v_perm_b32 v85, v247, v247, v241\n"
      "ds_read_b64 a[32:33], v196 offset:512\n"
      "ds_read_b64 a[34:35], v196 offset:544\n"
      "ds_read_b64 a[36:37], v196 offset:576\n"
      "v_perm_b32 v70, v249, v249, v242\n"
      "v_perm_b32 v71, v249, v249, v243\n"
      "v_perm_b32 v86, v249, v249, v240\n"
      "v_perm_b32 v87, v249, v249, v241\n"
      "ds_read_b64 a[38:39], v196 offset:608\n"
      "ds_read_b64 a[40:41], v196 offset:3072\n"
      "ds_read_b64 a[42:43], v196 offset:3104\n"
      "v_perm_b32 v74, v251, v251, v242\n"
      "v_perm_b32 v75, v251, v251, v243\n"
      "v_perm_b32 v90, v251, v251, v240\n"
      "v_perm_b32 v91, v251, v251, v241\n"
      "ds_read_b64 a[44:45], v196 offset:3136\n"
      "ds_read_b64 a[46:47], v196 offset:3168\n"
      "ds_read_b64 a[48:49], v196 offset:3200\n"
      "v_perm_b32 v78, v253, v253, v242\n"
      "v_perm_b32 v79, v253, v253, v243\n"
      "v_perm_b32 v94, v253, v253, v240\n"
      "v_perm_b32 v95, v253, v253, v241\n"
      "s_waitcnt lgkmcnt(14)\n"
      "v_mfma_f32_16x16x32_bf16 v[128:131], a[0:3], v[64:67], 0\n"
      "v_perm_b32 v96, v244, v244, v242\n"
      "v_perm_b32 v97, v244, v244, v243\n"
      "v_mfma_f32_16x16x32_bf16 v[132:135], a[10:13], v[80:83], 0\n"
      "buffer_load_dwordx4 v[8:11], v54, s[36:39], s92 offen\n"
      "v_perm_b32 v112, v244, v244, v240\n"
      "v_perm_b32 v113, v244, v244, v241\n"
      "v_mfma_f32_16x16x32_bf16 v[140:143], a[0:3], v[66:69], 0\n"
      "v_perm_b32 v100, v246, v246, v242\n"
      "v_perm_b32 v101, v246, v246, v243\n"
      "v_mfma_f32_16x16x32_bf16 v[144:147], a[10:13], v[82:85], 0\n"
      "buffer_load_dwordx4 v[14:17], v54, s[36:39], s47 offen\n"
      "v_perm_b32 v116, v246, v246, v240\n"
      "v_perm_b32 v117, v246, v246, v241\n"
      "v_mfma_f32_16x16x32_bf16 v[152:155], a[4:7], v[70:73], 0\n"
      "v_perm_b32 v102, v248, v248, v242\n"
      "v_perm_b32 v103, v248, v248, v243\n"
      "v_mfma_f32_16x16x32_bf16 v[156:159], a[14:17], v[86:89], 0\n"
      "buffer_load_dwordx4 v[20:23], v54, s[36:39], s93 offen\n"
      "v_perm_b32 v118, v248, v248, v240\n"
      "v_perm_b32 v119, v248, v248, v241\n"
      "v_mfma_f32_16x16x32_bf16 v[164:167], a[4:7], v[72:75], 0\n"
      "v_perm_b32 v106, v250, v250, v242\n"
      "v_perm_b32 v107, v250, v250, v243\n"
      "v_mfma_f32_16x16x32_bf16 v[168:171], a[14:17], v[88:91], 0\n"
      "buffer_load_dwordx2 v[26:27], v208, s[36:39], s93 offen\n"
      "v_perm_b32 v122, v250, v250, v240\n"
      "v_perm_b32 v123, v250, v250, v241\n"
      "v_mfma_f32_16x16x32_bf16 v[176:179], a[6:9], v[76:79], 0\n"
      "v_perm_b32 v110, v252, v252, v242\n"
      "v_perm_b32 v111, v252, v252, v243\n"
      "v_mfma_f32_16x16x32_bf16 v[180:183], a[16:19], v[92:95], 0\n"
      "v_perm_b32 v126, v252, v252, v240\n"
      "v_perm_b32 v127, v252, v252, v241\n"
      "s_waitcnt lgkmcnt(0)\n"
      "v_cvt_pk_bf16_f32 v136, v128, v132\n"
      "v_cvt_pk_bf16_f32 v137, v129, v133\n"
      "v_cvt_pk_bf16_f32 v138, v130, v134\n"
      "v_cvt_pk_bf16_f32 v139, v131, v135\n"
      "v_cvt_pk_bf16_f32 v148, v140, v144\n"
      "v_cvt_pk_bf16_f32 v149, v141, v145\n"
      "v_mfma_f32_16x16x32_bf16 v[128:131], v[192:195], v[136:139], 0\n"
      "v_cvt_pk_bf16_f32 v150, v142, v146\n"
      "v_cvt_pk_bf16_f32 v151, v143, v147\n"
      "v_mfma_f32_16x16x32_bf16 v[132:135], a[20:23], v[96:99], 0\n"
      "s_add_u32 m0, s45, 120896\n"
      "s_nop 0\n"
      "global_load_lds_dwordx4 v210, s[16:17]\n"
      "v_cvt_pk_bf16_f32 v160, v152, v156\n"
      "v_cvt_pk_bf16_f32 v161, v153, v157\n"
      "v_mfma_f32_16x16x32_bf16 v[140:143], v[192:195], v[148:151], 0\n"
      "v_cvt_pk_bf16_f32 v162, v154, v158\n"
      "v_cvt_pk_bf16_f32 v163, v155, v159\n"
      "v_mfma_f32_16x16x32_bf16 v[144:147], a[20:23], v[98:101], 0\n"
      "global_load_lds_dwordx4 v39, s[16:17] offset:1024\n"
      "v_cvt_pk_bf16_f32 v172, v164, v168\n"
      "v_cvt_pk_bf16_f32 v173, v165, v169\n"
      "v_mfma_f32_16x16x32_bf16 v[152:155], v[192:195], v[160:163], 0\n"
      "v_cvt_pk_bf16_f32 v174, v166, v170\n"
      "v_cvt_pk_bf16_f32 v175, v167, v171\n"
      "v_mfma_f32_16x16x32_bf16 v[156:159], a[24:27], v[102:105], 0\n"
      "global_load_lds_dwordx4 v210, s[18:19] offset:2048\n"
      "v_cvt_pk_bf16_f32 v184, v176, v180\n"
      "v_cvt_pk_bf16_f32 v185, v177, v181\n"
      "v_mfma_f32_16x16x32_bf16 v[164:167], v[192:195], v[172:175], 0\n"
      "v_cvt_pk_bf16_f32 v186, v178, v182\n"
      "v_cvt_pk_bf16_f32 v187, v179, v183\n"
      "v_mfma_f32_16x16x32_bf16 v[168:171], a[24:27], v[104:107], 0\n"
      "global_load_lds_dwordx4 v39, s[18:19] offset:3072\n"
      "v_cvt_pk_bf16_f32 v136, v128, v132\n"
      "v_cvt_pk_bf16_f32 v137, v129, v133\n"
      "v_mfma_f32_16x16x32_bf16 v[176:179], v[192:195], v[184:187], 0\n"
      "v_cvt_pk_bf16_f32 v138, v130, v134\n"
      "v_cvt_pk_bf16_f32 v139, v131, v135\n"
      "v_mfma_f32_16x16x32_bf16 v[180:183], a[26:29], v[108:111], 0\n"
      "v_cvt_pk_bf16_f32 v148, v140, v144\n"
      "v_cvt_pk_bf16_f32 v149, v141, v145\n"
      "v_mfma_f32_16x16x32_bf16 v[128:131], v[192:195], v[136:139], 0\n"
      "v_cvt_pk_bf16_f32 v150, v142, v146\n"
      "v_cvt_pk_bf16_f32 v151, v143, v147\n"
      "v_mfma_f32_16x16x32_bf16 v[132:135], a[30:33], v[112:115], 0\n"
      "v_cvt_pk_bf16_f32 v160, v152, v156\n"
      "v_cvt_pk_bf16_f32 v161, v153, v157\n"
      "v_mfma_f32_16x16x32_bf16 v[140:143], v[192:195], v[148:151], 0\n"
      "v_cvt_pk_bf16_f32 v162, v154, v158\n"
      "v_cvt_pk_bf16_f32 v163, v155, v159\n"
      "v_mfma_f32_16x16x32_bf16 v[144:147], a[30:33], v[114:117], 0\n"
      "v_cvt_pk_bf16_f32 v172, v164, v168\n"
      "v_cvt_pk_bf16_f32 v173, v165, v169\n"
      "v_mfma_f32_16x16x32_bf16 v[152:155], v[192:195], v[160:163], 0\n"
      "v_cvt_pk_bf16_f32 v174, v166, v170\n"
      "v_cvt_pk_bf16_f32 v175, v167, v171\n"
      "v_mfma_f32_16x16x32_bf16 v[156:159], a[34:37], v[118:121], 0\n"
      "v_cvt_pk_bf16_f32 v184, v176, v180\n"
      "v_cvt_pk_bf16_f32 v185, v177, v181\n"
      "v_mfma_f32_16x16x32_bf16 v[164:167], v[192:195], v[172:175], 0\n"
      "v_cvt_pk_bf16_f32 v186, v178, v182\n"
      "v_cvt_pk_bf16_f32 v187, v179, v183\n"
      "v_mfma_f32_16x16x32_bf16 v[168:171], a[34:37], v[120:123], 0\n"
      "v_cvt_pk_bf16_f32 v136, v128, v132\n"
      "v_cvt_pk_bf16_f32 v137, v129, v133\n"
      "v_mfma_f32_16x16x32_bf16 v[176:179], v[192:195], v[184:187], 0\n"
      "v_cvt_pk_bf16_f32 v138, v130, v134\n"
      "v_cvt_pk_bf16_f32 v139, v131, v135\n"
      "v_mfma_f32_16x16x32_bf16 v[180:183], a[36:39], v[124:127], 0\n"
      "v_cvt_pk_bf16_f32 v148, v140, v144\n"
      "v_cvt_pk_bf16_f32 v149, v141, v145\n"
      "v_mfma_f32_16x16x32_bf16 v[128:131], v[192:195], v[136:139], 0\n"
      "v_cvt_pk_bf16_f32 v150, v142, v146\n"
      "v_cvt_pk_bf16_f32 v151, v143, v147\n"
      "v_mfma_f32_16x16x32_bf16 v[132:135], a[40:43], v[32:35], 0\n"
      "v_cvt_pk_bf16_f32 v160, v152, v156\n"
      "v_cvt_pk_bf16_f32 v161, v153, v157\n"
      "v_mfma_f32_16x16x32_bf16 v[140:143], v[192:195], v[148:151], 0\n"
      "v_cvt_pk_bf16_f32 v162, v154, v158\n"
      "v_cvt_pk_bf16_f32 v163, v155, v159\n"
      "v_mfma_f32_16x16x32_bf16 v[144:147], a[40:43], v[34:37], 0\n"
      "v_cvt_pk_bf16_f32 v172, v164, v168\n"
      "v_cvt_pk_bf16_f32 v173, v165, v169\n"
      "v_mfma_f32_16x16x32_bf16 v[152:155], v[192:195], v[160:163], 0\n"
      "v_cvt_pk_bf16_f32 v174, v166, v170\n"
      "v_cvt_pk_bf16_f32 v175, v167, v171\n"
      "v_mfma_f32_16x16x32_bf16 v[156:159], a[44:47], v[32:35], 0\n"
      "v_cvt_pk_bf16_f32 v184, v176, v180\n"
      "v_cvt_pk_bf16_f32 v185, v177, v181\n"
      "v_mfma_f32_16x16x32_bf16 v[164:167], v[192:195], v[172:175], 0\n"
      "v_cvt_pk_bf16_f32 v186, v178, v182\n"
      "v_cvt_pk_bf16_f32 v187, v179, v183\n"
      "v_mfma_f32_16x16x32_bf16 v[168:171], a[44:47], v[34:37], 0\n"
      "v_cvt_pk_bf16_f32 v128, 0, v128\n"
      "v_cvt_pk_bf16_f32 v129, 0, v129\n"
      "v_mfma_f32_16x16x32_bf16 v[176:179], v[192:195], v[184:187], 0\n"
      "v_cvt_pk_bf16_f32 v130, 0, v130\n"
      "v_cvt_pk_bf16_f32 v131, 0, v131\n"
      "v_mfma_f32_16x16x32_bf16 v[180:183], a[46:49], v[34:37], 0\n"
      "v_mul_f32 v128, v128, v132\n"
      "v_mul_f32 v129, v129, v133\n"
      "v_mul_f32 v130, v130, v134\n"
      "v_mul_f32 v131, v131, v135\n"
      "v_cvt_pk_bf16_f32 v136, v131, v130\n"
      "v_cvt_pk_bf16_f32 v137, v129, v128\n"
      "v_cvt_pk_bf16_f32 v140, 0, v140\n"
      "v_cvt_pk_bf16_f32 v141, 0, v141\n"
      "v_cvt_pk_bf16_f32 v142, 0, v142\n"
      "v_cvt_pk_bf16_f32 v143, 0, v143\n"
      "ds_write_b64 v200, v[136:137] offset:0\n"
      "v_mul_f32 v140, v140, v144\n"
      "v_mul_f32 v141, v141, v145\n"
      "v_mul_f32 v142, v142, v146\n"
      "v_mul_f32 v143, v143, v147\n"
      "v_cvt_pk_bf16_f32 v148, v143, v142\n"
      "v_cvt_pk_bf16_f32 v149, v141, v140\n"
      "v_cvt_pk_bf16_f32 v152, 0, v152\n"
      "v_cvt_pk_bf16_f32 v153, 0, v153\n"
      "v_cvt_pk_bf16_f32 v154, 0, v154\n"
      "v_cvt_pk_bf16_f32 v155, 0, v155\n"
      "ds_write_b64 v200, v[148:149] offset:2304\n"
      "v_mul_f32 v152, v152, v156\n"
      "v_mul_f32 v153, v153, v157\n"
      "v_mul_f32 v154, v154, v158\n"
      "v_mul_f32 v155, v155, v159\n"
      "v_cvt_pk_bf16_f32 v160, v155, v154\n"
      "v_cvt_pk_bf16_f32 v161, v153, v152\n"
      "v_cvt_pk_bf16_f32 v164, 0, v164\n"
      "v_cvt_pk_bf16_f32 v165, 0, v165\n"
      "v_cvt_pk_bf16_f32 v166, 0, v166\n"
      "v_cvt_pk_bf16_f32 v167, 0, v167\n"
      "ds_write_b64 v200, v[160:161] offset:4608\n"
      "v_mul_f32 v164, v164, v168\n"
      "v_mul_f32 v165, v165, v169\n"
      "v_mul_f32 v166, v166, v170\n"
      "v_mul_f32 v167, v167, v171\n"
      "v_cvt_pk_bf16_f32 v172, v167, v166\n"
      "v_cvt_pk_bf16_f32 v173, v165, v164\n"
      "v_cvt_pk_bf16_f32 v176, 0, v176\n"
      "v_cvt_pk_bf16_f32 v177, 0, v177\n"
      "v_cvt_pk_bf16_f32 v178, 0, v178\n"
      "v_cvt_pk_bf16_f32 v179, 0, v179\n"
      "ds_write_b64 v200, v[172:173] offset:6912\n"
      "v_mul_f32 v176, v176, v180\n"
      "v_mul_f32 v177, v177, v181\n"
      "v_mul_f32 v178, v178, v182\n"
      "v_mul_f32 v179, v179, v183\n"
      "v_cvt_pk_bf16_f32 v184, v179, v178\n"
      "v_cvt_pk_bf16_f32 v185, v177, v176\n"
      "ds_write_b64 v200, v[184:185] offset:9216\n"
      "s_waitcnt lgkmcnt(0)\n"
      "s_barrier\n"
      "ds_read_b128 v[64:67], v201 offset:0\n"
      "ds_read_b128 v[68:71], v201 offset:1152\n"
      "ds_read_b128 v[72:75], v201 offset:2304\n"
      "s_waitcnt lgkmcnt(2)\n"
      "buffer_store_dwordx4 v[64:67], v202, s[32:35], s60 offen offset:0 sc1\n"
      "v_add3_u32 v76, v64, v65, v66\n"
      "v_add_u32 v76, v76, v67\n"
      "s_waitcnt lgkmcnt(1)\n"
      "buffer_store_dwordx4 v[68:71], v202, s[32:35], s40 offen offset:0 sc1\n"
      "v_add3_u32 v77, v68, v69, v70\n"
      "v_add_u32 v77, v77, v71\n"
      "s_waitcnt lgkmcnt(0)\n"
      "s_mov_b32 exec_hi, 0\n"
      "buffer_store_dwordx4 v[72:75], v202, s[32:35], s41 offen offset:0 sc1\n"
      "s_mov_b32 exec_hi, -1\n"
      "v_add3_u32 v78, v72, v73, v74\n"
      "v_add_u32 v78, v78, v75\n"
      "v_add_u32_dpp v76, v76, v76 quad_perm:[1,0,3,2] row_mask:0xf bank_mask:0xf\n"
      "v_add_u32_dpp v77, v77, v77 quad_perm:[1,0,3,2] row_mask:0xf bank_mask:0xf\n"
      "v_add_u32_dpp v78, v78, v78 quad_perm:[1,0,3,2] row_mask:0xf bank_mask:0xf\n"
      "v_add_u32_dpp v76, v76, v76 quad_perm:[2,3,0,1] row_mask:0xf bank_mask:0xf\n"
      "v_add_u32_dpp v77, v77, v77 quad_perm:[2,3,0,1] row_mask:0xf bank_mask:0xf\n"
      "v_add_u32_dpp v78, v78, v78 quad_perm:[2,3,0,1] row_mask:0xf bank_mask:0xf\n"
      "v_add_u32_dpp v76, v76, v76 row_half_mirror row_mask:0xf bank_mask:0xf\n"
      "v_add_u32_dpp v77, v77, v77 row_half_mirror row_mask:0xf bank_mask:0xf\n"
      "v_add_u32_dpp v78, v78, v78 row_half_mirror row_mask:0xf bank_mask:0xf\n"
      "v_cndmask_b32 v76, v76, v77, s[48:49]\n"
      "v_cndmask_b32 v76, v76, v78, s[50:51]\n"
      "v_sub_u32 v76, s30, v76\n"
      "buffer_store_dword v76, v203, s[32:35], s61 offen offset:0 sc1\n"
      "s_waitcnt vmcnt(12)\n"
      "v_lshlrev_b32 v38, 16, v235\n"
      "v_mul_f32 v172, 0xbfb8aa3b, v38\n"
      "v_exp_f32 v172, v172\n"
      "s_nop 0\n"
      "v_add_f32 v172, 1.0, v172\n"
      "v_rcp_f32 v172, v172\n"
      "s_nop 0\n"
      "v_mul_f32 v38, v38, v172\n"
      "v_cvt_pk_bf16_f32 v38, v38, v38\n"
      "ds_write_b16 v204, v38\n"
      "s_waitcnt lgkmcnt(0)\n"
      "s_barrier\n"
      "ds_read_b128 v[144:147], v206 offset:0\n"
      "ds_read_b64_tr_b16 v[148:149], v205 offset:0\n"
      "ds_read_b64_tr_b16 v[150:151], v205 offset:2048\n"
      "ds_read_b128 v[152:155], v206 offset:64\n"
      "ds_read_b64_tr_b16 v[156:157], v205 offset:4096\n"
      "ds_read_b64_tr_b16 v[158:159], v205 offset:6144\n"
      "ds_read_b128 v[160:163], v206 offset:128\n"
      "ds_read_b64_tr_b16 v[164:165], v205 offset:8192\n"
      "ds_read_b64_tr_b16 v[166:167], v205 offset:10240\n"
      "ds_read_b128 v[168:171], v206 offset:192\n"
      "ds_read_b64_tr_b16 v[172:173], v205 offset:12288\n"
      "ds_read_b64_tr_b16 v[174:175], v205 offset:14336\n"
      "ds_read_b128 v[176:179], v206 offset:256\n"
      "ds_read_b64_tr_b16 v[180:181], v205 offset:16384\n"
      "ds_read_b64_tr_b16 v[182:183], v205 offset:18432\n"
      "s_waitcnt lgkmcnt(12)\n"
      "v_mfma_f32_16x16x32_bf16 v[184:187], v[144:147], v[148:151], 0\n"
      "s_waitcnt lgkmcnt(9)\n"
      "v_mfma_f32_16x16x32_bf16 v[184:187], v[152:155], v[156:159], v[184:187]\n"
      "s_waitcnt lgkmcnt(6)\n"
      "v_mfma_f32_16x16x32_bf16 v[184:187], v[160:163], v[164:167], v[184:187]\n"
      "s_waitcnt lgkmcnt(3)\n"
      "v_mfma_f32_16x16x32_bf16 v[184:187], v[168:171], v[172:175], v[184:187]\n"
      "s_waitcnt lgkmcnt(0)\n"
      "v_mfma_f32_16x16x32_bf16 v[184:187], v[176:179], v[180:183], v[184:187]\n"
      "s_nop 7\n"
      "v_mov_b32 v185, s30\n"
      "buffer_store_dwordx2 v[184:185], v207, s[32:35], s62 offen offset:0 sc1\n"
      "s_mul_i32 s42, s3, 2560\n"
      "s_add_u32 m0, s42, 110656\n"
      "s_mov_b32 s36, s4\n"
      "s_and_b32 s37, s5, 0xffff\n"
      "buffer_load_dwordx4 v232, s[36:39], s52 offen lds\n"
      "s_add_u32 m0, m0, 1024\n"
      "s_nop 0\n"
      "buffer_load_dwordx4 v233, s[36:39], s55 offen lds\n"
      "s_add_u32 m0, m0, 1024\n"
      "s_mov_b32 exec_hi, 0\n"
      "buffer_load_dwordx4 v234, s[36:39], s55 offen lds\n"
      "s_mov_b32 exec_hi, -1\n"
      "buffer_load_dwordx4 v[188:191], v211, s[36:39], s66 offen\n"
      "buffer_load_dwordx4 v[228:231], v211, s[36:39], s66 offen offset:512\n"
      "v_and_b32 v2, 0xf8, v4\n"
      "v_sub_u32 v2, v211, v2\n"
      "buffer_load_dwordx2 v[2:3], v2, s[36:39], s66 offen offset:1024\n"
      "v_mov_b32 v12, s6\n"
      "v_mov_b32 v13, s7\n"
      "v_mov_b32 v18, s8\n"
      "v_mov_b32 v19, s9\n"
      "v_mov_b32 v24, s10\n"
      "v_mov_b32 v25, s11\n"
      "v_and_b32 v152, 48, v7\n"
      "v_xor_b32 v152, 48, v152\n"
      "s_waitcnt vmcnt(15)\n"
      "v_lshlrev_b64 v[26:27], v152, v[26:27]\n"
      "v_mov_b32_dpp v12, v8 wave_shl:1 row_mask:0xf bank_mask:0xf\n"
      "v_mov_b32_dpp v13, v9 wave_shl:1 row_mask:0xf bank_mask:0xf\n"
      "v_mov_b32_dpp v18, v14 wave_shl:1 row_mask:0xf bank_mask:0xf\n"
      "v_mov_b32_dpp v19, v15 wave_shl:1 row_mask:0xf bank_mask:0xf\n"
      "v_mov_b32_dpp v24, v20 wave_shl:1 row_mask:0xf bank_mask:0xf\n"
      "v_mov_b32_dpp v25, v21 wave_shl:1 row_mask:0xf bank_mask:0xf\n"
      "ds_write_b128 v54, v[10:13] offset:12544\n"
      "v_alignbit_b32 v128, v9, v8, 16\n"
      "v_alignbit_b32 v129, v10, v9, 16\n"
      "v_alignbit_b32 v130, v11, v10, 16\n"
      "v_alignbit_b32 v131, v12, v11, 16\n"
      "ds_write_b128 v54, v[128:131] offset:64\n"
      "v_alignbit_b32 v132, v10, v9, 16\n"
      "v_alignbit_b32 v133, v11, v10, 16\n"
      "v_alignbit_b32 v134, v12, v11, 16\n"
      "v_alignbit_b32 v135, v13, v12, 16\n"
      "ds_write_b128 v54, v[132:135] offset:8384\n"
      "v_mov_b32 v136, v9\n"
      "v_mov_b32 v137, v10\n"
      "v_mov_b32 v138, v11\n"
      "v_mov_b32 v139, v12\n"
      "ds_write_b128 v54, v[136:139] offset:4224\n"
      "ds_write_b128 v54, v[16:19] offset:29184\n"
      "v_alignbit_b32 v140, v15, v14, 16\n"
      "v_alignbit_b32 v141, v16, v15, 16\n"
      "v_alignbit_b32 v142, v17, v16, 16\n"
      "v_alignbit_b32 v143, v18, v17, 16\n"
      "ds_write_b128 v54, v[140:143] offset:16704\n"
      "v_alignbit_b32 v144, v16, v15, 16\n"
      "v_alignbit_b32 v145, v17, v16, 16\n"
      "v_alignbit_b32 v146, v18, v17, 16\n"
      "v_alignbit_b32 v147, v19, v18, 16\n"
      "ds_write_b128 v54, v[144:147] offset:25024\n"
      "v_mov_b32 v148, v15\n"
      "v_mov_b32 v149, v16\n"
      "v_mov_b32 v150, v17\n"
      "v_mov_b32 v151, v18\n"
      "ds_write_b128 v54, v[148:151] offset:20864\n"
      "ds_write_b128 v54, v[22:25] offset:45824\n"
      "v_alignbit_b32 v128, v21, v20, 16\n"
      "v_alignbit_b32 v129, v22, v21, 16\n"
      "v_alignbit_b32 v130, v23, v22, 16\n"
      "v_alignbit_b32 v131, v24, v23, 16\n"
      "ds_write_b128 v54, v[128:131] offset:33344\n"
      "v_alignbit_b32 v132, v22, v21, 16\n"
      "v_alignbit_b32 v133, v23, v22, 16\n"
      "v_alignbit_b32 v134, v24, v23, 16\n"
      "v_alignbit_b32 v135, v25, v24, 16\n"
      "ds_write_b128 v54, v[132:135] offset:41664\n"
      "v_mov_b32 v136, v21\n"
      "v_mov_b32 v137, v22\n"
      "v_mov_b32 v138, v23\n"
      "v_mov_b32 v139, v24\n"
      "ds_write_b128 v54, v[136:139] offset:37504\n"
      "ds_write_b64 v216, v[26:27]\n"
      "s_mov_b32 vcc_lo, 0x33333333\n"
      "s_mov_b32 vcc_hi, 0x33333333\n"
      "s_waitcnt vmcnt(11)\n"
      "s_barrier\n"
      "ds_read_b64_tr_b16 v[144:145], v219 offset:0\n"
      "ds_read_b64_tr_b16 v[146:147], v219 offset:8192\n"
      "ds_read_b64_tr_b16 v[148:149], v219 offset:2048\n"
      "ds_read_b64_tr_b16 v[150:151], v219 offset:10240\n"
      "ds_read_b64_tr_b16 v[152:153], v213 offset:0\n"
      "ds_read_b64_tr_b16 v[154:155], v213 offset:8192\n"
      "ds_read_b64_tr_b16 v[156:157], v213 offset:2048\n"
      "ds_read_b64_tr_b16 v[158:159], v213 offset:10240\n"
      "s_waitcnt lgkmcnt(0)\n"
      "v_cndmask_b32_dpp v80, v148, v144, vcc quad_perm:[0,1,0,1] row_mask:0xf bank_mask:0xf\n"
      "v_cndmask_b32_dpp v81, v149, v145, vcc quad_perm:[0,1,0,1] row_mask:0xf bank_mask:0xf\n"
      "v_cndmask_b32_dpp v82, v150, v146, vcc quad_perm:[0,1,0,1] row_mask:0xf bank_mask:0xf\n"
      "v_cndmask_b32_dpp v83, v151, v147, vcc quad_perm:[0,1,0,1] row_mask:0xf bank_mask:0xf\n"
      "v_cndmask_b32_dpp v88, v156, v152, vcc quad_perm:[0,1,0,1] row_mask:0xf bank_mask:0xf\n"
      "v_cndmask_b32_dpp v89, v157, v153, vcc quad_perm:[0,1,0,1] row_mask:0xf bank_mask:0xf\n"
      "v_cndmask_b32_dpp v90, v158, v154, vcc quad_perm:[0,1,0,1] row_mask:0xf bank_mask:0xf\n"
      "v_cndmask_b32_dpp v91, v159, v155, vcc quad_perm:[0,1,0,1] row_mask:0xf bank_mask:0xf\n"
      "s_not_b64 vcc, vcc\n"
      "v_cndmask_b32_dpp v84, v144, v148, vcc quad_perm:[2,3,2,3] row_mask:0xf bank_mask:0xf\n"
      "v_cndmask_b32_dpp v85, v145, v149, vcc quad_perm:[2,3,2,3] row_mask:0xf bank_mask:0xf\n"
      "v_cndmask_b32_dpp v86, v146, v150, vcc quad_perm:[2,3,2,3] row_mask:0xf bank_mask:0xf\n"
      "v_cndmask_b32_dpp v87, v147, v151, vcc quad_perm:[2,3,2,3] row_mask:0xf bank_mask:0xf\n"
      "v_cndmask_b32_dpp v92, v152, v156, vcc quad_perm:[2,3,2,3] row_mask:0xf bank_mask:0xf\n"
      "v_cndmask_b32_dpp v93, v153, v157, vcc quad_perm:[2,3,2,3] row_mask:0xf bank_mask:0xf\n"
      "v_cndmask_b32_dpp v94, v154, v158, vcc quad_perm:[2,3,2,3] row_mask:0xf bank_mask:0xf\n"
      "v_cndmask_b32_dpp v95, v155, v159, vcc quad_perm:[2,3,2,3] row_mask:0xf bank_mask:0xf\n"
      "s_waitcnt vmcnt(0)\n"
      "s_lshr_b32 s42, s92, 12\n"
      "s_lshr_b32 s43, s93, 12\n"
      "s_mulk_i32 s42, 4224\n"
      "s_mulk_i32 s43, 4224\n"
      "s_add_u32 s42, s42, 0x100000\n"
      "s_add_u32 s43, s43, 0x100000\n"
      "buffer_load_dwordx4 v[128:131], v54, s[32:35], s42 offen sc1\n"
      "s_add_u32 s42, s42, 1081344\n"
      "buffer_load_dwordx4 v[136:139], v54, s[32:35], s43 offen sc1\n"
      "buffer_load_dwordx4 v[132:135], v54, s[32:35], s42 offen sc1\n"
      "buffer_load_dword v140, v237, s[32:35], s70 offen sc1\n"
      "buffer_load_dword v141, v237, s[32:35], s98 offen sc1\n"
      "buffer_load_dword v142, v237, s[32:35], s71 offen sc1\n"
      "s_barrier\n"
      "ds_read_b64_tr_b16 v[64:65], v224 offset:0\n"
      "ds_read_b64_tr_b16 v[66:67], v224 offset:640\n"
      "ds_read_b64_tr_b16 v[68:69], v224 offset:5120\n"
      "ds_read_b64_tr_b16 v[70:71], v224 offset:5760\n"
      "ds_read_b64_tr_b16 v[72:73], v225 offset:0\n"
      "ds_read_b64_tr_b16 v[74:75], v225 offset:640\n"
      "v_mov_b32 v122, 0\n"
      "v_mov_b32 v123, 0\n"
      "v_mov_b32 v24, 0x3f803f80\n"
      "v_dot2c_f32_bf16 v122, v188, v24\n"
      "v_dot2c_f32_bf16 v123, v188, v188\n"
      "v_dot2c_f32_bf16 v122, v189, v24\n"
      "v_dot2c_f32_bf16 v123, v189, v189\n"
      "v_dot2c_f32_bf16 v122, v190, v24\n"
      "v_dot2c_f32_bf16 v123, v190, v190\n"
      "v_dot2c_f32_bf16 v122, v191, v24\n"
      "v_dot2c_f32_bf16 v123, v191, v191\n"
      "v_dot2c_f32_bf16 v122, v228, v24\n"
      "v_dot2c_f32_bf16 v123, v228, v228\n"
      "v_dot2c_f32_bf16 v122, v229, v24\n"
      "v_dot2c_f32_bf16 v123, v229, v229\n"
      "v_dot2c_f32_bf16 v122, v230, v24\n"
      "v_dot2c_f32_bf16 v123, v230, v230\n"
      "v_dot2c_f32_bf16 v122, v231, v24\n"
      "v_dot2c_f32_bf16 v123, v231, v231\n"
      "v_dot2c_f32_bf16 v122, v2, v24\n"
      "v_dot2c_f32_bf16 v123, v2, v2\n"
      "v_dot2c_f32_bf16 v122, v3, v24\n"
      "v_dot2c_f32_bf16 v123, v3, v3\n"
      "s_waitcnt lgkmcnt(0)\n"
      "v_add3_u32 v76, v64, v65, v66\n"
      "v_add_u32 v76, v76, v67\n"
      "v_add3_u32 v77, v68, v69, v70\n"
      "v_add_f32_dpp v122, v122, v122 quad_perm:[1,0,3,2] row_mask:0xf bank_mask:0xf\n"
      "v_add_u32 v77, v77, v71\n"
      "v_add_f32_dpp v123, v123, v123 quad_perm:[1,0,3,2] row_mask:0xf bank_mask:0xf\n"
      "v_add3_u32 v78, v72, v73, v74\n"
      "v_add_f32_dpp v122, v122, v122 quad_perm:[2,3,0,1] row_mask:0xf bank_mask:0xf\n"
      "v_add_u32 v78, v78, v75\n"
      "v_add_f32_dpp v123, v123, v123 quad_perm:[2,3,0,1] row_mask:0xf bank_mask:0xf\n"
      "v_mov_b32 v79, v78\n"
      "v_add_f32_dpp v122, v122, v122 row_half_mirror row_mask:0xf bank_mask:0xf\n"
      "v_permlane32_swap_b32 v76, v77\n"
      "v_add_f32_dpp v123, v123, v123 row_half_mirror row_mask:0xf bank_mask:0xf\n"
      "v_permlane32_swap_b32 v78, v79\n"
      "v_add_f32_dpp v122, v122, v122 row_mirror row_mask:0xf bank_mask:0xf\n"
      "v_add_u32 v76, v76, v77\n"
      "v_add_f32_dpp v123, v123, v123 row_mirror row_mask:0xf bank_mask:0xf\n"
      "v_add_u32 v78, v78, v79\n"
      "v_mov_b32 v24, v122\n"
      "s_nop 0\n"
      "v_permlane16_swap_b32 v76, v78\n"
      "v_mov_b32 v25, v123\n"
      "v_add_u32 v76, v76, v78\n"
      "v_permlane16_swap_b32 v122, v24\n"
      "v_sub_u32 v76, s30, v76\n"
      "v_permlane16_swap_b32 v123, v25\n"
      "v_add_f32 v122, v122, v24\n"
      "v_add_f32 v123, v123, v25\n"
      "v_mul_f32 v122, 0x3acccccd, v122\n"
      "v_mul_f32 v123, 0x3acccccd, v123\n"
      "v_fma_f32 v123, -v122, v122, v123\n"
      "v_max_f32 v123, 0, v123\n"
      "v_cvt_pk_bf16_f32 v123, 0, v123\n"
      "v_add_f32 v123, 0x3727c5ac, v123\n"
      "v_cvt_pk_bf16_f32 v123, 0, v123\n"
      "v_sqrt_f32 v123, v123\n"
      "v_readlane_b32 s42, v122, 32\n"
      "v_cvt_pk_bf16_f32 v123, 0, v123\n"
      "v_rcp_f32 v24, v123\n"
      "v_mov_b32 v27, s30\n"
      "v_cvt_pk_bf16_f32 v26, v122, s42\n"
      "v_readlane_b32 s42, v24, 32\n"
      "s_nop 1\n"
      "v_mov_b32 v25, s42\n"
      "v_mov_b64 v[8:9], v[80:81]\n"
      "v_mov_b64 v[10:11], v[82:83]\n"
      "v_mov_b64 v[12:13], v[84:85]\n"
      "v_mov_b64 v[14:15], v[86:87]\n"
      "v_mov_b64 v[16:17], v[88:89]\n"
      "v_mov_b64 v[18:19], v[90:91]\n"
      "v_mov_b64 v[20:21], v[92:93]\n"
      "v_mov_b64 v[22:23], v[94:95]\n"
      "v_mov_b32 v2, v212\n"
      "v_mov_b32 v3, v220\n"
      "s_mov_b32 s45, s68\n"
      "s_lshr_b32 s42, s93, 8\n"
      "s_and_b32 s42, s42, 0x60\n"
      "v_xor_b32 v36, s97, v221\n"
      "v_xor_b32 v37, s42, v222\n"
      "s_add_u32 s55, s78, 0x1000000\n"
      "s_mov_b32 s59, s69\n"
      "s_lshr_b32 s53, s92, 12\n"
      "s_mulk_i32 s53, 4224\n"
      "s_add_u32 s53, s53, 0x400000\n"
      "s_add_u32 s54, s53, 1081344\n"
      "buffer_store_dwordx4 v[64:67], v226, s[32:35], s64 offen offset:0 sc1\n"
      "buffer_store_dwordx4 v[68:71], v226, s[32:35], s64 offen offset:64 sc1\n"
      "buffer_store_dwordx4 v[72:75], v227, s[32:35], s64 offen offset:0 sc1\n"
      "buffer_store_dword v76, v215, s[32:35], s65 offen offset:0 sc1\n"
      "buffer_store_dwordx4 v[24:27], v214, s[32:35], s67 offen offset:0 sc1\n"
      "s_waitcnt vmcnt(5)\n"
      "v_add3_u32 v143, v128, v129, v130\n"
      "v_add3_u32 v143, v143, v131, v132\n"
      "v_add3_u32 v143, v143, v133, v134\n"
      "v_add3_u32 v143, v143, v135, v136\n"
      "v_add3_u32 v143, v143, v137, v138\n"
      "v_add_u32 v143, v143, v139\n"
      "v_add3_u32 v140, v140, v141, v142\n"
      "s_mul_i32 s47, s30, 3\n"
      "v_add_u32_dpp v143, v143, v143 quad_perm:[1,0,3,2] row_mask:0xf bank_mask:0xf\n"
      "s_nop 1\n"
      "v_add_u32_dpp v143, v143, v143 quad_perm:[2,3,0,1] row_mask:0xf bank_mask:0xf\n"
      "s_nop 1\n"
      "v_add_u32_dpp v143, v143, v143 row_half_mirror row_mask:0xf bank_mask:0xf\n"
      "v_add_u32 v143, v143, v140\n"
      "v_cmp_ne_u32 vcc, s47, v143\n"
      "s_cbranch_vccnz L_psx_%=\n"
      "L_pbx_%=:\n"
      "ds_write_b128 v238, v[128:131] offset:0\n"
      "ds_write_b128 v238, v[132:135] offset:7680\n"
      "ds_write_b128 v238, v[136:139] offset:15360\n"
      "buffer_store_dwordx4 v[80:83], v7, s[32:35], s72 offen offset:0 sc1\n"
      "buffer_store_dwordx4 v[84:87], v7, s[32:35], s73 offen offset:0 sc1\n"
      "buffer_store_dwordx4 v[88:91], v7, s[32:35], s74 offen offset:0 sc1\n"
      "buffer_store_dwordx4 v[92:95], v7, s[32:35], s75 offen offset:0 sc1\n"
      "s_mul_i32 s42, s3, 9216\n"
      "s_add_u32 m0, s42, 122880\n"
      "s_waitcnt lgkmcnt(0)\n"
      "s_barrier\n"
      "s_cmp_lg_u32 s99, 0\n"
      "s_cbranch_scc1 L_cv_b%=\n"
      "ds_read_b128 v[160:163], v43 offset:4608\n"
      "ds_read_b64 v[112:113], v42 offset:0\n"
      "ds_read_b64 v[114:115], v42 offset:8\n"
      "ds_read_b64 v[148:149], v42 offset:3072\n"
      "ds_read_b64 v[150:151], v42 offset:3080\n"
      "ds_read_b128 v[172:175], v43 offset:0\n"
      "ds_read_b128 v[168:171], v43 offset:1536\n"
      "ds_read_b64 v[124:125], v42 offset:1024\n"
      "ds_read_b64 v[126:127], v42 offset:1032\n"
      "ds_read_b128 v[164:167], v43 offset:3072\n"
      "ds_read_b64 v[136:137], v42 offset:2048\n"
      "ds_read_b64 v[138:139], v42 offset:2056\n"
      "ds_read_b64 v[116:117], v42 offset:32\n"
      "ds_read_b64 v[118:119], v42 offset:40\n"
      "ds_read_b64 v[128:129], v42 offset:1056\n"
      "ds_read_b64 v[130:131], v42 offset:1064\n"
      "ds_read_b64 v[140:141], v42 offset:2080\n"
      "ds_read_b64 v[142:143], v42 offset:2088\n"
      "ds_read_b64 v[152:153], v42 offset:3104\n"
      "ds_read_b64 v[154:155], v42 offset:3112\n"
      "s_waitcnt lgkmcnt(8)\n"
      "v_mfma_f32_16x16x32_bf16 v[96:99], v[112:115], v[160:163], 0\n"
      "ds_read_b128 v[176:179], v43 offset:4640\n"
      "ds_read_b128 v[180:183], v43 offset:3104\n"
      "v_mfma_f32_16x16x32_bf16 v[104:107], v[148:151], v[160:163], 0\n"
      "ds_read_b128 v[184:187], v43 offset:1568\n"
      "v_mfma_f32_16x16x32_bf16 v[104:107], v[112:115], v[172:175], v[104:107]\n"
      "ds_read_b128 v[188:191], v43 offset:32\n"
      "v_mfma_f32_16x16x32_bf16 v[104:107], v[124:127], v[168:171], v[104:107]\n"
      "ds_read_b64 v[120:121], v42 offset:64\n"
      "ds_read_b64 v[122:123], v42 offset:72\n"
      "v_mfma_f32_16x16x32_bf16 v[104:107], v[136:139], v[164:167], v[104:107]\n"
      "ds_read_b64 v[132:133], v42 offset:1088\n"
      "s_waitcnt lgkmcnt(7)\n"
      "v_mfma_f32_16x16x32_bf16 v[100:103], v[116:119], v[160:163], 0\n"
      "ds_read_b64 v[134:135], v42 offset:1096\n"
      "v_mfma_f32_16x16x32_bf16 v[108:111], v[152:155], v[160:163], 0\n"
      "ds_read_b64 v[144:145], v42 offset:2112\n"
      "v_mfma_f32_16x16x32_bf16 v[108:111], v[116:119], v[172:175], v[108:111]\n"
      "ds_read_b64 v[146:147], v42 offset:2120\n"
      "v_mfma_f32_16x16x32_bf16 v[108:111], v[128:131], v[168:171], v[108:111]\n"
      "ds_read_b64 v[156:157], v42 offset:3136\n"
      "v_mfma_f32_16x16x32_bf16 v[108:111], v[140:143], v[164:167], v[108:111]\n"
      "ds_read_b64 v[158:159], v42 offset:3144\n"
      "s_waitcnt lgkmcnt(8)\n"
      "v_mfma_f32_16x16x32_bf16 v[96:99], v[116:119], v[176:179], v[96:99]\n"
      "ds_read_b128 v[160:163], v43 offset:4704\n"
      "ds_read_b128 v[164:167], v43 offset:3168\n"
      "v_mfma_f32_16x16x32_bf16 v[104:107], v[152:155], v[176:179], v[104:107]\n"
      "ds_read_b128 v[168:171], v43 offset:1632\n"
      "v_mfma_f32_16x16x32_bf16 v[104:107], v[116:119], v[188:191], v[104:107]\n"
      "ds_read_b128 v[172:175], v43 offset:96\n"
      "v_mfma_f32_16x16x32_bf16 v[104:107], v[128:131], v[184:187], v[104:107]\n"
      "ds_read_b64 v[112:113], v42 offset:96\n"
      "ds_read_b64 v[114:115], v42 offset:104\n"
      "v_mfma_f32_16x16x32_bf16 v[104:107], v[140:143], v[180:183], v[104:107]\n"
      "ds_read_b64 v[124:125], v42 offset:1120\n"
      "s_waitcnt lgkmcnt(7)\n"
      "v_mfma_f32_16x16x32_bf16 v[100:103], v[120:123], v[176:179], v[100:103]\n"
      "ds_read_b64 v[126:127], v42 offset:1128\n"
      "v_mfma_f32_16x16x32_bf16 v[108:111], v[156:159], v[176:179], v[108:111]\n"
      "ds_read_b64 v[136:137], v42 offset:2144\n"
      "v_mfma_f32_16x16x32_bf16 v[108:111], v[120:123], v[188:191], v[108:111]\n"
      "ds_read_b64 v[138:139], v42 offset:2152\n"
      "v_mfma_f32_16x16x32_bf16 v[108:111], v[132:135], v[184:187], v[108:111]\n"
      "ds_read_b64 v[148:149], v42 offset:3168\n"
      "v_mfma_f32_16x16x32_bf16 v[108:111], v[144:147], v[180:183], v[108:111]\n"
      "ds_read_b64 v[150:151], v42 offset:3176\n"
      "s_waitcnt lgkmcnt(8)\n"
      "v_mfma_f32_16x16x32_bf16 v[96:99], v[120:123], v[160:163], v[96:99]\n"
      "ds_read_b128 v[176:179], v43 offset:4736\n"
      "ds_read_b128 v[180:183], v43 offset:3200\n"
      "v_mfma_f32_16x16x32_bf16 v[104:107], v[156:159], v[160:163], v[104:107]\n"
      "ds_read_b128 v[184:187], v43 offset:1664\n"
      "v_mfma_f32_16x16x32_bf16 v[104:107], v[120:123], v[172:175], v[104:107]\n"
      "ds_read_b128 v[188:191], v43 offset:128\n"
      "v_mfma_f32_16x16x32_bf16 v[104:107], v[132:135], v[168:171], v[104:107]\n"
      "ds_read_b64 v[116:117], v42 offset:128\n"
      "ds_read_b64 v[118:119], v42 offset:136\n"
      "v_mfma_f32_16x16x32_bf16 v[104:107], v[144:147], v[164:167], v[104:107]\n"
      "ds_read_b64 v[128:129], v42 offset:1152\n"
      "s_waitcnt lgkmcnt(7)\n"
      "v_mfma_f32_16x16x32_bf16 v[100:103], v[112:115], v[160:163], v[100:103]\n"
      "ds_read_b64 v[130:131], v42 offset:1160\n"
      "v_mfma_f32_16x16x32_bf16 v[108:111], v[148:151], v[160:163], v[108:111]\n"
      "ds_read_b64 v[140:141], v42 offset:2176\n"
      "v_mfma_f32_16x16x32_bf16 v[108:111], v[112:115], v[172:175], v[108:111]\n"
      "ds_read_b64 v[142:143], v42 offset:2184\n"
      "v_mfma_f32_16x16x32_bf16 v[108:111], v[124:127], v[168:171], v[108:111]\n"
      "ds_read_b64 v[152:153], v42 offset:3200\n"
      "v_mfma_f32_16x16x32_bf16 v[108:111], v[136:139], v[164:167], v[108:111]\n"
      "ds_read_b64 v[154:155], v42 offset:3208\n"
      "s_waitcnt lgkmcnt(8)\n"
      "v_mfma_f32_16x16x32_bf16 v[96:99], v[112:115], v[176:179], v[96:99]\n"
      "ds_read_b128 v[160:163], v41 offset:4608\n"
      "ds_read_b64 v[120:121], v40 offset:0\n"
      "v_mfma_f32_16x16x32_bf16 v[104:107], v[148:151], v[176:179], v[104:107]\n"
      "ds_read_b64 v[122:123], v40 offset:8\n"
      "ds_read_b128 v[164:167], v41 offset:3072\n"
      "v_mfma_f32_16x16x32_bf16 v[104:107], v[112:115], v[188:191], v[104:107]\n"
      "ds_read_b64 v[132:133], v40 offset:1024\n"
      "ds_read_b64 v[134:135], v40 offset:1032\n"
      "v_mfma_f32_16x16x32_bf16 v[104:107], v[124:127], v[184:187], v[104:107]\n"
      "ds_read_b128 v[168:171], v41 offset:1536\n"
      "ds_read_b64 v[144:145], v40 offset:2048\n"
      "v_mfma_f32_16x16x32_bf16 v[104:107], v[136:139], v[180:183], v[104:107]\n"
      "ds_read_b64 v[146:147], v40 offset:2056\n"
      "ds_read_b128 v[172:175], v41 offset:0\n"
      "s_waitcnt lgkmcnt(10)\n"
      "v_mfma_f32_16x16x32_bf16 v[100:103], v[116:119], v[176:179], v[100:103]\n"
      "ds_read_b64 v[156:157], v40 offset:3072\n"
      "v_mfma_f32_16x16x32_bf16 v[108:111], v[152:155], v[176:179], v[108:111]\n"
      "ds_read_b64 v[158:159], v40 offset:3080\n"
      "v_mfma_f32_16x16x32_bf16 v[108:111], v[116:119], v[188:191], v[108:111]\n"
      "v_mfma_f32_16x16x32_bf16 v[108:111], v[128:131], v[184:187], v[108:111]\n"
      "v_mfma_f32_16x16x32_bf16 v[108:111], v[140:143], v[180:183], v[108:111]\n"
      "s_waitcnt lgkmcnt(0)\n"
      "s_branch L_cv_e%=\n"
      "L_cv_b%=:\n"
      "ds_read_b128 v[164:167], v43 offset:3072\n"
      "ds_read_b64 v[112:113], v42 offset:0\n"
      "ds_read_b64 v[114:115], v42 offset:8\n"
      "ds_read_b64 v[124:125], v42 offset:1024\n"
      "ds_read_b64 v[126:127], v42 offset:1032\n"
      "ds_read_b128 v[160:163], v43 offset:4608\n"
      "ds_read_b64 v[136:137], v42 offset:2048\n"
      "ds_read_b64 v[138:139], v42 offset:2056\n"
      "ds_read_b128 v[168:171], v43 offset:1536\n"
      "ds_read_b64 v[116:117], v42 offset:32\n"
      "ds_read_b64 v[118:119], v42 offset:40\n"
      "ds_read_b64 v[128:129], v42 offset:1056\n"
      "ds_read_b64 v[130:131], v42 offset:1064\n"
      "ds_read_b64 v[140:141], v42 offset:2080\n"
      "ds_read_b64 v[142:143], v42 offset:2088\n"
      "s_waitcnt lgkmcnt(6)\n"
      "v_mfma_f32_16x16x32_bf16 v[96:99], v[112:115], v[164:167], 0\n"
      "ds_read_b128 v[176:179], v43 offset:4640\n"
      "v_mfma_f32_16x16x32_bf16 v[104:107], v[124:127], v[164:167], 0\n"
      "ds_read_b128 v[180:183], v43 offset:3104\n"
      "v_mfma_f32_16x16x32_bf16 v[96:99], v[124:127], v[160:163], v[96:99]\n"
      "ds_read_b128 v[184:187], v43 offset:1568\n"
      "v_mfma_f32_16x16x32_bf16 v[104:107], v[136:139], v[160:163], v[104:107]\n"
      "ds_read_b64 v[120:121], v42 offset:64\n"
      "v_mfma_f32_16x16x32_bf16 v[104:107], v[112:115], v[168:171], v[104:107]\n"
      "ds_read_b64 v[122:123], v42 offset:72\n"
      "s_waitcnt lgkmcnt(5)\n"
      "v_mfma_f32_16x16x32_bf16 v[100:103], v[116:119], v[164:167], 0\n"
      "ds_read_b64 v[132:133], v42 offset:1088\n"
      "v_mfma_f32_16x16x32_bf16 v[108:111], v[128:131], v[164:167], 0\n"
      "ds_read_b64 v[134:135], v42 offset:1096\n"
      "v_mfma_f32_16x16x32_bf16 v[100:103], v[128:131], v[160:163], v[100:103]\n"
      "ds_read_b64 v[144:145], v42 offset:2112\n"
      "v_mfma_f32_16x16x32_bf16 v[108:111], v[140:143], v[160:163], v[108:111]\n"
      "ds_read_b64 v[146:147], v42 offset:2120\n"
      "v_mfma_f32_16x16x32_bf16 v[108:111], v[116:119], v[168:171], v[108:111]\n"
      "s_waitcnt lgkmcnt(6)\n"
      "v_mfma_f32_16x16x32_bf16 v[96:99], v[116:119], v[180:183], v[96:99]\n"
      "ds_read_b128 v[160:163], v43 offset:4704\n"
      "v_mfma_f32_16x16x32_bf16 v[104:107], v[128:131], v[180:183], v[104:107]\n"
      "ds_read_b128 v[164:167], v43 offset:3168\n"
      "v_mfma_f32_16x16x32_bf16 v[96:99], v[128:131], v[176:179], v[96:99]\n"
      "ds_read_b128 v[168:171], v43 offset:1632\n"
      "v_mfma_f32_16x16x32_bf16 v[104:107], v[140:143], v[176:179], v[104:107]\n"
      "ds_read_b64 v[112:113], v42 offset:96\n"
      "v_mfma_f32_16x16x32_bf16 v[104:107], v[116:119], v[184:187], v[104:107]\n"
      "ds_read_b64 v[114:115], v42 offset:104\n"
      "s_waitcnt lgkmcnt(5)\n"
      "v_mfma_f32_16x16x32_bf16 v[100:103], v[120:123], v[180:183], v[100:103]\n"
      "ds_read_b64 v[124:125], v42 offset:1120\n"
      "v_mfma_f32_16x16x32_bf16 v[108:111], v[132:135], v[180:183], v[108:111]\n"
      "ds_read_b64 v[126:127], v42 offset:1128\n"
      "v_mfma_f32_16x16x32_bf16 v[100:103], v[132:135], v[176:179], v[100:103]\n"
      "ds_read_b64 v[136:137], v42 offset:2144\n"
      "v_mfma_f32_16x16x32_bf16 v[108:111], v[144:147], v[176:179], v[108:111]\n"
      "ds_read_b64 v[138:139], v42 offset:2152\n"
      "v_mfma_f32_16x16x32_bf16 v[108:111], v[120:123], v[184:187], v[108:111]\n"
      "s_waitcnt lgkmcnt(6)\n"
      "v_mfma_f32_16x16x32_bf16 v[96:99], v[120:123], v[164:167], v[96:99]\n"
      "ds_read_b128 v[176:179], v43 offset:4736\n"
      "v_mfma_f32_16x16x32_bf16 v[104:107], v[132:135], v[164:167], v[104:107]\n"
      "ds_read_b128 v[180:183], v43 offset:3200\n"
      "v_mfma_f32_16x16x32_bf16 v[96:99], v[132:135], v[160:163], v[96:99]\n"
      "ds_read_b128 v[184:187], v43 offset:1664\n"
      "v_mfma_f32_16x16x32_bf16 v[104:107], v[144:147], v[160:163], v[104:107]\n"
      "ds_read_b64 v[116:117], v42 offset:128\n"
      "v_mfma_f32_16x16x32_bf16 v[104:107], v[120:123], v[168:171], v[104:107]\n"
      "ds_read_b64 v[118:119], v42 offset:136\n"
      "s_waitcnt lgkmcnt(5)\n"
      "v_mfma_f32_16x16x32_bf16 v[100:103], v[112:115], v[164:167], v[100:103]\n"
      "ds_read_b64 v[128:129], v42 offset:1152\n"
      "v_mfma_f32_16x16x32_bf16 v[108:111], v[124:127], v[164:167], v[108:111]\n"
      "ds_read_b64 v[130:131], v42 offset:1160\n"
      "v_mfma_f32_16x16x32_bf16 v[100:103], v[124:127], v[160:163], v[100:103]\n"
      "ds_read_b64 v[140:141], v42 offset:2176\n"
      "v_mfma_f32_16x16x32_bf16 v[108:111], v[136:139], v[160:163], v[108:111]\n"
      "ds_read_b64 v[142:143], v42 offset:2184\n"
      "v_mfma_f32_16x16x32_bf16 v[108:111], v[112:115], v[168:171], v[108:111]\n"
      "s_waitcnt lgkmcnt(6)\n"
      "v_mfma_f32_16x16x32_bf16 v[96:99], v[112:115], v[180:183], v[96:99]\n"
      "ds_read_b128 v[160:163], v41 offset:4608\n"
      "ds_read_b64 v[120:121], v40 offset:0\n"
      "v_mfma_f32_16x16x32_bf16 v[104:107], v[124:127], v[180:183], v[104:107]\n"
      "ds_read_b64 v[122:123], v40 offset:8\n"
      "ds_read_b128 v[164:167], v41 offset:3072\n"
      "v_mfma_f32_16x16x32_bf16 v[96:99], v[124:127], v[176:179], v[96:99]\n"
      "ds_read_b64 v[132:133], v40 offset:1024\n"
      "ds_read_b64 v[134:135], v40 offset:1032\n"
      "v_mfma_f32_16x16x32_bf16 v[104:107], v[136:139], v[176:179], v[104:107]\n"
      "ds_read_b128 v[168:171], v41 offset:1536\n"
      "ds_read_b64 v[144:145], v40 offset:2048\n"
      "v_mfma_f32_16x16x32_bf16 v[104:107], v[112:115], v[184:187], v[104:107]\n"
      "ds_read_b64 v[146:147], v40 offset:2056\n"
      "ds_read_b128 v[172:175], v41 offset:0\n"
      "s_waitcnt lgkmcnt(10)\n"
      "v_mfma_f32_16x16x32_bf16 v[100:103], v[116:119], v[180:183], v[100:103]\n"
      "ds_read_b64 v[156:157], v40 offset:3072\n"
      "v_mfma_f32_16x16x32_bf16 v[108:111], v[128:131], v[180:183], v[108:111]\n"
      "ds_read_b64 v[158:159], v40 offset:3080\n"
      "v_mfma_f32_16x16x32_bf16 v[100:103], v[128:131], v[176:179], v[100:103]\n"
      "v_mfma_f32_16x16x32_bf16 v[108:111], v[140:143], v[176:179], v[108:111]\n"
      "v_mfma_f32_16x16x32_bf16 v[108:111], v[116:119], v[184:187], v[108:111]\n"
      "s_waitcnt lgkmcnt(0)\n"
      "L_cv_e%=:\n"
      "ds_read_b64 v[112:113], v40 offset:32\n"
      "ds_read_b64 v[114:115], v40 offset:40\n"
      "s_add_u32 s8, s26, s91\n"
      "s_addc_u32 s9, s27, 0\n"
      "v_mfma_f32_16x16x32_bf16 v[64:67], v[120:123], v[160:163], 0\n"
      "global_load_lds_dwordx4 v63, s[20:21]\n"
      "ds_read_b64 v[124:125], v40 offset:1056\n"
      "v_mfma_f32_16x16x32_bf16 v[72:75], v[120:123], v[164:167], 0\n"
      "ds_read_b64 v[126:127], v40 offset:1064\n"
      "ds_read_b64 v[136:137], v40 offset:2080\n"
      "v_mfma_f32_16x16x32_bf16 v[80:83], v[120:123], v[168:171], 0\n"
      "ds_read_b64 v[138:139], v40 offset:2088\n"
      "v_mfma_f32_16x16x32_bf16 v[88:91], v[120:123], v[172:175], 0\n"
      "ds_read_b64 v[148:149], v40 offset:3104\n"
      "v_mfma_f32_16x16x32_bf16 v[72:75], v[132:135], v[160:163], v[72:75]\n"
      "ds_read_b64 v[150:151], v40 offset:3112\n"
      "v_mfma_f32_16x16x32_bf16 v[80:83], v[132:135], v[164:167], v[80:83]\n"
      "ds_write_b128 v56, v[96:99]\n"
      "ds_read_b128 v[176:179], v41 offset:4640\n"
      "ds_read_b128 v[180:183], v41 offset:3104\n"
      "ds_read_b128 v[184:187], v41 offset:1568\n"
      "v_mfma_f32_16x16x32_bf16 v[88:91], v[132:135], v[168:171], v[88:91]\n"
      "ds_read_b128 v[188:191], v41 offset:32\n"
      "v_mfma_f32_16x16x32_bf16 v[80:83], v[144:147], v[160:163], v[80:83]\n"
      "ds_read_b64 v[116:117], v40 offset:64\n"
      "v_mfma_f32_16x16x32_bf16 v[88:91], v[144:147], v[164:167], v[88:91]\n"
      "global_load_lds_dwordx4 v63, s[22:23] offset:1024\n"
      "v_mfma_f32_16x16x32_bf16 v[88:91], v[156:159], v[160:163], v[88:91]\n"
      "ds_write_b128 v56, v[104:107] offset:2048\n"
      "ds_read_b64 v[118:119], v40 offset:72\n"
      "ds_read_b64 v[128:129], v40 offset:1088\n"
      "ds_read_b64 v[130:131], v40 offset:1096\n"
      "s_waitcnt lgkmcnt(10)\n"
      "v_mfma_f32_16x16x32_bf16 v[68:71], v[112:115], v[160:163], 0\n"
      "ds_read_b64 v[140:141], v40 offset:2112\n"
      "v_mfma_f32_16x16x32_bf16 v[76:79], v[112:115], v[164:167], 0\n"
      "ds_read_b64 v[142:143], v40 offset:2120\n"
      "v_mfma_f32_16x16x32_bf16 v[84:87], v[112:115], v[168:171], 0\n"
      "ds_read_b64 v[152:153], v40 offset:3136\n"
      "s_load_dwordx16 s[60:75], s[56:57], 0xc0\n"
      "v_mfma_f32_16x16x32_bf16 v[92:95], v[112:115], v[172:175], 0\n"
      "ds_write_b128 v62, v[100:103]\n"
      "ds_read_b64 v[154:155], v40 offset:3144\n"
      "s_load_dwordx4 s[92:95], s[56:57], 0xa0\n"
      "v_mfma_f32_16x16x32_bf16 v[76:79], v[124:127], v[160:163], v[76:79]\n"
      "v_mfma_f32_16x16x32_bf16 v[84:87], v[124:127], v[164:167], v[84:87]\n"
      "v_mfma_f32_16x16x32_bf16 v[92:95], v[124:127], v[168:171], v[92:95]\n"
      "v_mfma_f32_16x16x32_bf16 v[84:87], v[136:139], v[160:163], v[84:87]\n"
      "ds_write_b128 v62, v[108:111] offset:2048\n"
      "v_mfma_f32_16x16x32_bf16 v[92:95], v[136:139], v[164:167], v[92:95]\n"
      "v_mfma_f32_16x16x32_bf16 v[92:95], v[148:151], v[160:163], v[92:95]\n"
      "s_waitcnt lgkmcnt(11)\n"
      "v_mfma_f32_16x16x32_bf16 v[64:67], v[112:115], v[176:179], v[64:67]\n"
      "buffer_load_dwordx2 v[254:255], v2, s[32:35], s45 offen sc1\n"
      "s_waitcnt vmcnt(3)\n"
      "v_mfma_f32_16x16x32_bf16 v[72:75], v[112:115], v[180:183], v[72:75]\n"
      "buffer_store_dwordx2 v[52:53], v48, s[32:35], s79 offen sc1\n"
      "v_mfma_f32_16x16x32_bf16 v[80:83], v[112:115], v[184:187], v[80:83]\n"
      "ds_read_b128 v[160:163], v41 offset:4704\n"
      "v_mfma_f32_16x16x32_bf16 v[88:91], v[112:115], v[188:191], v[88:91]\n"
      "ds_read_b128 v[164:167], v41 offset:3168\n"
      "v_mfma_f32_16x16x32_bf16 v[72:75], v[124:127], v[176:179], v[72:75]\n"
      "ds_read_b128 v[168:171], v41 offset:1632\n"
      "v_mfma_f32_16x16x32_bf16 v[80:83], v[124:127], v[180:183], v[80:83]\n"
      "ds_read_b128 v[172:175], v41 offset:96\n"
      "v_mfma_f32_16x16x32_bf16 v[88:91], v[124:127], v[184:187], v[88:91]\n"
      "s_waitcnt lgkmcnt(4)\n"
      "s_barrier\n"
      "v_mfma_f32_16x16x32_bf16 v[80:83], v[136:139], v[176:179], v[80:83]\n"
      "ds_read_b128 v[96:99], v45 offset:0\n"
      "v_mfma_f32_16x16x32_bf16 v[88:91], v[136:139], v[180:183], v[88:91]\n"
      "ds_read_b128 v[100:103], v45 offset:4096\n"
      "v_mfma_f32_16x16x32_bf16 v[88:91], v[148:151], v[176:179], v[88:91]\n"
      "ds_read_b128 v[104:107], v45 offset:8192\n"
      "v_mfma_f32_16x16x32_bf16 v[68:71], v[116:119], v[176:179], v[68:71]\n"
      "ds_read_b128 v[108:111], v45 offset:12288\n"
      "v_mfma_f32_16x16x32_bf16 v[76:79], v[116:119], v[180:183], v[76:79]\n"
      "ds_read_b64 v[202:203], v47\n"
      "v_mfma_f32_16x16x32_bf16 v[84:87], v[116:119], v[184:187], v[84:87]\n"
      "ds_read_b64 v[120:121], v40 offset:96\n"
      "v_mfma_f32_16x16x32_bf16 v[92:95], v[116:119], v[188:191], v[92:95]\n"
      "ds_read_b64 v[122:123], v40 offset:104\n"
      "v_mfma_f32_16x16x32_bf16 v[76:79], v[128:131], v[176:179], v[76:79]\n"
      "ds_read_b64 v[132:133], v40 offset:1120\n"
      "v_mfma_f32_16x16x32_bf16 v[84:87], v[128:131], v[180:183], v[84:87]\n"
      "ds_read_b64 v[134:135], v40 offset:1128\n"
      "v_mfma_f32_16x16x32_bf16 v[92:95], v[128:131], v[184:187], v[92:95]\n"
      "buffer_load_dwordx2 v[200:201], v4, s[32:35], s80 offen offset:0 sc1\n"
      "s_waitcnt lgkmcnt(4)\n"
      "v_mfma_f32_16x16x32_bf16 v[84:87], v[140:143], v[176:179], v[84:87]\n"
      "ds_read_b64 v[144:145], v40 offset:2144\n"
      "v_add_f32 v96, v96, v100\n"
      "v_mfma_f32_16x16x32_bf16 v[92:95], v[140:143], v[180:183], v[92:95]\n"
      "buffer_load_dword v250, v61, s[32:35], s85 offen offset:0 sc1\n"
      "v_add_f32 v97, v97, v101\n"
      "v_mfma_f32_16x16x32_bf16 v[92:95], v[152:155], v[176:179], v[92:95]\n"
      "ds_read_b64 v[146:147], v40 offset:2152\n"
      "v_mfma_f32_16x16x32_bf16 v[64:67], v[116:119], v[160:163], v[64:67]\n"
      "buffer_load_dwordx4 v[224:227], v57, s[32:35], s82 offen offset:0 sc1\n"
      "v_add_f32 v98, v98, v102\n"
      "v_mfma_f32_16x16x32_bf16 v[72:75], v[116:119], v[164:167], v[72:75]\n"
      "ds_read_b64 v[156:157], v40 offset:3168\n"
      "v_add_f32 v99, v99, v103\n"
      "v_mfma_f32_16x16x32_bf16 v[80:83], v[116:119], v[168:171], v[80:83]\n"
      "ds_read_b64 v[158:159], v40 offset:3176\n"
      "v_add_f32 v104, v104, v108\n"
      "v_mfma_f32_16x16x32_bf16 v[88:91], v[116:119], v[172:175], v[88:91]\n"
      "ds_read_b128 v[176:179], v41 offset:4736\n"
      "v_add_f32 v105, v105, v109\n"
      "v_mfma_f32_16x16x32_bf16 v[72:75], v[128:131], v[160:163], v[72:75]\n"
      "buffer_load_dwordx4 v[228:231], v57, s[32:35], s82 offen offset:1024 sc1\n"
      "v_add_f32 v106, v106, v110\n"
      "v_mfma_f32_16x16x32_bf16 v[80:83], v[128:131], v[164:167], v[80:83]\n"
      "ds_read_b128 v[180:183], v41 offset:3200\n"
      "v_add_f32 v107, v107, v111\n"
      "v_mfma_f32_16x16x32_bf16 v[88:91], v[128:131], v[168:171], v[88:91]\n"
      "ds_read_b128 v[184:187], v41 offset:1664\n"
      "v_add_f32 v96, v96, v104\n"
      "v_mfma_f32_16x16x32_bf16 v[80:83], v[140:143], v[160:163], v[80:83]\n"
      "ds_read_b128 v[188:191], v41 offset:128\n"
      "v_add_f32 v97, v97, v105\n"
      "v_mfma_f32_16x16x32_bf16 v[88:91], v[140:143], v[164:167], v[88:91]\n"
      "buffer_load_dwordx4 v[192:195], v54, s[32:35], s53 offen offset:0 sc1\n"
      "v_add_f32 v98, v98, v106\n"
      "v_mfma_f32_16x16x32_bf16 v[88:91], v[152:155], v[160:163], v[88:91]\n"
      "ds_read_b64 v[112:113], v40 offset:128\n"
      "s_waitcnt lgkmcnt(5)\n"
      "v_mfma_f32_16x16x32_bf16 v[68:71], v[120:123], v[160:163], v[68:71]\n"
      "ds_read_b64 v[114:115], v40 offset:136\n"
      "v_add_f32 v99, v99, v107\n"
      "v_mfma_f32_16x16x32_bf16 v[76:79], v[120:123], v[164:167], v[76:79]\n"
      "ds_read_b64 v[124:125], v40 offset:1152\n"
      "v_cvt_pk_bf16_f32 v96, 0, v96\n"
      "v_mfma_f32_16x16x32_bf16 v[84:87], v[120:123], v[168:171], v[84:87]\n"
      "buffer_load_dwordx4 v[196:199], v54, s[32:35], s54 offen offset:0 sc1\n"
      "v_cvt_pk_bf16_f32 v97, 0, v97\n"
      "v_mfma_f32_16x16x32_bf16 v[92:95], v[120:123], v[172:175], v[92:95]\n"
      "ds_read_b64 v[126:127], v40 offset:1160\n"
      "v_cvt_pk_bf16_f32 v98, 0, v98\n"
      "v_mfma_f32_16x16x32_bf16 v[76:79], v[132:135], v[160:163], v[76:79]\n"
      "ds_read_b64 v[136:137], v40 offset:2176\n"
      "v_cvt_pk_bf16_f32 v99, 0, v99\n"
      "v_mfma_f32_16x16x32_bf16 v[84:87], v[132:135], v[164:167], v[84:87]\n"
      "ds_read_b64 v[138:139], v40 offset:2184\n"
      "v_and_b32 v100, 0xffff0000, v203\n"
      "v_mfma_f32_16x16x32_bf16 v[92:95], v[132:135], v[168:171], v[92:95]\n"
      "buffer_load_dword v248, v60, s[32:35], s84 offen offset:0 sc1\n"
      "v_lshlrev_b32 v101, 16, v203\n"
      "v_mfma_f32_16x16x32_bf16 v[84:87], v[144:147], v[160:163], v[84:87]\n"
      "ds_read_b64 v[148:149], v40 offset:3200\n"
      "v_and_b32 v102, 0xffff0000, v202\n"
      "v_mfma_f32_16x16x32_bf16 v[92:95], v[144:147], v[164:167], v[92:95]\n"
      "ds_read_b64 v[150:151], v40 offset:3208\n"
      "v_lshlrev_b32 v103, 16, v202\n"
      "v_mfma_f32_16x16x32_bf16 v[92:95], v[156:159], v[160:163], v[92:95]\n"
      "s_lshr_b32 s58, s3, 1\n"
      "s_mul_i32 s58, s58, 0x3f800000\n"
      "s_waitcnt lgkmcnt(8)\n"
      "v_mfma_f32_16x16x32_bf16 v[64:67], v[120:123], v[176:179], v[64:67]\n"
      "buffer_load_dword v249, v60, s[32:35], s84 offen offset:2048 sc1\n"
      "s_waitcnt vmcnt(9)\n"
      "v_cmp_ne_u32 vcc, s30, v255\n"
      "v_mfma_f32_16x16x32_bf16 v[72:75], v[120:123], v[180:183], v[72:75]\n"
      "ds_read_b128 v[160:163], v41 offset:4800\n"
      "v_mul_f32 v100, s25, v100\n"
      "v_mfma_f32_16x16x32_bf16 v[80:83], v[120:123], v[184:187], v[80:83]\n"
      "ds_read_b128 v[164:167], v41 offset:3264\n"
      "s_cbranch_vccnz L_cm_s%=\n"
      "L_cm_r%=:\n"
      "v_add_f32_dpp v254, v254, v254 quad_perm:[1,0,3,2] row_mask:0xf bank_mask:0xf\n"
      "v_mfma_f32_16x16x32_bf16 v[88:91], v[120:123], v[188:191], v[88:91]\n"
      "ds_read_b128 v[168:171], v41 offset:1728\n"
      "v_mul_f32 v101, s25, v101\n"
      "v_mfma_f32_16x16x32_bf16 v[72:75], v[132:135], v[176:179], v[72:75]\n"
      "buffer_load_dwordx4 v[208:211], v55, s[32:35], 0 offen offset:0 sc1\n"
      "v_add_f32_dpp v254, v254, v254 quad_perm:[2,3,0,1] row_mask:0xf bank_mask:0xf\n"
      "v_mfma_f32_16x16x32_bf16 v[80:83], v[132:135], v[180:183], v[80:83]\n"
      "ds_read_b128 v[172:175], v41 offset:192\n"
      "v_cvt_pk_bf16_f32 v254, 0, v254\n"
      "v_mfma_f32_16x16x32_bf16 v[88:91], v[132:135], v[184:187], v[88:91]\n"
      "ds_read_b64 v[116:117], v40 offset:160\n"
      "v_add_f32 v254, s58, v254\n"
      "v_mfma_f32_16x16x32_bf16 v[80:83], v[144:147], v[176:179], v[80:83]\n"
      "ds_read_b64 v[118:119], v40 offset:168\n"
      "v_cvt_pk_bf16_f32 v254, v254, v254\n"
      "v_mfma_f32_16x16x32_bf16 v[88:91], v[144:147], v[180:183], v[88:91]\n"
      "buffer_load_dwordx4 v[212:215], v55, s[32:35], 0 offen offset:1024 sc1\n"
      "ds_write_b16 v3, v254\n"
      "ds_read_b64 v[128:129], v40 offset:1184\n"
      "v_mul_f32 v102, s25, v102\n"
      "v_mfma_f32_16x16x32_bf16 v[88:91], v[156:159], v[176:179], v[88:91]\n"
      "ds_read_b64 v[130:131], v40 offset:1192\n"
      "s_waitcnt lgkmcnt(9)\n"
      "v_mfma_f32_16x16x32_bf16 v[68:71], v[112:115], v[176:179], v[68:71]\n"
      "ds_read_b64 v[140:141], v40 offset:2208\n"
      "v_mfma_f32_16x16x32_bf16 v[76:79], v[112:115], v[180:183], v[76:79]\n"
      "ds_read_b64_tr_b16 v[240:241], v28 offset:0\n"
      "v_mul_f32 v103, s25, v103\n"
      "v_mfma_f32_16x16x32_bf16 v[84:87], v[112:115], v[184:187], v[84:87]\n"
      "buffer_load_dwordx4 v[216:219], v55, s[32:35], 0 offen offset:2048 sc1\n"
      "v_cvt_pk_bf16_f32 v100, 0, v100\n"
      "v_mfma_f32_16x16x32_bf16 v[92:95], v[112:115], v[188:191], v[92:95]\n"
      "ds_read_b64_tr_b16 v[242:243], v28 offset:1024\n"
      "s_waitcnt lgkmcnt(5)\n"
      "s_barrier\n"
      "v_mfma_f32_16x16x32_bf16 v[76:79], v[124:127], v[176:179], v[76:79]\n"
      "ds_read2_b64 v[24:27], v236 offset0:0 offset1:4\n"
      "v_cvt_pk_bf16_f32 v101, 0, v101\n"
      "v_mfma_f32_16x16x32_bf16 v[84:87], v[124:127], v[180:183], v[84:87]\n"
      "ds_read_b64_tr_b16 v[244:245], v28 offset:32\n"
      "v_cvt_pk_bf16_f32 v102, 0, v102\n"
      "v_mfma_f32_16x16x32_bf16 v[92:95], v[124:127], v[184:187], v[92:95]\n"
      "buffer_load_dwordx4 v[220:223], v55, s[32:35], 0 offen offset:3072 sc1\n"
      "v_cvt_pk_bf16_f32 v103, 0, v103\n"
      "v_mfma_f32_16x16x32_bf16 v[84:87], v[136:139], v[176:179], v[84:87]\n"
      "ds_read_b64_tr_b16 v[246:247], v28 offset:1056\n"
      "v_add_f32 v96, v96, v100\n"
      "v_mfma_f32_16x16x32_bf16 v[92:95], v[136:139], v[180:183], v[92:95]\n"
      "ds_read_b64 v[142:143], v40 offset:2216\n"
      "s_mov_b32 s48, 0\n"
      "s_mov_b32 s49, -1\n"
      "v_mfma_f32_16x16x32_bf16 v[92:95], v[148:151], v[176:179], v[92:95]\n"
      "ds_read_b64 v[152:153], v40 offset:3232\n"
      "s_waitcnt lgkmcnt(4)\n"
      "v_mfma_f32_16x16x32_bf16 v[64:67], v[112:115], v[160:163], v[64:67]\n"
      "buffer_load_dwordx2 v[204:205], v58, s[32:35], s83 offen offset:0 sc1\n"
      "v_mfma_f32_16x16x32_bf16 v[232:235], v[24:27], v[8:11], 0\n"
      "v_mfma_f32_16x16x32_bf16 v[72:75], v[112:115], v[164:167], v[72:75]\n"
      "ds_read_b64 v[154:155], v40 offset:3240\n"
      "v_mfma_f32_16x16x32_bf16 v[8:11], v[24:27], v[12:15], 0\n"
      "v_mfma_f32_16x16x32_bf16 v[80:83], v[112:115], v[168:171], v[80:83]\n"
      "ds_read_b128 v[176:179], v41 offset:4832\n"
      "v_mfma_f32_16x16x32_bf16 v[12:15], v[24:27], v[16:19], 0\n"
      "v_mfma_f32_16x16x32_bf16 v[88:91], v[112:115], v[172:175], v[88:91]\n"
      "ds_read_b128 v[180:183], v41 offset:3296\n"
      "v_mfma_f32_16x16x32_bf16 v[16:19], v[24:27], v[20:23], 0\n"
      "v_mfma_f32_16x16x32_bf16 v[72:75], v[124:127], v[160:163], v[72:75]\n"
      "ds_read_b128 v[184:187], v41 offset:1760\n"
      "s_waitcnt lgkmcnt(6)\n"
      "v_mfma_f32_16x16x32_bf16 v[80:83], v[124:127], v[164:167], v[80:83]\n"
      "ds_read_b128 v[188:191], v41 offset:224\n"
      "v_add_f32 v97, v97, v101\n"
      "v_mfma_f32_16x16x32_bf16 v[88:91], v[124:127], v[168:171], v[88:91]\n"
      "v_cndmask_b32 v206, v232, v12, s[48:49]\n"
      "v_cndmask_b32 v207, v8, v16, s[48:49]\n"
      "v_mfma_f32_16x16x32_bf16 v[80:83], v[136:139], v[160:163], v[80:83]\n"
      "ds_read_b64 v[120:121], v40 offset:192\n"
      "v_add_f32 v98, v98, v102\n"
      "v_mfma_f32_16x16x32_bf16 v[88:91], v[136:139], v[164:167], v[88:91]\n"
      "ds_read_b64 v[122:123], v40 offset:200\n"
      "v_add_f32 v99, v99, v103\n"
      "v_mfma_f32_16x16x32_bf16 v[88:91], v[148:151], v[160:163], v[88:91]\n"
      "ds_read_b64 v[132:133], v40 offset:1216\n"
      "s_waitcnt lgkmcnt(7)\n"
      "v_mfma_f32_16x16x32_bf16 v[68:71], v[116:119], v[160:163], v[68:71]\n"
      "ds_read_b64 v[134:135], v40 offset:1224\n"
      "v_cvt_pk_bf16_f32 v96, 0, v96\n"
      "v_mfma_f32_16x16x32_bf16 v[76:79], v[116:119], v[164:167], v[76:79]\n"
      "ds_read_b64 v[144:145], v40 offset:2240\n"
      "v_cvt_pk_bf16_f32 v97, 0, v97\n"
      "v_mfma_f32_16x16x32_bf16 v[84:87], v[116:119], v[168:171], v[84:87]\n"
      "buffer_store_dwordx4 v[240:243], v7, s[32:35], s86 offen sc1\n"
      "v_cvt_pk_bf16_f32 v98, 0, v98\n"
      "v_mfma_f32_16x16x32_bf16 v[92:95], v[116:119], v[172:175], v[92:95]\n"
      "ds_read_b64 v[146:147], v40 offset:2248\n"
      "v_cvt_pk_bf16_f32 v99, 0, v99\n"
      "v_mfma_f32_16x16x32_bf16 v[76:79], v[128:131], v[160:163], v[76:79]\n"
      "buffer_store_dwordx4 v[244:247], v7, s[32:35], s87 offen sc1\n"
      "v_mfma_f32_16x16x32_bf16 v[84:87], v[128:131], v[164:167], v[84:87]\n"
      "ds_read_b64 v[156:157], v40 offset:3264\n"
      "v_mfma_f32_16x16x32_bf16 v[92:95], v[128:131], v[168:171], v[92:95]\n"
      "ds_read_b64 v[158:159], v40 offset:3272\n"
      "v_mfma_f32_16x16x32_bf16 v[84:87], v[140:143], v[160:163], v[84:87]\n"
      "v_mfma_f32_16x16x32_bf16 v[92:95], v[140:143], v[164:167], v[92:95]\n"
      "v_mfma_f32_16x16x32_bf16 v[92:95], v[152:155], v[160:163], v[92:95]\n"
      "s_waitcnt lgkmcnt(8)\n"
      "v_mfma_f32_16x16x32_bf16 v[64:67], v[116:119], v[176:179], v[64:67]\n"
      "buffer_store_dword v206, v239, s[32:35], s59 offen offset:0 sc1\n"
      "buffer_store_dword v207, v239, s[32:35], s59 offen offset:128 sc1\n"
      "v_mfma_f32_16x16x32_bf16 v[72:75], v[116:119], v[180:183], v[72:75]\n"
      "ds_read_b128 v[160:163], v41 offset:4896\n"
      "v_mfma_f32_16x16x32_bf16 v[80:83], v[116:119], v[184:187], v[80:83]\n"
      "buffer_load_dwordx2 v[252:253], v51, s[32:35], s76 offen offset:0 sc1\n"
      "v_mfma_f32_16x16x32_bf16 v[88:91], v[116:119], v[188:191], v[88:91]\n"
      "ds_read_b128 v[164:167], v41 offset:3360\n"
      "v_mfma_f32_16x16x32_bf16 v[72:75], v[128:131], v[176:179], v[72:75]\n"
      "ds_read_b128 v[168:171], v41 offset:1824\n"
      "v_mfma_f32_16x16x32_bf16 v[80:83], v[128:131], v[180:183], v[80:83]\n"
      "ds_read_b128 v[172:175], v41 offset:288\n"
      "v_mfma_f32_16x16x32_bf16 v[88:91], v[128:131], v[184:187], v[88:91]\n"
      "ds_read_b64 v[112:113], v40 offset:224\n"
      "v_mfma_f32_16x16x32_bf16 v[80:83], v[140:143], v[176:179], v[80:83]\n"
      "ds_read_b64 v[114:115], v40 offset:232\n"
      "v_mfma_f32_16x16x32_bf16 v[88:91], v[140:143], v[180:183], v[88:91]\n"
      "ds_read_b64 v[124:125], v40 offset:1248\n"
      "v_mfma_f32_16x16x32_bf16 v[88:91], v[152:155], v[176:179], v[88:91]\n"
      "ds_read_b64 v[126:127], v40 offset:1256\n"
      "s_waitcnt lgkmcnt(8)\n"
      "v_mfma_f32_16x16x32_bf16 v[68:71], v[120:123], v[176:179], v[68:71]\n"
      "ds_read_b64 v[136:137], v40 offset:2272\n"
      "v_mfma_f32_16x16x32_bf16 v[76:79], v[120:123], v[180:183], v[76:79]\n"
      "ds_read_b64 v[138:139], v40 offset:2280\n"
      "v_mfma_f32_16x16x32_bf16 v[84:87], v[120:123], v[184:187], v[84:87]\n"
      "ds_read_b64 v[148:149], v40 offset:3296\n"
      "s_waitcnt vmcnt(5)\n"
      "v_mfma_f32_16x16x32_bf16 v[92:95], v[120:123], v[188:191], v[92:95]\n"
      "v_add_u32 v251, v200, v201\n"
      "v_add3_u32 v34, v192, v193, v194\n"
      "v_add3_u32 v34, v34, v195, v196\n"
      "v_add_u32_dpp v251, v251, v251 row_half_mirror row_mask:0xf bank_mask:0xf\n"
      "v_mfma_f32_16x16x32_bf16 v[76:79], v[132:135], v[176:179], v[76:79]\n"
      "v_add3_u32 v34, v34, v197, v198\n"
      "v_add3_u32 v251, v251, v34, v199\n"
      "v_add3_u32 v34, v248, v249, v250\n"
      "v_mfma_f32_16x16x32_bf16 v[84:87], v[132:135], v[180:183], v[84:87]\n"
      "v_add3_u32 v34, v34, v211, v215\n"
      "v_add_u32_dpp v251, v251, v251 quad_perm:[1,0,3,2] row_mask:0xf bank_mask:0xf\n"
      "v_add3_u32 v34, v34, v219, v223\n"
      "v_mfma_f32_16x16x32_bf16 v[92:95], v[132:135], v[184:187], v[92:95]\n"
      "ds_read_b64 v[150:151], v40 offset:3304\n"
      "v_add3_u32 v34, v34, v227, v231\n"
      "v_add_u32_dpp v251, v251, v251 quad_perm:[2,3,0,1] row_mask:0xf bank_mask:0xf\n"
      "v_add3_u32 v251, v251, v34, v205\n"
      "v_mfma_f32_16x16x32_bf16 v[84:87], v[144:147], v[176:179], v[84:87]\n"
      "ds_read_b128 v[232:235], v46\n"
      "v_cmp_ne_u32 vcc, s24, v251\n"
      "v_mfma_f32_16x16x32_bf16 v[92:95], v[144:147], v[180:183], v[92:95]\n"
      "ds_read_b128 v[236:239], v46 offset:7680\n"
      "v_mfma_f32_16x16x32_bf16 v[92:95], v[156:159], v[176:179], v[92:95]\n"
      "s_cbranch_vccnz L_cv_s%=\n"
      "L_cv_r%=:\n"
      "v_add_f32_dpp v204, v204, v204 quad_perm:[1,0,3,2] row_mask:0xf bank_mask:0xf\n"
      "v_lshlrev_b32 v100, 16, v200\n"
      "v_and_b32 v101, 0xffff0000, v200\n"
      "s_waitcnt lgkmcnt(10)\n"
      "v_mfma_f32_16x16x32_bf16 v[64:67], v[120:123], v[160:163], v[64:67]\n"
      "ds_read_b128 v[176:179], v41 offset:4928\n"
      "v_add_f32_dpp v204, v204, v204 quad_perm:[2,3,0,1] row_mask:0xf bank_mask:0xf\n"
      "v_cvt_pk_bf16_f32 v204, 0, v204\n"
      "v_lshlrev_b32 v102, 16, v201\n"
      "v_mfma_f32_16x16x32_bf16 v[72:75], v[120:123], v[164:167], v[72:75]\n"
      "ds_read_b128 v[180:183], v41 offset:3392\n"
      "v_add_f32 v205, 1.0, v204\n"
      "v_and_b32 v103, 0xffff0000, v201\n"
      "v_cvt_pk_bf16_f32 v205, 0, v205\n"
      "v_mfma_f32_16x16x32_bf16 v[80:83], v[120:123], v[168:171], v[80:83]\n"
      "ds_read_b128 v[184:187], v41 offset:1856\n"
      "v_lshlrev_b32 v104, 16, v226\n"
      "v_readlane_b32 s40, v204, 32\n"
      "v_and_b32 v105, 0xffff0000, v226\n"
      "v_mfma_f32_16x16x32_bf16 v[88:91], v[120:123], v[172:175], v[88:91]\n"
      "ds_read_b128 v[188:191], v41 offset:320\n"
      "v_readlane_b32 s41, v205, 36\n"
      "v_lshlrev_b32 v106, 16, v230\n"
      "v_readlane_b32 s42, v204, 40\n"
      "v_mfma_f32_16x16x32_bf16 v[72:75], v[132:135], v[160:163], v[72:75]\n"
      "ds_read_b64 v[116:117], v40 offset:256\n"
      "v_and_b32 v107, 0xffff0000, v230\n"
      "v_readlane_b32 s43, v205, 44\n"
      "v_sub_f32 v104, v100, v104\n"
      "v_mfma_f32_16x16x32_bf16 v[80:83], v[132:135], v[164:167], v[80:83]\n"
      "ds_read_b64 v[118:119], v40 offset:264\n"
      "v_sub_f32 v105, v101, v105\n"
      "v_sub_f32 v106, v102, v106\n"
      "v_sub_f32 v107, v103, v107\n"
      "v_mfma_f32_16x16x32_bf16 v[88:91], v[132:135], v[168:171], v[88:91]\n"
      "ds_read_b64 v[128:129], v40 offset:1280\n"
      "v_cvt_pk_bf16_f32 v104, 0, v104\n"
      "v_cvt_pk_bf16_f32 v105, 0, v105\n"
      "v_cvt_pk_bf16_f32 v106, 0, v106\n"
      "v_mfma_f32_16x16x32_bf16 v[80:83], v[144:147], v[160:163], v[80:83]\n"
      "ds_read_b64 v[130:131], v40 offset:1288\n"
      "v_cvt_pk_bf16_f32 v107, 0, v107\n"
      "v_mul_f32 v104, v104, v224\n"
      "v_mul_f32 v105, v105, v225\n"
      "v_mfma_f32_16x16x32_bf16 v[88:91], v[144:147], v[164:167], v[88:91]\n"
      "ds_read_b64 v[140:141], v40 offset:2304\n"
      "v_mul_f32 v106, v106, v228\n"
      "v_mul_f32 v107, v107, v229\n"
      "v_cvt_pk_bf16_f32 v104, 0, v104\n"
      "s_waitcnt vmcnt(0)\n"
      "v_mfma_f32_16x16x32_bf16 v[88:91], v[156:159], v[160:163], v[88:91]\n"
      "ds_read_b64 v[142:143], v40 offset:2312\n"
      "v_cvt_pk_bf16_f32 v105, 0, v105\n"
      "v_cvt_pk_bf16_f32 v106, 0, v106\n"
      "v_cvt_pk_bf16_f32 v107, 0, v107\n"
      "v_cmp_ne_u64 vcc, s[30:31], v[252:253]\n"
      "s_waitcnt lgkmcnt(12)\n"
      "v_mfma_f32_16x16x32_bf16 v[68:71], v[112:115], v[160:163], v[68:71]\n"
      "ds_read_b64 v[152:153], v40 offset:3328\n"
      "v_mul_f32 v104, s41, v104\n"
      "v_mul_f32 v105, s41, v105\n"
      "v_mul_f32 v106, s41, v106\n"
      "v_mfma_f32_16x16x32_bf16 v[76:79], v[112:115], v[164:167], v[76:79]\n"
      "ds_read_b64 v[154:155], v40 offset:3336\n"
      "v_mul_f32 v107, s41, v107\n"
      "v_cvt_pk_bf16_f32 v104, 0, v104\n"
      "v_cvt_pk_bf16_f32 v105, 0, v105\n"
      "s_cbranch_vccnz L_cw_s%=\n"
      "L_cw_r%=:\n"
      "v_add_u32 v35, 4096, v7\n"
      "v_mfma_f32_16x16x32_bf16 v[84:87], v[112:115], v[168:171], v[84:87]\n"
      "global_load_dwordx4 a[0:3], v35, s[8:9] offset:-4096 sc1\n"
      "v_cvt_pk_bf16_f32 v106, 0, v106\n"
      "v_cvt_pk_bf16_f32 v107, 0, v107\n"
      "v_mfma_f32_16x16x32_bf16 v[92:95], v[112:115], v[172:175], v[92:95]\n"
      "v_add_f32 v104, s40, v104\n"
      "v_add_f32 v105, s40, v105\n"
      "v_add_f32 v106, s40, v106\n"
      "v_mfma_f32_16x16x32_bf16 v[76:79], v[124:127], v[160:163], v[76:79]\n"
      "v_add_f32 v107, s40, v107\n"
      "v_cvt_pk_bf16_f32 v104, 0, v104\n"
      "v_cvt_pk_bf16_f32 v105, 0, v105\n"
      "v_mfma_f32_16x16x32_bf16 v[84:87], v[124:127], v[164:167], v[84:87]\n"
      "v_cvt_pk_bf16_f32 v106, 0, v106\n"
      "v_cvt_pk_bf16_f32 v107, 0, v107\n"
      "v_mul_f32 v104, v104, v96\n"
      "v_mfma_f32_16x16x32_bf16 v[92:95], v[124:127], v[168:171], v[92:95]\n"
      "global_load_dwordx4 a[4:7], v35, s[8:9] offset:-3072 sc1\n"
      "v_mul_f32 v105, v105, v97\n"
      "v_mul_f32 v106, v106, v98\n"
      "v_mul_f32 v107, v107, v99\n"
      "v_mfma_f32_16x16x32_bf16 v[84:87], v[136:139], v[160:163], v[84:87]\n"
      "v_cvt_pk_bf16_f32 v104, 0, v104\n"
      "v_cvt_pk_bf16_f32 v105, 0, v105\n"
      "v_cvt_pk_bf16_f32 v106, 0, v106\n"
      "v_mfma_f32_16x16x32_bf16 v[92:95], v[136:139], v[164:167], v[92:95]\n"
      "v_cvt_pk_bf16_f32 v107, 0, v107\n"
      "v_mul_f32 v104, s42, v104\n"
      "v_mul_f32 v105, s42, v105\n"
      "v_mfma_f32_16x16x32_bf16 v[92:95], v[148:151], v[160:163], v[92:95]\n"
      "v_mul_f32 v106, s42, v106\n"
      "v_mul_f32 v107, s42, v107\n"
      "v_cvt_pk_bf16_f32 v104, 0, v104\n"
      "s_waitcnt lgkmcnt(0)\n"
      "v_mfma_f32_16x16x32_bf16 v[64:67], v[112:115], v[176:179], v[64:67]\n"
      "global_load_dwordx4 a[8:11], v35, s[8:9] offset:-2048 sc1\n"
      "v_cvt_pk_bf16_f32 v105, 0, v105\n"
      "v_cvt_pk_bf16_f32 v106, 0, v106\n"
      "v_cvt_pk_bf16_f32 v107, 0, v107\n"
      "v_mfma_f32_16x16x32_bf16 v[68:71], v[116:119], v[176:179], v[68:71]\n"
      "v_add_f32 v104, v104, v100\n"
      "v_add_f32 v105, v105, v101\n"
      "v_add_f32 v106, v106, v102\n"
      "v_mfma_f32_16x16x32_bf16 v[72:75], v[112:115], v[180:183], v[72:75]\n"
      "v_add_f32 v107, v107, v103\n"
      "v_cvt_pk_bf16_f32 v104, 0, v104\n"
      "v_cvt_pk_bf16_f32 v105, 0, v105\n"
      "v_mfma_f32_16x16x32_bf16 v[72:75], v[124:127], v[176:179], v[72:75]\n"
      "ds_write_b128 v44, v[64:67] offset:0\n"
      "v_cvt_pk_bf16_f32 v106, 0, v106\n"
      "v_cvt_pk_bf16_f32 v107, 0, v107\n"
      "v_cvt_pk_bf16_f32 v108, v104, v105\n"
      "v_cvt_pk_bf16_f32 v109, v106, v107\n"
      "v_mfma_f32_16x16x32_bf16 v[76:79], v[116:119], v[180:183], v[76:79]\n"
      "global_load_dwordx4 a[12:15], v35, s[8:9] offset:-1024 sc1\n"
      "ds_write_b128 v59, v[68:71] offset:0\n"
      "v_mul_f32 v104, s43, v104\n"
      "v_mul_f32 v105, s43, v105\n"
      "v_mul_f32 v106, s43, v106\n"
      "v_mul_f32 v107, s43, v107\n"
      "v_mfma_f32_16x16x32_bf16 v[76:79], v[128:131], v[176:179], v[76:79]\n"
      "v_cvt_pk_bf16_f32 v110, v104, v105\n"
      "v_cvt_pk_bf16_f32 v111, v106, v107\n"
      "v_mfma_f32_16x16x32_bf16 v[80:83], v[112:115], v[184:187], v[80:83]\n"
      "ds_write_b128 v44, v[72:75] offset:2048\n"
      "v_add_u32 v96, v108, v109\n"
      "v_add_u32 v97, v110, v111\n"
      "s_add_u32 s4, s26, s71\n"
      "s_addc_u32 s5, s27, 0\n"
      "v_mfma_f32_16x16x32_bf16 v[80:83], v[124:127], v[180:183], v[80:83]\n"
      "s_add_u32 s6, s26, s72\n"
      "s_addc_u32 s7, s27, 0\n"
      "v_mfma_f32_16x16x32_bf16 v[80:83], v[136:139], v[176:179], v[80:83]\n"
      "buffer_store_dwordx2 v[110:111], v50, s[32:35], s78 offen sc1\n"
      "ds_write_b128 v59, v[76:79] offset:2048\n"
      "v_add_u32_dpp v96, v96, v96 quad_perm:[1,0,3,2] row_mask:0xf bank_mask:0xf\n"
      "v_add_u32_dpp v97, v97, v97 quad_perm:[1,0,3,2] row_mask:0xf bank_mask:0xf\n"
      "s_add_u32 s10, s26, s75\n"
      "s_addc_u32 s11, s27, 0\n"
      "v_mfma_f32_16x16x32_bf16 v[84:87], v[116:119], v[184:187], v[84:87]\n"
      "v_add_u32_dpp v96, v96, v96 quad_perm:[2,3,0,1] row_mask:0xf bank_mask:0xf\n"
      "v_add_u32_dpp v97, v97, v97 quad_perm:[2,3,0,1] row_mask:0xf bank_mask:0xf\n"
      "v_mfma_f32_16x16x32_bf16 v[84:87], v[128:131], v[180:183], v[84:87]\n"
      "global_load_dwordx4 a[16:19], v35, s[8:9] offset:0 sc1\n"
      "v_add_u32_dpp v96, v96, v96 row_half_mirror row_mask:0xf bank_mask:0xf\n"
      "v_add_u32_dpp v97, v97, v97 row_half_mirror row_mask:0xf bank_mask:0xf\n"
      "v_mfma_f32_16x16x32_bf16 v[84:87], v[140:143], v[176:179], v[84:87]\n"
      "buffer_store_dwordx2 v[108:109], v37, s[32:35], s55 offen sc1\n"
      "ds_write_b128 v44, v[80:83] offset:4096\n"
      "v_mov_b32 v98, v96\n"
      "v_add_u32_dpp v97, v97, v97 row_mirror row_mask:0xf bank_mask:0xf\n"
      "s_mov_b32 s42, 0xaaaa5555\n"
      "s_mov_b32 s43, s42\n"
      "v_mfma_f32_16x16x32_bf16 v[88:91], v[112:115], v[188:191], v[88:91]\n"
      "v_permlane16_swap_b32 v96, v98\n"
      "v_add_u32 v96, v96, v98\n"
      "v_mfma_f32_16x16x32_bf16 v[88:91], v[124:127], v[184:187], v[88:91]\n"
      "global_load_dwordx4 a[20:23], v35, s[8:9] offset:1024 sc1\n"
      "v_mfma_f32_16x16x32_bf16 v[88:91], v[136:139], v[180:183], v[88:91]\n"
      "ds_write_b128 v59, v[84:87] offset:4096\n"
      "v_cndmask_b32 v96, v97, v96, s[42:43]\n"
      "v_sub_u32 v96, v52, v96\n"
      "s_add_u32 s12, s10, 49152\n"
      "s_addc_u32 s13, s11, 0\n"
      "v_mfma_f32_16x16x32_bf16 v[88:91], v[148:151], v[176:179], v[88:91]\n"
      "buffer_store_dword v96, v49, s[32:35], s77 offen sc1\n"
      "s_lshl_b32 s71, s30, 2\n"
      "v_mfma_f32_16x16x32_bf16 v[92:95], v[116:119], v[188:191], v[92:95]\n"
      "global_load_dwordx4 a[24:27], v35, s[8:9] offset:2048 sc1\n"
      "v_mfma_f32_16x16x32_bf16 v[92:95], v[128:131], v[184:187], v[92:95]\n"
      "v_mfma_f32_16x16x32_bf16 v[92:95], v[140:143], v[180:183], v[92:95]\n"
      "ds_write_b128 v44, v[88:91] offset:6144\n"
      "v_mfma_f32_16x16x32_bf16 v[92:95], v[152:155], v[176:179], v[92:95]\n"
      "global_load_dwordx4 a[28:31], v35, s[8:9] offset:3072 sc1\n"
      "s_nop 7\n"
      "ds_write_b128 v59, v[92:95] offset:6144\n"
      "v_readlane_b32 s40, v204, 0\n"
      "v_readlane_b32 s42, v205, 4\n"
      "v_xor_b32 v33, 16, v31\n"
      "v_readlane_b32 s58, v204, 8\n"
      "v_readlane_b32 s48, v204, 16\n"
      "v_readlane_b32 s50, v205, 20\n"
      "v_readlane_b32 s52, v204, 24\n"
      "v_readlane_b32 s41, v205, 12\n"
      "v_readlane_b32 s49, v205, 28\n"
      "s_mov_b32 vcc_lo, 0xaa55aa55\n"
      "s_mov_b32 vcc_hi, 0xaa55aa55\n"
      "v_add_u32 v40, s73, v7\n"
      "v_lshlrev_b32 v96, 16, v210\n"
      "v_and_b32 v97, 0xffff0000, v210\n"
      "v_lshlrev_b32 v98, 16, v214\n"
      "v_and_b32 v99, 0xffff0000, v214\n"
      "v_lshlrev_b32 v100, 16, v218\n"
      "v_and_b32 v101, 0xffff0000, v218\n"
      "v_lshlrev_b32 v102, 16, v222\n"
      "v_and_b32 v103, 0xffff0000, v222\n"
      "s_waitcnt lgkmcnt(0)\n"
      "s_barrier\n"
      "ds_read_b128 v[64:67], v31 offset:0\n"
      "ds_read_b128 v[68:71], v33 offset:0\n"
      "ds_read_b128 v[72:75], v31 offset:8192\n"
      "ds_read_b128 v[76:79], v33 offset:8192\n"
      "ds_read_b128 v[80:83], v31 offset:16384\n"
      "ds_read_b128 v[84:87], v33 offset:16384\n"
      "ds_read_b128 v[88:91], v31 offset:24576\n"
      "ds_read_b128 v[92:95], v33 offset:24576\n"
      "v_add_u32 v34, 12288, v7\n"
      "global_load_dwordx4 a[32:35], v34, s[8:9] offset:-4096 sc1\n"
      "v_lshlrev_b32 v112, 16, v192\n"
      "v_and_b32 v113, 0xffff0000, v192\n"
      "v_lshlrev_b32 v114, 16, v193\n"
      "v_and_b32 v115, 0xffff0000, v193\n"
      "v_lshlrev_b32 v116, 16, v194\n"
      "v_and_b32 v117, 0xffff0000, v194\n"
      "v_lshlrev_b32 v118, 16, v195\n"
      "v_and_b32 v119, 0xffff0000, v195\n"
      "v_pk_add_f32 v[104:105], v[112:113], v[96:97] neg_lo:[0,1] neg_hi:[0,1]\n"
      "v_pk_add_f32 v[106:107], v[114:115], v[98:99] neg_lo:[0,1] neg_hi:[0,1]\n"
      "v_pk_add_f32 v[108:109], v[116:117], v[100:101] neg_lo:[0,1] neg_hi:[0,1]\n"
      "v_pk_add_f32 v[110:111], v[118:119], v[102:103] neg_lo:[0,1] neg_hi:[0,1]\n"
      "global_load_dwordx4 a[36:39], v34, s[8:9] offset:-3072 sc1\n"
      "v_cvt_pk_bf16_f32 v104, 0, v104\n"
      "v_cvt_pk_bf16_f32 v105, 0, v105\n"
      "v_cvt_pk_bf16_f32 v106, 0, v106\n"
      "v_cvt_pk_bf16_f32 v107, 0, v107\n"
      "v_cvt_pk_bf16_f32 v108, 0, v108\n"
      "v_cvt_pk_bf16_f32 v109, 0, v109\n"
      "v_cvt_pk_bf16_f32 v110, 0, v110\n"
      "v_cvt_pk_bf16_f32 v111, 0, v111\n"
      "v_pk_mul_f32 v[104:105], v[104:105], v[208:209]\n"
      "v_pk_mul_f32 v[106:107], v[106:107], v[212:213]\n"
      "v_pk_mul_f32 v[108:109], v[108:109], v[216:217]\n"
      "v_pk_mul_f32 v[110:111], v[110:111], v[220:221]\n"
      "global_load_dwordx4 a[40:43], v34, s[8:9] offset:-2048 sc1\n"
      "v_cvt_pk_bf16_f32 v104, 0, v104\n"
      "v_cvt_pk_bf16_f32 v105, 0, v105\n"
      "v_cvt_pk_bf16_f32 v106, 0, v106\n"
      "v_cvt_pk_bf16_f32 v107, 0, v107\n"
      "v_cvt_pk_bf16_f32 v108, 0, v108\n"
      "v_cvt_pk_bf16_f32 v109, 0, v109\n"
      "v_cvt_pk_bf16_f32 v110, 0, v110\n"
      "v_cvt_pk_bf16_f32 v111, 0, v111\n"
      "v_pk_mul_f32 v[104:105], v[104:105], s[42:43] op_sel_hi:[1,0]\n"
      "v_pk_mul_f32 v[106:107], v[106:107], s[42:43] op_sel_hi:[1,0]\n"
      "v_pk_mul_f32 v[108:109], v[108:109], s[42:43] op_sel_hi:[1,0]\n"
      "v_pk_mul_f32 v[110:111], v[110:111], s[42:43] op_sel_hi:[1,0]\n"
      "global_load_dwordx4 a[44:47], v34, s[8:9] offset:-1024 sc1\n"
      "v_cvt_pk_bf16_f32 v104, 0, v104\n"
      "v_cvt_pk_bf16_f32 v105, 0, v105\n"
      "v_cvt_pk_bf16_f32 v106, 0, v106\n"
      "v_cvt_pk_bf16_f32 v107, 0, v107\n"
      "v_cvt_pk_bf16_f32 v108, 0, v108\n"
      "v_cvt_pk_bf16_f32 v109, 0, v109\n"
      "v_cvt_pk_bf16_f32 v110, 0, v110\n"
      "v_cvt_pk_bf16_f32 v111, 0, v111\n"
      "v_pk_add_f32 v[104:105], v[104:105], s[40:41] op_sel_hi:[1,0]\n"
      "v_pk_add_f32 v[106:107], v[106:107], s[40:41] op_sel_hi:[1,0]\n"
      "v_pk_add_f32 v[108:109], v[108:109], s[40:41] op_sel_hi:[1,0]\n"
      "v_pk_add_f32 v[110:111], v[110:111], s[40:41] op_sel_hi:[1,0]\n"
      "global_load_dwordx4 a[48:51], v34, s[8:9] offset:0 sc1\n"
      "v_cvt_pk_bf16_f32 v104, 0, v104\n"
      "v_cvt_pk_bf16_f32 v105, 0, v105\n"
      "v_cvt_pk_bf16_f32 v106, 0, v106\n"
      "v_cvt_pk_bf16_f32 v107, 0, v107\n"
      "v_cvt_pk_bf16_f32 v108, 0, v108\n"
      "v_cvt_pk_bf16_f32 v109, 0, v109\n"
      "v_cvt_pk_bf16_f32 v110, 0, v110\n"
      "v_cvt_pk_bf16_f32 v111, 0, v111\n"
      "v_and_b32 v120, 0xffff0000, v235\n"
      "v_lshlrev_b32 v121, 16, v235\n"
      "v_and_b32 v122, 0xffff0000, v234\n"
      "v_lshlrev_b32 v123, 16, v234\n"
      "global_load_dwordx4 a[52:55], v34, s[8:9] offset:1024 sc1\n"
      "v_and_b32 v124, 0xffff0000, v233\n"
      "v_lshlrev_b32 v125, 16, v233\n"
      "v_and_b32 v126, 0xffff0000, v232\n"
      "v_lshlrev_b32 v127, 16, v232\n"
      "v_pk_mul_f32 v[120:121], v[120:121], s[14:15] op_sel_hi:[1,0]\n"
      "v_pk_mul_f32 v[122:123], v[122:123], s[14:15] op_sel_hi:[1,0]\n"
      "v_pk_mul_f32 v[124:125], v[124:125], s[14:15] op_sel_hi:[1,0]\n"
      "v_pk_mul_f32 v[126:127], v[126:127], s[14:15] op_sel_hi:[1,0]\n"
      "v_cvt_pk_bf16_f32 v120, 0, v120\n"
      "v_cvt_pk_bf16_f32 v121, 0, v121\n"
      "v_cvt_pk_bf16_f32 v122, 0, v122\n"
      "v_cvt_pk_bf16_f32 v123, 0, v123\n"
      "global_load_dwordx4 a[56:59], v34, s[8:9] offset:2048 sc1\n"
      "v_cvt_pk_bf16_f32 v124, 0, v124\n"
      "v_cvt_pk_bf16_f32 v125, 0, v125\n"
      "v_cvt_pk_bf16_f32 v126, 0, v126\n"
      "v_cvt_pk_bf16_f32 v127, 0, v127\n"
      "s_add_u32 s43, s89, 32768\n"
      "s_add_u32 s53, s89, 0x1000000\n"
      "s_add_u32 s54, s43, 0x1000000\n"
      "s_add_u32 s55, s79, 176128\n"
      "s_add_u32 s45, s81, 65536\n"
      "s_waitcnt lgkmcnt(0)\n"
      "v_pk_add_f32 v[64:65], v[64:65], v[72:73]\n"
      "v_pk_add_f32 v[66:67], v[66:67], v[74:75]\n"
      "global_load_dwordx4 a[60:63], v34, s[8:9] offset:3072 sc1\n"
      "v_pk_add_f32 v[68:69], v[68:69], v[76:77]\n"
      "v_pk_add_f32 v[70:71], v[70:71], v[78:79]\n"
      "v_cvt_pk_bf16_f32 v64, 0, v64\n"
      "v_cvt_pk_bf16_f32 v65, 0, v65\n"
      "v_cvt_pk_bf16_f32 v66, 0, v66\n"
      "v_cvt_pk_bf16_f32 v67, 0, v67\n"
      "v_cvt_pk_bf16_f32 v68, 0, v68\n"
      "v_cvt_pk_bf16_f32 v69, 0, v69\n"
      "v_cvt_pk_bf16_f32 v70, 0, v70\n"
      "v_cvt_pk_bf16_f32 v71, 0, v71\n"
      "v_pk_add_f32 v[120:121], v[120:121], v[64:65]\n"
      "v_pk_add_f32 v[122:123], v[122:123], v[66:67]\n"
      "v_add_u32 v34, 20480, v7\n"
      "global_load_dwordx4 a[64:67], v34, s[8:9] offset:-4096 sc1\n"
      "v_pk_add_f32 v[124:125], v[124:125], v[68:69]\n"
      "v_pk_add_f32 v[126:127], v[126:127], v[70:71]\n"
      "v_cvt_pk_bf16_f32 v120, 0, v120\n"
      "v_cvt_pk_bf16_f32 v121, 0, v121\n"
      "v_cvt_pk_bf16_f32 v122, 0, v122\n"
      "v_cvt_pk_bf16_f32 v123, 0, v123\n"
      "v_cvt_pk_bf16_f32 v124, 0, v124\n"
      "v_cvt_pk_bf16_f32 v125, 0, v125\n"
      "v_cvt_pk_bf16_f32 v126, 0, v126\n"
      "v_cvt_pk_bf16_f32 v127, 0, v127\n"
      "v_pk_mul_f32 v[104:105], v[104:105], v[120:121]\n"
      "v_pk_mul_f32 v[106:107], v[106:107], v[122:123]\n"
      "global_load_dwordx4 a[68:71], v34, s[8:9] offset:-3072 sc1\n"
      "v_pk_mul_f32 v[108:109], v[108:109], v[124:125]\n"
      "v_pk_mul_f32 v[110:111], v[110:111], v[126:127]\n"
      "v_cvt_pk_bf16_f32 v104, 0, v104\n"
      "v_cvt_pk_bf16_f32 v105, 0, v105\n"
      "v_cvt_pk_bf16_f32 v106, 0, v106\n"
      "v_cvt_pk_bf16_f32 v107, 0, v107\n"
      "v_cvt_pk_bf16_f32 v108, 0, v108\n"
      "v_cvt_pk_bf16_f32 v109, 0, v109\n"
      "v_cvt_pk_bf16_f32 v110, 0, v110\n"
      "v_cvt_pk_bf16_f32 v111, 0, v111\n"
      "v_pk_mul_f32 v[104:105], v[104:105], s[58:59] op_sel_hi:[1,0]\n"
      "v_pk_mul_f32 v[106:107], v[106:107], s[58:59] op_sel_hi:[1,0]\n"
      "global_load_dwordx4 a[72:75], v34, s[8:9] offset:-2048 sc1\n"
      "v_pk_mul_f32 v[108:109], v[108:109], s[58:59] op_sel_hi:[1,0]\n"
      "v_pk_mul_f32 v[110:111], v[110:111], s[58:59] op_sel_hi:[1,0]\n"
      "v_cvt_pk_bf16_f32 v104, 0, v104\n"
      "v_cvt_pk_bf16_f32 v105, 0, v105\n"
      "v_cvt_pk_bf16_f32 v106, 0, v106\n"
      "v_cvt_pk_bf16_f32 v107, 0, v107\n"
      "v_cvt_pk_bf16_f32 v108, 0, v108\n"
      "v_cvt_pk_bf16_f32 v109, 0, v109\n"
      "v_cvt_pk_bf16_f32 v110, 0, v110\n"
      "v_cvt_pk_bf16_f32 v111, 0, v111\n"
      "v_pk_add_f32 v[104:105], v[104:105], v[112:113]\n"
      "v_pk_add_f32 v[106:107], v[106:107], v[114:115]\n"
      "global_load_dwordx4 a[76:79], v34, s[8:9] offset:-1024 sc1\n"
      "v_pk_add_f32 v[108:109], v[108:109], v[116:117]\n"
      "v_pk_add_f32 v[110:111], v[110:111], v[118:119]\n"
      "v_cvt_pk_bf16_f32 v104, 0, v104\n"
      "v_cvt_pk_bf16_f32 v105, 0, v105\n"
      "v_cvt_pk_bf16_f32 v106, 0, v106\n"
      "v_cvt_pk_bf16_f32 v107, 0, v107\n"
      "v_cvt_pk_bf16_f32 v108, 0, v108\n"
      "v_cvt_pk_bf16_f32 v109, 0, v109\n"
      "v_cvt_pk_bf16_f32 v110, 0, v110\n"
      "v_cvt_pk_bf16_f32 v111, 0, v111\n"
      "v_pk_mul_f32 v[120:121], v[104:105], s[40:41] op_sel:[0,1] op_sel_hi:[1,1]\n"
      "v_pk_mul_f32 v[122:123], v[106:107], s[40:41] op_sel:[0,1] op_sel_hi:[1,1]\n"
      "v_pk_mul_f32 v[124:125], v[108:109], s[40:41] op_sel:[0,1] op_sel_hi:[1,1]\n"
      "v_pk_mul_f32 v[126:127], v[110:111], s[40:41] op_sel:[0,1] op_sel_hi:[1,1]\n"
      "v_cvt_pk_bf16_f32 v128, v120, v121\n"
      "v_cvt_pk_bf16_f32 v129, v122, v123\n"
      "v_cvt_pk_bf16_f32 v130, v124, v125\n"
      "v_cvt_pk_bf16_f32 v131, v126, v127\n"
      "buffer_store_dwordx4 v[128:131], v29, s[32:35], s89 offen sc1\n"
      "v_cvt_pk_bf16_f32 v132, v104, v105\n"
      "v_cvt_pk_bf16_f32 v133, v106, v107\n"
      "v_cvt_pk_bf16_f32 v134, v108, v109\n"
      "v_cvt_pk_bf16_f32 v135, v110, v111\n"
      "v_add3_u32 v35, v128, v129, v130\n"
      "v_add_u32 v35, v35, v131\n"
      "buffer_store_dwordx4 v[132:135], v36, s[32:35], s53 offen sc1\n"
      "v_add3_u32 v38, v132, v133, v134\n"
      "v_add_u32 v38, v38, v135\n"
      "v_lshlrev_b32 v112, 16, v196\n"
      "v_add_u32_dpp v35, v35, v35 quad_perm:[1,0,3,2] row_mask:0xf bank_mask:0xf\n"
      "v_and_b32 v113, 0xffff0000, v196\n"
      "v_add_u32_dpp v38, v38, v38 quad_perm:[1,0,3,2] row_mask:0xf bank_mask:0xf\n"
      "v_lshlrev_b32 v114, 16, v197\n"
      "v_add_u32_dpp v35, v35, v35 quad_perm:[2,3,0,1] row_mask:0xf bank_mask:0xf\n"
      "v_and_b32 v115, 0xffff0000, v197\n"
      "v_add_u32_dpp v38, v38, v38 quad_perm:[2,3,0,1] row_mask:0xf bank_mask:0xf\n"
      "v_lshlrev_b32 v116, 16, v198\n"
      "v_add_u32_dpp v35, v35, v35 row_half_mirror row_mask:0xf bank_mask:0xf\n"
      "v_and_b32 v117, 0xffff0000, v198\n"
      "v_add_u32_dpp v38, v38, v38 row_ror:8 row_mask:0xf bank_mask:0xf\n"
      "v_lshlrev_b32 v118, 16, v199\n"
      "v_cndmask_b32 v35, v35, v38, vcc\n"
      "v_and_b32 v119, 0xffff0000, v199\n"
      "v_sub_u32 v35, s30, v35\n"
      "v_pk_add_f32 v[104:105], v[112:113], v[96:97] neg_lo:[0,1] neg_hi:[0,1]\n"
      "buffer_store_dword v35, v30, s[32:35], s81 offen sc1\n"
      "buffer_store_dwordx2 v[52:53], v48, s[32:35], s88 offen sc1\n"
      "v_pk_add_f32 v[106:107], v[114:115], v[98:99] neg_lo:[0,1] neg_hi:[0,1]\n"
      "v_pk_add_f32 v[108:109], v[116:117], v[100:101] neg_lo:[0,1] neg_hi:[0,1]\n"
      "v_pk_add_f32 v[110:111], v[118:119], v[102:103] neg_lo:[0,1] neg_hi:[0,1]\n"
      "v_cvt_pk_bf16_f32 v104, 0, v104\n"
      "v_cvt_pk_bf16_f32 v105, 0, v105\n"
      "v_cvt_pk_bf16_f32 v106, 0, v106\n"
      "v_cvt_pk_bf16_f32 v107, 0, v107\n"
      "v_cvt_pk_bf16_f32 v108, 0, v108\n"
      "v_cvt_pk_bf16_f32 v109, 0, v109\n"
      "v_cvt_pk_bf16_f32 v110, 0, v110\n"
      "v_cvt_pk_bf16_f32 v111, 0, v111\n"
      "v_pk_mul_f32 v[104:105], v[104:105], v[208:209]\n"
      "v_pk_mul_f32 v[106:107], v[106:107], v[212:213]\n"
      "v_pk_mul_f32 v[108:109], v[108:109], v[216:217]\n"
      "v_pk_mul_f32 v[110:111], v[110:111], v[220:221]\n"
      "v_cvt_pk_bf16_f32 v104, 0, v104\n"
      "buffer_store_dwordx2 v[52:53], v48, s[32:35], s55 offen sc1\n"
      "v_cvt_pk_bf16_f32 v105, 0, v105\n"
      "v_cvt_pk_bf16_f32 v106, 0, v106\n"
      "v_cvt_pk_bf16_f32 v107, 0, v107\n"
      "v_cvt_pk_bf16_f32 v108, 0, v108\n"
      "v_cvt_pk_bf16_f32 v109, 0, v109\n"
      "v_cvt_pk_bf16_f32 v110, 0, v110\n"
      "v_cvt_pk_bf16_f32 v111, 0, v111\n"
      "v_pk_mul_f32 v[104:105], v[104:105], s[50:51] op_sel_hi:[1,0]\n"
      "v_pk_mul_f32 v[106:107], v[106:107], s[50:51] op_sel_hi:[1,0]\n"
      "v_pk_mul_f32 v[108:109], v[108:109], s[50:51] op_sel_hi:[1,0]\n"
      "v_pk_mul_f32 v[110:111], v[110:111], s[50:51] op_sel_hi:[1,0]\n"
      "v_cvt_pk_bf16_f32 v104, 0, v104\n"
      "v_cvt_pk_bf16_f32 v105, 0, v105\n"
      "v_cvt_pk_bf16_f32 v106, 0, v106\n"
      "v_cvt_pk_bf16_f32 v107, 0, v107\n"
      "v_cvt_pk_bf16_f32 v108, 0, v108\n"
      "global_load_dwordx4 a[80:83], v34, s[8:9] offset:0 sc1\n"
      "v_cvt_pk_bf16_f32 v109, 0, v109\n"
      "v_cvt_pk_bf16_f32 v110, 0, v110\n"
      "v_cvt_pk_bf16_f32 v111, 0, v111\n"
      "v_pk_add_f32 v[104:105], v[104:105], s[48:49] op_sel_hi:[1,0]\n"
      "v_pk_add_f32 v[106:107], v[106:107], s[48:49] op_sel_hi:[1,0]\n"
      "v_pk_add_f32 v[108:109], v[108:109], s[48:49] op_sel_hi:[1,0]\n"
      "v_pk_add_f32 v[110:111], v[110:111], s[48:49] op_sel_hi:[1,0]\n"
      "v_cvt_pk_bf16_f32 v104, 0, v104\n"
      "v_cvt_pk_bf16_f32 v105, 0, v105\n"
      "v_cvt_pk_bf16_f32 v106, 0, v106\n"
      "v_cvt_pk_bf16_f32 v107, 0, v107\n"
      "v_cvt_pk_bf16_f32 v108, 0, v108\n"
      "v_cvt_pk_bf16_f32 v109, 0, v109\n"
      "v_cvt_pk_bf16_f32 v110, 0, v110\n"
      "v_cvt_pk_bf16_f32 v111, 0, v111\n"
      "v_and_b32 v120, 0xffff0000, v239\n"
      "global_load_dwordx4 a[84:87], v34, s[8:9] offset:1024 sc1\n"
      "v_lshlrev_b32 v121, 16, v239\n"
      "v_and_b32 v122, 0xffff0000, v238\n"
      "v_lshlrev_b32 v123, 16, v238\n"
      "v_and_b32 v124, 0xffff0000, v237\n"
      "v_lshlrev_b32 v125, 16, v237\n"
      "v_and_b32 v126, 0xffff0000, v236\n"
      "v_lshlrev_b32 v127, 16, v236\n"
      "v_pk_mul_f32 v[120:121], v[120:121], s[14:15] op_sel:[0,1] op_sel_hi:[1,1]\n"
      "v_pk_mul_f32 v[122:123], v[122:123], s[14:15] op_sel:[0,1] op_sel_hi:[1,1]\n"
      "v_pk_mul_f32 v[124:125], v[124:125], s[14:15] op_sel:[0,1] op_sel_hi:[1,1]\n"
      "v_pk_mul_f32 v[126:127], v[126:127], s[14:15] op_sel:[0,1] op_sel_hi:[1,1]\n"
      "v_cvt_pk_bf16_f32 v120, 0, v120\n"
      "v_cvt_pk_bf16_f32 v121, 0, v121\n"
      "v_cvt_pk_bf16_f32 v122, 0, v122\n"
      "v_cvt_pk_bf16_f32 v123, 0, v123\n"
      "v_cvt_pk_bf16_f32 v124, 0, v124\n"
      "global_load_dwordx4 a[88:91], v34, s[8:9] offset:2048 sc1\n"
      "v_cvt_pk_bf16_f32 v125, 0, v125\n"
      "v_cvt_pk_bf16_f32 v126, 0, v126\n"
      "v_cvt_pk_bf16_f32 v127, 0, v127\n"
      "v_pk_add_f32 v[80:81], v[80:81], v[88:89]\n"
      "v_pk_add_f32 v[82:83], v[82:83], v[90:91]\n"
      "v_pk_add_f32 v[84:85], v[84:85], v[92:93]\n"
      "v_pk_add_f32 v[86:87], v[86:87], v[94:95]\n"
      "v_cvt_pk_bf16_f32 v80, 0, v80\n"
      "v_cvt_pk_bf16_f32 v81, 0, v81\n"
      "v_cvt_pk_bf16_f32 v82, 0, v82\n"
      "v_cvt_pk_bf16_f32 v83, 0, v83\n"
      "v_cvt_pk_bf16_f32 v84, 0, v84\n"
      "v_cvt_pk_bf16_f32 v85, 0, v85\n"
      "v_cvt_pk_bf16_f32 v86, 0, v86\n"
      "v_cvt_pk_bf16_f32 v87, 0, v87\n"
      "v_pk_add_f32 v[120:121], v[120:121], v[80:81]\n"
      "global_load_dwordx4 a[92:95], v34, s[8:9] offset:3072 sc1\n"
      "v_pk_add_f32 v[122:123], v[122:123], v[82:83]\n"
      "v_pk_add_f32 v[124:125], v[124:125], v[84:85]\n"
      "v_pk_add_f32 v[126:127], v[126:127], v[86:87]\n"
      "v_cvt_pk_bf16_f32 v120, 0, v120\n"
      "v_cvt_pk_bf16_f32 v121, 0, v121\n"
      "v_cvt_pk_bf16_f32 v122, 0, v122\n"
      "v_cvt_pk_bf16_f32 v123, 0, v123\n"
      "v_cvt_pk_bf16_f32 v124, 0, v124\n"
      "v_cvt_pk_bf16_f32 v125, 0, v125\n"
      "v_cvt_pk_bf16_f32 v126, 0, v126\n"
      "v_cvt_pk_bf16_f32 v127, 0, v127\n"
      "v_pk_mul_f32 v[104:105], v[104:105], v[120:121]\n"
      "v_pk_mul_f32 v[106:107], v[106:107], v[122:123]\n"
      "v_pk_mul_f32 v[108:109], v[108:109], v[124:125]\n"
      "v_pk_mul_f32 v[110:111], v[110:111], v[126:127]\n"
      "v_cvt_pk_bf16_f32 v104, 0, v104\n"
      "v_add_u32 v34, 28672, v7\n"
      "global_load_dwordx4 a[96:99], v34, s[8:9] offset:-4096 sc1\n"
      "v_cvt_pk_bf16_f32 v105, 0, v105\n"
      "v_cvt_pk_bf16_f32 v106, 0, v106\n"
      "v_cvt_pk_bf16_f32 v107, 0, v107\n"
      "v_cvt_pk_bf16_f32 v108, 0, v108\n"
      "v_cvt_pk_bf16_f32 v109, 0, v109\n"
      "v_cvt_pk_bf16_f32 v110, 0, v110\n"
      "v_cvt_pk_bf16_f32 v111, 0, v111\n"
      "v_pk_mul_f32 v[104:105], v[104:105], s[52:53] op_sel_hi:[1,0]\n"
      "v_pk_mul_f32 v[106:107], v[106:107], s[52:53] op_sel_hi:[1,0]\n"
      "v_pk_mul_f32 v[108:109], v[108:109], s[52:53] op_sel_hi:[1,0]\n"
      "v_pk_mul_f32 v[110:111], v[110:111], s[52:53] op_sel_hi:[1,0]\n"
      "v_cvt_pk_bf16_f32 v104, 0, v104\n"
      "v_cvt_pk_bf16_f32 v105, 0, v105\n"
      "v_cvt_pk_bf16_f32 v106, 0, v106\n"
      "v_cvt_pk_bf16_f32 v107, 0, v107\n"
      "v_cvt_pk_bf16_f32 v108, 0, v108\n"
      "global_load_dwordx4 a[100:103], v34, s[8:9] offset:-3072 sc1\n"
      "v_cvt_pk_bf16_f32 v109, 0, v109\n"
      "v_cvt_pk_bf16_f32 v110, 0, v110\n"
      "v_cvt_pk_bf16_f32 v111, 0, v111\n"
      "v_pk_add_f32 v[104:105], v[104:105], v[112:113]\n"
      "v_pk_add_f32 v[106:107], v[106:107], v[114:115]\n"
      "v_pk_add_f32 v[108:109], v[108:109], v[116:117]\n"
      "v_pk_add_f32 v[110:111], v[110:111], v[118:119]\n"
      "v_cvt_pk_bf16_f32 v104, 0, v104\n"
      "v_cvt_pk_bf16_f32 v105, 0, v105\n"
      "v_cvt_pk_bf16_f32 v106, 0, v106\n"
      "v_cvt_pk_bf16_f32 v107, 0, v107\n"
      "v_cvt_pk_bf16_f32 v108, 0, v108\n"
      "v_cvt_pk_bf16_f32 v109, 0, v109\n"
      "v_cvt_pk_bf16_f32 v110, 0, v110\n"
      "v_cvt_pk_bf16_f32 v111, 0, v111\n"
      "v_pk_mul_f32 v[120:121], v[104:105], s[48:49] op_sel:[0,1] op_sel_hi:[1,1]\n"
      "v_pk_mul_f32 v[122:123], v[106:107], s[48:49] op_sel:[0,1] op_sel_hi:[1,1]\n"
      "v_pk_mul_f32 v[124:125], v[108:109], s[48:49] op_sel:[0,1] op_sel_hi:[1,1]\n"
      "v_pk_mul_f32 v[126:127], v[110:111], s[48:49] op_sel:[0,1] op_sel_hi:[1,1]\n"
      "v_cvt_pk_bf16_f32 v136, v120, v121\n"
      "v_cvt_pk_bf16_f32 v137, v122, v123\n"
      "v_cvt_pk_bf16_f32 v138, v124, v125\n"
      "v_cvt_pk_bf16_f32 v139, v126, v127\n"
      "buffer_store_dwordx4 v[136:139], v29, s[32:35], s43 offen sc1\n"
      "v_cvt_pk_bf16_f32 v140, v104, v105\n"
      "v_cvt_pk_bf16_f32 v141, v106, v107\n"
      "v_cvt_pk_bf16_f32 v142, v108, v109\n"
      "v_cvt_pk_bf16_f32 v143, v110, v111\n"
      "v_add3_u32 v35, v136, v137, v138\n"
      "v_add_u32 v35, v35, v139\n"
      "buffer_store_dwordx4 v[140:143], v36, s[32:35], s54 offen sc1\n"
      "v_add3_u32 v38, v140, v141, v142\n"
      "v_add_u32 v38, v38, v143\n"
      "v_add_u32_dpp v35, v35, v35 quad_perm:[1,0,3,2] row_mask:0xf bank_mask:0xf\n"
      "s_nop 0\n"
      "v_add_u32_dpp v38, v38, v38 quad_perm:[1,0,3,2] row_mask:0xf bank_mask:0xf\n"
      "v_add_u32_dpp v35, v35, v35 quad_perm:[2,3,0,1] row_mask:0xf bank_mask:0xf\n"
      "s_nop 0\n"
      "v_add_u32_dpp v38, v38, v38 quad_perm:[2,3,0,1] row_mask:0xf bank_mask:0xf\n"
      "v_add_u32_dpp v35, v35, v35 row_half_mirror row_mask:0xf bank_mask:0xf\n"
      "s_nop 0\n"
      "v_add_u32_dpp v38, v38, v38 row_ror:8 row_mask:0xf bank_mask:0xf\n"
      "v_cndmask_b32 v35, v35, v38, vcc\n"
      "v_sub_u32 v35, s30, v35\n"
      "buffer_store_dword v35, v30, s[32:35], s45 offen sc1\n"
      "s_add_u32 m0, s73, 0\n"
      "v_add_u32 v43, 0, v40\n"
      "global_load_lds_dwordx4 v43, s[4:5] offset:0 sc1\n"
      "global_load_lds_dwordx4 v43, s[4:5] offset:1024 sc1\n"
      "global_load_lds_dwordx4 v43, s[4:5] offset:2048 sc1\n"
      "global_load_lds_dwordx4 v43, s[4:5] offset:3072 sc1\n"
      "s_mov_b32 s47, 0\n"
      "buffer_load_dwordx2 v[2:3], v5, s[32:35], s47 offen sc1\n"
      "s_add_u32 m0, s73, 16384\n"
      "v_add_u32 v43, 81920, v40\n"
      "global_load_lds_dwordx4 v43, s[4:5] offset:0 sc1\n"
      "global_load_lds_dwordx4 v43, s[4:5] offset:1024 sc1\n"
      "global_load_lds_dwordx4 v43, s[4:5] offset:2048 sc1\n"
      "global_load_lds_dwordx4 v43, s[4:5] offset:3072 sc1\n"
      "global_load_dwordx4 a[104:107], v34, s[8:9] offset:-2048 sc1\n"
      "global_load_dwordx4 a[108:111], v34, s[8:9] offset:-1024 sc1\n"
      "global_load_dwordx4 a[112:115], v34, s[8:9] offset:0 sc1\n"
      "global_load_dwordx4 a[116:119], v34, s[8:9] offset:1024 sc1\n"
      "global_load_dwordx4 a[120:123], v34, s[8:9] offset:2048 sc1\n"
      "global_load_dwordx4 a[124:127], v34, s[8:9] offset:3072 sc1\n"
      "v_add_u32 v34, 36864, v7\n"
      "global_load_dwordx4 a[128:131], v34, s[8:9] offset:-4096 sc1\n"
      "global_load_dwordx4 a[132:135], v34, s[8:9] offset:-3072 sc1\n"
      "v_xor_b32 v33, 32, v6\n"
      "v_xor_b32 v34, 64, v6\n"
      "v_xor_b32 v35, 96, v6\n"
      "v_add_u32 v45, 36864, v7\n"
      "s_waitcnt vmcnt(12)\n"
      "ds_read_b128 v[248:251], v40 offset:0\n"
      "ds_read_b128 v[252:255], v40 offset:1024\n"
      "ds_read_b128 v[36:39], v40 offset:2048\n"
      "ds_read_b128 v[60:63], v40 offset:3072\n"
      "s_waitcnt lgkmcnt(3)\n"
      "v_add3_u32 v2, v2, v248, v249\n"
      "v_add3_u32 v2, v2, v250, v251\n"
      "s_waitcnt lgkmcnt(2)\n"
      "v_add3_u32 v2, v2, v252, v253\n"
      "v_add3_u32 v2, v2, v254, v255\n"
      "s_waitcnt lgkmcnt(1)\n"
      "v_add3_u32 v2, v2, v36, v37\n"
      "v_add3_u32 v2, v2, v38, v39\n"
      "s_waitcnt lgkmcnt(0)\n"
      "v_add3_u32 v2, v2, v60, v61\n"
      "v_add3_u32 v2, v2, v62, v63\n"
      "s_nop 1\n"
      "v_add_u32_dpp v2, v2, v2 quad_perm:[1,0,3,2] row_mask:0xf bank_mask:0xf\n"
      "s_nop 1\n"
      "v_add_u32_dpp v2, v2, v2 quad_perm:[2,3,0,1] row_mask:0xf bank_mask:0xf\n"
      "s_nop 1\n"
      "v_add_u32_dpp v2, v2, v2 row_half_mirror row_mask:0xf bank_mask:0xf\n"
      "v_cmp_ne_u32 vcc, s71, v2\n"
      "s_cbranch_vccnz L_sx0_%=\n"
      "L_bx0_%=:\n"
      "s_barrier\n"
      "ds_read_b64_tr_b16 v[64:65], v6 offset:0\n"
      "ds_read_b64_tr_b16 v[66:67], v6 offset:2048\n"
      "ds_read_b64_tr_b16 v[68:69], v33 offset:0\n"
      "ds_read_b64_tr_b16 v[70:71], v33 offset:2048\n"
      "ds_read_b64_tr_b16 v[72:73], v34 offset:0\n"
      "ds_read_b64_tr_b16 v[74:75], v34 offset:2048\n"
      "ds_read_b64_tr_b16 v[76:77], v35 offset:0\n"
      "ds_read_b64_tr_b16 v[78:79], v35 offset:2048\n"
      "ds_read_b64_tr_b16 v[80:81], v6 offset:4096\n"
      "ds_read_b64_tr_b16 v[82:83], v6 offset:6144\n"
      "ds_read_b64_tr_b16 v[84:85], v33 offset:4096\n"
      "ds_read_b64_tr_b16 v[86:87], v33 offset:6144\n"
      "ds_read_b64_tr_b16 v[88:89], v34 offset:4096\n"
      "ds_read_b64_tr_b16 v[90:91], v34 offset:6144\n"
      "ds_read_b64_tr_b16 v[92:93], v35 offset:4096\n"
      "ds_read_b64_tr_b16 v[94:95], v35 offset:6144\n"
      "s_waitcnt lgkmcnt(0)\n"
      "v_mfma_f32_16x16x32_bf16 v[128:131], a[0:3], v[64:67], 0\n"
      "global_load_dwordx4 a[136:139], v45, s[8:9] offset:-2048 sc1\n"
      "v_lshrrev_b32 v42, 8, v7\n"
      "v_mfma_f32_16x16x32_bf16 v[144:147], a[4:7], v[64:67], 0\n"
      "ds_read_b64_tr_b16 v[96:97], v6 offset:8192\n"
      "v_bfe_u32 v41, v7, 4, 4\n"
      "v_mfma_f32_16x16x32_bf16 v[160:163], a[8:11], v[64:67], 0\n"
      "ds_read_b64_tr_b16 v[98:99], v6 offset:10240\n"
      "s_waitcnt vmcnt(9)\n"
      "v_mfma_f32_16x16x32_bf16 v[132:135], a[0:3], v[68:71], 0\n"
      "ds_read_b128 v[248:251], v40 offset:16384\n"
      "v_min_u32 v42, 2, v42\n"
      "v_mfma_f32_16x16x32_bf16 v[148:151], a[4:7], v[68:71], 0\n"
      "global_load_dwordx4 a[140:143], v45, s[8:9] offset:-1024 sc1\n"
      "ds_read_b128 v[252:255], v40 offset:17408\n"
      "ds_read_b64_tr_b16 v[100:101], v33 offset:8192\n"
      "v_min_u32 v41, 11, v41\n"
      "v_mfma_f32_16x16x32_bf16 v[164:167], a[8:11], v[68:71], 0\n"
      "ds_read_b128 v[36:39], v40 offset:18432\n"
      "ds_read_b64_tr_b16 v[102:103], v33 offset:10240\n"
      "v_mad_u32_u24 v42, v42, 48, v41\n"
      "v_mfma_f32_16x16x32_bf16 v[136:139], a[0:3], v[72:75], 0\n"
      "ds_read_b128 v[60:63], v40 offset:19456\n"
      "ds_read_b64_tr_b16 v[104:105], v34 offset:8192\n"
      "v_lshl_add_u32 v42, v42, 6, s64\n"
      "v_mfma_f32_16x16x32_bf16 v[152:155], a[4:7], v[72:75], 0\n"
      "ds_read_b64_tr_b16 v[106:107], v34 offset:10240\n"
      "s_add_u32 s75, s76, 176128\n"
      "v_mfma_f32_16x16x32_bf16 v[168:171], a[8:11], v[72:75], 0\n"
      "global_load_dwordx4 a[144:147], v45, s[8:9] offset:0 sc1\n"
      "ds_read_b64_tr_b16 v[108:109], v35 offset:8192\n"
      "v_mfma_f32_16x16x32_bf16 v[140:143], a[0:3], v[76:79], 0\n"
      "ds_read_b64_tr_b16 v[110:111], v35 offset:10240\n"
      "v_mfma_f32_16x16x32_bf16 v[156:159], a[4:7], v[76:79], 0\n"
      "ds_read_b64_tr_b16 v[112:113], v6 offset:12288\n"
      "s_add_u32 m0, s73, 32768\n"
      "v_add_u32 v43, 16384, v40\n"
      "s_waitcnt lgkmcnt(5)\n"
      "v_add3_u32 v3, v3, v248, v249\n"
      "v_add3_u32 v3, v3, v250, v251\n"
      "v_mfma_f32_16x16x32_bf16 v[172:175], a[8:11], v[76:79], 0\n"
      "ds_read_b64_tr_b16 v[114:115], v6 offset:14336\n"
      "v_add3_u32 v3, v3, v252, v253\n"
      "v_add3_u32 v3, v3, v254, v255\n"
      "v_mfma_f32_16x16x32_bf16 v[128:131], a[12:15], v[80:83], v[128:131]\n"
      "global_load_lds_dwordx4 v43, s[4:5] offset:0 sc1\n"
      "v_add3_u32 v3, v3, v36, v37\n"
      "v_add3_u32 v3, v3, v38, v39\n"
      "v_mfma_f32_16x16x32_bf16 v[144:147], a[16:19], v[80:83], v[144:147]\n"
      "ds_read_b64_tr_b16 v[116:117], v33 offset:12288\n"
      "v_add3_u32 v3, v3, v60, v61\n"
      "v_mfma_f32_16x16x32_bf16 v[160:163], a[20:23], v[80:83], v[160:163]\n"
      "ds_read_b64_tr_b16 v[118:119], v33 offset:14336\n"
      "v_add3_u32 v3, v3, v62, v63\n"
      "v_mfma_f32_16x16x32_bf16 v[132:135], a[12:15], v[84:87], v[132:135]\n"
      "ds_read_b64_tr_b16 v[120:121], v34 offset:12288\n"
      "v_mfma_f32_16x16x32_bf16 v[148:151], a[16:19], v[84:87], v[148:151]\n"
      "global_load_lds_dwordx4 v43, s[4:5] offset:1024 sc1\n"
      "ds_read_b64_tr_b16 v[122:123], v34 offset:14336\n"
      "v_add_u32_dpp v3, v3, v3 quad_perm:[1,0,3,2] row_mask:0xf bank_mask:0xf\n"
      "v_mfma_f32_16x16x32_bf16 v[164:167], a[20:23], v[84:87], v[164:167]\n"
      "ds_read_b64_tr_b16 v[124:125], v35 offset:12288\n"
      "v_mfma_f32_16x16x32_bf16 v[136:139], a[12:15], v[88:91], v[136:139]\n"
      "ds_read_b64_tr_b16 v[126:127], v35 offset:14336\n"
      "v_add_u32_dpp v3, v3, v3 quad_perm:[2,3,0,1] row_mask:0xf bank_mask:0xf\n"
      "v_mfma_f32_16x16x32_bf16 v[152:155], a[16:19], v[88:91], v[152:155]\n"
      "v_mfma_f32_16x16x32_bf16 v[168:171], a[20:23], v[88:91], v[168:171]\n"
      "global_load_lds_dwordx4 v43, s[4:5] offset:2048 sc1\n"
      "v_add_u32_dpp v3, v3, v3 row_half_mirror row_mask:0xf bank_mask:0xf\n"
      "v_cmp_ne_u32 vcc, s71, v3\n"
      "v_mfma_f32_16x16x32_bf16 v[140:143], a[12:15], v[92:95], v[140:143]\n"
      "v_mfma_f32_16x16x32_bf16 v[156:159], a[16:19], v[92:95], v[156:159]\n"
      "s_cbranch_vccnz L_sx1_%=\n"
      "L_bx1_%=:\n"
      "v_mfma_f32_16x16x32_bf16 v[172:175], a[20:23], v[92:95], v[172:175]\n"
      "s_waitcnt lgkmcnt(0)\n"
      "s_barrier\n"
      "v_mfma_f32_16x16x32_bf16 v[128:131], a[24:27], v[96:99], v[128:131]\n"
      "global_load_lds_dwordx4 v43, s[4:5] offset:3072 sc1\n"
      "s_mov_b32 s47, 32768\n"
      "v_mfma_f32_16x16x32_bf16 v[144:147], a[28:31], v[96:99], v[144:147]\n"
      "ds_read_b64_tr_b16 v[64:65], v6 offset:16384\n"
      "v_and_b32 v41, 0x1f0, v7\n"
      "v_mfma_f32_16x16x32_bf16 v[160:163], a[32:35], v[96:99], v[160:163]\n"
      "buffer_load_dwordx2 v[48:49], v5, s[32:35], s47 offen sc1\n"
      "v_min_u32 v41, 0x170, v41\n"
      "v_mfma_f32_16x16x32_bf16 v[132:135], a[24:27], v[100:103], v[132:135]\n"
      "ds_read_b64_tr_b16 v[66:67], v6 offset:18432\n"
      "v_lshrrev_b32 v52, 9, v7\n"
      "v_mfma_f32_16x16x32_bf16 v[148:151], a[28:31], v[100:103], v[148:151]\n"
      "ds_read_b64_tr_b16 v[68:69], v34 offset:16384\n"
      "v_mul_u32_u24 v52, 0x3c000, v52\n"
      "v_mfma_f32_16x16x32_bf16 v[164:167], a[32:35], v[100:103], v[164:167]\n"
      "ds_read_b64_tr_b16 v[70:71], v34 offset:18432\n"
      "v_add3_u32 v53, v52, v41, s65\n"
      "v_mfma_f32_16x16x32_bf16 v[136:139], a[24:27], v[104:107], v[136:139]\n"
      "ds_read_b64_tr_b16 v[72:73], v33 offset:16384\n"
      "s_mul_i32 s96, s60, 384\n"
      "v_mfma_f32_16x16x32_bf16 v[152:155], a[28:31], v[104:107], v[152:155]\n"
      "ds_read_b64_tr_b16 v[74:75], v33 offset:18432\n"
      "s_add_u32 m0, s73, 49152\n"
      "v_add_u32 v43, 32768, v40\n"
      "v_mfma_f32_16x16x32_bf16 v[168:171], a[32:35], v[104:107], v[168:171]\n"
      "global_load_dwordx2 v[58:59], v42, s[26:27] sc1\n"
      "global_load_lds_dwordx4 v43, s[4:5] offset:0 sc1\n"
      "ds_read_b64_tr_b16 v[76:77], v35 offset:16384\n"
      "s_add_u32 s96, s96, 160768\n"
      "v_mfma_f32_16x16x32_bf16 v[140:143], a[24:27], v[108:111], v[140:143]\n"
      "ds_read_b64_tr_b16 v[78:79], v35 offset:18432\n"
      "v_lshrrev_b32 v52, 4, v7\n"
      "v_mfma_f32_16x16x32_bf16 v[156:159], a[28:31], v[108:111], v[156:159]\n"
      "ds_read_b64_tr_b16 v[80:81], v6 offset:20480\n"
      "v_and_b32 v52, 0x30, v52\n"
      "v_mfma_f32_16x16x32_bf16 v[172:175], a[32:35], v[108:111], v[172:175]\n"
      "ds_read_b64_tr_b16 v[82:83], v6 offset:22528\n"
      "v_add_u32 v52, s96, v52\n"
      "v_mfma_f32_16x16x32_bf16 v[128:131], a[36:39], v[112:115], v[128:131]\n"
      "global_load_lds_dwordx4 v43, s[4:5] offset:1024 sc1\n"
      "v_bfe_u32 v30, v7, 4, 3\n"
      "v_mfma_f32_16x16x32_bf16 v[144:147], a[40:43], v[112:115], v[144:147]\n"
      "ds_read_b64_tr_b16 v[84:85], v34 offset:20480\n"
      "v_lshrrev_b32 v31, 8, v7\n"
      "v_mfma_f32_16x16x32_bf16 v[160:163], a[44:47], v[112:115], v[160:163]\n"
      "ds_read_b64_tr_b16 v[86:87], v34 offset:22528\n"
      "v_lshl_add_u32 v31, v30, 2, v31\n"
      "v_mfma_f32_16x16x32_bf16 v[132:135], a[36:39], v[116:119], v[132:135]\n"
      "ds_read_b64_tr_b16 v[88:89], v33 offset:20480\n"
      "v_sub_u32 v30, 2, v30\n"
      "v_mfma_f32_16x16x32_bf16 v[148:151], a[40:43], v[116:119], v[148:151]\n"
      "global_load_lds_dwordx4 v43, s[4:5] offset:2048 sc1\n"
      "ds_read_b64_tr_b16 v[90:91], v33 offset:22528\n"
      "v_and_b32 v29, 64, v4\n"
      "v_mfma_f32_16x16x32_bf16 v[164:167], a[44:47], v[116:119], v[164:167]\n"
      "ds_read_b64_tr_b16 v[92:93], v35 offset:20480\n"
      "v_lshl_add_u32 v31, v31, 9, v29\n"
      "v_mfma_f32_16x16x32_bf16 v[136:139], a[36:39], v[120:123], v[136:139]\n"
      "buffer_load_dwordx2 v[46:47], v51, s[32:35], s75 offen sc1\n"
      "v_and_b32 v30, 0x80000000, v30\n"
      "v_mfma_f32_16x16x32_bf16 v[152:155], a[40:43], v[120:123], v[152:155]\n"
      "ds_read_b64_tr_b16 v[94:95], v35 offset:22528\n"
      "v_or_b32 v31, v31, v30\n"
      "v_mfma_f32_16x16x32_bf16 v[168:171], a[44:47], v[120:123], v[168:171]\n"
      "global_load_lds_dwordx4 v43, s[4:5] offset:3072 sc1\n"
      "s_mov_b32 s47, 65536\n"
      "v_mfma_f32_16x16x32_bf16 v[140:143], a[36:39], v[124:127], v[140:143]\n"
      "v_add_u32 v50, s94, v7\n"
      "s_mul_i32 s72, s30, 3\n"
      "v_mfma_f32_16x16x32_bf16 v[156:159], a[40:43], v[124:127], v[156:159]\n"
      "buffer_load_dwordx2 v[54:55], v5, s[32:35], s47 offen sc1\n"
      "s_lshl_b32 s42, s60, 5\n"
      "v_mfma_f32_16x16x32_bf16 v[172:175], a[44:47], v[124:127], v[172:175]\n"
      "v_xor_b32 v27, s42, v6\n"
      "s_waitcnt lgkmcnt(0)\n"
      "v_mfma_f32_16x16x32_bf16 v[176:179], a[0:3], v[64:67], 0\n"
      "ds_read_b64_tr_b16 v[96:97], v6 offset:24576\n"
      "v_add_u32 v27, 110592, v27\n"
      "v_mov_b32 v18, 0\n"
      "v_mov_b32 v19, 0\n"
      "v_mov_b32 v28, 0x3f803f80\n"
      "v_mov_b32 v29, 0x3f803f80\n"
      "v_mov_b32 v30, 0x3f803f80\n"
      "s_mul_i32 s40, s60, 9216\n"
      "v_mfma_f32_16x16x32_bf16 v[184:187], a[4:7], v[64:67], 0\n"
      "ds_read_b64_tr_b16 v[98:99], v6 offset:26624\n"
      "s_add_u32 s40, s40, 122880\n"
      "v_mfma_f32_16x16x32_bf16 v[192:195], a[8:11], v[64:67], 0\n"
      "ds_read_b64_tr_b16 v[100:101], v34 offset:24576\n"
      "s_waitcnt vmcnt(7)\n"
      "v_mfma_f32_16x16x32_bf16 v[180:183], a[0:3], v[68:71], 0\n"
      "ds_read_b128 v[248:251], v40 offset:32768\n"
      "s_lshl_b32 s41, s60, 13\n"
      "v_mfma_f32_16x16x32_bf16 v[188:191], a[4:7], v[68:71], 0\n"
      "ds_read_b128 v[252:255], v40 offset:33792\n"
      "s_add_u32 s41, s41, 65536\n"
      "v_mfma_f32_16x16x32_bf16 v[196:199], a[8:11], v[68:71], 0\n"
      "ds_read_b128 v[36:39], v40 offset:34816\n"
      "ds_read_b64_tr_b16 v[102:103], v34 offset:26624\n"
      "s_mov_b32 s48, 0xaaaaaaaa\n"
      "v_mfma_f32_16x16x32_bf16 v[200:203], a[0:3], v[72:75], 0\n"
      "ds_read_b128 v[60:63], v40 offset:35840\n"
      "ds_read_b64_tr_b16 v[104:105], v33 offset:24576\n"
      "s_mov_b32 m0, s94\n"
      "v_mfma_f32_16x16x32_bf16 v[208:211], a[4:7], v[72:75], 0\n"
      "buffer_load_dwordx4 v7, s[32:35], s67 offen offset:0 sc1 lds\n"
      "ds_read_b64_tr_b16 v[106:107], v33 offset:26624\n"
      "s_mov_b32 s49, s48\n"
      "v_mfma_f32_16x16x32_bf16 v[216:219], a[8:11], v[72:75], 0\n"
      "ds_read_b64_tr_b16 v[108:109], v35 offset:24576\n"
      "s_mov_b32 s50, 0xcccccccc\n"
      "v_mfma_f32_16x16x32_bf16 v[204:207], a[0:3], v[76:79], 0\n"
      "ds_read_b64_tr_b16 v[110:111], v35 offset:26624\n"
      "s_mov_b32 s51, s50\n"
      "v_mfma_f32_16x16x32_bf16 v[212:215], a[4:7], v[76:79], 0\n"
      "ds_read_b64_tr_b16 v[112:113], v6 offset:28672\n"
      "s_waitcnt lgkmcnt(5)\n"
      "v_add3_u32 v48, v48, v248, v249\n"
      "v_add3_u32 v48, v48, v250, v251\n"
      "v_mfma_f32_16x16x32_bf16 v[220:223], a[8:11], v[76:79], 0\n"
      "buffer_load_dwordx4 v7, s[32:35], s67 offen offset:1024 sc1 lds\n"
      "v_add3_u32 v48, v48, v252, v253\n"
      "v_add3_u32 v48, v48, v254, v255\n"
      "v_mfma_f32_16x16x32_bf16 v[176:179], a[12:15], v[80:83], v[176:179]\n"
      "ds_read_b64_tr_b16 v[114:115], v6 offset:30720\n"
      "v_add3_u32 v48, v48, v36, v37\n"
      "v_add3_u32 v48, v48, v38, v39\n"
      "v_mfma_f32_16x16x32_bf16 v[184:187], a[16:19], v[80:83], v[184:187]\n"
      "ds_read_b64_tr_b16 v[116:117], v34 offset:28672\n"
      "v_add3_u32 v48, v48, v60, v61\n"
      "v_mfma_f32_16x16x32_bf16 v[192:195], a[20:23], v[80:83], v[192:195]\n"
      "ds_read_b64_tr_b16 v[118:119], v34 offset:30720\n"
      "v_add3_u32 v48, v48, v62, v63\n"
      "v_mfma_f32_16x16x32_bf16 v[180:183], a[12:15], v[84:87], v[180:183]\n"
      "buffer_load_dwordx4 v7, s[32:35], s67 offen offset:2048 sc1 lds\n"
      "ds_read_b64_tr_b16 v[120:121], v33 offset:28672\n"
      "s_lshl_b32 s59, s60, 31\n"
      "v_mfma_f32_16x16x32_bf16 v[188:191], a[16:19], v[84:87], v[188:191]\n"
      "ds_read_b64_tr_b16 v[122:123], v33 offset:30720\n"
      "v_add_u32_dpp v48, v48, v48 quad_perm:[1,0,3,2] row_mask:0xf bank_mask:0xf\n"
      "v_mfma_f32_16x16x32_bf16 v[196:199], a[20:23], v[84:87], v[196:199]\n"
      "ds_read_b64_tr_b16 v[124:125], v35 offset:28672\n"
      "s_lshl_b32 s54, s30, 1\n"
      "v_mfma_f32_16x16x32_bf16 v[200:203], a[12:15], v[88:91], v[200:203]\n"
      "ds_read_b64_tr_b16 v[126:127], v35 offset:30720\n"
      "v_add_u32_dpp v48, v48, v48 quad_perm:[2,3,0,1] row_mask:0xf bank_mask:0xf\n"
      "v_mfma_f32_16x16x32_bf16 v[208:211], a[16:19], v[88:91], v[208:211]\n"
      "buffer_load_dword v32, v31, s[32:35], s68 offen sc1\n"
      "s_lshl_b32 s55, s60, 6\n"
      "v_mfma_f32_16x16x32_bf16 v[216:219], a[20:23], v[88:91], v[216:219]\n"
      "v_add_u32_dpp v48, v48, v48 row_half_mirror row_mask:0xf bank_mask:0xf\n"
      "v_cmp_ne_u32 vcc, s71, v48\n"
      "v_mfma_f32_16x16x32_bf16 v[204:207], a[12:15], v[92:95], v[204:207]\n"
      "s_add_u32 s55, s55, 163328\n"
      "v_mfma_f32_16x16x32_bf16 v[212:215], a[16:19], v[92:95], v[212:215]\n"
      "s_cbranch_vccnz L_sx2_%=\n"
      "L_bx2_%=:\n"
      "v_mfma_f32_16x16x32_bf16 v[220:223], a[20:23], v[92:95], v[220:223]\n"
      "s_waitcnt lgkmcnt(0)\n"
      "s_barrier\n"
      "v_mfma_f32_16x16x32_bf16 v[176:179], a[24:27], v[96:99], v[176:179]\n"
      "ds_read_b64_tr_b16 v[64:65], v6 offset:32768\n"
      "v_mfma_f32_16x16x32_bf16 v[184:187], a[28:31], v[96:99], v[184:187]\n"
      "ds_read_b64_tr_b16 v[66:67], v6 offset:34816\n"
      "v_mfma_f32_16x16x32_bf16 v[192:195], a[32:35], v[96:99], v[192:195]\n"
      "ds_read_b64_tr_b16 v[68:69], v33 offset:32768\n"
      "s_add_u32 m0, s73, 0\n"
      "v_add_u32 v43, 49152, v40\n"
      "v_add_u32 v44, 4096, v7\n"
      "s_waitcnt vmcnt(6)\n"
      "v_cmp_ne_u64 s[52:53], s[30:31], v[46:47]\n"
      "v_mfma_f32_16x16x32_bf16 v[180:183], a[24:27], v[100:103], v[180:183]\n"
      "ds_read_b64_tr_b16 v[70:71], v33 offset:34816\n"
      "v_cmp_ne_u64 vcc, s[30:31], v[58:59]\n"
      "v_mfma_f32_16x16x32_bf16 v[188:191], a[28:31], v[100:103], v[188:191]\n"
      "global_load_lds_dwordx4 v43, s[4:5] offset:0 sc1\n"
      "s_waitcnt vmcnt(5)\n"
      "v_mfma_f32_16x16x32_bf16 v[196:199], a[32:35], v[100:103], v[196:199]\n"
      "ds_read_b128 v[248:251], v40 offset:49152\n"
      "s_or_b64 vcc, vcc, s[52:53]\n"
      "v_mfma_f32_16x16x32_bf16 v[200:203], a[24:27], v[104:107], v[200:203]\n"
      "ds_read_b128 v[252:255], v40 offset:50176\n"
      "ds_read_b64_tr_b16 v[72:73], v34 offset:32768\n"
      "s_cbranch_vccnz L_sfd_%=\n"
      "L_bfd_%=:\n"
      "v_mfma_f32_16x16x32_bf16 v[208:211], a[28:31], v[104:107], v[208:211]\n"
      "ds_read_b128 v[36:39], v40 offset:51200\n"
      "ds_read_b64_tr_b16 v[74:75], v34 offset:34816\n"
      "v_add_u32 v51, s96, v41\n"
      "v_mfma_f32_16x16x32_bf16 v[216:219], a[32:35], v[104:107], v[216:219]\n"
      "global_load_lds_dwordx4 v43, s[4:5] offset:1024 sc1\n"
      "ds_read_b128 v[60:63], v40 offset:52224\n"
      "ds_read_b64_tr_b16 v[76:77], v35 offset:32768\n"
      "v_lshrrev_b32 v42, 2, v7\n"
      "v_mfma_f32_16x16x32_bf16 v[204:207], a[24:27], v[108:111], v[204:207]\n"
      "ds_read_b64_tr_b16 v[78:79], v35 offset:34816\n"
      "s_lshl_b32 s97, s60, 10\n"
      "v_mfma_f32_16x16x32_bf16 v[212:215], a[28:31], v[108:111], v[212:215]\n"
      "ds_read_b64_tr_b16 v[80:81], v6 offset:36864\n"
      "s_add_u32 s96, s97, 98304\n"
      "v_mfma_f32_16x16x32_bf16 v[220:223], a[32:35], v[108:111], v[220:223]\n"
      "ds_read_b64_tr_b16 v[82:83], v6 offset:38912\n"
      "v_add_u32 v47, s96, v7\n"
      "v_mfma_f32_16x16x32_bf16 v[176:179], a[36:39], v[112:115], v[176:179]\n"
      "global_load_lds_dwordx4 v43, s[4:5] offset:2048 sc1\n"
      "s_waitcnt lgkmcnt(4)\n"
      "v_add3_u32 v54, v54, v248, v249\n"
      "v_add3_u32 v54, v54, v250, v251\n"
      "v_mfma_f32_16x16x32_bf16 v[184:187], a[40:43], v[112:115], v[184:187]\n"
      "ds_read_b64_tr_b16 v[84:85], v33 offset:36864\n"
      "v_add3_u32 v54, v54, v252, v253\n"
      "v_add3_u32 v54, v54, v254, v255\n"
      "v_mfma_f32_16x16x32_bf16 v[192:195], a[44:47], v[112:115], v[192:195]\n"
      "ds_read_b64_tr_b16 v[86:87], v33 offset:38912\n"
      "v_add3_u32 v54, v54, v36, v37\n"
      "v_add3_u32 v54, v54, v38, v39\n"
      "v_mfma_f32_16x16x32_bf16 v[180:183], a[36:39], v[116:119], v[180:183]\n"
      "ds_read_b64_tr_b16 v[88:89], v34 offset:36864\n"
      "v_add3_u32 v54, v54, v60, v61\n"
      "v_mfma_f32_16x16x32_bf16 v[188:191], a[40:43], v[116:119], v[188:191]\n"
      "global_load_lds_dwordx4 v43, s[4:5] offset:3072 sc1\n"
      "ds_read_b64_tr_b16 v[90:91], v34 offset:38912\n"
      "v_add3_u32 v54, v54, v62, v63\n"
      "v_mfma_f32_16x16x32_bf16 v[196:199], a[44:47], v[116:119], v[196:199]\n"
      "ds_read_b64_tr_b16 v[92:93], v35 offset:36864\n"
      "s_mov_b32 s47, 98304\n"
      "v_mfma_f32_16x16x32_bf16 v[200:203], a[36:39], v[120:123], v[200:203]\n"
      "ds_read_b64_tr_b16 v[94:95], v35 offset:38912\n"
      "v_add_u32_dpp v54, v54, v54 quad_perm:[1,0,3,2] row_mask:0xf bank_mask:0xf\n"
      "v_mfma_f32_16x16x32_bf16 v[208:211], a[40:43], v[120:123], v[208:211]\n"
      "v_bfe_u32 v46, v7, 8, 1\n"
      "v_xor_b32 v46, s60, v46\n"
      "v_mfma_f32_16x16x32_bf16 v[216:219], a[44:47], v[120:123], v[216:219]\n"
      "buffer_load_dwordx2 v[56:57], v5, s[32:35], s47 offen sc1\n"
      "v_add_u32_dpp v54, v54, v54 quad_perm:[2,3,0,1] row_mask:0xf bank_mask:0xf\n"
      "v_mfma_f32_16x16x32_bf16 v[204:207], a[36:39], v[124:127], v[204:207]\n"
      "v_lshl_add_u32 v46, v46, 2, v7\n"
      "v_add_u32 v46, 98304, v46\n"
      "v_mfma_f32_16x16x32_bf16 v[212:215], a[40:43], v[124:127], v[212:215]\n"
      "v_add_u32_dpp v54, v54, v54 row_half_mirror row_mask:0xf bank_mask:0xf\n"
      "v_cmp_ne_u32 vcc, s71, v54\n"
      "v_mfma_f32_16x16x32_bf16 v[220:223], a[44:47], v[124:127], v[220:223]\n"
      "s_waitcnt lgkmcnt(0)\n"
      "v_mfma_f32_16x16x32_bf16 v[128:131], a[48:51], v[64:67], v[128:131]\n"
      "ds_read_b64_tr_b16 v[96:97], v6 offset:40960\n"
      "s_cbranch_vccnz L_sx3_%=\n"
      "L_bx3_%=:\n"
      "v_and_b32 v41, 0x78, v4\n"
      "v_mfma_f32_16x16x32_bf16 v[144:147], a[52:55], v[64:67], v[144:147]\n"
      "ds_read_b64_tr_b16 v[98:99], v6 offset:43008\n"
      "v_add_u32 v41, 162304, v41\n"
      "v_mfma_f32_16x16x32_bf16 v[160:163], a[56:59], v[64:67], v[160:163]\n"
      "ds_read_b64_tr_b16 v[100:101], v33 offset:40960\n"
      "s_add_u32 m0, s73, 16384\n"
      "v_add_u32 v43, 65536, v40\n"
      "s_waitcnt vmcnt(5)\n"
      "v_mfma_f32_16x16x32_bf16 v[132:135], a[48:51], v[68:71], v[132:135]\n"
      "ds_read_b128 v[248:251], v50 offset:0\n"
      "v_mfma_f32_16x16x32_bf16 v[148:151], a[52:55], v[68:71], v[148:151]\n"
      "global_load_dwordx4 a[148:151], v45, s[8:9] offset:1024 sc1\n"
      "ds_read_b128 v[252:255], v50 offset:1024\n"
      "v_mfma_f32_16x16x32_bf16 v[164:167], a[56:59], v[68:71], v[164:167]\n"
      "ds_read_b64_tr_b16 v[102:103], v33 offset:43008\n"
      "v_mfma_f32_16x16x32_bf16 v[136:139], a[48:51], v[72:75], v[136:139]\n"
      "ds_read_b128 v[36:39], v50 offset:2048\n"
      "ds_read_b64_tr_b16 v[104:105], v34 offset:40960\n"
      "v_mfma_f32_16x16x32_bf16 v[152:155], a[52:55], v[72:75], v[152:155]\n"
      "ds_read_b64_tr_b16 v[106:107], v34 offset:43008\n"
      "v_mfma_f32_16x16x32_bf16 v[168:171], a[56:59], v[72:75], v[168:171]\n"
      "global_load_dwordx4 v[224:227], v53, s[26:27] sc1\n"
      "ds_read_b64_tr_b16 v[108:109], v35 offset:40960\n"
      "v_mfma_f32_16x16x32_bf16 v[140:143], a[48:51], v[76:79], v[140:143]\n"
      "ds_read_b64_tr_b16 v[110:111], v35 offset:43008\n"
      "v_add_u32 v53, 0x6000, v53\n"
      "v_mfma_f32_16x16x32_bf16 v[156:159], a[52:55], v[76:79], v[156:159]\n"
      "ds_read_b64_tr_b16 v[112:113], v6 offset:45056\n"
      "s_waitcnt lgkmcnt(5)\n"
      "v_add3_u32 v32, v32, v248, v249\n"
      "v_add3_u32 v32, v32, v250, v251\n"
      "v_mfma_f32_16x16x32_bf16 v[172:175], a[56:59], v[76:79], v[172:175]\n"
      "global_load_dwordx4 a[152:155], v45, s[8:9] offset:2048 sc1\n"
      "v_add3_u32 v32, v32, v252, v253\n"
      "v_mfma_f32_16x16x32_bf16 v[128:131], a[60:63], v[80:83], v[128:131]\n"
      "ds_read_b64_tr_b16 v[114:115], v6 offset:47104\n"
      "v_add3_u32 v32, v32, v254, v255\n"
      "v_mfma_f32_16x16x32_bf16 v[144:147], a[64:67], v[80:83], v[144:147]\n"
      "ds_read_b64_tr_b16 v[116:117], v33 offset:45056\n"
      "v_add3_u32 v32, v32, v36, v37\n"
      "v_mfma_f32_16x16x32_bf16 v[160:163], a[68:71], v[80:83], v[160:163]\n"
      "ds_read_b64_tr_b16 v[118:119], v33 offset:47104\n"
      "v_add3_u32 v32, v32, v38, v39\n"
      "v_mfma_f32_16x16x32_bf16 v[132:135], a[60:63], v[84:87], v[132:135]\n"
      "global_load_dwordx4 v[228:231], v53, s[26:27] sc1\n"
      "ds_read_b64_tr_b16 v[120:121], v34 offset:45056\n"
      "v_mfma_f32_16x16x32_bf16 v[148:151], a[64:67], v[84:87], v[148:151]\n"
      "ds_read_b64_tr_b16 v[122:123], v34 offset:47104\n"
      "v_add_u32 v53, 0x6000, v53\n"
      "v_add_u32_dpp v32, v32, v32 quad_perm:[1,0,3,2] row_mask:0xf bank_mask:0xf\n"
      "v_mfma_f32_16x16x32_bf16 v[164:167], a[68:71], v[84:87], v[164:167]\n"
      "ds_read_b64_tr_b16 v[124:125], v35 offset:45056\n"
      "v_mfma_f32_16x16x32_bf16 v[136:139], a[60:63], v[88:91], v[136:139]\n"
      "global_load_dwordx4 v[232:235], v53, s[26:27] sc1\n"
      "v_add_u32_dpp v32, v32, v32 quad_perm:[2,3,0,1] row_mask:0xf bank_mask:0xf\n"
      "v_mfma_f32_16x16x32_bf16 v[152:155], a[64:67], v[88:91], v[152:155]\n"
      "ds_read_b64_tr_b16 v[126:127], v35 offset:47104\n"
      "v_add_u32 v53, 0x6000, v53\n"
      "v_mfma_f32_16x16x32_bf16 v[168:171], a[68:71], v[88:91], v[168:171]\n"
      "v_add_u32_dpp v32, v32, v32 row_half_mirror row_mask:0xf bank_mask:0xf\n"
      "v_cmp_ne_u32 vcc, s72, v32\n"
      "v_mfma_f32_16x16x32_bf16 v[140:143], a[60:63], v[92:95], v[140:143]\n"
      "global_load_dwordx4 v[236:239], v53, s[26:27] sc1\n"
      "v_mfma_f32_16x16x32_bf16 v[156:159], a[64:67], v[92:95], v[156:159]\n"
      "v_add_u32 v53, 0x6000, v53\n"
      "s_cbranch_vccnz L_sxp_%=\n"
      "L_bxp_%=:\n"
      "v_mfma_f32_16x16x32_bf16 v[172:175], a[68:71], v[92:95], v[172:175]\n"
      "s_waitcnt lgkmcnt(0)\n"
      "s_barrier\n"
      "v_mfma_f32_16x16x32_bf16 v[128:131], a[72:75], v[96:99], v[128:131]\n"
      "global_load_dwordx4 v[240:243], v53, s[26:27] sc1\n"
      "ds_read_b64_tr_b16 v[8:9], v27 offset:0\n"
      "v_mfma_f32_16x16x32_bf16 v[144:147], a[76:79], v[96:99], v[144:147]\n"
      "ds_read_b64_tr_b16 v[10:11], v27 offset:2048\n"
      "v_add_u32 v53, 0x6000, v53\n"
      "v_mfma_f32_16x16x32_bf16 v[160:163], a[80:83], v[96:99], v[160:163]\n"
      "ds_read_b64_tr_b16 v[12:13], v27 offset:4096\n"
      "ds_read_b64_tr_b16 v[64:65], v6 offset:49152\n"
      "v_mfma_f32_16x16x32_bf16 v[132:135], a[72:75], v[100:103], v[132:135]\n"
      "global_load_dwordx4 a[156:159], v45, s[8:9] offset:3072 sc1\n"
      "ds_read_b64_tr_b16 v[14:15], v27 offset:6144\n"
      "ds_read_b64_tr_b16 v[66:67], v6 offset:51200\n"
      "v_mfma_f32_16x16x32_bf16 v[148:151], a[76:79], v[100:103], v[148:151]\n"
      "ds_read_b64_tr_b16 v[16:17], v27 offset:8192\n"
      "v_mov_b32 v31, 0x3f803f80\n"
      "v_mfma_f32_16x16x32_bf16 v[164:167], a[80:83], v[100:103], v[164:167]\n"
      "ds_read_b64_tr_b16 v[68:69], v33 offset:49152\n"
      "v_add_u32 v45, 45056, v7\n"
      "v_mfma_f32_16x16x32_bf16 v[136:139], a[72:75], v[104:107], v[136:139]\n"
      "ds_read_b64_tr_b16 v[70:71], v33 offset:51200\n"
      "v_and_b32 v50, 0xf0, v7\n"
      "v_mfma_f32_16x16x32_bf16 v[152:155], a[76:79], v[104:107], v[152:155]\n"
      "global_load_dwordx4 v[244:247], v53, s[26:27] sc1\n"
      "s_mov_b32 s42, 0x00f0000f\n"
      "v_mfma_f32_16x16x32_bf16 v[168:171], a[80:83], v[104:107], v[168:171]\n"
      "ds_read_b64_tr_b16 v[72:73], v34 offset:49152\n"
      "s_mov_b32 s43, 0xf0000f00\n"
      "v_mfma_f32_16x16x32_bf16 v[140:143], a[72:75], v[108:111], v[140:143]\n"
      "ds_read_b64_tr_b16 v[74:75], v34 offset:51200\n"
      "v_bfrev_b32 v32, 1\n"
      "v_cndmask_b32 v50, v32, v50, s[42:43]\n"
      "v_mfma_f32_16x16x32_bf16 v[156:159], a[76:79], v[108:111], v[156:159]\n"
      "global_load_dwordx4 a[160:163], v45, s[8:9] offset:-4096 sc1\n"
      "v_mfma_f32_16x16x32_bf16 v[172:175], a[80:83], v[108:111], v[172:175]\n"
      "ds_read_b64_tr_b16 v[76:77], v35 offset:49152\n"
      "s_waitcnt lgkmcnt(5)\n"
      "v_mfma_f32_16x16x32_bf16 v[20:23], v[28:31], v[8:11], 0\n"
      "ds_read_b64_tr_b16 v[78:79], v35 offset:51200\n"
      "v_mfma_f32_16x16x32_bf16 v[24:27], v[8:11], v[8:11], 0\n"
      "ds_read_b64_tr_b16 v[80:81], v6 offset:53248\n"
      "v_mfma_f32_16x16x32_bf16 v[20:23], v[28:31], v[12:15], v[20:23]\n"
      "global_load_dwordx4 a[164:167], v45, s[8:9] offset:-3072 sc1\n"
      "v_mfma_f32_16x16x32_bf16 v[24:27], v[12:15], v[12:15], v[24:27]\n"
      "ds_read_b64_tr_b16 v[82:83], v6 offset:55296\n"
      "v_mfma_f32_16x16x32_bf16 v[20:23], v[28:31], v[16:19], v[20:23]\n"
      "ds_read_b64_tr_b16 v[84:85], v33 offset:53248\n"
      "v_mfma_f32_16x16x32_bf16 v[24:27], v[16:19], v[16:19], v[24:27]\n"
      "ds_read_b64_tr_b16 v[86:87], v33 offset:55296\n"
      "v_mfma_f32_16x16x32_bf16 v[128:131], a[84:87], v[112:115], v[128:131]\n"
      "global_load_lds_dwordx4 v43, s[4:5] offset:0 sc1\n"
      "v_mfma_f32_16x16x32_bf16 v[144:147], a[88:91], v[112:115], v[144:147]\n"
      "ds_read_b64_tr_b16 v[88:89], v34 offset:53248\n"
      "v_mfma_f32_16x16x32_bf16 v[160:163], a[92:95], v[112:115], v[160:163]\n"
      "ds_read_b64_tr_b16 v[90:91], v34 offset:55296\n"
      "v_cndmask_b32 v24, v24, v25, s[48:49]\n"
      "v_cndmask_b32 v26, v26, v27, s[48:49]\n"
      "v_mfma_f32_16x16x32_bf16 v[132:135], a[84:87], v[116:119], v[132:135]\n"
      "ds_read_b64_tr_b16 v[92:93], v35 offset:53248\n"
      "v_mov_b32 v22, s30\n"
      "v_cndmask_b32 v21, v24, v26, s[50:51]\n"
      "v_mfma_f32_16x16x32_bf16 v[148:151], a[88:91], v[116:119], v[148:151]\n"
      "global_load_dwordx4 a[168:171], v45, s[8:9] offset:-2048 sc1\n"
      "v_mov_b32 v23, s31\n"
      "v_mfma_f32_16x16x32_bf16 v[164:167], a[92:95], v[116:119], v[164:167]\n"
      "buffer_store_dwordx4 v[20:23], v50, s[32:35], s92 offen sc1\n"
      "v_and_b32 v32, 0x200, v7\n"
      "v_mfma_f32_16x16x32_bf16 v[136:139], a[84:87], v[120:123], v[136:139]\n"
      "ds_read_b64_tr_b16 v[94:95], v35 offset:55296\n"
      "v_and_b32 v50, 0x1f0, v7\n"
      "v_mfma_f32_16x16x32_bf16 v[152:155], a[88:91], v[120:123], v[152:155]\n"
      "global_load_lds_dwordx4 v43, s[4:5] offset:1024 sc1\n"
      "v_lshl_add_u32 v50, v32, 3, v50\n"
      "v_mfma_f32_16x16x32_bf16 v[168:171], a[92:95], v[120:123], v[168:171]\n"
      "global_load_dwordx4 a[172:175], v45, s[8:9] offset:-1024 sc1\n"
      "v_and_b32 v32, 0xf8, v4\n"
      "v_mfma_f32_16x16x32_bf16 v[140:143], a[84:87], v[124:127], v[140:143]\n"
      "v_add_u32 v32, s95, v32\n"
      "v_mfma_f32_16x16x32_bf16 v[156:159], a[88:91], v[124:127], v[156:159]\n"
      "v_mfma_f32_16x16x32_bf16 v[172:175], a[92:95], v[124:127], v[172:175]\n"
      "global_load_lds_dwordx4 v43, s[4:5] offset:2048 sc1\n"
      "s_waitcnt lgkmcnt(0)\n"
      "v_mfma_f32_16x16x32_bf16 v[128:131], a[96:99], v[64:67], v[128:131]\n"
      "global_load_dwordx4 a[176:179], v45, s[8:9] offset:0 sc1\n"
      "v_mfma_f32_16x16x32_bf16 v[144:147], a[100:103], v[64:67], v[144:147]\n"
      "ds_read_b64_tr_b16 v[96:97], v6 offset:57344\n"
      "v_mfma_f32_16x16x32_bf16 v[160:163], a[104:107], v[64:67], v[160:163]\n"
      "ds_read_b64_tr_b16 v[98:99], v6 offset:59392\n"
      "s_waitcnt vmcnt(18)\n"
      "v_mfma_f32_16x16x32_bf16 v[132:135], a[96:99], v[68:71], v[132:135]\n"
      "global_load_lds_dwordx4 v43, s[4:5] offset:3072 sc1\n"
      "ds_read_b128 v[248:251], v40 offset:0\n"
      "s_mov_b32 s47, 131072\n"
      "v_mfma_f32_16x16x32_bf16 v[148:151], a[100:103], v[68:71], v[148:151]\n"
      "global_load_dwordx4 a[180:183], v45, s[8:9] offset:1024 sc1\n"
      "ds_read_b128 v[252:255], v40 offset:1024\n"
      "ds_read_b64_tr_b16 v[100:101], v33 offset:57344\n"
      "v_mfma_f32_16x16x32_bf16 v[164:167], a[104:107], v[68:71], v[164:167]\n"
      "buffer_load_dwordx2 v[2:3], v5, s[32:35], s47 offen sc1\n"
      "ds_read_b128 v[36:39], v40 offset:2048\n"
      "ds_read_b64_tr_b16 v[102:103], v33 offset:59392\n"
      "v_mfma_f32_16x16x32_bf16 v[136:139], a[96:99], v[72:75], v[136:139]\n"
      "ds_read_b128 v[60:63], v40 offset:3072\n"
      "ds_read_b64_tr_b16 v[104:105], v34 offset:57344\n"
      "v_mfma_f32_16x16x32_bf16 v[152:155], a[100:103], v[72:75], v[152:155]\n"
      "ds_read_b64_tr_b16 v[106:107], v34 offset:59392\n"
      "v_mfma_f32_16x16x32_bf16 v[168:171], a[104:107], v[72:75], v[168:171]\n"
      "global_load_dwordx4 a[184:187], v45, s[8:9] offset:2048 sc1\n"
      "ds_read_b64_tr_b16 v[108:109], v35 offset:57344\n"
      "v_mfma_f32_16x16x32_bf16 v[140:143], a[96:99], v[76:79], v[140:143]\n"
      "ds_read_b64_tr_b16 v[110:111], v35 offset:59392\n"
      "v_mfma_f32_16x16x32_bf16 v[156:159], a[100:103], v[76:79], v[156:159]\n"
      "ds_read_b64_tr_b16 v[112:113], v6 offset:61440\n"
      "s_waitcnt lgkmcnt(5)\n"
      "v_add3_u32 v56, v56, v248, v249\n"
      "v_add3_u32 v56, v56, v250, v251\n"
      "v_mfma_f32_16x16x32_bf16 v[172:175], a[104:107], v[76:79], v[172:175]\n"
      "ds_read_b64_tr_b16 v[114:115], v6 offset:63488\n"
      "v_add3_u32 v56, v56, v252, v253\n"
      "v_add3_u32 v56, v56, v254, v255\n"
      "v_mfma_f32_16x16x32_bf16 v[128:131], a[108:111], v[80:83], v[128:131]\n"
      "global_load_dwordx4 a[188:191], v45, s[8:9] offset:3072 sc1\n"
      "v_add3_u32 v56, v56, v36, v37\n"
      "v_add3_u32 v56, v56, v38, v39\n"
      "v_mfma_f32_16x16x32_bf16 v[144:147], a[112:115], v[80:83], v[144:147]\n"
      "ds_read_b64_tr_b16 v[116:117], v33 offset:61440\n"
      "v_add3_u32 v56, v56, v60, v61\n"
      "v_mfma_f32_16x16x32_bf16 v[160:163], a[116:119], v[80:83], v[160:163]\n"
      "ds_read_b64_tr_b16 v[118:119], v33 offset:63488\n"
      "v_add3_u32 v56, v56, v62, v63\n"
      "v_mfma_f32_16x16x32_bf16 v[132:135], a[108:111], v[84:87], v[132:135]\n"
      "ds_read_b64_tr_b16 v[120:121], v34 offset:61440\n"
      "v_add_u32 v45, 53248, v7\n"
      "v_mfma_f32_16x16x32_bf16 v[148:151], a[112:115], v[84:87], v[148:151]\n"
      "global_load_dwordx4 a[192:195], v45, s[8:9] offset:-4096 sc1\n"
      "ds_read_b64_tr_b16 v[122:123], v34 offset:63488\n"
      "v_add_u32_dpp v56, v56, v56 quad_perm:[1,0,3,2] row_mask:0xf bank_mask:0xf\n"
      "v_mfma_f32_16x16x32_bf16 v[164:167], a[116:119], v[84:87], v[164:167]\n"
      "ds_read_b64_tr_b16 v[124:125], v35 offset:61440\n"
      "s_waitcnt vmcnt(15)\n"
      "v_mfma_f32_16x16x32_bf16 v[136:139], a[108:111], v[88:91], v[136:139]\n"
      "ds_read_b64_tr_b16 v[126:127], v35 offset:63488\n"
      "v_add_u32_dpp v56, v56, v56 quad_perm:[2,3,0,1] row_mask:0xf bank_mask:0xf\n"
      "v_mfma_f32_16x16x32_bf16 v[152:155], a[112:115], v[88:91], v[152:155]\n"
      "v_add_f32 v224, v224, v228\n"
      "v_add_f32 v225, v225, v229\n"
      "v_mfma_f32_16x16x32_bf16 v[168:171], a[116:119], v[88:91], v[168:171]\n"
      "global_load_dwordx4 a[196:199], v45, s[8:9] offset:-3072 sc1\n"
      "v_add_u32_dpp v56, v56, v56 row_half_mirror row_mask:0xf bank_mask:0xf\n"
      "v_cmp_ne_u32 vcc, s71, v56\n"
      "v_mfma_f32_16x16x32_bf16 v[140:143], a[108:111], v[92:95], v[140:143]\n"
      "v_add_f32 v226, v226, v230\n"
      "v_add_f32 v227, v227, v231\n"
      "v_mfma_f32_16x16x32_bf16 v[156:159], a[112:115], v[92:95], v[156:159]\n"
      "s_cbranch_vccnz L_sx4_%=\n"
      "L_bx4_%=:\n"
      "v_add_f32 v232, v232, v236\n"
      "v_mfma_f32_16x16x32_bf16 v[172:175], a[116:119], v[92:95], v[172:175]\n"
      "s_waitcnt lgkmcnt(0)\n"
      "s_barrier\n"
      "v_mfma_f32_16x16x32_bf16 v[128:131], a[120:123], v[96:99], v[128:131]\n"
      "global_load_dwordx4 a[200:203], v45, s[8:9] offset:-2048 sc1\n"
      "v_add_f32 v233, v233, v237\n"
      "v_mfma_f32_16x16x32_bf16 v[144:147], a[124:127], v[96:99], v[144:147]\n"
      "ds_read_b64_tr_b16 v[64:65], v6 offset:0\n"
      "v_add_f32 v234, v234, v238\n"
      "v_mfma_f32_16x16x32_bf16 v[160:163], a[128:131], v[96:99], v[160:163]\n"
      "ds_read_b64_tr_b16 v[66:67], v6 offset:2048\n"
      "v_add_f32 v235, v235, v239\n"
      "v_mfma_f32_16x16x32_bf16 v[132:135], a[120:123], v[100:103], v[132:135]\n"
      "ds_read_b64_tr_b16 v[68:69], v33 offset:0\n"
      "v_add_f32 v240, v240, v244\n"
      "v_mfma_f32_16x16x32_bf16 v[148:151], a[124:127], v[100:103], v[148:151]\n"
      "global_load_dwordx4 a[204:207], v45, s[8:9] offset:-1024 sc1\n"
      "v_add_f32 v241, v241, v245\n"
      "v_mfma_f32_16x16x32_bf16 v[164:167], a[128:131], v[100:103], v[164:167]\n"
      "ds_read_b64_tr_b16 v[70:71], v33 offset:2048\n"
      "v_add_f32 v242, v242, v246\n"
      "v_mfma_f32_16x16x32_bf16 v[136:139], a[120:123], v[104:107], v[136:139]\n"
      "ds_read_b64_tr_b16 v[72:73], v34 offset:0\n"
      "v_add_f32 v243, v243, v247\n"
      "v_mfma_f32_16x16x32_bf16 v[152:155], a[124:127], v[104:107], v[152:155]\n"
      "ds_read_b64_tr_b16 v[74:75], v34 offset:2048\n"
      "v_add_f32 v224, v224, v232\n"
      "v_mfma_f32_16x16x32_bf16 v[168:171], a[128:131], v[104:107], v[168:171]\n"
      "global_load_dwordx4 a[208:211], v45, s[8:9] offset:0 sc1\n"
      "ds_read_b64_tr_b16 v[76:77], v35 offset:0\n"
      "v_add_f32 v225, v225, v233\n"
      "v_mfma_f32_16x16x32_bf16 v[140:143], a[120:123], v[108:111], v[140:143]\n"
      "ds_read_b64_tr_b16 v[78:79], v35 offset:2048\n"
      "v_add_f32 v226, v226, v234\n"
      "v_mfma_f32_16x16x32_bf16 v[156:159], a[124:127], v[108:111], v[156:159]\n"
      "ds_read_b64_tr_b16 v[80:81], v6 offset:4096\n"
      "v_add_f32 v227, v227, v235\n"
      "v_mfma_f32_16x16x32_bf16 v[172:175], a[128:131], v[108:111], v[172:175]\n"
      "ds_read_b64_tr_b16 v[82:83], v6 offset:6144\n"
      "v_add_f32 v224, v224, v240\n"
      "v_mfma_f32_16x16x32_bf16 v[128:131], a[132:135], v[112:115], v[128:131]\n"
      "global_load_dwordx4 a[212:215], v45, s[8:9] offset:1024 sc1\n"
      "v_add_f32 v225, v225, v241\n"
      "v_mfma_f32_16x16x32_bf16 v[144:147], a[136:139], v[112:115], v[144:147]\n"
      "ds_read_b64_tr_b16 v[84:85], v33 offset:4096\n"
      "v_add_f32 v226, v226, v242\n"
      "v_mfma_f32_16x16x32_bf16 v[160:163], a[140:143], v[112:115], v[160:163]\n"
      "ds_read_b64_tr_b16 v[86:87], v33 offset:6144\n"
      "s_add_u32 m0, s73, 32768\n"
      "v_add_u32 v43, 98304, v40\n"
      "v_mfma_f32_16x16x32_bf16 v[132:135], a[132:135], v[116:119], v[132:135]\n"
      "global_load_lds_dwordx4 v43, s[4:5] offset:0 sc1\n"
      "ds_read_b64_tr_b16 v[88:89], v34 offset:4096\n"
      "v_add_f32 v227, v227, v243\n"
      "v_mfma_f32_16x16x32_bf16 v[148:151], a[136:139], v[116:119], v[148:151]\n"
      "global_load_dwordx4 a[216:219], v45, s[8:9] offset:2048 sc1\n"
      "ds_read_b64_tr_b16 v[90:91], v34 offset:6144\n"
      "v_add_u32 v53, 0x6000, v53\n"
      "v_mfma_f32_16x16x32_bf16 v[164:167], a[140:143], v[116:119], v[164:167]\n"
      "global_load_dwordx4 v[228:231], v53, s[26:27] sc1\n"
      "ds_read_b64_tr_b16 v[92:93], v35 offset:4096\n"
      "v_add_u32 v53, 0x6000, v53\n"
      "v_mfma_f32_16x16x32_bf16 v[136:139], a[132:135], v[120:123], v[136:139]\n"
      "global_load_dwordx4 v[232:235], v53, s[26:27] sc1\n"
      "v_add_u32 v53, 0x6000, v53\n"
      "v_mfma_f32_16x16x32_bf16 v[152:155], a[136:139], v[120:123], v[152:155]\n"
      "global_load_lds_dwordx4 v43, s[4:5] offset:1024 sc1\n"
      "global_load_dwordx4 v[236:239], v53, s[26:27] sc1\n"
      "ds_read_b64_tr_b16 v[94:95], v35 offset:6144\n"
      "v_add_u32 v53, 0x6000, v53\n"
      "v_mfma_f32_16x16x32_bf16 v[168:171], a[140:143], v[120:123], v[168:171]\n"
      "global_load_dwordx4 a[220:223], v45, s[8:9] offset:3072 sc1\n"
      "global_load_dwordx4 v[240:243], v53, s[26:27] sc1\n"
      "v_and_b32 v53, 0x300, v7\n"
      "v_mfma_f32_16x16x32_bf16 v[140:143], a[132:135], v[124:127], v[140:143]\n"
      "v_and_b32 v59, 0x30, v7\n"
      "v_lshl_add_u32 v53, v59, 2, v53\n"
      "v_mfma_f32_16x16x32_bf16 v[156:159], a[136:139], v[124:127], v[156:159]\n"
      "v_bfe_u32 v59, v7, 6, 2\n"
      "v_lshl_add_u32 v53, v59, 4, v53\n"
      "v_lshrrev_b32 v59, 6, v7\n"
      "v_mfma_f32_16x16x32_bf16 v[172:175], a[140:143], v[124:127], v[172:175]\n"
      "global_load_lds_dwordx4 v43, s[4:5] offset:2048 sc1\n"
      "v_add_u32 v45, 61440, v7\n"
      "s_waitcnt vmcnt(27)\n"
      "s_waitcnt lgkmcnt(0)\n"
      "v_mfma_f32_16x16x32_bf16 v[128:131], a[144:147], v[64:67], v[128:131]\n"
      "global_load_dwordx4 a[224:227], v45, s[8:9] offset:-4096 sc1\n"
      "buffer_load_dwordx4 v[244:247], v53, s[32:35], s66 offen sc1\n"
      "v_mfma_f32_16x16x32_bf16 v[144:147], a[148:151], v[64:67], v[144:147]\n"
      "ds_read_b64_tr_b16 v[96:97], v6 offset:8192\n"
      "v_mfma_f32_16x16x32_bf16 v[160:163], a[152:155], v[64:67], v[160:163]\n"
      "ds_read_b64_tr_b16 v[98:99], v6 offset:10240\n"
      "s_waitcnt vmcnt(19)\n"
      "v_mfma_f32_16x16x32_bf16 v[132:135], a[144:147], v[68:71], v[132:135]\n"
      "global_load_lds_dwordx4 v43, s[4:5] offset:3072 sc1\n"
      "ds_read_b128 v[248:251], v40 offset:16384\n"
      "v_mfma_f32_16x16x32_bf16 v[148:151], a[148:151], v[68:71], v[148:151]\n"
      "global_load_dwordx4 a[228:231], v45, s[8:9] offset:-3072 sc1\n"
      "ds_read_b128 v[252:255], v40 offset:17408\n"
      "ds_read_b64_tr_b16 v[100:101], v33 offset:8192\n"
      "v_mfma_f32_16x16x32_bf16 v[164:167], a[152:155], v[68:71], v[164:167]\n"
      "ds_read_b128 v[36:39], v40 offset:18432\n"
      "ds_read_b64_tr_b16 v[102:103], v33 offset:10240\n"
      "v_mfma_f32_16x16x32_bf16 v[136:139], a[144:147], v[72:75], v[136:139]\n"
      "ds_read_b128 v[60:63], v40 offset:19456\n"
      "ds_read_b64_tr_b16 v[104:105], v34 offset:8192\n"
      "v_mfma_f32_16x16x32_bf16 v[152:155], a[148:151], v[72:75], v[152:155]\n"
      "ds_read_b64_tr_b16 v[106:107], v34 offset:10240\n"
      "v_mfma_f32_16x16x32_bf16 v[168:171], a[152:155], v[72:75], v[168:171]\n"
      "buffer_load_dwordx4 v[8:11], v50, s[32:35], s93 offen offset:0 sc1\n"
      "global_load_dwordx4 a[232:235], v45, s[8:9] offset:-2048 sc1\n"
      "ds_read_b64_tr_b16 v[108:109], v35 offset:8192\n"
      "v_mfma_f32_16x16x32_bf16 v[140:143], a[144:147], v[76:79], v[140:143]\n"
      "ds_read_b64_tr_b16 v[110:111], v35 offset:10240\n"
      "v_mfma_f32_16x16x32_bf16 v[156:159], a[148:151], v[76:79], v[156:159]\n"
      "ds_read_b64_tr_b16 v[112:113], v6 offset:12288\n"
      "s_waitcnt lgkmcnt(5)\n"
      "v_add3_u32 v2, v2, v248, v249\n"
      "v_add3_u32 v2, v2, v250, v251\n"
      "v_mfma_f32_16x16x32_bf16 v[172:175], a[152:155], v[76:79], v[172:175]\n"
      "ds_read_b64_tr_b16 v[114:115], v6 offset:14336\n"
      "v_add3_u32 v2, v2, v252, v253\n"
      "v_add3_u32 v2, v2, v254, v255\n"
      "v_mfma_f32_16x16x32_bf16 v[128:131], a[156:159], v[80:83], v[128:131]\n"
      "buffer_load_dwordx4 v[12:15], v50, s[32:35], s93 offen offset:1024 sc1\n"
      "global_load_dwordx4 a[236:239], v45, s[8:9] offset:-1024 sc1\n"
      "v_add3_u32 v2, v2, v36, v37\n"
      "v_add3_u32 v2, v2, v38, v39\n"
      "v_mfma_f32_16x16x32_bf16 v[144:147], a[160:163], v[80:83], v[144:147]\n"
      "ds_read_b64_tr_b16 v[116:117], v33 offset:12288\n"
      "v_add3_u32 v2, v2, v60, v61\n"
      "v_mfma_f32_16x16x32_bf16 v[160:163], a[164:167], v[80:83], v[160:163]\n"
      "ds_read_b64_tr_b16 v[118:119], v33 offset:14336\n"
      "v_add3_u32 v2, v2, v62, v63\n"
      "v_mfma_f32_16x16x32_bf16 v[132:135], a[156:159], v[84:87], v[132:135]\n"
      "ds_read_b64_tr_b16 v[120:121], v34 offset:12288\n"
      "v_mfma_f32_16x16x32_bf16 v[148:151], a[160:163], v[84:87], v[148:151]\n"
      "buffer_load_dwordx4 v[16:19], v50, s[32:35], s93 offen offset:2048 sc1\n"
      "ds_read_b64_tr_b16 v[122:123], v34 offset:14336\n"
      "v_add_u32_dpp v2, v2, v2 quad_perm:[1,0,3,2] row_mask:0xf bank_mask:0xf\n"
      "v_mfma_f32_16x16x32_bf16 v[164:167], a[164:167], v[84:87], v[164:167]\n"
      "ds_read_b64_tr_b16 v[124:125], v35 offset:12288\n"
      "v_mfma_f32_16x16x32_bf16 v[136:139], a[156:159], v[88:91], v[136:139]\n"
      "ds_read_b64_tr_b16 v[126:127], v35 offset:14336\n"
      "v_add_u32_dpp v2, v2, v2 quad_perm:[2,3,0,1] row_mask:0xf bank_mask:0xf\n"
      "v_mfma_f32_16x16x32_bf16 v[152:155], a[160:163], v[88:91], v[152:155]\n"
      "v_mfma_f32_16x16x32_bf16 v[168:171], a[164:167], v[88:91], v[168:171]\n"
      "buffer_load_dwordx4 v[20:23], v50, s[32:35], s93 offen offset:3072 sc1\n"
      "v_add_u32_dpp v2, v2, v2 row_half_mirror row_mask:0xf bank_mask:0xf\n"
      "v_cmp_ne_u32 vcc, s71, v2\n"
      "v_mfma_f32_16x16x32_bf16 v[140:143], a[156:159], v[92:95], v[140:143]\n"
      "v_mfma_f32_16x16x32_bf16 v[156:159], a[160:163], v[92:95], v[156:159]\n"
      "s_cbranch_vccnz L_sx5_%=\n"
      "L_bx5_%=:\n"
      "v_mfma_f32_16x16x32_bf16 v[172:175], a[164:167], v[92:95], v[172:175]\n"
      "s_waitcnt vmcnt(25)\n"
      "s_waitcnt lgkmcnt(0)\n"
      "s_barrier\n"
      "v_mfma_f32_16x16x32_bf16 v[128:131], a[168:171], v[96:99], v[128:131]\n"
      "ds_read_b64_tr_b16 v[64:65], v6 offset:16384\n"
      "v_mfma_f32_16x16x32_bf16 v[144:147], a[172:175], v[96:99], v[144:147]\n"
      "ds_read_b64_tr_b16 v[66:67], v6 offset:18432\n"
      "v_mfma_f32_16x16x32_bf16 v[160:163], a[176:179], v[96:99], v[160:163]\n"
      "ds_read_b64_tr_b16 v[68:69], v33 offset:16384\n"
      "s_add_u32 m0, s73, 49152\n"
      "v_add_u32 v43, 114688, v40\n"
      "v_mfma_f32_16x16x32_bf16 v[132:135], a[168:171], v[100:103], v[132:135]\n"
      "ds_read_b64_tr_b16 v[70:71], v33 offset:18432\n"
      "v_mfma_f32_16x16x32_bf16 v[148:151], a[172:175], v[100:103], v[148:151]\n"
      "global_load_lds_dwordx4 v43, s[4:5] offset:0 sc1\n"
      "v_mfma_f32_16x16x32_bf16 v[164:167], a[176:179], v[100:103], v[164:167]\n"
      "ds_read_b64_tr_b16 v[72:73], v34 offset:16384\n"
      "v_mfma_f32_16x16x32_bf16 v[136:139], a[168:171], v[104:107], v[136:139]\n"
      "ds_read_b64_tr_b16 v[74:75], v34 offset:18432\n"
      "v_mfma_f32_16x16x32_bf16 v[152:155], a[172:175], v[104:107], v[152:155]\n"
      "ds_read_b64_tr_b16 v[76:77], v35 offset:16384\n"
      "v_mfma_f32_16x16x32_bf16 v[168:171], a[176:179], v[104:107], v[168:171]\n"
      "global_load_lds_dwordx4 v43, s[4:5] offset:1024 sc1\n"
      "v_mfma_f32_16x16x32_bf16 v[140:143], a[168:171], v[108:111], v[140:143]\n"
      "ds_read_b64_tr_b16 v[78:79], v35 offset:18432\n"
      "v_mfma_f32_16x16x32_bf16 v[156:159], a[172:175], v[108:111], v[156:159]\n"
      "ds_read_b64_tr_b16 v[80:81], v6 offset:20480\n"
      "v_mfma_f32_16x16x32_bf16 v[172:175], a[176:179], v[108:111], v[172:175]\n"
      "ds_read_b64_tr_b16 v[82:83], v6 offset:22528\n"
      "v_mfma_f32_16x16x32_bf16 v[128:131], a[180:183], v[112:115], v[128:131]\n"
      "global_load_lds_dwordx4 v43, s[4:5] offset:2048 sc1\n"
      "s_waitcnt vmcnt(11)\n"
      "v_mfma_f32_16x16x32_bf16 v[144:147], a[184:187], v[112:115], v[144:147]\n"
      "ds_read_b64_tr_b16 v[84:85], v33 offset:20480\n"
      "v_add_f32 v228, v228, v232\n"
      "v_mfma_f32_16x16x32_bf16 v[160:163], a[188:191], v[112:115], v[160:163]\n"
      "ds_read_b64_tr_b16 v[86:87], v33 offset:22528\n"
      "v_add_f32 v229, v229, v233\n"
      "v_mfma_f32_16x16x32_bf16 v[132:135], a[180:183], v[116:119], v[132:135]\n"
      "ds_read_b64_tr_b16 v[88:89], v34 offset:20480\n"
      "v_add_f32 v230, v230, v234\n"
      "v_mfma_f32_16x16x32_bf16 v[148:151], a[184:187], v[116:119], v[148:151]\n"
      "global_load_lds_dwordx4 v43, s[4:5] offset:3072 sc1\n"
      "ds_read_b64_tr_b16 v[90:91], v34 offset:22528\n"
      "v_add_f32 v231, v231, v235\n"
      "v_mfma_f32_16x16x32_bf16 v[164:167], a[188:191], v[116:119], v[164:167]\n"
      "ds_read_b64_tr_b16 v[92:93], v35 offset:20480\n"
      "v_add_f32 v236, v236, v240\n"
      "v_mfma_f32_16x16x32_bf16 v[136:139], a[180:183], v[120:123], v[136:139]\n"
      "ds_read_b64_tr_b16 v[94:95], v35 offset:22528\n"
      "s_add_u32 m0, s73, 0\n"
      "v_add_u32 v43, 131072, v40\n"
      "v_mfma_f32_16x16x32_bf16 v[152:155], a[184:187], v[120:123], v[152:155]\n"
      "v_add_f32 v237, v237, v241\n"
      "v_add_f32 v238, v238, v242\n"
      "v_mfma_f32_16x16x32_bf16 v[168:171], a[188:191], v[120:123], v[168:171]\n"
      "global_load_dwordx4 a[240:243], v44, s[6:7] offset:-4096 sc1\n"
      "v_add_f32 v239, v239, v243\n"
      "v_mfma_f32_16x16x32_bf16 v[140:143], a[180:183], v[124:127], v[140:143]\n"
      "v_add_f32 v224, v224, v228\n"
      "v_add_f32 v225, v225, v229\n"
      "v_mfma_f32_16x16x32_bf16 v[156:159], a[184:187], v[124:127], v[156:159]\n"
      "v_add_f32 v226, v226, v230\n"
      "v_add_f32 v227, v227, v231\n"
      "v_mfma_f32_16x16x32_bf16 v[172:175], a[188:191], v[124:127], v[172:175]\n"
      "s_waitcnt lgkmcnt(0)\n"
      "v_mfma_f32_16x16x32_bf16 v[128:131], a[192:195], v[64:67], v[128:131]\n"
      "global_load_dwordx4 a[244:247], v44, s[6:7] offset:-3072 sc1\n"
      "v_add_f32 v224, v224, v236\n"
      "v_mfma_f32_16x16x32_bf16 v[144:147], a[196:199], v[64:67], v[144:147]\n"
      "ds_read_b64_tr_b16 v[96:97], v6 offset:24576\n"
      "v_add_f32 v225, v225, v237\n"
      "v_mfma_f32_16x16x32_bf16 v[160:163], a[200:203], v[64:67], v[160:163]\n"
      "ds_read_b64_tr_b16 v[98:99], v6 offset:26624\n"
      "s_waitcnt vmcnt(13)\n"
      "v_mfma_f32_16x16x32_bf16 v[132:135], a[192:195], v[68:71], v[132:135]\n"
      "ds_read_b128 v[248:251], v40 offset:32768\n"
      "v_add_f32 v226, v226, v238\n"
      "v_mfma_f32_16x16x32_bf16 v[148:151], a[196:199], v[68:71], v[148:151]\n"
      "global_load_dwordx4 a[248:251], v44, s[6:7] offset:-2048 sc1\n"
      "ds_read_b128 v[252:255], v40 offset:33792\n"
      "ds_read_b64_tr_b16 v[100:101], v33 offset:24576\n"
      "v_add_f32 v227, v227, v239\n"
      "v_mfma_f32_16x16x32_bf16 v[164:167], a[200:203], v[68:71], v[164:167]\n"
      "ds_read_b128 v[36:39], v40 offset:34816\n"
      "ds_read_b64_tr_b16 v[102:103], v33 offset:26624\n"
      "v_mov_b32 v228, v224\n"
      "v_mfma_f32_16x16x32_bf16 v[136:139], a[192:195], v[72:75], v[136:139]\n"
      "ds_read_b128 v[60:63], v40 offset:35840\n"
      "ds_read_b64_tr_b16 v[104:105], v34 offset:24576\n"
      "v_mov_b32 v229, v225\n"
      "v_mfma_f32_16x16x32_bf16 v[152:155], a[196:199], v[72:75], v[152:155]\n"
      "ds_read_b64_tr_b16 v[106:107], v34 offset:26624\n"
      "v_mov_b32 v230, v226\n"
      "v_mfma_f32_16x16x32_bf16 v[168:171], a[200:203], v[72:75], v[168:171]\n"
      "global_load_lds_dwordx4 v43, s[4:5] offset:0 sc1\n"
      "ds_read_b64_tr_b16 v[108:109], v35 offset:24576\n"
      "v_mov_b32 v231, v227\n"
      "v_mfma_f32_16x16x32_bf16 v[140:143], a[192:195], v[76:79], v[140:143]\n"
      "ds_read_b64_tr_b16 v[110:111], v35 offset:26624\n"
      "v_lshl_add_u32 v59, v59, 3, s55\n"
      "v_mfma_f32_16x16x32_bf16 v[156:159], a[196:199], v[76:79], v[156:159]\n"
      "ds_read_b64_tr_b16 v[112:113], v6 offset:28672\n"
      "s_waitcnt lgkmcnt(5)\n"
      "v_add3_u32 v49, v49, v248, v249\n"
      "v_add3_u32 v49, v49, v250, v251\n"
      "v_mfma_f32_16x16x32_bf16 v[172:175], a[200:203], v[76:79], v[172:175]\n"
      "ds_read_b64_tr_b16 v[114:115], v6 offset:30720\n"
      "v_add3_u32 v49, v49, v252, v253\n"
      "v_add3_u32 v49, v49, v254, v255\n"
      "v_mfma_f32_16x16x32_bf16 v[128:131], a[204:207], v[80:83], v[128:131]\n"
      "global_load_dwordx4 a[252:255], v44, s[6:7] offset:-1024 sc1\n"
      "v_add3_u32 v49, v49, v36, v37\n"
      "v_add3_u32 v49, v49, v38, v39\n"
      "v_mfma_f32_16x16x32_bf16 v[144:147], a[208:211], v[80:83], v[144:147]\n"
      "ds_read_b64_tr_b16 v[116:117], v33 offset:28672\n"
      "v_add3_u32 v49, v49, v60, v61\n"
      "v_mfma_f32_16x16x32_bf16 v[160:163], a[212:215], v[80:83], v[160:163]\n"
      "ds_read_b64_tr_b16 v[118:119], v33 offset:30720\n"
      "v_add3_u32 v49, v49, v62, v63\n"
      "v_permlane32_swap_b32 v224, v228\n"
      "v_mfma_f32_16x16x32_bf16 v[132:135], a[204:207], v[84:87], v[132:135]\n"
      "ds_read_b64_tr_b16 v[120:121], v34 offset:28672\n"
      "v_permlane32_swap_b32 v225, v229\n"
      "v_mfma_f32_16x16x32_bf16 v[148:151], a[208:211], v[84:87], v[148:151]\n"
      "global_load_lds_dwordx4 v43, s[4:5] offset:1024 sc1\n"
      "ds_read_b64_tr_b16 v[122:123], v34 offset:30720\n"
      "v_add_u32_dpp v49, v49, v49 quad_perm:[1,0,3,2] row_mask:0xf bank_mask:0xf\n"
      "v_permlane32_swap_b32 v226, v230\n"
      "v_mfma_f32_16x16x32_bf16 v[164:167], a[212:215], v[84:87], v[164:167]\n"
      "ds_read_b64_tr_b16 v[124:125], v35 offset:28672\n"
      "v_permlane32_swap_b32 v227, v231\n"
      "v_mfma_f32_16x16x32_bf16 v[136:139], a[204:207], v[88:91], v[136:139]\n"
      "ds_read_b64_tr_b16 v[126:127], v35 offset:30720\n"
      "v_add_u32_dpp v49, v49, v49 quad_perm:[2,3,0,1] row_mask:0xf bank_mask:0xf\n"
      "v_add_f32 v224, v224, v228\n"
      "v_mfma_f32_16x16x32_bf16 v[152:155], a[208:211], v[88:91], v[152:155]\n"
      "v_add_f32 v225, v225, v229\n"
      "v_add_f32 v226, v226, v230\n"
      "v_mfma_f32_16x16x32_bf16 v[168:171], a[212:215], v[88:91], v[168:171]\n"
      "global_load_dwordx4 a[0:3], v44, s[6:7] offset:0 sc1\n"
      "v_add_u32_dpp v49, v49, v49 row_half_mirror row_mask:0xf bank_mask:0xf\n"
      "v_cmp_ne_u32 vcc, s71, v49\n"
      "v_mfma_f32_16x16x32_bf16 v[140:143], a[204:207], v[92:95], v[140:143]\n"
      "v_add_f32 v227, v227, v231\n"
      "v_mfma_f32_16x16x32_bf16 v[156:159], a[208:211], v[92:95], v[156:159]\n"
      "ds_write_b128 v51, v[224:227]\n"
      "s_cbranch_vccnz L_sx6_%=\n"
      "L_bx6_%=:\n"
      "v_mfma_f32_16x16x32_bf16 v[172:175], a[212:215], v[92:95], v[172:175]\n"
      "s_waitcnt vmcnt(13)\n"
      "s_waitcnt lgkmcnt(1)\n"
      "s_barrier\n"
      "v_mfma_f32_16x16x32_bf16 v[128:131], a[216:219], v[96:99], v[128:131]\n"
      "global_load_lds_dwordx4 v43, s[4:5] offset:2048 sc1\n"
      "v_mfma_f32_16x16x32_bf16 v[144:147], a[220:223], v[96:99], v[144:147]\n"
      "ds_read_b64_tr_b16 v[64:65], v6 offset:32768\n"
      "v_mfma_f32_16x16x32_bf16 v[160:163], a[224:227], v[96:99], v[160:163]\n"
      "ds_read_b64_tr_b16 v[66:67], v6 offset:34816\n"
      "s_waitcnt vmcnt(12)\n"
      "v_add3_u32 v10, v10, v14, v18\n"
      "v_mfma_f32_16x16x32_bf16 v[132:135], a[216:219], v[100:103], v[132:135]\n"
      "ds_read_b64_tr_b16 v[68:69], v34 offset:32768\n"
      "v_add_u32 v10, v10, v22\n"
      "v_cmp_ne_u32 vcc, s71, v10\n"
      "v_mfma_f32_16x16x32_bf16 v[148:151], a[220:223], v[100:103], v[148:151]\n"
      "global_load_dwordx4 a[4:7], v44, s[6:7] offset:1024 sc1\n"
      "v_mfma_f32_16x16x32_bf16 v[164:167], a[224:227], v[100:103], v[164:167]\n"
      "global_load_lds_dwordx4 v43, s[4:5] offset:3072 sc1\n"
      "ds_read_b64_tr_b16 v[70:71], v34 offset:34816\n"
      "s_cbranch_vccnz L_sg_%=\n"
      "L_bg_%=:\n"
      "v_add_f32 v8, v8, v12\n"
      "v_add_f32 v9, v9, v13\n"
      "v_mfma_f32_16x16x32_bf16 v[136:139], a[216:219], v[104:107], v[136:139]\n"
      "ds_read_b64_tr_b16 v[72:73], v33 offset:32768\n"
      "v_add_f32 v16, v16, v20\n"
      "v_add_f32 v17, v17, v21\n"
      "v_mfma_f32_16x16x32_bf16 v[152:155], a[220:223], v[104:107], v[152:155]\n"
      "ds_read_b64_tr_b16 v[74:75], v33 offset:34816\n"
      "v_add_f32 v8, v8, v16\n"
      "v_add_u32 v245, v245, v247\n"
      "v_mfma_f32_16x16x32_bf16 v[168:171], a[224:227], v[104:107], v[168:171]\n"
      "global_load_dwordx4 a[8:11], v44, s[6:7] offset:2048 sc1\n"
      "ds_read_b64_tr_b16 v[76:77], v35 offset:32768\n"
      "v_add_f32 v9, v9, v17\n"
      "v_mov_b32 v10, v8\n"
      "v_cmp_ne_u32 vcc, s54, v245\n"
      "v_mfma_f32_16x16x32_bf16 v[140:143], a[216:219], v[108:111], v[140:143]\n"
      "ds_read_b64_tr_b16 v[78:79], v35 offset:34816\n"
      "v_mov_b32 v11, v9\n"
      "s_nop 0\n"
      "v_mfma_f32_16x16x32_bf16 v[156:159], a[220:223], v[108:111], v[156:159]\n"
      "ds_read_b64_tr_b16 v[80:81], v6 offset:36864\n"
      "v_permlane32_swap_b32 v8, v10\n"
      "v_permlane32_swap_b32 v9, v11\n"
      "s_cbranch_vccnz L_sgb_%=\n"
      "L_bgb_%=:\n"
      "v_add_f32 v8, v8, v10\n"
      "v_mfma_f32_16x16x32_bf16 v[172:175], a[224:227], v[108:111], v[172:175]\n"
      "ds_read_b64_tr_b16 v[82:83], v6 offset:38912\n"
      "v_add_f32_dpp v244, v244, v244 quad_perm:[1,0,3,2] row_mask:0xf bank_mask:0xf\n"
      "v_add_f32_dpp v246, v246, v246 quad_perm:[1,0,3,2] row_mask:0xf bank_mask:0xf\n"
      "v_mfma_f32_16x16x32_bf16 v[128:131], a[228:231], v[112:115], v[128:131]\n"
      "global_load_dwordx4 a[12:15], v44, s[6:7] offset:3072 sc1\n"
      "v_add_f32 v9, v9, v11\n"
      "v_mul_f32 v8, 0x3acccccd, v8\n"
      "v_add_f32_dpp v244, v244, v244 quad_perm:[2,3,0,1] row_mask:0xf bank_mask:0xf\n"
      "v_add_f32_dpp v246, v246, v246 quad_perm:[2,3,0,1] row_mask:0xf bank_mask:0xf\n"
      "v_mfma_f32_16x16x32_bf16 v[144:147], a[232:235], v[112:115], v[144:147]\n"
      "ds_read_b64_tr_b16 v[84:85], v34 offset:36864\n"
      "v_mul_f32 v9, 0x3acccccd, v9\n"
      "v_fma_f32 v9, -v8, v8, v9\n"
      "s_add_u32 m0, s40, 0\n"
      "v_add_u32 v43, 40960, v7\n"
      "v_mfma_f32_16x16x32_bf16 v[160:163], a[236:239], v[112:115], v[160:163]\n"
      "global_load_lds_dwordx4 v43, s[6:7] offset:0 sc1\n"
      "global_load_lds_dwordx4 v43, s[6:7] offset:1024 sc1\n"
      "global_load_lds_dwordx4 v43, s[6:7] offset:2048 sc1\n"
      "global_load_lds_dwordx4 v43, s[6:7] offset:3072 sc1\n"
      "ds_read_b64_tr_b16 v[86:87], v34 offset:38912\n"
      "v_max_f32 v9, 0, v9\n"
      "s_add_u32 m0, s73, 16384\n"
      "v_add_u32 v43, 147456, v40\n"
      "v_cvt_pk_bf16_f32 v244, 0, v244\n"
      "v_cvt_pk_bf16_f32 v245, 0, v246\n"
      "v_mfma_f32_16x16x32_bf16 v[132:135], a[228:231], v[116:119], v[132:135]\n"
      "global_load_lds_dwordx4 v43, s[4:5] offset:0 sc1\n"
      "ds_read_b64_tr_b16 v[88:89], v33 offset:36864\n"
      "ds_write_b64 v59, v[244:245]\n"
      "v_cvt_pk_bf16_f32 v9, 0, v9\n"
      "v_add_f32 v9, 0x3727c5ac, v9\n"
      "v_add_u32 v44, 12288, v7\n"
      "v_mfma_f32_16x16x32_bf16 v[148:151], a[232:235], v[116:119], v[148:151]\n"
      "global_load_dwordx4 a[16:19], v44, s[6:7] offset:-4096 sc1\n"
      "ds_read_b64_tr_b16 v[90:91], v33 offset:38912\n"
      "ds_read_b128 v[224:227], v52 offset:0\n"
      "v_cvt_pk_bf16_f32 v9, 0, v9\n"
      "v_mfma_f32_16x16x32_bf16 v[164:167], a[236:239], v[116:119], v[164:167]\n"
      "ds_read_b64_tr_b16 v[92:93], v35 offset:36864\n"
      "ds_read_b128 v[228:231], v52 offset:128\n"
      "v_sqrt_f32 v9, v9\n"
      "v_sub_f32 v8, 0, v8\n"
      "v_cvt_pk_bf16_f32 v9, 0, v9\n"
      "v_mfma_f32_16x16x32_bf16 v[136:139], a[228:231], v[120:123], v[136:139]\n"
      "ds_read_b128 v[232:235], v52 offset:256\n"
      "v_rcp_f32 v9, v9\n"
      "v_cvt_pk_bf16_f32 v8, 0, v8\n"
      "v_mfma_f32_16x16x32_bf16 v[152:155], a[232:235], v[120:123], v[152:155]\n"
      "global_load_lds_dwordx4 v43, s[4:5] offset:1024 sc1\n"
      "ds_write_b64 v32, v[8:9]\n"
      "ds_read_b64_tr_b16 v[94:95], v35 offset:38912\n"
      "v_mfma_f32_16x16x32_bf16 v[168:171], a[236:239], v[120:123], v[168:171]\n"
      "global_load_dwordx4 a[20:23], v44, s[6:7] offset:-3072 sc1\n"
      "ds_read_b128 v[236:239], v52 offset:64\n"
      "v_mfma_f32_16x16x32_bf16 v[140:143], a[228:231], v[124:127], v[140:143]\n"
      "ds_read_b128 v[240:243], v52 offset:192\n"
      "v_mfma_f32_16x16x32_bf16 v[156:159], a[232:235], v[124:127], v[156:159]\n"
      "ds_read_b128 v[244:247], v52 offset:320\n"
      "v_mfma_f32_16x16x32_bf16 v[172:175], a[236:239], v[124:127], v[172:175]\n"
      "global_load_lds_dwordx4 v43, s[4:5] offset:2048 sc1\n"
      "s_waitcnt lgkmcnt(3)\n"
      "s_barrier\n"
      "v_mfma_f32_16x16x32_bf16 v[176:179], a[48:51], v[64:67], v[176:179]\n"
      "global_load_dwordx4 a[24:27], v44, s[6:7] offset:-2048 sc1\n"
      "ds_read_b64 v[16:17], v41 offset:0\n"
      "v_mfma_f32_16x16x32_bf16 v[184:187], a[52:55], v[64:67], v[184:187]\n"
      "ds_read_b64 v[18:19], v41 offset:128\n"
      "v_mfma_f32_16x16x32_bf16 v[192:195], a[56:59], v[64:67], v[192:195]\n"
      "ds_read_b64 v[20:21], v41 offset:256\n"
      "ds_read_b64_tr_b16 v[96:97], v6 offset:40960\n"
      "s_waitcnt vmcnt(22)\n"
      "v_mfma_f32_16x16x32_bf16 v[180:183], a[48:51], v[68:71], v[180:183]\n"
      "global_load_lds_dwordx4 v43, s[4:5] offset:3072 sc1\n"
      "ds_read_b128 v[248:251], v40 offset:49152\n"
      "ds_read_b64 v[22:23], v41 offset:384\n"
      "ds_read_b64_tr_b16 v[98:99], v6 offset:43008\n"
      "v_mfma_f32_16x16x32_bf16 v[188:191], a[52:55], v[68:71], v[188:191]\n"
      "global_load_dwordx4 a[28:31], v44, s[6:7] offset:-1024 sc1\n"
      "ds_read_b128 v[252:255], v40 offset:50176\n"
      "ds_read_b64_tr_b16 v[100:101], v34 offset:40960\n"
      "v_mfma_f32_16x16x32_bf16 v[196:199], a[56:59], v[68:71], v[196:199]\n"
      "ds_read_b128 v[36:39], v40 offset:51200\n"
      "ds_read_b64_tr_b16 v[102:103], v34 offset:43008\n"
      "v_mfma_f32_16x16x32_bf16 v[200:203], a[48:51], v[72:75], v[200:203]\n"
      "ds_read_b128 v[60:63], v40 offset:52224\n"
      "ds_read_b64_tr_b16 v[104:105], v33 offset:40960\n"
      "s_waitcnt lgkmcnt(7)\n"
      "v_mfma_f32_16x16x32_bf16 v[208:211], a[52:55], v[72:75], v[208:211]\n"
      "ds_read_b64_tr_b16 v[106:107], v33 offset:43008\n"
      "v_fmac_f32 v128, v16, v224\n"
      "v_fmac_f32 v129, v16, v225\n"
      "v_mfma_f32_16x16x32_bf16 v[216:219], a[56:59], v[72:75], v[216:219]\n"
      "global_load_dwordx4 a[32:35], v44, s[6:7] offset:0 sc1\n"
      "ds_read_b64_tr_b16 v[108:109], v35 offset:40960\n"
      "v_fmac_f32 v130, v16, v226\n"
      "v_fmac_f32 v131, v16, v227\n"
      "v_mfma_f32_16x16x32_bf16 v[204:207], a[48:51], v[76:79], v[204:207]\n"
      "ds_read_b64_tr_b16 v[110:111], v35 offset:43008\n"
      "v_fma_f32 v128, v128, v17, v236\n"
      "v_fma_f32 v129, v129, v17, v237\n"
      "v_fma_f32 v130, v130, v17, v238\n"
      "v_mfma_f32_16x16x32_bf16 v[212:215], a[52:55], v[76:79], v[212:215]\n"
      "ds_read_b64_tr_b16 v[112:113], v6 offset:45056\n"
      "s_waitcnt lgkmcnt(5)\n"
      "v_add3_u32 v55, v55, v248, v249\n"
      "v_add3_u32 v55, v55, v250, v251\n"
      "v_fma_f32 v131, v131, v17, v239\n"
      "v_mul_f32 v128, v128, v130\n"
      "v_mfma_f32_16x16x32_bf16 v[220:223], a[56:59], v[76:79], v[220:223]\n"
      "ds_read_b64_tr_b16 v[114:115], v6 offset:47104\n"
      "v_add3_u32 v55, v55, v252, v253\n"
      "v_add3_u32 v55, v55, v254, v255\n"
      "v_mul_f32 v129, v129, v131\n"
      "v_cvt_pk_bf16_f32 v128, v128, v129\n"
      "v_mfma_f32_16x16x32_bf16 v[176:179], a[60:63], v[80:83], v[176:179]\n"
      "global_load_dwordx4 a[36:39], v44, s[6:7] offset:1024 sc1\n"
      "ds_write_b32 v46, v128 offset:0\n"
      "v_add3_u32 v55, v55, v36, v37\n"
      "v_add3_u32 v55, v55, v38, v39\n"
      "v_fmac_f32 v144, v16, v228\n"
      "v_fmac_f32 v145, v16, v229\n"
      "v_mfma_f32_16x16x32_bf16 v[184:187], a[64:67], v[80:83], v[184:187]\n"
      "ds_read_b64_tr_b16 v[116:117], v34 offset:45056\n"
      "v_add3_u32 v55, v55, v60, v61\n"
      "v_fmac_f32 v146, v16, v230\n"
      "v_fmac_f32 v147, v16, v231\n"
      "v_mfma_f32_16x16x32_bf16 v[192:195], a[68:71], v[80:83], v[192:195]\n"
      "ds_read_b64_tr_b16 v[118:119], v34 offset:47104\n"
      "v_add3_u32 v55, v55, v62, v63\n"
      "v_fma_f32 v144, v144, v17, v240\n"
      "v_fma_f32 v145, v145, v17, v241\n"
      "v_mfma_f32_16x16x32_bf16 v[180:183], a[60:63], v[84:87], v[180:183]\n"
      "ds_read_b64_tr_b16 v[120:121], v33 offset:45056\n"
      "v_fma_f32 v146, v146, v17, v242\n"
      "v_fma_f32 v147, v147, v17, v243\n"
      "v_mul_f32 v144, v144, v146\n"
      "v_mfma_f32_16x16x32_bf16 v[188:191], a[64:67], v[84:87], v[188:191]\n"
      "global_load_dwordx4 a[40:43], v44, s[6:7] offset:2048 sc1\n"
      "ds_read_b64_tr_b16 v[122:123], v33 offset:47104\n"
      "v_add_u32_dpp v55, v55, v55 quad_perm:[1,0,3,2] row_mask:0xf bank_mask:0xf\n"
      "v_mul_f32 v145, v145, v147\n"
      "v_cvt_pk_bf16_f32 v144, v144, v145\n"
      "v_mfma_f32_16x16x32_bf16 v[196:199], a[68:71], v[84:87], v[196:199]\n"
      "ds_write_b32 v46, v144 offset:4096\n"
      "ds_read_b64_tr_b16 v[124:125], v35 offset:45056\n"
      "v_fmac_f32 v160, v16, v232\n"
      "v_mfma_f32_16x16x32_bf16 v[200:203], a[60:63], v[88:91], v[200:203]\n"
      "ds_read_b64_tr_b16 v[126:127], v35 offset:47104\n"
      "v_add_u32_dpp v55, v55, v55 quad_perm:[2,3,0,1] row_mask:0xf bank_mask:0xf\n"
      "v_fmac_f32 v161, v16, v233\n"
      "v_fmac_f32 v162, v16, v234\n"
      "v_fmac_f32 v163, v16, v235\n"
      "v_mfma_f32_16x16x32_bf16 v[208:211], a[64:67], v[88:91], v[208:211]\n"
      "v_fma_f32 v160, v160, v17, v244\n"
      "v_fma_f32 v161, v161, v17, v245\n"
      "v_mfma_f32_16x16x32_bf16 v[216:219], a[68:71], v[88:91], v[216:219]\n"
      "global_load_dwordx4 a[44:47], v44, s[6:7] offset:3072 sc1\n"
      "v_add_u32_dpp v55, v55, v55 row_half_mirror row_mask:0xf bank_mask:0xf\n"
      "v_cmp_ne_u32 vcc, s71, v55\n"
      "v_fma_f32 v162, v162, v17, v246\n"
      "v_fma_f32 v163, v163, v17, v247\n"
      "v_mul_f32 v160, v160, v162\n"
      "v_mfma_f32_16x16x32_bf16 v[204:207], a[60:63], v[92:95], v[204:207]\n"
      "v_mul_f32 v161, v161, v163\n"
      "v_cvt_pk_bf16_f32 v160, v160, v161\n"
      "v_mfma_f32_16x16x32_bf16 v[212:215], a[64:67], v[92:95], v[212:215]\n"
      "ds_write_b32 v46, v160 offset:8192\n"
      "s_cbranch_vccnz L_sx7_%=\n"
      "L_bx7_%=:\n"
      "v_fmac_f32 v132, v18, v224\n"
      "v_mfma_f32_16x16x32_bf16 v[220:223], a[68:71], v[92:95], v[220:223]\n"
      "v_fmac_f32 v133, v18, v225\n"
      "v_fmac_f32 v134, v18, v226\n"
      "v_fmac_f32 v135, v18, v227\n"
      "v_add_u32 v44, 20480, v7\n"
      "s_waitcnt lgkmcnt(1)\n"
      "s_barrier\n"
      "v_mfma_f32_16x16x32_bf16 v[176:179], a[72:75], v[96:99], v[176:179]\n"
      "global_load_dwordx4 a[48:51], v44, s[6:7] offset:-4096 sc1\n"
      "v_fma_f32 v132, v132, v19, v236\n"
      "v_fma_f32 v133, v133, v19, v237\n"
      "v_mfma_f32_16x16x32_bf16 v[184:187], a[76:79], v[96:99], v[184:187]\n"
      "ds_read_b64_tr_b16 v[64:65], v6 offset:49152\n"
      "v_fma_f32 v134, v134, v19, v238\n"
      "v_fma_f32 v135, v135, v19, v239\n"
      "v_mfma_f32_16x16x32_bf16 v[192:195], a[80:83], v[96:99], v[192:195]\n"
      "ds_read_b64_tr_b16 v[66:67], v6 offset:51200\n"
      "v_mul_f32 v132, v132, v134\n"
      "v_mul_f32 v133, v133, v135\n"
      "v_cvt_pk_bf16_f32 v132, v132, v133\n"
      "v_mfma_f32_16x16x32_bf16 v[180:183], a[72:75], v[100:103], v[180:183]\n"
      "ds_write_b32 v46, v132 offset:1024\n"
      "ds_read_b64_tr_b16 v[68:69], v34 offset:49152\n"
      "v_fmac_f32 v148, v18, v228\n"
      "v_mfma_f32_16x16x32_bf16 v[188:191], a[76:79], v[100:103], v[188:191]\n"
      "global_load_dwordx4 a[52:55], v44, s[6:7] offset:-3072 sc1\n"
      "s_waitcnt vmcnt(20)\n"
      "v_fmac_f32 v149, v18, v229\n"
      "v_fmac_f32 v150, v18, v230\n"
      "v_mfma_f32_16x16x32_bf16 v[196:199], a[80:83], v[100:103], v[196:199]\n"
      "ds_read_b128 v[248:251], v40 offset:0\n"
      "ds_read_b64_tr_b16 v[70:71], v34 offset:51200\n"
      "v_fmac_f32 v151, v18, v231\n"
      "v_fma_f32 v148, v148, v19, v240\n"
      "v_fma_f32 v149, v149, v19, v241\n"
      "v_mfma_f32_16x16x32_bf16 v[200:203], a[72:75], v[104:107], v[200:203]\n"
      "ds_read_b128 v[252:255], v40 offset:1024\n"
      "ds_read_b64_tr_b16 v[72:73], v33 offset:49152\n"
      "v_fma_f32 v150, v150, v19, v242\n"
      "v_fma_f32 v151, v151, v19, v243\n"
      "v_mfma_f32_16x16x32_bf16 v[208:211], a[76:79], v[104:107], v[208:211]\n"
      "ds_read_b128 v[36:39], v40 offset:2048\n"
      "ds_read_b64_tr_b16 v[74:75], v33 offset:51200\n"
      "v_mul_f32 v148, v148, v150\n"
      "v_mul_f32 v149, v149, v151\n"
      "v_cvt_pk_bf16_f32 v148, v148, v149\n"
      "v_mfma_f32_16x16x32_bf16 v[216:219], a[80:83], v[104:107], v[216:219]\n"
      "global_load_dwordx4 a[56:59], v44, s[6:7] offset:-2048 sc1\n"
      "ds_read_b128 v[60:63], v40 offset:3072\n"
      "ds_write_b32 v46, v148 offset:5120\n"
      "ds_read_b64_tr_b16 v[76:77], v35 offset:49152\n"
      "v_fmac_f32 v164, v18, v232\n"
      "v_mfma_f32_16x16x32_bf16 v[204:207], a[72:75], v[108:111], v[204:207]\n"
      "ds_read_b64_tr_b16 v[78:79], v35 offset:51200\n"
      "v_fmac_f32 v165, v18, v233\n"
      "v_fmac_f32 v166, v18, v234\n"
      "v_mfma_f32_16x16x32_bf16 v[212:215], a[76:79], v[108:111], v[212:215]\n"
      "ds_read_b64_tr_b16 v[80:81], v6 offset:53248\n"
      "v_fmac_f32 v167, v18, v235\n"
      "v_fma_f32 v164, v164, v19, v244\n"
      "v_fma_f32 v165, v165, v19, v245\n"
      "v_mfma_f32_16x16x32_bf16 v[220:223], a[80:83], v[108:111], v[220:223]\n"
      "ds_read_b64_tr_b16 v[82:83], v6 offset:55296\n"
      "v_fma_f32 v166, v166, v19, v246\n"
      "v_fma_f32 v167, v167, v19, v247\n"
      "v_mfma_f32_16x16x32_bf16 v[176:179], a[84:87], v[112:115], v[176:179]\n"
      "global_load_dwordx4 a[60:63], v44, s[6:7] offset:-1024 sc1\n"
      "s_waitcnt lgkmcnt(5)\n"
      "v_add3_u32 v57, v57, v248, v249\n"
      "v_add3_u32 v57, v57, v250, v251\n"
      "v_mul_f32 v164, v164, v166\n"
      "v_mul_f32 v165, v165, v167\n"
      "v_cvt_pk_bf16_f32 v164, v164, v165\n"
      "v_mfma_f32_16x16x32_bf16 v[184:187], a[88:91], v[112:115], v[184:187]\n"
      "ds_write_b32 v46, v164 offset:9216\n"
      "ds_read_b64_tr_b16 v[84:85], v34 offset:53248\n"
      "v_add3_u32 v57, v57, v252, v253\n"
      "v_add3_u32 v57, v57, v254, v255\n"
      "v_fmac_f32 v136, v20, v224\n"
      "v_mfma_f32_16x16x32_bf16 v[192:195], a[92:95], v[112:115], v[192:195]\n"
      "ds_read_b64_tr_b16 v[86:87], v34 offset:55296\n"
      "v_add3_u32 v57, v57, v36, v37\n"
      "v_add3_u32 v57, v57, v38, v39\n"
      "v_fmac_f32 v137, v20, v225\n"
      "v_fmac_f32 v138, v20, v226\n"
      "v_mfma_f32_16x16x32_bf16 v[180:183], a[84:87], v[116:119], v[180:183]\n"
      "ds_read_b64_tr_b16 v[88:89], v33 offset:53248\n"
      "v_add3_u32 v57, v57, v60, v61\n"
      "v_fmac_f32 v139, v20, v227\n"
      "v_fma_f32 v136, v136, v21, v236\n"
      "v_mfma_f32_16x16x32_bf16 v[188:191], a[88:91], v[116:119], v[188:191]\n"
      "global_load_dwordx4 a[64:67], v44, s[6:7] offset:0 sc1\n"
      "ds_read_b64_tr_b16 v[90:91], v33 offset:55296\n"
      "v_add3_u32 v57, v57, v62, v63\n"
      "v_fma_f32 v137, v137, v21, v237\n"
      "v_fma_f32 v138, v138, v21, v238\n"
      "v_fma_f32 v139, v139, v21, v239\n"
      "v_mfma_f32_16x16x32_bf16 v[196:199], a[92:95], v[116:119], v[196:199]\n"
      "ds_read_b64_tr_b16 v[92:93], v35 offset:53248\n"
      "v_mul_f32 v136, v136, v138\n"
      "v_mul_f32 v137, v137, v139\n"
      "v_cvt_pk_bf16_f32 v136, v136, v137\n"
      "v_mfma_f32_16x16x32_bf16 v[200:203], a[84:87], v[120:123], v[200:203]\n"
      "ds_write_b32 v46, v136 offset:2048\n"
      "ds_read_b64_tr_b16 v[94:95], v35 offset:55296\n"
      "v_add_u32_dpp v57, v57, v57 quad_perm:[1,0,3,2] row_mask:0xf bank_mask:0xf\n"
      "v_mfma_f32_16x16x32_bf16 v[208:211], a[88:91], v[120:123], v[208:211]\n"
      "v_fmac_f32 v152, v20, v228\n"
      "v_fmac_f32 v153, v20, v229\n"
      "v_fmac_f32 v154, v20, v230\n"
      "v_mfma_f32_16x16x32_bf16 v[216:219], a[92:95], v[120:123], v[216:219]\n"
      "global_load_dwordx4 a[68:71], v44, s[6:7] offset:1024 sc1\n"
      "v_add_u32_dpp v57, v57, v57 quad_perm:[2,3,0,1] row_mask:0xf bank_mask:0xf\n"
      "v_fmac_f32 v155, v20, v231\n"
      "v_fma_f32 v152, v152, v21, v240\n"
      "v_mfma_f32_16x16x32_bf16 v[204:207], a[84:87], v[124:127], v[204:207]\n"
      "v_fma_f32 v153, v153, v21, v241\n"
      "v_fma_f32 v154, v154, v21, v242\n"
      "v_fma_f32 v155, v155, v21, v243\n"
      "v_mfma_f32_16x16x32_bf16 v[212:215], a[88:91], v[124:127], v[212:215]\n"
      "v_add_u32_dpp v57, v57, v57 row_half_mirror row_mask:0xf bank_mask:0xf\n"
      "v_cmp_ne_u32 vcc, s71, v57\n"
      "v_mul_f32 v152, v152, v154\n"
      "v_mul_f32 v153, v153, v155\n"
      "v_cvt_pk_bf16_f32 v152, v152, v153\n"
      "v_mfma_f32_16x16x32_bf16 v[220:223], a[92:95], v[124:127], v[220:223]\n"
      "ds_write_b32 v46, v152 offset:6144\n"
      "v_fmac_f32 v168, v20, v232\n"
      "s_waitcnt lgkmcnt(1)\n"
      "v_mfma_f32_16x16x32_bf16 v[176:179], a[96:99], v[64:67], v[176:179]\n"
      "global_load_dwordx4 a[72:75], v44, s[6:7] offset:2048 sc1\n"
      "s_cbranch_vccnz L_sx8_%=\n"
      "L_bx8_%=:\n"
      "v_fmac_f32 v169, v20, v233\n"
      "v_fmac_f32 v170, v20, v234\n"
      "s_add_u32 m0, s40, 4096\n"
      "v_add_u32 v43, 45056, v7\n"
      "v_mfma_f32_16x16x32_bf16 v[184:187], a[100:103], v[64:67], v[184:187]\n"
      "global_load_lds_dwordx4 v43, s[6:7] offset:0 sc1\n"
      "v_fmac_f32 v171, v20, v235\n"
      "v_fma_f32 v168, v168, v21, v244\n"
      "v_mfma_f32_16x16x32_bf16 v[192:195], a[104:107], v[64:67], v[192:195]\n"
      "ds_read_b64_tr_b16 v[96:97], v6 offset:57344\n"
      "s_waitcnt vmcnt(13)\n"
      "v_fma_f32 v169, v169, v21, v245\n"
      "v_fma_f32 v170, v170, v21, v246\n"
      "v_mfma_f32_16x16x32_bf16 v[180:183], a[96:99], v[68:71], v[180:183]\n"
      "ds_read_b128 v[248:251], v40 offset:16384\n"
      "ds_read_b64_tr_b16 v[98:99], v6 offset:59392\n"
      "v_fma_f32 v171, v171, v21, v247\n"
      "v_mul_f32 v168, v168, v170\n"
      "v_mul_f32 v169, v169, v171\n"
      "v_cvt_pk_bf16_f32 v168, v168, v169\n"
      "v_mfma_f32_16x16x32_bf16 v[188:191], a[100:103], v[68:71], v[188:191]\n"
      "global_load_dwordx4 a[76:79], v44, s[6:7] offset:3072 sc1\n"
      "ds_read_b128 v[252:255], v40 offset:17408\n"
      "ds_write_b32 v46, v168 offset:10240\n"
      "ds_read_b64_tr_b16 v[100:101], v34 offset:57344\n"
      "v_fmac_f32 v140, v22, v224\n"
      "v_mfma_f32_16x16x32_bf16 v[196:199], a[104:107], v[68:71], v[196:199]\n"
      "global_load_lds_dwordx4 v43, s[6:7] offset:1024 sc1\n"
      "ds_read_b128 v[36:39], v40 offset:18432\n"
      "ds_read_b64_tr_b16 v[102:103], v34 offset:59392\n"
      "v_fmac_f32 v141, v22, v225\n"
      "v_mfma_f32_16x16x32_bf16 v[200:203], a[96:99], v[72:75], v[200:203]\n"
      "ds_read_b128 v[60:63], v40 offset:19456\n"
      "ds_read_b64_tr_b16 v[104:105], v33 offset:57344\n"
      "v_fmac_f32 v142, v22, v226\n"
      "v_fmac_f32 v143, v22, v227\n"
      "v_fma_f32 v140, v140, v23, v236\n"
      "v_mfma_f32_16x16x32_bf16 v[208:211], a[100:103], v[72:75], v[208:211]\n"
      "ds_read_b64_tr_b16 v[106:107], v33 offset:59392\n"
      "v_fma_f32 v141, v141, v23, v237\n"
      "v_fma_f32 v142, v142, v23, v238\n"
      "v_add_u32 v44, 28672, v7\n"
      "v_mfma_f32_16x16x32_bf16 v[216:219], a[104:107], v[72:75], v[216:219]\n"
      "global_load_dwordx4 a[80:83], v44, s[6:7] offset:-4096 sc1\n"
      "ds_read_b64_tr_b16 v[108:109], v35 offset:57344\n"
      "v_fma_f32 v143, v143, v23, v239\n"
      "v_mul_f32 v140, v140, v142\n"
      "v_mul_f32 v141, v141, v143\n"
      "v_cvt_pk_bf16_f32 v140, v140, v141\n"
      "v_mfma_f32_16x16x32_bf16 v[204:207], a[96:99], v[76:79], v[204:207]\n"
      "global_load_lds_dwordx4 v43, s[6:7] offset:2048 sc1\n"
      "ds_write_b32 v46, v140 offset:3072\n"
      "ds_read_b64_tr_b16 v[110:111], v35 offset:59392\n"
      "v_fmac_f32 v156, v22, v228\n"
      "v_mfma_f32_16x16x32_bf16 v[212:215], a[100:103], v[76:79], v[212:215]\n"
      "ds_read_b64_tr_b16 v[112:113], v6 offset:61440\n"
      "s_waitcnt lgkmcnt(6)\n"
      "v_add3_u32 v3, v3, v248, v249\n"
      "v_add3_u32 v3, v3, v250, v251\n"
      "v_fmac_f32 v157, v22, v229\n"
      "v_mfma_f32_16x16x32_bf16 v[220:223], a[104:107], v[76:79], v[220:223]\n"
      "ds_read_b64_tr_b16 v[114:115], v6 offset:63488\n"
      "v_add3_u32 v3, v3, v252, v253\n"
      "v_add3_u32 v3, v3, v254, v255\n"
      "v_fmac_f32 v158, v22, v230\n"
      "v_fmac_f32 v159, v22, v231\n"
      "v_fma_f32 v156, v156, v23, v240\n"
      "v_mfma_f32_16x16x32_bf16 v[176:179], a[108:111], v[80:83], v[176:179]\n"
      "global_load_dwordx4 a[84:87], v44, s[6:7] offset:-3072 sc1\n"
      "v_add3_u32 v3, v3, v36, v37\n"
      "v_add3_u32 v3, v3, v38, v39\n"
      "v_fma_f32 v157, v157, v23, v241\n"
      "v_fma_f32 v158, v158, v23, v242\n"
      "v_mfma_f32_16x16x32_bf16 v[184:187], a[112:115], v[80:83], v[184:187]\n"
      "global_load_lds_dwordx4 v43, s[6:7] offset:3072 sc1\n"
      "ds_read_b64_tr_b16 v[116:117], v34 offset:61440\n"
      "v_add3_u32 v3, v3, v60, v61\n"
      "v_fma_f32 v159, v159, v23, v243\n"
      "v_mul_f32 v156, v156, v158\n"
      "v_mul_f32 v157, v157, v159\n"
      "v_cvt_pk_bf16_f32 v156, v156, v157\n"
      "v_mfma_f32_16x16x32_bf16 v[192:195], a[116:119], v[80:83], v[192:195]\n"
      "ds_write_b32 v46, v156 offset:7168\n"
      "ds_read_b64_tr_b16 v[118:119], v34 offset:63488\n"
      "v_add3_u32 v3, v3, v62, v63\n"
      "v_mfma_f32_16x16x32_bf16 v[180:183], a[108:111], v[84:87], v[180:183]\n"
      "ds_read_b64_tr_b16 v[120:121], v33 offset:61440\n"
      "v_fmac_f32 v172, v22, v232\n"
      "v_fmac_f32 v173, v22, v233\n"
      "v_mfma_f32_16x16x32_bf16 v[188:191], a[112:115], v[84:87], v[188:191]\n"
      "global_load_dwordx4 a[88:91], v44, s[6:7] offset:-2048 sc1\n"
      "ds_read_b64_tr_b16 v[122:123], v33 offset:63488\n"
      "v_add_u32_dpp v3, v3, v3 quad_perm:[1,0,3,2] row_mask:0xf bank_mask:0xf\n"
      "v_fmac_f32 v174, v22, v234\n"
      "v_fmac_f32 v175, v22, v235\n"
      "s_add_u32 m0, s40, 8192\n"
      "v_add_u32 v43, 49152, v7\n"
      "v_mfma_f32_16x16x32_bf16 v[196:199], a[116:119], v[84:87], v[196:199]\n"
      "global_load_lds_dwordx4 v43, s[6:7] offset:0 sc1\n"
      "ds_read_b64_tr_b16 v[124:125], v35 offset:61440\n"
      "v_fma_f32 v172, v172, v23, v244\n"
      "v_fma_f32 v173, v173, v23, v245\n"
      "v_fma_f32 v174, v174, v23, v246\n"
      "v_mfma_f32_16x16x32_bf16 v[200:203], a[108:111], v[88:91], v[200:203]\n"
      "ds_read_b64_tr_b16 v[126:127], v35 offset:63488\n"
      "v_add_u32_dpp v3, v3, v3 quad_perm:[2,3,0,1] row_mask:0xf bank_mask:0xf\n"
      "v_fma_f32 v175, v175, v23, v247\n"
      "v_mul_f32 v172, v172, v174\n"
      "v_mfma_f32_16x16x32_bf16 v[208:211], a[112:115], v[88:91], v[208:211]\n"
      "v_mul_f32 v173, v173, v175\n"
      "v_cvt_pk_bf16_f32 v172, v172, v173\n"
      "v_mfma_f32_16x16x32_bf16 v[216:219], a[116:119], v[88:91], v[216:219]\n"
      "global_load_dwordx4 a[92:95], v44, s[6:7] offset:-1024 sc1\n"
      "ds_write_b32 v46, v172 offset:11264\n"
      "v_add_u32_dpp v3, v3, v3 row_half_mirror row_mask:0xf bank_mask:0xf\n"
      "v_cmp_ne_u32 vcc, s71, v3\n"
      "v_mfma_f32_16x16x32_bf16 v[204:207], a[108:111], v[92:95], v[204:207]\n"
      "v_mfma_f32_16x16x32_bf16 v[212:215], a[112:115], v[92:95], v[212:215]\n"
      "s_cbranch_vccnz L_sx9_%=\n"
      "L_bx9_%=:\n"
      "v_mfma_f32_16x16x32_bf16 v[220:223], a[116:119], v[92:95], v[220:223]\n"
      "s_waitcnt lgkmcnt(0)\n"
      "s_barrier\n"
      "v_mfma_f32_16x16x32_bf16 v[176:179], a[120:123], v[96:99], v[176:179]\n"
      "global_load_dwordx4 a[96:99], v44, s[6:7] offset:0 sc1\n"
      "ds_read_b128 v[160:163], v47 offset:0\n"
      "v_add_u32 v57, s40, v7\n"
      "v_mfma_f32_16x16x32_bf16 v[184:187], a[124:127], v[96:99], v[184:187]\n"
      "ds_read_b128 v[164:167], v47 offset:4096\n"
      "v_mfma_f32_16x16x32_bf16 v[192:195], a[128:131], v[96:99], v[192:195]\n"
      "ds_read_b128 v[168:171], v47 offset:8192\n"
      "ds_read_b64_tr_b16 v[64:65], v6 offset:0\n"
      "v_mfma_f32_16x16x32_bf16 v[180:183], a[120:123], v[100:103], v[180:183]\n"
      "ds_read_b64_tr_b16 v[66:67], v6 offset:2048\n"
      "v_mfma_f32_16x16x32_bf16 v[188:191], a[124:127], v[100:103], v[188:191]\n"
      "global_load_dwordx4 a[100:103], v44, s[6:7] offset:1024 sc1\n"
      "ds_read_b64_tr_b16 v[68:69], v34 offset:0\n"
      "v_mfma_f32_16x16x32_bf16 v[196:199], a[128:131], v[100:103], v[196:199]\n"
      "ds_read_b64_tr_b16 v[70:71], v34 offset:2048\n"
      "s_waitcnt lgkmcnt(4)\n"
      "v_mfma_f32_16x16x32_bf16 v[200:203], a[120:123], v[104:107], v[200:203]\n"
      "ds_read_b64_tr_b16 v[72:73], v6 offset:4096\n"
      "v_mfma_f32_16x16x32_bf16 v[208:211], a[124:127], v[104:107], v[208:211]\n"
      "buffer_store_dwordx4 v[160:163], v7, s[32:35], s61 offen offset:0 sc1\n"
      "ds_read_b64_tr_b16 v[74:75], v6 offset:6144\n"
      "v_add3_u32 v58, v160, v161, s30\n"
      "v_mfma_f32_16x16x32_bf16 v[216:219], a[128:131], v[104:107], v[216:219]\n"
      "global_load_dwordx4 a[104:107], v44, s[6:7] offset:2048 sc1\n"
      "ds_read_b64_tr_b16 v[76:77], v34 offset:4096\n"
      "v_add3_u32 v58, v58, v162, v163\n"
      "v_add3_u32 v58, v58, v164, v165\n"
      "s_add_u32 m0, s41, 0\n"
      "v_add_u32 v43, 50176, v7\n"
      "v_mfma_f32_16x16x32_bf16 v[204:207], a[120:123], v[108:111], v[204:207]\n"
      "buffer_store_dwordx4 v[164:167], v7, s[32:35], s61 offen offset:1024 sc1\n"
      "global_load_lds_dwordx4 v43, s[6:7] offset:0 sc1\n"
      "ds_read_b64_tr_b16 v[78:79], v34 offset:6144\n"
      "v_add3_u32 v58, v58, v166, v167\n"
      "v_mfma_f32_16x16x32_bf16 v[212:215], a[124:127], v[108:111], v[212:215]\n"
      "ds_read_b64_tr_b16 v[80:81], v6 offset:8192\n"
      "v_add3_u32 v58, v58, v168, v169\n"
      "v_add3_u32 v58, v58, v170, v171\n"
      "v_mfma_f32_16x16x32_bf16 v[220:223], a[128:131], v[108:111], v[220:223]\n"
      "buffer_store_dwordx4 v[168:171], v7, s[32:35], s61 offen offset:2048 sc1\n"
      "v_mfma_f32_16x16x32_bf16 v[176:179], a[132:135], v[112:115], v[176:179]\n"
      "global_load_dwordx4 a[108:111], v44, s[6:7] offset:3072 sc1\n"
      "ds_read_b64_tr_b16 v[82:83], v6 offset:10240\n"
      "v_mfma_f32_16x16x32_bf16 v[184:187], a[136:139], v[112:115], v[184:187]\n"
      "buffer_store_dword v58, v42, s[32:35], s62 offen sc1\n"
      "global_load_lds_dwordx4 v43, s[6:7] offset:1024 sc1\n"
      "ds_read_b64_tr_b16 v[84:85], v34 offset:8192\n"
      "v_mfma_f32_16x16x32_bf16 v[192:195], a[140:143], v[112:115], v[192:195]\n"
      "ds_read_b64_tr_b16 v[86:87], v34 offset:10240\n"
      "v_mfma_f32_16x16x32_bf16 v[180:183], a[132:135], v[116:119], v[180:183]\n"
      "ds_read_b64_tr_b16 v[88:89], v6 offset:12288\n"
      "v_add_u32 v44, 36864, v7\n"
      "v_mfma_f32_16x16x32_bf16 v[188:191], a[136:139], v[116:119], v[188:191]\n"
      "global_load_dwordx4 a[112:115], v44, s[6:7] offset:-4096 sc1\n"
      "ds_read_b64_tr_b16 v[90:91], v6 offset:14336\n"
      "v_mfma_f32_16x16x32_bf16 v[196:199], a[140:143], v[116:119], v[196:199]\n"
      "global_load_lds_dwordx4 v43, s[6:7] offset:2048 sc1\n"
      "ds_read_b64_tr_b16 v[92:93], v34 offset:12288\n"
      "v_mfma_f32_16x16x32_bf16 v[200:203], a[132:135], v[120:123], v[200:203]\n"
      "ds_read_b64_tr_b16 v[94:95], v34 offset:14336\n"
      "v_mfma_f32_16x16x32_bf16 v[208:211], a[136:139], v[120:123], v[208:211]\n"
      "ds_read_b64 v[24:25], v41 offset:512\n"
      "v_mfma_f32_16x16x32_bf16 v[216:219], a[140:143], v[120:123], v[216:219]\n"
      "global_load_dwordx4 a[116:119], v44, s[6:7] offset:-3072 sc1\n"
      "v_mfma_f32_16x16x32_bf16 v[204:207], a[132:135], v[124:127], v[204:207]\n"
      "global_load_lds_dwordx4 v43, s[6:7] offset:3072 sc1\n"
      "v_mfma_f32_16x16x32_bf16 v[212:215], a[136:139], v[124:127], v[212:215]\n"
      "ds_read_b64 v[26:27], v41 offset:768\n"
      "v_mfma_f32_16x16x32_bf16 v[220:223], a[140:143], v[124:127], v[220:223]\n"
      "ds_read_b64 v[28:29], v41 offset:640\n"
      "s_waitcnt lgkmcnt(3)\n"
      "v_mfma_f32_16x16x32_bf16 v[176:179], a[144:147], v[64:67], v[176:179]\n"
      "global_load_dwordx4 a[120:123], v44, s[6:7] offset:-2048 sc1\n"
      "s_add_u32 m0, s41, 4096\n"
      "v_add_u32 v43, 54272, v7\n"
      "v_mfma_f32_16x16x32_bf16 v[184:187], a[148:151], v[64:67], v[184:187]\n"
      "global_load_lds_dwordx4 v43, s[6:7] offset:0 sc1\n"
      "v_mfma_f32_16x16x32_bf16 v[192:195], a[152:155], v[64:67], v[192:195]\n"
      "ds_read_b64_tr_b16 v[96:97], v6 offset:16384\n"
      "v_mfma_f32_16x16x32_bf16 v[180:183], a[144:147], v[68:71], v[180:183]\n"
      "ds_read_b64_tr_b16 v[98:99], v6 offset:18432\n"
      "v_mfma_f32_16x16x32_bf16 v[188:191], a[148:151], v[68:71], v[188:191]\n"
      "global_load_dwordx4 a[124:127], v44, s[6:7] offset:-1024 sc1\n"
      "ds_read_b64_tr_b16 v[100:101], v34 offset:16384\n"
      "v_mfma_f32_16x16x32_bf16 v[196:199], a[152:155], v[68:71], v[196:199]\n"
      "global_load_lds_dwordx4 v43, s[6:7] offset:1024 sc1\n"
      "ds_read_b64_tr_b16 v[102:103], v34 offset:18432\n"
      "v_mfma_f32_16x16x32_bf16 v[176:179], a[156:159], v[72:75], v[176:179]\n"
      "ds_read_b64_tr_b16 v[104:105], v6 offset:20480\n"
      "v_mfma_f32_16x16x32_bf16 v[184:187], a[160:163], v[72:75], v[184:187]\n"
      "ds_read_b64_tr_b16 v[106:107], v6 offset:22528\n"
      "v_mfma_f32_16x16x32_bf16 v[192:195], a[164:167], v[72:75], v[192:195]\n"
      "global_load_dwordx4 a[128:131], v44, s[6:7] offset:0 sc1\n"
      "ds_read_b64_tr_b16 v[108:109], v34 offset:20480\n"
      "v_mfma_f32_16x16x32_bf16 v[180:183], a[156:159], v[76:79], v[180:183]\n"
      "global_load_lds_dwordx4 v43, s[6:7] offset:2048 sc1\n"
      "ds_read_b64_tr_b16 v[110:111], v34 offset:22528\n"
      "v_mfma_f32_16x16x32_bf16 v[188:191], a[160:163], v[76:79], v[188:191]\n"
      "ds_read_b64_tr_b16 v[112:113], v6 offset:24576\n"
      "v_mfma_f32_16x16x32_bf16 v[196:199], a[164:167], v[76:79], v[196:199]\n"
      "ds_read_b64_tr_b16 v[114:115], v6 offset:26624\n"
      "v_mfma_f32_16x16x32_bf16 v[176:179], a[168:171], v[80:83], v[176:179]\n"
      "global_load_dwordx4 a[132:135], v44, s[6:7] offset:1024 sc1\n"
      "v_mfma_f32_16x16x32_bf16 v[184:187], a[172:175], v[80:83], v[184:187]\n"
      "global_load_lds_dwordx4 v43, s[6:7] offset:3072 sc1\n"
      "ds_read_b64_tr_b16 v[116:117], v34 offset:24576\n"
      "v_mfma_f32_16x16x32_bf16 v[192:195], a[176:179], v[80:83], v[192:195]\n"
      "ds_read_b64_tr_b16 v[118:119], v34 offset:26624\n"
      "v_mfma_f32_16x16x32_bf16 v[180:183], a[168:171], v[84:87], v[180:183]\n"
      "ds_read_b64_tr_b16 v[120:121], v6 offset:28672\n"
      "v_mfma_f32_16x16x32_bf16 v[188:191], a[172:175], v[84:87], v[188:191]\n"
      "global_load_dwordx4 a[136:139], v44, s[6:7] offset:2048 sc1\n"
      "ds_read_b64_tr_b16 v[122:123], v6 offset:30720\n"
      "v_mfma_f32_16x16x32_bf16 v[196:199], a[176:179], v[84:87], v[196:199]\n"
      "ds_read_b64_tr_b16 v[124:125], v34 offset:28672\n"
      "v_mfma_f32_16x16x32_bf16 v[176:179], a[180:183], v[88:91], v[176:179]\n"
      "ds_read_b64_tr_b16 v[126:127], v34 offset:30720\n"
      "v_mfma_f32_16x16x32_bf16 v[184:187], a[184:187], v[88:91], v[184:187]\n"
      "ds_read_b64 v[30:31], v41 offset:896\n"
      "v_mfma_f32_16x16x32_bf16 v[192:195], a[188:191], v[88:91], v[192:195]\n"
      "global_load_dwordx4 a[140:143], v44, s[6:7] offset:3072 sc1\n"
      "v_mfma_f32_16x16x32_bf16 v[180:183], a[180:183], v[92:95], v[180:183]\n"
      "v_mfma_f32_16x16x32_bf16 v[188:191], a[184:187], v[92:95], v[188:191]\n"
      "v_mfma_f32_16x16x32_bf16 v[196:199], a[188:191], v[92:95], v[196:199]\n"
      "s_waitcnt lgkmcnt(1)\n"
      "v_mfma_f32_16x16x32_bf16 v[176:179], a[192:195], v[96:99], v[176:179]\n"
      "ds_read_b64_tr_b16 v[64:65], v33 offset:0\n"
      "v_mfma_f32_16x16x32_bf16 v[184:187], a[196:199], v[96:99], v[184:187]\n"
      "ds_read_b64_tr_b16 v[66:67], v33 offset:2048\n"
      "v_mfma_f32_16x16x32_bf16 v[192:195], a[200:203], v[96:99], v[192:195]\n"
      "ds_read_b64_tr_b16 v[68:69], v35 offset:0\n"
      "v_mfma_f32_16x16x32_bf16 v[180:183], a[192:195], v[100:103], v[180:183]\n"
      "ds_read_b64_tr_b16 v[70:71], v35 offset:2048\n"
      "v_mfma_f32_16x16x32_bf16 v[188:191], a[196:199], v[100:103], v[188:191]\n"
      "ds_read_b64_tr_b16 v[72:73], v33 offset:4096\n"
      "v_mfma_f32_16x16x32_bf16 v[196:199], a[200:203], v[100:103], v[196:199]\n"
      "ds_read_b64_tr_b16 v[74:75], v33 offset:6144\n"
      "v_mfma_f32_16x16x32_bf16 v[176:179], a[204:207], v[104:107], v[176:179]\n"
      "ds_read_b64_tr_b16 v[76:77], v35 offset:4096\n"
      "v_add_u32 v45, 4096, v7\n"
      "v_mfma_f32_16x16x32_bf16 v[184:187], a[208:211], v[104:107], v[184:187]\n"
      "ds_read_b64_tr_b16 v[78:79], v35 offset:6144\n"
      "v_mfma_f32_16x16x32_bf16 v[192:195], a[212:215], v[104:107], v[192:195]\n"
      "global_load_dwordx4 v[8:11], v45, s[10:11] offset:-4096 sc1\n"
      "v_mfma_f32_16x16x32_bf16 v[180:183], a[204:207], v[108:111], v[180:183]\n"
      "ds_read_b64_tr_b16 v[80:81], v33 offset:8192\n"
      "v_mfma_f32_16x16x32_bf16 v[188:191], a[208:211], v[108:111], v[188:191]\n"
      "global_load_dwordx4 v[12:15], v45, s[12:13] offset:-4096 sc1\n"
      "v_mfma_f32_16x16x32_bf16 v[196:199], a[212:215], v[108:111], v[196:199]\n"
      "ds_read_b64_tr_b16 v[82:83], v33 offset:10240\n"
      "v_mfma_f32_16x16x32_bf16 v[176:179], a[216:219], v[112:115], v[176:179]\n"
      "ds_read_b64_tr_b16 v[84:85], v35 offset:8192\n"
      "v_mfma_f32_16x16x32_bf16 v[184:187], a[220:223], v[112:115], v[184:187]\n"
      "global_load_dwordx4 v[16:19], v45, s[10:11] offset:-3072 sc1\n"
      "v_mfma_f32_16x16x32_bf16 v[192:195], a[224:227], v[112:115], v[192:195]\n"
      "ds_read_b64_tr_b16 v[86:87], v35 offset:10240\n"
      "v_mfma_f32_16x16x32_bf16 v[180:183], a[216:219], v[116:119], v[180:183]\n"
      "ds_read_b64_tr_b16 v[88:89], v33 offset:12288\n"
      "v_mfma_f32_16x16x32_bf16 v[188:191], a[220:223], v[116:119], v[188:191]\n"
      "global_load_dwordx4 v[20:23], v45, s[12:13] offset:-3072 sc1\n"
      "ds_read_b64_tr_b16 v[90:91], v33 offset:14336\n"
      "v_mfma_f32_16x16x32_bf16 v[196:199], a[224:227], v[116:119], v[196:199]\n"
      "ds_read_b64_tr_b16 v[92:93], v35 offset:12288\n"
      "v_mfma_f32_16x16x32_bf16 v[176:179], a[228:231], v[120:123], v[176:179]\n"
      "ds_read_b64_tr_b16 v[94:95], v35 offset:14336\n"
      "v_mfma_f32_16x16x32_bf16 v[184:187], a[232:235], v[120:123], v[184:187]\n"
      "global_load_dwordx4 v[128:131], v45, s[10:11] offset:-2048 sc1\n"
      "v_mfma_f32_16x16x32_bf16 v[192:195], a[236:239], v[120:123], v[192:195]\n"
      "v_mfma_f32_16x16x32_bf16 v[180:183], a[228:231], v[124:127], v[180:183]\n"
      "v_mfma_f32_16x16x32_bf16 v[188:191], a[232:235], v[124:127], v[188:191]\n"
      "global_load_dwordx4 v[132:135], v45, s[12:13] offset:-2048 sc1\n"
      "v_mfma_f32_16x16x32_bf16 v[196:199], a[236:239], v[124:127], v[196:199]\n"
      "buffer_load_dword v36, v42, s[32:35], s63 offen offset:0 sc1\n"
      "buffer_load_dword v37, v42, s[32:35], s63 offen offset:256 sc1\n"
      "s_waitcnt lgkmcnt(0)\n"
      "s_barrier\n"
      "v_mfma_f32_16x16x32_bf16 v[200:203], a[144:147], v[64:67], v[200:203]\n"
      "ds_read_b64_tr_b16 v[96:97], v33 offset:16384\n"
      "v_mfma_f32_16x16x32_bf16 v[208:211], a[148:151], v[64:67], v[208:211]\n"
      "global_load_dwordx4 v[136:139], v45, s[10:11] offset:-1024 sc1\n"
      "v_mfma_f32_16x16x32_bf16 v[216:219], a[152:155], v[64:67], v[216:219]\n"
      "ds_read_b64_tr_b16 v[98:99], v33 offset:18432\n"
      "v_mfma_f32_16x16x32_bf16 v[204:207], a[144:147], v[68:71], v[204:207]\n"
      "ds_read_b64_tr_b16 v[100:101], v35 offset:16384\n"
      "v_fmac_f32 v176, v24, v224\n"
      "v_mfma_f32_16x16x32_bf16 v[212:215], a[148:151], v[68:71], v[212:215]\n"
      "global_load_dwordx4 v[140:143], v45, s[12:13] offset:-1024 sc1\n"
      "v_fmac_f32 v177, v24, v225\n"
      "v_fmac_f32 v178, v24, v226\n"
      "v_mfma_f32_16x16x32_bf16 v[220:223], a[152:155], v[68:71], v[220:223]\n"
      "ds_read_b64_tr_b16 v[102:103], v35 offset:18432\n"
      "v_fmac_f32 v179, v24, v227\n"
      "v_fma_f32 v176, v176, v25, v236\n"
      "v_mfma_f32_16x16x32_bf16 v[200:203], a[156:159], v[72:75], v[200:203]\n"
      "ds_read_b64_tr_b16 v[104:105], v33 offset:20480\n"
      "v_fma_f32 v177, v177, v25, v237\n"
      "v_mfma_f32_16x16x32_bf16 v[208:211], a[160:163], v[72:75], v[208:211]\n"
      "global_load_dwordx4 v[144:147], v45, s[10:11] offset:0 sc1\n"
      "ds_read_b64_tr_b16 v[106:107], v33 offset:22528\n"
      "v_fma_f32 v178, v178, v25, v238\n"
      "v_fma_f32 v179, v179, v25, v239\n"
      "v_mfma_f32_16x16x32_bf16 v[216:219], a[164:167], v[72:75], v[216:219]\n"
      "ds_read_b64_tr_b16 v[108:109], v35 offset:20480\n"
      "v_mul_f32 v176, v176, v178\n"
      "v_mul_f32 v177, v177, v179\n"
      "v_mfma_f32_16x16x32_bf16 v[204:207], a[156:159], v[76:79], v[204:207]\n"
      "ds_read_b64_tr_b16 v[110:111], v35 offset:22528\n"
      "v_cvt_pk_bf16_f32 v176, v176, v177\n"
      "v_mfma_f32_16x16x32_bf16 v[212:215], a[160:163], v[76:79], v[212:215]\n"
      "global_load_dwordx4 v[148:151], v45, s[12:13] offset:0 sc1\n"
      "ds_write_b32 v46, v176 offset:0\n"
      "ds_read_b64_tr_b16 v[112:113], v33 offset:24576\n"
      "v_fmac_f32 v184, v24, v228\n"
      "v_mfma_f32_16x16x32_bf16 v[220:223], a[164:167], v[76:79], v[220:223]\n"
      "ds_read_b64_tr_b16 v[114:115], v33 offset:26624\n"
      "v_fmac_f32 v185, v24, v229\n"
      "v_fmac_f32 v186, v24, v230\n"
      "v_mfma_f32_16x16x32_bf16 v[200:203], a[168:171], v[80:83], v[200:203]\n"
      "ds_read_b64_tr_b16 v[116:117], v35 offset:24576\n"
      "v_fmac_f32 v187, v24, v231\n"
      "v_fma_f32 v184, v184, v25, v240\n"
      "v_mfma_f32_16x16x32_bf16 v[208:211], a[172:175], v[80:83], v[208:211]\n"
      "global_load_dwordx4 v[152:155], v45, s[10:11] offset:1024 sc1\n"
      "v_fma_f32 v185, v185, v25, v241\n"
      "v_mfma_f32_16x16x32_bf16 v[216:219], a[176:179], v[80:83], v[216:219]\n"
      "ds_read_b64_tr_b16 v[118:119], v35 offset:26624\n"
      "v_fma_f32 v186, v186, v25, v242\n"
      "v_fma_f32 v187, v187, v25, v243\n"
      "v_mfma_f32_16x16x32_bf16 v[204:207], a[168:171], v[84:87], v[204:207]\n"
      "ds_read_b64_tr_b16 v[120:121], v33 offset:28672\n"
      "v_mul_f32 v184, v184, v186\n"
      "v_mul_f32 v185, v185, v187\n"
      "v_mfma_f32_16x16x32_bf16 v[212:215], a[172:175], v[84:87], v[212:215]\n"
      "global_load_dwordx4 v[156:159], v45, s[12:13] offset:1024 sc1\n"
      "ds_read_b64_tr_b16 v[122:123], v33 offset:30720\n"
      "v_cvt_pk_bf16_f32 v184, v184, v185\n"
      "v_mfma_f32_16x16x32_bf16 v[220:223], a[176:179], v[84:87], v[220:223]\n"
      "buffer_load_dword v38, v42, s[32:35], s63 offen offset:512 sc1\n"
      "buffer_load_dword v39, v42, s[32:35], s63 offen offset:768 sc1\n"
      "ds_write_b32 v46, v184 offset:4096\n"
      "ds_read_b64_tr_b16 v[124:125], v35 offset:28672\n"
      "v_fmac_f32 v192, v24, v232\n"
      "v_mfma_f32_16x16x32_bf16 v[200:203], a[180:183], v[88:91], v[200:203]\n"
      "ds_read_b64_tr_b16 v[126:127], v35 offset:30720\n"
      "v_fmac_f32 v193, v24, v233\n"
      "v_fmac_f32 v194, v24, v234\n"
      "v_add_u32 v45, 10240, v7\n"
      "v_mfma_f32_16x16x32_bf16 v[208:211], a[184:187], v[88:91], v[208:211]\n"
      "global_load_dwordx4 v[172:175], v45, s[10:11] offset:-4096 sc1\n"
      "v_fmac_f32 v195, v24, v235\n"
      "v_fma_f32 v192, v192, v25, v244\n"
      "v_mfma_f32_16x16x32_bf16 v[216:219], a[188:191], v[88:91], v[216:219]\n"
      "v_fma_f32 v193, v193, v25, v245\n"
      "v_fma_f32 v194, v194, v25, v246\n"
      "v_mfma_f32_16x16x32_bf16 v[204:207], a[180:183], v[92:95], v[204:207]\n"
      "v_fma_f32 v195, v195, v25, v247\n"
      "v_mul_f32 v192, v192, v194\n"
      "v_mfma_f32_16x16x32_bf16 v[212:215], a[184:187], v[92:95], v[212:215]\n"
      "global_load_dwordx4 v[160:163], v45, s[12:13] offset:-4096 sc1\n"
      "v_mul_f32 v193, v193, v195\n"
      "v_mfma_f32_16x16x32_bf16 v[220:223], a[188:191], v[92:95], v[220:223]\n"
      "v_cvt_pk_bf16_f32 v192, v192, v193\n"
      "s_waitcnt lgkmcnt(0)\n"
      "v_mfma_f32_16x16x32_bf16 v[200:203], a[192:195], v[96:99], v[200:203]\n"
      "ds_write_b32 v46, v192 offset:8192\n"
      "v_fmac_f32 v180, v26, v224\n"
      "v_fmac_f32 v181, v26, v225\n"
      "v_mfma_f32_16x16x32_bf16 v[208:211], a[196:199], v[96:99], v[208:211]\n"
      "global_load_dwordx4 v[164:167], v45, s[10:11] offset:-3072 sc1\n"
      "v_fmac_f32 v182, v26, v226\n"
      "v_mfma_f32_16x16x32_bf16 v[216:219], a[200:203], v[96:99], v[216:219]\n"
      "v_fmac_f32 v183, v26, v227\n"
      "v_fma_f32 v180, v180, v27, v236\n"
      "v_mfma_f32_16x16x32_bf16 v[204:207], a[192:195], v[100:103], v[204:207]\n"
      "v_fma_f32 v181, v181, v27, v237\n"
      "v_fma_f32 v182, v182, v27, v238\n"
      "v_mfma_f32_16x16x32_bf16 v[212:215], a[196:199], v[100:103], v[212:215]\n"
      "global_load_dwordx4 v[168:171], v45, s[12:13] offset:-3072 sc1\n"
      "v_fma_f32 v183, v183, v27, v239\n"
      "v_mfma_f32_16x16x32_bf16 v[220:223], a[200:203], v[100:103], v[220:223]\n"
      "v_mul_f32 v180, v180, v182\n"
      "v_mul_f32 v181, v181, v183\n"
      "v_mfma_f32_16x16x32_bf16 v[200:203], a[204:207], v[104:107], v[200:203]\n"
      "v_cvt_pk_bf16_f32 v180, v180, v181\n"
      "s_waitcnt vmcnt(45)\n"
      "v_mfma_f32_16x16x32_bf16 v[208:211], a[208:211], v[104:107], v[208:211]\n"
      "global_load_dwordx4 v[64:67], v45, s[10:11] offset:-2048 sc1\n"
      "ds_write_b32 v46, v180 offset:2048\n"
      "ds_read_b128 a[144:147], v57 offset:0\n"
      "v_fmac_f32 v188, v26, v228\n"
      "v_mfma_f32_16x16x32_bf16 v[216:219], a[212:215], v[104:107], v[216:219]\n"
      "ds_read_b128 a[148:151], v57 offset:1024\n"
      "v_fmac_f32 v189, v26, v229\n"
      "v_fmac_f32 v190, v26, v230\n"
      "v_mfma_f32_16x16x32_bf16 v[204:207], a[204:207], v[108:111], v[204:207]\n"
      "ds_read_b128 a[152:155], v57 offset:2048\n"
      "v_fmac_f32 v191, v26, v231\n"
      "v_fma_f32 v188, v188, v27, v240\n"
      "v_mfma_f32_16x16x32_bf16 v[212:215], a[208:211], v[108:111], v[212:215]\n"
      "global_load_dwordx4 v[68:71], v45, s[12:13] offset:-2048 sc1\n"
      "v_fma_f32 v189, v189, v27, v241\n"
      "v_mfma_f32_16x16x32_bf16 v[220:223], a[212:215], v[108:111], v[220:223]\n"
      "buffer_load_dword v60, v42, s[32:35], s63 offen offset:1024 sc1\n"
      "buffer_load_dword v61, v42, s[32:35], s63 offen offset:1280 sc1\n"
      "v_fma_f32 v190, v190, v27, v242\n"
      "v_fma_f32 v191, v191, v27, v243\n"
      "v_mfma_f32_16x16x32_bf16 v[200:203], a[216:219], v[112:115], v[200:203]\n"
      "ds_read_b128 a[156:159], v57 offset:3072\n"
      "v_mul_f32 v188, v188, v190\n"
      "v_mul_f32 v189, v189, v191\n"
      "v_mfma_f32_16x16x32_bf16 v[208:211], a[220:223], v[112:115], v[208:211]\n"
      "global_load_dwordx4 v[72:75], v45, s[10:11] offset:-1024 sc1\n"
      "v_cvt_pk_bf16_f32 v188, v188, v189\n"
      "v_mfma_f32_16x16x32_bf16 v[216:219], a[224:227], v[112:115], v[216:219]\n"
      "ds_write_b32 v46, v188 offset:6144\n"
      "ds_read_b128 a[160:163], v57 offset:4096\n"
      "v_fmac_f32 v196, v26, v232\n"
      "v_mfma_f32_16x16x32_bf16 v[204:207], a[216:219], v[116:119], v[204:207]\n"
      "ds_read_b128 a[164:167], v57 offset:5120\n"
      "s_waitcnt vmcnt(17)\n"
      "v_fmac_f32 v197, v26, v233\n"
      "v_fmac_f32 v198, v26, v234\n"
      "v_mfma_f32_16x16x32_bf16 v[212:215], a[220:223], v[116:119], v[212:215]\n"
      "global_load_dwordx4 v[76:79], v45, s[12:13] offset:-1024 sc1\n"
      "v_add3_u32 v2, v8, v9, s30\n"
      "v_add3_u32 v2, v2, v10, v11\n"
      "v_add3_u32 v2, v2, v16, v17\n"
      "v_fmac_f32 v199, v26, v235\n"
      "v_fma_f32 v196, v196, v27, v244\n"
      "v_mfma_f32_16x16x32_bf16 v[220:223], a[224:227], v[116:119], v[220:223]\n"
      "ds_read_b128 a[168:171], v57 offset:6144\n"
      "v_add3_u32 v2, v2, v18, v19\n"
      "v_add3_u32 v2, v2, v128, v129\n"
      "v_add3_u32 v2, v2, v130, v131\n"
      "v_fma_f32 v197, v197, v27, v245\n"
      "v_mfma_f32_16x16x32_bf16 v[200:203], a[228:231], v[120:123], v[200:203]\n"
      "ds_read_b128 a[172:175], v57 offset:7168\n"
      "v_xor_b32 v2, v2, v36\n"
      "v_add3_u32 v3, v12, v13, s30\n"
      "v_add3_u32 v3, v3, v14, v15\n"
      "v_fma_f32 v198, v198, v27, v246\n"
      "v_fma_f32 v199, v199, v27, v247\n"
      "v_mfma_f32_16x16x32_bf16 v[208:211], a[232:235], v[120:123], v[208:211]\n"
      "global_load_dwordx4 v[80:83], v45, s[10:11] offset:0 sc1\n"
      "v_add3_u32 v3, v3, v20, v21\n"
      "v_add3_u32 v3, v3, v22, v23\n"
      "v_add3_u32 v3, v3, v132, v133\n"
      "v_mul_f32 v196, v196, v198\n"
      "v_mul_f32 v197, v197, v199\n"
      "v_add_u32 v44, 61440, v7\n"
      "v_mfma_f32_16x16x32_bf16 v[216:219], a[236:239], v[120:123], v[216:219]\n"
      "global_load_dwordx4 a[212:215], v44, s[6:7] offset:-3072 sc1\n"
      "v_add3_u32 v3, v3, v134, v135\n"
      "v_xor_b32 v3, v3, v37\n"
      "v_or_b32 v2, v2, v3\n"
      "v_cvt_pk_bf16_f32 v196, v196, v197\n"
      "v_mfma_f32_16x16x32_bf16 v[204:207], a[228:231], v[124:127], v[204:207]\n"
      "ds_write_b32 v46, v196 offset:10240\n"
      "ds_read_b128 a[176:179], v57 offset:8192\n"
      "v_cmp_ne_u32 vcc, 0, v2\n"
      "v_mfma_f32_16x16x32_bf16 v[212:215], a[232:235], v[124:127], v[212:215]\n"
      "global_load_dwordx4 v[84:87], v45, s[12:13] offset:0 sc1\n"
      "v_mfma_f32_16x16x32_bf16 v[220:223], a[236:239], v[124:127], v[220:223]\n"
      "s_cbranch_vccnz L_shA0_%=\n"
      "L_bhA0_%=:\n"
      "s_waitcnt lgkmcnt(1)\n"
      "s_barrier\n"
      "v_mfma_f32_16x16x32_bf16 v[96:99], a[240:243], v[8:11], 0\n"
      "global_load_dwordx4 a[216:219], v44, s[6:7] offset:-2048 sc1\n"
      "global_load_dwordx4 a[220:223], v44, s[6:7] offset:-1024 sc1\n"
      "ds_read_b128 v[176:179], v47 offset:0\n"
      "v_mfma_f32_16x16x32_bf16 v[104:107], a[244:247], v[8:11], 0\n"
      "global_load_dwordx4 v[88:91], v45, s[10:11] offset:1024 sc1\n"
      "ds_read_b128 v[180:183], v47 offset:4096\n"
      "v_fmac_f32 v200, v28, v224\n"
      "v_mfma_f32_16x16x32_bf16 v[112:115], a[248:251], v[8:11], 0\n"
      "ds_read_b128 v[184:187], v47 offset:8192\n"
      "v_fmac_f32 v201, v28, v225\n"
      "v_fmac_f32 v202, v28, v226\n"
      "v_fmac_f32 v203, v28, v227\n"
      "v_fma_f32 v200, v200, v29, v236\n"
      "v_fma_f32 v201, v201, v29, v237\n"
      "v_fma_f32 v202, v202, v29, v238\n"
      "v_fma_f32 v203, v203, v29, v239\n"
      "v_mul_f32 v200, v200, v202\n"
      "v_mul_f32 v201, v201, v203\n"
      "v_cvt_pk_bf16_f32 v200, v200, v201\n"
      "v_mfma_f32_16x16x32_bf16 v[120:123], a[252:255], v[8:11], 0\n"
      "ds_write_b32 v46, v200 offset:1024\n"
      "v_add_u32 v43, s59, v7\n"
      "v_add_u32 v59, s59, v42\n"
      "v_fmac_f32 v208, v28, v228\n"
      "v_fmac_f32 v209, v28, v229\n"
      "v_fmac_f32 v210, v28, v230\n"
      "v_fmac_f32 v211, v28, v231\n"
      "v_fma_f32 v208, v208, v29, v240\n"
      "v_fma_f32 v209, v209, v29, v241\n"
      "v_fma_f32 v210, v210, v29, v242\n"
      "v_fma_f32 v211, v211, v29, v243\n"
      "v_mul_f32 v208, v208, v210\n"
      "v_mul_f32 v209, v209, v211\n"
      "v_cvt_pk_bf16_f32 v208, v208, v209\n"
      "v_mfma_f32_16x16x32_bf16 v[248:251], a[0:3], v[8:11], 0\n"
      "global_load_dwordx4 v[92:95], v45, s[12:13] offset:1024 sc1\n"
      "ds_write_b32 v46, v208 offset:5120\n"
      "v_fmac_f32 v216, v28, v232\n"
      "v_fmac_f32 v217, v28, v233\n"
      "v_mfma_f32_16x16x32_bf16 v[100:103], a[240:243], v[12:15], 0\n"
      "buffer_load_dword v62, v42, s[32:35], s63 offen offset:1536 sc1\n"
      "buffer_load_dword v63, v42, s[32:35], s63 offen offset:1792 sc1\n"
      "v_fmac_f32 v218, v28, v234\n"
      "v_fmac_f32 v219, v28, v235\n"
      "v_fma_f32 v216, v216, v29, v244\n"
      "v_fma_f32 v217, v217, v29, v245\n"
      "v_fma_f32 v218, v218, v29, v246\n"
      "v_fma_f32 v219, v219, v29, v247\n"
      "v_mul_f32 v216, v216, v218\n"
      "v_mul_f32 v217, v217, v219\n"
      "v_cvt_pk_bf16_f32 v216, v216, v217\n"
      "v_mfma_f32_16x16x32_bf16 v[108:111], a[244:247], v[12:15], 0\n"
      "ds_write_b32 v46, v216 offset:9216\n"
      "s_waitcnt lgkmcnt(3)\n"
      "v_fmac_f32 v204, v30, v224\n"
      "v_fmac_f32 v205, v30, v225\n"
      "v_fmac_f32 v206, v30, v226\n"
      "v_fmac_f32 v207, v30, v227\n"
      "v_fma_f32 v204, v204, v31, v236\n"
      "v_fma_f32 v205, v205, v31, v237\n"
      "v_fma_f32 v206, v206, v31, v238\n"
      "v_fma_f32 v207, v207, v31, v239\n"
      "v_mul_f32 v204, v204, v206\n"
      "v_mul_f32 v205, v205, v207\n"
      "v_cvt_pk_bf16_f32 v204, v204, v205\n"
      "v_mfma_f32_16x16x32_bf16 v[116:119], a[248:251], v[12:15], 0\n"
      "buffer_store_dwordx4 v[176:179], v43, s[32:35], s69 offen offset:0 sc1\n"
      "ds_write_b32 v46, v204 offset:3072\n"
      "v_add3_u32 v58, v176, v177, s30\n"
      "v_add3_u32 v58, v58, v178, v179\n"
      "v_mfma_f32_16x16x32_bf16 v[124:127], a[252:255], v[12:15], 0\n"
      "v_add3_u32 v58, v58, v180, v181\n"
      "v_fmac_f32 v212, v30, v228\n"
      "v_fmac_f32 v213, v30, v229\n"
      "v_fmac_f32 v214, v30, v230\n"
      "v_fmac_f32 v215, v30, v231\n"
      "v_fma_f32 v212, v212, v31, v240\n"
      "v_fma_f32 v213, v213, v31, v241\n"
      "v_fma_f32 v214, v214, v31, v242\n"
      "v_fma_f32 v215, v215, v31, v243\n"
      "v_mul_f32 v212, v212, v214\n"
      "v_mul_f32 v213, v213, v215\n"
      "v_cvt_pk_bf16_f32 v212, v212, v213\n"
      "v_mfma_f32_16x16x32_bf16 v[252:255], a[0:3], v[12:15], 0\n"
      "buffer_store_dwordx4 v[180:183], v43, s[32:35], s69 offen offset:1024 sc1\n"
      "ds_write_b32 v46, v212 offset:7168\n"
      "v_add3_u32 v58, v58, v182, v183\n"
      "v_fmac_f32 v220, v30, v232\n"
      "v_fmac_f32 v221, v30, v233\n"
      "v_fmac_f32 v222, v30, v234\n"
      "v_fmac_f32 v223, v30, v235\n"
      "v_mfma_f32_16x16x32_bf16 v[96:99], a[4:7], v[16:19], v[96:99]\n"
      "v_add3_u32 v58, v58, v184, v185\n"
      "v_add3_u32 v58, v58, v186, v187\n"
      "v_fma_f32 v220, v220, v31, v244\n"
      "v_fma_f32 v221, v221, v31, v245\n"
      "v_fma_f32 v222, v222, v31, v246\n"
      "v_fma_f32 v223, v223, v31, v247\n"
      "v_mul_f32 v220, v220, v222\n"
      "v_mul_f32 v221, v221, v223\n"
      "v_cvt_pk_bf16_f32 v220, v220, v221\n"
      "v_mfma_f32_16x16x32_bf16 v[104:107], a[8:11], v[16:19], v[104:107]\n"
      "buffer_store_dwordx4 v[184:187], v43, s[32:35], s69 offen offset:2048 sc1\n"
      "ds_write_b32 v46, v220 offset:11264\n"
      "v_add_u32 v57, s41, v7\n"
      "v_mfma_f32_16x16x32_bf16 v[112:115], a[12:15], v[16:19], v[112:115]\n"
      "ds_read_b128 a[180:183], v57 offset:0\n"
      "v_mfma_f32_16x16x32_bf16 v[120:123], a[16:19], v[16:19], v[120:123]\n"
      "buffer_store_dword v58, v59, s[32:35], s70 offen sc1\n"
      "v_mfma_f32_16x16x32_bf16 v[248:251], a[20:23], v[16:19], v[248:251]\n"
      "ds_read_b128 a[184:187], v57 offset:1024\n"
      "s_waitcnt lgkmcnt(2)\n"
      "s_barrier\n"
      "v_mfma_f32_16x16x32_bf16 v[100:103], a[4:7], v[20:23], v[100:103]\n"
      "ds_read_b128 v[200:203], v47 offset:0\n"
      "v_mfma_f32_16x16x32_bf16 v[108:111], a[8:11], v[20:23], v[108:111]\n"
      "ds_read_b128 v[204:207], v47 offset:4096\n"
      "v_mfma_f32_16x16x32_bf16 v[116:119], a[12:15], v[20:23], v[116:119]\n"
      "ds_read_b128 v[208:211], v47 offset:8192\n"
      "v_xor_b32 v43, 0x80000000, v43\n"
      "v_mfma_f32_16x16x32_bf16 v[124:127], a[16:19], v[20:23], v[124:127]\n"
      "ds_read_b128 a[188:191], v57 offset:2048\n"
      "v_xor_b32 v59, 0x80000000, v59\n"
      "v_mfma_f32_16x16x32_bf16 v[252:255], a[20:23], v[20:23], v[252:255]\n"
      "ds_read_b128 a[192:195], v57 offset:3072\n"
      "v_mfma_f32_16x16x32_bf16 v[96:99], a[24:27], v[128:131], v[96:99]\n"
      "ds_read_b128 a[196:199], v57 offset:4096\n"
      "v_mfma_f32_16x16x32_bf16 v[104:107], a[28:31], v[128:131], v[104:107]\n"
      "ds_read_b128 a[200:203], v57 offset:5120\n"
      "s_waitcnt vmcnt(23)\n"
      "s_waitcnt lgkmcnt(4)\n"
      "v_mfma_f32_16x16x32_bf16 v[112:115], a[32:35], v[128:131], v[112:115]\n"
      "buffer_store_dwordx4 v[200:203], v43, s[32:35], s69 offen offset:0 sc1\n"
      "v_add3_u32 v2, v136, v137, s30\n"
      "v_add3_u32 v2, v2, v138, v139\n"
      "v_add3_u32 v2, v2, v144, v145\n"
      "v_add3_u32 v58, v200, v201, s30\n"
      "v_mfma_f32_16x16x32_bf16 v[120:123], a[36:39], v[128:131], v[120:123]\n"
      "ds_read_b128 a[204:207], v57 offset:6144\n"
      "v_add3_u32 v2, v2, v146, v147\n"
      "v_add3_u32 v2, v2, v152, v153\n"
      "v_add3_u32 v2, v2, v154, v155\n"
      "v_add3_u32 v58, v58, v202, v203\n"
      "v_add3_u32 v58, v58, v204, v205\n"
      "v_mfma_f32_16x16x32_bf16 v[248:251], a[40:43], v[128:131], v[248:251]\n"
      "buffer_store_dwordx4 v[204:207], v43, s[32:35], s69 offen offset:1024 sc1\n"
      "v_xor_b32 v2, v2, v38\n"
      "v_add3_u32 v3, v140, v141, s30\n"
      "v_add3_u32 v3, v3, v142, v143\n"
      "v_add3_u32 v58, v58, v206, v207\n"
      "v_mfma_f32_16x16x32_bf16 v[100:103], a[24:27], v[132:135], v[100:103]\n"
      "ds_read_b128 a[208:211], v57 offset:7168\n"
      "v_add3_u32 v3, v3, v148, v149\n"
      "v_add3_u32 v3, v3, v150, v151\n"
      "v_add3_u32 v3, v3, v156, v157\n"
      "v_add3_u32 v58, v58, v208, v209\n"
      "v_add3_u32 v58, v58, v210, v211\n"
      "v_mfma_f32_16x16x32_bf16 v[108:111], a[28:31], v[132:135], v[108:111]\n"
      "buffer_store_dwordx4 v[208:211], v43, s[32:35], s69 offen offset:2048 sc1\n"
      "v_add3_u32 v3, v3, v158, v159\n"
      "v_xor_b32 v3, v3, v39\n"
      "v_or_b32 v2, v2, v3\n"
      "v_mfma_f32_16x16x32_bf16 v[116:119], a[32:35], v[132:135], v[116:119]\n"
      "v_cmp_ne_u32 vcc, 0, v2\n"
      "v_mfma_f32_16x16x32_bf16 v[124:127], a[36:39], v[132:135], v[124:127]\n"
      "buffer_store_dword v58, v59, s[32:35], s70 offen sc1\n"
      "v_mfma_f32_16x16x32_bf16 v[252:255], a[40:43], v[132:135], v[252:255]\n"
      "s_cbranch_vccnz L_shA1_%=\n"
      "L_bhA1_%=:\n"
      "v_mfma_f32_16x16x32_bf16 v[96:99], a[44:47], v[136:139], v[96:99]\n"
      "s_add_u32 s43, s73, s97\n"
      "v_add_u32 v56, s43, v7\n"
      "v_mfma_f32_16x16x32_bf16 v[104:107], a[48:51], v[136:139], v[104:107]\n"
      "s_and_b32 s52, s60, 1\n"
      "s_lshr_b32 s53, s60, 1\n"
      "v_mfma_f32_16x16x32_bf16 v[112:115], a[52:55], v[136:139], v[112:115]\n"
      "s_mul_i32 s54, s52, 20480\n"
      "s_lshl_b32 s53, s53, 11\n"
      "v_mfma_f32_16x16x32_bf16 v[120:123], a[56:59], v[136:139], v[120:123]\n"
      "s_add_u32 s54, s54, s53\n"
      "v_add_u32 v49, s54, v7\n"
      "v_mfma_f32_16x16x32_bf16 v[248:251], a[60:63], v[136:139], v[248:251]\n"
      "s_lshl_b32 s52, s52, 5\n"
      "v_xor_b32 v53, s52, v6\n"
      "v_mfma_f32_16x16x32_bf16 v[100:103], a[44:47], v[140:143], v[100:103]\n"
      "s_lshl_b32 s52, s53, 1\n"
      "s_add_u32 s52, s52, 110592\n"
      "v_mfma_f32_16x16x32_bf16 v[108:111], a[48:51], v[140:143], v[108:111]\n"
      "v_add_u32 v53, s52, v53\n"
      "s_lshr_b32 s53, s53, 4\n"
      "v_mfma_f32_16x16x32_bf16 v[116:119], a[52:55], v[140:143], v[116:119]\n"
      "s_add_u32 s53, s53, 163328\n"
      "v_lshrrev_b32 v48, 4, v7\n"
      "v_mfma_f32_16x16x32_bf16 v[124:127], a[56:59], v[140:143], v[124:127]\n"
      "v_and_b32 v50, 0x30, v48\n"
      "v_add_u32 v54, s53, v50\n"
      "v_mfma_f32_16x16x32_bf16 v[252:255], a[60:63], v[140:143], v[252:255]\n"
      "v_and_b32 v48, 15, v48\n"
      "v_lshrrev_b32 v50, 1, v50\n"
      "v_mfma_f32_16x16x32_bf16 v[96:99], a[64:67], v[144:147], v[96:99]\n"
      "v_mul_u32_u24 v48, 1280, v48\n"
      "v_add3_u32 v55, v48, v50, s74\n"
      "v_mfma_f32_16x16x32_bf16 v[104:107], a[68:71], v[144:147], v[104:107]\n"
      "v_mfma_f32_16x16x32_bf16 v[112:115], a[72:75], v[144:147], v[112:115]\n"
      "v_mfma_f32_16x16x32_bf16 v[120:123], a[76:79], v[144:147], v[120:123]\n"
      "v_mfma_f32_16x16x32_bf16 v[248:251], a[80:83], v[144:147], v[248:251]\n"
      "v_mfma_f32_16x16x32_bf16 v[100:103], a[64:67], v[148:151], v[100:103]\n"
      "v_mfma_f32_16x16x32_bf16 v[108:111], a[68:71], v[148:151], v[108:111]\n"
      "v_mfma_f32_16x16x32_bf16 v[116:119], a[72:75], v[148:151], v[116:119]\n"
      "v_mfma_f32_16x16x32_bf16 v[124:127], a[76:79], v[148:151], v[124:127]\n"
      "v_mfma_f32_16x16x32_bf16 v[252:255], a[80:83], v[148:151], v[252:255]\n"
      "v_mfma_f32_16x16x32_bf16 v[96:99], a[84:87], v[152:155], v[96:99]\n"
      "v_mfma_f32_16x16x32_bf16 v[104:107], a[88:91], v[152:155], v[104:107]\n"
      "s_waitcnt vmcnt(19)\n"
      "s_waitcnt lgkmcnt(0)\n"
      "v_mfma_f32_16x16x32_bf16 v[112:115], a[92:95], v[152:155], v[112:115]\n"
      "v_add3_u32 v2, v172, v173, s30\n"
      "v_add3_u32 v2, v2, v174, v175\n"
      "v_add3_u32 v2, v2, v164, v165\n"
      "v_mfma_f32_16x16x32_bf16 v[120:123], a[96:99], v[152:155], v[120:123]\n"
      "v_add3_u32 v2, v2, v166, v167\n"
      "v_add3_u32 v2, v2, v64, v65\n"
      "v_add3_u32 v2, v2, v66, v67\n"
      "v_mfma_f32_16x16x32_bf16 v[248:251], a[100:103], v[152:155], v[248:251]\n"
      "v_xor_b32 v2, v2, v60\n"
      "v_add3_u32 v3, v160, v161, s30\n"
      "v_add3_u32 v3, v3, v162, v163\n"
      "v_mfma_f32_16x16x32_bf16 v[100:103], a[84:87], v[156:159], v[100:103]\n"
      "v_add3_u32 v3, v3, v168, v169\n"
      "v_add3_u32 v3, v3, v170, v171\n"
      "v_add3_u32 v3, v3, v68, v69\n"
      "v_mfma_f32_16x16x32_bf16 v[108:111], a[88:91], v[156:159], v[108:111]\n"
      "v_add3_u32 v3, v3, v70, v71\n"
      "v_xor_b32 v3, v3, v61\n"
      "v_or_b32 v2, v2, v3\n"
      "v_mfma_f32_16x16x32_bf16 v[116:119], a[92:95], v[156:159], v[116:119]\n"
      "v_cmp_ne_u32 vcc, 0, v2\n"
      "v_mfma_f32_16x16x32_bf16 v[124:127], a[96:99], v[156:159], v[124:127]\n"
      "v_mfma_f32_16x16x32_bf16 v[252:255], a[100:103], v[156:159], v[252:255]\n"
      "s_cbranch_vccnz L_shA2_%=\n"
      "L_bhA2_%=:\n"
      "v_mfma_f32_16x16x32_bf16 v[96:99], a[104:107], v[172:175], v[96:99]\n"
      "v_mfma_f32_16x16x32_bf16 v[104:107], a[108:111], v[172:175], v[104:107]\n"
      "v_mfma_f32_16x16x32_bf16 v[112:115], a[112:115], v[172:175], v[112:115]\n"
      "v_add_u32 v45, 102400, v7\n"
      "v_mfma_f32_16x16x32_bf16 v[120:123], a[116:119], v[172:175], v[120:123]\n"
      "v_mfma_f32_16x16x32_bf16 v[248:251], a[120:123], v[172:175], v[248:251]\n"
      "global_load_dwordx4 v[224:227], v45, s[10:11] offset:-4096 sc1\n"
      "v_mfma_f32_16x16x32_bf16 v[100:103], a[104:107], v[160:163], v[100:103]\n"
      "v_mfma_f32_16x16x32_bf16 v[108:111], a[108:111], v[160:163], v[108:111]\n"
      "v_mfma_f32_16x16x32_bf16 v[116:119], a[112:115], v[160:163], v[116:119]\n"
      "v_mfma_f32_16x16x32_bf16 v[124:127], a[116:119], v[160:163], v[124:127]\n"
      "global_load_dwordx4 v[228:231], v45, s[10:11] offset:-3072 sc1\n"
      "v_mfma_f32_16x16x32_bf16 v[252:255], a[120:123], v[160:163], v[252:255]\n"
      "v_mfma_f32_16x16x32_bf16 v[96:99], a[124:127], v[164:167], v[96:99]\n"
      "v_mfma_f32_16x16x32_bf16 v[104:107], a[128:131], v[164:167], v[104:107]\n"
      "v_mfma_f32_16x16x32_bf16 v[112:115], a[132:135], v[164:167], v[112:115]\n"
      "global_load_dwordx4 v[232:235], v45, s[10:11] offset:-2048 sc1\n"
      "v_mfma_f32_16x16x32_bf16 v[120:123], a[136:139], v[164:167], v[120:123]\n"
      "v_mfma_f32_16x16x32_bf16 v[248:251], a[140:143], v[164:167], v[248:251]\n"
      "v_mfma_f32_16x16x32_bf16 v[100:103], a[124:127], v[168:171], v[100:103]\n"
      "global_load_dwordx4 v[236:239], v45, s[10:11] offset:-1024 sc1\n"
      "v_mfma_f32_16x16x32_bf16 v[108:111], a[128:131], v[168:171], v[108:111]\n"
      "v_mfma_f32_16x16x32_bf16 v[116:119], a[132:135], v[168:171], v[116:119]\n"
      "v_mfma_f32_16x16x32_bf16 v[124:127], a[136:139], v[168:171], v[124:127]\n"
      "global_load_dwordx4 v[240:243], v45, s[10:11] offset:0 sc1\n"
      "v_mfma_f32_16x16x32_bf16 v[252:255], a[140:143], v[168:171], v[252:255]\n"
      "v_mfma_f32_16x16x32_bf16 v[96:99], a[144:147], v[64:67], v[96:99]\n"
      "v_mfma_f32_16x16x32_bf16 v[104:107], a[148:151], v[64:67], v[104:107]\n"
      "global_load_dwordx4 v[244:247], v45, s[10:11] offset:1024 sc1\n"
      "s_waitcnt vmcnt(14)\n"
      "v_mfma_f32_16x16x32_bf16 v[112:115], a[152:155], v[64:67], v[112:115]\n"
      "buffer_load_dword v32, v42, s[32:35], s63 offen offset:2048 sc1\n"
      "buffer_load_dword v33, v42, s[32:35], s63 offen offset:2304 sc1\n"
      "v_add3_u32 v2, v72, v73, s30\n"
      "v_add3_u32 v2, v2, v74, v75\n"
      "v_add3_u32 v2, v2, v80, v81\n"
      "v_mfma_f32_16x16x32_bf16 v[120:123], a[156:159], v[64:67], v[120:123]\n"
      "v_add3_u32 v2, v2, v82, v83\n"
      "v_add3_u32 v2, v2, v88, v89\n"
      "v_add3_u32 v2, v2, v90, v91\n"
      "v_mfma_f32_16x16x32_bf16 v[248:251], a[160:163], v[64:67], v[248:251]\n"
      "v_xor_b32 v2, v2, v62\n"
      "v_add3_u32 v3, v76, v77, s30\n"
      "v_add3_u32 v3, v3, v78, v79\n"
      "v_mfma_f32_16x16x32_bf16 v[100:103], a[144:147], v[68:71], v[100:103]\n"
      "v_add_u32 v45, 108544, v7\n"
      "v_add3_u32 v3, v3, v84, v85\n"
      "v_add3_u32 v3, v3, v86, v87\n"
      "v_add3_u32 v3, v3, v92, v93\n"
      "v_mfma_f32_16x16x32_bf16 v[108:111], a[148:151], v[68:71], v[108:111]\n"
      "global_load_dwordx4 v[8:11], v45, s[10:11] offset:-4096 sc1\n"
      "v_add3_u32 v3, v3, v94, v95\n"
      "v_xor_b32 v3, v3, v63\n"
      "v_or_b32 v2, v2, v3\n"
      "v_mfma_f32_16x16x32_bf16 v[116:119], a[152:155], v[68:71], v[116:119]\n"
      "v_cmp_ne_u32 vcc, 0, v2\n"
      "v_mfma_f32_16x16x32_bf16 v[124:127], a[156:159], v[68:71], v[124:127]\n"
      "v_mfma_f32_16x16x32_bf16 v[252:255], a[160:163], v[68:71], v[252:255]\n"
      "global_load_dwordx4 v[12:15], v45, s[10:11] offset:-3072 sc1\n"
      "s_cbranch_vccnz L_shA3_%=\n"
      "L_bhA3_%=:\n"
      "v_mfma_f32_16x16x32_bf16 v[96:99], a[164:167], v[72:75], v[96:99]\n"
      "v_mfma_f32_16x16x32_bf16 v[104:107], a[168:171], v[72:75], v[104:107]\n"
      "v_mfma_f32_16x16x32_bf16 v[112:115], a[172:175], v[72:75], v[112:115]\n"
      "global_load_dwordx4 v[16:19], v45, s[10:11] offset:-2048 sc1\n"
      "v_mfma_f32_16x16x32_bf16 v[120:123], a[176:179], v[72:75], v[120:123]\n"
      "v_mfma_f32_16x16x32_bf16 v[248:251], a[180:183], v[72:75], v[248:251]\n"
      "v_mfma_f32_16x16x32_bf16 v[100:103], a[164:167], v[76:79], v[100:103]\n"
      "global_load_dwordx4 v[20:23], v45, s[10:11] offset:-1024 sc1\n"
      "v_mfma_f32_16x16x32_bf16 v[108:111], a[168:171], v[76:79], v[108:111]\n"
      "v_mfma_f32_16x16x32_bf16 v[116:119], a[172:175], v[76:79], v[116:119]\n"
      "v_mfma_f32_16x16x32_bf16 v[124:127], a[176:179], v[76:79], v[124:127]\n"
      "global_load_dwordx4 v[128:131], v45, s[10:11] offset:0 sc1\n"
      "v_mfma_f32_16x16x32_bf16 v[252:255], a[180:183], v[76:79], v[252:255]\n"
      "v_mfma_f32_16x16x32_bf16 v[96:99], a[184:187], v[80:83], v[96:99]\n"
      "v_mfma_f32_16x16x32_bf16 v[104:107], a[188:191], v[80:83], v[104:107]\n"
      "global_load_dwordx4 v[132:135], v45, s[10:11] offset:1024 sc1\n"
      "v_mfma_f32_16x16x32_bf16 v[112:115], a[192:195], v[80:83], v[112:115]\n"
      "buffer_load_dword v34, v42, s[32:35], s63 offen offset:2560 sc1\n"
      "buffer_load_dword v35, v42, s[32:35], s63 offen offset:2816 sc1\n"
      "v_mfma_f32_16x16x32_bf16 v[120:123], a[196:199], v[80:83], v[120:123]\n"
      "v_mfma_f32_16x16x32_bf16 v[248:251], a[200:203], v[80:83], v[248:251]\n"
      "v_mfma_f32_16x16x32_bf16 v[100:103], a[184:187], v[84:87], v[100:103]\n"
      "v_add_u32 v45, 102400, v7\n"
      "v_mfma_f32_16x16x32_bf16 v[108:111], a[188:191], v[84:87], v[108:111]\n"
      "v_mfma_f32_16x16x32_bf16 v[116:119], a[192:195], v[84:87], v[116:119]\n"
      "global_load_dwordx4 v[136:139], v45, s[12:13] offset:-4096 sc1\n"
      "v_mfma_f32_16x16x32_bf16 v[124:127], a[196:199], v[84:87], v[124:127]\n"
      "v_mfma_f32_16x16x32_bf16 v[252:255], a[200:203], v[84:87], v[252:255]\n"
      "v_mfma_f32_16x16x32_bf16 v[96:99], a[204:207], v[88:91], v[96:99]\n"
      "v_mfma_f32_16x16x32_bf16 v[104:107], a[208:211], v[88:91], v[104:107]\n"
      "global_load_dwordx4 v[140:143], v45, s[12:13] offset:-3072 sc1\n"
      "s_waitcnt vmcnt(10)\n"
      "v_mfma_f32_16x16x32_bf16 v[112:115], a[212:215], v[88:91], v[112:115]\n"
      "v_add3_u32 v2, v224, v225, s30\n"
      "v_add3_u32 v2, v2, v226, v227\n"
      "v_add3_u32 v2, v2, v228, v229\n"
      "v_mfma_f32_16x16x32_bf16 v[120:123], a[216:219], v[88:91], v[120:123]\n"
      "v_add3_u32 v2, v2, v230, v231\n"
      "v_add3_u32 v2, v2, v232, v233\n"
      "v_add3_u32 v2, v2, v234, v235\n"
      "v_mfma_f32_16x16x32_bf16 v[248:251], a[220:223], v[88:91], v[248:251]\n"
      "v_xor_b32 v2, v2, v32\n"
      "v_add3_u32 v3, v236, v237, s30\n"
      "v_add3_u32 v3, v3, v238, v239\n"
      "v_mfma_f32_16x16x32_bf16 v[100:103], a[204:207], v[92:95], v[100:103]\n"
      "global_load_dwordx4 v[144:147], v45, s[12:13] offset:-2048 sc1\n"
      "v_add3_u32 v3, v3, v240, v241\n"
      "v_add3_u32 v3, v3, v242, v243\n"
      "v_add3_u32 v3, v3, v244, v245\n"
      "v_mfma_f32_16x16x32_bf16 v[108:111], a[208:211], v[92:95], v[108:111]\n"
      "v_add3_u32 v3, v3, v246, v247\n"
      "v_xor_b32 v3, v3, v33\n"
      "v_or_b32 v2, v2, v3\n"
      "v_mfma_f32_16x16x32_bf16 v[116:119], a[212:215], v[92:95], v[116:119]\n"
      "v_cmp_ne_u32 vcc, 0, v2\n"
      "v_mfma_f32_16x16x32_bf16 v[124:127], a[216:219], v[92:95], v[124:127]\n"
      "v_mfma_f32_16x16x32_bf16 v[252:255], a[220:223], v[92:95], v[252:255]\n"
      "global_load_dwordx4 v[148:151], v45, s[12:13] offset:-1024 sc1\n"
      "s_cbranch_vccnz L_shB0_%=\n"
      "L_bhB0_%=:\n"
      "v_mfma_f32_16x16x32_bf16 v[176:179], a[240:243], v[224:227], 0\n"
      "ds_read_b128 v[80:83], v54 offset:0\n"
      "v_mfma_f32_16x16x32_bf16 v[180:183], a[244:247], v[224:227], 0\n"
      "ds_read_b128 v[84:87], v54 offset:64\n"
      "v_mfma_f32_16x16x32_bf16 v[184:187], a[248:251], v[224:227], 0\n"
      "ds_read_b128 v[88:91], v54 offset:128\n"
      "v_mfma_f32_16x16x32_bf16 v[188:191], a[252:255], v[224:227], 0\n"
      "global_load_dwordx4 v[152:155], v45, s[12:13] offset:0 sc1\n"
      "v_mfma_f32_16x16x32_bf16 v[192:195], a[0:3], v[224:227], 0\n"
      "ds_write_b128 v56, v[96:99] offset:0\n"
      "v_mfma_f32_16x16x32_bf16 v[176:179], a[4:7], v[228:231], v[176:179]\n"
      "v_mfma_f32_16x16x32_bf16 v[180:183], a[8:11], v[228:231], v[180:183]\n"
      "v_mfma_f32_16x16x32_bf16 v[184:187], a[12:15], v[228:231], v[184:187]\n"
      "global_load_dwordx4 v[156:159], v45, s[12:13] offset:1024 sc1\n"
      "v_mfma_f32_16x16x32_bf16 v[188:191], a[16:19], v[228:231], v[188:191]\n"
      "ds_write_b128 v56, v[100:103] offset:20480\n"
      "v_mfma_f32_16x16x32_bf16 v[192:195], a[20:23], v[228:231], v[192:195]\n"
      "v_mfma_f32_16x16x32_bf16 v[176:179], a[24:27], v[232:235], v[176:179]\n"
      "v_mfma_f32_16x16x32_bf16 v[180:183], a[28:31], v[232:235], v[180:183]\n"
      "buffer_load_dword v24, v42, s[32:35], s63 offen offset:3072 sc1\n"
      "v_mfma_f32_16x16x32_bf16 v[184:187], a[32:35], v[232:235], v[184:187]\n"
      "buffer_load_dword v25, v42, s[32:35], s63 offen offset:3328 sc1\n"
      "v_add_u32 v45, 108544, v7\n"
      "v_mfma_f32_16x16x32_bf16 v[188:191], a[36:39], v[232:235], v[188:191]\n"
      "global_load_dwordx4 v[172:175], v45, s[12:13] offset:-4096 sc1\n"
      "ds_write_b128 v56, v[104:107] offset:1024\n"
      "v_mfma_f32_16x16x32_bf16 v[192:195], a[40:43], v[232:235], v[192:195]\n"
      "v_mfma_f32_16x16x32_bf16 v[176:179], a[44:47], v[236:239], v[176:179]\n"
      "v_mfma_f32_16x16x32_bf16 v[180:183], a[48:51], v[236:239], v[180:183]\n"
      "ds_write_b128 v56, v[108:111] offset:21504\n"
      "v_mfma_f32_16x16x32_bf16 v[184:187], a[52:55], v[236:239], v[184:187]\n"
      "global_load_dwordx4 v[160:163], v45, s[12:13] offset:-3072 sc1\n"
      "v_mfma_f32_16x16x32_bf16 v[188:191], a[56:59], v[236:239], v[188:191]\n"
      "v_mfma_f32_16x16x32_bf16 v[192:195], a[60:63], v[236:239], v[192:195]\n"
      "ds_write_b128 v56, v[112:115] offset:2048\n"
      "v_mfma_f32_16x16x32_bf16 v[176:179], a[64:67], v[240:243], v[176:179]\n"
      "v_mfma_f32_16x16x32_bf16 v[180:183], a[68:71], v[240:243], v[180:183]\n"
      "global_load_dwordx4 v[164:167], v45, s[12:13] offset:-2048 sc1\n"
      "s_waitcnt vmcnt(11)\n"
      "v_mfma_f32_16x16x32_bf16 v[184:187], a[72:75], v[240:243], v[184:187]\n"
      "ds_write_b128 v56, v[116:119] offset:22528\n"
      "v_add3_u32 v2, v8, v9, s30\n"
      "v_add3_u32 v2, v2, v10, v11\n"
      "v_add3_u32 v2, v2, v12, v13\n"
      "v_add3_u32 v2, v2, v14, v15\n"
      "v_mfma_f32_16x16x32_bf16 v[188:191], a[76:79], v[240:243], v[188:191]\n"
      "v_add3_u32 v2, v2, v16, v17\n"
      "v_add3_u32 v2, v2, v18, v19\n"
      "v_mfma_f32_16x16x32_bf16 v[192:195], a[80:83], v[240:243], v[192:195]\n"
      "v_xor_b32 v2, v2, v34\n"
      "v_add3_u32 v3, v20, v21, s30\n"
      "v_add3_u32 v3, v3, v22, v23\n"
      "v_mfma_f32_16x16x32_bf16 v[176:179], a[84:87], v[244:247], v[176:179]\n"
      "global_load_dwordx4 v[168:171], v45, s[12:13] offset:-1024 sc1\n"
      "ds_write_b128 v56, v[120:123] offset:3072\n"
      "v_add3_u32 v3, v3, v128, v129\n"
      "v_add3_u32 v3, v3, v130, v131\n"
      "v_add3_u32 v3, v3, v132, v133\n"
      "v_add3_u32 v3, v3, v134, v135\n"
      "v_mfma_f32_16x16x32_bf16 v[180:183], a[88:91], v[244:247], v[180:183]\n"
      "v_xor_b32 v3, v3, v35\n"
      "v_or_b32 v2, v2, v3\n"
      "v_mfma_f32_16x16x32_bf16 v[184:187], a[92:95], v[244:247], v[184:187]\n"
      "v_cmp_ne_u32 vcc, 0, v2\n"
      "v_mfma_f32_16x16x32_bf16 v[188:191], a[96:99], v[244:247], v[188:191]\n"
      "ds_write_b128 v56, v[124:127] offset:23552\n"
      "v_mfma_f32_16x16x32_bf16 v[192:195], a[100:103], v[244:247], v[192:195]\n"
      "global_load_dwordx4 v[64:67], v45, s[12:13] offset:0 sc1\n"
      "s_cbranch_vccnz L_shB1_%=\n"
      "L_bhB1_%=:\n"
      "v_mfma_f32_16x16x32_bf16 v[176:179], a[104:107], v[8:11], v[176:179]\n"
      "v_mfma_f32_16x16x32_bf16 v[180:183], a[108:111], v[8:11], v[180:183]\n"
      "ds_write_b128 v56, v[248:251] offset:4096\n"
      "v_mfma_f32_16x16x32_bf16 v[184:187], a[112:115], v[8:11], v[184:187]\n"
      "v_mfma_f32_16x16x32_bf16 v[188:191], a[116:119], v[8:11], v[188:191]\n"
      "global_load_dwordx4 v[68:71], v45, s[12:13] offset:1024 sc1\n"
      "v_mfma_f32_16x16x32_bf16 v[192:195], a[120:123], v[8:11], v[192:195]\n"
      "ds_write_b128 v56, v[252:255] offset:24576\n"
      "v_mfma_f32_16x16x32_bf16 v[176:179], a[124:127], v[12:15], v[176:179]\n"
      "v_mfma_f32_16x16x32_bf16 v[180:183], a[128:131], v[12:15], v[180:183]\n"
      "v_mfma_f32_16x16x32_bf16 v[184:187], a[132:135], v[12:15], v[184:187]\n"
      "buffer_load_dword v26, v42, s[32:35], s63 offen offset:3584 sc1\n"
      "v_mfma_f32_16x16x32_bf16 v[188:191], a[136:139], v[12:15], v[188:191]\n"
      "buffer_load_dword v27, v42, s[32:35], s63 offen offset:3840 sc1\n"
      "v_mfma_f32_16x16x32_bf16 v[192:195], a[140:143], v[12:15], v[192:195]\n"
      "v_mfma_f32_16x16x32_bf16 v[176:179], a[144:147], v[16:19], v[176:179]\n"
      "s_waitcnt lgkmcnt(0)\n"
      "s_barrier\n"
      "v_mfma_f32_16x16x32_bf16 v[180:183], a[148:151], v[16:19], v[180:183]\n"
      "ds_read_b128 v[96:99], v49 offset:0\n"
      "v_add_u32 v56, 0xa000, v56\n"
      "v_mfma_f32_16x16x32_bf16 v[184:187], a[152:155], v[16:19], v[184:187]\n"
      "ds_read_b128 v[100:103], v49 offset:5120\n"
      "v_mfma_f32_16x16x32_bf16 v[188:191], a[156:159], v[16:19], v[188:191]\n"
      "ds_read_b128 v[104:107], v49 offset:10240\n"
      "v_mfma_f32_16x16x32_bf16 v[192:195], a[160:163], v[16:19], v[192:195]\n"
      "ds_read_b128 v[108:111], v49 offset:15360\n"
      "v_mfma_f32_16x16x32_bf16 v[176:179], a[164:167], v[20:23], v[176:179]\n"
      "ds_read_b64_tr_b16 v[72:73], v53 offset:0\n"
      "v_mfma_f32_16x16x32_bf16 v[180:183], a[168:171], v[20:23], v[180:183]\n"
      "ds_read_b64_tr_b16 v[74:75], v53 offset:2048\n"
      "v_mfma_f32_16x16x32_bf16 v[184:187], a[172:175], v[20:23], v[184:187]\n"
      "ds_read_b64_tr_b16 v[76:77], v53 offset:4096\n"
      "v_mfma_f32_16x16x32_bf16 v[188:191], a[176:179], v[20:23], v[188:191]\n"
      "ds_read_b128 v[112:115], v49 offset:1024\n"
      "v_mfma_f32_16x16x32_bf16 v[192:195], a[180:183], v[20:23], v[192:195]\n"
      "ds_read_b128 v[116:119], v49 offset:6144\n"
      "v_mfma_f32_16x16x32_bf16 v[176:179], a[184:187], v[128:131], v[176:179]\n"
      "ds_read_b128 v[120:123], v49 offset:11264\n"
      "v_mfma_f32_16x16x32_bf16 v[180:183], a[188:191], v[128:131], v[180:183]\n"
      "ds_read_b128 v[124:127], v49 offset:16384\n"
      "s_waitcnt vmcnt(8)\n"
      "v_mfma_f32_16x16x32_bf16 v[184:187], a[192:195], v[128:131], v[184:187]\n"
      "v_add3_u32 v2, v136, v137, s30\n"
      "v_add3_u32 v2, v2, v138, v139\n"
      "v_add3_u32 v2, v2, v140, v141\n"
      "s_waitcnt lgkmcnt(4)\n"
      "v_mfma_f32_16x16x32_bf16 v[188:191], a[196:199], v[128:131], v[188:191]\n"
      "v_add3_u32 v2, v2, v142, v143\n"
      "v_add3_u32 v2, v2, v144, v145\n"
      "v_add3_u32 v2, v2, v146, v147\n"
      "v_add_f32 v96, v96, v100\n"
      "v_add_f32 v97, v97, v101\n"
      "v_mfma_f32_16x16x32_bf16 v[192:195], a[200:203], v[128:131], v[192:195]\n"
      "v_xor_b32 v2, v2, v24\n"
      "v_add3_u32 v3, v148, v149, s30\n"
      "v_add3_u32 v3, v3, v150, v151\n"
      "v_add_f32 v98, v98, v102\n"
      "v_add_f32 v99, v99, v103\n"
      "v_mfma_f32_16x16x32_bf16 v[176:179], a[204:207], v[132:135], v[176:179]\n"
      "v_add3_u32 v3, v3, v152, v153\n"
      "v_add3_u32 v3, v3, v154, v155\n"
      "v_add3_u32 v3, v3, v156, v157\n"
      "v_add_f32 v104, v104, v108\n"
      "v_add_f32 v105, v105, v109\n"
      "v_mfma_f32_16x16x32_bf16 v[180:183], a[208:211], v[132:135], v[180:183]\n"
      "v_add3_u32 v3, v3, v158, v159\n"
      "v_xor_b32 v3, v3, v25\n"
      "v_or_b32 v2, v2, v3\n"
      "v_add_f32 v106, v106, v110\n"
      "v_add_f32 v107, v107, v111\n"
      "v_mfma_f32_16x16x32_bf16 v[184:187], a[212:215], v[132:135], v[184:187]\n"
      "v_cmp_ne_u32 vcc, 0, v2\n"
      "v_add_f32 v96, v96, v104\n"
      "v_add_f32 v97, v97, v105\n"
      "v_mfma_f32_16x16x32_bf16 v[188:191], a[216:219], v[132:135], v[188:191]\n"
      "v_add_f32 v98, v98, v106\n"
      "v_add_f32 v99, v99, v107\n"
      "v_mfma_f32_16x16x32_bf16 v[192:195], a[220:223], v[132:135], v[192:195]\n"
      "s_cbranch_vccnz L_shB2_%=\n"
      "L_bhB2_%=:\n"
      "v_cvt_pk_bf16_f32 v96, 0, v96\n"
      "v_mfma_f32_16x16x32_bf16 v[200:203], a[240:243], v[136:139], 0\n"
      "v_cvt_pk_bf16_f32 v97, 0, v97\n"
      "v_cvt_pk_bf16_f32 v98, 0, v98\n"
      "v_mfma_f32_16x16x32_bf16 v[204:207], a[244:247], v[136:139], 0\n"
      "v_cvt_pk_bf16_f32 v99, 0, v99\n"
      "v_mul_f32 v96, v96, v80\n"
      "v_mfma_f32_16x16x32_bf16 v[208:211], a[248:251], v[136:139], 0\n"
      "v_mul_f32 v97, v97, v81\n"
      "v_mul_f32 v98, v98, v82\n"
      "v_mfma_f32_16x16x32_bf16 v[212:215], a[252:255], v[136:139], 0\n"
      "v_mul_f32 v99, v99, v83\n"
      "v_cvt_pk_bf16_f32 v96, 0, v96\n"
      "v_mfma_f32_16x16x32_bf16 v[216:219], a[0:3], v[136:139], 0\n"
      "v_cvt_pk_bf16_f32 v97, 0, v97\n"
      "v_cvt_pk_bf16_f32 v98, 0, v98\n"
      "v_mfma_f32_16x16x32_bf16 v[200:203], a[4:7], v[140:143], v[200:203]\n"
      "v_cvt_pk_bf16_f32 v99, 0, v99\n"
      "v_lshlrev_b32 v100, 16, v72\n"
      "v_mfma_f32_16x16x32_bf16 v[204:207], a[8:11], v[140:143], v[204:207]\n"
      "v_and_b32 v101, 0xffff0000, v72\n"
      "v_lshlrev_b32 v102, 16, v73\n"
      "v_mfma_f32_16x16x32_bf16 v[208:211], a[12:15], v[140:143], v[208:211]\n"
      "v_and_b32 v103, 0xffff0000, v73\n"
      "v_add_f32 v96, v96, v100\n"
      "v_mfma_f32_16x16x32_bf16 v[212:215], a[16:19], v[140:143], v[212:215]\n"
      "v_add_f32 v97, v97, v101\n"
      "v_add_f32 v98, v98, v102\n"
      "v_mfma_f32_16x16x32_bf16 v[216:219], a[20:23], v[140:143], v[216:219]\n"
      "v_add_f32 v99, v99, v103\n"
      "v_cvt_pk_bf16_f32 v72, v96, v97\n"
      "v_mfma_f32_16x16x32_bf16 v[200:203], a[24:27], v[144:147], v[200:203]\n"
      "ds_write_b128 v56, v[176:179] offset:0\n"
      "v_cvt_pk_bf16_f32 v73, v98, v99\n"
      "v_mfma_f32_16x16x32_bf16 v[204:207], a[28:31], v[144:147], v[204:207]\n"
      "global_store_dwordx2 v55, v[72:73], s[28:29] offset:0\n"
      "s_waitcnt lgkmcnt(1)\n"
      "v_mfma_f32_16x16x32_bf16 v[208:211], a[32:35], v[144:147], v[208:211]\n"
      "ds_read_b128 v[96:99], v49 offset:2048\n"
      "v_add_f32 v112, v112, v116\n"
      "v_mfma_f32_16x16x32_bf16 v[212:215], a[36:39], v[144:147], v[212:215]\n"
      "ds_read_b128 v[100:103], v49 offset:7168\n"
      "v_add_f32 v113, v113, v117\n"
      "v_mfma_f32_16x16x32_bf16 v[216:219], a[40:43], v[144:147], v[216:219]\n"
      "ds_read_b128 v[104:107], v49 offset:12288\n"
      "v_add_f32 v114, v114, v118\n"
      "v_mfma_f32_16x16x32_bf16 v[200:203], a[44:47], v[148:151], v[200:203]\n"
      "ds_read_b128 v[108:111], v49 offset:17408\n"
      "v_add_f32 v115, v115, v119\n"
      "v_mfma_f32_16x16x32_bf16 v[204:207], a[48:51], v[148:151], v[204:207]\n"
      "v_add_f32 v120, v120, v124\n"
      "v_add_f32 v121, v121, v125\n"
      "v_mfma_f32_16x16x32_bf16 v[208:211], a[52:55], v[148:151], v[208:211]\n"
      "v_add_f32 v122, v122, v126\n"
      "v_add_f32 v123, v123, v127\n"
      "v_mfma_f32_16x16x32_bf16 v[212:215], a[56:59], v[148:151], v[212:215]\n"
      "v_add_f32 v112, v112, v120\n"
      "v_add_f32 v113, v113, v121\n"
      "v_mfma_f32_16x16x32_bf16 v[216:219], a[60:63], v[148:151], v[216:219]\n"
      "v_add_f32 v114, v114, v122\n"
      "v_add_f32 v115, v115, v123\n"
      "v_mfma_f32_16x16x32_bf16 v[200:203], a[64:67], v[152:155], v[200:203]\n"
      "v_cvt_pk_bf16_f32 v112, 0, v112\n"
      "v_cvt_pk_bf16_f32 v113, 0, v113\n"
      "v_mfma_f32_16x16x32_bf16 v[204:207], a[68:71], v[152:155], v[204:207]\n"
      "s_waitcnt vmcnt(1)\n"
      "v_cvt_pk_bf16_f32 v114, 0, v114\n"
      "v_mfma_f32_16x16x32_bf16 v[208:211], a[72:75], v[152:155], v[208:211]\n"
      "v_add3_u32 v2, v172, v173, s30\n"
      "v_add3_u32 v2, v2, v174, v175\n"
      "v_add3_u32 v2, v2, v160, v161\n"
      "v_cvt_pk_bf16_f32 v115, 0, v115\n"
      "v_mul_f32 v112, v112, v84\n"
      "v_mfma_f32_16x16x32_bf16 v[212:215], a[76:79], v[152:155], v[212:215]\n"
      "v_add3_u32 v2, v2, v162, v163\n"
      "v_add3_u32 v2, v2, v164, v165\n"
      "v_add3_u32 v2, v2, v166, v167\n"
      "v_mul_f32 v113, v113, v85\n"
      "v_mul_f32 v114, v114, v86\n"
      "v_mfma_f32_16x16x32_bf16 v[216:219], a[80:83], v[152:155], v[216:219]\n"
      "v_xor_b32 v2, v2, v26\n"
      "v_add3_u32 v3, v168, v169, s30\n"
      "v_add3_u32 v3, v3, v170, v171\n"
      "v_mul_f32 v115, v115, v87\n"
      "v_cvt_pk_bf16_f32 v112, 0, v112\n"
      "v_mfma_f32_16x16x32_bf16 v[200:203], a[84:87], v[156:159], v[200:203]\n"
      "ds_write_b128 v56, v[180:183] offset:1024\n"
      "v_add3_u32 v3, v3, v64, v65\n"
      "v_add3_u32 v3, v3, v66, v67\n"
      "v_add3_u32 v3, v3, v68, v69\n"
      "v_cvt_pk_bf16_f32 v113, 0, v113\n"
      "v_mfma_f32_16x16x32_bf16 v[204:207], a[88:91], v[156:159], v[204:207]\n"
      "v_add3_u32 v3, v3, v70, v71\n"
      "v_xor_b32 v3, v3, v27\n"
      "v_or_b32 v2, v2, v3\n"
      "v_cvt_pk_bf16_f32 v114, 0, v114\n"
      "v_cvt_pk_bf16_f32 v115, 0, v115\n"
      "v_mfma_f32_16x16x32_bf16 v[208:211], a[92:95], v[156:159], v[208:211]\n"
      "v_cmp_ne_u32 vcc, 0, v2\n"
      "v_lshlrev_b32 v116, 16, v74\n"
      "v_and_b32 v117, 0xffff0000, v74\n"
      "v_mfma_f32_16x16x32_bf16 v[212:215], a[96:99], v[156:159], v[212:215]\n"
      "v_lshlrev_b32 v118, 16, v75\n"
      "v_and_b32 v119, 0xffff0000, v75\n"
      "v_mfma_f32_16x16x32_bf16 v[216:219], a[100:103], v[156:159], v[216:219]\n"
      "ds_write_b128 v56, v[184:187] offset:2048\n"
      "s_cbranch_vccnz L_shB3_%=\n"
      "L_bhB3_%=:\n"
      "v_add_f32 v112, v112, v116\n"
      "v_add_f32 v113, v113, v117\n"
      "v_add_f32 v114, v114, v118\n"
      "v_mfma_f32_16x16x32_bf16 v[200:203], a[104:107], v[172:175], v[200:203]\n"
      "v_add_f32 v115, v115, v119\n"
      "v_cvt_pk_bf16_f32 v74, v112, v113\n"
      "v_mfma_f32_16x16x32_bf16 v[204:207], a[108:111], v[172:175], v[204:207]\n"
      "v_cvt_pk_bf16_f32 v75, v114, v115\n"
      "v_mfma_f32_16x16x32_bf16 v[208:211], a[112:115], v[172:175], v[208:211]\n"
      "global_store_dwordx2 v55, v[74:75], s[28:29] offset:32\n"
      "s_waitcnt lgkmcnt(2)\n"
      "v_mfma_f32_16x16x32_bf16 v[212:215], a[116:119], v[172:175], v[212:215]\n"
      "ds_write_b128 v56, v[188:191] offset:3072\n"
      "v_add_f32 v96, v96, v100\n"
      "v_add_f32 v97, v97, v101\n"
      "v_add_f32 v98, v98, v102\n"
      "v_add_f32 v99, v99, v103\n"
      "v_mfma_f32_16x16x32_bf16 v[216:219], a[120:123], v[172:175], v[216:219]\n"
      "v_add_f32 v104, v104, v108\n"
      "v_add_f32 v105, v105, v109\n"
      "v_mfma_f32_16x16x32_bf16 v[200:203], a[124:127], v[160:163], v[200:203]\n"
      "v_add_f32 v106, v106, v110\n"
      "v_add_f32 v107, v107, v111\n"
      "v_mfma_f32_16x16x32_bf16 v[204:207], a[128:131], v[160:163], v[204:207]\n"
      "v_add_f32 v96, v96, v104\n"
      "v_add_f32 v97, v97, v105\n"
      "v_mfma_f32_16x16x32_bf16 v[208:211], a[132:135], v[160:163], v[208:211]\n"
      "ds_write_b128 v56, v[192:195] offset:4096\n"
      "v_add_f32 v98, v98, v106\n"
      "v_add_f32 v99, v99, v107\n"
      "v_cvt_pk_bf16_f32 v96, 0, v96\n"
      "v_cvt_pk_bf16_f32 v97, 0, v97\n"
      "v_mfma_f32_16x16x32_bf16 v[212:215], a[136:139], v[160:163], v[212:215]\n"
      "v_cvt_pk_bf16_f32 v98, 0, v98\n"
      "v_cvt_pk_bf16_f32 v99, 0, v99\n"
      "v_mfma_f32_16x16x32_bf16 v[216:219], a[140:143], v[160:163], v[216:219]\n"
      "v_mul_f32 v96, v96, v88\n"
      "v_mul_f32 v97, v97, v89\n"
      "v_mfma_f32_16x16x32_bf16 v[200:203], a[144:147], v[164:167], v[200:203]\n"
      "v_mul_f32 v98, v98, v90\n"
      "v_mul_f32 v99, v99, v91\n"
      "v_mfma_f32_16x16x32_bf16 v[204:207], a[148:151], v[164:167], v[204:207]\n"
      "v_cvt_pk_bf16_f32 v96, 0, v96\n"
      "v_cvt_pk_bf16_f32 v97, 0, v97\n"
      "v_mfma_f32_16x16x32_bf16 v[208:211], a[152:155], v[164:167], v[208:211]\n"
      "v_cvt_pk_bf16_f32 v98, 0, v98\n"
      "v_cvt_pk_bf16_f32 v99, 0, v99\n"
      "v_mfma_f32_16x16x32_bf16 v[212:215], a[156:159], v[164:167], v[212:215]\n"
      "v_lshlrev_b32 v100, 16, v76\n"
      "v_and_b32 v101, 0xffff0000, v76\n"
      "v_mfma_f32_16x16x32_bf16 v[216:219], a[160:163], v[164:167], v[216:219]\n"
      "v_lshlrev_b32 v102, 16, v77\n"
      "v_and_b32 v103, 0xffff0000, v77\n"
      "v_mfma_f32_16x16x32_bf16 v[200:203], a[164:167], v[168:171], v[200:203]\n"
      "v_add_f32 v96, v96, v100\n"
      "v_add_f32 v97, v97, v101\n"
      "v_mfma_f32_16x16x32_bf16 v[200:203], a[184:187], v[64:67], v[200:203]\n"
      "v_add_f32 v98, v98, v102\n"
      "v_add_f32 v99, v99, v103\n"
      "v_mfma_f32_16x16x32_bf16 v[200:203], a[204:207], v[68:71], v[200:203]\n"
      "v_cvt_pk_bf16_f32 v76, v96, v97\n"
      "v_cvt_pk_bf16_f32 v77, v98, v99\n"
      "v_mfma_f32_16x16x32_bf16 v[204:207], a[168:171], v[168:171], v[204:207]\n"
      "global_store_dwordx2 v55, v[76:77], s[28:29] offset:64\n"
      "v_mfma_f32_16x16x32_bf16 v[204:207], a[188:191], v[64:67], v[204:207]\n"
      "v_mfma_f32_16x16x32_bf16 v[204:207], a[208:211], v[68:71], v[204:207]\n"
      "ds_write_b128 v56, v[200:203] offset:20480\n"
      "v_mfma_f32_16x16x32_bf16 v[208:211], a[172:175], v[168:171], v[208:211]\n"
      "v_mfma_f32_16x16x32_bf16 v[208:211], a[192:195], v[64:67], v[208:211]\n"
      "v_mfma_f32_16x16x32_bf16 v[208:211], a[212:215], v[68:71], v[208:211]\n"
      "ds_write_b128 v56, v[204:207] offset:21504\n"
      "v_mfma_f32_16x16x32_bf16 v[212:215], a[176:179], v[168:171], v[212:215]\n"
      "v_mfma_f32_16x16x32_bf16 v[212:215], a[196:199], v[64:67], v[212:215]\n"
      "v_mfma_f32_16x16x32_bf16 v[212:215], a[216:219], v[68:71], v[212:215]\n"
      "ds_write_b128 v56, v[208:211] offset:22528\n"
      "v_mfma_f32_16x16x32_bf16 v[216:219], a[180:183], v[168:171], v[216:219]\n"
      "v_mfma_f32_16x16x32_bf16 v[216:219], a[200:203], v[64:67], v[216:219]\n"
      "v_mfma_f32_16x16x32_bf16 v[216:219], a[220:223], v[68:71], v[216:219]\n"
      "ds_write_b128 v56, v[212:215] offset:23552\n"
      "v_xor_b32 v53, 64, v53\n"
      "ds_read_b64_tr_b16 v[64:65], v53 offset:0\n"
      "ds_read_b64_tr_b16 v[66:67], v53 offset:2048\n"
      "ds_read_b64_tr_b16 v[68:69], v53 offset:4096\n"
      "v_add_u32 v49, 0xa000, v49\n"
      "v_add_u32 v55, 81920, v55\n"
      "s_nop 0\n"
      "ds_write_b128 v56, v[216:219] offset:24576\n"
      "s_waitcnt lgkmcnt(0)\n"
      "s_barrier\n"
      "ds_read_b128 v[96:99], v49 offset:0\n"
      "ds_read_b128 v[100:103], v49 offset:5120\n"
      "ds_read_b128 v[104:107], v49 offset:10240\n"
      "ds_read_b128 v[108:111], v49 offset:15360\n"
      "ds_read_b128 v[112:115], v49 offset:1024\n"
      "ds_read_b128 v[116:119], v49 offset:6144\n"
      "ds_read_b128 v[120:123], v49 offset:11264\n"
      "ds_read_b128 v[124:127], v49 offset:16384\n"
      "ds_read_b128 v[128:131], v49 offset:2048\n"
      "ds_read_b128 v[132:135], v49 offset:7168\n"
      "ds_read_b128 v[136:139], v49 offset:12288\n"
      "ds_read_b128 v[140:143], v49 offset:17408\n"
      "v_lshlrev_b32 v144, 16, v64\n"
      "v_and_b32 v145, 0xffff0000, v64\n"
      "v_lshlrev_b32 v146, 16, v65\n"
      "v_and_b32 v147, 0xffff0000, v65\n"
      "v_lshlrev_b32 v148, 16, v66\n"
      "v_and_b32 v149, 0xffff0000, v66\n"
      "v_lshlrev_b32 v150, 16, v67\n"
      "v_and_b32 v151, 0xffff0000, v67\n"
      "v_lshlrev_b32 v152, 16, v68\n"
      "v_and_b32 v153, 0xffff0000, v68\n"
      "v_lshlrev_b32 v154, 16, v69\n"
      "v_and_b32 v155, 0xffff0000, v69\n"
      "s_waitcnt lgkmcnt(8)\n"
      "v_pk_add_f32 v[96:97], v[96:97], v[100:101]\n"
      "v_pk_add_f32 v[98:99], v[98:99], v[102:103]\n"
      "v_pk_add_f32 v[104:105], v[104:105], v[108:109]\n"
      "v_pk_add_f32 v[106:107], v[106:107], v[110:111]\n"
      "v_pk_add_f32 v[96:97], v[96:97], v[104:105]\n"
      "v_pk_add_f32 v[98:99], v[98:99], v[106:107]\n"
      "v_cvt_pk_bf16_f32 v96, 0, v96\n"
      "v_cvt_pk_bf16_f32 v97, 0, v97\n"
      "v_cvt_pk_bf16_f32 v98, 0, v98\n"
      "v_cvt_pk_bf16_f32 v99, 0, v99\n"
      "v_pk_mul_f32 v[96:97], v[96:97], v[80:81]\n"
      "v_pk_mul_f32 v[98:99], v[98:99], v[82:83]\n"
      "v_cvt_pk_bf16_f32 v96, 0, v96\n"
      "v_cvt_pk_bf16_f32 v97, 0, v97\n"
      "v_cvt_pk_bf16_f32 v98, 0, v98\n"
      "v_cvt_pk_bf16_f32 v99, 0, v99\n"
      "v_pk_add_f32 v[96:97], v[96:97], v[144:145]\n"
      "v_pk_add_f32 v[98:99], v[98:99], v[146:147]\n"
      "v_cvt_pk_bf16_f32 v96, v96, v97\n"
      "v_cvt_pk_bf16_f32 v97, v98, v99\n"
      "global_store_dwordx2 v55, v[96:97], s[28:29] offset:0\n"
      "s_waitcnt lgkmcnt(4)\n"
      "v_pk_add_f32 v[112:113], v[112:113], v[116:117]\n"
      "v_pk_add_f32 v[114:115], v[114:115], v[118:119]\n"
      "v_pk_add_f32 v[120:121], v[120:121], v[124:125]\n"
      "v_pk_add_f32 v[122:123], v[122:123], v[126:127]\n"
      "v_pk_add_f32 v[112:113], v[112:113], v[120:121]\n"
      "v_pk_add_f32 v[114:115], v[114:115], v[122:123]\n"
      "v_cvt_pk_bf16_f32 v112, 0, v112\n"
      "v_cvt_pk_bf16_f32 v113, 0, v113\n"
      "v_cvt_pk_bf16_f32 v114, 0, v114\n"
      "v_cvt_pk_bf16_f32 v115, 0, v115\n"
      "v_pk_mul_f32 v[112:113], v[112:113], v[84:85]\n"
      "v_pk_mul_f32 v[114:115], v[114:115], v[86:87]\n"
      "v_cvt_pk_bf16_f32 v112, 0, v112\n"
      "v_cvt_pk_bf16_f32 v113, 0, v113\n"
      "v_cvt_pk_bf16_f32 v114, 0, v114\n"
      "v_cvt_pk_bf16_f32 v115, 0, v115\n"
      "v_pk_add_f32 v[112:113], v[112:113], v[148:149]\n"
      "v_pk_add_f32 v[114:115], v[114:115], v[150:151]\n"
      "v_cvt_pk_bf16_f32 v112, v112, v113\n"
      "v_cvt_pk_bf16_f32 v113, v114, v115\n"
      "global_store_dwordx2 v55, v[112:113], s[28:29] offset:32\n"
      "s_waitcnt lgkmcnt(0)\n"
      "v_pk_add_f32 v[128:129], v[128:129], v[132:133]\n"
      "v_pk_add_f32 v[130:131], v[130:131], v[134:135]\n"
      "v_pk_add_f32 v[136:137], v[136:137], v[140:141]\n"
      "v_pk_add_f32 v[138:139], v[138:139], v[142:143]\n"
      "v_pk_add_f32 v[128:129], v[128:129], v[136:137]\n"
      "v_pk_add_f32 v[130:131], v[130:131], v[138:139]\n"
      "v_cvt_pk_bf16_f32 v128, 0, v128\n"
      "v_cvt_pk_bf16_f32 v129, 0, v129\n"
      "v_cvt_pk_bf16_f32 v130, 0, v130\n"
      "v_cvt_pk_bf16_f32 v131, 0, v131\n"
      "v_pk_mul_f32 v[128:129], v[128:129], v[88:89]\n"
      "v_pk_mul_f32 v[130:131], v[130:131], v[90:91]\n"
      "v_cvt_pk_bf16_f32 v128, 0, v128\n"
      "v_cvt_pk_bf16_f32 v129, 0, v129\n"
      "v_cvt_pk_bf16_f32 v130, 0, v130\n"
      "v_cvt_pk_bf16_f32 v131, 0, v131\n"
      "v_pk_add_f32 v[128:129], v[128:129], v[152:153]\n"
      "v_pk_add_f32 v[130:131], v[130:131], v[154:155]\n"
      "v_cvt_pk_bf16_f32 v128, v128, v129\n"
      "v_cvt_pk_bf16_f32 v129, v130, v131\n"
      "global_store_dwordx2 v55, v[128:129], s[28:29] offset:64\n"
      "s_or_b32 s40, s2, s60\n"
      "s_cmp_lg_u32 s40, 0\n"
      "s_cbranch_scc1 L_fin%=\n"
      "v_mov_b32 v8, s30\n"
      "v_mov_b32 v9, s31\n"
      "v_mov_b32 v10, 0\n"
      "s_mov_b64 exec, 1\n"
      "global_store_dwordx2 v10, v[8:9], s[26:27] sc1\n"
      "s_mov_b64 exec, -1\n"
      "L_fin%=:\n"
      "s_endpgm\n"
      "L_psx_%=:\n"
      "s_lshr_b32 s42, s92, 12\n"
      "s_lshr_b32 s43, s93, 12\n"
      "s_mulk_i32 s42, 4224\n"
      "s_mulk_i32 s43, 4224\n"
      "s_add_u32 s42, s42, 0x100000\n"
      "s_add_u32 s43, s43, 0x100000\n"
      "buffer_load_dwordx4 v[128:131], v54, s[32:35], s42 offen sc1\n"
      "s_add_u32 s42, s42, 1081344\n"
      "buffer_load_dwordx4 v[136:139], v54, s[32:35], s43 offen sc1\n"
      "buffer_load_dwordx4 v[132:135], v54, s[32:35], s42 offen sc1\n"
      "buffer_load_dword v140, v237, s[32:35], s70 offen sc1\n"
      "buffer_load_dword v141, v237, s[32:35], s98 offen sc1\n"
      "buffer_load_dword v142, v237, s[32:35], s71 offen sc1\n"
      "s_waitcnt vmcnt(0)\n"
      "v_add3_u32 v143, v128, v129, v130\n"
      "v_add3_u32 v143, v143, v131, v132\n"
      "v_add3_u32 v143, v143, v133, v134\n"
      "v_add3_u32 v143, v143, v135, v136\n"
      "v_add3_u32 v143, v143, v137, v138\n"
      "v_add_u32 v143, v143, v139\n"
      "v_add3_u32 v140, v140, v141, v142\n"
      "s_mul_i32 s47, s30, 3\n"
      "v_add_u32_dpp v143, v143, v143 quad_perm:[1,0,3,2] row_mask:0xf bank_mask:0xf\n"
      "s_nop 1\n"
      "v_add_u32_dpp v143, v143, v143 quad_perm:[2,3,0,1] row_mask:0xf bank_mask:0xf\n"
      "s_nop 1\n"
      "v_add_u32_dpp v143, v143, v143 row_half_mirror row_mask:0xf bank_mask:0xf\n"
      "v_add_u32 v143, v143, v140\n"
      "v_cmp_ne_u32 vcc, s47, v143\n"
      "s_sub_u32 s44, s44, 1\n"
      "s_cbranch_vccz L_pbx_%=\n"
      "s_cmp_lg_u32 s44, 0\n"
      "s_cbranch_scc1 L_psx_%=\n"
      "s_branch L_pbx_%=\n"
      "L_cv_s%=:\n"
      "buffer_load_dwordx2 v[200:201], v4, s[32:35], s80 offen offset:0 sc1\n"
      "buffer_load_dword v250, v61, s[32:35], s85 offen offset:0 sc1\n"
      "buffer_load_dwordx4 v[224:227], v57, s[32:35], s82 offen offset:0 sc1\n"
      "buffer_load_dwordx4 v[228:231], v57, s[32:35], s82 offen offset:1024 sc1\n"
      "buffer_load_dwordx4 v[192:195], v54, s[32:35], s53 offen offset:0 sc1\n"
      "buffer_load_dwordx4 v[196:199], v54, s[32:35], s54 offen offset:0 sc1\n"
      "buffer_load_dword v248, v60, s[32:35], s84 offen offset:0 sc1\n"
      "buffer_load_dword v249, v60, s[32:35], s84 offen offset:2048 sc1\n"
      "buffer_load_dwordx4 v[208:211], v55, s[32:35], 0 offen offset:0 sc1\n"
      "buffer_load_dwordx4 v[212:215], v55, s[32:35], 0 offen offset:1024 sc1\n"
      "buffer_load_dwordx4 v[216:219], v55, s[32:35], 0 offen offset:2048 sc1\n"
      "buffer_load_dwordx4 v[220:223], v55, s[32:35], 0 offen offset:3072 sc1\n"
      "buffer_load_dwordx2 v[204:205], v58, s[32:35], s83 offen offset:0 sc1\n"
      "s_waitcnt vmcnt(0)\n"
      "v_add_u32 v251, v200, v201\n"
      "v_add3_u32 v34, v192, v193, v194\n"
      "v_add3_u32 v34, v34, v195, v196\n"
      "v_add_u32_dpp v251, v251, v251 row_half_mirror row_mask:0xf bank_mask:0xf\n"
      "v_add3_u32 v34, v34, v197, v198\n"
      "v_add3_u32 v251, v251, v34, v199\n"
      "v_add3_u32 v34, v248, v249, v250\n"
      "v_add3_u32 v34, v34, v211, v215\n"
      "v_add_u32_dpp v251, v251, v251 quad_perm:[1,0,3,2] row_mask:0xf bank_mask:0xf\n"
      "v_add3_u32 v34, v34, v219, v223\n"
      "v_add3_u32 v34, v34, v227, v231\n"
      "v_add_u32_dpp v251, v251, v251 quad_perm:[2,3,0,1] row_mask:0xf bank_mask:0xf\n"
      "v_add3_u32 v251, v251, v34, v205\n"
      "v_cmp_ne_u32 vcc, s24, v251\n"
      "s_sub_u32 s44, s44, 1\n"
      "s_cbranch_vccz L_cv_r%=\n"
      "s_cmp_lg_u32 s44, 0\n"
      "s_cbranch_scc1 L_cv_s%=\n"
      "s_branch L_cv_r%=\n"
      "L_cw_s%=:\n"
      "buffer_load_dwordx2 v[252:253], v51, s[32:35], s76 offen offset:0 sc1\n"
      "s_waitcnt vmcnt(0)\n"
      "v_cmp_ne_u64 vcc, s[30:31], v[252:253]\n"
      "s_sub_u32 s44, s44, 1\n"
      "s_cbranch_vccz L_cw_r%=\n"
      "s_cmp_lg_u32 s44, 0\n"
      "s_cbranch_scc1 L_cw_s%=\n"
      "s_branch L_cw_r%=\n"
      "L_cm_s%=:\n"
      "buffer_load_dwordx2 v[254:255], v2, s[32:35], s45 offen sc1\n"
      "s_waitcnt vmcnt(0)\n"
      "v_cmp_ne_u32 vcc, s30, v255\n"
      "s_sub_u32 s44, s44, 1\n"
      "s_cbranch_vccz L_cm_r%=\n"
      "s_cmp_lg_u32 s44, 0\n"
      "s_cbranch_scc1 L_cm_s%=\n"
      "s_branch L_cm_r%=\n"
      "L_sx1_%=:\n"
      "s_mov_b32 s45, m0\n"
      "s_mov_b32 s58, s47\n"
      "s_add_u32 m0, s73, 16384\n"
      "v_add_u32 v248, 81920, v40\n"
      "global_load_lds_dwordx4 v248, s[4:5] offset:0 sc1\n"
      "global_load_lds_dwordx4 v248, s[4:5] offset:1024 sc1\n"
      "global_load_lds_dwordx4 v248, s[4:5] offset:2048 sc1\n"
      "global_load_lds_dwordx4 v248, s[4:5] offset:3072 sc1\n"
      "s_mov_b32 s47, 4\n"
      "buffer_load_dword v3, v5, s[32:35], s47 offen sc1\n"
      "s_waitcnt vmcnt(0)\n"
      "s_mov_b32 m0, s45\n"
      "s_mov_b32 s47, s58\n"
      "ds_read_b128 v[248:251], v40 offset:16384\n"
      "ds_read_b128 v[252:255], v40 offset:17408\n"
      "ds_read_b128 v[36:39], v40 offset:18432\n"
      "ds_read_b128 v[60:63], v40 offset:19456\n"
      "s_waitcnt lgkmcnt(0)\n"
      "v_add3_u32 v3, v3, v248, v249\n"
      "v_add3_u32 v3, v3, v250, v251\n"
      "v_add3_u32 v3, v3, v252, v253\n"
      "v_add3_u32 v3, v3, v254, v255\n"
      "v_add3_u32 v3, v3, v36, v37\n"
      "v_add3_u32 v3, v3, v38, v39\n"
      "v_add3_u32 v3, v3, v60, v61\n"
      "v_add3_u32 v3, v3, v62, v63\n"
      "s_nop 1\n"
      "v_add_u32_dpp v3, v3, v3 quad_perm:[1,0,3,2] row_mask:0xf bank_mask:0xf\n"
      "s_nop 1\n"
      "v_add_u32_dpp v3, v3, v3 quad_perm:[2,3,0,1] row_mask:0xf bank_mask:0xf\n"
      "s_nop 1\n"
      "v_add_u32_dpp v3, v3, v3 row_half_mirror row_mask:0xf bank_mask:0xf\n"
      "v_cmp_ne_u32 vcc, s71, v3\n"
      "s_sub_u32 s44, s44, 1\n"
      "s_cbranch_vccz L_bx1_%=\n"
      "s_cmp_lg_u32 s44, 0\n"
      "s_cbranch_scc1 L_sx1_%=\n"
      "s_branch L_bx1_%=\n"
      "L_sx2_%=:\n"
      "s_mov_b32 s45, m0\n"
      "s_mov_b32 s58, s47\n"
      "s_add_u32 m0, s73, 32768\n"
      "v_add_u32 v248, 16384, v40\n"
      "global_load_lds_dwordx4 v248, s[4:5] offset:0 sc1\n"
      "global_load_lds_dwordx4 v248, s[4:5] offset:1024 sc1\n"
      "global_load_lds_dwordx4 v248, s[4:5] offset:2048 sc1\n"
      "global_load_lds_dwordx4 v248, s[4:5] offset:3072 sc1\n"
      "s_mov_b32 s47, 32768\n"
      "buffer_load_dword v48, v5, s[32:35], s47 offen sc1\n"
      "s_waitcnt vmcnt(0)\n"
      "s_mov_b32 m0, s45\n"
      "s_mov_b32 s47, s58\n"
      "ds_read_b128 v[248:251], v40 offset:32768\n"
      "ds_read_b128 v[252:255], v40 offset:33792\n"
      "ds_read_b128 v[36:39], v40 offset:34816\n"
      "ds_read_b128 v[60:63], v40 offset:35840\n"
      "s_waitcnt lgkmcnt(0)\n"
      "v_add3_u32 v48, v48, v248, v249\n"
      "v_add3_u32 v48, v48, v250, v251\n"
      "v_add3_u32 v48, v48, v252, v253\n"
      "v_add3_u32 v48, v48, v254, v255\n"
      "v_add3_u32 v48, v48, v36, v37\n"
      "v_add3_u32 v48, v48, v38, v39\n"
      "v_add3_u32 v48, v48, v60, v61\n"
      "v_add3_u32 v48, v48, v62, v63\n"
      "s_nop 1\n"
      "v_add_u32_dpp v48, v48, v48 quad_perm:[1,0,3,2] row_mask:0xf bank_mask:0xf\n"
      "s_nop 1\n"
      "v_add_u32_dpp v48, v48, v48 quad_perm:[2,3,0,1] row_mask:0xf bank_mask:0xf\n"
      "s_nop 1\n"
      "v_add_u32_dpp v48, v48, v48 row_half_mirror row_mask:0xf bank_mask:0xf\n"
      "v_cmp_ne_u32 vcc, s71, v48\n"
      "s_sub_u32 s44, s44, 1\n"
      "s_cbranch_vccz L_bx2_%=\n"
      "s_cmp_lg_u32 s44, 0\n"
      "s_cbranch_scc1 L_sx2_%=\n"
      "s_branch L_bx2_%=\n"
      "L_sx3_%=:\n"
      "s_mov_b32 s45, m0\n"
      "s_mov_b32 s58, s47\n"
      "s_add_u32 m0, s73, 49152\n"
      "v_add_u32 v248, 32768, v40\n"
      "global_load_lds_dwordx4 v248, s[4:5] offset:0 sc1\n"
      "global_load_lds_dwordx4 v248, s[4:5] offset:1024 sc1\n"
      "global_load_lds_dwordx4 v248, s[4:5] offset:2048 sc1\n"
      "global_load_lds_dwordx4 v248, s[4:5] offset:3072 sc1\n"
      "s_mov_b32 s47, 65536\n"
      "buffer_load_dword v54, v5, s[32:35], s47 offen sc1\n"
      "s_waitcnt vmcnt(0)\n"
      "s_mov_b32 m0, s45\n"
      "s_mov_b32 s47, s58\n"
      "ds_read_b128 v[248:251], v40 offset:49152\n"
      "ds_read_b128 v[252:255], v40 offset:50176\n"
      "ds_read_b128 v[36:39], v40 offset:51200\n"
      "ds_read_b128 v[60:63], v40 offset:52224\n"
      "s_waitcnt lgkmcnt(0)\n"
      "v_add3_u32 v54, v54, v248, v249\n"
      "v_add3_u32 v54, v54, v250, v251\n"
      "v_add3_u32 v54, v54, v252, v253\n"
      "v_add3_u32 v54, v54, v254, v255\n"
      "v_add3_u32 v54, v54, v36, v37\n"
      "v_add3_u32 v54, v54, v38, v39\n"
      "v_add3_u32 v54, v54, v60, v61\n"
      "v_add3_u32 v54, v54, v62, v63\n"
      "s_nop 1\n"
      "v_add_u32_dpp v54, v54, v54 quad_perm:[1,0,3,2] row_mask:0xf bank_mask:0xf\n"
      "s_nop 1\n"
      "v_add_u32_dpp v54, v54, v54 quad_perm:[2,3,0,1] row_mask:0xf bank_mask:0xf\n"
      "s_nop 1\n"
      "v_add_u32_dpp v54, v54, v54 row_half_mirror row_mask:0xf bank_mask:0xf\n"
      "v_cmp_ne_u32 vcc, s71, v54\n"
      "s_sub_u32 s44, s44, 1\n"
      "s_cbranch_vccz L_bx3_%=\n"
      "s_cmp_lg_u32 s44, 0\n"
      "s_cbranch_scc1 L_sx3_%=\n"
      "s_branch L_bx3_%=\n"
      "L_sx4_%=:\n"
      "s_mov_b32 s45, m0\n"
      "s_mov_b32 s58, s47\n"
      "s_add_u32 m0, s73, 0\n"
      "v_add_u32 v248, 49152, v40\n"
      "global_load_lds_dwordx4 v248, s[4:5] offset:0 sc1\n"
      "global_load_lds_dwordx4 v248, s[4:5] offset:1024 sc1\n"
      "global_load_lds_dwordx4 v248, s[4:5] offset:2048 sc1\n"
      "global_load_lds_dwordx4 v248, s[4:5] offset:3072 sc1\n"
      "s_mov_b32 s47, 98304\n"
      "buffer_load_dword v56, v5, s[32:35], s47 offen sc1\n"
      "s_waitcnt vmcnt(0)\n"
      "s_mov_b32 m0, s45\n"
      "s_mov_b32 s47, s58\n"
      "ds_read_b128 v[248:251], v40 offset:0\n"
      "ds_read_b128 v[252:255], v40 offset:1024\n"
      "ds_read_b128 v[36:39], v40 offset:2048\n"
      "ds_read_b128 v[60:63], v40 offset:3072\n"
      "s_waitcnt lgkmcnt(0)\n"
      "v_add3_u32 v56, v56, v248, v249\n"
      "v_add3_u32 v56, v56, v250, v251\n"
      "v_add3_u32 v56, v56, v252, v253\n"
      "v_add3_u32 v56, v56, v254, v255\n"
      "v_add3_u32 v56, v56, v36, v37\n"
      "v_add3_u32 v56, v56, v38, v39\n"
      "v_add3_u32 v56, v56, v60, v61\n"
      "v_add3_u32 v56, v56, v62, v63\n"
      "s_nop 1\n"
      "v_add_u32_dpp v56, v56, v56 quad_perm:[1,0,3,2] row_mask:0xf bank_mask:0xf\n"
      "s_nop 1\n"
      "v_add_u32_dpp v56, v56, v56 quad_perm:[2,3,0,1] row_mask:0xf bank_mask:0xf\n"
      "s_nop 1\n"
      "v_add_u32_dpp v56, v56, v56 row_half_mirror row_mask:0xf bank_mask:0xf\n"
      "v_cmp_ne_u32 vcc, s71, v56\n"
      "s_sub_u32 s44, s44, 1\n"
      "s_cbranch_vccz L_bx4_%=\n"
      "s_cmp_lg_u32 s44, 0\n"
      "s_cbranch_scc1 L_sx4_%=\n"
      "s_branch L_bx4_%=\n"
      "L_sx5_%=:\n"
      "s_mov_b32 s45, m0\n"
      "s_mov_b32 s58, s47\n"
      "s_add_u32 m0, s73, 16384\n"
      "v_add_u32 v248, 65536, v40\n"
      "global_load_lds_dwordx4 v248, s[4:5] offset:0 sc1\n"
      "global_load_lds_dwordx4 v248, s[4:5] offset:1024 sc1\n"
      "global_load_lds_dwordx4 v248, s[4:5] offset:2048 sc1\n"
      "global_load_lds_dwordx4 v248, s[4:5] offset:3072 sc1\n"
      "s_mov_b32 s47, 131072\n"
      "buffer_load_dword v2, v5, s[32:35], s47 offen sc1\n"
      "s_waitcnt vmcnt(0)\n"
      "s_mov_b32 m0, s45\n"
      "s_mov_b32 s47, s58\n"
      "ds_read_b128 v[248:251], v40 offset:16384\n"
      "ds_read_b128 v[252:255], v40 offset:17408\n"
      "ds_read_b128 v[36:39], v40 offset:18432\n"
      "ds_read_b128 v[60:63], v40 offset:19456\n"
      "s_waitcnt lgkmcnt(0)\n"
      "v_add3_u32 v2, v2, v248, v249\n"
      "v_add3_u32 v2, v2, v250, v251\n"
      "v_add3_u32 v2, v2, v252, v253\n"
      "v_add3_u32 v2, v2, v254, v255\n"
      "v_add3_u32 v2, v2, v36, v37\n"
      "v_add3_u32 v2, v2, v38, v39\n"
      "v_add3_u32 v2, v2, v60, v61\n"
      "v_add3_u32 v2, v2, v62, v63\n"
      "s_nop 1\n"
      "v_add_u32_dpp v2, v2, v2 quad_perm:[1,0,3,2] row_mask:0xf bank_mask:0xf\n"
      "s_nop 1\n"
      "v_add_u32_dpp v2, v2, v2 quad_perm:[2,3,0,1] row_mask:0xf bank_mask:0xf\n"
      "s_nop 1\n"
      "v_add_u32_dpp v2, v2, v2 row_half_mirror row_mask:0xf bank_mask:0xf\n"
      "v_cmp_ne_u32 vcc, s71, v2\n"
      "s_sub_u32 s44, s44, 1\n"
      "s_cbranch_vccz L_bx5_%=\n"
      "s_cmp_lg_u32 s44, 0\n"
      "s_cbranch_scc1 L_sx5_%=\n"
      "s_branch L_bx5_%=\n"
      "L_sx6_%=:\n"
      "s_mov_b32 s45, m0\n"
      "s_mov_b32 s58, s47\n"
      "s_add_u32 m0, s73, 32768\n"
      "v_add_u32 v248, 98304, v40\n"
      "global_load_lds_dwordx4 v248, s[4:5] offset:0 sc1\n"
      "global_load_lds_dwordx4 v248, s[4:5] offset:1024 sc1\n"
      "global_load_lds_dwordx4 v248, s[4:5] offset:2048 sc1\n"
      "global_load_lds_dwordx4 v248, s[4:5] offset:3072 sc1\n"
      "s_mov_b32 s47, 32772\n"
      "buffer_load_dword v49, v5, s[32:35], s47 offen sc1\n"
      "s_waitcnt vmcnt(0)\n"
      "s_mov_b32 m0, s45\n"
      "s_mov_b32 s47, s58\n"
      "ds_read_b128 v[248:251], v40 offset:32768\n"
      "ds_read_b128 v[252:255], v40 offset:33792\n"
      "ds_read_b128 v[36:39], v40 offset:34816\n"
      "ds_read_b128 v[60:63], v40 offset:35840\n"
      "s_waitcnt lgkmcnt(0)\n"
      "v_add3_u32 v49, v49, v248, v249\n"
      "v_add3_u32 v49, v49, v250, v251\n"
      "v_add3_u32 v49, v49, v252, v253\n"
      "v_add3_u32 v49, v49, v254, v255\n"
      "v_add3_u32 v49, v49, v36, v37\n"
      "v_add3_u32 v49, v49, v38, v39\n"
      "v_add3_u32 v49, v49, v60, v61\n"
      "v_add3_u32 v49, v49, v62, v63\n"
      "s_nop 1\n"
      "v_add_u32_dpp v49, v49, v49 quad_perm:[1,0,3,2] row_mask:0xf bank_mask:0xf\n"
      "s_nop 1\n"
      "v_add_u32_dpp v49, v49, v49 quad_perm:[2,3,0,1] row_mask:0xf bank_mask:0xf\n"
      "s_nop 1\n"
      "v_add_u32_dpp v49, v49, v49 row_half_mirror row_mask:0xf bank_mask:0xf\n"
      "v_cmp_ne_u32 vcc, s71, v49\n"
      "s_sub_u32 s44, s44, 1\n"
      "s_cbranch_vccz L_bx6_%=\n"
      "s_cmp_lg_u32 s44, 0\n"
      "s_cbranch_scc1 L_sx6_%=\n"
      "s_branch L_bx6_%=\n"
      "L_sx7_%=:\n"
      "s_mov_b32 s45, m0\n"
      "s_mov_b32 s58, s47\n"
      "s_add_u32 m0, s73, 49152\n"
      "v_add_u32 v248, 114688, v40\n"
      "global_load_lds_dwordx4 v248, s[4:5] offset:0 sc1\n"
      "global_load_lds_dwordx4 v248, s[4:5] offset:1024 sc1\n"
      "global_load_lds_dwordx4 v248, s[4:5] offset:2048 sc1\n"
      "global_load_lds_dwordx4 v248, s[4:5] offset:3072 sc1\n"
      "s_mov_b32 s47, 65540\n"
      "buffer_load_dword v55, v5, s[32:35], s47 offen sc1\n"
      "s_waitcnt vmcnt(0)\n"
      "s_mov_b32 m0, s45\n"
      "s_mov_b32 s47, s58\n"
      "ds_read_b128 v[248:251], v40 offset:49152\n"
      "ds_read_b128 v[252:255], v40 offset:50176\n"
      "ds_read_b128 v[36:39], v40 offset:51200\n"
      "ds_read_b128 v[60:63], v40 offset:52224\n"
      "s_waitcnt lgkmcnt(0)\n"
      "v_add3_u32 v55, v55, v248, v249\n"
      "v_add3_u32 v55, v55, v250, v251\n"
      "v_add3_u32 v55, v55, v252, v253\n"
      "v_add3_u32 v55, v55, v254, v255\n"
      "v_add3_u32 v55, v55, v36, v37\n"
      "v_add3_u32 v55, v55, v38, v39\n"
      "v_add3_u32 v55, v55, v60, v61\n"
      "v_add3_u32 v55, v55, v62, v63\n"
      "s_nop 1\n"
      "v_add_u32_dpp v55, v55, v55 quad_perm:[1,0,3,2] row_mask:0xf bank_mask:0xf\n"
      "s_nop 1\n"
      "v_add_u32_dpp v55, v55, v55 quad_perm:[2,3,0,1] row_mask:0xf bank_mask:0xf\n"
      "s_nop 1\n"
      "v_add_u32_dpp v55, v55, v55 row_half_mirror row_mask:0xf bank_mask:0xf\n"
      "v_cmp_ne_u32 vcc, s71, v55\n"
      "s_sub_u32 s44, s44, 1\n"
      "s_cbranch_vccz L_bx7_%=\n"
      "s_cmp_lg_u32 s44, 0\n"
      "s_cbranch_scc1 L_sx7_%=\n"
      "s_branch L_bx7_%=\n"
      "L_sx8_%=:\n"
      "s_mov_b32 s45, m0\n"
      "s_mov_b32 s58, s47\n"
      "s_add_u32 m0, s73, 0\n"
      "v_add_u32 v248, 131072, v40\n"
      "global_load_lds_dwordx4 v248, s[4:5] offset:0 sc1\n"
      "global_load_lds_dwordx4 v248, s[4:5] offset:1024 sc1\n"
      "global_load_lds_dwordx4 v248, s[4:5] offset:2048 sc1\n"
      "global_load_lds_dwordx4 v248, s[4:5] offset:3072 sc1\n"
      "s_mov_b32 s47, 98308\n"
      "buffer_load_dword v57, v5, s[32:35], s47 offen sc1\n"
      "s_waitcnt vmcnt(0)\n"
      "s_mov_b32 m0, s45\n"
      "s_mov_b32 s47, s58\n"
      "ds_read_b128 v[248:251], v40 offset:0\n"
      "ds_read_b128 v[252:255], v40 offset:1024\n"
      "ds_read_b128 v[36:39], v40 offset:2048\n"
      "ds_read_b128 v[60:63], v40 offset:3072\n"
      "s_waitcnt lgkmcnt(0)\n"
      "v_add3_u32 v57, v57, v248, v249\n"
      "v_add3_u32 v57, v57, v250, v251\n"
      "v_add3_u32 v57, v57, v252, v253\n"
      "v_add3_u32 v57, v57, v254, v255\n"
      "v_add3_u32 v57, v57, v36, v37\n"
      "v_add3_u32 v57, v57, v38, v39\n"
      "v_add3_u32 v57, v57, v60, v61\n"
      "v_add3_u32 v57, v57, v62, v63\n"
      "s_nop 1\n"
      "v_add_u32_dpp v57, v57, v57 quad_perm:[1,0,3,2] row_mask:0xf bank_mask:0xf\n"
      "s_nop 1\n"
      "v_add_u32_dpp v57, v57, v57 quad_perm:[2,3,0,1] row_mask:0xf bank_mask:0xf\n"
      "s_nop 1\n"
      "v_add_u32_dpp v57, v57, v57 row_half_mirror row_mask:0xf bank_mask:0xf\n"
      "v_cmp_ne_u32 vcc, s71, v57\n"
      "s_sub_u32 s44, s44, 1\n"
      "s_cbranch_vccz L_bx8_%=\n"
      "s_cmp_lg_u32 s44, 0\n"
      "s_cbranch_scc1 L_sx8_%=\n"
      "s_branch L_bx8_%=\n"
      "L_sx9_%=:\n"
      "s_mov_b32 s45, m0\n"
      "s_mov_b32 s58, s47\n"
      "s_add_u32 m0, s73, 16384\n"
      "v_add_u32 v248, 147456, v40\n"
      "global_load_lds_dwordx4 v248, s[4:5] offset:0 sc1\n"
      "global_load_lds_dwordx4 v248, s[4:5] offset:1024 sc1\n"
      "global_load_lds_dwordx4 v248, s[4:5] offset:2048 sc1\n"
      "global_load_lds_dwordx4 v248, s[4:5] offset:3072 sc1\n"
      "s_mov_b32 s47, 131076\n"
      "buffer_load_dword v3, v5, s[32:35], s47 offen sc1\n"
      "s_waitcnt vmcnt(0)\n"
      "s_mov_b32 m0, s45\n"
      "s_mov_b32 s47, s58\n"
      "ds_read_b128 v[248:251], v40 offset:16384\n"
      "ds_read_b128 v[252:255], v40 offset:17408\n"
      "ds_read_b128 v[36:39], v40 offset:18432\n"
      "ds_read_b128 v[60:63], v40 offset:19456\n"
      "s_waitcnt lgkmcnt(0)\n"
      "v_add3_u32 v3, v3, v248, v249\n"
      "v_add3_u32 v3, v3, v250, v251\n"
      "v_add3_u32 v3, v3, v252, v253\n"
      "v_add3_u32 v3, v3, v254, v255\n"
      "v_add3_u32 v3, v3, v36, v37\n"
      "v_add3_u32 v3, v3, v38, v39\n"
      "v_add3_u32 v3, v3, v60, v61\n"
      "v_add3_u32 v3, v3, v62, v63\n"
      "s_nop 1\n"
      "v_add_u32_dpp v3, v3, v3 quad_perm:[1,0,3,2] row_mask:0xf bank_mask:0xf\n"
      "s_nop 1\n"
      "v_add_u32_dpp v3, v3, v3 quad_perm:[2,3,0,1] row_mask:0xf bank_mask:0xf\n"
      "s_nop 1\n"
      "v_add_u32_dpp v3, v3, v3 row_half_mirror row_mask:0xf bank_mask:0xf\n"
      "v_cmp_ne_u32 vcc, s71, v3\n"
      "s_sub_u32 s44, s44, 1\n"
      "s_cbranch_vccz L_bx9_%=\n"
      "s_cmp_lg_u32 s44, 0\n"
      "s_cbranch_scc1 L_sx9_%=\n"
      "s_branch L_bx9_%=\n"
      "L_sx0_%=:\n"
      "s_mov_b32 s45, m0\n"
      "s_mov_b32 s58, s47\n"
      "s_add_u32 m0, s73, 0\n"
      "v_add_u32 v248, 0, v40\n"
      "global_load_lds_dwordx4 v248, s[4:5] offset:0 sc1\n"
      "global_load_lds_dwordx4 v248, s[4:5] offset:1024 sc1\n"
      "global_load_lds_dwordx4 v248, s[4:5] offset:2048 sc1\n"
      "global_load_lds_dwordx4 v248, s[4:5] offset:3072 sc1\n"
      "s_mov_b32 s47, 0\n"
      "buffer_load_dword v2, v5, s[32:35], s47 offen sc1\n"
      "s_waitcnt vmcnt(0)\n"
      "s_mov_b32 m0, s45\n"
      "s_mov_b32 s47, s58\n"
      "ds_read_b128 v[248:251], v40 offset:0\n"
      "ds_read_b128 v[252:255], v40 offset:1024\n"
      "ds_read_b128 v[36:39], v40 offset:2048\n"
      "ds_read_b128 v[60:63], v40 offset:3072\n"
      "s_waitcnt lgkmcnt(0)\n"
      "v_add3_u32 v2, v2, v248, v249\n"
      "v_add3_u32 v2, v2, v250, v251\n"
      "v_add3_u32 v2, v2, v252, v253\n"
      "v_add3_u32 v2, v2, v254, v255\n"
      "v_add3_u32 v2, v2, v36, v37\n"
      "v_add3_u32 v2, v2, v38, v39\n"
      "v_add3_u32 v2, v2, v60, v61\n"
      "v_add3_u32 v2, v2, v62, v63\n"
      "s_nop 1\n"
      "v_add_u32_dpp v2, v2, v2 quad_perm:[1,0,3,2] row_mask:0xf bank_mask:0xf\n"
      "s_nop 1\n"
      "v_add_u32_dpp v2, v2, v2 quad_perm:[2,3,0,1] row_mask:0xf bank_mask:0xf\n"
      "s_nop 1\n"
      "v_add_u32_dpp v2, v2, v2 row_half_mirror row_mask:0xf bank_mask:0xf\n"
      "v_cmp_ne_u32 vcc, s71, v2\n"
      "s_sub_u32 s44, s44, 1\n"
      "s_cbranch_vccz L_bx0_%=\n"
      "s_cmp_lg_u32 s44, 0\n"
      "s_cbranch_scc1 L_sx0_%=\n"
      "s_branch L_bx0_%=\n"
      "L_sfd_%=:\n"
      "global_load_dwordx2 v[58:59], v42, s[26:27] sc1\n"
      "buffer_load_dwordx2 v[46:47], v51, s[32:35], s75 offen sc1\n"
      "s_waitcnt vmcnt(0)\n"
      "v_cmp_ne_u64 s[52:53], s[30:31], v[46:47]\n"
      "v_cmp_ne_u64 vcc, s[30:31], v[58:59]\n"
      "s_or_b64 vcc, vcc, s[52:53]\n"
      "s_sub_u32 s44, s44, 1\n"
      "s_cbranch_vccz L_bfd_%=\n"
      "s_cmp_lg_u32 s44, 0\n"
      "s_cbranch_scc1 L_sfd_%=\n"
      "s_branch L_bfd_%=\n"
      "L_sxp_%=:\n"
      "s_mov_b32 s45, m0\n"
      "s_mov_b32 s58, s47\n"
      "s_mov_b32 m0, s94\n"
      "s_nop 0\n"
      "buffer_load_dwordx4 v7, s[32:35], s67 offen offset:0 sc1 lds\n"
      "buffer_load_dwordx4 v7, s[32:35], s67 offen offset:1024 sc1 lds\n"
      "buffer_load_dwordx4 v7, s[32:35], s67 offen offset:2048 sc1 lds\n"
      "buffer_load_dword v32, v31, s[32:35], s68 offen sc1\n"
      "s_waitcnt vmcnt(0)\n"
      "s_mov_b32 m0, s45\n"
      "s_mov_b32 s47, s58\n"
      "ds_read_b128 v[248:251], v50 offset:0\n"
      "ds_read_b128 v[252:255], v50 offset:1024\n"
      "ds_read_b128 v[36:39], v50 offset:2048\n"
      "s_waitcnt lgkmcnt(0)\n"
      "v_add3_u32 v32, v32, v248, v249\n"
      "v_add3_u32 v32, v32, v250, v251\n"
      "v_add3_u32 v32, v32, v252, v253\n"
      "v_add3_u32 v32, v32, v254, v255\n"
      "v_add3_u32 v32, v32, v36, v37\n"
      "v_add3_u32 v32, v32, v38, v39\n"
      "s_nop 1\n"
      "v_add_u32_dpp v32, v32, v32 quad_perm:[1,0,3,2] row_mask:0xf bank_mask:0xf\n"
      "s_nop 1\n"
      "v_add_u32_dpp v32, v32, v32 quad_perm:[2,3,0,1] row_mask:0xf bank_mask:0xf\n"
      "s_nop 1\n"
      "v_add_u32_dpp v32, v32, v32 row_half_mirror row_mask:0xf bank_mask:0xf\n"
      "v_cmp_ne_u32 vcc, s72, v32\n"
      "s_sub_u32 s44, s44, 1\n"
      "s_cbranch_vccz L_bxp_%=\n"
      "s_cmp_lg_u32 s44, 0\n"
      "s_cbranch_scc1 L_sxp_%=\n"
      "s_branch L_bxp_%=\n"
      "L_sg_%=:\n"
      "buffer_load_dwordx4 v[8:11], v50, s[32:35], s93 offen offset:0 sc1\n"
      "buffer_load_dwordx4 v[12:15], v50, s[32:35], s93 offen offset:1024 sc1\n"
      "buffer_load_dwordx4 v[16:19], v50, s[32:35], s93 offen offset:2048 sc1\n"
      "buffer_load_dwordx4 v[20:23], v50, s[32:35], s93 offen offset:3072 sc1\n"
      "s_waitcnt vmcnt(0)\n"
      "v_add3_u32 v10, v10, v14, v18\n"
      "v_add_u32 v10, v10, v22\n"
      "v_cmp_ne_u32 vcc, s71, v10\n"
      "s_sub_u32 s44, s44, 1\n"
      "s_cbranch_vccz L_bg_%=\n"
      "s_cmp_lg_u32 s44, 0\n"
      "s_cbranch_scc1 L_sg_%=\n"
      "s_branch L_bg_%=\n"
      "L_shA0_%=:\n"
      "v_add_u32 v2, 4096, v7\n"
      "global_load_dwordx4 v[8:11], v2, s[10:11] offset:-4096 sc1\n"
      "global_load_dwordx4 v[16:19], v2, s[10:11] offset:-3072 sc1\n"
      "global_load_dwordx4 v[128:131], v2, s[10:11] offset:-2048 sc1\n"
      "v_add_u32 v2, 4096, v7\n"
      "global_load_dwordx4 v[12:15], v2, s[12:13] offset:-4096 sc1\n"
      "global_load_dwordx4 v[20:23], v2, s[12:13] offset:-3072 sc1\n"
      "global_load_dwordx4 v[132:135], v2, s[12:13] offset:-2048 sc1\n"
      "buffer_load_dword v36, v42, s[32:35], s63 offen offset:0 sc1\n"
      "buffer_load_dword v37, v42, s[32:35], s63 offen offset:256 sc1\n"
      "s_waitcnt vmcnt(0)\n"
      "v_add3_u32 v2, v8, v9, s30\n"
      "v_add3_u32 v2, v2, v10, v11\n"
      "v_add3_u32 v2, v2, v16, v17\n"
      "v_add3_u32 v2, v2, v18, v19\n"
      "v_add3_u32 v2, v2, v128, v129\n"
      "v_add3_u32 v2, v2, v130, v131\n"
      "v_xor_b32 v2, v2, v36\n"
      "v_add3_u32 v3, v12, v13, s30\n"
      "v_add3_u32 v3, v3, v14, v15\n"
      "v_add3_u32 v3, v3, v20, v21\n"
      "v_add3_u32 v3, v3, v22, v23\n"
      "v_add3_u32 v3, v3, v132, v133\n"
      "v_add3_u32 v3, v3, v134, v135\n"
      "v_xor_b32 v3, v3, v37\n"
      "v_or_b32 v2, v2, v3\n"
      "v_cmp_ne_u32 vcc, 0, v2\n"
      "s_sub_u32 s44, s44, 1\n"
      "s_cbranch_vccz L_bhA0_%=\n"
      "s_cmp_lg_u32 s44, 0\n"
      "s_cbranch_scc1 L_shA0_%=\n"
      "s_branch L_bhA0_%=\n"
      "L_shA1_%=:\n"
      "v_add_u32 v2, 4096, v7\n"
      "global_load_dwordx4 v[136:139], v2, s[10:11] offset:-1024 sc1\n"
      "global_load_dwordx4 v[144:147], v2, s[10:11] offset:0 sc1\n"
      "global_load_dwordx4 v[152:155], v2, s[10:11] offset:1024 sc1\n"
      "v_add_u32 v2, 4096, v7\n"
      "global_load_dwordx4 v[140:143], v2, s[12:13] offset:-1024 sc1\n"
      "global_load_dwordx4 v[148:151], v2, s[12:13] offset:0 sc1\n"
      "global_load_dwordx4 v[156:159], v2, s[12:13] offset:1024 sc1\n"
      "buffer_load_dword v38, v42, s[32:35], s63 offen offset:512 sc1\n"
      "buffer_load_dword v39, v42, s[32:35], s63 offen offset:768 sc1\n"
      "s_waitcnt vmcnt(0)\n"
      "v_add3_u32 v2, v136, v137, s30\n"
      "v_add3_u32 v2, v2, v138, v139\n"
      "v_add3_u32 v2, v2, v144, v145\n"
      "v_add3_u32 v2, v2, v146, v147\n"
      "v_add3_u32 v2, v2, v152, v153\n"
      "v_add3_u32 v2, v2, v154, v155\n"
      "v_xor_b32 v2, v2, v38\n"
      "v_add3_u32 v3, v140, v141, s30\n"
      "v_add3_u32 v3, v3, v142, v143\n"
      "v_add3_u32 v3, v3, v148, v149\n"
      "v_add3_u32 v3, v3, v150, v151\n"
      "v_add3_u32 v3, v3, v156, v157\n"
      "v_add3_u32 v3, v3, v158, v159\n"
      "v_xor_b32 v3, v3, v39\n"
      "v_or_b32 v2, v2, v3\n"
      "v_cmp_ne_u32 vcc, 0, v2\n"
      "s_sub_u32 s44, s44, 1\n"
      "s_cbranch_vccz L_bhA1_%=\n"
      "s_cmp_lg_u32 s44, 0\n"
      "s_cbranch_scc1 L_shA1_%=\n"
      "s_branch L_bhA1_%=\n"
      "L_shA2_%=:\n"
      "v_add_u32 v2, 10240, v7\n"
      "global_load_dwordx4 v[172:175], v2, s[10:11] offset:-4096 sc1\n"
      "global_load_dwordx4 v[164:167], v2, s[10:11] offset:-3072 sc1\n"
      "global_load_dwordx4 v[64:67], v2, s[10:11] offset:-2048 sc1\n"
      "v_add_u32 v2, 10240, v7\n"
      "global_load_dwordx4 v[160:163], v2, s[12:13] offset:-4096 sc1\n"
      "global_load_dwordx4 v[168:171], v2, s[12:13] offset:-3072 sc1\n"
      "global_load_dwordx4 v[68:71], v2, s[12:13] offset:-2048 sc1\n"
      "buffer_load_dword v60, v42, s[32:35], s63 offen offset:1024 sc1\n"
      "buffer_load_dword v61, v42, s[32:35], s63 offen offset:1280 sc1\n"
      "s_waitcnt vmcnt(0)\n"
      "v_add3_u32 v2, v172, v173, s30\n"
      "v_add3_u32 v2, v2, v174, v175\n"
      "v_add3_u32 v2, v2, v164, v165\n"
      "v_add3_u32 v2, v2, v166, v167\n"
      "v_add3_u32 v2, v2, v64, v65\n"
      "v_add3_u32 v2, v2, v66, v67\n"
      "v_xor_b32 v2, v2, v60\n"
      "v_add3_u32 v3, v160, v161, s30\n"
      "v_add3_u32 v3, v3, v162, v163\n"
      "v_add3_u32 v3, v3, v168, v169\n"
      "v_add3_u32 v3, v3, v170, v171\n"
      "v_add3_u32 v3, v3, v68, v69\n"
      "v_add3_u32 v3, v3, v70, v71\n"
      "v_xor_b32 v3, v3, v61\n"
      "v_or_b32 v2, v2, v3\n"
      "v_cmp_ne_u32 vcc, 0, v2\n"
      "s_sub_u32 s44, s44, 1\n"
      "s_cbranch_vccz L_bhA2_%=\n"
      "s_cmp_lg_u32 s44, 0\n"
      "s_cbranch_scc1 L_shA2_%=\n"
      "s_branch L_bhA2_%=\n"
      "L_shA3_%=:\n"
      "v_add_u32 v2, 10240, v7\n"
      "global_load_dwordx4 v[72:75], v2, s[10:11] offset:-1024 sc1\n"
      "global_load_dwordx4 v[80:83], v2, s[10:11] offset:0 sc1\n"
      "global_load_dwordx4 v[88:91], v2, s[10:11] offset:1024 sc1\n"
      "v_add_u32 v2, 10240, v7\n"
      "global_load_dwordx4 v[76:79], v2, s[12:13] offset:-1024 sc1\n"
      "global_load_dwordx4 v[84:87], v2, s[12:13] offset:0 sc1\n"
      "global_load_dwordx4 v[92:95], v2, s[12:13] offset:1024 sc1\n"
      "buffer_load_dword v62, v42, s[32:35], s63 offen offset:1536 sc1\n"
      "buffer_load_dword v63, v42, s[32:35], s63 offen offset:1792 sc1\n"
      "s_waitcnt vmcnt(0)\n"
      "v_add3_u32 v2, v72, v73, s30\n"
      "v_add3_u32 v2, v2, v74, v75\n"
      "v_add3_u32 v2, v2, v80, v81\n"
      "v_add3_u32 v2, v2, v82, v83\n"
      "v_add3_u32 v2, v2, v88, v89\n"
      "v_add3_u32 v2, v2, v90, v91\n"
      "v_xor_b32 v2, v2, v62\n"
      "v_add3_u32 v3, v76, v77, s30\n"
      "v_add3_u32 v3, v3, v78, v79\n"
      "v_add3_u32 v3, v3, v84, v85\n"
      "v_add3_u32 v3, v3, v86, v87\n"
      "v_add3_u32 v3, v3, v92, v93\n"
      "v_add3_u32 v3, v3, v94, v95\n"
      "v_xor_b32 v3, v3, v63\n"
      "v_or_b32 v2, v2, v3\n"
      "v_cmp_ne_u32 vcc, 0, v2\n"
      "s_sub_u32 s44, s44, 1\n"
      "s_cbranch_vccz L_bhA3_%=\n"
      "s_cmp_lg_u32 s44, 0\n"
      "s_cbranch_scc1 L_shA3_%=\n"
      "s_branch L_bhA3_%=\n"
      "L_shB0_%=:\n"
      "v_add_u32 v2, 102400, v7\n"
      "global_load_dwordx4 v[224:227], v2, s[10:11] offset:-4096 sc1\n"
      "global_load_dwordx4 v[228:231], v2, s[10:11] offset:-3072 sc1\n"
      "global_load_dwordx4 v[232:235], v2, s[10:11] offset:-2048 sc1\n"
      "v_add_u32 v2, 102400, v7\n"
      "global_load_dwordx4 v[236:239], v2, s[10:11] offset:-1024 sc1\n"
      "global_load_dwordx4 v[240:243], v2, s[10:11] offset:0 sc1\n"
      "global_load_dwordx4 v[244:247], v2, s[10:11] offset:1024 sc1\n"
      "buffer_load_dword v32, v42, s[32:35], s63 offen offset:2048 sc1\n"
      "buffer_load_dword v33, v42, s[32:35], s63 offen offset:2304 sc1\n"
      "s_waitcnt vmcnt(0)\n"
      "v_add3_u32 v2, v224, v225, s30\n"
      "v_add3_u32 v2, v2, v226, v227\n"
      "v_add3_u32 v2, v2, v228, v229\n"
      "v_add3_u32 v2, v2, v230, v231\n"
      "v_add3_u32 v2, v2, v232, v233\n"
      "v_add3_u32 v2, v2, v234, v235\n"
      "v_xor_b32 v2, v2, v32\n"
      "v_add3_u32 v3, v236, v237, s30\n"
      "v_add3_u32 v3, v3, v238, v239\n"
      "v_add3_u32 v3, v3, v240, v241\n"
      "v_add3_u32 v3, v3, v242, v243\n"
      "v_add3_u32 v3, v3, v244, v245\n"
      "v_add3_u32 v3, v3, v246, v247\n"
      "v_xor_b32 v3, v3, v33\n"
      "v_or_b32 v2, v2, v3\n"
      "v_cmp_ne_u32 vcc, 0, v2\n"
      "s_sub_u32 s44, s44, 1\n"
      "s_cbranch_vccz L_bhB0_%=\n"
      "s_cmp_lg_u32 s44, 0\n"
      "s_cbranch_scc1 L_shB0_%=\n"
      "s_branch L_bhB0_%=\n"
      "L_shB1_%=:\n"
      "v_add_u32 v2, 108544, v7\n"
      "global_load_dwordx4 v[8:11], v2, s[10:11] offset:-4096 sc1\n"
      "global_load_dwordx4 v[12:15], v2, s[10:11] offset:-3072 sc1\n"
      "global_load_dwordx4 v[16:19], v2, s[10:11] offset:-2048 sc1\n"
      "v_add_u32 v2, 108544, v7\n"
      "global_load_dwordx4 v[20:23], v2, s[10:11] offset:-1024 sc1\n"
      "global_load_dwordx4 v[128:131], v2, s[10:11] offset:0 sc1\n"
      "global_load_dwordx4 v[132:135], v2, s[10:11] offset:1024 sc1\n"
      "buffer_load_dword v34, v42, s[32:35], s63 offen offset:2560 sc1\n"
      "buffer_load_dword v35, v42, s[32:35], s63 offen offset:2816 sc1\n"
      "s_waitcnt vmcnt(0)\n"
      "v_add3_u32 v2, v8, v9, s30\n"
      "v_add3_u32 v2, v2, v10, v11\n"
      "v_add3_u32 v2, v2, v12, v13\n"
      "v_add3_u32 v2, v2, v14, v15\n"
      "v_add3_u32 v2, v2, v16, v17\n"
      "v_add3_u32 v2, v2, v18, v19\n"
      "v_xor_b32 v2, v2, v34\n"
      "v_add3_u32 v3, v20, v21, s30\n"
      "v_add3_u32 v3, v3, v22, v23\n"
      "v_add3_u32 v3, v3, v128, v129\n"
      "v_add3_u32 v3, v3, v130, v131\n"
      "v_add3_u32 v3, v3, v132, v133\n"
      "v_add3_u32 v3, v3, v134, v135\n"
      "v_xor_b32 v3, v3, v35\n"
      "v_or_b32 v2, v2, v3\n"
      "v_cmp_ne_u32 vcc, 0, v2\n"
      "s_sub_u32 s44, s44, 1\n"
      "s_cbranch_vccz L_bhB1_%=\n"
      "s_cmp_lg_u32 s44, 0\n"
      "s_cbranch_scc1 L_shB1_%=\n"
      "s_branch L_bhB1_%=\n"
      "L_shB2_%=:\n"
      "v_add_u32 v2, 102400, v7\n"
      "global_load_dwordx4 v[136:139], v2, s[12:13] offset:-4096 sc1\n"
      "global_load_dwordx4 v[140:143], v2, s[12:13] offset:-3072 sc1\n"
      "global_load_dwordx4 v[144:147], v2, s[12:13] offset:-2048 sc1\n"
      "v_add_u32 v2, 102400, v7\n"
      "global_load_dwordx4 v[148:151], v2, s[12:13] offset:-1024 sc1\n"
      "global_load_dwordx4 v[152:155], v2, s[12:13] offset:0 sc1\n"
      "global_load_dwordx4 v[156:159], v2, s[12:13] offset:1024 sc1\n"
      "buffer_load_dword v24, v42, s[32:35], s63 offen offset:3072 sc1\n"
      "buffer_load_dword v25, v42, s[32:35], s63 offen offset:3328 sc1\n"
      "s_waitcnt vmcnt(0)\n"
      "v_add3_u32 v2, v136, v137, s30\n"
      "v_add3_u32 v2, v2, v138, v139\n"
      "v_add3_u32 v2, v2, v140, v141\n"
      "v_add3_u32 v2, v2, v142, v143\n"
      "v_add3_u32 v2, v2, v144, v145\n"
      "v_add3_u32 v2, v2, v146, v147\n"
      "v_xor_b32 v2, v2, v24\n"
      "v_add3_u32 v3, v148, v149, s30\n"
      "v_add3_u32 v3, v3, v150, v151\n"
      "v_add3_u32 v3, v3, v152, v153\n"
      "v_add3_u32 v3, v3, v154, v155\n"
      "v_add3_u32 v3, v3, v156, v157\n"
      "v_add3_u32 v3, v3, v158, v159\n"
      "v_xor_b32 v3, v3, v25\n"
      "v_or_b32 v2, v2, v3\n"
      "v_cmp_ne_u32 vcc, 0, v2\n"
      "s_sub_u32 s44, s44, 1\n"
      "s_cbranch_vccz L_bhB2_%=\n"
      "s_cmp_lg_u32 s44, 0\n"
      "s_cbranch_scc1 L_shB2_%=\n"
      "s_branch L_bhB2_%=\n"
      "L_shB3_%=:\n"
      "v_add_u32 v2, 108544, v7\n"
      "global_load_dwordx4 v[172:175], v2, s[12:13] offset:-4096 sc1\n"
      "global_load_dwordx4 v[160:163], v2, s[12:13] offset:-3072 sc1\n"
      "global_load_dwordx4 v[164:167], v2, s[12:13] offset:-2048 sc1\n"
      "v_add_u32 v2, 108544, v7\n"
      "global_load_dwordx4 v[168:171], v2, s[12:13] offset:-1024 sc1\n"
      "global_load_dwordx4 v[64:67], v2, s[12:13] offset:0 sc1\n"
      "global_load_dwordx4 v[68:71], v2, s[12:13] offset:1024 sc1\n"
      "buffer_load_dword v26, v42, s[32:35], s63 offen offset:3584 sc1\n"
      "buffer_load_dword v27, v42, s[32:35], s63 offen offset:3840 sc1\n"
      "s_waitcnt vmcnt(0)\n"
      "v_add3_u32 v2, v172, v173, s30\n"
      "v_add3_u32 v2, v2, v174, v175\n"
      "v_add3_u32 v2, v2, v160, v161\n"
      "v_add3_u32 v2, v2, v162, v163\n"
      "v_add3_u32 v2, v2, v164, v165\n"
      "v_add3_u32 v2, v2, v166, v167\n"
      "v_xor_b32 v2, v2, v26\n"
      "v_add3_u32 v3, v168, v169, s30\n"
      "v_add3_u32 v3, v3, v170, v171\n"
      "v_add3_u32 v3, v3, v64, v65\n"
      "v_add3_u32 v3, v3, v66, v67\n"
      "v_add3_u32 v3, v3, v68, v69\n"
      "v_add3_u32 v3, v3, v70, v71\n"
      "v_xor_b32 v3, v3, v27\n"
      "v_or_b32 v2, v2, v3\n"
      "v_cmp_ne_u32 vcc, 0, v2\n"
      "s_sub_u32 s44, s44, 1\n"
      "s_cbranch_vccz L_bhB3_%=\n"
      "s_cmp_lg_u32 s44, 0\n"
      "s_cbranch_scc1 L_shB3_%=\n"
      "s_branch L_bhB3_%=\n"
      "L_sgb_%=:\n"
      "buffer_load_dwordx4 v[244:247], v53, s[32:35], s66 offen sc1\n"
      "s_waitcnt vmcnt(0)\n"
      "v_add_u32 v245, v245, v247\n"
      "v_cmp_ne_u32 vcc, s54, v245\n"
      "s_sub_u32 s44, s44, 1\n"
      "s_cbranch_vccz L_bgb_%=\n"
      "s_cmp_lg_u32 s44, 0\n"
      "s_cbranch_scc1 L_sgb_%=\n"
      "s_branch L_bgb_%=\n"
      : STATE, "+{s[32:35]}"(s32), "+{v[8:23]}"(v8), "+{v[24:27]}"(v24), "+{v[52:55]}"(v52), "+{v[192:195]}"(t[0]), "+{v[240:243]}"(t[1]), "+{v[196:199]}"(t[2]), "+{v[200:203]}"(t[3]), "+{v[204:207]}"(t[4]), "+{v[208:211]}"(t[5]), "+{v[212:215]}"(t[6]), "+{v[216:219]}"(t[7]), "+{v[220:223]}"(t[8]), "+{v[224:227]}"(t[9]), "+{v[236:239]}"(t[10]), "+{v[40:43]}"(t[11]), "+{v[44:47]}"(t[12]), "+{v[48:51]}"(t[13]), "+{v[56:59]}"(t[14]), "+{v[60:63]}"(t[15]), "+{v[28:31]}"(t[16]), "+{v[4:7]}"(t[17])
      : "{s2}"(blockIdx.x)
      : "memory", "scc", "vcc", "s40", "s41", "s42", "s43", "s44", "s45", "s46", "s47", "s48", "s49", "s50", "s51", "v2", "v3", "v32", "v33", "v34", "v35", "v36", "v37", "v38", "v39", "v64", "v65", "v66", "v67", "v68", "v69", "v70", "v71", "v72", "v73", "v74", "v75", "v76", "v77", "v78", "v79", "v80", "v81", "v82", "v83", "v84", "v85", "v86", "v87", "v88", "v89", "v90", "v91", "v92", "v93", "v94", "v95", "v96", "v97", "v98", "v99", "v100", "v101", "v102", "v103", "v104", "v105", "v106", "v107", "v108", "v109", "v110", "v111", "v112", "v113", "v114", "v115", "v116", "v117", "v118", "v119", "v120", "v121", "v122", "v123", "v124", "v125", "v126", "v127", "v128", "v129", "v130", "v131", "v132", "v133", "v134", "v135", "v136", "v137", "v138", "v139", "v140", "v141", "v142", "v143", "v144", "v145", "v146", "v147", "v148", "v149", "v150", "v151", "v152", "v153", "v154", "v155", "v156", "v157", "v158", "v159", "v160", "v161", "v162", "v163", "v164", "v165", "v166", "v167", "v168", "v169", "v170", "v171", "v172", "v173", "v174", "v175", "v176", "v177", "v178", "v179", "v180", "v181", "v182", "v183", "v184", "v185", "v186", "v187", "v188", "v189", "v190", "v191", "v228", "v229", "v230", "v231", "v244", "v245", "v246", "v247", "v248", "v249", "v250", "v251", "v252", "v253", "v254", "v255", "a0", "a1", "a2", "a3", "a4", "a5", "a6", "a7", "a8", "a9", "a10", "a11", "a12", "a13", "a14", "a15", "a16", "a17", "a18", "a19", "a20", "a21", "a22", "a23", "a24", "a25", "a26", "a27", "a28", "a29", "a30", "a31", "a32", "a33", "a34", "a35", "a36", "a37", "a38", "a39", "a40", "a41", "a42", "a43", "a44", "a45", "a46", "a47", "a48", "a49", "a50", "a51", "a52", "a53", "a54", "a55", "a56", "a57", "a58", "a59", "a60", "a61", "a62", "a63", "a64", "a65", "a66", "a67", "a68", "a69", "a70", "a71", "a72", "a73", "a74", "a75", "a76", "a77", "a78", "a79", "a80", "a81", "a82", "a83", "a84", "a85", "a86", "a87", "a88", "a89", "a90", "a91", "a92", "a93", "a94", "a95", "a96", "a97", "a98", "a99", "a100", "a101", "a102", "a103", "a104", "a105", "a106", "a107", "a108", "a109", "a110", "a111", "a112", "a113", "a114", "a115", "a116", "a117", "a118", "a119", "a120", "a121", "a122", "a123", "a124", "a125", "a126", "a127", "a128", "a129", "a130", "a131", "a132", "a133", "a134", "a135", "a136", "a137", "a138", "a139", "a140", "a141", "a142", "a143", "a144", "a145", "a146", "a147", "a148", "a149", "a150", "a151", "a152", "a153", "a154", "a155", "a156", "a157", "a158", "a159", "a160", "a161", "a162", "a163", "a164", "a165", "a166", "a167", "a168", "a169", "a170", "a171", "a172", "a173", "a174", "a175", "a176", "a177", "a178", "a179", "a180", "a181", "a182", "a183", "a184", "a185", "a186", "a187", "a188", "a189", "a190", "a191", "a192", "a193", "a194", "a195", "a196", "a197", "a198", "a199", "a200", "a201", "a202", "a203", "a204", "a205", "a206", "a207", "a208", "a209", "a210", "a211", "a212", "a213", "a214", "a215", "a216", "a217", "a218", "a219", "a220", "a221", "a222", "a223", "a224", "a225", "a226", "a227", "a228", "a229", "a230", "a231", "a232", "a233", "a234", "a235", "a236", "a237", "a238", "a239", "a240", "a241", "a242", "a243", "a244", "a245", "a246", "a247", "a248", "a249", "a250", "a251", "a252", "a253", "a254", "a255");
}

extern "C" void kernel_launch(void* const* d_in, const int* in_sizes, int n_in, void* d_out, int out_size,
                              void* d_ws, size_t ws_size, hipStream_t stream) {
  mega<<<256, 256, 0, stream>>>((const u16*)d_in[0], (const u16*)d_in[1], (const u16*)d_in[2], (const u16*)d_in[3],
                                (const u16*)d_in[4], (const u16*)d_in[5], (const u16*)d_in[7], (const u16*)d_in[8],
                                (const u16*)d_in[9], (const u16*)d_in[10], (const u16*)d_in[11], (char*)d_ws, (u16*)d_out);
}
```

```cpp
#include <hip/hip_runtime.h>
#include <stdint.h>

typedef unsigned short u16;
typedef __attribute__((ext_vector_type(4))) unsigned int u32x4;
typedef __attribute__((ext_vector_type(8))) unsigned int u32x8;
typedef __attribute__((ext_vector_type(16))) unsigned int u32x16;
static constexpr int D_ = 640, F_ = 1536, RS_ = 4224;

static constexpr unsigned WS_FW = 20480;
static constexpr unsigned WS_FD = WS_FW + 61440;
static constexpr unsigned WS_STAT = 147456;
static constexpr unsigned WS_PART = 262144;
static constexpr unsigned WS_MOD = 786432;
static constexpr unsigned WS_KVT = 1u << 20;
static constexpr unsigned WS_QT = 4u << 20;
static constexpr unsigned WS_X1T = 7u << 20;
static constexpr unsigned WS_CK = 2752512;
static constexpr unsigned WS_WP = 10u << 20;
static constexpr unsigned WS_WD = 14u << 20;
static constexpr unsigned WS_HH = 16u << 20;
static constexpr unsigned WS_HC = 22u << 20;
static constexpr unsigned WS_ST2 = WS_HC + (512u << 10);
static constexpr unsigned WS_X1R = 23u << 20;
static constexpr unsigned WS_DUMMY = WS_HC + (768u << 10);

static constexpr int KCP = 4160, KCH = 4 * KCP;
static constexpr int XR_CH = 7680;
static constexpr int L_XI = 3 * KCH;
static constexpr int L_EX = 72960;
static constexpr int L_KV = L_EX, KVW = 6400;
static constexpr int L_KT = L_KV + 4 * KVW;
static constexpr int L_QT = L_KT + 84 * 144;
static constexpr int L_WR = L_QT + 10240;
static constexpr int L_GW = L_WR + 16384;
static constexpr int L_GS = L_GW + 20480;
static constexpr int L_SC = L_GS + 320;
static constexpr int L_XR = 110592, L_RS = 162304;
static constexpr int L_WD1 = 122880;
static_assert(L_KT == 98560 && L_QT == 110656 && L_WR == 120896 && L_GW == 137280, "");

struct __attribute__((aligned(256))) Tab { unsigned s[1024][64]; };
static constexpr Tab make_tab() {
  Tab t = {};
  for (int i = 0; i < 1024; ++i) {
    const int b = i >> 2, w = i & 3, xcd = b & 7, idx = b >> 3, L = xcd * 32 + idx, side = L & 1;
    const int ct = L & 7, rt = L >> 3, srow = xcd * 256 + idx * 8;
    const int chA = 128 + L, chH = L >> 1, rH = 512 * (side + (3 - 2 * side) * (w >> 1)) + 256 * (w & 1);
    const int id = idx < 29 ? idx : 29, gj = xcd * 30 + id, j_ks = 5 * (xcd >> 1) + (id * 4 + w) / 24, j_b = 24 * (xcd & 1) + (id * 4 + w) % 24;
    const int d_ks = gj / 5, d_cb = 4 * (gj % 5) + w;
    const unsigned dummy = WS_DUMMY + (i & 63) * 1024;
    const int bm = (xcd >> 2) * 8 + (idx >> 2), bn = (xcd & 3) * 4 + (idx & 3), bm2 = (xcd >> 1) * 8 + (idx >> 2), bn2 = (xcd & 1) * 4 + (idx & 3);
    t.s[i][0] = WS_KVT + (1984 - 64 * rt) * 2 + ct * 80 * RS_;
    t.s[i][1] = WS_KVT + WS_CK + (31 - rt) * RS_ + ct * 512;
    t.s[i][2] = idx < 30 ? WS_MOD + (gj >> 2) * 2048 + (gj & 3) * 64 : dummy;
    t.s[i][3] = 32 * j_ks * (F_ * 2) + (j_b - w) * 64;
    t.s[i][4] = WS_QT + rt * 128 + ct * 80 * RS_;
    t.s[i][5] = WS_QT + WS_CK + (rt * 40 + ct * 5) * 128;
    t.s[i][6] = srow * 1280;
    t.s[i][7] = WS_STAT + (srow >> 9) * 4096 + ((srow >> 3) & 63) * 16;
    t.s[i][8] = WS_MOD + (1920 + 640 * (w >> 1) + 32 * j_ks + 16 * (w & 1)) * 32;
    t.s[i][9] = idx < 30 ? WS_PART + j_ks * 24576 + j_b * 512 : dummy;
    t.s[i][10] = WS_KVT + WS_CK + chA / 20 * 128 + chA % 20 * 4;
    t.s[i][11] = WS_KVT + WS_CK + chH / 20 * 128 + chH % 20 * 4;
    t.s[i][12] = idx < 30 ? WS_WP + (4 * j_b + 0) / 3 * 61440 + (j_ks * 3 + (4 * j_b + 0) % 3) * 1024 : dummy;
    t.s[i][13] = idx < 30 ? WS_WP + (4 * j_b + 1) / 3 * 61440 + (j_ks * 3 + (4 * j_b + 1) % 3) * 1024 : dummy;
    t.s[i][14] = idx < 30 ? WS_WP + (4 * j_b + 2) / 3 * 61440 + (j_ks * 3 + (4 * j_b + 2) % 3) * 1024 : dummy;
    t.s[i][15] = idx < 30 ? WS_WP + (4 * j_b + 3) / 3 * 61440 + (j_ks * 3 + (4 * j_b + 3) % 3) * 1024 : dummy;
    t.s[i][16] = WS_FW + (3 * (bn * 4 + w) / 4 < 46 ? 3 * (bn * 4 + w) / 4 : 46) * 1280;
    t.s[i][17] = WS_X1T + WS_CK + chH / 2 * 512 + chH % 2 * 64 + (rH >> 9) * 128 + ((rH >> 6) & 7) * 8;
    t.s[i][18] = WS_X1T + ((rH >> 6) * D_ + chH) * 128;
    t.s[i][19] = idx < 30 ? WS_FW + (j_b * 20 + j_ks) * 64 : dummy;
    t.s[i][20] = WS_QT + chH * RS_ + rH * 2;
    t.s[i][21] = WS_X1T + WS_CK + chA / 2 * 512 + chA % 2 * 64;
    t.s[i][22] = WS_STAT + (rH >> 9) * 4096 + ((rH >> 3) & 63) * 16;
    t.s[i][23] = WS_MOD + chH / 8 * 256 + chH % 8 * 8;
    t.s[i][24] = WS_QT + WS_CK + chA / 16 * 128 + chA % 16 * 4;
    t.s[i][25] = WS_QT + WS_CK + (rH >> 6) * 5120 + chH / 16 * 128 + chH % 16 * 4;
    t.s[i][26] = idx < 30 ? WS_WD + ((2 * d_cb / 5 * 48 + d_ks) * 5 + 2 * d_cb % 5) * 1024 : dummy;
    t.s[i][27] = idx < 30 ? WS_WD + (((2 * d_cb + 1) / 5 * 48 + d_ks) * 5 + (2 * d_cb + 1) % 5) * 1024 : dummy;
    t.s[i][28] = idx < 30 ? WS_FD + (d_cb * 48 + d_ks) * 64 : dummy;
    t.s[i][29] = WS_X1T + chA * 128;
    t.s[i][30] = WS_X1T + WS_CK + bm / 4 * 128 + bm % 4 * 16 + 4;
    t.s[i][31] = WS_WP + (bn * 4 + w) * 61440;
    t.s[i][32] = chA * 4096;
    t.s[i][33] = chH * 4096;
    t.s[i][34] = ((1792 - rH) >> 5) * 96;
    t.s[i][35] = (96 * (d_ks / 3) + 16 * (d_ks % 3 >> 1) + 2 * (d_ks % 3 & 1)) * (D_ * 2) + d_cb * 64;
    t.s[i][36] = (d_ks % 3 == 1 ? 30 : 18) * (D_ * 2) + ((chA / 8 * 256 + chA % 8 * 8 - chH / 8 * 256 - chH % 8 * 8) << 16);
    t.s[i][37] = ((chA >> 1) & 3) * 32;
    t.s[i][38] = WS_KVT + WS_CK + (chA + 256) / 20 * 128 + (chA + 256) % 20 * 4;
    t.s[i][39] = side;
    t.s[i][40] = WS_ST2 + (bm2 * 8 + bn2) * 1024 + w * 256;
    t.s[i][41] = WS_ST2 + (2 * bm + (w >> 1)) * 8192 + (w & 1) * 512;
    t.s[i][42] = L_XR + (w < 3 ? 20 * w : 56) * 128;
    t.s[i][43] = L_RS + (w & 1) * 512 + (w >> 1) * 256;
    t.s[i][48] = w;
    t.s[i][49] = WS_HH + (((2 * bm + (w >> 1)) * 4 + (w & 1)) * 48 + 3 * bn) * 1024;
    t.s[i][50] = WS_HC + (((2 * bm + (w >> 1)) * 4 + (bn >> 2)) * 8 + (bn & 3)) * 512 + (w & 1) * 256;
    t.s[i][51] = WS_HC + (bm2 * 4 + w) * 4096;
    t.s[i][52] = WS_FD + (((5 * bn2) >> 1) * 48 + 12 * w) * 64;
    t.s[i][53] = WS_PART + (bn * 4 + w) * 384;
    t.s[i][54] = WS_MOD + (3200 + bn2 * 80 + (w < 3 ? 16 * w : 48)) * 32;
    t.s[i][55] = WS_X1R + (bm2 * D_ + bn2 * 80 + (w < 3 ? 20 * w : 56)) * 128;
    t.s[i][56] = WS_X1T + WS_CK + (bn2 * 80 + (w < 3 ? 20 * w : 56)) * 256 + bm2 / 8 * 128 + bm2 % 8 * 8 + bm2 % 2 * 4;
    t.s[i][57] = WS_HH + (((2 * bm + (w >> 1)) * 4 + 2 + (w & 1)) * 48 + 3 * bn) * 1024;
    t.s[i][58] = WS_HC + (((2 * bm + (w >> 1)) * 4 + (bn >> 2)) * 8 + 4) * 512 + (4 * (w & 1) + (bn & 3)) * 256;
    t.s[i][59] = WS_X1T + bm * 163840;
    t.s[i][60] = WS_WD + (bn2 * 48 + 12 * w) * 5120;
    t.s[i][61] = w * 4096;
    t.s[i][62] = ((bm2 >> 1) * 128 + (bm2 & 1) * 32 + (w & 1) * 16) * (D_ * 2) + bn2 * 160 + (w >> 1) * 64;
    t.s[i][63] = WS_HH + (bm2 * 192 + 12 * w) * 1024;
  }
  return t;
}
static __device__ const Tab TAB = make_tab();

__global__ __launch_bounds__(256, 1) void mega(const u16* q, const u16* k, const u16* v, const u16* shift_k, const u16* ssm_k, const u16* ssm_D, const u16* w_gate,
                                               const u16* w_up, const u16* w_down, const u16* c, const u16* w_mod, char* ws, u16* out) {
  __shared__ __attribute__((aligned(16))) char lds[163840];
  int w = (int)(size_t)lds, tid = threadIdx.x, v1;
  u32x16 s4, s60, s76;
  u32x8 s20, s52, s92;
  u32x4 s28, s36, v232;
  asm volatile(
      "s_load_dwordx16 s[4:19], s[0:1], 0x0\n"
      "s_load_dwordx8 s[20:27], s[0:1], 0x40\n"
      "s_load_dwordx2 s[28:29], s[0:1], 0x60\n"
      "v_readfirstlane_b32 s3, v0\n"
      "v_and_b32 v2, 63, v0\n"
      "s_lshl_b32 s42, s2, 4\n"
      "s_and_b32 s43, s2, 7\n"
      "v_mul_u32_u24 v3, 205, v2\n"
      "s_and_b32 s52, s2, 0xc0\n"
      "s_lshr_b32 s3, s3, 6\n"
      "v_lshlrev_b32 v8, 4, v2\n"
      "s_cmp_lg_u32 s3, 3\n"
      "s_cbranch_scc1 L_pf%=\n"
      "s_and_b32 s47, s2, 0xf8\n"
      "s_lshl_b32 s47, s47, 10\n"
      "s_cmp_ge_u32 s47, 39168\n"
      "s_cbranch_scc1 L_pf%=\n"
      "s_getpc_b64 s[54:55]\n"
      "v_lshl_add_u32 v9, v8, 3, s47\n"
      "s_and_b32 s54, s54, 0xffffff00\n"
      "s_mov_b32 m0, 160512\n"
      "v_min_u32 v9, 39104, v9\n"
      "global_load_lds_dword v9, s[54:55]\n"
      "L_pf%=:\n"
      "s_lshl_b32 s47, s43, 8\n"
      "v_add_u32 v9, 13120, v3\n"
      "v_add_u32 v10, 26240, v3\n"
      "s_add_u32 s52, s52, s47\n"
      "s_lshl_b32 s47, s3, 4\n"
      "v_lshrrev_b32 v3, 11, v3\n"
      "v_lshrrev_b32 v9, 11, v9\n"
      "s_add_u32 s52, s52, s47\n"
      "s_movk_i32 s47, 1120\n"
      "v_lshrrev_b32 v10, 11, v10\n"
      "s_mulk_i32 s52, 1280\n"
      "s_bfe_u32 s54, s2, 0x30003\n"
      "v_mad_u32_u24 v232, v3, s47, v8\n"
      "v_mad_u32_u24 v233, v9, s47, v8\n"
      "v_mad_u32_u24 v234, v10, s47, v8\n"
      "s_mul_i32 s47, s54, 160\n"
      "s_mul_i32 s53, s3, 6400\n"
      "s_add_u32 s52, s52, s47\n"
      "v_subrev_u32 v233, 6656, v233\n"
      "v_subrev_u32 v234, 5632, v234\n"
      "s_sub_u32 s47, s52, 3840\n"
      "s_add_u32 s53, s53, 72960\n"
      "v_add_u32 v3, s47, v232\n"
      "s_add_u32 s47, s52, 3840\n"
      "s_add_u32 s55, s52, 7680\n"
      "s_mov_b32 s38, 2621440\n"
      "s_mov_b32 s39, 0x20000\n"
      "s_mulk_i32 s54, 640\n"
      "s_waitcnt lgkmcnt(0)\n"
      "s_mov_b32 m0, s53\n"
      "s_mov_b32 s36, s6\n"
      "s_and_b32 s37, s7, 0xffff\n"
      "buffer_load_dwordx4 v3, s[36:39], 0 offen lds\n"
      "s_add_u32 m0, s53, 1024\n"
      "s_nop 0\n"
      "buffer_load_dwordx4 v233, s[36:39], s47 offen lds\n"
      "s_add_u32 m0, s53, 2048\n"
      "v_add_u32 v9, s54, v8\n"
      "buffer_load_dwordx4 v234, s[36:39], s47 offen lds\n"
      "s_add_u32 m0, s53, 3072\n"
      "s_mov_b32 s36, s8\n"
      "s_and_b32 s37, s9, 0xffff\n"
      "buffer_load_dwordx4 v232, s[36:39], s52 offen lds\n"
      "s_add_u32 m0, s53, 4096\n"
      "s_nop 0\n"
      "buffer_load_dwordx4 v233, s[36:39], s55 offen lds\n"
      "s_add_u32 m0, s53, 5120\n"
      "s_mov_b32 exec_hi, 0\n"
      "buffer_load_dwordx4 v234, s[36:39], s55 offen lds\n"
      "s_add_u32 m0, s53, 5632\n"
      "s_mov_b32 exec_hi, 0xff\n"
      "global_load_lds_dwordx4 v9, s[10:11]\n"
      "s_mov_b32 exec_hi, -1\n"
      "s_load_dwordx2 s[30:31], s[26:27], 0x0\n"
      "s_lshl_b32 s51, s3, 2\n"
      "s_add_u32 s42, s42, s51\n"
      "s_lshl_b32 s42, s42, 6\n"
      "s_add_u32 s56, s40, s42\n"
      "s_addc_u32 s57, s41, 0\n"
      "s_load_dwordx16 s[60:75], s[56:57], 0x0\n"
      "s_load_dwordx16 s[76:91], s[56:57], 0x40\n"
      "s_load_dwordx8 s[92:99], s[56:57], 0x80\n"
      : "+{s3}"(w), "+{v0}"(tid), "=&{s[4:19]}"(s4), "=&{s[20:27]}"(s20), "=&{s[28:31]}"(s28), "=&{s[36:39]}"(s36), "=&{s[52:59]}"(s52), "=&{s[60:75]}"(s60), "=&{s[76:91]}"(s76),
        "=&{s[92:99]}"(s92), "=&{v[232:235]}"(v232), "=&{v1}"(v1)
      : "{s[0:1]}"(__builtin_amdgcn_kernarg_segment_ptr()), "{s2}"(blockIdx.x), "{s[40:41]}"(&TAB)
      : "memory", "scc", "s42", "s43", "s47", "s48", "s49", "s50", "s51", "v2", "v3", "v4", "v5", "v8", "v9", "v10", "v11");
#define STATE "+{s3}"(w), "+{v0}"(tid), "+{s[4:19]}"(s4), "+{s[20:27]}"(s20), "+{s[28:31]}"(s28), "+{s[36:39]}"(s36), "+{s[52:59]}"(s52), "+{s[60:75]}"(s60), "+{s[76:91]}"(s76), \
              "+{s[92:99]}"(s92), "+{v[232:235]}"(v232), "+{v1}"(v1)
  u32x4 t[18], s32, v24, v52;
  u32x16 v8, v112;
  {
    const int lane = tid & 63, ip = lane & 15, fq = lane >> 4, q4 = ip >> 2, p4 = lane & 3;
    const int ck = 40 * w + (lane < 40 ? lane : 39);
    const bool dg = fq == q4;
    const unsigned msk = dg ? ((ip & 1) ? 0xFFFF0000u : 0xFFFFu) : 0u, mk0 = (ip & 2) ? 0u : msk, mk1 = (ip & 2) ? msk : 0u;
    const int fo = (ip & 3) * KCP + 2 * ((ip & 12) + 8 * (fq & 1) + 256 * (fq >> 1)), go = L_XI + (15 - ip + 8 * (fq >> 1)) * 96 + 16 * (fq & 1);
    const int exw = L_EX + ip * 128 + (fq ^ (ip & 7)) * 16, z7 = ((tid < 84 ? tid : 83) * 37) >> 8, rowA = 8 * tid;
    t[0][0] = dg && p4 == 0 ? 0x3F803F80u : 0u;
    t[0][1] = dg && p4 == 1 ? 0x3F803F80u : 0u;
    t[0][2] = dg && p4 == 2 ? 0x3F803F80u : 0u;
    t[0][3] = dg && p4 == 3 ? 0x3F803F80u : 0u;
    t[1][0] = (mk0 & 0x0D0C0D0Cu) ^ 0x0C0C0C0Cu;
    t[1][1] = (mk1 & 0x0D0C0D0Cu) ^ 0x0C0C0C0Cu;
    t[1][2] = (mk0 & 0x0F0E0F0Eu) ^ 0x0C0C0C0Cu;
    t[1][3] = (mk1 & 0x0F0E0F0Eu) ^ 0x0C0C0C0Cu;
    t[2][0] = L_KV + w * KVW + ip * 160 + fq * 8;
    t[2][1] = L_KV + w * KVW + 5632 + ip * 8;
    t[2][2] = mk0;
    t[2][3] = mk1;
    t[3][0] = L_KT + ip * 144 + (60 - 16 * w - 4 * fq) * 2;
    t[3][1] = L_KT + (20 * w + (lane >> 3)) * 144 + (lane & 7) * 16;
    t[3][2] = (20 * w + (lane >> 3)) * RS_ + (lane & 7) * 16;
    t[3][3] = lane & 4 ? 0x80000000u : w * 128 + (8 * (lane & 7) + (lane >> 3)) * 4;
    t[4][0] = L_GS + ((ck & ~31) + ((ck >> 2) & 3) * 8 + ((ck >> 4) & 1) * 4 + (ck & 3)) * 2;
    t[4][1] = L_GW + (4 * fq + q4) * 128 + p4 * 8 + ((w ^ (2 * fq + (q4 >> 1))) & 3) * 32;
    t[4][2] = L_GS + fq * 16;
    t[4][3] = lane < 16 ? (2 * w + (lane >> 3)) * 256 + (lane & 7) * 8 : 0x80000000u;
    t[5][0] = (lane & 12) == 4 ? 1 << 20 : 0;
    t[5][1] = lane & 8 ? 0u : ~0u;
    t[5][2] = (8 * w + fq) * (F_ * 2) + (ip ^ (2 * fq)) * 16;
    t[5][3] = (2 * w + (lane >> 5)) * 1280 + (lane & 31) * 16;
    t[6][0] = p4 * 64 + (lane >> 5) * 256 + ((lane >> 2) & 7) * 8;
    t[6][1] = L_WR + (fq >> 1) * 4096 + (fq & 1) * 1024 + q4 * 256 + (((2 * w + 1) ^ q4) & 7) * 32 + p4 * 8;
    t[6][2] = lane == 0 ? w * 1024 : 0x80000000u;
    t[6][3] = ((fq & 1 ? w * 64 + 512 : fq * 32 + 128 * w) + ip * 4) | ((unsigned)(fq & (fq >> 1 | w >> 1)) << 31);
    t[7][0] = (ip < 8 ? ip : 8 + p4) * KCP + 56;
    t[7][1] = z7 * (KCP - 56) + (tid < 84 ? tid : 83) * 8;
    t[7][2] = L_XI + 64 * 96 + (tid < 90 ? tid : 89) * 16;
    t[7][3] = L_WR + (fq >> 1) * 4096 + (fq & 1) * 1024 + q4 * 256 + (((2 * w) ^ q4) & 7) * 32 + p4 * 8;
    t[8][0] = L_SC + w * 32 + (lane >> 2) * 2;
    t[8][1] = (2 * (tid >> 4) + ((tid >> 2) & 1)) * (D_ * 128) + (2 * ((tid >> 3) & 1) + ((tid >> 1) & 1)) * 32 + (tid & 1) * 16;
    t[8][2] = (4 * fq + ((lane >> 2) & 1)) * (D_ * 2) + p4 * 16;
    t[8][3] = ((lane >> 3) ^ (lane >> 4)) & 1;
  }
  asm volatile(
      "v_and_b32 v2, 63, v0\n"
      "s_and_b32 s43, s2, 7\n"
      "v_mul_u32_u24 v3, 205, v2\n"
      "v_lshlrev_b32 v8, 4, v2\n"
      "s_lshr_b32 s48, s2, 3\n"
      "s_min_u32 s48, s48, 29\n"
      "s_mul_i32 s49, s43, 30\n"
      "v_lshrrev_b32 v4, 3, v2\n"
      "s_add_u32 s48, s48, s49\n"
      "v_xor_b32 v5, v4, v2\n"
      "s_and_b32 s49, s48, 3\n"
      "v_and_b32 v11, 1, v2\n"
      "s_lshl_b32 s49, s49, 2\n"
      "v_and_or_b32 v5, v5, 6, v11\n"
      "s_add_u32 s49, s49, s3\n"
      "v_mul_u32_u24 v4, 7680, v4\n"
      "s_lshr_b32 s48, s48, 2\n"
      "s_mul_i32 s50, s49, 307200\n"
      "s_lshl_b32 s48, s48, 7\n"
      "v_lshl_add_u32 v4, v5, 4, v4\n"
      "s_add_u32 s48, s48, s50\n"
      "s_mul_i32 s50, s3, 5120\n"
      "s_mulk_i32 s49, 80\n"
      "v_min_u32 v5, 39, v2\n"
      "v_add_u32 v4, s48, v4\n"
      "v_lshl_add_u32 v5, v5, 1, s49\n"
      "s_add_u32 m0, s50, 137280\n"
      "global_load_ushort v235, v5, s[22:23]\n"
      "global_load_lds_dwordx4 v4, s[24:25]\n"
      "s_add_u32 m0, m0, 1024\n"
      "v_add_u32 v4, 61440, v4\n"
      "global_load_lds_dwordx4 v4, s[24:25]\n"
      "s_add_u32 m0, m0, 1024\n"
      "v_add_u32 v4, 61440, v4\n"
      "global_load_lds_dwordx4 v4, s[24:25]\n"
      "s_add_u32 m0, m0, 1024\n"
      "v_add_u32 v4, 61440, v4\n"
      "global_load_lds_dwordx4 v4, s[24:25]\n"
      "s_add_u32 m0, m0, 1024\n"
      "v_add_u32 v4, 61440, v4\n"
      "global_load_lds_dwordx4 v4, s[24:25]\n"
      "s_lshr_b32 s42, s2, 3\n"
      "s_lshl_b32 s47, s43, 5\n"
      "s_add_u32 s42, s47, s42\n"
      "s_and_b32 s54, s42, -4\n"
      "s_lshl_b32 s47, s42, 1\n"
      "s_and_b32 s47, s47, -4\n"
      "s_load_dword s53, s[14:15], s54\n"
      "s_load_dword s58, s[14:15], s47 offset:0x100\n"
      "s_load_dword s59, s[14:15], s47 offset:0x300\n"
      "s_min_u32 s54, s3, 2\n"
      "s_lshl_b32 s54, s54, 10\n"
      "s_lshr_b32 s10, s42, 1\n"
      "s_lshl_b32 s47, s42, 12\n"
      "s_lshl_b32 s10, s10, 12\n"
      "s_add_u32 s47, s47, s54\n"
      "s_add_u32 s54, s54, s10\n"
      "s_load_dwordx2 s[6:7], s[12:13], s47 offset:0x80400\n"
      "s_add_u32 s47, s47, 0x100000\n"
      "s_load_dwordx2 s[10:11], s[12:13], s54 offset:0x400\n"
      "s_load_dwordx2 s[8:9], s[12:13], s47 offset:0x80400\n"
      "s_mov_b32 s32, s26\n"
      "s_and_b32 s33, s27, 0xffff\n"
      "s_mov_b32 s34, 27262976\n"
      "s_mov_b32 s35, 0x20000\n"
      "v_lshlrev_b32 v54, 4, v0\n"
      "s_sub_u32 s18, s18, 2048\n"
      "s_subb_u32 s19, s19, 0\n"
      "s_add_u32 s22, s20, 60416\n"
      "s_addc_u32 s23, s21, 0\n"
      "v_mov_b32 v66, 0\n"
      "v_mov_b32 v67, 0\n"
      "v_mov_b32 v72, 0\n"
      "v_mov_b32 v73, 0\n"
      "v_mov_b32 v76, 0\n"
      "v_mov_b32 v77, 0\n"
      "v_mov_b32 v82, 0\n"
      "v_mov_b32 v83, 0\n"
      "v_mov_b32 v88, 0\n"
      "v_mov_b32 v89, 0\n"
      "v_mov_b32 v92, 0\n"
      "v_mov_b32 v93, 0\n"
      "v_mov_b32 v98, 0\n"
      "v_mov_b32 v99, 0\n"
      "v_mov_b32 v104, 0\n"
      "v_mov_b32 v105, 0\n"
      "v_mov_b32 v108, 0\n"
      "v_mov_b32 v109, 0\n"
      "v_mov_b32 v114, 0\n"
      "v_mov_b32 v115, 0\n"
      "v_mov_b32 v120, 0\n"
      "v_mov_b32 v121, 0\n"
      "v_mov_b32 v124, 0\n"
      "v_mov_b32 v125, 0\n"
      "v_mov_b32 v34, 0\n"
      "v_mov_b32 v35, 0\n"
      "v_and_b32 v32, 0x3f803f80, v198\n"
      "v_and_b32 v33, 0x3f803f80, v199\n"
      "v_and_b32 v36, 0x3f803f80, v198\n"
      "v_and_b32 v37, 0x3f803f80, v199\n"
      "s_mov_b32 s48, 0xaaaaaaaa\n"
      "s_mov_b32 s49, s48\n"
      "s_mov_b32 s50, 0xcccccccc\n"
      "s_mov_b32 s51, s50\n"
      "s_waitcnt lgkmcnt(0)\n"
      "s_add_u32 s30, s30, 1\n"
      "s_addc_u32 s31, s31, 0\n"
      "s_mul_i32 s24, s30, 10\n"
      "v_mov_b32 v52, s30\n"
      "v_mov_b32 v53, s31\n"
      "s_and_b32 s42, s96, 0xffff\n"
      "s_lshl_b32 s43, s3, 11\n"
      "v_mad_u32_u24 v222, v223, s42, v222\n"
      "s_cmp_eq_u32 s3, 3\n"
      "s_cmov_b32 s43, 7680\n"
      "v_add_u32 v222, s95, v222\n"
      "s_add_u32 s43, s43, 49920\n"
      "s_mov_b32 s36, s12\n"
      "s_and_b32 s37, s13, 0xffff\n"
      "s_add_u32 s40, s60, 33792\n"
      "s_add_u32 s41, s60, 67584\n"
      "s_add_u32 s47, s92, 0x100000\n"
      "s_sub_u32 s54, s92, s93\n"
      "s_lshl_b32 s45, s3, 12\n"
      "v_and_b32 v39, s54, v209\n"
      "v_add_u32 v210, s63, v210\n"
      "v_add_u32 v208, v39, v208\n"
      "v_add_u32 v39, 11264, v210\n"
      "s_waitcnt vmcnt(6)\n"
      "ds_read_b64 v[244:245], v197 offset:0\n"
      "ds_read_b64 v[246:247], v197 offset:128\n"
      "ds_read_b64 v[248:249], v197 offset:256\n"
      "ds_read_b64 v[250:251], v197 offset:384\n"
      "ds_read_b64 v[252:253], v197 offset:512\n"
      "ds_read_b64 a[0:1], v196 offset:0\n"
      "ds_read_b64 a[2:3], v196 offset:32\n"
      "ds_read_b64 a[4:5], v196 offset:64\n"
      "ds_read_b64 a[6:7], v196 offset:96\n"
      "ds_read_b64 a[8:9], v196 offset:128\n"
      "ds_read_b64 a[10:11], v196 offset:160\n"
      "ds_read_b64 a[12:13], v196 offset:192\n"
      "ds_read_b64 a[14:15], v196 offset:224\n"
      "ds_read_b64 a[16:17], v196 offset:256\n"
      "ds_read_b64 a[18:19], v196 offset:288\n"
      "s_waitcnt lgkmcnt(10)\n"
      "ds_read_b64 a[20:21], v196 offset:320\n"
      "ds_read_b64 a[22:23], v196 offset:352\n"
      "ds_read_b64 a[24:25], v196 offset:384\n"
      "v_perm_b32 v64, v245, v245, v242\n"
      "v_perm_b32 v65, v245, v245, v243\n"
      "v_perm_b32 v80, v245, v245, v240\n"
      "v_perm_b32 v81, v245, v245, v241\n"
      "ds_read_b64 a[26:27], v196 offset:416\n"
      "ds_read_b64 a[28:29], v196 offset:448\n"
      "ds_read_b64 a[30:31], v196 offset:480\n"
      "v_perm_b32 v68, v247, v247, v242\n"
      "v_perm_b32 v69, v247, v247, v243\n"
      "v_perm_b32 v84, v247, v247, v240\n"
      "v_perm_b32 v85, v247, v247, v241\n"
      "ds_read_b64 a[32:33], v196 offset:512\n"
      "ds_read_b64 a[34:35], v196 offset:544\n"
      "ds_read_b64 a[36:37], v196 offset:576\n"
      "v_perm_b32 v70, v249, v249, v242\n"
      "v_perm_b32 v71, v249, v249, v243\n"
      "v_perm_b32 v86, v249, v249, v240\n"
      "v_perm_b32 v87, v249, v249, v241\n"
      "ds_read_b64 a[38:39], v196 offset:608\n"
      "ds_read_b64 a[40:41], v196 offset:3072\n"
      "ds_read_b64 a[42:43], v196 offset:3104\n"
      "v_perm_b32 v74, v251, v251, v242\n"
      "v_perm_b32 v75, v251, v251, v243\n"
      "v_perm_b32 v90, v251, v251, v240\n"
      "v_perm_b32 v91, v251, v251, v241\n"
      "ds_read_b64 a[44:45], v196 offset:3136\n"
      "ds_read_b64 a[46:47], v196 offset:3168\n"
      "ds_read_b64 a[48:49], v196 offset:3200\n"
      "v_perm_b32 v78, v253, v253, v242\n"
      "v_perm_b32 v79, v253, v253, v243\n"
      "v_perm_b32 v94, v253, v253, v240\n"
      "v_perm_b32 v95, v253, v253, v241\n"
      "s_waitcnt lgkmcnt(14)\n"
      "v_mfma_f32_16x16x32_bf16 v[128:131], a[0:3], v[64:67], 0\n"
      "v_perm_b32 v96, v244, v244, v242\n"
      "v_perm_b32 v97, v244, v244, v243\n"
      "v_mfma_f32_16x16x32_bf16 v[132:135], a[10:13], v[80:83], 0\n"
      "buffer_load_dwordx4 v[8:11], v54, s[36:39], s92 offen\n"
      "v_perm_b32 v112, v244, v244, v240\n"
      "v_perm_b32 v113, v244, v244, v241\n"
      "v_mfma_f32_16x16x32_bf16 v[140:143], a[0:3], v[66:69], 0\n"
      "v_perm_b32 v100, v246, v246, v242\n"
      "v_perm_b32 v101, v246, v246, v243\n"
      "v_mfma_f32_16x16x32_bf16 v[144:147], a[10:13], v[82:85], 0\n"
      "buffer_load_dwordx4 v[14:17], v54, s[36:39], s47 offen\n"
      "v_perm_b32 v116, v246, v246, v240\n"
      "v_perm_b32 v117, v246, v246, v241\n"
      "v_mfma_f32_16x16x32_bf16 v[152:155], a[4:7], v[70:73], 0\n"
      "v_perm_b32 v102, v248, v248, v242\n"
      "v_perm_b32 v103, v248, v248, v243\n"
      "v_mfma_f32_16x16x32_bf16 v[156:159], a[14:17], v[86:89], 0\n"
      "buffer_load_dwordx4 v[20:23], v54, s[36:39], s93 offen\n"
      "v_perm_b32 v118, v248, v248, v240\n"
      "v_perm_b32 v119, v248, v248, v241\n"
      "v_mfma_f32_16x16x32_bf16 v[164:167], a[4:7], v[72:75], 0\n"
      "v_perm_b32 v106, v250, v250, v242\n"
      "v_perm_b32 v107, v250, v250, v243\n"
      "v_mfma_f32_16x16x32_bf16 v[168:171], a[14:17], v[88:91], 0\n"
      "buffer_load_dwordx2 v[26:27], v208, s[36:39], s93 offen\n"
      "v_perm_b32 v122, v250, v250, v240\n"
      "v_perm_b32 v123, v250, v250, v241\n"
      "v_mfma_f32_16x16x32_bf16 v[176:179], a[6:9], v[76:79], 0\n"
      "v_perm_b32 v110, v252, v252, v242\n"
      "v_perm_b32 v111, v252, v252, v243\n"
      "v_mfma_f32_16x16x32_bf16 v[180:183], a[16:19], v[92:95], 0\n"
      "v_perm_b32 v126, v252, v252, v240\n"
      "v_perm_b32 v127, v252, v252, v241\n"
      "s_waitcnt lgkmcnt(0)\n"
      "v_cvt_pk_bf16_f32 v136, v128, v132\n"
      "v_cvt_pk_bf16_f32 v137, v129, v133\n"
      "v_cvt_pk_bf16_f32 v138, v130, v134\n"
      "v_cvt_pk_bf16_f32 v139, v131, v135\n"
      "v_cvt_pk_bf16_f32 v148, v140, v144\n"
      "v_cvt_pk_bf16_f32 v149, v141, v145\n"
      "v_mfma_f32_16x16x32_bf16 v[128:131], v[192:195], v[136:139], 0\n"
      "v_cvt_pk_bf16_f32 v150, v142, v146\n"
      "v_cvt_pk_bf16_f32 v151, v143, v147\n"
      "v_mfma_f32_16x16x32_bf16 v[132:135], a[20:23], v[96:99], 0\n"
      "s_add_u32 m0, s45, 120896\n"
      "s_nop 0\n"
      "global_load_lds_dwordx4 v210, s[16:17]\n"
      "v_cvt_pk_bf16_f32 v160, v152, v156\n"
      "v_cvt_pk_bf16_f32 v161, v153, v157\n"
      "v_mfma_f32_16x16x32_bf16 v[140:143], v[192:195], v[148:151], 0\n"
      "v_cvt_pk_bf16_f32 v162, v154, v158\n"
      "v_cvt_pk_bf16_f32 v163, v155, v159\n"
      "v_mfma_f32_16x16x32_bf16 v[144:147], a[20:23], v[98:101], 0\n"
      "global_load_lds_dwordx4 v39, s[16:17] offset:1024\n"
      "v_cvt_pk_bf16_f32 v172, v164, v168\n"
      "v_cvt_pk_bf16_f32 v173, v165, v169\n"
      "v_mfma_f32_16x16x32_bf16 v[152:155], v[192:195], v[160:163], 0\n"
      "v_cvt_pk_bf16_f32 v174, v166, v170\n"
      "v_cvt_pk_bf16_f32 v175, v167, v171\n"
      "v_mfma_f32_16x16x32_bf16 v[156:159], a[24:27], v[102:105], 0\n"
      "global_load_lds_dwordx4 v210, s[18:19] offset:2048\n"
      "v_cvt_pk_bf16_f32 v184, v176, v180\n"
      "v_cvt_pk_bf16_f32 v185, v177, v181\n"
      "v_mfma_f32_16x16x32_bf16 v[164:167], v[192:195], v[172:175], 0\n"
      "v_cvt_pk_bf16_f32 v186, v178, v182\n"
      "v_cvt_pk_bf16_f32 v187, v179, v183\n"
      "v_mfma_f32_16x16x32_bf16 v[168:171], a[24:27], v[104:107], 0\n"
      "global_load_lds_dwordx4 v39, s[18:19] offset:3072\n"
      "v_cvt_pk_bf16_f32 v136, v128, v132\n"
      "v_cvt_pk_bf16_f32 v137, v129, v133\n"
      "v_mfma_f32_16x16x32_bf16 v[176:179], v[192:195], v[184:187], 0\n"
      "v_cvt_pk_bf16_f32 v138, v130, v134\n"
      "v_cvt_pk_bf16_f32 v139, v131, v135\n"
      "v_mfma_f32_16x16x32_bf16 v[180:183], a[26:29], v[108:111], 0\n"
      "v_cvt_pk_bf16_f32 v148, v140, v144\n"
      "v_cvt_pk_bf16_f32 v149, v141, v145\n"
      "v_mfma_f32_16x16x32_bf16 v[128:131], v[192:195], v[136:139], 0\n"
      "v_cvt_pk_bf16_f32 v150, v142, v146\n"
      "v_cvt_pk_bf16_f32 v151, v143, v147\n"
      "v_mfma_f32_16x16x32_bf16 v[132:135], a[30:33], v[112:115], 0\n"
      "v_cvt_pk_bf16_f32 v160, v152, v156\n"
      "v_cvt_pk_bf16_f32 v161, v153, v157\n"
      "v_mfma_f32_16x16x32_bf16 v[140:143], v[192:195], v[148:151], 0\n"
      "v_cvt_pk_bf16_f32 v162, v154, v158\n"
      "v_cvt_pk_bf16_f32 v163, v155, v159\n"
      "v_mfma_f32_16x16x32_bf16 v[144:147], a[30:33], v[114:117], 0\n"
      "v_cvt_pk_bf16_f32 v172, v164, v168\n"
      "v_cvt_pk_bf16_f32 v173, v165, v169\n"
      "v_mfma_f32_16x16x32_bf16 v[152:155], v[192:195], v[160:163], 0\n"
      "v_cvt_pk_bf16_f32 v174, v166, v170\n"
      "v_cvt_pk_bf16_f32 v175, v167, v171\n"
      "v_mfma_f32_16x16x32_bf16 v[156:159], a[34:37], v[118:121], 0\n"
      "v_cvt_pk_bf16_f32 v184, v176, v180\n"
      "v_cvt_pk_bf16_f32 v185, v177, v181\n"
      "v_mfma_f32_16x16x32_bf16 v[164:167], v[192:195], v[172:175], 0\n"
      "v_cvt_pk_bf16_f32 v186, v178, v182\n"
      "v_cvt_pk_bf16_f32 v187, v179, v183\n"
      "v_mfma_f32_16x16x32_bf16 v[168:171], a[34:37], v[120:123], 0\n"
      "v_cvt_pk_bf16_f32 v136, v128, v132\n"
      "v_cvt_pk_bf16_f32 v137, v129, v133\n"
      "v_mfma_f32_16x16x32_bf16 v[176:179], v[192:195], v[184:187], 0\n"
      "v_cvt_pk_bf16_f32 v138, v130, v134\n"
      "v_cvt_pk_bf16_f32 v139, v131, v135\n"
      "v_mfma_f32_16x16x32_bf16 v[180:183], a[36:39], v[124:127], 0\n"
      "v_cvt_pk_bf16_f32 v148, v140, v144\n"
      "v_cvt_pk_bf16_f32 v149, v141, v145\n"
      "v_mfma_f32_16x16x32_bf16 v[128:131], v[192:195], v[136:139], 0\n"
      "v_cvt_pk_bf16_f32 v150, v142, v146\n"
      "v_cvt_pk_bf16_f32 v151, v143, v147\n"
      "v_mfma_f32_16x16x32_bf16 v[132:135], a[40:43], v[32:35], 0\n"
      "v_cvt_pk_bf16_f32 v160, v152, v156\n"
      "v_cvt_pk_bf16_f32 v161, v153, v157\n"
      "v_mfma_f32_16x16x32_bf16 v[140:143], v[192:195], v[148:151], 0\n"
      "v_cvt_pk_bf16_f32 v162, v154, v158\n"
      "v_cvt_pk_bf16_f32 v163, v155, v159\n"
      "v_mfma_f32_16x16x32_bf16 v[144:147], a[40:43], v[34:37], 0\n"
      "v_cvt_pk_bf16_f32 v172, v164, v168\n"
      "v_cvt_pk_bf16_f32 v173, v165, v169\n"
      "v_mfma_f32_16x16x32_bf16 v[152:155], v[192:195], v[160:163], 0\n"
      "v_cvt_pk_bf16_f32 v174, v166, v170\n"
      "v_cvt_pk_bf16_f32 v175, v167, v171\n"
      "v_mfma_f32_16x16x32_bf16 v[156:159], a[44:47], v[32:35], 0\n"
      "v_cvt_pk_bf16_f32 v184, v176, v180\n"
      "v_cvt_pk_bf16_f32 v185, v177, v181\n"
      "v_mfma_f32_16x16x32_bf16 v[164:167], v[192:195], v[172:175], 0\n"
      "v_cvt_pk_bf16_f32 v186, v178, v182\n"
      "v_cvt_pk_bf16_f32 v187, v179, v183\n"
      "v_mfma_f32_16x16x32_bf16 v[168:171], a[44:47], v[34:37], 0\n"
      "v_cvt_pk_bf16_f32 v128, 0, v128\n"
      "v_cvt_pk_bf16_f32 v129, 0, v129\n"
      "v_mfma_f32_16x16x32_bf16 v[176:179], v[192:195], v[184:187], 0\n"
      "v_cvt_pk_bf16_f32 v130, 0, v130\n"
      "v_cvt_pk_bf16_f32 v131, 0, v131\n"
      "v_mfma_f32_16x16x32_bf16 v[180:183], a[46:49], v[34:37], 0\n"
      "v_mul_f32 v128, v128, v132\n"
      "v_mul_f32 v129, v129, v133\n"
      "v_mul_f32 v130, v130, v134\n"
      "v_mul_f32 v131, v131, v135\n"
      "v_cvt_pk_bf16_f32 v136, v131, v130\n"
      "v_cvt_pk_bf16_f32 v137, v129, v128\n"
      "v_cvt_pk_bf16_f32 v140, 0, v140\n"
      "v_cvt_pk_bf16_f32 v141, 0, v141\n"
      "v_cvt_pk_bf16_f32 v142, 0, v142\n"
      "v_cvt_pk_bf16_f32 v143, 0, v143\n"
      "ds_write_b64 v200, v[136:137] offset:0\n"
      "v_mul_f32 v140, v140, v144\n"
      "v_mul_f32 v141, v141, v145\n"
      "v_mul_f32 v142, v142, v146\n"
      "v_mul_f32 v143, v143, v147\n"
      "v_cvt_pk_bf16_f32 v148, v143, v142\n"
      "v_cvt_pk_bf16_f32 v149, v141, v140\n"
      "v_cvt_pk_bf16_f32 v152, 0, v152\n"
      "v_cvt_pk_bf16_f32 v153, 0, v153\n"
      "v_cvt_pk_bf16_f32 v154, 0, v154\n"
      "v_cvt_pk_bf16_f32 v155, 0, v155\n"
      "ds_write_b64 v200, v[148:149] offset:2304\n"
      "v_mul_f32 v152, v152, v156\n"
      "v_mul_f32 v153, v153, v157\n"
      "v_mul_f32 v154, v154, v158\n"
      "v_mul_f32 v155, v155, v159\n"
      "v_cvt_pk_bf16_f32 v160, v155, v154\n"
      "v_cvt_pk_bf16_f32 v161, v153, v152\n"
      "v_cvt_pk_bf16_f32 v164, 0, v164\n"
      "v_cvt_pk_bf16_f32 v165, 0, v165\n"
      "v_cvt_pk_bf16_f32 v166, 0, v166\n"
      "v_cvt_pk_bf16_f32 v167, 0, v167\n"
      "ds_write_b64 v200, v[160:161] offset:4608\n"
      "v_mul_f32 v164, v164, v168\n"
      "v_mul_f32 v165, v165, v169\n"
      "v_mul_f32 v166, v166, v170\n"
      "v_mul_f32 v167, v167, v171\n"
      "v_cvt_pk_bf16_f32 v172, v167, v166\n"
      "v_cvt_pk_bf16_f32 v173, v165, v164\n"
      "v_cvt_pk_bf16_f32 v176, 0, v176\n"
      "v_cvt_pk_bf16_f32 v177, 0, v177\n"
      "v_cvt_pk_bf16_f32 v178, 0, v178\n"
      "v_cvt_pk_bf16_f32 v179, 0, v179\n"
      "ds_write_b64 v200, v[172:173] offset:6912\n"
      "v_mul_f32 v176, v176, v180\n"
      "v_mul_f32 v177, v177, v181\n"
      "v_mul_f32 v178, v178, v182\n"
      "v_mul_f32 v179, v179, v183\n"
      "v_cvt_pk_bf16_f32 v184, v179, v178\n"
      "v_cvt_pk_bf16_f32 v185, v177, v176\n"
      "ds_write_b64 v200, v[184:185] offset:9216\n"
      "s_waitcnt lgkmcnt(0)\n"
      "s_barrier\n"
      "ds_read_b128 v[64:67], v201 offset:0\n"
      "ds_read_b128 v[68:71], v201 offset:1152\n"
      "ds_read_b128 v[72:75], v201 offset:2304\n"
      "s_waitcnt lgkmcnt(2)\n"
      "buffer_store_dwordx4 v[64:67], v202, s[32:35], s60 offen offset:0 sc1\n"
      "v_add3_u32 v76, v64, v65, v66\n"
      "v_add_u32 v76, v76, v67\n"
      "s_waitcnt lgkmcnt(1)\n"
      "buffer_store_dwordx4 v[68:71], v202, s[32:35], s40 offen offset:0 sc1\n"
      "v_add3_u32 v77, v68, v69, v70\n"
      "v_add_u32 v77, v77, v71\n"
      "s_waitcnt lgkmcnt(0)\n"
      "s_mov_b32 exec_hi, 0\n"
      "buffer_store_dwordx4 v[72:75], v202, s[32:35], s41 offen offset:0 sc1\n"
      "s_mov_b32 exec_hi, -1\n"
      "v_add3_u32 v78, v72, v73, v74\n"
      "v_add_u32 v78, v78, v75\n"
      "v_add_u32_dpp v76, v76, v76 quad_perm:[1,0,3,2] row_mask:0xf bank_mask:0xf\n"
      "v_add_u32_dpp v77, v77, v77 quad_perm:[1,0,3,2] row_mask:0xf bank_mask:0xf\n"
      "v_add_u32_dpp v78, v78, v78 quad_perm:[1,0,3,2] row_mask:0xf bank_mask:0xf\n"
      "v_add_u32_dpp v76, v76, v76 quad_perm:[2,3,0,1] row_mask:0xf bank_mask:0xf\n"
      "v_add_u32_dpp v77, v77, v77 quad_perm:[2,3,0,1] row_mask:0xf bank_mask:0xf\n"
      "v_add_u32_dpp v78, v78, v78 quad_perm:[2,3,0,1] row_mask:0xf bank_mask:0xf\n"
      "v_add_u32_dpp v76, v76, v76 row_half_mirror row_mask:0xf bank_mask:0xf\n"
      "v_add_u32_dpp v77, v77, v77 row_half_mirror row_mask:0xf bank_mask:0xf\n"
      "v_add_u32_dpp v78, v78, v78 row_half_mirror row_mask:0xf bank_mask:0xf\n"
      "v_cndmask_b32 v76, v76, v77, s[48:49]\n"
      "v_cndmask_b32 v76, v76, v78, s[50:51]\n"
      "v_sub_u32 v76, s30, v76\n"
      "buffer_store_dword v76, v203, s[32:35], s61 offen offset:0 sc1\n"
      "s_waitcnt vmcnt(12)\n"
      "v_lshlrev_b32 v38, 16, v235\n"
      "v_mul_f32 v172, 0xbfb8aa3b, v38\n"
      "v_exp_f32 v172, v172\n"
      "s_nop 0\n"
      "v_add_f32 v172, 1.0, v172\n"
      "v_rcp_f32 v172, v172\n"
      "s_nop 0\n"
      "v_mul_f32 v38, v38, v172\n"
      "v_cvt_pk_bf16_f32 v38, v38, v38\n"
      "ds_write_b16 v204, v38\n"
      "s_waitcnt lgkmcnt(0)\n"
      "s_barrier\n"
      "ds_read_b128 v[144:147], v206 offset:0\n"
      "ds_read_b64_tr_b16 v[148:149], v205 offset:0\n"
      "ds_read_b64_tr_b16 v[150:151], v205 offset:2048\n"
      "ds_read_b128 v[152:155], v206 offset:64\n"
      "ds_read_b64_tr_b16 v[156:157], v205 offset:4096\n"
      "ds_read_b64_tr_b16 v[158:159], v205 offset:6144\n"
      "ds_read_b128 v[160:163], v206 offset:128\n"
      "ds_read_b64_tr_b16 v[164:165], v205 offset:8192\n"
      "ds_read_b64_tr_b16 v[166:167], v205 offset:10240\n"
      "ds_read_b128 v[168:171], v206 offset:192\n"
      "ds_read_b64_tr_b16 v[172:173], v205 offset:12288\n"
      "ds_read_b64_tr_b16 v[174:175], v205 offset:14336\n"
      "ds_read_b128 v[176:179], v206 offset:256\n"
      "ds_read_b64_tr_b16 v[180:181], v205 offset:16384\n"
      "ds_read_b64_tr_b16 v[182:183], v205 offset:18432\n"
      "s_waitcnt lgkmcnt(12)\n"
      "v_mfma_f32_16x16x32_bf16 v[184:187], v[144:147], v[148:151], 0\n"
      "s_waitcnt lgkmcnt(9)\n"
      "v_mfma_f32_16x16x32_bf16 v[184:187], v[152:155], v[156:159], v[184:187]\n"
      "s_waitcnt lgkmcnt(6)\n"
      "v_mfma_f32_16x16x32_bf16 v[184:187], v[160:163], v[164:167], v[184:187]\n"
      "s_waitcnt lgkmcnt(3)\n"
      "v_mfma_f32_16x16x32_bf16 v[184:187], v[168:171], v[172:175], v[184:187]\n"
      "s_waitcnt lgkmcnt(0)\n"
      "v_mfma_f32_16x16x32_bf16 v[184:187], v[176:179], v[180:183], v[184:187]\n"
      "s_nop 7\n"
      "v_mov_b32 v185, s30\n"
      "buffer_store_dwordx2 v[184:185], v207, s[32:35], s62 offen offset:0 sc1\n"
      "s_mul_i32 s42, s3, 2560\n"
      "s_add_u32 m0, s42, 110656\n"
      "s_mov_b32 s36, s4\n"
      "s_and_b32 s37, s5, 0xffff\n"
      "buffer_load_dwordx4 v232, s[36:39], s52 offen lds\n"
      "s_add_u32 m0, m0, 1024\n"
      "s_nop 0\n"
      "buffer_load_dwordx4 v233, s[36:39], s55 offen lds\n"
      "s_add_u32 m0, m0, 1024\n"
      "s_mov_b32 exec_hi, 0\n"
      "buffer_load_dwordx4 v234, s[36:39], s55 offen lds\n"
      "s_mov_b32 exec_hi, -1\n"
      "buffer_load_dwordx4 v[112:115], v211, s[36:39], s66 offen\n"
      "buffer_load_dwordx4 v[116:119], v211, s[36:39], s66 offen offset:512\n"
      "v_bfe_u32 v2, v54, 1, 8\n"
      "v_sub_u32 v2, v211, v2\n"
      "buffer_load_dwordx2 v[120:121], v2, s[36:39], s66 offen offset:1024\n"
      : STATE, "=&{s[32:35]}"(s32), "=&{v[8:23]}"(v8), "=&{v[24:27]}"(v24), "=&{v[52:55]}"(v52), "=&{v[112:127]}"(v112), "+{v[192:195]}"(t[0]), "+{v[240:243]}"(t[1]), "+{v[196:199]}"(t[2]), "+{v[200:203]}"(t[3]), "+{v[204:207]}"(t[4]), "+{v[208:211]}"(t[5]), "+{v[212:215]}"(t[6]), "+{v[216:219]}"(t[7]), "+{v[220:223]}"(t[8])
      : "{s2}"(blockIdx.x)
      : "memory", "scc", "vcc", "s40", "s41", "s42", "s43", "s44", "s45", "s46", "s47", "s48", "s49", "s50", "s51", "v2", "v3", "v4", "v5", "v6", "v7", "v28", "v29", "v30", "v31", "v32", "v33", "v34", "v35", "v36", "v37", "v38", "v39", "v40", "v41", "v42", "v43", "v44", "v45", "v46", "v47", "v48", "v49", "v50", "v51", "v56", "v57", "v58", "v59", "v60", "v61", "v62", "v63", "v64", "v65", "v66", "v67", "v68", "v69", "v70", "v71", "v72", "v73", "v74", "v75", "v76", "v77", "v78", "v79", "v80", "v81", "v82", "v83", "v84", "v85", "v86", "v87", "v88", "v89", "v90", "v91", "v92", "v93", "v94", "v95", "v96", "v97", "v98", "v99", "v100", "v101", "v102", "v103", "v104", "v105", "v106", "v107", "v108", "v109", "v110", "v111", "v128", "v129", "v130", "v131", "v132", "v133", "v134", "v135", "v136", "v137", "v138", "v139", "v140", "v141", "v142", "v143", "v144", "v145", "v146", "v147", "v148", "v149", "v150", "v151", "v152", "v153", "v154", "v155", "v156", "v157", "v158", "v159", "v160", "v161", "v162", "v163", "v164", "v165", "v166", "v167", "v168", "v169", "v170", "v171", "v172", "v173", "v174", "v175", "v176", "v177", "v178", "v179", "v180", "v181", "v182", "v183", "v184", "v185", "v186", "v187", "v188", "v189", "v190", "v191", "v224", "v225", "v226", "v227", "v228", "v229", "v230", "v231", "v236", "v237", "v238", "v239", "v244", "v245", "v246", "v247", "v248", "v249", "v250", "v251", "v252", "v253", "v254", "v255", "a0", "a1", "a2", "a3", "a4", "a5", "a6", "a7", "a8", "a9", "a10", "a11", "a12", "a13", "a14", "a15", "a16", "a17", "a18", "a19", "a20", "a21", "a22", "a23", "a24", "a25", "a26", "a27", "a28", "a29", "a30", "a31", "a32", "a33", "a34", "a35", "a36", "a37", "a38", "a39", "a40", "a41", "a42", "a43", "a44", "a45", "a46", "a47", "a48", "a49", "a50", "a51", "a52", "a53", "a54", "a55", "a56", "a57", "a58", "a59", "a60", "a61", "a62", "a63", "a64", "a65", "a66", "a67", "a68", "a69", "a70", "a71", "a72", "a73", "a74", "a75", "a76", "a77", "a78", "a79", "a80", "a81", "a82", "a83", "a84", "a85", "a86", "a87", "a88", "a89", "a90", "a91", "a92", "a93", "a94", "a95", "a96", "a97", "a98", "a99", "a100", "a101", "a102", "a103", "a104", "a105", "a106", "a107", "a108", "a109", "a110", "a111", "a112", "a113", "a114", "a115", "a116", "a117", "a118", "a119", "a120", "a121", "a122", "a123", "a124", "a125", "a126", "a127", "a128", "a129", "a130", "a131", "a132", "a133", "a134", "a135", "a136", "a137", "a138", "a139", "a140", "a141", "a142", "a143", "a144", "a145", "a146", "a147", "a148", "a149", "a150", "a151", "a152", "a153", "a154", "a155", "a156", "a157", "a158", "a159", "a160", "a161", "a162", "a163", "a164", "a165", "a166", "a167", "a168", "a169", "a170", "a171", "a172", "a173", "a174", "a175", "a176", "a177", "a178", "a179", "a180", "a181", "a182", "a183", "a184", "a185", "a186", "a187", "a188", "a189", "a190", "a191", "a192", "a193", "a194", "a195", "a196", "a197", "a198", "a199", "a200", "a201", "a202", "a203", "a204", "a205", "a206", "a207", "a208", "a209", "a210", "a211", "a212", "a213", "a214", "a215", "a216", "a217", "a218", "a219", "a220", "a221", "a222", "a223", "a224", "a225", "a226", "a227", "a228", "a229", "a230", "a231", "a232", "a233", "a234", "a235", "a236", "a237", "a238", "a239", "a240", "a241", "a242", "a243", "a244", "a245", "a246", "a247", "a248", "a249", "a250", "a251", "a252", "a253", "a254", "a255");
  {
    const int lane = tid & 63, ip = lane & 15, fq = lane >> 4, q4 = ip >> 2, p4 = lane & 3;
    const int ck = 40 * w + (lane < 40 ? lane : 39);
    const bool dg = fq == q4;
    const unsigned msk = dg ? ((ip & 1) ? 0xFFFF0000u : 0xFFFFu) : 0u, mk0 = (ip & 2) ? 0u : msk, mk1 = (ip & 2) ? msk : 0u;
    const int fo = (ip & 3) * KCP + 2 * ((ip & 12) + 8 * (fq & 1) + 256 * (fq >> 1)), go = L_XI + (15 - ip + 8 * (fq >> 1)) * 96 + 16 * (fq & 1);
    const int exw = L_EX + ip * 128 + (fq ^ (ip & 7)) * 16, z7 = ((tid < 84 ? tid : 83) * 37) >> 8, rowA = 8 * tid;
    t[9][0] = L_QT + (8 * fq + q4) * 160 + p4 * 8 + w * 32;
    t[9][1] = L_QT + (8 * fq + q4) * 160 + p4 * 8 + 128 + (w & 1) * 5120;
    t[9][2] = (16 * w + ip) * RS_ + fq * 16;
    t[9][3] = w < 2 ? (64 + ip) * RS_ + fq * 16 + w * 64 : 0x80000000u;
    t[10][0] = L_SC + (ip & 4 ? 0 : 64) + fq * 8;
    t[10][1] = (tid >> 3) * RS_;
    t[10][2] = L_XI + (tid >> 2) * 96 + (tid & 3) * 16;
    t[10][3] = (fq >> 1) * 256 + (fq & 1) * 64 + ip * 4;
    t[11][0] = fo + (w >> 1) * KCH + (w & 1) * 256;
    t[11][1] = go + (w >> 1) * XR_CH + (w & 1) * 384;
    t[11][2] = fo + 2 * KCH + w * 128;
    t[11][3] = go + 2 * XR_CH + w * 192;
    t[12][0] = exw + w * 8192;
    t[12][1] = L_EX + 32768 + w * 1024 + (lane >> 3) * 128 + ((lane & 7) ^ (lane >> 3)) * 16;
    t[12][2] = L_XI + ((2040 - rowA) >> 5) * 96 + ((2040 - rowA) & 31) * 2;
    t[12][3] = L_XI + 2 * XR_CH + ((252 - 4 * lane) >> 5) * 96 + ((252 - 4 * lane) & 31) * 2;
    t[13][0] = lane < 8 ? lane * 8 : 0x80000000u;
    t[13][1] = (ip >> 1) == 4 * (fq & 1) ? fq * 8 + (ip & 1) * 4 : 0x80000000u;
    t[13][2] = fq * D_ * 128 + q4 * 32 + p4 * 8;
    t[13][3] = (lane < 40 ? lane : 39) * 64;
    t[14][0] = exw + 32768 + w * 4096;
    t[14][1] = (lane & 1) * 2048 + (lane >> 1) * 16;
    t[14][2] = p4 * 64 + (fq == 1 ? 256 * 32 : 0) + (q4 < 3 ? q4 : 4) * 20480;
    t[14][3] = (exw + w * 8192) ^ 64;
    t[15][0] = (tid >> 3) * 5120 + ((tid >> 2) & 1) * 64;
    t[15][1] = fq * 5120 + ((lane >> 3) & 1) * 64;
    t[15][2] = (exw + 32768 + w * 4096) ^ 64;
    t[15][3] = (2 * (lane >> 5) + (q4 >> 1)) * (D_ * 128) + (2 * (fq & 1) + (q4 & 1)) * 32 + p4 * 8;
    t[16][0] = L_GW + w * 2048 + (4 * fq + q4) * 64 + p4 * 8;
    t[16][1] = (rowA >> 6) * D_ * 128 + ((lane >> 1) & 3) * 32 + (lane & 1) * 16;
    t[16][2] = ((lane >> 1) & 3) == 2 * ((lane >> 3) & 1) ? w * 128 + (lane >> 3) * 8 + (lane & 1) * 4 : 0x80000000u;
    t[16][3] = L_EX + w * 2048 + (lane >> 2) * 128 + ((2 * p4) ^ ((lane >> 2) & 7)) * 16;
    t[17][0] = lane * 8;
    t[17][1] = lane & 4 ? 0x80000000u : (16 * w + 4 * (lane & 7) + fq) * 512 + ((lane >> 3) & 1) * 64;
    t[17][2] = (4 * fq + q4) * 128 + ((2 * fq + (q4 >> 1)) & 3) * 32 + p4 * 8;
    t[17][3] = lane * 16;
  }
  asm volatile(
      "v_mov_b32 v34, 0\n"
      "v_mov_b32 v35, 0\n"
      "v_mov_b32 v228, 0\n"
      "v_mov_b32 v229, 0\n"
      "v_mov_b32 v230, 0\n"
      "v_mov_b32 v231, 0\n"
      "s_mov_b32 s44, 0x40000\n"
      "s_mov_b32 s46, 0\n"
      "s_lshl_b32 s42, s3, 12\n"
      "s_add_u32 s42, s42, 147456\n"
      "v_add_u32 v55, s42, v7\n"
      "ds_write_b64 v217, v[228:229]\n"
      "ds_write_b128 v218, v[228:231] offset:0\n"
      "ds_write_b128 v218, v[228:231] offset:7680\n"
      "ds_write_b128 v218, v[228:231] offset:15360\n"
      "s_lshr_b32 s43, s96, 16\n"
      "s_lshr_b32 s42, s93, 8\n"
      "s_and_b32 s42, s42, 0x60\n"
      "v_add_u32 v47, s94, v47\n"
      "s_mov_b32 exec_hi, 0\n"
      "v_add_u32 v58, s43, v58\n"
      "s_mov_b32 exec_hi, -1\n"
      "v_xor_b32 v50, s42, v50\n"
      "v_xor_b32 v29, s97, v29\n"
      "v_add_u32 v5, s90, v5\n"
      "s_lshl_b32 s42, s99, 4\n"
      "s_lshr_b32 s58, s58, s42\n"
      "s_lshr_b32 s59, s59, s42\n"
      "s_lshr_b32 s42, s93, 8\n"
      "s_and_b32 s42, s42, 16\n"
      "s_lshr_b32 s25, s53, s42\n"
      "s_lshl_b32 s14, s58, 16\n"
      "s_lshl_b32 s15, s59, 16\n"
      "s_lshl_b32 s25, s25, 16\n"
      "s_waitcnt vmcnt(0)\n"
      "s_barrier\n"
      "ds_read_b64_tr_b16 v[64:65], v224 offset:0\n"
      "ds_read_b64_tr_b16 v[66:67], v224 offset:640\n"
      "ds_read_b64_tr_b16 v[68:69], v224 offset:5120\n"
      "ds_read_b64_tr_b16 v[70:71], v224 offset:5760\n"
      "ds_read_b64_tr_b16 v[72:73], v225 offset:0\n"
      "ds_read_b64_tr_b16 v[74:75], v225 offset:640\n"
      "ds_read_b64_tr_b16 v[144:145], v219 offset:0\n"
      "ds_read_b64_tr_b16 v[146:147], v219 offset:8192\n"
      "ds_read_b64_tr_b16 v[148:149], v219 offset:2048\n"
      "ds_read_b64_tr_b16 v[150:151], v219 offset:10240\n"
      "ds_read_b64_tr_b16 v[152:153], v213 offset:0\n"
      "ds_read_b64_tr_b16 v[154:155], v213 offset:8192\n"
      "ds_read_b64_tr_b16 v[156:157], v213 offset:2048\n"
      "ds_read_b64_tr_b16 v[158:159], v213 offset:10240\n"
      "v_mov_b32 v12, s6\n"
      "v_mov_b32 v13, s7\n"
      "v_mov_b32 v18, s8\n"
      "v_mov_b32 v19, s9\n"
      "v_mov_b32 v24, s10\n"
      "v_mov_b32 v25, s11\n"
      "v_and_b32 v188, 48, v7\n"
      "v_xor_b32 v188, 48, v188\n"
      "v_lshlrev_b64 v[26:27], v188, v[26:27]\n"
      "v_mov_b32_dpp v12, v8 wave_shl:1 row_mask:0xf bank_mask:0xf\n"
      "v_mov_b32_dpp v13, v9 wave_shl:1 row_mask:0xf bank_mask:0xf\n"
      "v_mov_b32_dpp v18, v14 wave_shl:1 row_mask:0xf bank_mask:0xf\n"
      "v_mov_b32_dpp v19, v15 wave_shl:1 row_mask:0xf bank_mask:0xf\n"
      "v_mov_b32_dpp v24, v20 wave_shl:1 row_mask:0xf bank_mask:0xf\n"
      "v_mov_b32_dpp v25, v21 wave_shl:1 row_mask:0xf bank_mask:0xf\n"
      "v_mov_b32 v122, 0\n"
      "v_mov_b32 v123, 0\n"
      "v_mov_b32 v96, 0x3f803f80\n"
      "v_dot2c_f32_bf16 v122, v112, v96\n"
      "v_dot2c_f32_bf16 v123, v112, v112\n"
      "v_dot2c_f32_bf16 v122, v113, v96\n"
      "v_dot2c_f32_bf16 v123, v113, v113\n"
      "v_dot2c_f32_bf16 v122, v114, v96\n"
      "v_dot2c_f32_bf16 v123, v114, v114\n"
      "v_dot2c_f32_bf16 v122, v115, v96\n"
      "v_dot2c_f32_bf16 v123, v115, v115\n"
      "v_dot2c_f32_bf16 v122, v116, v96\n"
      "v_dot2c_f32_bf16 v123, v116, v116\n"
      "v_dot2c_f32_bf16 v122, v117, v96\n"
      "v_dot2c_f32_bf16 v123, v117, v117\n"
      "v_dot2c_f32_bf16 v122, v118, v96\n"
      "v_dot2c_f32_bf16 v123, v118, v118\n"
      "v_dot2c_f32_bf16 v122, v119, v96\n"
      "v_dot2c_f32_bf16 v123, v119, v119\n"
      "v_dot2c_f32_bf16 v122, v120, v96\n"
      "v_dot2c_f32_bf16 v123, v120, v120\n"
      "v_dot2c_f32_bf16 v122, v121, v96\n"
      "v_dot2c_f32_bf16 v123, v121, v121\n"
      "s_lshr_b32 s42, s92, 12\n"
      "s_lshr_b32 s43, s93, 12\n"
      "s_mulk_i32 s42, 4224\n"
      "s_mulk_i32 s43, 4224\n"
      "s_add_u32 s42, s42, 0x100000\n"
      "s_add_u32 s43, s43, 0x100000\n"
      "buffer_load_dwordx4 v[128:131], v54, s[32:35], s42 offen sc1\n"
      "s_add_u32 s42, s42, 1081344\n"
      "buffer_load_dwordx4 v[136:139], v54, s[32:35], s43 offen sc1\n"
      "buffer_load_dwordx4 v[132:135], v54, s[32:35], s42 offen sc1\n"
      "buffer_load_dword v140, v237, s[32:35], s70 offen sc1\n"
      "buffer_load_dword v141, v237, s[32:35], s98 offen sc1\n"
      "buffer_load_dword v142, v237, s[32:35], s71 offen sc1\n"
      "s_waitcnt lgkmcnt(0)\n"
      "ds_write_b128 v54, v[10:13] offset:12544\n"
      "v_alignbit_b32 v160, v9, v8, 16\n"
      "v_alignbit_b32 v161, v10, v9, 16\n"
      "v_alignbit_b32 v162, v11, v10, 16\n"
      "v_alignbit_b32 v163, v12, v11, 16\n"
      "s_mov_b32 vcc_lo, 0x33333333\n"
      "s_mov_b32 vcc_hi, 0x33333333\n"
      "v_cndmask_b32_dpp v80, v148, v144, vcc quad_perm:[0,1,0,1] row_mask:0xf bank_mask:0xf\n"
      "v_cndmask_b32_dpp v81, v149, v145, vcc quad_perm:[0,1,0,1] row_mask:0xf bank_mask:0xf\n"
      "v_cndmask_b32_dpp v82, v150, v146, vcc quad_perm:[0,1,0,1] row_mask:0xf bank_mask:0xf\n"
      "v_cndmask_b32_dpp v83, v151, v147, vcc quad_perm:[0,1,0,1] row_mask:0xf bank_mask:0xf\n"
      "ds_write_b128 v54, v[160:163] offset:64\n"
      "v_alignbit_b32 v164, v10, v9, 16\n"
      "v_alignbit_b32 v165, v11, v10, 16\n"
      "v_alignbit_b32 v166, v12, v11, 16\n"
      "v_alignbit_b32 v167, v13, v12, 16\n"
      "v_cndmask_b32_dpp v88, v156, v152, vcc quad_perm:[0,1,0,1] row_mask:0xf bank_mask:0xf\n"
      "v_cndmask_b32_dpp v89, v157, v153, vcc quad_perm:[0,1,0,1] row_mask:0xf bank_mask:0xf\n"
      "v_cndmask_b32_dpp v90, v158, v154, vcc quad_perm:[0,1,0,1] row_mask:0xf bank_mask:0xf\n"
      "v_cndmask_b32_dpp v91, v159, v155, vcc quad_perm:[0,1,0,1] row_mask:0xf bank_mask:0xf\n"
      "s_not_b64 vcc, vcc\n"
      "v_cndmask_b32_dpp v84, v144, v148, vcc quad_perm:[2,3,2,3] row_mask:0xf bank_mask:0xf\n"
      "ds_write_b128 v54, v[164:167] offset:8384\n"
      "v_mov_b32 v168, v9\n"
      "v_mov_b32 v169, v10\n"
      "v_mov_b32 v170, v11\n"
      "v_mov_b32 v171, v12\n"
      "v_cndmask_b32_dpp v85, v145, v149, vcc quad_perm:[2,3,2,3] row_mask:0xf bank_mask:0xf\n"
      "v_cndmask_b32_dpp v86, v146, v150, vcc quad_perm:[2,3,2,3] row_mask:0xf bank_mask:0xf\n"
      "v_cndmask_b32_dpp v87, v147, v151, vcc quad_perm:[2,3,2,3] row_mask:0xf bank_mask:0xf\n"
      "v_cndmask_b32_dpp v92, v152, v156, vcc quad_perm:[2,3,2,3] row_mask:0xf bank_mask:0xf\n"
      "v_cndmask_b32_dpp v93, v153, v157, vcc quad_perm:[2,3,2,3] row_mask:0xf bank_mask:0xf\n"
      "v_cndmask_b32_dpp v94, v154, v158, vcc quad_perm:[2,3,2,3] row_mask:0xf bank_mask:0xf\n"
      "ds_write_b128 v54, v[168:171] offset:4224\n"
      "v_cndmask_b32_dpp v95, v155, v159, vcc quad_perm:[2,3,2,3] row_mask:0xf bank_mask:0xf\n"
      "buffer_store_dwordx4 v[80:83], v7, s[32:35], s72 offen offset:0 sc1\n"
      "buffer_store_dwordx4 v[84:87], v7, s[32:35], s73 offen offset:0 sc1\n"
      "buffer_store_dwordx4 v[88:91], v7, s[32:35], s74 offen offset:0 sc1\n"
      "buffer_store_dwordx4 v[92:95], v7, s[32:35], s75 offen offset:0 sc1\n"
      "v_add3_u32 v76, v64, v65, v66\n"
      "ds_write_b128 v54, v[16:19] offset:29184\n"
      "v_alignbit_b32 v172, v15, v14, 16\n"
      "v_alignbit_b32 v173, v16, v15, 16\n"
      "v_alignbit_b32 v174, v17, v16, 16\n"
      "v_alignbit_b32 v175, v18, v17, 16\n"
      "v_add_u32 v76, v76, v67\n"
      "v_add3_u32 v77, v68, v69, v70\n"
      "v_add_f32_dpp v122, v122, v122 quad_perm:[1,0,3,2] row_mask:0xf bank_mask:0xf\n"
      "v_add_u32 v77, v77, v71\n"
      "v_add_f32_dpp v123, v123, v123 quad_perm:[1,0,3,2] row_mask:0xf bank_mask:0xf\n"
      "v_add3_u32 v78, v72, v73, v74\n"
      "ds_write_b128 v54, v[172:175] offset:16704\n"
      "v_alignbit_b32 v176, v16, v15, 16\n"
      "v_alignbit_b32 v177, v17, v16, 16\n"
      "v_alignbit_b32 v178, v18, v17, 16\n"
      "v_alignbit_b32 v179, v19, v18, 16\n"
      "v_add_f32_dpp v122, v122, v122 quad_perm:[2,3,0,1] row_mask:0xf bank_mask:0xf\n"
      "v_add_u32 v78, v78, v75\n"
      "v_add_f32_dpp v123, v123, v123 quad_perm:[2,3,0,1] row_mask:0xf bank_mask:0xf\n"
      "v_mov_b32 v79, v78\n"
      "v_add_f32_dpp v122, v122, v122 row_half_mirror row_mask:0xf bank_mask:0xf\n"
      "v_permlane32_swap_b32 v76, v77\n"
      "ds_write_b128 v54, v[176:179] offset:25024\n"
      "v_mov_b32 v180, v15\n"
      "v_mov_b32 v181, v16\n"
      "v_mov_b32 v182, v17\n"
      "v_mov_b32 v183, v18\n"
      "v_add_f32_dpp v123, v123, v123 row_half_mirror row_mask:0xf bank_mask:0xf\n"
      "v_permlane32_swap_b32 v78, v79\n"
      "v_add_f32_dpp v122, v122, v122 row_mirror row_mask:0xf bank_mask:0xf\n"
      "v_add_u32 v76, v76, v77\n"
      "v_add_f32_dpp v123, v123, v123 row_mirror row_mask:0xf bank_mask:0xf\n"
      "v_add_u32 v78, v78, v79\n"
      "ds_write_b128 v54, v[180:183] offset:20864\n"
      "v_mov_b32 v96, v122\n"
      "v_permlane16_swap_b32 v76, v78\n"
      "v_mov_b32 v97, v123\n"
      "v_add_u32 v76, v76, v78\n"
      "v_permlane16_swap_b32 v122, v96\n"
      "v_sub_u32 v76, s30, v76\n"
      "ds_write_b128 v54, v[22:25] offset:45824\n"
      "v_alignbit_b32 v160, v21, v20, 16\n"
      "v_alignbit_b32 v161, v22, v21, 16\n"
      "v_alignbit_b32 v162, v23, v22, 16\n"
      "v_alignbit_b32 v163, v24, v23, 16\n"
      "v_permlane16_swap_b32 v123, v97\n"
      "v_add_f32 v122, v122, v96\n"
      "v_add_f32 v123, v123, v97\n"
      "v_mul_f32 v122, 0x3acccccd, v122\n"
      "v_mul_f32 v123, 0x3acccccd, v123\n"
      "v_fma_f32 v123, -v122, v122, v123\n"
      "ds_write_b128 v54, v[160:163] offset:33344\n"
      "v_alignbit_b32 v164, v22, v21, 16\n"
      "v_alignbit_b32 v165, v23, v22, 16\n"
      "v_alignbit_b32 v166, v24, v23, 16\n"
      "v_alignbit_b32 v167, v25, v24, 16\n"
      "v_max_f32 v123, 0, v123\n"
      "v_cvt_pk_bf16_f32 v123, 0, v123\n"
      "v_add_f32 v123, 0x3727c5ac, v123\n"
      "v_cvt_pk_bf16_f32 v123, 0, v123\n"
      "v_sqrt_f32 v123, v123\n"
      "v_readlane_b32 s42, v122, 32\n"
      "ds_write_b128 v54, v[164:167] offset:41664\n"
      "v_mov_b32 v168, v21\n"
      "v_mov_b32 v169, v22\n"
      "v_mov_b32 v170, v23\n"
      "v_mov_b32 v171, v24\n"
      "v_cvt_pk_bf16_f32 v123, 0, v123\n"
      "v_rcp_f32 v96, v123\n"
      "v_mov_b32 v99, s30\n"
      "v_cvt_pk_bf16_f32 v98, v122, s42\n"
      "v_readlane_b32 s42, v96, 32\n"
      "s_nop 1\n"
      "v_mov_b32 v97, s42\n"
      "ds_write_b128 v54, v[168:171] offset:37504\n"
      "buffer_store_dwordx4 v[96:99], v214, s[32:35], s67 offen offset:0 sc1\n"
      "buffer_store_dwordx4 v[64:67], v226, s[32:35], s64 offen offset:0 sc1\n"
      "buffer_store_dwordx4 v[68:71], v226, s[32:35], s64 offen offset:64 sc1\n"
      "buffer_store_dwordx4 v[72:75], v227, s[32:35], s64 offen offset:0 sc1\n"
      "buffer_store_dword v76, v215, s[32:35], s65 offen offset:0 sc1\n"
      "ds_write_b64 v216, v[26:27]\n"
      "v_mov_b64 v[8:9], v[80:81]\n"
      "v_mov_b64 v[10:11], v[82:83]\n"
      "v_mov_b64 v[12:13], v[84:85]\n"
      "v_mov_b64 v[14:15], v[86:87]\n"
      "v_mov_b64 v[16:17], v[88:89]\n"
      "v_mov_b64 v[18:19], v[90:91]\n"
      "v_mov_b64 v[20:21], v[92:93]\n"
      "v_mov_b64 v[22:23], v[94:95]\n"
      "v_mov_b32 v2, v212\n"
      "v_mov_b32 v3, v220\n"
      "s_mov_b32 s45, s68\n"
      "s_lshr_b32 s42, s93, 8\n"
      "s_and_b32 s42, s42, 0x60\n"
      "v_xor_b32 v36, s97, v221\n"
      "v_xor_b32 v37, s42, v63\n"
      "s_add_u32 s55, s78, 0x1000000\n"
      "s_mov_b32 s59, s69\n"
      "s_lshr_b32 s53, s92, 12\n"
      "s_mulk_i32 s53, 4224\n"
      "s_add_u32 s53, s53, 0x400000\n"
      "s_add_u32 s54, s53, 1081344\n"
      "s_waitcnt vmcnt(9)\n"
      "v_add3_u32 v143, v128, v129, v130\n"
      "v_add3_u32 v143, v143, v131, v132\n"
      "v_add3_u32 v143, v143, v133, v134\n"
      "v_add3_u32 v143, v143, v135, v136\n"
      "v_add3_u32 v143, v143, v137, v138\n"
      "v_add_u32 v143, v143, v139\n"
      "v_add3_u32 v140, v140, v141, v142\n"
      "s_mul_i32 s47, s30, 3\n"
      "v_add_u32_dpp v143, v143, v143 quad_perm:[1,0,3,2] row_mask:0xf bank_mask:0xf\n"
      "s_nop 1\n"
      "v_add_u32_dpp v143, v143, v143 quad_perm:[2,3,0,1] row_mask:0xf bank_mask:0xf\n"
      "s_nop 1\n"
      "v_add_u32_dpp v143, v143, v143 row_half_mirror row_mask:0xf bank_mask:0xf\n"
      "v_add_u32 v143, v143, v140\n"
      "v_cmp_ne_u32 vcc, s47, v143\n"
      "s_cbranch_vccnz L_psx_%=\n"
      "L_pbx_%=:\n"
      "ds_write_b128 v238, v[128:131] offset:0\n"
      "ds_write_b128 v238, v[132:135] offset:7680\n"
      "ds_write_b128 v238, v[136:139] offset:15360\n"
      "s_waitcnt vmcnt(5)\n"
      "buffer_store_dwordx2 v[52:53], v48, s[32:35], s79 offen offset:0 sc1\n"
      "s_mul_i32 s42, s3, 2048\n"
      "s_add_u32 m0, s42, 137280\n"
      "s_waitcnt lgkmcnt(0)\n"
      "s_barrier\n"
      "s_cmp_lg_u32 s99, 0\n"
      "s_cbranch_scc1 L_cv_b%=\n"
      "ds_read_b128 v[160:163], v43 offset:4608\n"
      "ds_read_b64 v[112:113], v42 offset:0\n"
      "ds_read_b64 v[114:115], v42 offset:8\n"
      "ds_read_b64 v[148:149], v42 offset:3072\n"
      "ds_read_b64 v[150:151], v42 offset:3080\n"
      "ds_read_b128 v[172:175], v43 offset:0\n"
      "ds_read_b128 v[168:171], v43 offset:1536\n"
      "ds_read_b64 v[124:125], v42 offset:1024\n"
      "ds_read_b64 v[126:127], v42 offset:1032\n"
      "ds_read_b128 v[164:167], v43 offset:3072\n"
      "ds_read_b64 v[136:137], v42 offset:2048\n"
      "ds_read_b64 v[138:139], v42 offset:2056\n"
      "ds_read_b64 v[116:117], v42 offset:32\n"
      "ds_read_b64 v[118:119], v42 offset:40\n"
      "ds_read_b64 v[128:129], v42 offset:1056\n"
      "ds_read_b64 v[130:131], v42 offset:1064\n"
      "ds_read_b64 v[140:141], v42 offset:2080\n"
      "ds_read_b64 v[142:143], v42 offset:2088\n"
      "ds_read_b64 v[152:153], v42 offset:3104\n"
      "ds_read_b64 v[154:155], v42 offset:3112\n"
      "s_waitcnt lgkmcnt(8)\n"
      "v_mfma_f32_16x16x32_bf16 v[96:99], v[112:115], v[160:163], 0\n"
      "ds_read_b128 v[176:179], v43 offset:4640\n"
      "ds_read_b128 v[180:183], v43 offset:3104\n"
      "v_mfma_f32_16x16x32_bf16 v[104:107], v[148:151], v[160:163], 0\n"
      "ds_read_b128 v[184:187], v43 offset:1568\n"
      "v_mfma_f32_16x16x32_bf16 v[104:107], v[112:115], v[172:175], v[104:107]\n"
      "ds_read_b128 v[188:191], v43 offset:32\n"
      "v_mfma_f32_16x16x32_bf16 v[104:107], v[124:127], v[168:171], v[104:107]\n"
      "ds_read_b64 v[120:121], v42 offset:64\n"
      "ds_read_b64 v[122:123], v42 offset:72\n"
      "v_mfma_f32_16x16x32_bf16 v[104:107], v[136:139], v[164:167], v[104:107]\n"
      "ds_read_b64 v[132:133], v42 offset:1088\n"
      "s_waitcnt lgkmcnt(7)\n"
      "v_mfma_f32_16x16x32_bf16 v[100:103], v[116:119], v[160:163], 0\n"
      "ds_read_b64 v[134:135], v42 offset:1096\n"
      "v_mfma_f32_16x16x32_bf16 v[108:111], v[152:155], v[160:163], 0\n"
      "ds_read_b64 v[144:145], v42 offset:2112\n"
      "v_mfma_f32_16x16x32_bf16 v[108:111], v[116:119], v[172:175], v[108:111]\n"
      "ds_read_b64 v[146:147], v42 offset:2120\n"
      "v_mfma_f32_16x16x32_bf16 v[108:111], v[128:131], v[168:171], v[108:111]\n"
      "ds_read_b64 v[156:157], v42 offset:3136\n"
      "v_mfma_f32_16x16x32_bf16 v[108:111], v[140:143], v[164:167], v[108:111]\n"
      "ds_read_b64 v[158:159], v42 offset:3144\n"
      "s_waitcnt lgkmcnt(8)\n"
      "v_mfma_f32_16x16x32_bf16 v[96:99], v[116:119], v[176:179], v[96:99]\n"
      "ds_read_b128 v[160:163], v43 offset:4704\n"
      "ds_read_b128 v[164:167], v43 offset:3168\n"
      "v_mfma_f32_16x16x32_bf16 v[104:107], v[152:155], v[176:179], v[104:107]\n"
      "ds_read_b128 v[168:171], v43 offset:1632\n"
      "v_mfma_f32_16x16x32_bf16 v[104:107], v[116:119], v[188:191], v[104:107]\n"
      "ds_read_b128 v[172:175], v43 offset:96\n"
      "v_mfma_f32_16x16x32_bf16 v[104:107], v[128:131], v[184:187], v[104:107]\n"
      "ds_read_b64 v[112:113], v42 offset:96\n"
      "ds_read_b64 v[114:115], v42 offset:104\n"
      "v_mfma_f32_16x16x32_bf16 v[104:107], v[140:143], v[180:183], v[104:107]\n"
      "ds_read_b64 v[124:125], v42 offset:1120\n"
      "s_waitcnt lgkmcnt(7)\n"
      "v_mfma_f32_16x16x32_bf16 v[100:103], v[120:123], v[176:179], v[100:103]\n"
      "ds_read_b64 v[126:127], v42 offset:1128\n"
      "v_mfma_f32_16x16x32_bf16 v[108:111], v[156:159], v[176:179], v[108:111]\n"
      "ds_read_b64 v[136:137], v42 offset:2144\n"
      "v_mfma_f32_16x16x32_bf16 v[108:111], v[120:123], v[188:191], v[108:111]\n"
      "ds_read_b64 v[138:139], v42 offset:2152\n"
      "v_mfma_f32_16x16x32_bf16 v[108:111], v[132:135], v[184:187], v[108:111]\n"
      "ds_read_b64 v[148:149], v42 offset:3168\n"
      "v_mfma_f32_16x16x32_bf16 v[108:111], v[144:147], v[180:183], v[108:111]\n"
      "ds_read_b64 v[150:151], v42 offset:3176\n"
      "s_waitcnt lgkmcnt(8)\n"
      "v_mfma_f32_16x16x32_bf16 v[96:99], v[120:123], v[160:163], v[96:99]\n"
      "ds_read_b128 v[176:179], v43 offset:4736\n"
      "ds_read_b128 v[180:183], v43 offset:3200\n"
      "v_mfma_f32_16x16x32_bf16 v[104:107], v[156:159], v[160:163], v[104:107]\n"
      "ds_read_b128 v[184:187], v43 offset:1664\n"
      "v_mfma_f32_16x16x32_bf16 v[104:107], v[120:123], v[172:175], v[104:107]\n"
      "ds_read_b128 v[188:191], v43 offset:128\n"
      "v_mfma_f32_16x16x32_bf16 v[104:107], v[132:135], v[168:171], v[104:107]\n"
      "ds_read_b64 v[116:117], v42 offset:128\n"
      "ds_read_b64 v[118:119], v42 offset:136\n"
      "v_mfma_f32_16x16x32_bf16 v[104:107], v[144:147], v[164:167], v[104:107]\n"
      "ds_read_b64 v[128:129], v42 offset:1152\n"
      "s_waitcnt lgkmcnt(7)\n"
      "v_mfma_f32_16x16x32_bf16 v[100:103], v[112:115], v[160:163], v[100:103]\n"
      "ds_read_b64 v[130:131], v42 offset:1160\n"
      "v_mfma_f32_16x16x32_bf16 v[108:111], v[148:151], v[160:163], v[108:111]\n"
      "ds_read_b64 v[140:141], v42 offset:2176\n"
      "v_mfma_f32_16x16x32_bf16 v[108:111], v[112:115], v[172:175], v[108:111]\n"
      "ds_read_b64 v[142:143], v42 offset:2184\n"
      "v_mfma_f32_16x16x32_bf16 v[108:111], v[124:127], v[168:171], v[108:111]\n"
      "ds_read_b64 v[152:153], v42 offset:3200\n"
      "v_mfma_f32_16x16x32_bf16 v[108:111], v[136:139], v[164:167], v[108:111]\n"
      "ds_read_b64 v[154:155], v42 offset:3208\n"
      "s_waitcnt lgkmcnt(8)\n"
      "v_mfma_f32_16x16x32_bf16 v[96:99], v[112:115], v[176:179], v[96:99]\n"
      "ds_read_b128 v[160:163], v41 offset:4608\n"
      "ds_read_b64 v[120:121], v40 offset:0\n"
      "v_mfma_f32_16x16x32_bf16 v[104:107], v[148:151], v[176:179], v[104:107]\n"
      "ds_read_b64 v[122:123], v40 offset:8\n"
      "ds_read_b128 v[164:167], v41 offset:3072\n"
      "v_mfma_f32_16x16x32_bf16 v[104:107], v[112:115], v[188:191], v[104:107]\n"
      "ds_read_b64 v[132:133], v40 offset:1024\n"
      "ds_read_b64 v[134:135], v40 offset:1032\n"
      "v_mfma_f32_16x16x32_bf16 v[104:107], v[124:127], v[184:187], v[104:107]\n"
      "ds_read_b128 v[168:171], v41 offset:1536\n"
      "ds_read_b64 v[144:145], v40 offset:2048\n"
      "v_mfma_f32_16x16x32_bf16 v[104:107], v[136:139], v[180:183], v[104:107]\n"
      "ds_read_b64 v[146:147], v40 offset:2056\n"
      "ds_read_b128 v[172:175], v41 offset:0\n"
      "s_waitcnt lgkmcnt(10)\n"
      "v_mfma_f32_16x16x32_bf16 v[100:103], v[116:119], v[176:179], v[100:103]\n"
      "ds_read_b64 v[156:157], v40 offset:3072\n"
      "v_mfma_f32_16x16x32_bf16 v[108:111], v[152:155], v[176:179], v[108:111]\n"
      "ds_read_b64 v[158:159], v40 offset:3080\n"
      "v_mfma_f32_16x16x32_bf16 v[108:111], v[116:119], v[188:191], v[108:111]\n"
      "v_mfma_f32_16x16x32_bf16 v[108:111], v[128:131], v[184:187], v[108:111]\n"
      "v_mfma_f32_16x16x32_bf16 v[108:111], v[140:143], v[180:183], v[108:111]\n"
      "s_waitcnt lgkmcnt(0)\n"
      "s_branch L_cv_e%=\n"
      "L_cv_b%=:\n"
      "ds_read_b128 v[164:167], v43 offset:3072\n"
      "ds_read_b64 v[112:113], v42 offset:0\n"
      "ds_read_b64 v[114:115], v42 offset:8\n"
      "ds_read_b64 v[124:125], v42 offset:1024\n"
      "ds_read_b64 v[126:127], v42 offset:1032\n"
      "ds_read_b128 v[160:163], v43 offset:4608\n"
      "ds_read_b64 v[136:137], v42 offset:2048\n"
      "ds_read_b64 v[138:139], v42 offset:2056\n"
      "ds_read_b128 v[168:171], v43 offset:1536\n"
      "ds_read_b64 v[116:117], v42 offset:32\n"
      "ds_read_b64 v[118:119], v42 offset:40\n"
      "ds_read_b64 v[128:129], v42 offset:1056\n"
      "ds_read_b64 v[130:131], v42 offset:1064\n"
      "ds_read_b64 v[140:141], v42 offset:2080\n"
      "ds_read_b64 v[142:143], v42 offset:2088\n"
      "s_waitcnt lgkmcnt(6)\n"
      "v_mfma_f32_16x16x32_bf16 v[96:99], v[112:115], v[164:167], 0\n"
      "ds_read_b128 v[176:179], v43 offset:4640\n"
      "v_mfma_f32_16x16x32_bf16 v[104:107], v[124:127], v[164:167], 0\n"
      "ds_read_b128 v[180:183], v43 offset:3104\n"
      "v_mfma_f32_16x16x32_bf16 v[96:99], v[124:127], v[160:163], v[96:99]\n"
      "ds_read_b128 v[184:187], v43 offset:1568\n"
      "v_mfma_f32_16x16x32_bf16 v[104:107], v[136:139], v[160:163], v[104:107]\n"
      "ds_read_b64 v[120:121], v42 offset:64\n"
      "v_mfma_f32_16x16x32_bf16 v[104:107], v[112:115], v[168:171], v[104:107]\n"
      "ds_read_b64 v[122:123], v42 offset:72\n"
      "s_waitcnt lgkmcnt(5)\n"
      "v_mfma_f32_16x16x32_bf16 v[100:103], v[116:119], v[164:167], 0\n"
      "ds_read_b64 v[132:133], v42 offset:1088\n"
      "v_mfma_f32_16x16x32_bf16 v[108:111], v[128:131], v[164:167], 0\n"
      "ds_read_b64 v[134:135], v42 offset:1096\n"
      "v_mfma_f32_16x16x32_bf16 v[100:103], v[128:131], v[160:163], v[100:103]\n"
      "ds_read_b64 v[144:145], v42 offset:2112\n"
      "v_mfma_f32_16x16x32_bf16 v[108:111], v[140:143], v[160:163], v[108:111]\n"
      "ds_read_b64 v[146:147], v42 offset:2120\n"
      "v_mfma_f32_16x16x32_bf16 v[108:111], v[116:119], v[168:171], v[108:111]\n"
      "s_waitcnt lgkmcnt(6)\n"
      "v_mfma_f32_16x16x32_bf16 v[96:99], v[116:119], v[180:183], v[96:99]\n"
      "ds_read_b128 v[160:163], v43 offset:4704\n"
      "v_mfma_f32_16x16x32_bf16 v[104:107], v[128:131], v[180:183], v[104:107]\n"
      "ds_read_b128 v[164:167], v43 offset:3168\n"
      "v_mfma_f32_16x16x32_bf16 v[96:99], v[128:131], v[176:179], v[96:99]\n"
      "ds_read_b128 v[168:171], v43 offset:1632\n"
      "v_mfma_f32_16x16x32_bf16 v[104:107], v[140:143], v[176:179], v[104:107]\n"
      "ds_read_b64 v[112:113], v42 offset:96\n"
      "v_mfma_f32_16x16x32_bf16 v[104:107], v[116:119], v[184:187], v[104:107]\n"
      "ds_read_b64 v[114:115], v42 offset:104\n"
      "s_waitcnt lgkmcnt(5)\n"
      "v_mfma_f32_16x16x32_bf16 v[100:103], v[120:123], v[180:183], v[100:103]\n"
      "ds_read_b64 v[124:125], v42 offset:1120\n"
      "v_mfma_f32_16x16x32_bf16 v[108:111], v[132:135], v[180:183], v[108:111]\n"
      "ds_read_b64 v[126:127], v42 offset:1128\n"
      "v_mfma_f32_16x16x32_bf16 v[100:103], v[132:135], v[176:179], v[100:103]\n"
      "ds_read_b64 v[136:137], v42 offset:2144\n"
      "v_mfma_f32_16x16x32_bf16 v[108:111], v[144:147], v[176:179], v[108:111]\n"
      "ds_read_b64 v[138:139], v42 offset:2152\n"
      "v_mfma_f32_16x16x32_bf16 v[108:111], v[120:123], v[184:187], v[108:111]\n"
      "s_waitcnt lgkmcnt(6)\n"
      "v_mfma_f32_16x16x32_bf16 v[96:99], v[120:123], v[164:167], v[96:99]\n"
      "ds_read_b128 v[176:179], v43 offset:4736\n"
      "v_mfma_f32_16x16x32_bf16 v[104:107], v[132:135], v[164:167], v[104:107]\n"
      "ds_read_b128 v[180:183], v43 offset:3200\n"
      "v_mfma_f32_16x16x32_bf16 v[96:99], v[132:135], v[160:163], v[96:99]\n"
      "ds_read_b128 v[184:187], v43 offset:1664\n"
      "v_mfma_f32_16x16x32_bf16 v[104:107], v[144:147], v[160:163], v[104:107]\n"
      "ds_read_b64 v[116:117], v42 offset:128\n"
      "v_mfma_f32_16x16x32_bf16 v[104:107], v[120:123], v[168:171], v[104:107]\n"
      "ds_read_b64 v[118:119], v42 offset:136\n"
      "s_waitcnt lgkmcnt(5)\n"
      "v_mfma_f32_16x16x32_bf16 v[100:103], v[112:115], v[164:167], v[100:103]\n"
      "ds_read_b64 v[128:129], v42 offset:1152\n"
      "v_mfma_f32_16x16x32_bf16 v[108:111], v[124:127], v[164:167], v[108:111]\n"
      "ds_read_b64 v[130:131], v42 offset:1160\n"
      "v_mfma_f32_16x16x32_bf16 v[100:103], v[124:127], v[160:163], v[100:103]\n"
      "ds_read_b64 v[140:141], v42 offset:2176\n"
      "v_mfma_f32_16x16x32_bf16 v[108:111], v[136:139], v[160:163], v[108:111]\n"
      "ds_read_b64 v[142:143], v42 offset:2184\n"
      "v_mfma_f32_16x16x32_bf16 v[108:111], v[112:115], v[168:171], v[108:111]\n"
      "s_waitcnt lgkmcnt(6)\n"
      "v_mfma_f32_16x16x32_bf16 v[96:99], v[112:115], v[180:183], v[96:99]\n"
      "ds_read_b128 v[160:163], v41 offset:4608\n"
      "ds_read_b64 v[120:121], v40 offset:0\n"
      "v_mfma_f32_16x16x32_bf16 v[104:107], v[124:127], v[180:183], v[104:107]\n"
      "ds_read_b64 v[122:123], v40 offset:8\n"
      "ds_read_b128 v[164:167], v41 offset:3072\n"
      "v_mfma_f32_16x16x32_bf16 v[96:99], v[124:127], v[176:179], v[96:99]\n"
      "ds_read_b64 v[132:133], v40 offset:1024\n"
      "ds_read_b64 v[134:135], v40 offset:1032\n"
      "v_mfma_f32_16x16x32_bf16 v[104:107], v[136:139], v[176:179], v[104:107]\n"
      "ds_read_b128 v[168:171], v41 offset:1536\n"
      "ds_read_b64 v[144:145], v40 offset:2048\n"
      "v_mfma_f32_16x16x32_bf16 v[104:107], v[112:115], v[184:187], v[104:107]\n"
      "ds_read_b64 v[146:147], v40 offset:2056\n"
      "ds_read_b128 v[172:175], v41 offset:0\n"
      "s_waitcnt lgkmcnt(10)\n"
      "v_mfma_f32_16x16x32_bf16 v[100:103], v[116:119], v[180:183], v[100:103]\n"
      "ds_read_b64 v[156:157], v40 offset:3072\n"
      "v_mfma_f32_16x16x32_bf16 v[108:111], v[128:131], v[180:183], v[108:111]\n"
      "ds_read_b64 v[158:159], v40 offset:3080\n"
      "v_mfma_f32_16x16x32_bf16 v[100:103], v[128:131], v[176:179], v[100:103]\n"
      "v_mfma_f32_16x16x32_bf16 v[108:111], v[140:143], v[176:179], v[108:111]\n"
      "v_mfma_f32_16x16x32_bf16 v[108:111], v[116:119], v[184:187], v[108:111]\n"
      "s_waitcnt lgkmcnt(0)\n"
      "L_cv_e%=:\n"
      "ds_read_b64 v[112:113], v40 offset:32\n"
      "ds_read_b64 v[114:115], v40 offset:40\n"
      "s_add_u32 s8, s26, s91\n"
      "s_addc_u32 s9, s27, 0\n"
      "v_mfma_f32_16x16x32_bf16 v[64:67], v[120:123], v[160:163], 0\n"
      "global_load_lds_dwordx4 v222, s[20:21]\n"
      "ds_read_b64 v[124:125], v40 offset:1056\n"
      "v_mfma_f32_16x16x32_bf16 v[72:75], v[120:123], v[164:167], 0\n"
      "ds_read_b64 v[126:127], v40 offset:1064\n"
      "ds_read_b64 v[136:137], v40 offset:2080\n"
      "v_mfma_f32_16x16x32_bf16 v[80:83], v[120:123], v[168:171], 0\n"
      "ds_read_b64 v[138:139], v40 offset:2088\n"
      "v_mfma_f32_16x16x32_bf16 v[88:91], v[120:123], v[172:175], 0\n"
      "ds_read_b64 v[148:149], v40 offset:3104\n"
      "v_mfma_f32_16x16x32_bf16 v[72:75], v[132:135], v[160:163], v[72:75]\n"
      "ds_read_b64 v[150:151], v40 offset:3112\n"
      "v_mfma_f32_16x16x32_bf16 v[80:83], v[132:135], v[164:167], v[80:83]\n"
      "ds_write_b128 v56, v[96:99]\n"
      "ds_read_b128 v[176:179], v41 offset:4640\n"
      "ds_read_b128 v[180:183], v41 offset:3104\n"
      "ds_read_b128 v[184:187], v41 offset:1568\n"
      "v_mfma_f32_16x16x32_bf16 v[88:91], v[132:135], v[168:171], v[88:91]\n"
      "ds_read_b128 v[188:191], v41 offset:32\n"
      "v_mfma_f32_16x16x32_bf16 v[80:83], v[144:147], v[160:163], v[80:83]\n"
      "ds_read_b64 v[116:117], v40 offset:64\n"
      "v_mfma_f32_16x16x32_bf16 v[88:91], v[144:147], v[164:167], v[88:91]\n"
      "global_load_lds_dwordx4 v222, s[22:23] offset:1024\n"
      "v_mfma_f32_16x16x32_bf16 v[88:91], v[156:159], v[160:163], v[88:91]\n"
      "ds_write_b128 v56, v[104:107] offset:2048\n"
      "ds_read_b64 v[118:119], v40 offset:72\n"
      "ds_read_b64 v[128:129], v40 offset:1088\n"
      "ds_read_b64 v[130:131], v40 offset:1096\n"
      "s_waitcnt lgkmcnt(10)\n"
      "v_mfma_f32_16x16x32_bf16 v[68:71], v[112:115], v[160:163], 0\n"
      "ds_read_b64 v[140:141], v40 offset:2112\n"
      "v_mfma_f32_16x16x32_bf16 v[76:79], v[112:115], v[164:167], 0\n"
      "ds_read_b64 v[142:143], v40 offset:2120\n"
      "v_mfma_f32_16x16x32_bf16 v[84:87], v[112:115], v[168:171], 0\n"
      "ds_read_b64 v[152:153], v40 offset:3136\n"
      "s_load_dwordx16 s[60:75], s[56:57], 0xc0\n"
      "v_mfma_f32_16x16x32_bf16 v[92:95], v[112:115], v[172:175], 0\n"
      "ds_write_b128 v62, v[100:103]\n"
      "ds_read_b64 v[154:155], v40 offset:3144\n"
      "s_load_dwordx4 s[92:95], s[56:57], 0xa0\n"
      "v_mfma_f32_16x16x32_bf16 v[76:79], v[124:127], v[160:163], v[76:79]\n"
      "v_mfma_f32_16x16x32_bf16 v[84:87], v[124:127], v[164:167], v[84:87]\n"
      "v_mfma_f32_16x16x32_bf16 v[92:95], v[124:127], v[168:171], v[92:95]\n"
      "v_mfma_f32_16x16x32_bf16 v[84:87], v[136:139], v[160:163], v[84:87]\n"
      "ds_write_b128 v62, v[108:111] offset:2048\n"
      "v_mfma_f32_16x16x32_bf16 v[92:95], v[136:139], v[164:167], v[92:95]\n"
      "v_mfma_f32_16x16x32_bf16 v[92:95], v[148:151], v[160:163], v[92:95]\n"
      "s_waitcnt lgkmcnt(11)\n"
      "v_mfma_f32_16x16x32_bf16 v[64:67], v[112:115], v[176:179], v[64:67]\n"
      "buffer_load_dwordx2 v[254:255], v2, s[32:35], s45 offen sc1\n"
      "v_mfma_f32_16x16x32_bf16 v[72:75], v[112:115], v[180:183], v[72:75]\n"
      "ds_read_b128 v[160:163], v41 offset:4704\n"
      "v_mfma_f32_16x16x32_bf16 v[80:83], v[112:115], v[184:187], v[80:83]\n"
      "ds_read_b128 v[164:167], v41 offset:3168\n"
      "v_mfma_f32_16x16x32_bf16 v[88:91], v[112:115], v[188:191], v[88:91]\n"
      "ds_read_b128 v[168:171], v41 offset:1632\n"
      "v_mfma_f32_16x16x32_bf16 v[72:75], v[124:127], v[176:179], v[72:75]\n"
      "ds_read_b128 v[172:175], v41 offset:96\n"
      "v_mfma_f32_16x16x32_bf16 v[80:83], v[124:127], v[180:183], v[80:83]\n"
      "ds_read_b64 v[120:121], v40 offset:96\n"
      "v_mfma_f32_16x16x32_bf16 v[88:91], v[124:127], v[184:187], v[88:91]\n"
      "s_waitcnt lgkmcnt(5)\n"
      "s_barrier\n"
      "v_mfma_f32_16x16x32_bf16 v[80:83], v[136:139], v[176:179], v[80:83]\n"
      "ds_read_b128 v[96:99], v45 offset:0\n"
      "v_mfma_f32_16x16x32_bf16 v[88:91], v[136:139], v[180:183], v[88:91]\n"
      "ds_read_b128 v[100:103], v45 offset:4096\n"
      "v_mfma_f32_16x16x32_bf16 v[88:91], v[148:151], v[176:179], v[88:91]\n"
      "ds_read_b128 v[104:107], v45 offset:8192\n"
      "v_mfma_f32_16x16x32_bf16 v[68:71], v[116:119], v[176:179], v[68:71]\n"
      "ds_read_b128 v[108:111], v45 offset:12288\n"
      "v_mfma_f32_16x16x32_bf16 v[76:79], v[116:119], v[180:183], v[76:79]\n"
      "ds_read_b64 v[202:203], v47\n"
      "v_mfma_f32_16x16x32_bf16 v[84:87], v[116:119], v[184:187], v[84:87]\n"
      "ds_read_b64 v[122:123], v40 offset:104\n"
      "v_mfma_f32_16x16x32_bf16 v[92:95], v[116:119], v[188:191], v[92:95]\n"
      "ds_read_b64 v[132:133], v40 offset:1120\n"
      "v_mfma_f32_16x16x32_bf16 v[76:79], v[128:131], v[176:179], v[76:79]\n"
      "ds_read_b64 v[134:135], v40 offset:1128\n"
      "v_mfma_f32_16x16x32_bf16 v[84:87], v[128:131], v[180:183], v[84:87]\n"
      "ds_read_b64 v[144:145], v40 offset:2144\n"
      "v_mfma_f32_16x16x32_bf16 v[92:95], v[128:131], v[184:187], v[92:95]\n"
      "ds_read_b64 v[146:147], v40 offset:2152\n"
      "s_waitcnt lgkmcnt(5)\n"
      "v_mfma_f32_16x16x32_bf16 v[84:87], v[140:143], v[176:179], v[84:87]\n"
      "ds_read_b64 v[156:157], v40 offset:3168\n"
      "v_add_f32 v96, v96, v100\n"
      "v_mfma_f32_16x16x32_bf16 v[92:95], v[140:143], v[180:183], v[92:95]\n"
      "ds_read_b64 v[158:159], v40 offset:3176\n"
      "v_add_f32 v97, v97, v101\n"
      "v_mfma_f32_16x16x32_bf16 v[92:95], v[152:155], v[176:179], v[92:95]\n"
      "v_add_f32 v98, v98, v102\n"
      "v_mfma_f32_16x16x32_bf16 v[64:67], v[116:119], v[160:163], v[64:67]\n"
      "buffer_load_dwordx2 v[200:201], v4, s[32:35], s80 offen offset:0 sc1\n"
      "v_add_f32 v99, v99, v103\n"
      "v_mfma_f32_16x16x32_bf16 v[72:75], v[116:119], v[164:167], v[72:75]\n"
      "ds_read_b128 v[176:179], v41 offset:4736\n"
      "v_add_f32 v104, v104, v108\n"
      "v_mfma_f32_16x16x32_bf16 v[80:83], v[116:119], v[168:171], v[80:83]\n"
      "buffer_load_dword v250, v61, s[32:35], s85 offen offset:0 sc1\n"
      "v_add_f32 v105, v105, v109\n"
      "v_mfma_f32_16x16x32_bf16 v[88:91], v[116:119], v[172:175], v[88:91]\n"
      "ds_read_b128 v[180:183], v41 offset:3200\n"
      "v_add_f32 v106, v106, v110\n"
      "v_mfma_f32_16x16x32_bf16 v[72:75], v[128:131], v[160:163], v[72:75]\n"
      "buffer_load_dwordx4 v[224:227], v57, s[32:35], s82 offen offset:0 sc1\n"
      "v_add_f32 v107, v107, v111\n"
      "v_mfma_f32_16x16x32_bf16 v[80:83], v[128:131], v[164:167], v[80:83]\n"
      "ds_read_b128 v[184:187], v41 offset:1664\n"
      "v_add_f32 v96, v96, v104\n"
      "v_mfma_f32_16x16x32_bf16 v[88:91], v[128:131], v[168:171], v[88:91]\n"
      "ds_read_b128 v[188:191], v41 offset:128\n"
      "v_add_f32 v97, v97, v105\n"
      "v_mfma_f32_16x16x32_bf16 v[80:83], v[140:143], v[160:163], v[80:83]\n"
      "ds_read_b64 v[112:113], v40 offset:128\n"
      "v_add_f32 v98, v98, v106\n"
      "v_mfma_f32_16x16x32_bf16 v[88:91], v[140:143], v[164:167], v[88:91]\n"
      "buffer_load_dwordx4 v[228:231], v57, s[32:35], s82 offen offset:1024 sc1\n"
      "v_add_f32 v99, v99, v107\n"
      "v_mfma_f32_16x16x32_bf16 v[88:91], v[152:155], v[160:163], v[88:91]\n"
      "ds_read_b64 v[114:115], v40 offset:136\n"
      "s_waitcnt lgkmcnt(6)\n"
      "v_mfma_f32_16x16x32_bf16 v[68:71], v[120:123], v[160:163], v[68:71]\n"
      "ds_read_b64 v[124:125], v40 offset:1152\n"
      "v_cvt_pk_bf16_f32 v96, 0, v96\n"
      "v_mfma_f32_16x16x32_bf16 v[76:79], v[120:123], v[164:167], v[76:79]\n"
      "ds_read_b64 v[126:127], v40 offset:1160\n"
      "v_cvt_pk_bf16_f32 v97, 0, v97\n"
      "v_mfma_f32_16x16x32_bf16 v[84:87], v[120:123], v[168:171], v[84:87]\n"
      "buffer_load_dwordx4 v[192:195], v54, s[32:35], s53 offen offset:0 sc1\n"
      "v_cvt_pk_bf16_f32 v98, 0, v98\n"
      "v_mfma_f32_16x16x32_bf16 v[92:95], v[120:123], v[172:175], v[92:95]\n"
      "ds_read_b64 v[136:137], v40 offset:2176\n"
      "v_cvt_pk_bf16_f32 v99, 0, v99\n"
      "v_mfma_f32_16x16x32_bf16 v[76:79], v[132:135], v[160:163], v[76:79]\n"
      "ds_read_b64 v[138:139], v40 offset:2184\n"
      "v_and_b32 v100, 0xffff0000, v203\n"
      "v_mfma_f32_16x16x32_bf16 v[84:87], v[132:135], v[164:167], v[84:87]\n"
      "ds_read_b64 v[148:149], v40 offset:3200\n"
      "v_lshlrev_b32 v101, 16, v203\n"
      "v_mfma_f32_16x16x32_bf16 v[92:95], v[132:135], v[168:171], v[92:95]\n"
      "buffer_load_dwordx4 v[196:199], v54, s[32:35], s54 offen offset:0 sc1\n"
      "v_and_b32 v102, 0xffff0000, v202\n"
      "v_mfma_f32_16x16x32_bf16 v[84:87], v[144:147], v[160:163], v[84:87]\n"
      "ds_read_b64 v[150:151], v40 offset:3208\n"
      "v_lshlrev_b32 v103, 16, v202\n"
      "v_mfma_f32_16x16x32_bf16 v[92:95], v[144:147], v[164:167], v[92:95]\n"
      "s_lshr_b32 s58, s3, 1\n"
      "s_mul_i32 s58, s58, 0x3f800000\n"
      "v_mfma_f32_16x16x32_bf16 v[92:95], v[156:159], v[160:163], v[92:95]\n"
      "v_mul_f32 v100, s25, v100\n"
      "s_waitcnt lgkmcnt(8)\n"
      "v_mfma_f32_16x16x32_bf16 v[64:67], v[120:123], v[176:179], v[64:67]\n"
      "buffer_load_dwordx2 v[252:253], v51, s[32:35], s76 offen offset:0 sc1\n"
      "s_waitcnt vmcnt(7)\n"
      "v_cmp_ne_u32 vcc, s30, v255\n"
      "v_mfma_f32_16x16x32_bf16 v[72:75], v[120:123], v[180:183], v[72:75]\n"
      "ds_read_b128 v[160:163], v41 offset:4800\n"
      "v_mul_f32 v101, s25, v101\n"
      "v_mfma_f32_16x16x32_bf16 v[80:83], v[120:123], v[184:187], v[80:83]\n"
      "ds_read_b128 v[164:167], v41 offset:3264\n"
      "s_cbranch_vccnz L_cm_s%=\n"
      "L_cm_r%=:\n"
      "v_add_f32_dpp v254, v254, v254 quad_perm:[1,0,3,2] row_mask:0xf bank_mask:0xf\n"
      "v_mfma_f32_16x16x32_bf16 v[88:91], v[120:123], v[188:191], v[88:91]\n"
      "ds_read_b128 v[168:171], v41 offset:1728\n"
      "v_mul_f32 v102, s25, v102\n"
      "v_mfma_f32_16x16x32_bf16 v[72:75], v[132:135], v[176:179], v[72:75]\n"
      "ds_read_b128 v[172:175], v41 offset:192\n"
      "v_add_f32_dpp v254, v254, v254 quad_perm:[2,3,0,1] row_mask:0xf bank_mask:0xf\n"
      "v_mfma_f32_16x16x32_bf16 v[80:83], v[132:135], v[180:183], v[80:83]\n"
      "ds_read_b64 v[116:117], v40 offset:160\n"
      "v_cvt_pk_bf16_f32 v254, 0, v254\n"
      "v_mfma_f32_16x16x32_bf16 v[88:91], v[132:135], v[184:187], v[88:91]\n"
      "buffer_load_dword v248, v60, s[32:35], s84 offen offset:0 sc1\n"
      "v_add_f32 v254, s58, v254\n"
      "v_mfma_f32_16x16x32_bf16 v[80:83], v[144:147], v[176:179], v[80:83]\n"
      "ds_read_b64 v[118:119], v40 offset:168\n"
      "v_cvt_pk_bf16_f32 v254, v254, v254\n"
      "v_mfma_f32_16x16x32_bf16 v[88:91], v[144:147], v[180:183], v[88:91]\n"
      "ds_write_b16 v3, v254\n"
      "ds_read_b64 v[128:129], v40 offset:1184\n"
      "v_mul_f32 v103, s25, v103\n"
      "v_mfma_f32_16x16x32_bf16 v[88:91], v[156:159], v[176:179], v[88:91]\n"
      "ds_read_b64 v[130:131], v40 offset:1192\n"
      "s_waitcnt lgkmcnt(9)\n"
      "v_mfma_f32_16x16x32_bf16 v[68:71], v[112:115], v[176:179], v[68:71]\n"
      "buffer_load_dword v249, v60, s[32:35], s84 offen offset:2048 sc1\n"
      "v_cvt_pk_bf16_f32 v100, 0, v100\n"
      "v_mfma_f32_16x16x32_bf16 v[76:79], v[112:115], v[180:183], v[76:79]\n"
      "ds_read_b64 v[140:141], v40 offset:2208\n"
      "v_cvt_pk_bf16_f32 v101, 0, v101\n"
      "v_mfma_f32_16x16x32_bf16 v[84:87], v[112:115], v[184:187], v[84:87]\n"
      "ds_read_b64 v[142:143], v40 offset:2216\n"
      "v_cvt_pk_bf16_f32 v102, 0, v102\n"
      "v_mfma_f32_16x16x32_bf16 v[92:95], v[112:115], v[188:191], v[92:95]\n"
      "s_waitcnt lgkmcnt(4)\n"
      "s_barrier\n"
      "v_mfma_f32_16x16x32_bf16 v[76:79], v[124:127], v[176:179], v[76:79]\n"
      "buffer_load_dwordx4 v[208:211], v55, s[32:35], 0 offen offset:0 sc1\n"
      "ds_read2_b64 v[24:27], v236 offset0:0 offset1:4\n"
      "v_cvt_pk_bf16_f32 v103, 0, v103\n"
      "v_mfma_f32_16x16x32_bf16 v[84:87], v[124:127], v[180:183], v[84:87]\n"
      "ds_read_b64 v[152:153], v40 offset:3232\n"
      "v_add_f32 v96, v96, v100\n"
      "v_mfma_f32_16x16x32_bf16 v[92:95], v[124:127], v[184:187], v[92:95]\n"
      "ds_read_b64 v[154:155], v40 offset:3240\n"
      "v_add_f32 v97, v97, v101\n"
      "v_mfma_f32_16x16x32_bf16 v[84:87], v[136:139], v[176:179], v[84:87]\n"
      "s_mov_b32 s48, 0\n"
      "s_mov_b32 s49, -1\n"
      "v_mfma_f32_16x16x32_bf16 v[92:95], v[136:139], v[180:183], v[92:95]\n"
      "buffer_load_dwordx4 v[212:215], v55, s[32:35], 0 offen offset:1024 sc1\n"
      "v_add_f32 v98, v98, v102\n"
      "v_mfma_f32_16x16x32_bf16 v[92:95], v[148:151], v[176:179], v[92:95]\n"
      "s_waitcnt lgkmcnt(2)\n"
      "v_mfma_f32_16x16x32_bf16 v[64:67], v[112:115], v[160:163], v[64:67]\n"
      "ds_read_b128 v[176:179], v41 offset:4832\n"
      "v_mfma_f32_16x16x32_bf16 v[232:235], v[24:27], v[8:11], 0\n"
      "v_mfma_f32_16x16x32_bf16 v[72:75], v[112:115], v[164:167], v[72:75]\n"
      "ds_read_b128 v[180:183], v41 offset:3296\n"
      "v_mfma_f32_16x16x32_bf16 v[8:11], v[24:27], v[12:15], 0\n"
      "v_mfma_f32_16x16x32_bf16 v[80:83], v[112:115], v[168:171], v[80:83]\n"
      "buffer_load_dwordx4 v[216:219], v55, s[32:35], 0 offen offset:2048 sc1\n"
      "v_mfma_f32_16x16x32_bf16 v[12:15], v[24:27], v[16:19], 0\n"
      "v_mfma_f32_16x16x32_bf16 v[88:91], v[112:115], v[172:175], v[88:91]\n"
      "ds_read_b128 v[184:187], v41 offset:1760\n"
      "v_mfma_f32_16x16x32_bf16 v[16:19], v[24:27], v[20:23], 0\n"
      "v_mfma_f32_16x16x32_bf16 v[72:75], v[124:127], v[160:163], v[72:75]\n"
      "ds_read_b128 v[188:191], v41 offset:224\n"
      "v_add_f32 v99, v99, v103\n"
      "v_mfma_f32_16x16x32_bf16 v[80:83], v[124:127], v[164:167], v[80:83]\n"
      "ds_read_b64 v[120:121], v40 offset:192\n"
      "v_cvt_pk_bf16_f32 v96, 0, v96\n"
      "v_mfma_f32_16x16x32_bf16 v[88:91], v[124:127], v[168:171], v[88:91]\n"
      "buffer_load_dwordx4 v[220:223], v55, s[32:35], 0 offen offset:3072 sc1\n"
      "v_cndmask_b32 v206, v232, v12, s[48:49]\n"
      "v_cndmask_b32 v207, v8, v16, s[48:49]\n"
      "v_mfma_f32_16x16x32_bf16 v[80:83], v[136:139], v[160:163], v[80:83]\n"
      "ds_read_b64 v[122:123], v40 offset:200\n"
      "v_cvt_pk_bf16_f32 v97, 0, v97\n"
      "v_mfma_f32_16x16x32_bf16 v[88:91], v[136:139], v[164:167], v[88:91]\n"
      "ds_read_b64 v[132:133], v40 offset:1216\n"
      "v_cvt_pk_bf16_f32 v98, 0, v98\n"
      "v_mfma_f32_16x16x32_bf16 v[88:91], v[148:151], v[160:163], v[88:91]\n"
      "ds_read_b64 v[134:135], v40 offset:1224\n"
      "s_waitcnt lgkmcnt(8)\n"
      "v_mfma_f32_16x16x32_bf16 v[68:71], v[116:119], v[160:163], v[68:71]\n"
      "buffer_load_dwordx2 v[204:205], v58, s[32:35], s83 offen offset:0 sc1\n"
      "v_cvt_pk_bf16_f32 v99, 0, v99\n"
      "v_mfma_f32_16x16x32_bf16 v[76:79], v[116:119], v[164:167], v[76:79]\n"
      "ds_read_b64 v[144:145], v40 offset:2240\n"
      "v_mfma_f32_16x16x32_bf16 v[84:87], v[116:119], v[168:171], v[84:87]\n"
      "ds_read_b64 v[146:147], v40 offset:2248\n"
      "s_waitcnt vmcnt(7)\n"
      "v_mfma_f32_16x16x32_bf16 v[92:95], v[116:119], v[172:175], v[92:95]\n"
      "ds_read_b64 v[156:157], v40 offset:3264\n"
      "v_cmp_ne_u64 vcc, s[30:31], v[252:253]\n"
      "v_mfma_f32_16x16x32_bf16 v[76:79], v[128:131], v[160:163], v[76:79]\n"
      "ds_read_b64 v[158:159], v40 offset:3272\n"
      "v_mfma_f32_16x16x32_bf16 v[84:87], v[128:131], v[164:167], v[84:87]\n"
      "s_cbranch_vccnz L_cw_s%=\n"
      "L_cw_r%=:\n"
      "v_mfma_f32_16x16x32_bf16 v[92:95], v[128:131], v[168:171], v[92:95]\n"
      "v_mfma_f32_16x16x32_bf16 v[84:87], v[140:143], v[160:163], v[84:87]\n"
      "v_mfma_f32_16x16x32_bf16 v[92:95], v[140:143], v[164:167], v[92:95]\n"
      "v_mfma_f32_16x16x32_bf16 v[92:95], v[152:155], v[160:163], v[92:95]\n"
      "s_waitcnt lgkmcnt(8)\n"
      "v_mfma_f32_16x16x32_bf16 v[64:67], v[116:119], v[176:179], v[64:67]\n"
      "ds_read_b128 v[160:163], v41 offset:4896\n"
      "v_mfma_f32_16x16x32_bf16 v[72:75], v[116:119], v[180:183], v[72:75]\n"
      "ds_read_b128 v[164:167], v41 offset:3360\n"
      "v_mfma_f32_16x16x32_bf16 v[80:83], v[116:119], v[184:187], v[80:83]\n"
      "ds_read_b128 v[168:171], v41 offset:1824\n"
      "v_mfma_f32_16x16x32_bf16 v[88:91], v[116:119], v[188:191], v[88:91]\n"
      "ds_read_b128 v[172:175], v41 offset:288\n"
      "v_mfma_f32_16x16x32_bf16 v[72:75], v[128:131], v[176:179], v[72:75]\n"
      "ds_read_b64 v[112:113], v40 offset:224\n"
      "v_add_u32 v35, 4096, v7\n"
      "v_mfma_f32_16x16x32_bf16 v[80:83], v[128:131], v[180:183], v[80:83]\n"
      "ds_read_b64 v[114:115], v40 offset:232\n"
      "v_mfma_f32_16x16x32_bf16 v[88:91], v[128:131], v[184:187], v[88:91]\n"
      "global_load_dwordx4 a[0:3], v35, s[8:9] offset:-4096 sc1\n"
      "v_mfma_f32_16x16x32_bf16 v[80:83], v[140:143], v[176:179], v[80:83]\n"
      "ds_read_b64 v[124:125], v40 offset:1248\n"
      "v_mfma_f32_16x16x32_bf16 v[88:91], v[140:143], v[180:183], v[88:91]\n"
      "ds_read_b64 v[126:127], v40 offset:1256\n"
      "v_mfma_f32_16x16x32_bf16 v[88:91], v[152:155], v[176:179], v[88:91]\n"
      "ds_read_b64 v[136:137], v40 offset:2272\n"
      "s_waitcnt lgkmcnt(9)\n"
      "v_mfma_f32_16x16x32_bf16 v[68:71], v[120:123], v[176:179], v[68:71]\n"
      "global_load_dwordx4 a[4:7], v35, s[8:9] offset:-3072 sc1\n"
      "v_mfma_f32_16x16x32_bf16 v[76:79], v[120:123], v[180:183], v[76:79]\n"
      "ds_read_b64 v[138:139], v40 offset:2280\n"
      "v_mfma_f32_16x16x32_bf16 v[84:87], v[120:123], v[184:187], v[84:87]\n"
      "ds_read_b64 v[148:149], v40 offset:3296\n"
      "v_mfma_f32_16x16x32_bf16 v[92:95], v[120:123], v[188:191], v[92:95]\n"
      "ds_read_b64 v[150:151], v40 offset:3304\n"
      "v_mfma_f32_16x16x32_bf16 v[76:79], v[132:135], v[176:179], v[76:79]\n"
      "global_load_dwordx4 a[8:11], v35, s[8:9] offset:-2048 sc1\n"
      "s_waitcnt vmcnt(3)\n"
      "v_mfma_f32_16x16x32_bf16 v[84:87], v[132:135], v[180:183], v[84:87]\n"
      "v_add_u32 v251, v200, v201\n"
      "v_add3_u32 v34, v192, v193, v194\n"
      "v_add3_u32 v34, v34, v195, v196\n"
      "v_add_u32_dpp v251, v251, v251 row_half_mirror row_mask:0xf bank_mask:0xf\n"
      "v_add3_u32 v34, v34, v197, v198\n"
      "v_mfma_f32_16x16x32_bf16 v[92:95], v[132:135], v[184:187], v[92:95]\n"
      "v_add3_u32 v251, v251, v34, v199\n"
      "v_add3_u32 v34, v248, v249, v250\n"
      "v_add3_u32 v34, v34, v211, v215\n"
      "v_add_u32_dpp v251, v251, v251 quad_perm:[1,0,3,2] row_mask:0xf bank_mask:0xf\n"
      "v_mfma_f32_16x16x32_bf16 v[84:87], v[144:147], v[176:179], v[84:87]\n"
      "v_add3_u32 v34, v34, v219, v223\n"
      "v_add3_u32 v34, v34, v227, v231\n"
      "v_add_u32_dpp v251, v251, v251 quad_perm:[2,3,0,1] row_mask:0xf bank_mask:0xf\n"
      "v_add3_u32 v251, v251, v34, v205\n"
      "v_mfma_f32_16x16x32_bf16 v[92:95], v[144:147], v[180:183], v[92:95]\n"
      "global_load_dwordx4 a[12:15], v35, s[8:9] offset:-1024 sc1\n"
      "v_cmp_ne_u32 vcc, s24, v251\n"
      "v_mfma_f32_16x16x32_bf16 v[92:95], v[156:159], v[176:179], v[92:95]\n"
      "s_waitcnt lgkmcnt(8)\n"
      "v_mfma_f32_16x16x32_bf16 v[64:67], v[120:123], v[160:163], v[64:67]\n"
      "ds_read_b128 v[176:179], v41 offset:4928\n"
      "s_cbranch_vccnz L_cv_s%=\n"
      "L_cv_r%=:\n"
      "v_add_f32_dpp v204, v204, v204 quad_perm:[1,0,3,2] row_mask:0xf bank_mask:0xf\n"
      "v_lshlrev_b32 v100, 16, v200\n"
      "v_and_b32 v101, 0xffff0000, v200\n"
      "v_add_f32_dpp v204, v204, v204 quad_perm:[2,3,0,1] row_mask:0xf bank_mask:0xf\n"
      "v_mfma_f32_16x16x32_bf16 v[72:75], v[120:123], v[164:167], v[72:75]\n"
      "ds_read_b128 v[180:183], v41 offset:3392\n"
      "v_cvt_pk_bf16_f32 v204, 0, v204\n"
      "v_lshlrev_b32 v102, 16, v201\n"
      "v_add_f32 v205, 1.0, v204\n"
      "v_mfma_f32_16x16x32_bf16 v[80:83], v[120:123], v[168:171], v[80:83]\n"
      "global_load_dwordx4 a[16:19], v35, s[8:9] offset:0 sc1\n"
      "v_and_b32 v103, 0xffff0000, v201\n"
      "v_cvt_pk_bf16_f32 v205, 0, v205\n"
      "v_lshlrev_b32 v104, 16, v226\n"
      "v_mfma_f32_16x16x32_bf16 v[88:91], v[120:123], v[172:175], v[88:91]\n"
      "ds_read_b128 v[184:187], v41 offset:1856\n"
      "v_readlane_b32 s40, v204, 32\n"
      "v_and_b32 v105, 0xffff0000, v226\n"
      "v_readlane_b32 s41, v205, 36\n"
      "v_mfma_f32_16x16x32_bf16 v[72:75], v[132:135], v[160:163], v[72:75]\n"
      "ds_read_b128 v[188:191], v41 offset:320\n"
      "v_lshlrev_b32 v106, 16, v230\n"
      "v_readlane_b32 s42, v204, 40\n"
      "v_and_b32 v107, 0xffff0000, v230\n"
      "v_mfma_f32_16x16x32_bf16 v[80:83], v[132:135], v[164:167], v[80:83]\n"
      "ds_read_b64 v[116:117], v40 offset:256\n"
      "v_readlane_b32 s43, v205, 44\n"
      "v_sub_f32 v104, v100, v104\n"
      "v_sub_f32 v105, v101, v105\n"
      "v_mfma_f32_16x16x32_bf16 v[88:91], v[132:135], v[168:171], v[88:91]\n"
      "global_load_dwordx4 a[20:23], v35, s[8:9] offset:1024 sc1\n"
      "v_sub_f32 v106, v102, v106\n"
      "v_sub_f32 v107, v103, v107\n"
      "v_cvt_pk_bf16_f32 v104, 0, v104\n"
      "v_mfma_f32_16x16x32_bf16 v[80:83], v[144:147], v[160:163], v[80:83]\n"
      "ds_read_b64 v[118:119], v40 offset:264\n"
      "v_cvt_pk_bf16_f32 v105, 0, v105\n"
      "v_cvt_pk_bf16_f32 v106, 0, v106\n"
      "v_cvt_pk_bf16_f32 v107, 0, v107\n"
      "v_mfma_f32_16x16x32_bf16 v[88:91], v[144:147], v[164:167], v[88:91]\n"
      "ds_read_b64_tr_b16 v[240:241], v28 offset:0\n"
      "ds_read_b64_tr_b16 v[242:243], v28 offset:1024\n"
      "ds_read_b64_tr_b16 v[244:245], v28 offset:32\n"
      "ds_read_b64_tr_b16 v[246:247], v28 offset:1056\n"
      "v_mul_f32 v104, v104, v224\n"
      "v_mul_f32 v105, v105, v225\n"
      "v_mul_f32 v106, v106, v228\n"
      "v_mfma_f32_16x16x32_bf16 v[88:91], v[156:159], v[160:163], v[88:91]\n"
      "ds_read_b64 v[128:129], v40 offset:1280\n"
      "v_mul_f32 v107, v107, v229\n"
      "v_cvt_pk_bf16_f32 v104, 0, v104\n"
      "v_cvt_pk_bf16_f32 v105, 0, v105\n"
      "s_waitcnt lgkmcnt(11)\n"
      "v_mfma_f32_16x16x32_bf16 v[68:71], v[112:115], v[160:163], v[68:71]\n"
      "global_load_dwordx4 a[24:27], v35, s[8:9] offset:2048 sc1\n"
      "ds_read_b64 v[130:131], v40 offset:1288\n"
      "v_cvt_pk_bf16_f32 v106, 0, v106\n"
      "v_cvt_pk_bf16_f32 v107, 0, v107\n"
      "v_mul_f32 v104, s41, v104\n"
      "v_mfma_f32_16x16x32_bf16 v[76:79], v[112:115], v[164:167], v[76:79]\n"
      "ds_read_b64 v[140:141], v40 offset:2304\n"
      "v_mul_f32 v105, s41, v105\n"
      "v_mul_f32 v106, s41, v106\n"
      "v_mul_f32 v107, s41, v107\n"
      "v_mfma_f32_16x16x32_bf16 v[84:87], v[112:115], v[168:171], v[84:87]\n"
      "ds_read_b64 v[142:143], v40 offset:2312\n"
      "v_cvt_pk_bf16_f32 v104, 0, v104\n"
      "v_cvt_pk_bf16_f32 v105, 0, v105\n"
      "v_cvt_pk_bf16_f32 v106, 0, v106\n"
      "v_mfma_f32_16x16x32_bf16 v[92:95], v[112:115], v[172:175], v[92:95]\n"
      "ds_read_b64 v[152:153], v40 offset:3328\n"
      "v_cvt_pk_bf16_f32 v107, 0, v107\n"
      "v_add_f32 v104, s40, v104\n"
      "v_add_f32 v105, s40, v105\n"
      "v_mfma_f32_16x16x32_bf16 v[76:79], v[124:127], v[160:163], v[76:79]\n"
      "global_load_dwordx4 a[28:31], v35, s[8:9] offset:3072 sc1\n"
      "ds_read_b64 v[154:155], v40 offset:3336\n"
      "v_add_f32 v106, s40, v106\n"
      "v_add_f32 v107, s40, v107\n"
      "v_cvt_pk_bf16_f32 v104, 0, v104\n"
      "v_mfma_f32_16x16x32_bf16 v[84:87], v[124:127], v[164:167], v[84:87]\n"
      "v_cvt_pk_bf16_f32 v105, 0, v105\n"
      "v_cvt_pk_bf16_f32 v106, 0, v106\n"
      "v_cvt_pk_bf16_f32 v107, 0, v107\n"
      "v_mfma_f32_16x16x32_bf16 v[92:95], v[124:127], v[168:171], v[92:95]\n"
      "v_add_u32 v35, 12288, v7\n"
      "v_mul_f32 v104, v104, v96\n"
      "v_mul_f32 v105, v105, v97\n"
      "v_mul_f32 v106, v106, v98\n"
      "s_waitcnt lgkmcnt(6)\n"
      "v_mfma_f32_16x16x32_bf16 v[84:87], v[136:139], v[160:163], v[84:87]\n"
      "v_mul_f32 v107, v107, v99\n"
      "v_cvt_pk_bf16_f32 v104, 0, v104\n"
      "v_cvt_pk_bf16_f32 v105, 0, v105\n"
      "v_mfma_f32_16x16x32_bf16 v[92:95], v[136:139], v[164:167], v[92:95]\n"
      "global_load_dwordx4 a[32:35], v35, s[8:9] offset:-4096 sc1\n"
      "v_cvt_pk_bf16_f32 v106, 0, v106\n"
      "v_cvt_pk_bf16_f32 v107, 0, v107\n"
      "v_mul_f32 v104, s42, v104\n"
      "v_mfma_f32_16x16x32_bf16 v[92:95], v[148:151], v[160:163], v[92:95]\n"
      "v_mul_f32 v105, s42, v105\n"
      "v_mul_f32 v106, s42, v106\n"
      "v_mul_f32 v107, s42, v107\n"
      "s_waitcnt lgkmcnt(0)\n"
      "v_mfma_f32_16x16x32_bf16 v[64:67], v[112:115], v[176:179], v[64:67]\n"
      "v_cvt_pk_bf16_f32 v104, 0, v104\n"
      "v_cvt_pk_bf16_f32 v105, 0, v105\n"
      "v_cvt_pk_bf16_f32 v106, 0, v106\n"
      "v_mfma_f32_16x16x32_bf16 v[68:71], v[116:119], v[176:179], v[68:71]\n"
      "global_load_dwordx4 a[36:39], v35, s[8:9] offset:-3072 sc1\n"
      "v_cvt_pk_bf16_f32 v107, 0, v107\n"
      "v_add_f32 v104, v104, v100\n"
      "v_add_f32 v105, v105, v101\n"
      "v_mfma_f32_16x16x32_bf16 v[72:75], v[112:115], v[180:183], v[72:75]\n"
      "v_add_f32 v106, v106, v102\n"
      "v_add_f32 v107, v107, v103\n"
      "v_cvt_pk_bf16_f32 v104, 0, v104\n"
      "v_mfma_f32_16x16x32_bf16 v[72:75], v[124:127], v[176:179], v[72:75]\n"
      "ds_write_b128 v44, v[64:67] offset:0\n"
      "v_cvt_pk_bf16_f32 v105, 0, v105\n"
      "v_cvt_pk_bf16_f32 v106, 0, v106\n"
      "v_cvt_pk_bf16_f32 v107, 0, v107\n"
      "v_cvt_pk_bf16_f32 v108, v104, v105\n"
      "v_mfma_f32_16x16x32_bf16 v[76:79], v[116:119], v[180:183], v[76:79]\n"
      "buffer_store_dwordx4 v[240:243], v7, s[32:35], s86 offen sc1\n"
      "buffer_store_dwordx4 v[244:247], v7, s[32:35], s87 offen sc1\n"
      "global_load_dwordx4 a[40:43], v35, s[8:9] offset:-2048 sc1\n"
      "ds_write_b128 v59, v[68:71] offset:0\n"
      "v_cvt_pk_bf16_f32 v109, v106, v107\n"
      "v_mul_f32 v104, s43, v104\n"
      "v_mul_f32 v105, s43, v105\n"
      "v_mul_f32 v106, s43, v106\n"
      "v_mfma_f32_16x16x32_bf16 v[76:79], v[128:131], v[176:179], v[76:79]\n"
      "v_mul_f32 v107, s43, v107\n"
      "v_cvt_pk_bf16_f32 v110, v104, v105\n"
      "v_mfma_f32_16x16x32_bf16 v[80:83], v[112:115], v[184:187], v[80:83]\n"
      "buffer_store_dword v206, v239, s[32:35], s59 offen offset:0 sc1\n"
      "buffer_store_dword v207, v239, s[32:35], s59 offen offset:128 sc1\n"
      "ds_write_b128 v44, v[72:75] offset:2048\n"
      "v_cvt_pk_bf16_f32 v111, v106, v107\n"
      "v_add_u32 v96, v108, v109\n"
      "v_add_u32 v97, v110, v111\n"
      "v_mfma_f32_16x16x32_bf16 v[80:83], v[124:127], v[180:183], v[80:83]\n"
      "global_load_dwordx4 a[44:47], v35, s[8:9] offset:-1024 sc1\n"
      "v_mfma_f32_16x16x32_bf16 v[80:83], v[136:139], v[176:179], v[80:83]\n"
      "buffer_store_dwordx2 v[110:111], v50, s[32:35], s78 offen sc1\n"
      "ds_write_b128 v59, v[76:79] offset:2048\n"
      "ds_read_b128 v[232:235], v46\n"
      "ds_read_b128 v[236:239], v46 offset:7680\n"
      "v_add_u32_dpp v96, v96, v96 quad_perm:[1,0,3,2] row_mask:0xf bank_mask:0xf\n"
      "v_add_u32_dpp v97, v97, v97 quad_perm:[1,0,3,2] row_mask:0xf bank_mask:0xf\n"
      "v_mfma_f32_16x16x32_bf16 v[84:87], v[116:119], v[184:187], v[84:87]\n"
      "v_add_u32_dpp v96, v96, v96 quad_perm:[2,3,0,1] row_mask:0xf bank_mask:0xf\n"
      "v_add_u32_dpp v97, v97, v97 quad_perm:[2,3,0,1] row_mask:0xf bank_mask:0xf\n"
      "v_mfma_f32_16x16x32_bf16 v[84:87], v[128:131], v[180:183], v[84:87]\n"
      "global_load_dwordx4 a[48:51], v35, s[8:9] offset:0 sc1\n"
      "v_add_u32_dpp v96, v96, v96 row_half_mirror row_mask:0xf bank_mask:0xf\n"
      "v_add_u32_dpp v97, v97, v97 row_half_mirror row_mask:0xf bank_mask:0xf\n"
      "s_add_u32 s4, s26, s71\n"
      "s_addc_u32 s5, s27, 0\n"
      "v_mfma_f32_16x16x32_bf16 v[84:87], v[140:143], v[176:179], v[84:87]\n"
      "buffer_store_dwordx2 v[108:109], v37, s[32:35], s55 offen sc1\n"
      "ds_write_b128 v44, v[80:83] offset:4096\n"
      "v_mov_b32 v98, v96\n"
      "v_add_u32_dpp v97, v97, v97 row_mirror row_mask:0xf bank_mask:0xf\n"
      "s_mov_b32 s42, 0xaaaa5555\n"
      "s_mov_b32 s43, s42\n"
      "v_mfma_f32_16x16x32_bf16 v[88:91], v[112:115], v[188:191], v[88:91]\n"
      "s_add_u32 s6, s26, s72\n"
      "s_addc_u32 s7, s27, 0\n"
      "v_mfma_f32_16x16x32_bf16 v[88:91], v[124:127], v[184:187], v[88:91]\n"
      "global_load_dwordx4 a[52:55], v35, s[8:9] offset:1024 sc1\n"
      "v_permlane16_swap_b32 v96, v98\n"
      "v_add_u32 v96, v96, v98\n"
      "v_mfma_f32_16x16x32_bf16 v[88:91], v[136:139], v[180:183], v[88:91]\n"
      "ds_write_b128 v59, v[84:87] offset:4096\n"
      "v_cndmask_b32 v96, v97, v96, s[42:43]\n"
      "v_sub_u32 v96, v52, v96\n"
      "s_add_u32 s10, s26, s75\n"
      "s_addc_u32 s11, s27, 0\n"
      "v_mfma_f32_16x16x32_bf16 v[88:91], v[148:151], v[176:179], v[88:91]\n"
      "buffer_store_dword v96, v49, s[32:35], s77 offen sc1\n"
      "v_mfma_f32_16x16x32_bf16 v[92:95], v[116:119], v[188:191], v[92:95]\n"
      "global_load_dwordx4 a[56:59], v35, s[8:9] offset:2048 sc1\n"
      "s_add_u32 s12, s10, 49152\n"
      "s_addc_u32 s13, s11, 0\n"
      "v_mfma_f32_16x16x32_bf16 v[92:95], v[128:131], v[184:187], v[92:95]\n"
      "s_lshl_b32 s71, s30, 2\n"
      "v_mfma_f32_16x16x32_bf16 v[92:95], v[140:143], v[180:183], v[92:95]\n"
      "ds_write_b128 v44, v[88:91] offset:6144\n"
      "v_mfma_f32_16x16x32_bf16 v[92:95], v[152:155], v[176:179], v[92:95]\n"
      "global_load_dwordx4 a[60:63], v35, s[8:9] offset:3072 sc1\n"
      "s_nop 7\n"
      "ds_write_b128 v59, v[92:95] offset:6144\n"
      "v_readlane_b32 s40, v204, 0\n"
      "v_readlane_b32 s42, v205, 4\n"
      "v_xor_b32 v33, 16, v31\n"
      "v_readlane_b32 s58, v204, 8\n"
      "v_readlane_b32 s48, v204, 16\n"
      "v_readlane_b32 s50, v205, 20\n"
      "v_readlane_b32 s52, v204, 24\n"
      "v_readlane_b32 s41, v205, 12\n"
      "v_readlane_b32 s49, v205, 28\n"
      "s_mov_b32 vcc_lo, 0xaa55aa55\n"
      "s_mov_b32 vcc_hi, 0xaa55aa55\n"
      "v_add_u32 v40, s73, v7\n"
      "v_lshlrev_b32 v96, 16, v210\n"
      "v_and_b32 v97, 0xffff0000, v210\n"
      "v_lshlrev_b32 v98, 16, v214\n"
      "v_and_b32 v99, 0xffff0000, v214\n"
      "v_lshlrev_b32 v100, 16, v218\n"
      "v_and_b32 v101, 0xffff0000, v218\n"
      "v_lshlrev_b32 v102, 16, v222\n"
      "v_and_b32 v103, 0xffff0000, v222\n"
      "s_waitcnt lgkmcnt(0)\n"
      "s_barrier\n"
      "ds_read_b128 v[64:67], v31 offset:0\n"
      "ds_read_b128 v[68:71], v33 offset:0\n"
      "ds_read_b128 v[72:75], v31 offset:8192\n"
      "ds_read_b128 v[76:79], v33 offset:8192\n"
      "ds_read_b128 v[80:83], v31 offset:16384\n"
      "ds_read_b128 v[84:87], v33 offset:16384\n"
      "ds_read_b128 v[88:91], v31 offset:24576\n"
      "ds_read_b128 v[92:95], v33 offset:24576\n"
      "v_add_u32 v34, 20480, v7\n"
      "global_load_dwordx4 a[64:67], v34, s[8:9] offset:-4096 sc1\n"
      "v_lshlrev_b32 v112, 16, v192\n"
      "v_and_b32 v113, 0xffff0000, v192\n"
      "v_lshlrev_b32 v114, 16, v193\n"
      "v_and_b32 v115, 0xffff0000, v193\n"
      "v_lshlrev_b32 v116, 16, v194\n"
      "v_and_b32 v117, 0xffff0000, v194\n"
      "v_lshlrev_b32 v118, 16, v195\n"
      "v_and_b32 v119, 0xffff0000, v195\n"
      "v_pk_add_f32 v[104:105], v[112:113], v[96:97] neg_lo:[0,1] neg_hi:[0,1]\n"
      "v_pk_add_f32 v[106:107], v[114:115], v[98:99] neg_lo:[0,1] neg_hi:[0,1]\n"
      "v_pk_add_f32 v[108:109], v[116:117], v[100:101] neg_lo:[0,1] neg_hi:[0,1]\n"
      "v_pk_add_f32 v[110:111], v[118:119], v[102:103] neg_lo:[0,1] neg_hi:[0,1]\n"
      "global_load_dwordx4 a[68:71], v34, s[8:9] offset:-3072 sc1\n"
      "v_cvt_pk_bf16_f32 v104, 0, v104\n"
      "v_cvt_pk_bf16_f32 v105, 0, v105\n"
      "v_cvt_pk_bf16_f32 v106, 0, v106\n"
      "v_cvt_pk_bf16_f32 v107, 0, v107\n"
      "v_cvt_pk_bf16_f32 v108, 0, v108\n"
      "v_cvt_pk_bf16_f32 v109, 0, v109\n"
      "v_cvt_pk_bf16_f32 v110, 0, v110\n"
      "v_cvt_pk_bf16_f32 v111, 0, v111\n"
      "v_pk_mul_f32 v[104:105], v[104:105], v[208:209]\n"
      "v_pk_mul_f32 v[106:107], v[106:107], v[212:213]\n"
      "v_pk_mul_f32 v[108:109], v[108:109], v[216:217]\n"
      "v_pk_mul_f32 v[110:111], v[110:111], v[220:221]\n"
      "global_load_dwordx4 a[72:75], v34, s[8:9] offset:-2048 sc1\n"
      "v_cvt_pk_bf16_f32 v104, 0, v104\n"
      "v_cvt_pk_bf16_f32 v105, 0, v105\n"
      "v_cvt_pk_bf16_f32 v106, 0, v106\n"
      "v_cvt_pk_bf16_f32 v107, 0, v107\n"
      "v_cvt_pk_bf16_f32 v108, 0, v108\n"
      "v_cvt_pk_bf16_f32 v109, 0, v109\n"
      "v_cvt_pk_bf16_f32 v110, 0, v110\n"
      "v_cvt_pk_bf16_f32 v111, 0, v111\n"
      "v_pk_mul_f32 v[104:105], v[104:105], s[42:43] op_sel_hi:[1,0]\n"
      "v_pk_mul_f32 v[106:107], v[106:107], s[42:43] op_sel_hi:[1,0]\n"
      "v_pk_mul_f32 v[108:109], v[108:109], s[42:43] op_sel_hi:[1,0]\n"
      "v_pk_mul_f32 v[110:111], v[110:111], s[42:43] op_sel_hi:[1,0]\n"
      "global_load_dwordx4 a[76:79], v34, s[8:9] offset:-1024 sc1\n"
      "v_cvt_pk_bf16_f32 v104, 0, v104\n"
      "v_cvt_pk_bf16_f32 v105, 0, v105\n"
      "v_cvt_pk_bf16_f32 v106, 0, v106\n"
      "v_cvt_pk_bf16_f32 v107, 0, v107\n"
      "v_cvt_pk_bf16_f32 v108, 0, v108\n"
      "v_cvt_pk_bf16_f32 v109, 0, v109\n"
      "v_cvt_pk_bf16_f32 v110, 0, v110\n"
      "v_cvt_pk_bf16_f32 v111, 0, v111\n"
      "v_pk_add_f32 v[104:105], v[104:105], s[40:41] op_sel_hi:[1,0]\n"
      "v_pk_add_f32 v[106:107], v[106:107], s[40:41] op_sel_hi:[1,0]\n"
      "v_pk_add_f32 v[108:109], v[108:109], s[40:41] op_sel_hi:[1,0]\n"
      "v_pk_add_f32 v[110:111], v[110:111], s[40:41] op_sel_hi:[1,0]\n"
      "global_load_dwordx4 a[80:83], v34, s[8:9] offset:0 sc1\n"
      "v_cvt_pk_bf16_f32 v104, 0, v104\n"
      "v_cvt_pk_bf16_f32 v105, 0, v105\n"
      "v_cvt_pk_bf16_f32 v106, 0, v106\n"
      "v_cvt_pk_bf16_f32 v107, 0, v107\n"
      "v_cvt_pk_bf16_f32 v108, 0, v108\n"
      "v_cvt_pk_bf16_f32 v109, 0, v109\n"
      "v_cvt_pk_bf16_f32 v110, 0, v110\n"
      "v_cvt_pk_bf16_f32 v111, 0, v111\n"
      "v_and_b32 v120, 0xffff0000, v235\n"
      "v_lshlrev_b32 v121, 16, v235\n"
      "v_and_b32 v122, 0xffff0000, v234\n"
      "v_lshlrev_b32 v123, 16, v234\n"
      "global_load_dwordx4 a[84:87], v34, s[8:9] offset:1024 sc1\n"
      "v_and_b32 v124, 0xffff0000, v233\n"
      "v_lshlrev_b32 v125, 16, v233\n"
      "v_and_b32 v126, 0xffff0000, v232\n"
      "v_lshlrev_b32 v127, 16, v232\n"
      "v_pk_mul_f32 v[120:121], v[120:121], s[14:15] op_sel_hi:[1,0]\n"
      "v_pk_mul_f32 v[122:123], v[122:123], s[14:15] op_sel_hi:[1,0]\n"
      "v_pk_mul_f32 v[124:125], v[124:125], s[14:15] op_sel_hi:[1,0]\n"
      "v_pk_mul_f32 v[126:127], v[126:127], s[14:15] op_sel_hi:[1,0]\n"
      "v_cvt_pk_bf16_f32 v120, 0, v120\n"
      "v_cvt_pk_bf16_f32 v121, 0, v121\n"
      "v_cvt_pk_bf16_f32 v122, 0, v122\n"
      "v_cvt_pk_bf16_f32 v123, 0, v123\n"
      "global_load_dwordx4 a[88:91], v34, s[8:9] offset:2048 sc1\n"
      "v_cvt_pk_bf16_f32 v124, 0, v124\n"
      "v_cvt_pk_bf16_f32 v125, 0, v125\n"
      "v_cvt_pk_bf16_f32 v126, 0, v126\n"
      "v_cvt_pk_bf16_f32 v127, 0, v127\n"
      "s_add_u32 s43, s89, 32768\n"
      "s_add_u32 s53, s89, 0x1000000\n"
      "s_add_u32 s54, s43, 0x1000000\n"
      "s_add_u32 s55, s79, 176128\n"
      "s_add_u32 s45, s81, 65536\n"
      "s_waitcnt lgkmcnt(0)\n"
      "v_pk_add_f32 v[64:65], v[64:65], v[72:73]\n"
      "v_pk_add_f32 v[66:67], v[66:67], v[74:75]\n"
      "global_load_dwordx4 a[92:95], v34, s[8:9] offset:3072 sc1\n"
      "v_pk_add_f32 v[68:69], v[68:69], v[76:77]\n"
      "v_pk_add_f32 v[70:71], v[70:71], v[78:79]\n"
      "v_cvt_pk_bf16_f32 v64, 0, v64\n"
      "v_cvt_pk_bf16_f32 v65, 0, v65\n"
      "v_cvt_pk_bf16_f32 v66, 0, v66\n"
      "v_cvt_pk_bf16_f32 v67, 0, v67\n"
      "v_cvt_pk_bf16_f32 v68, 0, v68\n"
      "v_cvt_pk_bf16_f32 v69, 0, v69\n"
      "v_cvt_pk_bf16_f32 v70, 0, v70\n"
      "v_cvt_pk_bf16_f32 v71, 0, v71\n"
      "v_pk_add_f32 v[120:121], v[120:121], v[64:65]\n"
      "v_pk_add_f32 v[122:123], v[122:123], v[66:67]\n"
      "v_add_u32 v34, 28672, v7\n"
      "global_load_dwordx4 a[96:99], v34, s[8:9] offset:-4096 sc1\n"
      "v_pk_add_f32 v[124:125], v[124:125], v[68:69]\n"
      "v_pk_add_f32 v[126:127], v[126:127], v[70:71]\n"
      "v_cvt_pk_bf16_f32 v120, 0, v120\n"
      "v_cvt_pk_bf16_f32 v121, 0, v121\n"
      "v_cvt_pk_bf16_f32 v122, 0, v122\n"
      "v_cvt_pk_bf16_f32 v123, 0, v123\n"
      "v_cvt_pk_bf16_f32 v124, 0, v124\n"
      "v_cvt_pk_bf16_f32 v125, 0, v125\n"
      "v_cvt_pk_bf16_f32 v126, 0, v126\n"
      "v_cvt_pk_bf16_f32 v127, 0, v127\n"
      "v_pk_mul_f32 v[104:105], v[104:105], v[120:121]\n"
      "v_pk_mul_f32 v[106:107], v[106:107], v[122:123]\n"
      "global_load_dwordx4 a[100:103], v34, s[8:9] offset:-3072 sc1\n"
      "v_pk_mul_f32 v[108:109], v[108:109], v[124:125]\n"
      "v_pk_mul_f32 v[110:111], v[110:111], v[126:127]\n"
      "v_cvt_pk_bf16_f32 v104, 0, v104\n"
      "v_cvt_pk_bf16_f32 v105, 0, v105\n"
      "v_cvt_pk_bf16_f32 v106, 0, v106\n"
      "v_cvt_pk_bf16_f32 v107, 0, v107\n"
      "v_cvt_pk_bf16_f32 v108, 0, v108\n"
      "v_cvt_pk_bf16_f32 v109, 0, v109\n"
      "v_cvt_pk_bf16_f32 v110, 0, v110\n"
      "v_cvt_pk_bf16_f32 v111, 0, v111\n"
      "v_pk_mul_f32 v[104:105], v[104:105], s[58:59] op_sel_hi:[1,0]\n"
      "v_pk_mul_f32 v[106:107], v[106:107], s[58:59] op_sel_hi:[1,0]\n"
      "global_load_dwordx4 a[104:107], v34, s[8:9] offset:-2048 sc1\n"
      "v_pk_mul_f32 v[108:109], v[108:109], s[58:59] op_sel_hi:[1,0]\n"
      "v_pk_mul_f32 v[110:111], v[110:111], s[58:59] op_sel_hi:[1,0]\n"
      "v_cvt_pk_bf16_f32 v104, 0, v104\n"
      "v_cvt_pk_bf16_f32 v105, 0, v105\n"
      "v_cvt_pk_bf16_f32 v106, 0, v106\n"
      "v_cvt_pk_bf16_f32 v107, 0, v107\n"
      "v_cvt_pk_bf16_f32 v108, 0, v108\n"
      "v_cvt_pk_bf16_f32 v109, 0, v109\n"
      "v_cvt_pk_bf16_f32 v110, 0, v110\n"
      "v_cvt_pk_bf16_f32 v111, 0, v111\n"
      "v_pk_add_f32 v[104:105], v[104:105], v[112:113]\n"
      "v_pk_add_f32 v[106:107], v[106:107], v[114:115]\n"
      "global_load_dwordx4 a[108:111], v34, s[8:9] offset:-1024 sc1\n"
      "v_pk_add_f32 v[108:109], v[108:109], v[116:117]\n"
      "v_pk_add_f32 v[110:111], v[110:111], v[118:119]\n"
      "v_cvt_pk_bf16_f32 v104, 0, v104\n"
      "v_cvt_pk_bf16_f32 v105, 0, v105\n"
      "v_cvt_pk_bf16_f32 v106, 0, v106\n"
      "v_cvt_pk_bf16_f32 v107, 0, v107\n"
      "v_cvt_pk_bf16_f32 v108, 0, v108\n"
      "v_cvt_pk_bf16_f32 v109, 0, v109\n"
      "v_cvt_pk_bf16_f32 v110, 0, v110\n"
      "v_cvt_pk_bf16_f32 v111, 0, v111\n"
      "v_pk_mul_f32 v[120:121], v[104:105], s[40:41] op_sel:[0,1] op_sel_hi:[1,1]\n"
      "v_pk_mul_f32 v[122:123], v[106:107], s[40:41] op_sel:[0,1] op_sel_hi:[1,1]\n"
      "v_pk_mul_f32 v[124:125], v[108:109], s[40:41] op_sel:[0,1] op_sel_hi:[1,1]\n"
      "v_pk_mul_f32 v[126:127], v[110:111], s[40:41] op_sel:[0,1] op_sel_hi:[1,1]\n"
      "v_cvt_pk_bf16_f32 v128, v120, v121\n"
      "v_cvt_pk_bf16_f32 v129, v122, v123\n"
      "v_cvt_pk_bf16_f32 v130, v124, v125\n"
      "v_cvt_pk_bf16_f32 v131, v126, v127\n"
      "buffer_store_dwordx4 v[128:131], v29, s[32:35], s89 offen sc1\n"
      "v_cvt_pk_bf16_f32 v132, v104, v105\n"
      "v_cvt_pk_bf16_f32 v133, v106, v107\n"
      "v_cvt_pk_bf16_f32 v134, v108, v109\n"
      "v_cvt_pk_bf16_f32 v135, v110, v111\n"
      "v_add3_u32 v35, v128, v129, v130\n"
      "v_add_u32 v35, v35, v131\n"
      "buffer_store_dwordx4 v[132:135], v36, s[32:35], s53 offen sc1\n"
      "v_add3_u32 v38, v132, v133, v134\n"
      "v_add_u32 v38, v38, v135\n"
      "v_lshlrev_b32 v112, 16, v196\n"
      "v_add_u32_dpp v35, v35, v35 quad_perm:[1,0,3,2] row_mask:0xf bank_mask:0xf\n"
      "v_and_b32 v113, 0xffff0000, v196\n"
      "v_add_u32_dpp v38, v38, v38 quad_perm:[1,0,3,2] row_mask:0xf bank_mask:0xf\n"
      "v_lshlrev_b32 v114, 16, v197\n"
      "v_add_u32_dpp v35, v35, v35 quad_perm:[2,3,0,1] row_mask:0xf bank_mask:0xf\n"
      "v_and_b32 v115, 0xffff0000, v197\n"
      "v_add_u32_dpp v38, v38, v38 quad_perm:[2,3,0,1] row_mask:0xf bank_mask:0xf\n"
      "v_lshlrev_b32 v116, 16, v198\n"
      "v_add_u32_dpp v35, v35, v35 row_half_mirror row_mask:0xf bank_mask:0xf\n"
      "v_and_b32 v117, 0xffff0000, v198\n"
      "v_add_u32_dpp v38, v38, v38 row_ror:8 row_mask:0xf bank_mask:0xf\n"
      "v_lshlrev_b32 v118, 16, v199\n"
      "v_cndmask_b32 v35, v35, v38, vcc\n"
      "v_and_b32 v119, 0xffff0000, v199\n"
      "v_sub_u32 v35, s30, v35\n"
      "v_pk_add_f32 v[104:105], v[112:113], v[96:97] neg_lo:[0,1] neg_hi:[0,1]\n"
      "buffer_store_dword v35, v30, s[32:35], s81 offen sc1\n"
      "s_waitcnt vmcnt(23)\n"
      "buffer_store_dwordx2 v[52:53], v48, s[32:35], s88 offen sc1\n"
      "v_pk_add_f32 v[106:107], v[114:115], v[98:99] neg_lo:[0,1] neg_hi:[0,1]\n"
      "v_pk_add_f32 v[108:109], v[116:117], v[100:101] neg_lo:[0,1] neg_hi:[0,1]\n"
      "v_pk_add_f32 v[110:111], v[118:119], v[102:103] neg_lo:[0,1] neg_hi:[0,1]\n"
      "v_cvt_pk_bf16_f32 v104, 0, v104\n"
      "v_cvt_pk_bf16_f32 v105, 0, v105\n"
      "v_cvt_pk_bf16_f32 v106, 0, v106\n"
      "v_cvt_pk_bf16_f32 v107, 0, v107\n"
      "v_cvt_pk_bf16_f32 v108, 0, v108\n"
      "v_cvt_pk_bf16_f32 v109, 0, v109\n"
      "v_cvt_pk_bf16_f32 v110, 0, v110\n"
      "v_cvt_pk_bf16_f32 v111, 0, v111\n"
      "v_pk_mul_f32 v[104:105], v[104:105], v[208:209]\n"
      "buffer_store_dwordx2 v[52:53], v48, s[32:35], s55 offen sc1\n"
      "v_pk_mul_f32 v[106:107], v[106:107], v[212:213]\n"
      "v_pk_mul_f32 v[108:109], v[108:109], v[216:217]\n"
      "v_pk_mul_f32 v[110:111], v[110:111], v[220:221]\n"
      "v_cvt_pk_bf16_f32 v104, 0, v104\n"
      "v_cvt_pk_bf16_f32 v105, 0, v105\n"
      "v_cvt_pk_bf16_f32 v106, 0, v106\n"
      "v_cvt_pk_bf16_f32 v107, 0, v107\n"
      "v_cvt_pk_bf16_f32 v108, 0, v108\n"
      "v_cvt_pk_bf16_f32 v109, 0, v109\n"
      "v_cvt_pk_bf16_f32 v110, 0, v110\n"
      "v_cvt_pk_bf16_f32 v111, 0, v111\n"
      "v_pk_mul_f32 v[104:105], v[104:105], s[50:51] op_sel_hi:[1,0]\n"
      "s_add_u32 m0, s73, 0\n"
      "v_add_u32 v43, 0, v40\n"
      "global_load_lds_dwordx4 v43, s[4:5] offset:0 sc1\n"
      "v_pk_mul_f32 v[106:107], v[106:107], s[50:51] op_sel_hi:[1,0]\n"
      "v_pk_mul_f32 v[108:109], v[108:109], s[50:51] op_sel_hi:[1,0]\n"
      "v_pk_mul_f32 v[110:111], v[110:111], s[50:51] op_sel_hi:[1,0]\n"
      "v_cvt_pk_bf16_f32 v104, 0, v104\n"
      "v_cvt_pk_bf16_f32 v105, 0, v105\n"
      "v_cvt_pk_bf16_f32 v106, 0, v106\n"
      "v_cvt_pk_bf16_f32 v107, 0, v107\n"
      "v_cvt_pk_bf16_f32 v108, 0, v108\n"
      "v_cvt_pk_bf16_f32 v109, 0, v109\n"
      "v_cvt_pk_bf16_f32 v110, 0, v110\n"
      "v_cvt_pk_bf16_f32 v111, 0, v111\n"
      "v_pk_add_f32 v[104:105], v[104:105], s[48:49] op_sel_hi:[1,0]\n"
      "global_load_lds_dwordx4 v43, s[4:5] offset:1024 sc1\n"
      "v_pk_add_f32 v[106:107], v[106:107], s[48:49] op_sel_hi:[1,0]\n"
      "v_pk_add_f32 v[108:109], v[108:109], s[48:49] op_sel_hi:[1,0]\n"
      "v_pk_add_f32 v[110:111], v[110:111], s[48:49] op_sel_hi:[1,0]\n"
      "v_cvt_pk_bf16_f32 v104, 0, v104\n"
      "v_cvt_pk_bf16_f32 v105, 0, v105\n"
      "v_cvt_pk_bf16_f32 v106, 0, v106\n"
      "v_cvt_pk_bf16_f32 v107, 0, v107\n"
      "v_cvt_pk_bf16_f32 v108, 0, v108\n"
      "v_cvt_pk_bf16_f32 v109, 0, v109\n"
      "v_cvt_pk_bf16_f32 v110, 0, v110\n"
      "v_cvt_pk_bf16_f32 v111, 0, v111\n"
      "v_and_b32 v120, 0xffff0000, v239\n"
      "global_load_lds_dwordx4 v43, s[4:5] offset:2048 sc1\n"
      "v_lshlrev_b32 v121, 16, v239\n"
      "v_and_b32 v122, 0xffff0000, v238\n"
      "v_lshlrev_b32 v123, 16, v238\n"
      "v_and_b32 v124, 0xffff0000, v237\n"
      "v_lshlrev_b32 v125, 16, v237\n"
      "v_and_b32 v126, 0xffff0000, v236\n"
      "v_lshlrev_b32 v127, 16, v236\n"
      "v_pk_mul_f32 v[120:121], v[120:121], s[14:15] op_sel:[0,1] op_sel_hi:[1,1]\n"
      "v_pk_mul_f32 v[122:123], v[122:123], s[14:15] op_sel:[0,1] op_sel_hi:[1,1]\n"
      "v_pk_mul_f32 v[124:125], v[124:125], s[14:15] op_sel:[0,1] op_sel_hi:[1,1]\n"
      "v_pk_mul_f32 v[126:127], v[126:127], s[14:15] op_sel:[0,1] op_sel_hi:[1,1]\n"
      "v_cvt_pk_bf16_f32 v120, 0, v120\n"
      "global_load_lds_dwordx4 v43, s[4:5] offset:3072 sc1\n"
      "v_cvt_pk_bf16_f32 v121, 0, v121\n"
      "v_cvt_pk_bf16_f32 v122, 0, v122\n"
      "v_cvt_pk_bf16_f32 v123, 0, v123\n"
      "v_cvt_pk_bf16_f32 v124, 0, v124\n"
      "v_cvt_pk_bf16_f32 v125, 0, v125\n"
      "v_cvt_pk_bf16_f32 v126, 0, v126\n"
      "v_cvt_pk_bf16_f32 v127, 0, v127\n"
      "v_pk_add_f32 v[80:81], v[80:81], v[88:89]\n"
      "v_pk_add_f32 v[82:83], v[82:83], v[90:91]\n"
      "v_pk_add_f32 v[84:85], v[84:85], v[92:93]\n"
      "v_pk_add_f32 v[86:87], v[86:87], v[94:95]\n"
      "v_cvt_pk_bf16_f32 v80, 0, v80\n"
      "s_mov_b32 s47, 0\n"
      "buffer_load_dwordx2 v[2:3], v5, s[32:35], s47 offen sc1\n"
      "v_cvt_pk_bf16_f32 v81, 0, v81\n"
      "v_cvt_pk_bf16_f32 v82, 0, v82\n"
      "v_cvt_pk_bf16_f32 v83, 0, v83\n"
      "v_cvt_pk_bf16_f32 v84, 0, v84\n"
      "v_cvt_pk_bf16_f32 v85, 0, v85\n"
      "v_cvt_pk_bf16_f32 v86, 0, v86\n"
      "v_cvt_pk_bf16_f32 v87, 0, v87\n"
      "v_pk_add_f32 v[120:121], v[120:121], v[80:81]\n"
      "v_pk_add_f32 v[122:123], v[122:123], v[82:83]\n"
      "v_pk_add_f32 v[124:125], v[124:125], v[84:85]\n"
      "v_pk_add_f32 v[126:127], v[126:127], v[86:87]\n"
      "v_cvt_pk_bf16_f32 v120, 0, v120\n"
      "s_add_u32 m0, s73, 16384\n"
      "v_add_u32 v43, 81920, v40\n"
      "global_load_lds_dwordx4 v43, s[4:5] offset:0 sc1\n"
      "v_cvt_pk_bf16_f32 v121, 0, v121\n"
      "v_cvt_pk_bf16_f32 v122, 0, v122\n"
      "v_cvt_pk_bf16_f32 v123, 0, v123\n"
      "v_cvt_pk_bf16_f32 v124, 0, v124\n"
      "v_cvt_pk_bf16_f32 v125, 0, v125\n"
      "v_cvt_pk_bf16_f32 v126, 0, v126\n"
      "v_cvt_pk_bf16_f32 v127, 0, v127\n"
      "v_pk_mul_f32 v[104:105], v[104:105], v[120:121]\n"
      "v_pk_mul_f32 v[106:107], v[106:107], v[122:123]\n"
      "v_pk_mul_f32 v[108:109], v[108:109], v[124:125]\n"
      "v_pk_mul_f32 v[110:111], v[110:111], v[126:127]\n"
      "v_cvt_pk_bf16_f32 v104, 0, v104\n"
      "global_load_lds_dwordx4 v43, s[4:5] offset:1024 sc1\n"
      "v_cvt_pk_bf16_f32 v105, 0, v105\n"
      "v_cvt_pk_bf16_f32 v106, 0, v106\n"
      "v_cvt_pk_bf16_f32 v107, 0, v107\n"
      "v_cvt_pk_bf16_f32 v108, 0, v108\n"
      "v_cvt_pk_bf16_f32 v109, 0, v109\n"
      "v_cvt_pk_bf16_f32 v110, 0, v110\n"
      "v_cvt_pk_bf16_f32 v111, 0, v111\n"
      "v_pk_mul_f32 v[104:105], v[104:105], s[52:53] op_sel_hi:[1,0]\n"
      "v_pk_mul_f32 v[106:107], v[106:107], s[52:53] op_sel_hi:[1,0]\n"
      "v_pk_mul_f32 v[108:109], v[108:109], s[52:53] op_sel_hi:[1,0]\n"
      "v_pk_mul_f32 v[110:111], v[110:111], s[52:53] op_sel_hi:[1,0]\n"
      "v_cvt_pk_bf16_f32 v104, 0, v104\n"
      "global_load_lds_dwordx4 v43, s[4:5] offset:2048 sc1\n"
      "v_cvt_pk_bf16_f32 v105, 0, v105\n"
      "v_cvt_pk_bf16_f32 v106, 0, v106\n"
      "v_cvt_pk_bf16_f32 v107, 0, v107\n"
      "v_cvt_pk_bf16_f32 v108, 0, v108\n"
      "v_cvt_pk_bf16_f32 v109, 0, v109\n"
      "v_cvt_pk_bf16_f32 v110, 0, v110\n"
      "v_cvt_pk_bf16_f32 v111, 0, v111\n"
      "v_pk_add_f32 v[104:105], v[104:105], v[112:113]\n"
      "v_pk_add_f32 v[106:107], v[106:107], v[114:115]\n"
      "v_pk_add_f32 v[108:109], v[108:109], v[116:117]\n"
      "v_pk_add_f32 v[110:111], v[110:111], v[118:119]\n"
      "v_cvt_pk_bf16_f32 v104, 0, v104\n"
      "global_load_lds_dwordx4 v43, s[4:5] offset:3072 sc1\n"
      "v_cvt_pk_bf16_f32 v105, 0, v105\n"
      "v_cvt_pk_bf16_f32 v106, 0, v106\n"
      "v_cvt_pk_bf16_f32 v107, 0, v107\n"
      "v_cvt_pk_bf16_f32 v108, 0, v108\n"
      "v_cvt_pk_bf16_f32 v109, 0, v109\n"
      "v_cvt_pk_bf16_f32 v110, 0, v110\n"
      "v_cvt_pk_bf16_f32 v111, 0, v111\n"
      "v_pk_mul_f32 v[120:121], v[104:105], s[48:49] op_sel:[0,1] op_sel_hi:[1,1]\n"
      "v_pk_mul_f32 v[122:123], v[106:107], s[48:49] op_sel:[0,1] op_sel_hi:[1,1]\n"
      "v_pk_mul_f32 v[124:125], v[108:109], s[48:49] op_sel:[0,1] op_sel_hi:[1,1]\n"
      "v_pk_mul_f32 v[126:127], v[110:111], s[48:49] op_sel:[0,1] op_sel_hi:[1,1]\n"
      "v_cvt_pk_bf16_f32 v136, v120, v121\n"
      "v_cvt_pk_bf16_f32 v137, v122, v123\n"
      "v_cvt_pk_bf16_f32 v138, v124, v125\n"
      "v_cvt_pk_bf16_f32 v139, v126, v127\n"
      "buffer_store_dwordx4 v[136:139], v29, s[32:35], s43 offen sc1\n"
      "v_cvt_pk_bf16_f32 v140, v104, v105\n"
      "v_cvt_pk_bf16_f32 v141, v106, v107\n"
      "v_cvt_pk_bf16_f32 v142, v108, v109\n"
      "v_cvt_pk_bf16_f32 v143, v110, v111\n"
      "v_add3_u32 v35, v136, v137, v138\n"
      "v_add_u32 v35, v35, v139\n"
      "buffer_store_dwordx4 v[140:143], v36, s[32:35], s54 offen sc1\n"
      "v_add3_u32 v38, v140, v141, v142\n"
      "v_add_u32 v38, v38, v143\n"
      "v_add_u32_dpp v35, v35, v35 quad_perm:[1,0,3,2] row_mask:0xf bank_mask:0xf\n"
      "s_nop 0\n"
      "v_add_u32_dpp v38, v38, v38 quad_perm:[1,0,3,2] row_mask:0xf bank_mask:0xf\n"
      "v_add_u32_dpp v35, v35, v35 quad_perm:[2,3,0,1] row_mask:0xf bank_mask:0xf\n"
      "s_nop 0\n"
      "v_add_u32_dpp v38, v38, v38 quad_perm:[2,3,0,1] row_mask:0xf bank_mask:0xf\n"
      "v_add_u32_dpp v35, v35, v35 row_half_mirror row_mask:0xf bank_mask:0xf\n"
      "s_nop 0\n"
      "v_add_u32_dpp v38, v38, v38 row_ror:8 row_mask:0xf bank_mask:0xf\n"
      "v_cndmask_b32 v35, v35, v38, vcc\n"
      "v_sub_u32 v35, s30, v35\n"
      "buffer_store_dword v35, v30, s[32:35], s45 offen sc1\n"
      "global_load_dwordx4 a[112:115], v34, s[8:9] offset:0 sc1\n"
      "global_load_dwordx4 a[116:119], v34, s[8:9] offset:1024 sc1\n"
      "global_load_dwordx4 a[120:123], v34, s[8:9] offset:2048 sc1\n"
      "global_load_dwordx4 a[124:127], v34, s[8:9] offset:3072 sc1\n"
      "v_add_u32 v34, 36864, v7\n"
      "global_load_dwordx4 a[128:131], v34, s[8:9] offset:-4096 sc1\n"
      "global_load_dwordx4 a[132:135], v34, s[8:9] offset:-3072 sc1\n"
      "global_load_dwordx4 a[136:139], v34, s[8:9] offset:-2048 sc1\n"
      "global_load_dwordx4 a[140:143], v34, s[8:9] offset:-1024 sc1\n"
      "global_load_dwordx4 a[144:147], v34, s[8:9] offset:0 sc1\n"
      "global_load_dwordx4 a[148:151], v34, s[8:9] offset:1024 sc1\n"
      "global_load_dwordx4 a[152:155], v34, s[8:9] offset:2048 sc1\n"
      "global_load_dwordx4 a[156:159], v34, s[8:9] offset:3072 sc1\n"
      "v_xor_b32 v33, 32, v6\n"
      "v_xor_b32 v34, 64, v6\n"
      "v_xor_b32 v35, 96, v6\n"
      "v_add_u32 v45, 45056, v7\n"
      "s_waitcnt vmcnt(19)\n"
      "ds_read_b128 v[248:251], v40 offset:0\n"
      "ds_read_b128 v[252:255], v40 offset:1024\n"
      "ds_read_b128 v[36:39], v40 offset:2048\n"
      "ds_read_b128 v[60:63], v40 offset:3072\n"
      "s_waitcnt lgkmcnt(3)\n"
      "v_add3_u32 v2, v2, v248, v249\n"
      "v_add3_u32 v2, v2, v250, v251\n"
      "s_waitcnt lgkmcnt(2)\n"
      "v_add3_u32 v2, v2, v252, v253\n"
      "v_add3_u32 v2, v2, v254, v255\n"
      "s_waitcnt lgkmcnt(1)\n"
      "v_add3_u32 v2, v2, v36, v37\n"
      "v_add3_u32 v2, v2, v38, v39\n"
      "s_waitcnt lgkmcnt(0)\n"
      "v_add3_u32 v2, v2, v60, v61\n"
      "v_add3_u32 v2, v2, v62, v63\n"
      "s_nop 1\n"
      "v_add_u32_dpp v2, v2, v2 quad_perm:[1,0,3,2] row_mask:0xf bank_mask:0xf\n"
      "s_nop 1\n"
      "v_add_u32_dpp v2, v2, v2 quad_perm:[2,3,0,1] row_mask:0xf bank_mask:0xf\n"
      "s_nop 1\n"
      "v_add_u32_dpp v2, v2, v2 row_half_mirror row_mask:0xf bank_mask:0xf\n"
      "v_cmp_ne_u32 vcc, s71, v2\n"
      "s_cbranch_vccnz L_sx0_%=\n"
      "L_bx0_%=:\n"
      "s_barrier\n"
      "ds_read_b64_tr_b16 v[64:65], v6 offset:0\n"
      "ds_read_b64_tr_b16 v[66:67], v6 offset:2048\n"
      "ds_read_b64_tr_b16 v[68:69], v33 offset:0\n"
      "ds_read_b64_tr_b16 v[70:71], v33 offset:2048\n"
      "ds_read_b64_tr_b16 v[72:73], v34 offset:0\n"
      "ds_read_b64_tr_b16 v[74:75], v34 offset:2048\n"
      "ds_read_b64_tr_b16 v[76:77], v35 offset:0\n"
      "ds_read_b64_tr_b16 v[78:79], v35 offset:2048\n"
      "ds_read_b64_tr_b16 v[80:81], v6 offset:4096\n"
      "ds_read_b64_tr_b16 v[82:83], v6 offset:6144\n"
      "ds_read_b64_tr_b16 v[84:85], v33 offset:4096\n"
      "ds_read_b64_tr_b16 v[86:87], v33 offset:6144\n"
      "ds_read_b64_tr_b16 v[88:89], v34 offset:4096\n"
      "ds_read_b64_tr_b16 v[90:91], v34 offset:6144\n"
      "ds_read_b64_tr_b16 v[92:93], v35 offset:4096\n"
      "ds_read_b64_tr_b16 v[94:95], v35 offset:6144\n"
      "s_waitcnt lgkmcnt(0)\n"
      "v_mfma_f32_16x16x32_bf16 v[128:131], a[0:3], v[64:67], 0\n"
      "global_load_dwordx4 a[160:163], v45, s[8:9] offset:-4096 sc1\n"
      "v_lshrrev_b32 v42, 8, v7\n"
      "v_mfma_f32_16x16x32_bf16 v[144:147], a[4:7], v[64:67], 0\n"
      "ds_read_b64_tr_b16 v[96:97], v6 offset:8192\n"
      "v_bfe_u32 v41, v7, 4, 4\n"
      "v_mfma_f32_16x16x32_bf16 v[160:163], a[8:11], v[64:67], 0\n"
      "ds_read_b64_tr_b16 v[98:99], v6 offset:10240\n"
      "s_waitcnt vmcnt(16)\n"
      "v_mfma_f32_16x16x32_bf16 v[132:135], a[0:3], v[68:71], 0\n"
      "ds_read_b128 v[248:251], v40 offset:16384\n"
      "v_min_u32 v42, 2, v42\n"
      "v_mfma_f32_16x16x32_bf16 v[148:151], a[4:7], v[68:71], 0\n"
      "global_load_dwordx4 a[164:167], v45, s[8:9] offset:-3072 sc1\n"
      "ds_read_b128 v[252:255], v40 offset:17408\n"
      "ds_read_b64_tr_b16 v[100:101], v33 offset:8192\n"
      "v_min_u32 v41, 11, v41\n"
      "v_mfma_f32_16x16x32_bf16 v[164:167], a[8:11], v[68:71], 0\n"
      "ds_read_b128 v[36:39], v40 offset:18432\n"
      "ds_read_b64_tr_b16 v[102:103], v33 offset:10240\n"
      "v_mad_u32_u24 v42, v42, 48, v41\n"
      "v_mfma_f32_16x16x32_bf16 v[136:139], a[0:3], v[72:75], 0\n"
      "ds_read_b128 v[60:63], v40 offset:19456\n"
      "ds_read_b64_tr_b16 v[104:105], v34 offset:8192\n"
      "v_lshl_add_u32 v42, v42, 6, s64\n"
      "v_mfma_f32_16x16x32_bf16 v[152:155], a[4:7], v[72:75], 0\n"
      "ds_read_b64_tr_b16 v[106:107], v34 offset:10240\n"
      "s_add_u32 s75, s76, 176128\n"
      "v_mfma_f32_16x16x32_bf16 v[168:171], a[8:11], v[72:75], 0\n"
      "global_load_dwordx4 a[168:171], v45, s[8:9] offset:-2048 sc1\n"
      "ds_read_b64_tr_b16 v[108:109], v35 offset:8192\n"
      "v_mfma_f32_16x16x32_bf16 v[140:143], a[0:3], v[76:79], 0\n"
      "ds_read_b64_tr_b16 v[110:111], v35 offset:10240\n"
      "v_mfma_f32_16x16x32_bf16 v[156:159], a[4:7], v[76:79], 0\n"
      "ds_read_b64_tr_b16 v[112:113], v6 offset:12288\n"
      "s_add_u32 m0, s73, 32768\n"
      "v_add_u32 v43, 16384, v40\n"
      "s_waitcnt lgkmcnt(5)\n"
      "v_add3_u32 v3, v3, v248, v249\n"
      "v_add3_u32 v3, v3, v250, v251\n"
      "v_mfma_f32_16x16x32_bf16 v[172:175], a[8:11], v[76:79], 0\n"
      "ds_read_b64_tr_b16 v[114:115], v6 offset:14336\n"
      "v_add3_u32 v3, v3, v252, v253\n"
      "v_add3_u32 v3, v3, v254, v255\n"
      "v_mfma_f32_16x16x32_bf16 v[128:131], a[12:15], v[80:83], v[128:131]\n"
      "global_load_lds_dwordx4 v43, s[4:5] offset:0 sc1\n"
      "v_add3_u32 v3, v3, v36, v37\n"
      "v_add3_u32 v3, v3, v38, v39\n"
      "v_mfma_f32_16x16x32_bf16 v[144:147], a[16:19], v[80:83], v[144:147]\n"
      "ds_read_b64_tr_b16 v[116:117], v33 offset:12288\n"
      "v_add3_u32 v3, v3, v60, v61\n"
      "v_mfma_f32_16x16x32_bf16 v[160:163], a[20:23], v[80:83], v[160:163]\n"
      "ds_read_b64_tr_b16 v[118:119], v33 offset:14336\n"
      "v_add3_u32 v3, v3, v62, v63\n"
      "v_mfma_f32_16x16x32_bf16 v[132:135], a[12:15], v[84:87], v[132:135]\n"
      "ds_read_b64_tr_b16 v[120:121], v34 offset:12288\n"
      "v_mfma_f32_16x16x32_bf16 v[148:151], a[16:19], v[84:87], v[148:151]\n"
      "global_load_lds_dwordx4 v43, s[4:5] offset:1024 sc1\n"
      "ds_read_b64_tr_b16 v[122:123], v34 offset:14336\n"
      "v_add_u32_dpp v3, v3, v3 quad_perm:[1,0,3,2] row_mask:0xf bank_mask:0xf\n"
      "v_mfma_f32_16x16x32_bf16 v[164:167], a[20:23], v[84:87], v[164:167]\n"
      "ds_read_b64_tr_b16 v[124:125], v35 offset:12288\n"
      "v_mfma_f32_16x16x32_bf16 v[136:139], a[12:15], v[88:91], v[136:139]\n"
      "ds_read_b64_tr_b16 v[126:127], v35 offset:14336\n"
      "v_add_u32_dpp v3, v3, v3 quad_perm:[2,3,0,1] row_mask:0xf bank_mask:0xf\n"
      "v_mfma_f32_16x16x32_bf16 v[152:155], a[16:19], v[88:91], v[152:155]\n"
      "v_mfma_f32_16x16x32_bf16 v[168:171], a[20:23], v[88:91], v[168:171]\n"
      "global_load_lds_dwordx4 v43, s[4:5] offset:2048 sc1\n"
      "v_add_u32_dpp v3, v3, v3 row_half_mirror row_mask:0xf bank_mask:0xf\n"
      "v_cmp_ne_u32 vcc, s71, v3\n"
      "v_mfma_f32_16x16x32_bf16 v[140:143], a[12:15], v[92:95], v[140:143]\n"
      "v_mfma_f32_16x16x32_bf16 v[156:159], a[16:19], v[92:95], v[156:159]\n"
      "s_cbranch_vccnz L_sx1_%=\n"
      "L_bx1_%=:\n"
      "v_mfma_f32_16x16x32_bf16 v[172:175], a[20:23], v[92:95], v[172:175]\n"
      "s_waitcnt lgkmcnt(0)\n"
      "s_barrier\n"
      "v_mfma_f32_16x16x32_bf16 v[128:131], a[24:27], v[96:99], v[128:131]\n"
      "global_load_lds_dwordx4 v43, s[4:5] offset:3072 sc1\n"
      "s_mov_b32 s47, 32768\n"
      "v_mfma_f32_16x16x32_bf16 v[144:147], a[28:31], v[96:99], v[144:147]\n"
      "ds_read_b64_tr_b16 v[64:65], v6 offset:16384\n"
      "v_and_b32 v41, 0x1f0, v7\n"
      "v_mfma_f32_16x16x32_bf16 v[160:163], a[32:35], v[96:99], v[160:163]\n"
      "buffer_load_dwordx2 v[48:49], v5, s[32:35], s47 offen sc1\n"
      "v_min_u32 v41, 0x170, v41\n"
      "v_mfma_f32_16x16x32_bf16 v[132:135], a[24:27], v[100:103], v[132:135]\n"
      "ds_read_b64_tr_b16 v[66:67], v6 offset:18432\n"
      "v_lshrrev_b32 v52, 9, v7\n"
      "v_mfma_f32_16x16x32_bf16 v[148:151], a[28:31], v[100:103], v[148:151]\n"
      "ds_read_b64_tr_b16 v[68:69], v34 offset:16384\n"
      "v_mul_u32_u24 v52, 0x3c000, v52\n"
      "v_mfma_f32_16x16x32_bf16 v[164:167], a[32:35], v[100:103], v[164:167]\n"
      "ds_read_b64_tr_b16 v[70:71], v34 offset:18432\n"
      "v_add3_u32 v53, v52, v41, s65\n"
      "v_mfma_f32_16x16x32_bf16 v[136:139], a[24:27], v[104:107], v[136:139]\n"
      "ds_read_b64_tr_b16 v[72:73], v33 offset:16384\n"
      "s_mul_i32 s96, s60, 384\n"
      "v_mfma_f32_16x16x32_bf16 v[152:155], a[28:31], v[104:107], v[152:155]\n"
      "ds_read_b64_tr_b16 v[74:75], v33 offset:18432\n"
      "s_add_u32 m0, s73, 49152\n"
      "v_add_u32 v43, 32768, v40\n"
      "v_mfma_f32_16x16x32_bf16 v[168:171], a[32:35], v[104:107], v[168:171]\n"
      "global_load_dwordx2 v[58:59], v42, s[26:27] sc1\n"
      "global_load_lds_dwordx4 v43, s[4:5] offset:0 sc1\n"
      "ds_read_b64_tr_b16 v[76:77], v35 offset:16384\n"
      "s_add_u32 s96, s96, 160768\n"
      "v_mfma_f32_16x16x32_bf16 v[140:143], a[24:27], v[108:111], v[140:143]\n"
      "ds_read_b64_tr_b16 v[78:79], v35 offset:18432\n"
      "v_lshrrev_b32 v52, 4, v7\n"
      "v_mfma_f32_16x16x32_bf16 v[156:159], a[28:31], v[108:111], v[156:159]\n"
      "ds_read_b64_tr_b16 v[80:81], v6 offset:20480\n"
      "v_and_b32 v52, 0x30, v52\n"
      "v_mfma_f32_16x16x32_bf16 v[172:175], a[32:35], v[108:111], v[172:175]\n"
      "ds_read_b64_tr_b16 v[82:83], v6 offset:22528\n"
      "v_add_u32 v52, s96, v52\n"
      "v_mfma_f32_16x16x32_bf16 v[128:131], a[36:39], v[112:115], v[128:131]\n"
      "global_load_lds_dwordx4 v43, s[4:5] offset:1024 sc1\n"
      "v_bfe_u32 v30, v7, 4, 3\n"
      "v_mfma_f32_16x16x32_bf16 v[144:147], a[40:43], v[112:115], v[144:147]\n"
      "ds_read_b64_tr_b16 v[84:85], v34 offset:20480\n"
      "v_lshrrev_b32 v31, 8, v7\n"
      "v_mfma_f32_16x16x32_bf16 v[160:163], a[44:47], v[112:115], v[160:163]\n"
      "ds_read_b64_tr_b16 v[86:87], v34 offset:22528\n"
      "v_lshl_add_u32 v31, v30, 2, v31\n"
      "v_mfma_f32_16x16x32_bf16 v[132:135], a[36:39], v[116:119], v[132:135]\n"
      "ds_read_b64_tr_b16 v[88:89], v33 offset:20480\n"
      "v_sub_u32 v30, 2, v30\n"
      "v_mfma_f32_16x16x32_bf16 v[148:151], a[40:43], v[116:119], v[148:151]\n"
      "global_load_lds_dwordx4 v43, s[4:5] offset:2048 sc1\n"
      "ds_read_b64_tr_b16 v[90:91], v33 offset:22528\n"
      "v_and_b32 v29, 64, v4\n"
      "v_mfma_f32_16x16x32_bf16 v[164:167], a[44:47], v[116:119], v[164:167]\n"
      "ds_read_b64_tr_b16 v[92:93], v35 offset:20480\n"
      "v_lshl_add_u32 v31, v31, 9, v29\n"
      "v_mfma_f32_16x16x32_bf16 v[136:139], a[36:39], v[120:123], v[136:139]\n"
      "buffer_load_dwordx2 v[46:47], v51, s[32:35], s75 offen sc1\n"
      "v_and_b32 v30, 0x80000000, v30\n"
      "v_mfma_f32_16x16x32_bf16 v[152:155], a[40:43], v[120:123], v[152:155]\n"
      "ds_read_b64_tr_b16 v[94:95], v35 offset:22528\n"
      "v_or_b32 v31, v31, v30\n"
      "v_mfma_f32_16x16x32_bf16 v[168:171], a[44:47], v[120:123], v[168:171]\n"
      "global_load_lds_dwordx4 v43, s[4:5] offset:3072 sc1\n"
      "s_mov_b32 s47, 65536\n"
      "v_mfma_f32_16x16x32_bf16 v[140:143], a[36:39], v[124:127], v[140:143]\n"
      "v_add_u32 v50, s94, v7\n"
      "s_mul_i32 s72, s30, 3\n"
      "v_mfma_f32_16x16x32_bf16 v[156:159], a[40:43], v[124:127], v[156:159]\n"
      "buffer_load_dwordx2 v[54:55], v5, s[32:35], s47 offen sc1\n"
      "s_lshl_b32 s42, s60, 5\n"
      "v_mfma_f32_16x16x32_bf16 v[172:175], a[44:47], v[124:127], v[172:175]\n"
      "v_xor_b32 v27, s42, v6\n"
      "s_waitcnt lgkmcnt(0)\n"
      "v_mfma_f32_16x16x32_bf16 v[176:179], a[0:3], v[64:67], 0\n"
      "ds_read_b64_tr_b16 v[96:97], v6 offset:24576\n"
      "v_add_u32 v27, 110592, v27\n"
      "v_mov_b32 v18, 0\n"
      "v_mov_b32 v19, 0\n"
      "v_mov_b32 v28, 0x3f803f80\n"
      "v_mov_b32 v29, 0x3f803f80\n"
      "v_mov_b32 v30, 0x3f803f80\n"
      "s_mul_i32 s40, s60, 9216\n"
      "v_mfma_f32_16x16x32_bf16 v[184:187], a[4:7], v[64:67], 0\n"
      "ds_read_b64_tr_b16 v[98:99], v6 offset:26624\n"
      "s_add_u32 s40, s40, 122880\n"
      "v_mfma_f32_16x16x32_bf16 v[192:195], a[8:11], v[64:67], 0\n"
      "ds_read_b64_tr_b16 v[100:101], v34 offset:24576\n"
      "s_waitcnt vmcnt(7)\n"
      "v_mfma_f32_16x16x32_bf16 v[180:183], a[0:3], v[68:71], 0\n"
      "ds_read_b128 v[248:251], v40 offset:32768\n"
      "s_lshl_b32 s41, s60, 13\n"
      "v_mfma_f32_16x16x32_bf16 v[188:191], a[4:7], v[68:71], 0\n"
      "ds_read_b128 v[252:255], v40 offset:33792\n"
      "s_add_u32 s41, s41, 65536\n"
      "v_mfma_f32_16x16x32_bf16 v[196:199], a[8:11], v[68:71], 0\n"
      "ds_read_b128 v[36:39], v40 offset:34816\n"
      "ds_read_b64_tr_b16 v[102:103], v34 offset:26624\n"
      "s_mov_b32 s48, 0xaaaaaaaa\n"
      "v_mfma_f32_16x16x32_bf16 v[200:203], a[0:3], v[72:75], 0\n"
      "ds_read_b128 v[60:63], v40 offset:35840\n"
      "ds_read_b64_tr_b16 v[104:105], v33 offset:24576\n"
      "s_mov_b32 m0, s94\n"
      "v_mfma_f32_16x16x32_bf16 v[208:211], a[4:7], v[72:75], 0\n"
      "buffer_load_dwordx4 v7, s[32:35], s67 offen offset:0 sc1 lds\n"
      "ds_read_b64_tr_b16 v[106:107], v33 offset:26624\n"
      "s_mov_b32 s49, s48\n"
      "v_mfma_f32_16x16x32_bf16 v[216:219], a[8:11], v[72:75], 0\n"
      "ds_read_b64_tr_b16 v[108:109], v35 offset:24576\n"
      "s_mov_b32 s50, 0xcccccccc\n"
      "v_mfma_f32_16x16x32_bf16 v[204:207], a[0:3], v[76:79], 0\n"
      "ds_read_b64_tr_b16 v[110:111], v35 offset:26624\n"
      "s_mov_b32 s51, s50\n"
      "v_mfma_f32_16x16x32_bf16 v[212:215], a[4:7], v[76:79], 0\n"
      "ds_read_b64_tr_b16 v[112:113], v6 offset:28672\n"
      "s_waitcnt lgkmcnt(5)\n"
      "v_add3_u32 v48, v48, v248, v249\n"
      "v_add3_u32 v48, v48, v250, v251\n"
      "v_mfma_f32_16x16x32_bf16 v[220:223], a[8:11], v[76:79], 0\n"
      "buffer_load_dwordx4 v7, s[32:35], s67 offen offset:1024 sc1 lds\n"
      "v_add3_u32 v48, v48, v252, v253\n"
      "v_add3_u32 v48, v48, v254, v255\n"
      "v_mfma_f32_16x16x32_bf16 v[176:179], a[12:15], v[80:83], v[176:179]\n"
      "ds_read_b64_tr_b16 v[114:115], v6 offset:30720\n"
      "v_add3_u32 v48, v48, v36, v37\n"
      "v_add3_u32 v48, v48, v38, v39\n"
      "v_mfma_f32_16x16x32_bf16 v[184:187], a[16:19], v[80:83], v[184:187]\n"
      "ds_read_b64_tr_b16 v[116:117], v34 offset:28672\n"
      "v_add3_u32 v48, v48, v60, v61\n"
      "v_mfma_f32_16x16x32_bf16 v[192:195], a[20:23], v[80:83], v[192:195]\n"
      "ds_read_b64_tr_b16 v[118:119], v34 offset:30720\n"
      "v_add3_u32 v48, v48, v62, v63\n"
      "v_mfma_f32_16x16x32_bf16 v[180:183], a[12:15], v[84:87], v[180:183]\n"
      "buffer_load_dwordx4 v7, s[32:35], s67 offen offset:2048 sc1 lds\n"
      "ds_read_b64_tr_b16 v[120:121], v33 offset:28672\n"
      "s_lshl_b32 s59, s60, 31\n"
      "v_mfma_f32_16x16x32_bf16 v[188:191], a[16:19], v[84:87], v[188:191]\n"
      "ds_read_b64_tr_b16 v[122:123], v33 offset:30720\n"
      "v_add_u32_dpp v48, v48, v48 quad_perm:[1,0,3,2] row_mask:0xf bank_mask:0xf\n"
      "v_mfma_f32_16x16x32_bf16 v[196:199], a[20:23], v[84:87], v[196:199]\n"
      "ds_read_b64_tr_b16 v[124:125], v35 offset:28672\n"
      "s_lshl_b32 s54, s30, 1\n"
      "v_mfma_f32_16x16x32_bf16 v[200:203], a[12:15], v[88:91], v[200:203]\n"
      "ds_read_b64_tr_b16 v[126:127], v35 offset:30720\n"
      "v_add_u32_dpp v48, v48, v48 quad_perm:[2,3,0,1] row_mask:0xf bank_mask:0xf\n"
      "v_mfma_f32_16x16x32_bf16 v[208:211], a[16:19], v[88:91], v[208:211]\n"
      "buffer_load_dword v32, v31, s[32:35], s68 offen sc1\n"
      "s_lshl_b32 s55, s60, 6\n"
      "v_mfma_f32_16x16x32_bf16 v[216:219], a[20:23], v[88:91], v[216:219]\n"
      "v_add_u32_dpp v48, v48, v48 row_half_mirror row_mask:0xf bank_mask:0xf\n"
      "v_cmp_ne_u32 vcc, s71, v48\n"
      "v_mfma_f32_16x16x32_bf16 v[204:207], a[12:15], v[92:95], v[204:207]\n"
      "s_add_u32 s55, s55, 163328\n"
      "v_mfma_f32_16x16x32_bf16 v[212:215], a[16:19], v[92:95], v[212:215]\n"
      "s_cbranch_vccnz L_sx2_%=\n"
      "L_bx2_%=:\n"
      "v_mfma_f32_16x16x32_bf16 v[220:223], a[20:23], v[92:95], v[220:223]\n"
      "s_waitcnt lgkmcnt(0)\n"
      "s_barrier\n"
      "v_mfma_f32_16x16x32_bf16 v[176:179], a[24:27], v[96:99], v[176:179]\n"
      "ds_read_b64_tr_b16 v[64:65], v6 offset:32768\n"
      "v_mfma_f32_16x16x32_bf16 v[184:187], a[28:31], v[96:99], v[184:187]\n"
      "ds_read_b64_tr_b16 v[66:67], v6 offset:34816\n"
      "v_mfma_f32_16x16x32_bf16 v[192:195], a[32:35], v[96:99], v[192:195]\n"
      "ds_read_b64_tr_b16 v[68:69], v33 offset:32768\n"
      "s_add_u32 m0, s73, 0\n"
      "v_add_u32 v43, 49152, v40\n"
      "v_add_u32 v44, 4096, v7\n"
      "s_waitcnt vmcnt(6)\n"
      "v_cmp_ne_u64 s[52:53], s[30:31], v[46:47]\n"
      "v_mfma_f32_16x16x32_bf16 v[180:183], a[24:27], v[100:103], v[180:183]\n"
      "ds_read_b64_tr_b16 v[70:71], v33 offset:34816\n"
      "v_cmp_ne_u64 vcc, s[30:31], v[58:59]\n"
      "v_mfma_f32_16x16x32_bf16 v[188:191], a[28:31], v[100:103], v[188:191]\n"
      "global_load_lds_dwordx4 v43, s[4:5] offset:0 sc1\n"
      "s_waitcnt vmcnt(5)\n"
      "v_mfma_f32_16x16x32_bf16 v[196:199], a[32:35], v[100:103], v[196:199]\n"
      "ds_read_b128 v[248:251], v40 offset:49152\n"
      "s_or_b64 vcc, vcc, s[52:53]\n"
      "v_mfma_f32_16x16x32_bf16 v[200:203], a[24:27], v[104:107], v[200:203]\n"
      "ds_read_b128 v[252:255], v40 offset:50176\n"
      "ds_read_b64_tr_b16 v[72:73], v34 offset:32768\n"
      "s_cbranch_vccnz L_sfd_%=\n"
      "L_bfd_%=:\n"
      "v_mfma_f32_16x16x32_bf16 v[208:211], a[28:31], v[104:107], v[208:211]\n"
      "ds_read_b128 v[36:39], v40 offset:51200\n"
      "ds_read_b64_tr_b16 v[74:75], v34 offset:34816\n"
      "v_add_u32 v51, s96, v41\n"
      "v_mfma_f32_16x16x32_bf16 v[216:219], a[32:35], v[104:107], v[216:219]\n"
      "global_load_lds_dwordx4 v43, s[4:5] offset:1024 sc1\n"
      "ds_read_b128 v[60:63], v40 offset:52224\n"
      "ds_read_b64_tr_b16 v[76:77], v35 offset:32768\n"
      "v_lshrrev_b32 v42, 2, v7\n"
      "v_mfma_f32_16x16x32_bf16 v[204:207], a[24:27], v[108:111], v[204:207]\n"
      "ds_read_b64_tr_b16 v[78:79], v35 offset:34816\n"
      "s_lshl_b32 s97, s60, 10\n"
      "v_mfma_f32_16x16x32_bf16 v[212:215], a[28:31], v[108:111], v[212:215]\n"
      "ds_read_b64_tr_b16 v[80:81], v6 offset:36864\n"
      "s_add_u32 s96, s97, 98304\n"
      "v_mfma_f32_16x16x32_bf16 v[220:223], a[32:35], v[108:111], v[220:223]\n"
      "ds_read_b64_tr_b16 v[82:83], v6 offset:38912\n"
      "v_add_u32 v47, s96, v7\n"
      "v_mfma_f32_16x16x32_bf16 v[176:179], a[36:39], v[112:115], v[176:179]\n"
      "global_load_lds_dwordx4 v43, s[4:5] offset:2048 sc1\n"
      "s_waitcnt lgkmcnt(4)\n"
      "v_add3_u32 v54, v54, v248, v249\n"
      "v_add3_u32 v54, v54, v250, v251\n"
      "v_mfma_f32_16x16x32_bf16 v[184:187], a[40:43], v[112:115], v[184:187]\n"
      "ds_read_b64_tr_b16 v[84:85], v33 offset:36864\n"
      "v_add3_u32 v54, v54, v252, v253\n"
      "v_add3_u32 v54, v54, v254, v255\n"
      "v_mfma_f32_16x16x32_bf16 v[192:195], a[44:47], v[112:115], v[192:195]\n"
      "ds_read_b64_tr_b16 v[86:87], v33 offset:38912\n"
      "v_add3_u32 v54, v54, v36, v37\n"
      "v_add3_u32 v54, v54, v38, v39\n"
      "v_mfma_f32_16x16x32_bf16 v[180:183], a[36:39], v[116:119], v[180:183]\n"
      "ds_read_b64_tr_b16 v[88:89], v34 offset:36864\n"
      "v_add3_u32 v54, v54, v60, v61\n"
      "v_mfma_f32_16x16x32_bf16 v[188:191], a[40:43], v[116:119], v[188:191]\n"
      "global_load_lds_dwordx4 v43, s[4:5] offset:3072 sc1\n"
      "ds_read_b64_tr_b16 v[90:91], v34 offset:38912\n"
      "v_add3_u32 v54, v54, v62, v63\n"
      "v_mfma_f32_16x16x32_bf16 v[196:199], a[44:47], v[116:119], v[196:199]\n"
      "ds_read_b64_tr_b16 v[92:93], v35 offset:36864\n"
      "s_mov_b32 s47, 98304\n"
      "v_mfma_f32_16x16x32_bf16 v[200:203], a[36:39], v[120:123], v[200:203]\n"
      "ds_read_b64_tr_b16 v[94:95], v35 offset:38912\n"
      "v_add_u32_dpp v54, v54, v54 quad_perm:[1,0,3,2] row_mask:0xf bank_mask:0xf\n"
      "v_mfma_f32_16x16x32_bf16 v[208:211], a[40:43], v[120:123], v[208:211]\n"
      "v_bfe_u32 v46, v7, 8, 1\n"
      "v_xor_b32 v46, s60, v46\n"
      "v_mfma_f32_16x16x32_bf16 v[216:219], a[44:47], v[120:123], v[216:219]\n"
      "buffer_load_dwordx2 v[56:57], v5, s[32:35], s47 offen sc1\n"
      "v_add_u32_dpp v54, v54, v54 quad_perm:[2,3,0,1] row_mask:0xf bank_mask:0xf\n"
      "v_mfma_f32_16x16x32_bf16 v[204:207], a[36:39], v[124:127], v[204:207]\n"
      "v_lshl_add_u32 v46, v46, 2, v7\n"
      "v_add_u32 v46, 98304, v46\n"
      "v_mfma_f32_16x16x32_bf16 v[212:215], a[40:43], v[124:127], v[212:215]\n"
      "v_add_u32_dpp v54, v54, v54 row_half_mirror row_mask:0xf bank_mask:0xf\n"
      "v_cmp_ne_u32 vcc, s71, v54\n"
      "v_mfma_f32_16x16x32_bf16 v[220:223], a[44:47], v[124:127], v[220:223]\n"
      "s_waitcnt lgkmcnt(0)\n"
      "v_mfma_f32_16x16x32_bf16 v[128:131], a[48:51], v[64:67], v[128:131]\n"
      "ds_read_b64_tr_b16 v[96:97], v6 offset:40960\n"
      "s_cbranch_vccnz L_sx3_%=\n"
      "L_bx3_%=:\n"
      "v_and_b32 v41, 0x78, v4\n"
      "v_mfma_f32_16x16x32_bf16 v[144:147], a[52:55], v[64:67], v[144:147]\n"
      "ds_read_b64_tr_b16 v[98:99], v6 offset:43008\n"
      "v_add_u32 v41, 162304, v41\n"
      "v_mfma_f32_16x16x32_bf16 v[160:163], a[56:59], v[64:67], v[160:163]\n"
      "ds_read_b64_tr_b16 v[100:101], v33 offset:40960\n"
      "s_add_u32 m0, s73, 16384\n"
      "v_add_u32 v43, 65536, v40\n"
      "s_waitcnt vmcnt(5)\n"
      "v_mfma_f32_16x16x32_bf16 v[132:135], a[48:51], v[68:71], v[132:135]\n"
      "ds_read_b128 v[248:251], v50 offset:0\n"
      "v_mfma_f32_16x16x32_bf16 v[148:151], a[52:55], v[68:71], v[148:151]\n"
      "global_load_dwordx4 v[224:227], v53, s[26:27] sc1\n"
      "ds_read_b128 v[252:255], v50 offset:1024\n"
      "v_mfma_f32_16x16x32_bf16 v[164:167], a[56:59], v[68:71], v[164:167]\n"
      "ds_read_b64_tr_b16 v[102:103], v33 offset:43008\n"
      "v_add_u32 v53, 0x6000, v53\n"
      "v_mfma_f32_16x16x32_bf16 v[136:139], a[48:51], v[72:75], v[136:139]\n"
      "ds_read_b128 v[36:39], v50 offset:2048\n"
      "ds_read_b64_tr_b16 v[104:105], v34 offset:40960\n"
      "v_mfma_f32_16x16x32_bf16 v[152:155], a[52:55], v[72:75], v[152:155]\n"
      "global_load_dwordx4 v[228:231], v53, s[26:27] sc1\n"
      "ds_read_b64_tr_b16 v[106:107], v34 offset:43008\n"
      "v_mfma_f32_16x16x32_bf16 v[168:171], a[56:59], v[72:75], v[168:171]\n"
      "ds_read_b64_tr_b16 v[108:109], v35 offset:40960\n"
      "v_add_u32 v53, 0x6000, v53\n"
      "v_mfma_f32_16x16x32_bf16 v[140:143], a[48:51], v[76:79], v[140:143]\n"
      "ds_read_b64_tr_b16 v[110:111], v35 offset:43008\n"
      "v_mfma_f32_16x16x32_bf16 v[156:159], a[52:55], v[76:79], v[156:159]\n"
      "global_load_dwordx4 v[232:235], v53, s[26:27] sc1\n"
      "s_waitcnt lgkmcnt(4)\n"
      "v_add3_u32 v32, v32, v248, v249\n"
      "v_add3_u32 v32, v32, v250, v251\n"
      "v_mfma_f32_16x16x32_bf16 v[172:175], a[56:59], v[76:79], v[172:175]\n"
      "ds_read_b64_tr_b16 v[112:113], v6 offset:45056\n"
      "v_add_u32 v53, 0x6000, v53\n"
      "v_add3_u32 v32, v32, v252, v253\n"
      "v_mfma_f32_16x16x32_bf16 v[128:131], a[60:63], v[80:83], v[128:131]\n"
      "ds_read_b64_tr_b16 v[114:115], v6 offset:47104\n"
      "v_add3_u32 v32, v32, v254, v255\n"
      "v_mfma_f32_16x16x32_bf16 v[144:147], a[64:67], v[80:83], v[144:147]\n"
      "global_load_dwordx4 v[236:239], v53, s[26:27] sc1\n"
      "ds_read_b64_tr_b16 v[116:117], v33 offset:45056\n"
      "v_add3_u32 v32, v32, v36, v37\n"
      "v_mfma_f32_16x16x32_bf16 v[160:163], a[68:71], v[80:83], v[160:163]\n"
      "ds_read_b64_tr_b16 v[118:119], v33 offset:47104\n"
      "v_add_u32 v53, 0x6000, v53\n"
      "v_add3_u32 v32, v32, v38, v39\n"
      "v_mfma_f32_16x16x32_bf16 v[132:135], a[60:63], v[84:87], v[132:135]\n"
      "ds_read_b64_tr_b16 v[120:121], v34 offset:45056\n"
      "v_mfma_f32_16x16x32_bf16 v[148:151], a[64:67], v[84:87], v[148:151]\n"
      "global_load_dwordx4 v[240:243], v53, s[26:27] sc1\n"
      "ds_read_b64_tr_b16 v[122:123], v34 offset:47104\n"
      "v_add_u32_dpp v32, v32, v32 quad_perm:[1,0,3,2] row_mask:0xf bank_mask:0xf\n"
      "v_mfma_f32_16x16x32_bf16 v[164:167], a[68:71], v[84:87], v[164:167]\n"
      "ds_read_b64_tr_b16 v[124:125], v35 offset:45056\n"
      "v_add_u32 v53, 0x6000, v53\n"
      "v_mfma_f32_16x16x32_bf16 v[136:139], a[60:63], v[88:91], v[136:139]\n"
      "ds_read_b64_tr_b16 v[126:127], v35 offset:47104\n"
      "v_add_u32_dpp v32, v32, v32 quad_perm:[2,3,0,1] row_mask:0xf bank_mask:0xf\n"
      "v_mfma_f32_16x16x32_bf16 v[152:155], a[64:67], v[88:91], v[152:155]\n"
      "global_load_dwordx4 v[244:247], v53, s[26:27] sc1\n"
      "v_mfma_f32_16x16x32_bf16 v[168:171], a[68:71], v[88:91], v[168:171]\n"
      "v_add_u32_dpp v32, v32, v32 row_half_mirror row_mask:0xf bank_mask:0xf\n"
      "v_cmp_ne_u32 vcc, s72, v32\n"
      "v_mfma_f32_16x16x32_bf16 v[140:143], a[60:63], v[92:95], v[140:143]\n"
      "v_mfma_f32_16x16x32_bf16 v[156:159], a[64:67], v[92:95], v[156:159]\n"
      "global_load_lds_dwordx4 v43, s[4:5] offset:0 sc1\n"
      "s_cbranch_vccnz L_sxp_%=\n"
      "L_bxp_%=:\n"
      "v_mfma_f32_16x16x32_bf16 v[172:175], a[68:71], v[92:95], v[172:175]\n"
      "s_waitcnt lgkmcnt(0)\n"
      "s_barrier\n"
      "v_mfma_f32_16x16x32_bf16 v[128:131], a[72:75], v[96:99], v[128:131]\n"
      "ds_read_b64_tr_b16 v[8:9], v27 offset:0\n"
      "v_mfma_f32_16x16x32_bf16 v[144:147], a[76:79], v[96:99], v[144:147]\n"
      "ds_read_b64_tr_b16 v[10:11], v27 offset:2048\n"
      "v_mfma_f32_16x16x32_bf16 v[160:163], a[80:83], v[96:99], v[160:163]\n"
      "global_load_lds_dwordx4 v43, s[4:5] offset:1024 sc1\n"
      "ds_read_b64_tr_b16 v[12:13], v27 offset:4096\n"
      "ds_read_b64_tr_b16 v[64:65], v6 offset:49152\n"
      "v_mfma_f32_16x16x32_bf16 v[132:135], a[72:75], v[100:103], v[132:135]\n"
      "ds_read_b64_tr_b16 v[14:15], v27 offset:6144\n"
      "ds_read_b64_tr_b16 v[66:67], v6 offset:51200\n"
      "v_mfma_f32_16x16x32_bf16 v[148:151], a[76:79], v[100:103], v[148:151]\n"
      "ds_read_b64_tr_b16 v[16:17], v27 offset:8192\n"
      "v_mov_b32 v31, 0x3f803f80\n"
      "v_mfma_f32_16x16x32_bf16 v[164:167], a[80:83], v[100:103], v[164:167]\n"
      "ds_read_b64_tr_b16 v[68:69], v33 offset:49152\n"
      "v_and_b32 v50, 0xf0, v7\n"
      "v_mfma_f32_16x16x32_bf16 v[136:139], a[72:75], v[104:107], v[136:139]\n"
      "global_load_dwordx4 a[172:175], v45, s[8:9] offset:-1024 sc1\n"
      "ds_read_b64_tr_b16 v[70:71], v33 offset:51200\n"
      "s_mov_b32 s42, 0x00f0000f\n"
      "v_mfma_f32_16x16x32_bf16 v[152:155], a[76:79], v[104:107], v[152:155]\n"
      "ds_read_b64_tr_b16 v[72:73], v34 offset:49152\n"
      "s_mov_b32 s43, 0xf0000f00\n"
      "v_mfma_f32_16x16x32_bf16 v[168:171], a[80:83], v[104:107], v[168:171]\n"
      "ds_read_b64_tr_b16 v[74:75], v34 offset:51200\n"
      "v_bfrev_b32 v32, 1\n"
      "v_mfma_f32_16x16x32_bf16 v[140:143], a[72:75], v[108:111], v[140:143]\n"
      "ds_read_b64_tr_b16 v[76:77], v35 offset:49152\n"
      "v_cndmask_b32 v50, v32, v50, s[42:43]\n"
      "v_mfma_f32_16x16x32_bf16 v[156:159], a[76:79], v[108:111], v[156:159]\n"
      "global_load_lds_dwordx4 v43, s[4:5] offset:2048 sc1\n"
      "v_mfma_f32_16x16x32_bf16 v[172:175], a[80:83], v[108:111], v[172:175]\n"
      "ds_read_b64_tr_b16 v[78:79], v35 offset:51200\n"
      "s_waitcnt lgkmcnt(6)\n"
      "v_mfma_f32_16x16x32_bf16 v[20:23], v[28:31], v[8:11], 0\n"
      "ds_read_b64_tr_b16 v[80:81], v6 offset:53248\n"
      "v_mfma_f32_16x16x32_bf16 v[24:27], v[8:11], v[8:11], 0\n"
      "ds_read_b64_tr_b16 v[82:83], v6 offset:55296\n"
      "v_mfma_f32_16x16x32_bf16 v[20:23], v[28:31], v[12:15], v[20:23]\n"
      "global_load_dwordx4 a[176:179], v45, s[8:9] offset:0 sc1\n"
      "v_mfma_f32_16x16x32_bf16 v[24:27], v[12:15], v[12:15], v[24:27]\n"
      "ds_read_b64_tr_b16 v[84:85], v33 offset:53248\n"
      "v_mfma_f32_16x16x32_bf16 v[20:23], v[28:31], v[16:19], v[20:23]\n"
      "ds_read_b64_tr_b16 v[86:87], v33 offset:55296\n"
      "v_mfma_f32_16x16x32_bf16 v[24:27], v[16:19], v[16:19], v[24:27]\n"
      "ds_read_b64_tr_b16 v[88:89], v34 offset:53248\n"
      "v_mfma_f32_16x16x32_bf16 v[128:131], a[84:87], v[112:115], v[128:131]\n"
      "global_load_lds_dwordx4 v43, s[4:5] offset:3072 sc1\n"
      "s_mov_b32 s47, 131072\n"
      "v_mfma_f32_16x16x32_bf16 v[144:147], a[88:91], v[112:115], v[144:147]\n"
      "ds_read_b64_tr_b16 v[90:91], v34 offset:55296\n"
      "v_mfma_f32_16x16x32_bf16 v[160:163], a[92:95], v[112:115], v[160:163]\n"
      "ds_read_b64_tr_b16 v[92:93], v35 offset:53248\n"
      "v_cndmask_b32 v24, v24, v25, s[48:49]\n"
      "v_cndmask_b32 v26, v26, v27, s[48:49]\n"
      "v_mfma_f32_16x16x32_bf16 v[132:135], a[84:87], v[116:119], v[132:135]\n"
      "ds_read_b64_tr_b16 v[94:95], v35 offset:55296\n"
      "v_mov_b32 v22, s30\n"
      "v_cndmask_b32 v21, v24, v26, s[50:51]\n"
      "v_mfma_f32_16x16x32_bf16 v[148:151], a[88:91], v[116:119], v[148:151]\n"
      "global_load_dwordx4 a[180:183], v45, s[8:9] offset:1024 sc1\n"
      "v_mov_b32 v23, s31\n"
      "v_mfma_f32_16x16x32_bf16 v[164:167], a[92:95], v[116:119], v[164:167]\n"
      "buffer_store_dwordx4 v[20:23], v50, s[32:35], s92 offen sc1\n"
      "v_and_b32 v32, 0x200, v7\n"
      "v_mfma_f32_16x16x32_bf16 v[136:139], a[84:87], v[120:123], v[136:139]\n"
      "v_and_b32 v50, 0x1f0, v7\n"
      "v_lshl_add_u32 v50, v32, 3, v50\n"
      "v_mfma_f32_16x16x32_bf16 v[152:155], a[88:91], v[120:123], v[152:155]\n"
      "v_and_b32 v32, 0xf8, v4\n"
      "v_add_u32 v32, s95, v32\n"
      "v_mfma_f32_16x16x32_bf16 v[168:171], a[92:95], v[120:123], v[168:171]\n"
      "v_mfma_f32_16x16x32_bf16 v[140:143], a[84:87], v[124:127], v[140:143]\n"
      "v_mfma_f32_16x16x32_bf16 v[156:159], a[88:91], v[124:127], v[156:159]\n"
      "buffer_load_dwordx2 v[2:3], v5, s[32:35], s47 offen sc1\n"
      "v_mfma_f32_16x16x32_bf16 v[172:175], a[92:95], v[124:127], v[172:175]\n"
      "s_waitcnt lgkmcnt(0)\n"
      "v_mfma_f32_16x16x32_bf16 v[128:131], a[96:99], v[64:67], v[128:131]\n"
      "ds_read_b64_tr_b16 v[96:97], v6 offset:57344\n"
      "v_mfma_f32_16x16x32_bf16 v[144:147], a[100:103], v[64:67], v[144:147]\n"
      "ds_read_b64_tr_b16 v[98:99], v6 offset:59392\n"
      "v_mfma_f32_16x16x32_bf16 v[160:163], a[104:107], v[64:67], v[160:163]\n"
      "ds_read_b64_tr_b16 v[100:101], v33 offset:57344\n"
      "s_waitcnt vmcnt(15)\n"
      "v_mfma_f32_16x16x32_bf16 v[132:135], a[96:99], v[68:71], v[132:135]\n"
      "ds_read_b128 v[248:251], v40 offset:0\n"
      "v_mfma_f32_16x16x32_bf16 v[148:151], a[100:103], v[68:71], v[148:151]\n"
      "ds_read_b128 v[252:255], v40 offset:1024\n"
      "s_add_u32 m0, s73, 32768\n"
      "v_add_u32 v43, 98304, v40\n"
      "v_mfma_f32_16x16x32_bf16 v[164:167], a[104:107], v[68:71], v[164:167]\n"
      "ds_read_b128 v[36:39], v40 offset:2048\n"
      "ds_read_b64_tr_b16 v[102:103], v33 offset:59392\n"
      "v_mfma_f32_16x16x32_bf16 v[136:139], a[96:99], v[72:75], v[136:139]\n"
      "global_load_dwordx4 a[184:187], v45, s[8:9] offset:2048 sc1\n"
      "ds_read_b128 v[60:63], v40 offset:3072\n"
      "ds_read_b64_tr_b16 v[104:105], v34 offset:57344\n"
      "v_mfma_f32_16x16x32_bf16 v[152:155], a[100:103], v[72:75], v[152:155]\n"
      "ds_read_b64_tr_b16 v[106:107], v34 offset:59392\n"
      "v_mfma_f32_16x16x32_bf16 v[168:171], a[104:107], v[72:75], v[168:171]\n"
      "ds_read_b64_tr_b16 v[108:109], v35 offset:57344\n"
      "v_mfma_f32_16x16x32_bf16 v[140:143], a[96:99], v[76:79], v[140:143]\n"
      "ds_read_b64_tr_b16 v[110:111], v35 offset:59392\n"
      "v_mfma_f32_16x16x32_bf16 v[156:159], a[100:103], v[76:79], v[156:159]\n"
      "global_load_dwordx4 a[188:191], v45, s[8:9] offset:3072 sc1\n"
      "s_waitcnt lgkmcnt(4)\n"
      "v_add3_u32 v56, v56, v248, v249\n"
      "v_add3_u32 v56, v56, v250, v251\n"
      "v_mfma_f32_16x16x32_bf16 v[172:175], a[104:107], v[76:79], v[172:175]\n"
      "ds_read_b64_tr_b16 v[112:113], v6 offset:61440\n"
      "v_add3_u32 v56, v56, v252, v253\n"
      "v_add3_u32 v56, v56, v254, v255\n"
      "v_mfma_f32_16x16x32_bf16 v[128:131], a[108:111], v[80:83], v[128:131]\n"
      "ds_read_b64_tr_b16 v[114:115], v6 offset:63488\n"
      "v_add_u32 v45, 53248, v7\n"
      "v_add3_u32 v56, v56, v36, v37\n"
      "v_add3_u32 v56, v56, v38, v39\n"
      "v_mfma_f32_16x16x32_bf16 v[144:147], a[112:115], v[80:83], v[144:147]\n"
      "ds_read_b64_tr_b16 v[116:117], v33 offset:61440\n"
      "v_add3_u32 v56, v56, v60, v61\n"
      "v_mfma_f32_16x16x32_bf16 v[160:163], a[116:119], v[80:83], v[160:163]\n"
      "global_load_dwordx4 a[192:195], v45, s[8:9] offset:-4096 sc1\n"
      "ds_read_b64_tr_b16 v[118:119], v33 offset:63488\n"
      "v_add3_u32 v56, v56, v62, v63\n"
      "v_mfma_f32_16x16x32_bf16 v[132:135], a[108:111], v[84:87], v[132:135]\n"
      "ds_read_b64_tr_b16 v[120:121], v34 offset:61440\n"
      "v_mfma_f32_16x16x32_bf16 v[148:151], a[112:115], v[84:87], v[148:151]\n"
      "ds_read_b64_tr_b16 v[122:123], v34 offset:63488\n"
      "v_add_u32_dpp v56, v56, v56 quad_perm:[1,0,3,2] row_mask:0xf bank_mask:0xf\n"
      "v_mfma_f32_16x16x32_bf16 v[164:167], a[116:119], v[84:87], v[164:167]\n"
      "ds_read_b64_tr_b16 v[124:125], v35 offset:61440\n"
      "s_waitcnt vmcnt(12)\n"
      "v_mfma_f32_16x16x32_bf16 v[136:139], a[108:111], v[88:91], v[136:139]\n"
      "global_load_dwordx4 a[196:199], v45, s[8:9] offset:-3072 sc1\n"
      "v_add_u32_dpp v56, v56, v56 quad_perm:[2,3,0,1] row_mask:0xf bank_mask:0xf\n"
      "v_mfma_f32_16x16x32_bf16 v[152:155], a[112:115], v[88:91], v[152:155]\n"
      "ds_read_b64_tr_b16 v[126:127], v35 offset:63488\n"
      "v_add_f32 v224, v224, v228\n"
      "v_mfma_f32_16x16x32_bf16 v[168:171], a[116:119], v[88:91], v[168:171]\n"
      "v_add_u32_dpp v56, v56, v56 row_half_mirror row_mask:0xf bank_mask:0xf\n"
      "v_cmp_ne_u32 vcc, s71, v56\n"
      "v_add_f32 v225, v225, v229\n"
      "v_mfma_f32_16x16x32_bf16 v[140:143], a[108:111], v[92:95], v[140:143]\n"
      "v_add_f32 v226, v226, v230\n"
      "v_add_f32 v227, v227, v231\n"
      "v_mfma_f32_16x16x32_bf16 v[156:159], a[112:115], v[92:95], v[156:159]\n"
      "global_load_dwordx4 a[200:203], v45, s[8:9] offset:-2048 sc1\n"
      "s_cbranch_vccnz L_sx4_%=\n"
      "L_bx4_%=:\n"
      "v_add_f32 v232, v232, v236\n"
      "v_mfma_f32_16x16x32_bf16 v[172:175], a[116:119], v[92:95], v[172:175]\n"
      "s_waitcnt lgkmcnt(0)\n"
      "s_barrier\n"
      "v_mfma_f32_16x16x32_bf16 v[128:131], a[120:123], v[96:99], v[128:131]\n"
      "ds_read_b64_tr_b16 v[64:65], v6 offset:0\n"
      "v_add_f32 v233, v233, v237\n"
      "v_mfma_f32_16x16x32_bf16 v[144:147], a[124:127], v[96:99], v[144:147]\n"
      "ds_read_b64_tr_b16 v[66:67], v6 offset:2048\n"
      "v_add_f32 v234, v234, v238\n"
      "v_mfma_f32_16x16x32_bf16 v[160:163], a[128:131], v[96:99], v[160:163]\n"
      "global_load_dwordx4 a[204:207], v45, s[8:9] offset:-1024 sc1\n"
      "v_add_f32 v235, v235, v239\n"
      "v_mfma_f32_16x16x32_bf16 v[132:135], a[120:123], v[100:103], v[132:135]\n"
      "ds_read_b64_tr_b16 v[68:69], v33 offset:0\n"
      "v_add_f32 v240, v240, v244\n"
      "v_mfma_f32_16x16x32_bf16 v[148:151], a[124:127], v[100:103], v[148:151]\n"
      "ds_read_b64_tr_b16 v[70:71], v33 offset:2048\n"
      "v_add_f32 v241, v241, v245\n"
      "v_mfma_f32_16x16x32_bf16 v[164:167], a[128:131], v[100:103], v[164:167]\n"
      "ds_read_b64_tr_b16 v[72:73], v34 offset:0\n"
      "v_add_f32 v242, v242, v246\n"
      "v_mfma_f32_16x16x32_bf16 v[136:139], a[120:123], v[104:107], v[136:139]\n"
      "global_load_dwordx4 a[208:211], v45, s[8:9] offset:0 sc1\n"
      "v_add_f32 v243, v243, v247\n"
      "v_mfma_f32_16x16x32_bf16 v[152:155], a[124:127], v[104:107], v[152:155]\n"
      "ds_read_b64_tr_b16 v[74:75], v34 offset:2048\n"
      "v_add_f32 v224, v224, v232\n"
      "v_mfma_f32_16x16x32_bf16 v[168:171], a[128:131], v[104:107], v[168:171]\n"
      "ds_read_b64_tr_b16 v[76:77], v35 offset:0\n"
      "v_add_f32 v225, v225, v233\n"
      "v_mfma_f32_16x16x32_bf16 v[140:143], a[120:123], v[108:111], v[140:143]\n"
      "ds_read_b64_tr_b16 v[78:79], v35 offset:2048\n"
      "v_add_f32 v226, v226, v234\n"
      "v_mfma_f32_16x16x32_bf16 v[156:159], a[124:127], v[108:111], v[156:159]\n"
      "global_load_dwordx4 a[212:215], v45, s[8:9] offset:1024 sc1\n"
      "v_add_f32 v227, v227, v235\n"
      "v_mfma_f32_16x16x32_bf16 v[172:175], a[128:131], v[108:111], v[172:175]\n"
      "ds_read_b64_tr_b16 v[80:81], v6 offset:4096\n"
      "v_add_f32 v224, v224, v240\n"
      "v_mfma_f32_16x16x32_bf16 v[128:131], a[132:135], v[112:115], v[128:131]\n"
      "ds_read_b64_tr_b16 v[82:83], v6 offset:6144\n"
      "v_add_f32 v225, v225, v241\n"
      "v_mfma_f32_16x16x32_bf16 v[144:147], a[136:139], v[112:115], v[144:147]\n"
      "ds_read_b64_tr_b16 v[84:85], v33 offset:4096\n"
      "v_add_f32 v226, v226, v242\n"
      "v_mfma_f32_16x16x32_bf16 v[160:163], a[140:143], v[112:115], v[160:163]\n"
      "global_load_lds_dwordx4 v43, s[4:5] offset:0 sc1\n"
      "ds_read_b64_tr_b16 v[86:87], v33 offset:6144\n"
      "v_add_f32 v227, v227, v243\n"
      "v_mfma_f32_16x16x32_bf16 v[132:135], a[132:135], v[116:119], v[132:135]\n"
      "ds_read_b64_tr_b16 v[88:89], v34 offset:4096\n"
      "v_mfma_f32_16x16x32_bf16 v[148:151], a[136:139], v[116:119], v[148:151]\n"
      "global_load_dwordx4 a[216:219], v45, s[8:9] offset:2048 sc1\n"
      "ds_read_b64_tr_b16 v[90:91], v34 offset:6144\n"
      "v_add_u32 v53, 0x6000, v53\n"
      "v_mfma_f32_16x16x32_bf16 v[164:167], a[140:143], v[116:119], v[164:167]\n"
      "global_load_dwordx4 v[228:231], v53, s[26:27] sc1\n"
      "ds_read_b64_tr_b16 v[92:93], v35 offset:4096\n"
      "v_add_u32 v53, 0x6000, v53\n"
      "v_mfma_f32_16x16x32_bf16 v[136:139], a[132:135], v[120:123], v[136:139]\n"
      "global_load_dwordx4 v[232:235], v53, s[26:27] sc1\n"
      "v_add_u32 v53, 0x6000, v53\n"
      "v_mfma_f32_16x16x32_bf16 v[152:155], a[136:139], v[120:123], v[152:155]\n"
      "global_load_lds_dwordx4 v43, s[4:5] offset:1024 sc1\n"
      "global_load_dwordx4 v[236:239], v53, s[26:27] sc1\n"
      "ds_read_b64_tr_b16 v[94:95], v35 offset:6144\n"
      "v_add_u32 v53, 0x6000, v53\n"
      "v_mfma_f32_16x16x32_bf16 v[168:171], a[140:143], v[120:123], v[168:171]\n"
      "global_load_dwordx4 a[220:223], v45, s[8:9] offset:3072 sc1\n"
      "global_load_dwordx4 v[240:243], v53, s[26:27] sc1\n"
      "v_and_b32 v53, 0x300, v7\n"
      "v_mfma_f32_16x16x32_bf16 v[140:143], a[132:135], v[124:127], v[140:143]\n"
      "v_and_b32 v59, 0x30, v7\n"
      "v_lshl_add_u32 v53, v59, 2, v53\n"
      "v_mfma_f32_16x16x32_bf16 v[156:159], a[136:139], v[124:127], v[156:159]\n"
      "v_bfe_u32 v59, v7, 6, 2\n"
      "v_lshl_add_u32 v53, v59, 4, v53\n"
      "v_lshrrev_b32 v59, 6, v7\n"
      "v_mfma_f32_16x16x32_bf16 v[172:175], a[140:143], v[124:127], v[172:175]\n"
      "global_load_lds_dwordx4 v43, s[4:5] offset:2048 sc1\n"
      "v_add_u32 v45, 61440, v7\n"
      "s_waitcnt lgkmcnt(0)\n"
      "v_mfma_f32_16x16x32_bf16 v[128:131], a[144:147], v[64:67], v[128:131]\n"
      "global_load_dwordx4 a[224:227], v45, s[8:9] offset:-4096 sc1\n"
      "buffer_load_dwordx4 v[244:247], v53, s[32:35], s66 offen sc1\n"
      "v_mfma_f32_16x16x32_bf16 v[144:147], a[148:151], v[64:67], v[144:147]\n"
      "ds_read_b64_tr_b16 v[96:97], v6 offset:8192\n"
      "v_mfma_f32_16x16x32_bf16 v[160:163], a[152:155], v[64:67], v[160:163]\n"
      "ds_read_b64_tr_b16 v[98:99], v6 offset:10240\n"
      "s_waitcnt vmcnt(19)\n"
      "v_mfma_f32_16x16x32_bf16 v[132:135], a[144:147], v[68:71], v[132:135]\n"
      "global_load_lds_dwordx4 v43, s[4:5] offset:3072 sc1\n"
      "ds_read_b128 v[248:251], v40 offset:16384\n"
      "v_mfma_f32_16x16x32_bf16 v[148:151], a[148:151], v[68:71], v[148:151]\n"
      "global_load_dwordx4 a[228:231], v45, s[8:9] offset:-3072 sc1\n"
      "ds_read_b128 v[252:255], v40 offset:17408\n"
      "ds_read_b64_tr_b16 v[100:101], v33 offset:8192\n"
      "v_mfma_f32_16x16x32_bf16 v[164:167], a[152:155], v[68:71], v[164:167]\n"
      "ds_read_b128 v[36:39], v40 offset:18432\n"
      "ds_read_b64_tr_b16 v[102:103], v33 offset:10240\n"
      "v_mfma_f32_16x16x32_bf16 v[136:139], a[144:147], v[72:75], v[136:139]\n"
      "ds_read_b128 v[60:63], v40 offset:19456\n"
      "ds_read_b64_tr_b16 v[104:105], v34 offset:8192\n"
      "v_mfma_f32_16x16x32_bf16 v[152:155], a[148:151], v[72:75], v[152:155]\n"
      "ds_read_b64_tr_b16 v[106:107], v34 offset:10240\n"
      "v_mfma_f32_16x16x32_bf16 v[168:171], a[152:155], v[72:75], v[168:171]\n"
      "buffer_load_dwordx4 v[8:11], v50, s[32:35], s93 offen offset:0 sc1\n"
      "global_load_dwordx4 a[232:235], v45, s[8:9] offset:-2048 sc1\n"
      "ds_read_b64_tr_b16 v[108:109], v35 offset:8192\n"
      "v_mfma_f32_16x16x32_bf16 v[140:143], a[144:147], v[76:79], v[140:143]\n"
      "ds_read_b64_tr_b16 v[110:111], v35 offset:10240\n"
      "v_mfma_f32_16x16x32_bf16 v[156:159], a[148:151], v[76:79], v[156:159]\n"
      "ds_read_b64_tr_b16 v[112:113], v6 offset:12288\n"
      "s_waitcnt lgkmcnt(5)\n"
      "v_add3_u32 v2, v2, v248, v249\n"
      "v_add3_u32 v2, v2, v250, v251\n"
      "v_mfma_f32_16x16x32_bf16 v[172:175], a[152:155], v[76:79], v[172:175]\n"
      "ds_read_b64_tr_b16 v[114:115], v6 offset:14336\n"
      "v_add3_u32 v2, v2, v252, v253\n"
      "v_add3_u32 v2, v2, v254, v255\n"
      "v_mfma_f32_16x16x32_bf16 v[128:131], a[156:159], v[80:83], v[128:131]\n"
      "buffer_load_dwordx4 v[12:15], v50, s[32:35], s93 offen offset:1024 sc1\n"
      "global_load_dwordx4 a[236:239], v45, s[8:9] offset:-1024 sc1\n"
      "v_add3_u32 v2, v2, v36, v37\n"
      "v_add3_u32 v2, v2, v38, v39\n"
      "v_mfma_f32_16x16x32_bf16 v[144:147], a[160:163], v[80:83], v[144:147]\n"
      "ds_read_b64_tr_b16 v[116:117], v33 offset:12288\n"
      "v_add3_u32 v2, v2, v60, v61\n"
      "v_mfma_f32_16x16x32_bf16 v[160:163], a[164:167], v[80:83], v[160:163]\n"
      "ds_read_b64_tr_b16 v[118:119], v33 offset:14336\n"
      "v_add3_u32 v2, v2, v62, v63\n"
      "v_mfma_f32_16x16x32_bf16 v[132:135], a[156:159], v[84:87], v[132:135]\n"
      "ds_read_b64_tr_b16 v[120:121], v34 offset:12288\n"
      "v_mfma_f32_16x16x32_bf16 v[148:151], a[160:163], v[84:87], v[148:151]\n"
      "buffer_load_dwordx4 v[16:19], v50, s[32:35], s93 offen offset:2048 sc1\n"
      "ds_read_b64_tr_b16 v[122:123], v34 offset:14336\n"
      "v_add_u32_dpp v2, v2, v2 quad_perm:[1,0,3,2] row_mask:0xf bank_mask:0xf\n"
      "v_mfma_f32_16x16x32_bf16 v[164:167], a[164:167], v[84:87], v[164:167]\n"
      "ds_read_b64_tr_b16 v[124:125], v35 offset:12288\n"
      "v_mfma_f32_16x16x32_bf16 v[136:139], a[156:159], v[88:91], v[136:139]\n"
      "ds_read_b64_tr_b16 v[126:127], v35 offset:14336\n"
      "v_add_u32_dpp v2, v2, v2 quad_perm:[2,3,0,1] row_mask:0xf bank_mask:0xf\n"
      "v_mfma_f32_16x16x32_bf16 v[152:155], a[160:163], v[88:91], v[152:155]\n"
      "v_mfma_f32_16x16x32_bf16 v[168:171], a[164:167], v[88:91], v[168:171]\n"
      "buffer_load_dwordx4 v[20:23], v50, s[32:35], s93 offen offset:3072 sc1\n"
      "v_add_u32_dpp v2, v2, v2 row_half_mirror row_mask:0xf bank_mask:0xf\n"
      "v_cmp_ne_u32 vcc, s71, v2\n"
      "v_mfma_f32_16x16x32_bf16 v[140:143], a[156:159], v[92:95], v[140:143]\n"
      "v_mfma_f32_16x16x32_bf16 v[156:159], a[160:163], v[92:95], v[156:159]\n"
      "s_cbranch_vccnz L_sx5_%=\n"
      "L_bx5_%=:\n"
      "v_mfma_f32_16x16x32_bf16 v[172:175], a[164:167], v[92:95], v[172:175]\n"
      "s_waitcnt vmcnt(25)\n"
      "s_waitcnt lgkmcnt(0)\n"
      "s_barrier\n"
      "v_mfma_f32_16x16x32_bf16 v[128:131], a[168:171], v[96:99], v[128:131]\n"
      "ds_read_b64_tr_b16 v[64:65], v6 offset:16384\n"
      "v_mfma_f32_16x16x32_bf16 v[144:147], a[172:175], v[96:99], v[144:147]\n"
      "ds_read_b64_tr_b16 v[66:67], v6 offset:18432\n"
      "v_mfma_f32_16x16x32_bf16 v[160:163], a[176:179], v[96:99], v[160:163]\n"
      "ds_read_b64_tr_b16 v[68:69], v33 offset:16384\n"
      "s_add_u32 m0, s73, 49152\n"
      "v_add_u32 v43, 114688, v40\n"
      "v_mfma_f32_16x16x32_bf16 v[132:135], a[168:171], v[100:103], v[132:135]\n"
      "ds_read_b64_tr_b16 v[70:71], v33 offset:18432\n"
      "v_mfma_f32_16x16x32_bf16 v[148:151], a[172:175], v[100:103], v[148:151]\n"
      "global_load_lds_dwordx4 v43, s[4:5] offset:0 sc1\n"
      "v_mfma_f32_16x16x32_bf16 v[164:167], a[176:179], v[100:103], v[164:167]\n"
      "ds_read_b64_tr_b16 v[72:73], v34 offset:16384\n"
      "v_mfma_f32_16x16x32_bf16 v[136:139], a[168:171], v[104:107], v[136:139]\n"
      "ds_read_b64_tr_b16 v[74:75], v34 offset:18432\n"
      "v_mfma_f32_16x16x32_bf16 v[152:155], a[172:175], v[104:107], v[152:155]\n"
      "ds_read_b64_tr_b16 v[76:77], v35 offset:16384\n"
      "v_mfma_f32_16x16x32_bf16 v[168:171], a[176:179], v[104:107], v[168:171]\n"
      "global_load_lds_dwordx4 v43, s[4:5] offset:1024 sc1\n"
      "v_mfma_f32_16x16x32_bf16 v[140:143], a[168:171], v[108:111], v[140:143]\n"
      "ds_read_b64_tr_b16 v[78:79], v35 offset:18432\n"
      "v_mfma_f32_16x16x32_bf16 v[156:159], a[172:175], v[108:111], v[156:159]\n"
      "ds_read_b64_tr_b16 v[80:81], v6 offset:20480\n"
      "v_mfma_f32_16x16x32_bf16 v[172:175], a[176:179], v[108:111], v[172:175]\n"
      "ds_read_b64_tr_b16 v[82:83], v6 offset:22528\n"
      "v_mfma_f32_16x16x32_bf16 v[128:131], a[180:183], v[112:115], v[128:131]\n"
      "global_load_lds_dwordx4 v43, s[4:5] offset:2048 sc1\n"
      "s_waitcnt vmcnt(11)\n"
      "v_mfma_f32_16x16x32_bf16 v[144:147], a[184:187], v[112:115], v[144:147]\n"
      "ds_read_b64_tr_b16 v[84:85], v33 offset:20480\n"
      "v_add_f32 v228, v228, v232\n"
      "v_mfma_f32_16x16x32_bf16 v[160:163], a[188:191], v[112:115], v[160:163]\n"
      "ds_read_b64_tr_b16 v[86:87], v33 offset:22528\n"
      "v_add_f32 v229, v229, v233\n"
      "v_mfma_f32_16x16x32_bf16 v[132:135], a[180:183], v[116:119], v[132:135]\n"
      "ds_read_b64_tr_b16 v[88:89], v34 offset:20480\n"
      "v_add_f32 v230, v230, v234\n"
      "v_mfma_f32_16x16x32_bf16 v[148:151], a[184:187], v[116:119], v[148:151]\n"
      "global_load_lds_dwordx4 v43, s[4:5] offset:3072 sc1\n"
      "ds_read_b64_tr_b16 v[90:91], v34 offset:22528\n"
      "v_add_f32 v231, v231, v235\n"
      "v_mfma_f32_16x16x32_bf16 v[164:167], a[188:191], v[116:119], v[164:167]\n"
      "ds_read_b64_tr_b16 v[92:93], v35 offset:20480\n"
      "v_add_f32 v236, v236, v240\n"
      "v_mfma_f32_16x16x32_bf16 v[136:139], a[180:183], v[120:123], v[136:139]\n"
      "ds_read_b64_tr_b16 v[94:95], v35 offset:22528\n"
      "s_add_u32 m0, s73, 0\n"
      "v_add_u32 v43, 131072, v40\n"
      "v_mfma_f32_16x16x32_bf16 v[152:155], a[184:187], v[120:123], v[152:155]\n"
      "v_add_f32 v237, v237, v241\n"
      "v_add_f32 v238, v238, v242\n"
      "v_mfma_f32_16x16x32_bf16 v[168:171], a[188:191], v[120:123], v[168:171]\n"
      "global_load_dwordx4 a[240:243], v44, s[6:7] offset:-4096 sc1\n"
      "v_add_f32 v239, v239, v243\n"
      "v_mfma_f32_16x16x32_bf16 v[140:143], a[180:183], v[124:127], v[140:143]\n"
      "v_add_f32 v224, v224, v228\n"
      "v_add_f32 v225, v225, v229\n"
      "v_mfma_f32_16x16x32_bf16 v[156:159], a[184:187], v[124:127], v[156:159]\n"
      "v_add_f32 v226, v226, v230\n"
      "v_add_f32 v227, v227, v231\n"
      "v_mfma_f32_16x16x32_bf16 v[172:175], a[188:191], v[124:127], v[172:175]\n"
      "s_waitcnt lgkmcnt(0)\n"
      "v_mfma_f32_16x16x32_bf16 v[128:131], a[192:195], v[64:67], v[128:131]\n"
      "global_load_dwordx4 a[244:247], v44, s[6:7] offset:-3072 sc1\n"
      "v_add_f32 v224, v224, v236\n"
      "v_mfma_f32_16x16x32_bf16 v[144:147], a[196:199], v[64:67], v[144:147]\n"
      "ds_read_b64_tr_b16 v[96:97], v6 offset:24576\n"
      "v_add_f32 v225, v225, v237\n"
      "v_mfma_f32_16x16x32_bf16 v[160:163], a[200:203], v[64:67], v[160:163]\n"
      "ds_read_b64_tr_b16 v[98:99], v6 offset:26624\n"
      "s_waitcnt vmcnt(13)\n"
      "v_mfma_f32_16x16x32_bf16 v[132:135], a[192:195], v[68:71], v[132:135]\n"
      "ds_read_b128 v[248:251], v40 offset:32768\n"
      "v_add_f32 v226, v226, v238\n"
      "v_mfma_f32_16x16x32_bf16 v[148:151], a[196:199], v[68:71], v[148:151]\n"
      "global_load_dwordx4 a[248:251], v44, s[6:7] offset:-2048 sc1\n"
      "ds_read_b128 v[252:255], v40 offset:33792\n"
      "ds_read_b64_tr_b16 v[100:101], v33 offset:24576\n"
      "v_add_f32 v227, v227, v239\n"
      "v_mfma_f32_16x16x32_bf16 v[164:167], a[200:203], v[68:71], v[164:167]\n"
      "ds_read_b128 v[36:39], v40 offset:34816\n"
      "ds_read_b64_tr_b16 v[102:103], v33 offset:26624\n"
      "v_mov_b32 v228, v224\n"
      "v_mfma_f32_16x16x32_bf16 v[136:139], a[192:195], v[72:75], v[136:139]\n"
      "ds_read_b128 v[60:63], v40 offset:35840\n"
      "ds_read_b64_tr_b16 v[104:105], v34 offset:24576\n"
      "v_mov_b32 v229, v225\n"
      "v_mfma_f32_16x16x32_bf16 v[152:155], a[196:199], v[72:75], v[152:155]\n"
      "ds_read_b64_tr_b16 v[106:107], v34 offset:26624\n"
      "v_mov_b32 v230, v226\n"
      "v_mfma_f32_16x16x32_bf16 v[168:171], a[200:203], v[72:75], v[168:171]\n"
      "global_load_lds_dwordx4 v43, s[4:5] offset:0 sc1\n"
      "ds_read_b64_tr_b16 v[108:109], v35 offset:24576\n"
      "v_mov_b32 v231, v227\n"
      "v_mfma_f32_16x16x32_bf16 v[140:143], a[192:195], v[76:79], v[140:143]\n"
      "ds_read_b64_tr_b16 v[110:111], v35 offset:26624\n"
      "v_lshl_add_u32 v59, v59, 3, s55\n"
      "v_mfma_f32_16x16x32_bf16 v[156:159], a[196:199], v[76:79], v[156:159]\n"
      "ds_read_b64_tr_b16 v[112:113], v6 offset:28672\n"
      "s_waitcnt lgkmcnt(5)\n"
      "v_add3_u32 v49, v49, v248, v249\n"
      "v_add3_u32 v49, v49, v250, v251\n"
      "v_mfma_f32_16x16x32_bf16 v[172:175], a[200:203], v[76:79], v[172:175]\n"
      "ds_read_b64_tr_b16 v[114:115], v6 offset:30720\n"
      "v_add3_u32 v49, v49, v252, v253\n"
      "v_add3_u32 v49, v49, v254, v255\n"
      "v_mfma_f32_16x16x32_bf16 v[128:131], a[204:207], v[80:83], v[128:131]\n"
      "global_load_dwordx4 a[252:255], v44, s[6:7] offset:-1024 sc1\n"
      "v_add3_u32 v49, v49, v36, v37\n"
      "v_add3_u32 v49, v49, v38, v39\n"
      "v_mfma_f32_16x16x32_bf16 v[144:147], a[208:211], v[80:83], v[144:147]\n"
      "ds_read_b64_tr_b16 v[116:117], v33 offset:28672\n"
      "v_add3_u32 v49, v49, v60, v61\n"
      "v_mfma_f32_16x16x32_bf16 v[160:163], a[212:215], v[80:83], v[160:163]\n"
      "ds_read_b64_tr_b16 v[118:119], v33 offset:30720\n"
      "v_add3_u32 v49, v49, v62, v63\n"
      "v_permlane32_swap_b32 v224, v228\n"
      "v_mfma_f32_16x16x32_bf16 v[132:135], a[204:207], v[84:87], v[132:135]\n"
      "ds_read_b64_tr_b16 v[120:121], v34 offset:28672\n"
      "v_permlane32_swap_b32 v225, v229\n"
      "v_mfma_f32_16x16x32_bf16 v[148:151], a[208:211], v[84:87], v[148:151]\n"
      "global_load_lds_dwordx4 v43, s[4:5] offset:1024 sc1\n"
      "ds_read_b64_tr_b16 v[122:123], v34 offset:30720\n"
      "v_add_u32_dpp v49, v49, v49 quad_perm:[1,0,3,2] row_mask:0xf bank_mask:0xf\n"
      "v_permlane32_swap_b32 v226, v230\n"
      "v_mfma_f32_16x16x32_bf16 v[164:167], a[212:215], v[84:87], v[164:167]\n"
      "ds_read_b64_tr_b16 v[124:125], v35 offset:28672\n"
      "v_permlane32_swap_b32 v227, v231\n"
      "v_mfma_f32_16x16x32_bf16 v[136:139], a[204:207], v[88:91], v[136:139]\n"
      "ds_read_b64_tr_b16 v[126:127], v35 offset:30720\n"
      "v_add_u32_dpp v49, v49, v49 quad_perm:[2,3,0,1] row_mask:0xf bank_mask:0xf\n"
      "v_add_f32 v224, v224, v228\n"
      "v_mfma_f32_16x16x32_bf16 v[152:155], a[208:211], v[88:91], v[152:155]\n"
      "v_add_f32 v225, v225, v229\n"
      "v_add_f32 v226, v226, v230\n"
      "v_mfma_f32_16x16x32_bf16 v[168:171], a[212:215], v[88:91], v[168:171]\n"
      "global_load_dwordx4 a[0:3], v44, s[6:7] offset:0 sc1\n"
      "v_add_u32_dpp v49, v49, v49 row_half_mirror row_mask:0xf bank_mask:0xf\n"
      "v_cmp_ne_u32 vcc, s71, v49\n"
      "v_mfma_f32_16x16x32_bf16 v[140:143], a[204:207], v[92:95], v[140:143]\n"
      "v_add_f32 v227, v227, v231\n"
      "v_mfma_f32_16x16x32_bf16 v[156:159], a[208:211], v[92:95], v[156:159]\n"
      "ds_write_b128 v51, v[224:227]\n"
      "s_cbranch_vccnz L_sx6_%=\n"
      "L_bx6_%=:\n"
      "v_mfma_f32_16x16x32_bf16 v[172:175], a[212:215], v[92:95], v[172:175]\n"
      "s_waitcnt vmcnt(13)\n"
      "s_waitcnt lgkmcnt(1)\n"
      "s_barrier\n"
      "v_mfma_f32_16x16x32_bf16 v[128:131], a[216:219], v[96:99], v[128:131]\n"
      "global_load_lds_dwordx4 v43, s[4:5] offset:2048 sc1\n"
      "v_mfma_f32_16x16x32_bf16 v[144:147], a[220:223], v[96:99], v[144:147]\n"
      "ds_read_b64_tr_b16 v[64:65], v6 offset:32768\n"
      "v_mfma_f32_16x16x32_bf16 v[160:163], a[224:227], v[96:99], v[160:163]\n"
      "ds_read_b64_tr_b16 v[66:67], v6 offset:34816\n"
      "s_waitcnt vmcnt(12)\n"
      "v_add3_u32 v10, v10, v14, v18\n"
      "v_mfma_f32_16x16x32_bf16 v[132:135], a[216:219], v[100:103], v[132:135]\n"
      "ds_read_b64_tr_b16 v[68:69], v34 offset:32768\n"
      "v_add_u32 v10, v10, v22\n"
      "v_cmp_ne_u32 vcc, s71, v10\n"
      "v_mfma_f32_16x16x32_bf16 v[148:151], a[220:223], v[100:103], v[148:151]\n"
      "global_load_dwordx4 a[4:7], v44, s[6:7] offset:1024 sc1\n"
      "v_mfma_f32_16x16x32_bf16 v[164:167], a[224:227], v[100:103], v[164:167]\n"
      "global_load_lds_dwordx4 v43, s[4:5] offset:3072 sc1\n"
      "ds_read_b64_tr_b16 v[70:71], v34 offset:34816\n"
      "s_cbranch_vccnz L_sg_%=\n"
      "L_bg_%=:\n"
      "v_add_f32 v8, v8, v12\n"
      "v_add_f32 v9, v9, v13\n"
      "v_mfma_f32_16x16x32_bf16 v[136:139], a[216:219], v[104:107], v[136:139]\n"
      "ds_read_b64_tr_b16 v[72:73], v33 offset:32768\n"
      "v_add_f32 v16, v16, v20\n"
      "v_add_f32 v17, v17, v21\n"
      "v_mfma_f32_16x16x32_bf16 v[152:155], a[220:223], v[104:107], v[152:155]\n"
      "ds_read_b64_tr_b16 v[74:75], v33 offset:34816\n"
      "v_add_f32 v8, v8, v16\n"
      "v_add_u32 v245, v245, v247\n"
      "v_mfma_f32_16x16x32_bf16 v[168:171], a[224:227], v[104:107], v[168:171]\n"
      "global_load_dwordx4 a[8:11], v44, s[6:7] offset:2048 sc1\n"
      "ds_read_b64_tr_b16 v[76:77], v35 offset:32768\n"
      "v_add_f32 v9, v9, v17\n"
      "v_mov_b32 v10, v8\n"
      "v_cmp_ne_u32 vcc, s54, v245\n"
      "v_mfma_f32_16x16x32_bf16 v[140:143], a[216:219], v[108:111], v[140:143]\n"
      "ds_read_b64_tr_b16 v[78:79], v35 offset:34816\n"
      "v_mov_b32 v11, v9\n"
      "s_nop 0\n"
      "v_mfma_f32_16x16x32_bf16 v[156:159], a[220:223], v[108:111], v[156:159]\n"
      "ds_read_b64_tr_b16 v[80:81], v6 offset:36864\n"
      "v_permlane32_swap_b32 v8, v10\n"
      "v_permlane32_swap_b32 v9, v11\n"
      "s_cbranch_vccnz L_sgb_%=\n"
      "L_bgb_%=:\n"
      "v_add_f32 v8, v8, v10\n"
      "v_mfma_f32_16x16x32_bf16 v[172:175], a[224:227], v[108:111], v[172:175]\n"
      "ds_read_b64_tr_b16 v[82:83], v6 offset:38912\n"
      "v_add_f32_dpp v244, v244, v244 quad_perm:[1,0,3,2] row_mask:0xf bank_mask:0xf\n"
      "v_add_f32_dpp v246, v246, v246 quad_perm:[1,0,3,2] row_mask:0xf bank_mask:0xf\n"
      "v_mfma_f32_16x16x32_bf16 v[128:131], a[228:231], v[112:115], v[128:131]\n"
      "global_load_dwordx4 a[12:15], v44, s[6:7] offset:3072 sc1\n"
      "v_add_f32 v9, v9, v11\n"
      "v_mul_f32 v8, 0x3acccccd, v8\n"
      "v_add_f32_dpp v244, v244, v244 quad_perm:[2,3,0,1] row_mask:0xf bank_mask:0xf\n"
      "v_add_f32_dpp v246, v246, v246 quad_perm:[2,3,0,1] row_mask:0xf bank_mask:0xf\n"
      "v_mfma_f32_16x16x32_bf16 v[144:147], a[232:235], v[112:115], v[144:147]\n"
      "ds_read_b64_tr_b16 v[84:85], v34 offset:36864\n"
      "v_mul_f32 v9, 0x3acccccd, v9\n"
      "v_fma_f32 v9, -v8, v8, v9\n"
      "s_add_u32 m0, s40, 0\n"
      "v_add_u32 v43, 40960, v7\n"
      "v_mfma_f32_16x16x32_bf16 v[160:163], a[236:239], v[112:115], v[160:163]\n"
      "global_load_lds_dwordx4 v43, s[6:7] offset:0 sc1\n"
      "global_load_lds_dwordx4 v43, s[6:7] offset:1024 sc1\n"
      "global_load_lds_dwordx4 v43, s[6:7] offset:2048 sc1\n"
      "global_load_lds_dwordx4 v43, s[6:7] offset:3072 sc1\n"
      "ds_read_b64_tr_b16 v[86:87], v34 offset:38912\n"
      "v_max_f32 v9, 0, v9\n"
      "s_add_u32 m0, s73, 16384\n"
      "v_add_u32 v43, 147456, v40\n"
      "v_cvt_pk_bf16_f32 v244, 0, v244\n"
      "v_cvt_pk_bf16_f32 v245, 0, v246\n"
      "v_mfma_f32_16x16x32_bf16 v[132:135], a[228:231], v[116:119], v[132:135]\n"
      "global_load_lds_dwordx4 v43, s[4:5] offset:0 sc1\n"
      "ds_read_b64_tr_b16 v[88:89], v33 offset:36864\n"
      "ds_write_b64 v59, v[244:245]\n"
      "v_cvt_pk_bf16_f32 v9, 0, v9\n"
      "v_add_f32 v9, 0x3727c5ac, v9\n"
      "v_add_u32 v44, 12288, v7\n"
      "v_mfma_f32_16x16x32_bf16 v[148:151], a[232:235], v[116:119], v[148:151]\n"
      "global_load_dwordx4 a[16:19], v44, s[6:7] offset:-4096 sc1\n"
      "ds_read_b64_tr_b16 v[90:91], v33 offset:38912\n"
      "ds_read_b128 v[224:227], v52 offset:0\n"
      "v_cvt_pk_bf16_f32 v9, 0, v9\n"
      "v_mfma_f32_16x16x32_bf16 v[164:167], a[236:239], v[116:119], v[164:167]\n"
      "ds_read_b64_tr_b16 v[92:93], v35 offset:36864\n"
      "ds_read_b128 v[228:231], v52 offset:128\n"
      "v_sqrt_f32 v9, v9\n"
      "v_sub_f32 v8, 0, v8\n"
      "v_cvt_pk_bf16_f32 v9, 0, v9\n"
      "v_mfma_f32_16x16x32_bf16 v[136:139], a[228:231], v[120:123], v[136:139]\n"
      "ds_read_b128 v[232:235], v52 offset:256\n"
      "v_rcp_f32 v9, v9\n"
      "v_cvt_pk_bf16_f32 v8, 0, v8\n"
      "v_mfma_f32_16x16x32_bf16 v[152:155], a[232:235], v[120:123], v[152:155]\n"
      "global_load_lds_dwordx4 v43, s[4:5] offset:1024 sc1\n"
      "ds_write_b64 v32, v[8:9]\n"
      "ds_read_b64_tr_b16 v[94:95], v35 offset:38912\n"
      "v_mfma_f32_16x16x32_bf16 v[168:171], a[236:239], v[120:123], v[168:171]\n"
      "global_load_dwordx4 a[20:23], v44, s[6:7] offset:-3072 sc1\n"
      "ds_read_b128 v[236:239], v52 offset:64\n"
      "v_mfma_f32_16x16x32_bf16 v[140:143], a[228:231], v[124:127], v[140:143]\n"
      "ds_read_b128 v[240:243], v52 offset:192\n"
      "v_mfma_f32_16x16x32_bf16 v[156:159], a[232:235], v[124:127], v[156:159]\n"
      "ds_read_b128 v[244:247], v52 offset:320\n"
      "v_mfma_f32_16x16x32_bf16 v[172:175], a[236:239], v[124:127], v[172:175]\n"
      "global_load_lds_dwordx4 v43, s[4:5] offset:2048 sc1\n"
      "s_waitcnt lgkmcnt(3)\n"
      "s_barrier\n"
      "v_mfma_f32_16x16x32_bf16 v[176:179], a[48:51], v[64:67], v[176:179]\n"
      "global_load_dwordx4 a[24:27], v44, s[6:7] offset:-2048 sc1\n"
      "ds_read_b64 v[16:17], v41 offset:0\n"
      "v_mfma_f32_16x16x32_bf16 v[184:187], a[52:55], v[64:67], v[184:187]\n"
      "ds_read_b64 v[18:19], v41 offset:128\n"
      "v_mfma_f32_16x16x32_bf16 v[192:195], a[56:59], v[64:67], v[192:195]\n"
      "ds_read_b64 v[20:21], v41 offset:256\n"
      "ds_read_b64_tr_b16 v[96:97], v6 offset:40960\n"
      "s_waitcnt vmcnt(22)\n"
      "v_mfma_f32_16x16x32_bf16 v[180:183], a[48:51], v[68:71], v[180:183]\n"
      "global_load_lds_dwordx4 v43, s[4:5] offset:3072 sc1\n"
      "ds_read_b128 v[248:251], v40 offset:49152\n"
      "ds_read_b64 v[22:23], v41 offset:384\n"
      "ds_read_b64_tr_b16 v[98:99], v6 offset:43008\n"
      "v_mfma_f32_16x16x32_bf16 v[188:191], a[52:55], v[68:71], v[188:191]\n"
      "global_load_dwordx4 a[28:31], v44, s[6:7] offset:-1024 sc1\n"
      "ds_read_b128 v[252:255], v40 offset:50176\n"
      "ds_read_b64_tr_b16 v[100:101], v34 offset:40960\n"
      "v_mfma_f32_16x16x32_bf16 v[196:199], a[56:59], v[68:71], v[196:199]\n"
      "ds_read_b128 v[36:39], v40 offset:51200\n"
      "ds_read_b64_tr_b16 v[102:103], v34 offset:43008\n"
      "v_mfma_f32_16x16x32_bf16 v[200:203], a[48:51], v[72:75], v[200:203]\n"
      "ds_read_b128 v[60:63], v40 offset:52224\n"
      "ds_read_b64_tr_b16 v[104:105], v33 offset:40960\n"
      "s_waitcnt lgkmcnt(7)\n"
      "v_mfma_f32_16x16x32_bf16 v[208:211], a[52:55], v[72:75], v[208:211]\n"
      "ds_read_b64_tr_b16 v[106:107], v33 offset:43008\n"
      "v_fmac_f32 v128, v16, v224\n"
      "v_fmac_f32 v129, v16, v225\n"
      "v_mfma_f32_16x16x32_bf16 v[216:219], a[56:59], v[72:75], v[216:219]\n"
      "global_load_dwordx4 a[32:35], v44, s[6:7] offset:0 sc1\n"
      "ds_read_b64_tr_b16 v[108:109], v35 offset:40960\n"
      "v_fmac_f32 v130, v16, v226\n"
      "v_fmac_f32 v131, v16, v227\n"
      "v_mfma_f32_16x16x32_bf16 v[204:207], a[48:51], v[76:79], v[204:207]\n"
      "ds_read_b64_tr_b16 v[110:111], v35 offset:43008\n"
      "v_fma_f32 v128, v128, v17, v236\n"
      "v_fma_f32 v129, v129, v17, v237\n"
      "v_fma_f32 v130, v130, v17, v238\n"
      "v_mfma_f32_16x16x32_bf16 v[212:215], a[52:55], v[76:79], v[212:215]\n"
      "ds_read_b64_tr_b16 v[112:113], v6 offset:45056\n"
      "s_waitcnt lgkmcnt(5)\n"
      "v_add3_u32 v55, v55, v248, v249\n"
      "v_add3_u32 v55, v55, v250, v251\n"
      "v_fma_f32 v131, v131, v17, v239\n"
      "v_mul_f32 v128, v128, v130\n"
      "v_mfma_f32_16x16x32_bf16 v[220:223], a[56:59], v[76:79], v[220:223]\n"
      "ds_read_b64_tr_b16 v[114:115], v6 offset:47104\n"
      "v_add3_u32 v55, v55, v252, v253\n"
      "v_add3_u32 v55, v55, v254, v255\n"
      "v_mul_f32 v129, v129, v131\n"
      "v_cvt_pk_bf16_f32 v128, v128, v129\n"
      "v_mfma_f32_16x16x32_bf16 v[176:179], a[60:63], v[80:83], v[176:179]\n"
      "global_load_dwordx4 a[36:39], v44, s[6:7] offset:1024 sc1\n"
      "ds_write_b32 v46, v128 offset:0\n"
      "v_add3_u32 v55, v55, v36, v37\n"
      "v_add3_u32 v55, v55, v38, v39\n"
      "v_fmac_f32 v144, v16, v228\n"
      "v_fmac_f32 v145, v16, v229\n"
      "v_mfma_f32_16x16x32_bf16 v[184:187], a[64:67], v[80:83], v[184:187]\n"
      "ds_read_b64_tr_b16 v[116:117], v34 offset:45056\n"
      "v_add3_u32 v55, v55, v60, v61\n"
      "v_fmac_f32 v146, v16, v230\n"
      "v_fmac_f32 v147, v16, v231\n"
      "v_mfma_f32_16x16x32_bf16 v[192:195], a[68:71], v[80:83], v[192:195]\n"
      "ds_read_b64_tr_b16 v[118:119], v34 offset:47104\n"
      "v_add3_u32 v55, v55, v62, v63\n"
      "v_fma_f32 v144, v144, v17, v240\n"
      "v_fma_f32 v145, v145, v17, v241\n"
      "v_mfma_f32_16x16x32_bf16 v[180:183], a[60:63], v[84:87], v[180:183]\n"
      "ds_read_b64_tr_b16 v[120:121], v33 offset:45056\n"
      "v_fma_f32 v146, v146, v17, v242\n"
      "v_fma_f32 v147, v147, v17, v243\n"
      "v_mul_f32 v144, v144, v146\n"
      "v_mfma_f32_16x16x32_bf16 v[188:191], a[64:67], v[84:87], v[188:191]\n"
      "global_load_dwordx4 a[40:43], v44, s[6:7] offset:2048 sc1\n"
      "ds_read_b64_tr_b16 v[122:123], v33 offset:47104\n"
      "v_add_u32_dpp v55, v55, v55 quad_perm:[1,0,3,2] row_mask:0xf bank_mask:0xf\n"
      "v_mul_f32 v145, v145, v147\n"
      "v_cvt_pk_bf16_f32 v144, v144, v145\n"
      "v_mfma_f32_16x16x32_bf16 v[196:199], a[68:71], v[84:87], v[196:199]\n"
      "ds_write_b32 v46, v144 offset:4096\n"
      "ds_read_b64_tr_b16 v[124:125], v35 offset:45056\n"
      "v_fmac_f32 v160, v16, v232\n"
      "v_mfma_f32_16x16x32_bf16 v[200:203], a[60:63], v[88:91], v[200:203]\n"
      "ds_read_b64_tr_b16 v[126:127], v35 offset:47104\n"
      "v_add_u32_dpp v55, v55, v55 quad_perm:[2,3,0,1] row_mask:0xf bank_mask:0xf\n"
      "v_fmac_f32 v161, v16, v233\n"
      "v_fmac_f32 v162, v16, v234\n"
      "v_fmac_f32 v163, v16, v235\n"
      "v_mfma_f32_16x16x32_bf16 v[208:211], a[64:67], v[88:91], v[208:211]\n"
      "v_fma_f32 v160, v160, v17, v244\n"
      "v_fma_f32 v161, v161, v17, v245\n"
      "v_mfma_f32_16x16x32_bf16 v[216:219], a[68:71], v[88:91], v[216:219]\n"
      "global_load_dwordx4 a[44:47], v44, s[6:7] offset:3072 sc1\n"
      "v_add_u32_dpp v55, v55, v55 row_half_mirror row_mask:0xf bank_mask:0xf\n"
      "v_cmp_ne_u32 vcc, s71, v55\n"
      "v_fma_f32 v162, v162, v17, v246\n"
      "v_fma_f32 v163, v163, v17, v247\n"
      "v_mul_f32 v160, v160, v162\n"
      "v_mfma_f32_16x16x32_bf16 v[204:207], a[60:63], v[92:95], v[204:207]\n"
      "v_mul_f32 v161, v161, v163\n"
      "v_cvt_pk_bf16_f32 v160, v160, v161\n"
      "v_mfma_f32_16x16x32_bf16 v[212:215], a[64:67], v[92:95], v[212:215]\n"
      "ds_write_b32 v46, v160 offset:8192\n"
      "s_cbranch_vccnz L_sx7_%=\n"
      "L_bx7_%=:\n"
      "v_fmac_f32 v132, v18, v224\n"
      "v_mfma_f32_16x16x32_bf16 v[220:223], a[68:71], v[92:95], v[220:223]\n"
      "v_fmac_f32 v133, v18, v225\n"
      "v_fmac_f32 v134, v18, v226\n"
      "v_fmac_f32 v135, v18, v227\n"
      "v_add_u32 v44, 20480, v7\n"
      "s_waitcnt lgkmcnt(1)\n"
      "s_barrier\n"
      "v_mfma_f32_16x16x32_bf16 v[176:179], a[72:75], v[96:99], v[176:179]\n"
      "global_load_dwordx4 a[48:51], v44, s[6:7] offset:-4096 sc1\n"
      "v_fma_f32 v132, v132, v19, v236\n"
      "v_fma_f32 v133, v133, v19, v237\n"
      "v_mfma_f32_16x16x32_bf16 v[184:187], a[76:79], v[96:99], v[184:187]\n"
      "ds_read_b64_tr_b16 v[64:65], v6 offset:49152\n"
      "v_fma_f32 v134, v134, v19, v238\n"
      "v_fma_f32 v135, v135, v19, v239\n"
      "v_mfma_f32_16x16x32_bf16 v[192:195], a[80:83], v[96:99], v[192:195]\n"
      "ds_read_b64_tr_b16 v[66:67], v6 offset:51200\n"
      "v_mul_f32 v132, v132, v134\n"
      "v_mul_f32 v133, v133, v135\n"
      "v_cvt_pk_bf16_f32 v132, v132, v133\n"
      "v_mfma_f32_16x16x32_bf16 v[180:183], a[72:75], v[100:103], v[180:183]\n"
      "ds_write_b32 v46, v132 offset:1024\n"
      "ds_read_b64_tr_b16 v[68:69], v34 offset:49152\n"
      "v_fmac_f32 v148, v18, v228\n"
      "v_mfma_f32_16x16x32_bf16 v[188:191], a[76:79], v[100:103], v[188:191]\n"
      "global_load_dwordx4 a[52:55], v44, s[6:7] offset:-3072 sc1\n"
      "s_waitcnt vmcnt(20)\n"
      "v_fmac_f32 v149, v18, v229\n"
      "v_fmac_f32 v150, v18, v230\n"
      "v_mfma_f32_16x16x32_bf16 v[196:199], a[80:83], v[100:103], v[196:199]\n"
      "ds_read_b128 v[248:251], v40 offset:0\n"
      "ds_read_b64_tr_b16 v[70:71], v34 offset:51200\n"
      "v_fmac_f32 v151, v18, v231\n"
      "v_fma_f32 v148, v148, v19, v240\n"
      "v_fma_f32 v149, v149, v19, v241\n"
      "v_mfma_f32_16x16x32_bf16 v[200:203], a[72:75], v[104:107], v[200:203]\n"
      "ds_read_b128 v[252:255], v40 offset:1024\n"
      "ds_read_b64_tr_b16 v[72:73], v33 offset:49152\n"
      "v_fma_f32 v150, v150, v19, v242\n"
      "v_fma_f32 v151, v151, v19, v243\n"
      "v_mfma_f32_16x16x32_bf16 v[208:211], a[76:79], v[104:107], v[208:211]\n"
      "ds_read_b128 v[36:39], v40 offset:2048\n"
      "ds_read_b64_tr_b16 v[74:75], v33 offset:51200\n"
      "v_mul_f32 v148, v148, v150\n"
      "v_mul_f32 v149, v149, v151\n"
      "v_cvt_pk_bf16_f32 v148, v148, v149\n"
      "v_mfma_f32_16x16x32_bf16 v[216:219], a[80:83], v[104:107], v[216:219]\n"
      "global_load_dwordx4 a[56:59], v44, s[6:7] offset:-2048 sc1\n"
      "ds_read_b128 v[60:63], v40 offset:3072\n"
      "ds_write_b32 v46, v148 offset:5120\n"
      "ds_read_b64_tr_b16 v[76:77], v35 offset:49152\n"
      "v_fmac_f32 v164, v18, v232\n"
      "v_mfma_f32_16x16x32_bf16 v[204:207], a[72:75], v[108:111], v[204:207]\n"
      "ds_read_b64_tr_b16 v[78:79], v35 offset:51200\n"
      "v_fmac_f32 v165, v18, v233\n"
      "v_fmac_f32 v166, v18, v234\n"
      "v_mfma_f32_16x16x32_bf16 v[212:215], a[76:79], v[108:111], v[212:215]\n"
      "ds_read_b64_tr_b16 v[80:81], v6 offset:53248\n"
      "v_fmac_f32 v167, v18, v235\n"
      "v_fma_f32 v164, v164, v19, v244\n"
      "v_fma_f32 v165, v165, v19, v245\n"
      "v_mfma_f32_16x16x32_bf16 v[220:223], a[80:83], v[108:111], v[220:223]\n"
      "ds_read_b64_tr_b16 v[82:83], v6 offset:55296\n"
      "v_fma_f32 v166, v166, v19, v246\n"
      "v_fma_f32 v167, v167, v19, v247\n"
      "v_mfma_f32_16x16x32_bf16 v[176:179], a[84:87], v[112:115], v[176:179]\n"
      "global_load_dwordx4 a[60:63], v44, s[6:7] offset:-1024 sc1\n"
      "s_waitcnt lgkmcnt(5)\n"
      "v_add3_u32 v57, v57, v248, v249\n"
      "v_add3_u32 v57, v57, v250, v251\n"
      "v_mul_f32 v164, v164, v166\n"
      "v_mul_f32 v165, v165, v167\n"
      "v_cvt_pk_bf16_f32 v164, v164, v165\n"
      "v_mfma_f32_16x16x32_bf16 v[184:187], a[88:91], v[112:115], v[184:187]\n"
      "ds_write_b32 v46, v164 offset:9216\n"
      "ds_read_b64_tr_b16 v[84:85], v34 offset:53248\n"
      "v_add3_u32 v57, v57, v252, v253\n"
      "v_add3_u32 v57, v57, v254, v255\n"
      "v_fmac_f32 v136, v20, v224\n"
      "v_mfma_f32_16x16x32_bf16 v[192:195], a[92:95], v[112:115], v[192:195]\n"
      "ds_read_b64_tr_b16 v[86:87], v34 offset:55296\n"
      "v_add3_u32 v57, v57, v36, v37\n"
      "v_add3_u32 v57, v57, v38, v39\n"
      "v_fmac_f32 v137, v20, v225\n"
      "v_fmac_f32 v138, v20, v226\n"
      "v_mfma_f32_16x16x32_bf16 v[180:183], a[84:87], v[116:119], v[180:183]\n"
      "ds_read_b64_tr_b16 v[88:89], v33 offset:53248\n"
      "v_add3_u32 v57, v57, v60, v61\n"
      "v_fmac_f32 v139, v20, v227\n"
      "v_fma_f32 v136, v136, v21, v236\n"
      "v_mfma_f32_16x16x32_bf16 v[188:191], a[88:91], v[116:119], v[188:191]\n"
      "global_load_dwordx4 a[64:67], v44, s[6:7] offset:0 sc1\n"
      "ds_read_b64_tr_b16 v[90:91], v33 offset:55296\n"
      "v_add3_u32 v57, v57, v62, v63\n"
      "v_fma_f32 v137, v137, v21, v237\n"
      "v_fma_f32 v138, v138, v21, v238\n"
      "v_fma_f32 v139, v139, v21, v239\n"
      "v_mfma_f32_16x16x32_bf16 v[196:199], a[92:95], v[116:119], v[196:199]\n"
      "ds_read_b64_tr_b16 v[92:93], v35 offset:53248\n"
      "v_mul_f32 v136, v136, v138\n"
      "v_mul_f32 v137, v137, v139\n"
      "v_cvt_pk_bf16_f32 v136, v136, v137\n"
      "v_mfma_f32_16x16x32_bf16 v[200:203], a[84:87], v[120:123], v[200:203]\n"
      "ds_write_b32 v46, v136 offset:2048\n"
      "ds_read_b64_tr_b16 v[94:95], v35 offset:55296\n"
      "v_add_u32_dpp v57, v57, v57 quad_perm:[1,0,3,2] row_mask:0xf bank_mask:0xf\n"
      "v_mfma_f32_16x16x32_bf16 v[208:211], a[88:91], v[120:123], v[208:211]\n"
      "v_fmac_f32 v152, v20, v228\n"
      "v_fmac_f32 v153, v20, v229\n"
      "v_fmac_f32 v154, v20, v230\n"
      "v_mfma_f32_16x16x32_bf16 v[216:219], a[92:95], v[120:123], v[216:219]\n"
      "global_load_dwordx4 a[68:71], v44, s[6:7] offset:1024 sc1\n"
      "v_add_u32_dpp v57, v57, v57 quad_perm:[2,3,0,1] row_mask:0xf bank_mask:0xf\n"
      "v_fmac_f32 v155, v20, v231\n"
      "v_fma_f32 v152, v152, v21, v240\n"
      "v_mfma_f32_16x16x32_bf16 v[204:207], a[84:87], v[124:127], v[204:207]\n"
      "v_fma_f32 v153, v153, v21, v241\n"
      "v_fma_f32 v154, v154, v21, v242\n"
      "v_fma_f32 v155, v155, v21, v243\n"
      "v_mfma_f32_16x16x32_bf16 v[212:215], a[88:91], v[124:127], v[212:215]\n"
      "v_add_u32_dpp v57, v57, v57 row_half_mirror row_mask:0xf bank_mask:0xf\n"
      "v_cmp_ne_u32 vcc, s71, v57\n"
      "v_mul_f32 v152, v152, v154\n"
      "v_mul_f32 v153, v153, v155\n"
      "v_cvt_pk_bf16_f32 v152, v152, v153\n"
      "v_mfma_f32_16x16x32_bf16 v[220:223], a[92:95], v[124:127], v[220:223]\n"
      "ds_write_b32 v46, v152 offset:6144\n"
      "v_fmac_f32 v168, v20, v232\n"
      "s_waitcnt lgkmcnt(1)\n"
      "v_mfma_f32_16x16x32_bf16 v[176:179], a[96:99], v[64:67], v[176:179]\n"
      "global_load_dwordx4 a[72:75], v44, s[6:7] offset:2048 sc1\n"
      "s_cbranch_vccnz L_sx8_%=\n"
      "L_bx8_%=:\n"
      "v_fmac_f32 v169, v20, v233\n"
      "v_fmac_f32 v170, v20, v234\n"
      "s_add_u32 m0, s40, 4096\n"
      "v_add_u32 v43, 45056, v7\n"
      "v_mfma_f32_16x16x32_bf16 v[184:187], a[100:103], v[64:67], v[184:187]\n"
      "global_load_lds_dwordx4 v43, s[6:7] offset:0 sc1\n"
      "v_fmac_f32 v171, v20, v235\n"
      "v_fma_f32 v168, v168, v21, v244\n"
      "v_mfma_f32_16x16x32_bf16 v[192:195], a[104:107], v[64:67], v[192:195]\n"
      "ds_read_b64_tr_b16 v[96:97], v6 offset:57344\n"
      "s_waitcnt vmcnt(13)\n"
      "v_fma_f32 v169, v169, v21, v245\n"
      "v_fma_f32 v170, v170, v21, v246\n"
      "v_mfma_f32_16x16x32_bf16 v[180:183], a[96:99], v[68:71], v[180:183]\n"
      "ds_read_b128 v[248:251], v40 offset:16384\n"
      "ds_read_b64_tr_b16 v[98:99], v6 offset:59392\n"
      "v_fma_f32 v171, v171, v21, v247\n"
      "v_mul_f32 v168, v168, v170\n"
      "v_mul_f32 v169, v169, v171\n"
      "v_cvt_pk_bf16_f32 v168, v168, v169\n"
      "v_mfma_f32_16x16x32_bf16 v[188:191], a[100:103], v[68:71], v[188:191]\n"
      "global_load_dwordx4 a[76:79], v44, s[6:7] offset:3072 sc1\n"
      "ds_read_b128 v[252:255], v40 offset:17408\n"
      "ds_write_b32 v46, v168 offset:10240\n"
      "ds_read_b64_tr_b16 v[100:101], v34 offset:57344\n"
      "v_fmac_f32 v140, v22, v224\n"
      "v_mfma_f32_16x16x32_bf16 v[196:199], a[104:107], v[68:71], v[196:199]\n"
      "global_load_lds_dwordx4 v43, s[6:7] offset:1024 sc1\n"
      "ds_read_b128 v[36:39], v40 offset:18432\n"
      "ds_read_b64_tr_b16 v[102:103], v34 offset:59392\n"
      "v_fmac_f32 v141, v22, v225\n"
      "v_mfma_f32_16x16x32_bf16 v[200:203], a[96:99], v[72:75], v[200:203]\n"
      "ds_read_b128 v[60:63], v40 offset:19456\n"
      "ds_read_b64_tr_b16 v[104:105], v33 offset:57344\n"
      "v_fmac_f32 v142, v22, v226\n"
      "v_fmac_f32 v143, v22, v227\n"
      "v_fma_f32 v140, v140, v23, v236\n"
      "v_mfma_f32_16x16x32_bf16 v[208:211], a[100:103], v[72:75], v[208:211]\n"
      "ds_read_b64_tr_b16 v[106:107], v33 offset:59392\n"
      "v_fma_f32 v141, v141, v23, v237\n"
      "v_fma_f32 v142, v142, v23, v238\n"
      "v_add_u32 v44, 28672, v7\n"
      "v_mfma_f32_16x16x32_bf16 v[216:219], a[104:107], v[72:75], v[216:219]\n"
      "global_load_dwordx4 a[80:83], v44, s[6:7] offset:-4096 sc1\n"
      "ds_read_b64_tr_b16 v[108:109], v35 offset:57344\n"
      "v_fma_f32 v143, v143, v23, v239\n"
      "v_mul_f32 v140, v140, v142\n"
      "v_mul_f32 v141, v141, v143\n"
      "v_cvt_pk_bf16_f32 v140, v140, v141\n"
      "v_mfma_f32_16x16x32_bf16 v[204:207], a[96:99], v[76:79], v[204:207]\n"
      "global_load_lds_dwordx4 v43, s[6:7] offset:2048 sc1\n"
      "ds_write_b32 v46, v140 offset:3072\n"
      "ds_read_b64_tr_b16 v[110:111], v35 offset:59392\n"
      "v_fmac_f32 v156, v22, v228\n"
      "v_mfma_f32_16x16x32_bf16 v[212:215], a[100:103], v[76:79], v[212:215]\n"
      "ds_read_b64_tr_b16 v[112:113], v6 offset:61440\n"
      "s_waitcnt lgkmcnt(6)\n"
      "v_add3_u32 v3, v3, v248, v249\n"
      "v_add3_u32 v3, v3, v250, v251\n"
      "v_fmac_f32 v157, v22, v229\n"
      "v_mfma_f32_16x16x32_bf16 v[220:223], a[104:107], v[76:79], v[220:223]\n"
      "ds_read_b64_tr_b16 v[114:115], v6 offset:63488\n"
      "v_add3_u32 v3, v3, v252, v253\n"
      "v_add3_u32 v3, v3, v254, v255\n"
      "v_fmac_f32 v158, v22, v230\n"
      "v_fmac_f32 v159, v22, v231\n"
      "v_fma_f32 v156, v156, v23, v240\n"
      "v_mfma_f32_16x16x32_bf16 v[176:179], a[108:111], v[80:83], v[176:179]\n"
      "global_load_dwordx4 a[84:87], v44, s[6:7] offset:-3072 sc1\n"
      "v_add3_u32 v3, v3, v36, v37\n"
      "v_add3_u32 v3, v3, v38, v39\n"
      "v_fma_f32 v157, v157, v23, v241\n"
      "v_fma_f32 v158, v158, v23, v242\n"
      "v_mfma_f32_16x16x32_bf16 v[184:187], a[112:115], v[80:83], v[184:187]\n"
      "global_load_lds_dwordx4 v43, s[6:7] offset:3072 sc1\n"
      "ds_read_b64_tr_b16 v[116:117], v34 offset:61440\n"
      "v_add3_u32 v3, v3, v60, v61\n"
      "v_fma_f32 v159, v159, v23, v243\n"
      "v_mul_f32 v156, v156, v158\n"
      "v_mul_f32 v157, v157, v159\n"
      "v_cvt_pk_bf16_f32 v156, v156, v157\n"
      "v_mfma_f32_16x16x32_bf16 v[192:195], a[116:119], v[80:83], v[192:195]\n"
      "ds_write_b32 v46, v156 offset:7168\n"
      "ds_read_b64_tr_b16 v[118:119], v34 offset:63488\n"
      "v_add3_u32 v3, v3, v62, v63\n"
      "v_mfma_f32_16x16x32_bf16 v[180:183], a[108:111], v[84:87], v[180:183]\n"
      "ds_read_b64_tr_b16 v[120:121], v33 offset:61440\n"
      "v_fmac_f32 v172, v22, v232\n"
      "v_fmac_f32 v173, v22, v233\n"
      "v_mfma_f32_16x16x32_bf16 v[188:191], a[112:115], v[84:87], v[188:191]\n"
      "global_load_dwordx4 a[88:91], v44, s[6:7] offset:-2048 sc1\n"
      "ds_read_b64_tr_b16 v[122:123], v33 offset:63488\n"
      "v_add_u32_dpp v3, v3, v3 quad_perm:[1,0,3,2] row_mask:0xf bank_mask:0xf\n"
      "v_fmac_f32 v174, v22, v234\n"
      "v_fmac_f32 v175, v22, v235\n"
      "s_add_u32 m0, s40, 8192\n"
      "v_add_u32 v43, 49152, v7\n"
      "v_mfma_f32_16x16x32_bf16 v[196:199], a[116:119], v[84:87], v[196:199]\n"
      "global_load_lds_dwordx4 v43, s[6:7] offset:0 sc1\n"
      "ds_read_b64_tr_b16 v[124:125], v35 offset:61440\n"
      "v_fma_f32 v172, v172, v23, v244\n"
      "v_fma_f32 v173, v173, v23, v245\n"
      "v_fma_f32 v174, v174, v23, v246\n"
      "v_mfma_f32_16x16x32_bf16 v[200:203], a[108:111], v[88:91], v[200:203]\n"
      "ds_read_b64_tr_b16 v[126:127], v35 offset:63488\n"
      "v_add_u32_dpp v3, v3, v3 quad_perm:[2,3,0,1] row_mask:0xf bank_mask:0xf\n"
      "v_fma_f32 v175, v175, v23, v247\n"
      "v_mul_f32 v172, v172, v174\n"
      "v_mfma_f32_16x16x32_bf16 v[208:211], a[112:115], v[88:91], v[208:211]\n"
      "v_mul_f32 v173, v173, v175\n"
      "v_cvt_pk_bf16_f32 v172, v172, v173\n"
      "v_mfma_f32_16x16x32_bf16 v[216:219], a[116:119], v[88:91], v[216:219]\n"
      "global_load_dwordx4 a[92:95], v44, s[6:7] offset:-1024 sc1\n"
      "ds_write_b32 v46, v172 offset:11264\n"
      "v_add_u32_dpp v3, v3, v3 row_half_mirror row_mask:0xf bank_mask:0xf\n"
      "v_cmp_ne_u32 vcc, s71, v3\n"
      "v_mfma_f32_16x16x32_bf16 v[204:207], a[108:111], v[92:95], v[204:207]\n"
      "v_mfma_f32_16x16x32_bf16 v[212:215], a[112:115], v[92:95], v[212:215]\n"
      "s_cbranch_vccnz L_sx9_%=\n"
      "L_bx9_%=:\n"
      "v_mfma_f32_16x16x32_bf16 v[220:223], a[116:119], v[92:95], v[220:223]\n"
      "s_waitcnt lgkmcnt(0)\n"
      "s_barrier\n"
      "v_mfma_f32_16x16x32_bf16 v[176:179], a[120:123], v[96:99], v[176:179]\n"
      "global_load_dwordx4 a[96:99], v44, s[6:7] offset:0 sc1\n"
      "ds_read_b128 v[160:163], v47 offset:0\n"
      "v_add_u32 v57, s40, v7\n"
      "v_mfma_f32_16x16x32_bf16 v[184:187], a[124:127], v[96:99], v[184:187]\n"
      "ds_read_b128 v[164:167], v47 offset:4096\n"
      "v_mfma_f32_16x16x32_bf16 v[192:195], a[128:131], v[96:99], v[192:195]\n"
      "ds_read_b128 v[168:171], v47 offset:8192\n"
      "ds_read_b64_tr_b16 v[64:65], v6 offset:0\n"
      "v_mfma_f32_16x16x32_bf16 v[180:183], a[120:123], v[100:103], v[180:183]\n"
      "ds_read_b64_tr_b16 v[66:67], v6 offset:2048\n"
      "v_mfma_f32_16x16x32_bf16 v[188:191], a[124:127], v[100:103], v[188:191]\n"
      "global_load_dwordx4 a[100:103], v44, s[6:7] offset:1024 sc1\n"
      "ds_read_b64_tr_b16 v[68:69], v34 offset:0\n"
      "v_mfma_f32_16x16x32_bf16 v[196:199], a[128:131], v[100:103], v[196:199]\n"
      "ds_read_b64_tr_b16 v[70:71], v34 offset:2048\n"
      "s_waitcnt lgkmcnt(4)\n"
      "v_mfma_f32_16x16x32_bf16 v[200:203], a[120:123], v[104:107], v[200:203]\n"
      "ds_read_b64_tr_b16 v[72:73], v6 offset:4096\n"
      "v_mfma_f32_16x16x32_bf16 v[208:211], a[124:127], v[104:107], v[208:211]\n"
      "buffer_store_dwordx4 v[160:163], v7, s[32:35], s61 offen offset:0 sc1\n"
      "ds_read_b64_tr_b16 v[74:75], v6 offset:6144\n"
      "v_add3_u32 v58, v160, v161, s30\n"
      "v_mfma_f32_16x16x32_bf16 v[216:219], a[128:131], v[104:107], v[216:219]\n"
      "global_load_dwordx4 a[104:107], v44, s[6:7] offset:2048 sc1\n"
      "ds_read_b64_tr_b16 v[76:77], v34 offset:4096\n"
      "v_add3_u32 v58, v58, v162, v163\n"
      "v_add3_u32 v58, v58, v164, v165\n"
      "s_add_u32 m0, s41, 0\n"
      "v_add_u32 v43, 50176, v7\n"
      "v_mfma_f32_16x16x32_bf16 v[204:207], a[120:123], v[108:111], v[204:207]\n"
      "buffer_store_dwordx4 v[164:167], v7, s[32:35], s61 offen offset:1024 sc1\n"
      "global_load_lds_dwordx4 v43, s[6:7] offset:0 sc1\n"
      "ds_read_b64_tr_b16 v[78:79], v34 offset:6144\n"
      "v_add3_u32 v58, v58, v166, v167\n"
      "v_mfma_f32_16x16x32_bf16 v[212:215], a[124:127], v[108:111], v[212:215]\n"
      "ds_read_b64_tr_b16 v[80:81], v6 offset:8192\n"
      "v_add3_u32 v58, v58, v168, v169\n"
      "v_add3_u32 v58, v58, v170, v171\n"
      "v_mfma_f32_16x16x32_bf16 v[220:223], a[128:131], v[108:111], v[220:223]\n"
      "buffer_store_dwordx4 v[168:171], v7, s[32:35], s61 offen offset:2048 sc1\n"
      "v_mfma_f32_16x16x32_bf16 v[176:179], a[132:135], v[112:115], v[176:179]\n"
      "global_load_dwordx4 a[108:111], v44, s[6:7] offset:3072 sc1\n"
      "ds_read_b64_tr_b16 v[82:83], v6 offset:10240\n"
      "v_mfma_f32_16x16x32_bf16 v[184:187], a[136:139], v[112:115], v[184:187]\n"
      "buffer_store_dword v58, v42, s[32:35], s62 offen sc1\n"
      "global_load_lds_dwordx4 v43, s[6:7] offset:1024 sc1\n"
      "ds_read_b64_tr_b16 v[84:85], v34 offset:8192\n"
      "v_mfma_f32_16x16x32_bf16 v[192:195], a[140:143], v[112:115], v[192:195]\n"
      "ds_read_b64_tr_b16 v[86:87], v34 offset:10240\n"
      "v_mfma_f32_16x16x32_bf16 v[180:183], a[132:135], v[116:119], v[180:183]\n"
      "ds_read_b64_tr_b16 v[88:89], v6 offset:12288\n"
      "v_add_u32 v44, 36864, v7\n"
      "v_mfma_f32_16x16x32_bf16 v[188:191], a[136:139], v[116:119], v[188:191]\n"
      "global_load_dwordx4 a[112:115], v44, s[6:7] offset:-4096 sc1\n"
      "ds_read_b64_tr_b16 v[90:91], v6 offset:14336\n"
      "v_mfma_f32_16x16x32_bf16 v[196:199], a[140:143], v[116:119], v[196:199]\n"
      "global_load_lds_dwordx4 v43, s[6:7] offset:2048 sc1\n"
      "ds_read_b64_tr_b16 v[92:93], v34 offset:12288\n"
      "v_mfma_f32_16x16x32_bf16 v[200:203], a[132:135], v[120:123], v[200:203]\n"
      "ds_read_b64_tr_b16 v[94:95], v34 offset:14336\n"
      "v_mfma_f32_16x16x32_bf16 v[208:211], a[136:139], v[120:123], v[208:211]\n"
      "ds_read_b64 v[24:25], v41 offset:512\n"
      "v_mfma_f32_16x16x32_bf16 v[216:219], a[140:143], v[120:123], v[216:219]\n"
      "global_load_dwordx4 a[116:119], v44, s[6:7] offset:-3072 sc1\n"
      "v_mfma_f32_16x16x32_bf16 v[204:207], a[132:135], v[124:127], v[204:207]\n"
      "global_load_lds_dwordx4 v43, s[6:7] offset:3072 sc1\n"
      "v_mfma_f32_16x16x32_bf16 v[212:215], a[136:139], v[124:127], v[212:215]\n"
      "ds_read_b64 v[26:27], v41 offset:768\n"
      "v_mfma_f32_16x16x32_bf16 v[220:223], a[140:143], v[124:127], v[220:223]\n"
      "ds_read_b64 v[28:29], v41 offset:640\n"
      "s_waitcnt lgkmcnt(3)\n"
      "v_mfma_f32_16x16x32_bf16 v[176:179], a[144:147], v[64:67], v[176:179]\n"
      "global_load_dwordx4 a[120:123], v44, s[6:7] offset:-2048 sc1\n"
      "s_add_u32 m0, s41, 4096\n"
      "v_add_u32 v43, 54272, v7\n"
      "v_mfma_f32_16x16x32_bf16 v[184:187], a[148:151], v[64:67], v[184:187]\n"
      "global_load_lds_dwordx4 v43, s[6:7] offset:0 sc1\n"
      "v_mfma_f32_16x16x32_bf16 v[192:195], a[152:155], v[64:67], v[192:195]\n"
      "ds_read_b64_tr_b16 v[96:97], v6 offset:16384\n"
      "v_mfma_f32_16x16x32_bf16 v[180:183], a[144:147], v[68:71], v[180:183]\n"
      "ds_read_b64_tr_b16 v[98:99], v6 offset:18432\n"
      "v_mfma_f32_16x16x32_bf16 v[188:191], a[148:151], v[68:71], v[188:191]\n"
      "global_load_dwordx4 a[124:127], v44, s[6:7] offset:-1024 sc1\n"
      "ds_read_b64_tr_b16 v[100:101], v34 offset:16384\n"
      "v_mfma_f32_16x16x32_bf16 v[196:199], a[152:155], v[68:71], v[196:199]\n"
      "global_load_lds_dwordx4 v43, s[6:7] offset:1024 sc1\n"
      "ds_read_b64_tr_b16 v[102:103], v34 offset:18432\n"
      "v_mfma_f32_16x16x32_bf16 v[176:179], a[156:159], v[72:75], v[176:179]\n"
      "ds_read_b64_tr_b16 v[104:105], v6 offset:20480\n"
      "v_mfma_f32_16x16x32_bf16 v[184:187], a[160:163], v[72:75], v[184:187]\n"
      "ds_read_b64_tr_b16 v[106:107], v6 offset:22528\n"
      "v_mfma_f32_16x16x32_bf16 v[192:195], a[164:167], v[72:75], v[192:195]\n"
      "global_load_dwordx4 a[128:131], v44, s[6:7] offset:0 sc1\n"
      "ds_read_b64_tr_b16 v[108:109], v34 offset:20480\n"
      "v_mfma_f32_16x16x32_bf16 v[180:183], a[156:159], v[76:79], v[180:183]\n"
      "global_load_lds_dwordx4 v43, s[6:7] offset:2048 sc1\n"
      "ds_read_b64_tr_b16 v[110:111], v34 offset:22528\n"
      "v_mfma_f32_16x16x32_bf16 v[188:191], a[160:163], v[76:79], v[188:191]\n"
      "ds_read_b64_tr_b16 v[112:113], v6 offset:24576\n"
      "v_mfma_f32_16x16x32_bf16 v[196:199], a[164:167], v[76:79], v[196:199]\n"
      "ds_read_b64_tr_b16 v[114:115], v6 offset:26624\n"
      "v_mfma_f32_16x16x32_bf16 v[176:179], a[168:171], v[80:83], v[176:179]\n"
      "global_load_dwordx4 a[132:135], v44, s[6:7] offset:1024 sc1\n"
      "v_mfma_f32_16x16x32_bf16 v[184:187], a[172:175], v[80:83], v[184:187]\n"
      "global_load_lds_dwordx4 v43, s[6:7] offset:3072 sc1\n"
      "ds_read_b64_tr_b16 v[116:117], v34 offset:24576\n"
      "v_mfma_f32_16x16x32_bf16 v[192:195], a[176:179], v[80:83], v[192:195]\n"
      "ds_read_b64_tr_b16 v[118:119], v34 offset:26624\n"
      "v_mfma_f32_16x16x32_bf16 v[180:183], a[168:171], v[84:87], v[180:183]\n"
      "ds_read_b64_tr_b16 v[120:121], v6 offset:28672\n"
      "v_mfma_f32_16x16x32_bf16 v[188:191], a[172:175], v[84:87], v[188:191]\n"
      "global_load_dwordx4 a[136:139], v44, s[6:7] offset:2048 sc1\n"
      "ds_read_b64_tr_b16 v[122:123], v6 offset:30720\n"
      "v_mfma_f32_16x16x32_bf16 v[196:199], a[176:179], v[84:87], v[196:199]\n"
      "ds_read_b64_tr_b16 v[124:125], v34 offset:28672\n"
      "v_mfma_f32_16x16x32_bf16 v[176:179], a[180:183], v[88:91], v[176:179]\n"
      "ds_read_b64_tr_b16 v[126:127], v34 offset:30720\n"
      "v_mfma_f32_16x16x32_bf16 v[184:187], a[184:187], v[88:91], v[184:187]\n"
      "ds_read_b64 v[30:31], v41 offset:896\n"
      "v_mfma_f32_16x16x32_bf16 v[192:195], a[188:191], v[88:91], v[192:195]\n"
      "global_load_dwordx4 a[140:143], v44, s[6:7] offset:3072 sc1\n"
      "v_mfma_f32_16x16x32_bf16 v[180:183], a[180:183], v[92:95], v[180:183]\n"
      "v_mfma_f32_16x16x32_bf16 v[188:191], a[184:187], v[92:95], v[188:191]\n"
      "v_mfma_f32_16x16x32_bf16 v[196:199], a[188:191], v[92:95], v[196:199]\n"
      "s_waitcnt lgkmcnt(1)\n"
      "v_mfma_f32_16x16x32_bf16 v[176:179], a[192:195], v[96:99], v[176:179]\n"
      "ds_read_b64_tr_b16 v[64:65], v33 offset:0\n"
      "v_mfma_f32_16x16x32_bf16 v[184:187], a[196:199], v[96:99], v[184:187]\n"
      "ds_read_b64_tr_b16 v[66:67], v33 offset:2048\n"
      "v_mfma_f32_16x16x32_bf16 v[192:195], a[200:203], v[96:99], v[192:195]\n"
      "ds_read_b64_tr_b16 v[68:69], v35 offset:0\n"
      "v_mfma_f32_16x16x32_bf16 v[180:183], a[192:195], v[100:103], v[180:183]\n"
      "ds_read_b64_tr_b16 v[70:71], v35 offset:2048\n"
      "v_mfma_f32_16x16x32_bf16 v[188:191], a[196:199], v[100:103], v[188:191]\n"
      "ds_read_b64_tr_b16 v[72:73], v33 offset:4096\n"
      "v_mfma_f32_16x16x32_bf16 v[196:199], a[200:203], v[100:103], v[196:199]\n"
      "ds_read_b64_tr_b16 v[74:75], v33 offset:6144\n"
      "v_mfma_f32_16x16x32_bf16 v[176:179], a[204:207], v[104:107], v[176:179]\n"
      "ds_read_b64_tr_b16 v[76:77], v35 offset:4096\n"
      "v_add_u32 v45, 4096, v7\n"
      "v_mfma_f32_16x16x32_bf16 v[184:187], a[208:211], v[104:107], v[184:187]\n"
      "ds_read_b64_tr_b16 v[78:79], v35 offset:6144\n"
      "v_mfma_f32_16x16x32_bf16 v[192:195], a[212:215], v[104:107], v[192:195]\n"
      "global_load_dwordx4 v[8:11], v45, s[10:11] offset:-4096 sc1\n"
      "v_mfma_f32_16x16x32_bf16 v[180:183], a[204:207], v[108:111], v[180:183]\n"
      "ds_read_b64_tr_b16 v[80:81], v33 offset:8192\n"
      "v_mfma_f32_16x16x32_bf16 v[188:191], a[208:211], v[108:111], v[188:191]\n"
      "global_load_dwordx4 v[12:15], v45, s[12:13] offset:-4096 sc1\n"
      "v_mfma_f32_16x16x32_bf16 v[196:199], a[212:215], v[108:111], v[196:199]\n"
      "ds_read_b64_tr_b16 v[82:83], v33 offset:10240\n"
      "v_mfma_f32_16x16x32_bf16 v[176:179], a[216:219], v[112:115], v[176:179]\n"
      "ds_read_b64_tr_b16 v[84:85], v35 offset:8192\n"
      "v_mfma_f32_16x16x32_bf16 v[184:187], a[220:223], v[112:115], v[184:187]\n"
      "global_load_dwordx4 v[16:19], v45, s[10:11] offset:-3072 sc1\n"
      "v_mfma_f32_16x16x32_bf16 v[192:195], a[224:227], v[112:115], v[192:195]\n"
      "ds_read_b64_tr_b16 v[86:87], v35 offset:10240\n"
      "v_mfma_f32_16x16x32_bf16 v[180:183], a[216:219], v[116:119], v[180:183]\n"
      "ds_read_b64_tr_b16 v[88:89], v33 offset:12288\n"
      "v_mfma_f32_16x16x32_bf16 v[188:191], a[220:223], v[116:119], v[188:191]\n"
      "global_load_dwordx4 v[20:23], v45, s[12:13] offset:-3072 sc1\n"
      "ds_read_b64_tr_b16 v[90:91], v33 offset:14336\n"
      "v_mfma_f32_16x16x32_bf16 v[196:199], a[224:227], v[116:119], v[196:199]\n"
      "ds_read_b64_tr_b16 v[92:93], v35 offset:12288\n"
      "v_mfma_f32_16x16x32_bf16 v[176:179], a[228:231], v[120:123], v[176:179]\n"
      "ds_read_b64_tr_b16 v[94:95], v35 offset:14336\n"
      "v_mfma_f32_16x16x32_bf16 v[184:187], a[232:235], v[120:123], v[184:187]\n"
      "global_load_dwordx4 v[128:131], v45, s[10:11] offset:-2048 sc1\n"
      "v_mfma_f32_16x16x32_bf16 v[192:195], a[236:239], v[120:123], v[192:195]\n"
      "v_mfma_f32_16x16x32_bf16 v[180:183], a[228:231], v[124:127], v[180:183]\n"
      "v_mfma_f32_16x16x32_bf16 v[188:191], a[232:235], v[124:127], v[188:191]\n"
      "global_load_dwordx4 v[132:135], v45, s[12:13] offset:-2048 sc1\n"
      "v_mfma_f32_16x16x32_bf16 v[196:199], a[236:239], v[124:127], v[196:199]\n"
      "buffer_load_dword v36, v42, s[32:35], s63 offen offset:0 sc1\n"
      "buffer_load_dword v37, v42, s[32:35], s63 offen offset:256 sc1\n"
      "s_waitcnt lgkmcnt(0)\n"
      "s_barrier\n"
      "v_mfma_f32_16x16x32_bf16 v[200:203], a[144:147], v[64:67], v[200:203]\n"
      "ds_read_b64_tr_b16 v[96:97], v33 offset:16384\n"
      "v_mfma_f32_16x16x32_bf16 v[208:211], a[148:151], v[64:67], v[208:211]\n"
      "global_load_dwordx4 v[136:139], v45, s[10:11] offset:-1024 sc1\n"
      "v_mfma_f32_16x16x32_bf16 v[216:219], a[152:155], v[64:67], v[216:219]\n"
      "ds_read_b64_tr_b16 v[98:99], v33 offset:18432\n"
      "v_mfma_f32_16x16x32_bf16 v[204:207], a[144:147], v[68:71], v[204:207]\n"
      "ds_read_b64_tr_b16 v[100:101], v35 offset:16384\n"
      "v_fmac_f32 v176, v24, v224\n"
      "v_mfma_f32_16x16x32_bf16 v[212:215], a[148:151], v[68:71], v[212:215]\n"
      "global_load_dwordx4 v[140:143], v45, s[12:13] offset:-1024 sc1\n"
      "v_fmac_f32 v177, v24, v225\n"
      "v_fmac_f32 v178, v24, v226\n"
      "v_mfma_f32_16x16x32_bf16 v[220:223], a[152:155], v[68:71], v[220:223]\n"
      "ds_read_b64_tr_b16 v[102:103], v35 offset:18432\n"
      "v_fmac_f32 v179, v24, v227\n"
      "v_fma_f32 v176, v176, v25, v236\n"
      "v_mfma_f32_16x16x32_bf16 v[200:203], a[156:159], v[72:75], v[200:203]\n"
      "ds_read_b64_tr_b16 v[104:105], v33 offset:20480\n"
      "v_fma_f32 v177, v177, v25, v237\n"
      "v_mfma_f32_16x16x32_bf16 v[208:211], a[160:163], v[72:75], v[208:211]\n"
      "global_load_dwordx4 v[144:147], v45, s[10:11] offset:0 sc1\n"
      "ds_read_b64_tr_b16 v[106:107], v33 offset:22528\n"
      "v_fma_f32 v178, v178, v25, v238\n"
      "v_fma_f32 v179, v179, v25, v239\n"
      "v_mfma_f32_16x16x32_bf16 v[216:219], a[164:167], v[72:75], v[216:219]\n"
      "ds_read_b64_tr_b16 v[108:109], v35 offset:20480\n"
      "v_mul_f32 v176, v176, v178\n"
      "v_mul_f32 v177, v177, v179\n"
      "v_mfma_f32_16x16x32_bf16 v[204:207], a[156:159], v[76:79], v[204:207]\n"
      "ds_read_b64_tr_b16 v[110:111], v35 offset:22528\n"
      "v_cvt_pk_bf16_f32 v176, v176, v177\n"
      "v_mfma_f32_16x16x32_bf16 v[212:215], a[160:163], v[76:79], v[212:215]\n"
      "global_load_dwordx4 v[148:151], v45, s[12:13] offset:0 sc1\n"
      "ds_write_b32 v46, v176 offset:0\n"
      "ds_read_b64_tr_b16 v[112:113], v33 offset:24576\n"
      "v_fmac_f32 v184, v24, v228\n"
      "v_mfma_f32_16x16x32_bf16 v[220:223], a[164:167], v[76:79], v[220:223]\n"
      "ds_read_b64_tr_b16 v[114:115], v33 offset:26624\n"
      "v_fmac_f32 v185, v24, v229\n"
      "v_fmac_f32 v186, v24, v230\n"
      "v_mfma_f32_16x16x32_bf16 v[200:203], a[168:171], v[80:83], v[200:203]\n"
      "ds_read_b64_tr_b16 v[116:117], v35 offset:24576\n"
      "v_fmac_f32 v187, v24, v231\n"
      "v_fma_f32 v184, v184, v25, v240\n"
      "v_mfma_f32_16x16x32_bf16 v[208:211], a[172:175], v[80:83], v[208:211]\n"
      "global_load_dwordx4 v[152:155], v45, s[10:11] offset:1024 sc1\n"
      "v_fma_f32 v185, v185, v25, v241\n"
      "v_mfma_f32_16x16x32_bf16 v[216:219], a[176:179], v[80:83], v[216:219]\n"
      "ds_read_b64_tr_b16 v[118:119], v35 offset:26624\n"
      "v_fma_f32 v186, v186, v25, v242\n"
      "v_fma_f32 v187, v187, v25, v243\n"
      "v_mfma_f32_16x16x32_bf16 v[204:207], a[168:171], v[84:87], v[204:207]\n"
      "ds_read_b64_tr_b16 v[120:121], v33 offset:28672\n"
      "v_mul_f32 v184, v184, v186\n"
      "v_mul_f32 v185, v185, v187\n"
      "v_mfma_f32_16x16x32_bf16 v[212:215], a[172:175], v[84:87], v[212:215]\n"
      "global_load_dwordx4 v[156:159], v45, s[12:13] offset:1024 sc1\n"
      "ds_read_b64_tr_b16 v[122:123], v33 offset:30720\n"
      "v_cvt_pk_bf16_f32 v184, v184, v185\n"
      "v_mfma_f32_16x16x32_bf16 v[220:223], a[176:179], v[84:87], v[220:223]\n"
      "buffer_load_dword v38, v42, s[32:35], s63 offen offset:512 sc1\n"
      "buffer_load_dword v39, v42, s[32:35], s63 offen offset:768 sc1\n"
      "ds_write_b32 v46, v184 offset:4096\n"
      "ds_read_b64_tr_b16 v[124:125], v35 offset:28672\n"
      "v_fmac_f32 v192, v24, v232\n"
      "v_mfma_f32_16x16x32_bf16 v[200:203], a[180:183], v[88:91], v[200:203]\n"
      "ds_read_b64_tr_b16 v[126:127], v35 offset:30720\n"
      "v_fmac_f32 v193, v24, v233\n"
      "v_fmac_f32 v194, v24, v234\n"
      "v_add_u32 v45, 10240, v7\n"
      "v_mfma_f32_16x16x32_bf16 v[208:211], a[184:187], v[88:91], v[208:211]\n"
      "global_load_dwordx4 v[172:175], v45, s[10:11] offset:-4096 sc1\n"
      "v_fmac_f32 v195, v24, v235\n"
      "v_fma_f32 v192, v192, v25, v244\n"
      "v_mfma_f32_16x16x32_bf16 v[216:219], a[188:191], v[88:91], v[216:219]\n"
      "v_fma_f32 v193, v193, v25, v245\n"
      "v_fma_f32 v194, v194, v25, v246\n"
      "v_mfma_f32_16x16x32_bf16 v[204:207], a[180:183], v[92:95], v[204:207]\n"
      "v_fma_f32 v195, v195, v25, v247\n"
      "v_mul_f32 v192, v192, v194\n"
      "v_mfma_f32_16x16x32_bf16 v[212:215], a[184:187], v[92:95], v[212:215]\n"
      "global_load_dwordx4 v[160:163], v45, s[12:13] offset:-4096 sc1\n"
      "v_mul_f32 v193, v193, v195\n"
      "v_mfma_f32_16x16x32_bf16 v[220:223], a[188:191], v[92:95], v[220:223]\n"
      "v_cvt_pk_bf16_f32 v192, v192, v193\n"
      "s_waitcnt lgkmcnt(0)\n"
      "v_mfma_f32_16x16x32_bf16 v[200:203], a[192:195], v[96:99], v[200:203]\n"
      "ds_write_b32 v46, v192 offset:8192\n"
      "v_fmac_f32 v180, v26, v224\n"
      "v_fmac_f32 v181, v26, v225\n"
      "v_mfma_f32_16x16x32_bf16 v[208:211], a[196:199], v[96:99], v[208:211]\n"
      "global_load_dwordx4 v[164:167], v45, s[10:11] offset:-3072 sc1\n"
      "v_fmac_f32 v182, v26, v226\n"
      "v_mfma_f32_16x16x32_bf16 v[216:219], a[200:203], v[96:99], v[216:219]\n"
      "v_fmac_f32 v183, v26, v227\n"
      "v_fma_f32 v180, v180, v27, v236\n"
      "v_mfma_f32_16x16x32_bf16 v[204:207], a[192:195], v[100:103], v[204:207]\n"
      "v_fma_f32 v181, v181, v27, v237\n"
      "v_fma_f32 v182, v182, v27, v238\n"
      "v_mfma_f32_16x16x32_bf16 v[212:215], a[196:199], v[100:103], v[212:215]\n"
      "global_load_dwordx4 v[168:171], v45, s[12:13] offset:-3072 sc1\n"
      "v_fma_f32 v183, v183, v27, v239\n"
      "v_mfma_f32_16x16x32_bf16 v[220:223], a[200:203], v[100:103], v[220:223]\n"
      "v_mul_f32 v180, v180, v182\n"
      "v_mul_f32 v181, v181, v183\n"
      "v_mfma_f32_16x16x32_bf16 v[200:203], a[204:207], v[104:107], v[200:203]\n"
      "v_cvt_pk_bf16_f32 v180, v180, v181\n"
      "s_waitcnt vmcnt(45)\n"
      "v_mfma_f32_16x16x32_bf16 v[208:211], a[208:211], v[104:107], v[208:211]\n"
      "global_load_dwordx4 v[64:67], v45, s[10:11] offset:-2048 sc1\n"
      "ds_write_b32 v46, v180 offset:2048\n"
      "ds_read_b128 a[144:147], v57 offset:0\n"
      "v_fmac_f32 v188, v26, v228\n"
      "v_mfma_f32_16x16x32_bf16 v[216:219], a[212:215], v[104:107], v[216:219]\n"
      "ds_read_b128 a[148:151], v57 offset:1024\n"
      "v_fmac_f32 v189, v26, v229\n"
      "v_fmac_f32 v190, v26, v230\n"
      "v_mfma_f32_16x16x32_bf16 v[204:207], a[204:207], v[108:111], v[204:207]\n"
      "ds_read_b128 a[152:155], v57 offset:2048\n"
      "v_fmac_f32 v191, v26, v231\n"
      "v_fma_f32 v188, v188, v27, v240\n"
      "v_mfma_f32_16x16x32_bf16 v[212:215], a[208:211], v[108:111], v[212:215]\n"
      "global_load_dwordx4 v[68:71], v45, s[12:13] offset:-2048 sc1\n"
      "v_fma_f32 v189, v189, v27, v241\n"
      "v_mfma_f32_16x16x32_bf16 v[220:223], a[212:215], v[108:111], v[220:223]\n"
      "buffer_load_dword v60, v42, s[32:35], s63 offen offset:1024 sc1\n"
      "buffer_load_dword v61, v42, s[32:35], s63 offen offset:1280 sc1\n"
      "v_fma_f32 v190, v190, v27, v242\n"
      "v_fma_f32 v191, v191, v27, v243\n"
      "v_mfma_f32_16x16x32_bf16 v[200:203], a[216:219], v[112:115], v[200:203]\n"
      "ds_read_b128 a[156:159], v57 offset:3072\n"
      "v_mul_f32 v188, v188, v190\n"
      "v_mul_f32 v189, v189, v191\n"
      "v_mfma_f32_16x16x32_bf16 v[208:211], a[220:223], v[112:115], v[208:211]\n"
      "global_load_dwordx4 v[72:75], v45, s[10:11] offset:-1024 sc1\n"
      "v_cvt_pk_bf16_f32 v188, v188, v189\n"
      "v_mfma_f32_16x16x32_bf16 v[216:219], a[224:227], v[112:115], v[216:219]\n"
      "ds_write_b32 v46, v188 offset:6144\n"
      "ds_read_b128 a[160:163], v57 offset:4096\n"
      "v_fmac_f32 v196, v26, v232\n"
      "v_mfma_f32_16x16x32_bf16 v[204:207], a[216:219], v[116:119], v[204:207]\n"
      "ds_read_b128 a[164:167], v57 offset:5120\n"
      "s_waitcnt vmcnt(17)\n"
      "v_fmac_f32 v197, v26, v233\n"
      "v_fmac_f32 v198, v26, v234\n"
      "v_mfma_f32_16x16x32_bf16 v[212:215], a[220:223], v[116:119], v[212:215]\n"
      "global_load_dwordx4 v[76:79], v45, s[12:13] offset:-1024 sc1\n"
      "v_add3_u32 v2, v8, v9, s30\n"
      "v_add3_u32 v2, v2, v10, v11\n"
      "v_add3_u32 v2, v2, v16, v17\n"
      "v_fmac_f32 v199, v26, v235\n"
      "v_fma_f32 v196, v196, v27, v244\n"
      "v_mfma_f32_16x16x32_bf16 v[220:223], a[224:227], v[116:119], v[220:223]\n"
      "ds_read_b128 a[168:171], v57 offset:6144\n"
      "v_add3_u32 v2, v2, v18, v19\n"
      "v_add3_u32 v2, v2, v128, v129\n"
      "v_add3_u32 v2, v2, v130, v131\n"
      "v_fma_f32 v197, v197, v27, v245\n"
      "v_mfma_f32_16x16x32_bf16 v[200:203], a[228:231], v[120:123], v[200:203]\n"
      "ds_read_b128 a[172:175], v57 offset:7168\n"
      "v_xor_b32 v2, v2, v36\n"
      "v_add3_u32 v3, v12, v13, s30\n"
      "v_add3_u32 v3, v3, v14, v15\n"
      "v_fma_f32 v198, v198, v27, v246\n"
      "v_fma_f32 v199, v199, v27, v247\n"
      "v_mfma_f32_16x16x32_bf16 v[208:211], a[232:235], v[120:123], v[208:211]\n"
      "global_load_dwordx4 v[80:83], v45, s[10:11] offset:0 sc1\n"
      "v_add3_u32 v3, v3, v20, v21\n"
      "v_add3_u32 v3, v3, v22, v23\n"
      "v_add3_u32 v3, v3, v132, v133\n"
      "v_mul_f32 v196, v196, v198\n"
      "v_mul_f32 v197, v197, v199\n"
      "v_add_u32 v44, 61440, v7\n"
      "v_mfma_f32_16x16x32_bf16 v[216:219], a[236:239], v[120:123], v[216:219]\n"
      "global_load_dwordx4 a[212:215], v44, s[6:7] offset:-3072 sc1\n"
      "v_add3_u32 v3, v3, v134, v135\n"
      "v_xor_b32 v3, v3, v37\n"
      "v_or_b32 v2, v2, v3\n"
      "v_cvt_pk_bf16_f32 v196, v196, v197\n"
      "v_mfma_f32_16x16x32_bf16 v[204:207], a[228:231], v[124:127], v[204:207]\n"
      "ds_write_b32 v46, v196 offset:10240\n"
      "ds_read_b128 a[176:179], v57 offset:8192\n"
      "v_cmp_ne_u32 vcc, 0, v2\n"
      "v_mfma_f32_16x16x32_bf16 v[212:215], a[232:235], v[124:127], v[212:215]\n"
      "global_load_dwordx4 v[84:87], v45, s[12:13] offset:0 sc1\n"
      "v_mfma_f32_16x16x32_bf16 v[220:223], a[236:239], v[124:127], v[220:223]\n"
      "s_cbranch_vccnz L_shA0_%=\n"
      "L_bhA0_%=:\n"
      "s_waitcnt lgkmcnt(1)\n"
      "s_barrier\n"
      "v_mfma_f32_16x16x32_bf16 v[96:99], a[240:243], v[8:11], 0\n"
      "global_load_dwordx4 a[216:219], v44, s[6:7] offset:-2048 sc1\n"
      "global_load_dwordx4 a[220:223], v44, s[6:7] offset:-1024 sc1\n"
      "ds_read_b128 v[176:179], v47 offset:0\n"
      "v_mfma_f32_16x16x32_bf16 v[104:107], a[244:247], v[8:11], 0\n"
      "global_load_dwordx4 v[88:91], v45, s[10:11] offset:1024 sc1\n"
      "ds_read_b128 v[180:183], v47 offset:4096\n"
      "v_fmac_f32 v200, v28, v224\n"
      "v_mfma_f32_16x16x32_bf16 v[112:115], a[248:251], v[8:11], 0\n"
      "ds_read_b128 v[184:187], v47 offset:8192\n"
      "v_fmac_f32 v201, v28, v225\n"
      "v_fmac_f32 v202, v28, v226\n"
      "v_fmac_f32 v203, v28, v227\n"
      "v_fma_f32 v200, v200, v29, v236\n"
      "v_fma_f32 v201, v201, v29, v237\n"
      "v_fma_f32 v202, v202, v29, v238\n"
      "v_fma_f32 v203, v203, v29, v239\n"
      "v_mul_f32 v200, v200, v202\n"
      "v_mul_f32 v201, v201, v203\n"
      "v_cvt_pk_bf16_f32 v200, v200, v201\n"
      "v_mfma_f32_16x16x32_bf16 v[120:123], a[252:255], v[8:11], 0\n"
      "ds_write_b32 v46, v200 offset:1024\n"
      "v_add_u32 v43, s59, v7\n"
      "v_add_u32 v59, s59, v42\n"
      "v_fmac_f32 v208, v28, v228\n"
      "v_fmac_f32 v209, v28, v229\n"
      "v_fmac_f32 v210, v28, v230\n"
      "v_fmac_f32 v211, v28, v231\n"
      "v_fma_f32 v208, v208, v29, v240\n"
      "v_fma_f32 v209, v209, v29, v241\n"
      "v_fma_f32 v210, v210, v29, v242\n"
      "v_fma_f32 v211, v211, v29, v243\n"
      "v_mul_f32 v208, v208, v210\n"
      "v_mul_f32 v209, v209, v211\n"
      "v_cvt_pk_bf16_f32 v208, v208, v209\n"
      "v_mfma_f32_16x16x32_bf16 v[248:251], a[0:3], v[8:11], 0\n"
      "global_load_dwordx4 v[92:95], v45, s[12:13] offset:1024 sc1\n"
      "ds_write_b32 v46, v208 offset:5120\n"
      "v_fmac_f32 v216, v28, v232\n"
      "v_fmac_f32 v217, v28, v233\n"
      "v_mfma_f32_16x16x32_bf16 v[100:103], a[240:243], v[12:15], 0\n"
      "buffer_load_dword v62, v42, s[32:35], s63 offen offset:1536 sc1\n"
      "buffer_load_dword v63, v42, s[32:35], s63 offen offset:1792 sc1\n"
      "v_fmac_f32 v218, v28, v234\n"
      "v_fmac_f32 v219, v28, v235\n"
      "v_fma_f32 v216, v216, v29, v244\n"
      "v_fma_f32 v217, v217, v29, v245\n"
      "v_fma_f32 v218, v218, v29, v246\n"
      "v_fma_f32 v219, v219, v29, v247\n"
      "v_mul_f32 v216, v216, v218\n"
      "v_mul_f32 v217, v217, v219\n"
      "v_cvt_pk_bf16_f32 v216, v216, v217\n"
      "v_mfma_f32_16x16x32_bf16 v[108:111], a[244:247], v[12:15], 0\n"
      "ds_write_b32 v46, v216 offset:9216\n"
      "s_waitcnt lgkmcnt(3)\n"
      "v_fmac_f32 v204, v30, v224\n"
      "v_fmac_f32 v205, v30, v225\n"
      "v_fmac_f32 v206, v30, v226\n"
      "v_fmac_f32 v207, v30, v227\n"
      "v_fma_f32 v204, v204, v31, v236\n"
      "v_fma_f32 v205, v205, v31, v237\n"
      "v_fma_f32 v206, v206, v31, v238\n"
      "v_fma_f32 v207, v207, v31, v239\n"
      "v_mul_f32 v204, v204, v206\n"
      "v_mul_f32 v205, v205, v207\n"
      "v_cvt_pk_bf16_f32 v204, v204, v205\n"
      "v_mfma_f32_16x16x32_bf16 v[116:119], a[248:251], v[12:15], 0\n"
      "buffer_store_dwordx4 v[176:179], v43, s[32:35], s69 offen offset:0 sc1\n"
      "ds_write_b32 v46, v204 offset:3072\n"
      "v_add3_u32 v58, v176, v177, s30\n"
      "v_add3_u32 v58, v58, v178, v179\n"
      "v_mfma_f32_16x16x32_bf16 v[124:127], a[252:255], v[12:15], 0\n"
      "v_add3_u32 v58, v58, v180, v181\n"
      "v_fmac_f32 v212, v30, v228\n"
      "v_fmac_f32 v213, v30, v229\n"
      "v_fmac_f32 v214, v30, v230\n"
      "v_fmac_f32 v215, v30, v231\n"
      "v_fma_f32 v212, v212, v31, v240\n"
      "v_fma_f32 v213, v213, v31, v241\n"
      "v_fma_f32 v214, v214, v31, v242\n"
      "v_fma_f32 v215, v215, v31, v243\n"
      "v_mul_f32 v212, v212, v214\n"
      "v_mul_f32 v213, v213, v215\n"
      "v_cvt_pk_bf16_f32 v212, v212, v213\n"
      "v_mfma_f32_16x16x32_bf16 v[252:255], a[0:3], v[12:15], 0\n"
      "buffer_store_dwordx4 v[180:183], v43, s[32:35], s69 offen offset:1024 sc1\n"
      "ds_write_b32 v46, v212 offset:7168\n"
      "v_add3_u32 v58, v58, v182, v183\n"
      "v_fmac_f32 v220, v30, v232\n"
      "v_fmac_f32 v221, v30, v233\n"
      "v_fmac_f32 v222, v30, v234\n"
      "v_fmac_f32 v223, v30, v235\n"
      "v_mfma_f32_16x16x32_bf16 v[96:99], a[4:7], v[16:19], v[96:99]\n"
      "v_add3_u32 v58, v58, v184, v185\n"
      "v_add3_u32 v58, v58, v186, v187\n"
      "v_fma_f32 v220, v220, v31, v244\n"
      "v_fma_f32 v221, v221, v31, v245\n"
      "v_fma_f32 v222, v222, v31, v246\n"
      "v_fma_f32 v223, v223, v31, v247\n"
      "v_mul_f32 v220, v220, v222\n"
      "v_mul_f32 v221, v221, v223\n"
      "v_cvt_pk_bf16_f32 v220, v220, v221\n"
      "v_mfma_f32_16x16x32_bf16 v[104:107], a[8:11], v[16:19], v[104:107]\n"
      "buffer_store_dwordx4 v[184:187], v43, s[32:35], s69 offen offset:2048 sc1\n"
      "ds_write_b32 v46, v220 offset:11264\n"
      "v_add_u32 v57, s41, v7\n"
      "v_mfma_f32_16x16x32_bf16 v[112:115], a[12:15], v[16:19], v[112:115]\n"
      "ds_read_b128 a[180:183], v57 offset:0\n"
      "v_mfma_f32_16x16x32_bf16 v[120:123], a[16:19], v[16:19], v[120:123]\n"
      "buffer_store_dword v58, v59, s[32:35], s70 offen sc1\n"
      "v_mfma_f32_16x16x32_bf16 v[248:251], a[20:23], v[16:19], v[248:251]\n"
      "ds_read_b128 a[184:187], v57 offset:1024\n"
      "s_waitcnt lgkmcnt(2)\n"
      "s_barrier\n"
      "v_mfma_f32_16x16x32_bf16 v[100:103], a[4:7], v[20:23], v[100:103]\n"
      "ds_read_b128 v[200:203], v47 offset:0\n"
      "v_mfma_f32_16x16x32_bf16 v[108:111], a[8:11], v[20:23], v[108:111]\n"
      "ds_read_b128 v[204:207], v47 offset:4096\n"
      "v_mfma_f32_16x16x32_bf16 v[116:119], a[12:15], v[20:23], v[116:119]\n"
      "ds_read_b128 v[208:211], v47 offset:8192\n"
      "v_xor_b32 v43, 0x80000000, v43\n"
      "v_mfma_f32_16x16x32_bf16 v[124:127], a[16:19], v[20:23], v[124:127]\n"
      "ds_read_b128 a[188:191], v57 offset:2048\n"
      "v_xor_b32 v59, 0x80000000, v59\n"
      "v_mfma_f32_16x16x32_bf16 v[252:255], a[20:23], v[20:23], v[252:255]\n"
      "ds_read_b128 a[192:195], v57 offset:3072\n"
      "v_mfma_f32_16x16x32_bf16 v[96:99], a[24:27], v[128:131], v[96:99]\n"
      "ds_read_b128 a[196:199], v57 offset:4096\n"
      "v_mfma_f32_16x16x32_bf16 v[104:107], a[28:31], v[128:131], v[104:107]\n"
      "ds_read_b128 a[200:203], v57 offset:5120\n"
      "s_waitcnt vmcnt(23)\n"
      "s_waitcnt lgkmcnt(4)\n"
      "v_mfma_f32_16x16x32_bf16 v[112:115], a[32:35], v[128:131], v[112:115]\n"
      "buffer_store_dwordx4 v[200:203], v43, s[32:35], s69 offen offset:0 sc1\n"
      "v_add3_u32 v2, v136, v137, s30\n"
      "v_add3_u32 v2, v2, v138, v139\n"
      "v_add3_u32 v2, v2, v144, v145\n"
      "v_add3_u32 v58, v200, v201, s30\n"
      "v_mfma_f32_16x16x32_bf16 v[120:123], a[36:39], v[128:131], v[120:123]\n"
      "ds_read_b128 a[204:207], v57 offset:6144\n"
      "v_add3_u32 v2, v2, v146, v147\n"
      "v_add3_u32 v2, v2, v152, v153\n"
      "v_add3_u32 v2, v2, v154, v155\n"
      "v_add3_u32 v58, v58, v202, v203\n"
      "v_add3_u32 v58, v58, v204, v205\n"
      "v_mfma_f32_16x16x32_bf16 v[248:251], a[40:43], v[128:131], v[248:251]\n"
      "buffer_store_dwordx4 v[204:207], v43, s[32:35], s69 offen offset:1024 sc1\n"
      "v_xor_b32 v2, v2, v38\n"
      "v_add3_u32 v3, v140, v141, s30\n"
      "v_add3_u32 v3, v3, v142, v143\n"
      "v_add3_u32 v58, v58, v206, v207\n"
      "v_mfma_f32_16x16x32_bf16 v[100:103], a[24:27], v[132:135], v[100:103]\n"
      "ds_read_b128 a[208:211], v57 offset:7168\n"
      "v_add3_u32 v3, v3, v148, v149\n"
      "v_add3_u32 v3, v3, v150, v151\n"
      "v_add3_u32 v3, v3, v156, v157\n"
      "v_add3_u32 v58, v58, v208, v209\n"
      "v_add3_u32 v58, v58, v210, v211\n"
      "v_mfma_f32_16x16x32_bf16 v[108:111], a[28:31], v[132:135], v[108:111]\n"
      "buffer_store_dwordx4 v[208:211], v43, s[32:35], s69 offen offset:2048 sc1\n"
      "v_add3_u32 v3, v3, v158, v159\n"
      "v_xor_b32 v3, v3, v39\n"
      "v_or_b32 v2, v2, v3\n"
      "v_mfma_f32_16x16x32_bf16 v[116:119], a[32:35], v[132:135], v[116:119]\n"
      "v_cmp_ne_u32 vcc, 0, v2\n"
      "v_mfma_f32_16x16x32_bf16 v[124:127], a[36:39], v[132:135], v[124:127]\n"
      "buffer_store_dword v58, v59, s[32:35], s70 offen sc1\n"
      "v_mfma_f32_16x16x32_bf16 v[252:255], a[40:43], v[132:135], v[252:255]\n"
      "s_cbranch_vccnz L_shA1_%=\n"
      "L_bhA1_%=:\n"
      "v_mfma_f32_16x16x32_bf16 v[96:99], a[44:47], v[136:139], v[96:99]\n"
      "s_add_u32 s43, s73, s97\n"
      "v_add_u32 v56, s43, v7\n"
      "v_mfma_f32_16x16x32_bf16 v[104:107], a[48:51], v[136:139], v[104:107]\n"
      "s_and_b32 s52, s60, 1\n"
      "s_lshr_b32 s53, s60, 1\n"
      "v_mfma_f32_16x16x32_bf16 v[112:115], a[52:55], v[136:139], v[112:115]\n"
      "s_mul_i32 s54, s52, 20480\n"
      "s_lshl_b32 s53, s53, 11\n"
      "v_mfma_f32_16x16x32_bf16 v[120:123], a[56:59], v[136:139], v[120:123]\n"
      "s_add_u32 s54, s54, s53\n"
      "v_add_u32 v49, s54, v7\n"
      "v_mfma_f32_16x16x32_bf16 v[248:251], a[60:63], v[136:139], v[248:251]\n"
      "s_lshl_b32 s52, s52, 5\n"
      "v_xor_b32 v53, s52, v6\n"
      "v_mfma_f32_16x16x32_bf16 v[100:103], a[44:47], v[140:143], v[100:103]\n"
      "s_lshl_b32 s52, s53, 1\n"
      "s_add_u32 s52, s52, 110592\n"
      "v_mfma_f32_16x16x32_bf16 v[108:111], a[48:51], v[140:143], v[108:111]\n"
      "v_add_u32 v53, s52, v53\n"
      "s_lshr_b32 s53, s53, 4\n"
      "v_mfma_f32_16x16x32_bf16 v[116:119], a[52:55], v[140:143], v[116:119]\n"
      "s_add_u32 s53, s53, 163328\n"
      "v_lshrrev_b32 v48, 4, v7\n"
      "v_mfma_f32_16x16x32_bf16 v[124:127], a[56:59], v[140:143], v[124:127]\n"
      "v_and_b32 v50, 0x30, v48\n"
      "v_add_u32 v54, s53, v50\n"
      "v_mfma_f32_16x16x32_bf16 v[252:255], a[60:63], v[140:143], v[252:255]\n"
      "v_and_b32 v48, 15, v48\n"
      "v_lshrrev_b32 v50, 1, v50\n"
      "v_mfma_f32_16x16x32_bf16 v[96:99], a[64:67], v[144:147], v[96:99]\n"
      "v_mul_u32_u24 v48, 1280, v48\n"
      "v_add3_u32 v55, v48, v50, s74\n"
      "v_mfma_f32_16x16x32_bf16 v[104:107], a[68:71], v[144:147], v[104:107]\n"
      "v_mfma_f32_16x16x32_bf16 v[112:115], a[72:75], v[144:147], v[112:115]\n"
      "v_mfma_f32_16x16x32_bf16 v[120:123], a[76:79], v[144:147], v[120:123]\n"
      "v_mfma_f32_16x16x32_bf16 v[248:251], a[80:83], v[144:147], v[248:251]\n"
      "v_mfma_f32_16x16x32_bf16 v[100:103], a[64:67], v[148:151], v[100:103]\n"
      "v_mfma_f32_16x16x32_bf16 v[108:111], a[68:71], v[148:151], v[108:111]\n"
      "v_mfma_f32_16x16x32_bf16 v[116:119], a[72:75], v[148:151], v[116:119]\n"
      "v_mfma_f32_16x16x32_bf16 v[124:127], a[76:79], v[148:151], v[124:127]\n"
      "v_mfma_f32_16x16x32_bf16 v[252:255], a[80:83], v[148:151], v[252:255]\n"
      "v_mfma_f32_16x16x32_bf16 v[96:99], a[84:87], v[152:155], v[96:99]\n"
      "v_mfma_f32_16x16x32_bf16 v[104:107], a[88:91], v[152:155], v[104:107]\n"
      "s_waitcnt vmcnt(19)\n"
      "s_waitcnt lgkmcnt(0)\n"
      "v_mfma_f32_16x16x32_bf16 v[112:115], a[92:95], v[152:155], v[112:115]\n"
      "v_add3_u32 v2, v172, v173, s30\n"
      "v_add3_u32 v2, v2, v174, v175\n"
      "v_add3_u32 v2, v2, v164, v165\n"
      "v_mfma_f32_16x16x32_bf16 v[120:123], a[96:99], v[152:155], v[120:123]\n"
      "v_add3_u32 v2, v2, v166, v167\n"
      "v_add3_u32 v2, v2, v64, v65\n"
      "v_add3_u32 v2, v2, v66, v67\n"
      "v_mfma_f32_16x16x32_bf16 v[248:251], a[100:103], v[152:155], v[248:251]\n"
      "v_xor_b32 v2, v2, v60\n"
      "v_add3_u32 v3, v160, v161, s30\n"
      "v_add3_u32 v3, v3, v162, v163\n"
      "v_mfma_f32_16x16x32_bf16 v[100:103], a[84:87], v[156:159], v[100:103]\n"
      "v_add3_u32 v3, v3, v168, v169\n"
      "v_add3_u32 v3, v3, v170, v171\n"
      "v_add3_u32 v3, v3, v68, v69\n"
      "v_mfma_f32_16x16x32_bf16 v[108:111], a[88:91], v[156:159], v[108:111]\n"
      "v_add3_u32 v3, v3, v70, v71\n"
      "v_xor_b32 v3, v3, v61\n"
      "v_or_b32 v2, v2, v3\n"
      "v_mfma_f32_16x16x32_bf16 v[116:119], a[92:95], v[156:159], v[116:119]\n"
      "v_cmp_ne_u32 vcc, 0, v2\n"
      "v_mfma_f32_16x16x32_bf16 v[124:127], a[96:99], v[156:159], v[124:127]\n"
      "v_mfma_f32_16x16x32_bf16 v[252:255], a[100:103], v[156:159], v[252:255]\n"
      "s_cbranch_vccnz L_shA2_%=\n"
      "L_bhA2_%=:\n"
      "v_mfma_f32_16x16x32_bf16 v[96:99], a[104:107], v[172:175], v[96:99]\n"
      "v_mfma_f32_16x16x32_bf16 v[104:107], a[108:111], v[172:175], v[104:107]\n"
      "v_mfma_f32_16x16x32_bf16 v[112:115], a[112:115], v[172:175], v[112:115]\n"
      "v_mfma_f32_16x16x32_bf16 v[120:123], a[116:119], v[172:175], v[120:123]\n"
      "v_mfma_f32_16x16x32_bf16 v[248:251], a[120:123], v[172:175], v[248:251]\n"
      "v_mfma_f32_16x16x32_bf16 v[100:103], a[104:107], v[160:163], v[100:103]\n"
      "v_mfma_f32_16x16x32_bf16 v[108:111], a[108:111], v[160:163], v[108:111]\n"
      "v_mfma_f32_16x16x32_bf16 v[116:119], a[112:115], v[160:163], v[116:119]\n"
      "v_mfma_f32_16x16x32_bf16 v[124:127], a[116:119], v[160:163], v[124:127]\n"
      "v_mfma_f32_16x16x32_bf16 v[252:255], a[120:123], v[160:163], v[252:255]\n"
      "v_mfma_f32_16x16x32_bf16 v[96:99], a[124:127], v[164:167], v[96:99]\n"
      "v_add_u32 v45, 102400, v7\n"
      "v_mfma_f32_16x16x32_bf16 v[104:107], a[128:131], v[164:167], v[104:107]\n"
      "v_mfma_f32_16x16x32_bf16 v[112:115], a[132:135], v[164:167], v[112:115]\n"
      "global_load_dwordx4 v[224:227], v45, s[10:11] offset:-4096 sc1\n"
      "v_mfma_f32_16x16x32_bf16 v[120:123], a[136:139], v[164:167], v[120:123]\n"
      "v_mfma_f32_16x16x32_bf16 v[248:251], a[140:143], v[164:167], v[248:251]\n"
      "v_mfma_f32_16x16x32_bf16 v[100:103], a[124:127], v[168:171], v[100:103]\n"
      "v_mfma_f32_16x16x32_bf16 v[108:111], a[128:131], v[168:171], v[108:111]\n"
      "global_load_dwordx4 v[228:231], v45, s[10:11] offset:-3072 sc1\n"
      "v_mfma_f32_16x16x32_bf16 v[116:119], a[132:135], v[168:171], v[116:119]\n"
      "v_mfma_f32_16x16x32_bf16 v[124:127], a[136:139], v[168:171], v[124:127]\n"
      "v_mfma_f32_16x16x32_bf16 v[252:255], a[140:143], v[168:171], v[252:255]\n"
      "v_mfma_f32_16x16x32_bf16 v[96:99], a[144:147], v[64:67], v[96:99]\n"
      "global_load_dwordx4 v[232:235], v45, s[10:11] offset:-2048 sc1\n"
      "v_mfma_f32_16x16x32_bf16 v[104:107], a[148:151], v[64:67], v[104:107]\n"
      "s_waitcnt vmcnt(11)\n"
      "v_add3_u32 v2, v72, v73, s30\n"
      "v_mfma_f32_16x16x32_bf16 v[112:115], a[152:155], v[64:67], v[112:115]\n"
      "v_add3_u32 v2, v2, v74, v75\n"
      "v_add3_u32 v2, v2, v80, v81\n"
      "v_mfma_f32_16x16x32_bf16 v[120:123], a[156:159], v[64:67], v[120:123]\n"
      "global_load_dwordx4 v[236:239], v45, s[10:11] offset:-1024 sc1\n"
      "v_add3_u32 v2, v2, v82, v83\n"
      "v_add3_u32 v2, v2, v88, v89\n"
      "v_add3_u32 v2, v2, v90, v91\n"
      "v_mfma_f32_16x16x32_bf16 v[248:251], a[160:163], v[64:67], v[248:251]\n"
      "v_xor_b32 v2, v2, v62\n"
      "v_add3_u32 v3, v76, v77, s30\n"
      "v_add3_u32 v3, v3, v78, v79\n"
      "v_mfma_f32_16x16x32_bf16 v[100:103], a[144:147], v[68:71], v[100:103]\n"
      "v_add3_u32 v3, v3, v84, v85\n"
      "v_add3_u32 v3, v3, v86, v87\n"
      "v_add3_u32 v3, v3, v92, v93\n"
      "v_mfma_f32_16x16x32_bf16 v[108:111], a[148:151], v[68:71], v[108:111]\n"
      "global_load_dwordx4 v[240:243], v45, s[10:11] offset:0 sc1\n"
      "v_add3_u32 v3, v3, v94, v95\n"
      "v_xor_b32 v3, v3, v63\n"
      "v_or_b32 v2, v2, v3\n"
      "v_mfma_f32_16x16x32_bf16 v[116:119], a[152:155], v[68:71], v[116:119]\n"
      "v_cmp_ne_u32 vcc, 0, v2\n"
      "v_mfma_f32_16x16x32_bf16 v[124:127], a[156:159], v[68:71], v[124:127]\n"
      "v_mfma_f32_16x16x32_bf16 v[252:255], a[160:163], v[68:71], v[252:255]\n"
      "global_load_dwordx4 v[244:247], v45, s[10:11] offset:1024 sc1\n"
      "s_cbranch_vccnz L_shA3_%=\n"
      "L_bhA3_%=:\n"
      "v_mfma_f32_16x16x32_bf16 v[96:99], a[164:167], v[72:75], v[96:99]\n"
      "buffer_load_dword v32, v42, s[32:35], s63 offen offset:2048 sc1\n"
      "buffer_load_dword v33, v42, s[32:35], s63 offen offset:2304 sc1\n"
      "v_mfma_f32_16x16x32_bf16 v[104:107], a[168:171], v[72:75], v[104:107]\n"
      "v_mfma_f32_16x16x32_bf16 v[112:115], a[172:175], v[72:75], v[112:115]\n"
      "v_add_u32 v45, 108544, v7\n"
      "v_mfma_f32_16x16x32_bf16 v[120:123], a[176:179], v[72:75], v[120:123]\n"
      "global_load_dwordx4 v[8:11], v45, s[10:11] offset:-4096 sc1\n"
      "v_mfma_f32_16x16x32_bf16 v[248:251], a[180:183], v[72:75], v[248:251]\n"
      "v_mfma_f32_16x16x32_bf16 v[100:103], a[164:167], v[76:79], v[100:103]\n"
      "v_mfma_f32_16x16x32_bf16 v[108:111], a[168:171], v[76:79], v[108:111]\n"
      "global_load_dwordx4 v[12:15], v45, s[10:11] offset:-3072 sc1\n"
      "v_mfma_f32_16x16x32_bf16 v[116:119], a[172:175], v[76:79], v[116:119]\n"
      "v_mfma_f32_16x16x32_bf16 v[124:127], a[176:179], v[76:79], v[124:127]\n"
      "v_mfma_f32_16x16x32_bf16 v[252:255], a[180:183], v[76:79], v[252:255]\n"
      "global_load_dwordx4 v[16:19], v45, s[10:11] offset:-2048 sc1\n"
      "v_mfma_f32_16x16x32_bf16 v[96:99], a[184:187], v[80:83], v[96:99]\n"
      "v_mfma_f32_16x16x32_bf16 v[104:107], a[188:191], v[80:83], v[104:107]\n"
      "v_mfma_f32_16x16x32_bf16 v[112:115], a[192:195], v[80:83], v[112:115]\n"
      "global_load_dwordx4 v[20:23], v45, s[10:11] offset:-1024 sc1\n"
      "v_mfma_f32_16x16x32_bf16 v[120:123], a[196:199], v[80:83], v[120:123]\n"
      "v_mfma_f32_16x16x32_bf16 v[248:251], a[200:203], v[80:83], v[248:251]\n"
      "v_mfma_f32_16x16x32_bf16 v[100:103], a[184:187], v[84:87], v[100:103]\n"
      "global_load_dwordx4 v[128:131], v45, s[10:11] offset:0 sc1\n"
      "v_mfma_f32_16x16x32_bf16 v[108:111], a[188:191], v[84:87], v[108:111]\n"
      "v_mfma_f32_16x16x32_bf16 v[116:119], a[192:195], v[84:87], v[116:119]\n"
      "v_mfma_f32_16x16x32_bf16 v[124:127], a[196:199], v[84:87], v[124:127]\n"
      "global_load_dwordx4 v[132:135], v45, s[10:11] offset:1024 sc1\n"
      "v_mfma_f32_16x16x32_bf16 v[252:255], a[200:203], v[84:87], v[252:255]\n"
      "buffer_load_dword v34, v42, s[32:35], s63 offen offset:2560 sc1\n"
      "buffer_load_dword v35, v42, s[32:35], s63 offen offset:2816 sc1\n"
      "v_mfma_f32_16x16x32_bf16 v[96:99], a[204:207], v[88:91], v[96:99]\n"
      "v_mfma_f32_16x16x32_bf16 v[104:107], a[208:211], v[88:91], v[104:107]\n"
      "s_waitcnt vmcnt(8)\n"
      "v_add3_u32 v2, v224, v225, s30\n"
      "v_mfma_f32_16x16x32_bf16 v[112:115], a[212:215], v[88:91], v[112:115]\n"
      "v_add_u32 v45, 102400, v7\n"
      "v_add3_u32 v2, v2, v226, v227\n"
      "v_add3_u32 v2, v2, v228, v229\n"
      "v_mfma_f32_16x16x32_bf16 v[120:123], a[216:219], v[88:91], v[120:123]\n"
      "v_add3_u32 v2, v2, v230, v231\n"
      "v_add3_u32 v2, v2, v232, v233\n"
      "v_add3_u32 v2, v2, v234, v235\n"
      "v_mfma_f32_16x16x32_bf16 v[248:251], a[220:223], v[88:91], v[248:251]\n"
      "global_load_dwordx4 v[136:139], v45, s[12:13] offset:-4096 sc1\n"
      "v_xor_b32 v2, v2, v32\n"
      "v_add3_u32 v3, v236, v237, s30\n"
      "v_add3_u32 v3, v3, v238, v239\n"
      "v_mfma_f32_16x16x32_bf16 v[100:103], a[204:207], v[92:95], v[100:103]\n"
      "v_add3_u32 v3, v3, v240, v241\n"
      "v_add3_u32 v3, v3, v242, v243\n"
      "v_add3_u32 v3, v3, v244, v245\n"
      "v_mfma_f32_16x16x32_bf16 v[108:111], a[208:211], v[92:95], v[108:111]\n"
      "v_add3_u32 v3, v3, v246, v247\n"
      "v_xor_b32 v3, v3, v33\n"
      "v_or_b32 v2, v2, v3\n"
      "v_mfma_f32_16x16x32_bf16 v[116:119], a[212:215], v[92:95], v[116:119]\n"
      "v_cmp_ne_u32 vcc, 0, v2\n"
      "v_mfma_f32_16x16x32_bf16 v[124:127], a[216:219], v[92:95], v[124:127]\n"
      "global_load_dwordx4 v[140:143], v45, s[12:13] offset:-3072 sc1\n"
      "v_mfma_f32_16x16x32_bf16 v[252:255], a[220:223], v[92:95], v[252:255]\n"
      "s_cbranch_vccnz L_shB0_%=\n"
      "L_bhB0_%=:\n"
      "v_mfma_f32_16x16x32_bf16 v[176:179], a[240:243], v[224:227], 0\n"
      "ds_read_b128 v[80:83], v54 offset:0\n"
      "v_mfma_f32_16x16x32_bf16 v[180:183], a[244:247], v[224:227], 0\n"
      "ds_read_b128 v[84:87], v54 offset:64\n"
      "v_mfma_f32_16x16x32_bf16 v[184:187], a[248:251], v[224:227], 0\n"
      "global_load_dwordx4 v[144:147], v45, s[12:13] offset:-2048 sc1\n"
      "v_mfma_f32_16x16x32_bf16 v[188:191], a[252:255], v[224:227], 0\n"
      "ds_write_b128 v56, v[96:99] offset:0\n"
      "ds_read_b128 v[88:91], v54 offset:128\n"
      "v_mfma_f32_16x16x32_bf16 v[192:195], a[0:3], v[224:227], 0\n"
      "v_mfma_f32_16x16x32_bf16 v[176:179], a[4:7], v[228:231], v[176:179]\n"
      "global_load_dwordx4 v[148:151], v45, s[12:13] offset:-1024 sc1\n"
      "v_mfma_f32_16x16x32_bf16 v[180:183], a[8:11], v[228:231], v[180:183]\n"
      "ds_write_b128 v56, v[100:103] offset:20480\n"
      "v_mfma_f32_16x16x32_bf16 v[184:187], a[12:15], v[228:231], v[184:187]\n"
      "v_mfma_f32_16x16x32_bf16 v[188:191], a[16:19], v[228:231], v[188:191]\n"
      "global_load_dwordx4 v[152:155], v45, s[12:13] offset:0 sc1\n"
      "v_mfma_f32_16x16x32_bf16 v[192:195], a[20:23], v[228:231], v[192:195]\n"
      "ds_write_b128 v56, v[104:107] offset:1024\n"
      "v_mfma_f32_16x16x32_bf16 v[176:179], a[24:27], v[232:235], v[176:179]\n"
      "v_mfma_f32_16x16x32_bf16 v[180:183], a[28:31], v[232:235], v[180:183]\n"
      "global_load_dwordx4 v[156:159], v45, s[12:13] offset:1024 sc1\n"
      "v_mfma_f32_16x16x32_bf16 v[184:187], a[32:35], v[232:235], v[184:187]\n"
      "buffer_load_dword v24, v42, s[32:35], s63 offen offset:3072 sc1\n"
      "buffer_load_dword v25, v42, s[32:35], s63 offen offset:3328 sc1\n"
      "v_mfma_f32_16x16x32_bf16 v[188:191], a[36:39], v[232:235], v[188:191]\n"
      "ds_write_b128 v56, v[108:111] offset:21504\n"
      "v_mfma_f32_16x16x32_bf16 v[192:195], a[40:43], v[232:235], v[192:195]\n"
      "v_mfma_f32_16x16x32_bf16 v[176:179], a[44:47], v[236:239], v[176:179]\n"
      "v_add_u32 v45, 108544, v7\n"
      "v_mfma_f32_16x16x32_bf16 v[180:183], a[48:51], v[236:239], v[180:183]\n"
      "ds_write_b128 v56, v[112:115] offset:2048\n"
      "v_mfma_f32_16x16x32_bf16 v[184:187], a[52:55], v[236:239], v[184:187]\n"
      "global_load_dwordx4 v[172:175], v45, s[12:13] offset:-4096 sc1\n"
      "v_mfma_f32_16x16x32_bf16 v[188:191], a[56:59], v[236:239], v[188:191]\n"
      "v_mfma_f32_16x16x32_bf16 v[192:195], a[60:63], v[236:239], v[192:195]\n"
      "ds_write_b128 v56, v[116:119] offset:22528\n"
      "v_mfma_f32_16x16x32_bf16 v[176:179], a[64:67], v[240:243], v[176:179]\n"
      "v_mfma_f32_16x16x32_bf16 v[180:183], a[68:71], v[240:243], v[180:183]\n"
      "global_load_dwordx4 v[160:163], v45, s[12:13] offset:-3072 sc1\n"
      "s_waitcnt vmcnt(10)\n"
      "v_mfma_f32_16x16x32_bf16 v[184:187], a[72:75], v[240:243], v[184:187]\n"
      "ds_write_b128 v56, v[120:123] offset:3072\n"
      "v_add3_u32 v2, v8, v9, s30\n"
      "v_add3_u32 v2, v2, v10, v11\n"
      "v_add3_u32 v2, v2, v12, v13\n"
      "v_add3_u32 v2, v2, v14, v15\n"
      "v_mfma_f32_16x16x32_bf16 v[188:191], a[76:79], v[240:243], v[188:191]\n"
      "v_add3_u32 v2, v2, v16, v17\n"
      "v_add3_u32 v2, v2, v18, v19\n"
      "v_mfma_f32_16x16x32_bf16 v[192:195], a[80:83], v[240:243], v[192:195]\n"
      "v_xor_b32 v2, v2, v34\n"
      "v_add3_u32 v3, v20, v21, s30\n"
      "v_add3_u32 v3, v3, v22, v23\n"
      "v_mfma_f32_16x16x32_bf16 v[176:179], a[84:87], v[244:247], v[176:179]\n"
      "global_load_dwordx4 v[164:167], v45, s[12:13] offset:-2048 sc1\n"
      "v_add3_u32 v3, v3, v128, v129\n"
      "v_add3_u32 v3, v3, v130, v131\n"
      "v_add3_u32 v3, v3, v132, v133\n"
      "v_mfma_f32_16x16x32_bf16 v[180:183], a[88:91], v[244:247], v[180:183]\n"
      "ds_write_b128 v56, v[124:127] offset:23552\n"
      "v_add3_u32 v3, v3, v134, v135\n"
      "v_xor_b32 v3, v3, v35\n"
      "v_or_b32 v2, v2, v3\n"
      "v_cmp_ne_u32 vcc, 0, v2\n"
      "v_mfma_f32_16x16x32_bf16 v[184:187], a[92:95], v[244:247], v[184:187]\n"
      "v_mfma_f32_16x16x32_bf16 v[188:191], a[96:99], v[244:247], v[188:191]\n"
      "s_cbranch_vccnz L_shB1_%=\n"
      "L_bhB1_%=:\n"
      "v_mfma_f32_16x16x32_bf16 v[192:195], a[100:103], v[244:247], v[192:195]\n"
      "global_load_dwordx4 v[168:171], v45, s[12:13] offset:-1024 sc1\n"
      "v_mfma_f32_16x16x32_bf16 v[176:179], a[104:107], v[8:11], v[176:179]\n"
      "ds_write_b128 v56, v[248:251] offset:4096\n"
      "v_mfma_f32_16x16x32_bf16 v[180:183], a[108:111], v[8:11], v[180:183]\n"
      "v_mfma_f32_16x16x32_bf16 v[184:187], a[112:115], v[8:11], v[184:187]\n"
      "v_mfma_f32_16x16x32_bf16 v[188:191], a[116:119], v[8:11], v[188:191]\n"
      "global_load_dwordx4 v[64:67], v45, s[12:13] offset:0 sc1\n"
      "v_mfma_f32_16x16x32_bf16 v[192:195], a[120:123], v[8:11], v[192:195]\n"
      "ds_write_b128 v56, v[252:255] offset:24576\n"
      "v_mfma_f32_16x16x32_bf16 v[176:179], a[124:127], v[12:15], v[176:179]\n"
      "v_mfma_f32_16x16x32_bf16 v[180:183], a[128:131], v[12:15], v[180:183]\n"
      "v_mfma_f32_16x16x32_bf16 v[184:187], a[132:135], v[12:15], v[184:187]\n"
      "global_load_dwordx4 v[68:71], v45, s[12:13] offset:1024 sc1\n"
      "v_mfma_f32_16x16x32_bf16 v[188:191], a[136:139], v[12:15], v[188:191]\n"
      "v_mfma_f32_16x16x32_bf16 v[192:195], a[140:143], v[12:15], v[192:195]\n"
      "v_mfma_f32_16x16x32_bf16 v[176:179], a[144:147], v[16:19], v[176:179]\n"
      "s_waitcnt lgkmcnt(0)\n"
      "s_barrier\n"
      "v_mfma_f32_16x16x32_bf16 v[180:183], a[148:151], v[16:19], v[180:183]\n"
      "buffer_load_dword v26, v42, s[32:35], s63 offen offset:3584 sc1\n"
      "ds_read_b128 v[96:99], v49 offset:0\n"
      "v_add_u32 v56, 0xa000, v56\n"
      "v_mfma_f32_16x16x32_bf16 v[184:187], a[152:155], v[16:19], v[184:187]\n"
      "buffer_load_dword v27, v42, s[32:35], s63 offen offset:3840 sc1\n"
      "ds_read_b128 v[100:103], v49 offset:5120\n"
      "v_mfma_f32_16x16x32_bf16 v[188:191], a[156:159], v[16:19], v[188:191]\n"
      "ds_read_b128 v[104:107], v49 offset:10240\n"
      "v_mfma_f32_16x16x32_bf16 v[192:195], a[160:163], v[16:19], v[192:195]\n"
      "ds_read_b128 v[108:111], v49 offset:15360\n"
      "v_mfma_f32_16x16x32_bf16 v[176:179], a[164:167], v[20:23], v[176:179]\n"
      "ds_read_b64_tr_b16 v[72:73], v53 offset:0\n"
      "v_mfma_f32_16x16x32_bf16 v[180:183], a[168:171], v[20:23], v[180:183]\n"
      "ds_read_b64_tr_b16 v[74:75], v53 offset:2048\n"
      "v_mfma_f32_16x16x32_bf16 v[184:187], a[172:175], v[20:23], v[184:187]\n"
      "ds_read_b64_tr_b16 v[76:77], v53 offset:4096\n"
      "v_mfma_f32_16x16x32_bf16 v[188:191], a[176:179], v[20:23], v[188:191]\n"
      "ds_read_b128 v[112:115], v49 offset:1024\n"
      "v_mfma_f32_16x16x32_bf16 v[192:195], a[180:183], v[20:23], v[192:195]\n"
      "ds_read_b128 v[116:119], v49 offset:6144\n"
      "v_mfma_f32_16x16x32_bf16 v[176:179], a[184:187], v[128:131], v[176:179]\n"
      "ds_read_b128 v[120:123], v49 offset:11264\n"
      "v_mfma_f32_16x16x32_bf16 v[180:183], a[188:191], v[128:131], v[180:183]\n"
      "ds_read_b128 v[124:127], v49 offset:16384\n"
      "s_waitcnt vmcnt(8)\n"
      "v_mfma_f32_16x16x32_bf16 v[184:187], a[192:195], v[128:131], v[184:187]\n"
      "v_add3_u32 v2, v136, v137, s30\n"
      "v_add3_u32 v2, v2, v138, v139\n"
      "v_add3_u32 v2, v2, v140, v141\n"
      "s_waitcnt lgkmcnt(4)\n"
      "v_mfma_f32_16x16x32_bf16 v[188:191], a[196:199], v[128:131], v[188:191]\n"
      "v_add3_u32 v2, v2, v142, v143\n"
      "v_add3_u32 v2, v2, v144, v145\n"
      "v_add3_u32 v2, v2, v146, v147\n"
      "v_add_f32 v96, v96, v100\n"
      "v_add_f32 v97, v97, v101\n"
      "v_mfma_f32_16x16x32_bf16 v[192:195], a[200:203], v[128:131], v[192:195]\n"
      "v_xor_b32 v2, v2, v24\n"
      "v_add3_u32 v3, v148, v149, s30\n"
      "v_add3_u32 v3, v3, v150, v151\n"
      "v_add_f32 v98, v98, v102\n"
      "v_add_f32 v99, v99, v103\n"
      "v_mfma_f32_16x16x32_bf16 v[176:179], a[204:207], v[132:135], v[176:179]\n"
      "v_add3_u32 v3, v3, v152, v153\n"
      "v_add3_u32 v3, v3, v154, v155\n"
      "v_add3_u32 v3, v3, v156, v157\n"
      "v_add_f32 v104, v104, v108\n"
      "v_add_f32 v105, v105, v109\n"
      "v_mfma_f32_16x16x32_bf16 v[180:183], a[208:211], v[132:135], v[180:183]\n"
      "v_add3_u32 v3, v3, v158, v159\n"
      "v_xor_b32 v3, v3, v25\n"
      "v_or_b32 v2, v2, v3\n"
      "v_add_f32 v106, v106, v110\n"
      "v_add_f32 v107, v107, v111\n"
      "v_mfma_f32_16x16x32_bf16 v[184:187], a[212:215], v[132:135], v[184:187]\n"
      "v_cmp_ne_u32 vcc, 0, v2\n"
      "v_add_f32 v96, v96, v104\n"
      "v_add_f32 v97, v97, v105\n"
      "v_mfma_f32_16x16x32_bf16 v[188:191], a[216:219], v[132:135], v[188:191]\n"
      "v_add_f32 v98, v98, v106\n"
      "v_add_f32 v99, v99, v107\n"
      "v_mfma_f32_16x16x32_bf16 v[192:195], a[220:223], v[132:135], v[192:195]\n"
      "s_cbranch_vccnz L_shB2_%=\n"
      "L_bhB2_%=:\n"
      "v_cvt_pk_bf16_f32 v96, 0, v96\n"
      "v_mfma_f32_16x16x32_bf16 v[200:203], a[240:243], v[136:139], 0\n"
      "v_cvt_pk_bf16_f32 v97, 0, v97\n"
      "v_cvt_pk_bf16_f32 v98, 0, v98\n"
      "v_mfma_f32_16x16x32_bf16 v[204:207], a[244:247], v[136:139], 0\n"
      "v_cvt_pk_bf16_f32 v99, 0, v99\n"
      "v_mul_f32 v96, v96, v80\n"
      "v_mfma_f32_16x16x32_bf16 v[208:211], a[248:251], v[136:139], 0\n"
      "v_mul_f32 v97, v97, v81\n"
      "v_mul_f32 v98, v98, v82\n"
      "v_mfma_f32_16x16x32_bf16 v[212:215], a[252:255], v[136:139], 0\n"
      "v_mul_f32 v99, v99, v83\n"
      "v_cvt_pk_bf16_f32 v96, 0, v96\n"
      "v_mfma_f32_16x16x32_bf16 v[216:219], a[0:3], v[136:139], 0\n"
      "v_cvt_pk_bf16_f32 v97, 0, v97\n"
      "v_cvt_pk_bf16_f32 v98, 0, v98\n"
      "v_mfma_f32_16x16x32_bf16 v[200:203], a[4:7], v[140:143], v[200:203]\n"
      "v_cvt_pk_bf16_f32 v99, 0, v99\n"
      "v_lshlrev_b32 v100, 16, v72\n"
      "v_mfma_f32_16x16x32_bf16 v[204:207], a[8:11], v[140:143], v[204:207]\n"
      "v_and_b32 v101, 0xffff0000, v72\n"
      "v_lshlrev_b32 v102, 16, v73\n"
      "v_mfma_f32_16x16x32_bf16 v[208:211], a[12:15], v[140:143], v[208:211]\n"
      "v_and_b32 v103, 0xffff0000, v73\n"
      "v_add_f32 v96, v96, v100\n"
      "v_mfma_f32_16x16x32_bf16 v[212:215], a[16:19], v[140:143], v[212:215]\n"
      "v_add_f32 v97, v97, v101\n"
      "v_add_f32 v98, v98, v102\n"
      "v_mfma_f32_16x16x32_bf16 v[216:219], a[20:23], v[140:143], v[216:219]\n"
      "v_add_f32 v99, v99, v103\n"
      "v_cvt_pk_bf16_f32 v72, v96, v97\n"
      "v_mfma_f32_16x16x32_bf16 v[200:203], a[24:27], v[144:147], v[200:203]\n"
      "ds_write_b128 v56, v[176:179] offset:0\n"
      "v_cvt_pk_bf16_f32 v73, v98, v99\n"
      "v_mfma_f32_16x16x32_bf16 v[204:207], a[28:31], v[144:147], v[204:207]\n"
      "global_store_dwordx2 v55, v[72:73], s[28:29] offset:0\n"
      "s_waitcnt lgkmcnt(1)\n"
      "v_mfma_f32_16x16x32_bf16 v[208:211], a[32:35], v[144:147], v[208:211]\n"
      "ds_read_b128 v[96:99], v49 offset:2048\n"
      "v_add_f32 v112, v112, v116\n"
      "v_mfma_f32_16x16x32_bf16 v[212:215], a[36:39], v[144:147], v[212:215]\n"
      "ds_read_b128 v[100:103], v49 offset:7168\n"
      "v_add_f32 v113, v113, v117\n"
      "v_mfma_f32_16x16x32_bf16 v[216:219], a[40:43], v[144:147], v[216:219]\n"
      "ds_read_b128 v[104:107], v49 offset:12288\n"
      "v_add_f32 v114, v114, v118\n"
      "v_mfma_f32_16x16x32_bf16 v[200:203], a[44:47], v[148:151], v[200:203]\n"
      "ds_read_b128 v[108:111], v49 offset:17408\n"
      "v_add_f32 v115, v115, v119\n"
      "v_mfma_f32_16x16x32_bf16 v[204:207], a[48:51], v[148:151], v[204:207]\n"
      "v_add_f32 v120, v120, v124\n"
      "v_add_f32 v121, v121, v125\n"
      "v_mfma_f32_16x16x32_bf16 v[208:211], a[52:55], v[148:151], v[208:211]\n"
      "v_add_f32 v122, v122, v126\n"
      "v_add_f32 v123, v123, v127\n"
      "v_mfma_f32_16x16x32_bf16 v[212:215], a[56:59], v[148:151], v[212:215]\n"
      "v_add_f32 v112, v112, v120\n"
      "v_add_f32 v113, v113, v121\n"
      "v_mfma_f32_16x16x32_bf16 v[216:219], a[60:63], v[148:151], v[216:219]\n"
      "v_add_f32 v114, v114, v122\n"
      "v_add_f32 v115, v115, v123\n"
      "v_mfma_f32_16x16x32_bf16 v[200:203], a[64:67], v[152:155], v[200:203]\n"
      "v_cvt_pk_bf16_f32 v112, 0, v112\n"
      "v_cvt_pk_bf16_f32 v113, 0, v113\n"
      "v_mfma_f32_16x16x32_bf16 v[204:207], a[68:71], v[152:155], v[204:207]\n"
      "s_waitcnt vmcnt(1)\n"
      "v_cvt_pk_bf16_f32 v114, 0, v114\n"
      "v_mfma_f32_16x16x32_bf16 v[208:211], a[72:75], v[152:155], v[208:211]\n"
      "v_add3_u32 v2, v172, v173, s30\n"
      "v_add3_u32 v2, v2, v174, v175\n"
      "v_add3_u32 v2, v2, v160, v161\n"
      "v_cvt_pk_bf16_f32 v115, 0, v115\n"
      "v_mul_f32 v112, v112, v84\n"
      "v_mfma_f32_16x16x32_bf16 v[212:215], a[76:79], v[152:155], v[212:215]\n"
      "v_add3_u32 v2, v2, v162, v163\n"
      "v_add3_u32 v2, v2, v164, v165\n"
      "v_add3_u32 v2, v2, v166, v167\n"
      "v_mul_f32 v113, v113, v85\n"
      "v_mul_f32 v114, v114, v86\n"
      "v_mfma_f32_16x16x32_bf16 v[216:219], a[80:83], v[152:155], v[216:219]\n"
      "v_xor_b32 v2, v2, v26\n"
      "v_add3_u32 v3, v168, v169, s30\n"
      "v_add3_u32 v3, v3, v170, v171\n"
      "v_mul_f32 v115, v115, v87\n"
      "v_cvt_pk_bf16_f32 v112, 0, v112\n"
      "v_mfma_f32_16x16x32_bf16 v[200:203], a[84:87], v[156:159], v[200:203]\n"
      "ds_write_b128 v56, v[180:183] offset:1024\n"
      "v_add3_u32 v3, v3, v64, v65\n"
      "v_add3_u32 v3, v3, v66, v67\n"
      "v_add3_u32 v3, v3, v68, v69\n"
      "v_cvt_pk_bf16_f32 v113, 0, v113\n"
      "v_mfma_f32_16x16x32_bf16 v[204:207], a[88:91], v[156:159], v[204:207]\n"
      "v_add3_u32 v3, v3, v70, v71\n"
      "v_xor_b32 v3, v3, v27\n"
      "v_or_b32 v2, v2, v3\n"
      "v_cvt_pk_bf16_f32 v114, 0, v114\n"
      "v_cvt_pk_bf16_f32 v115, 0, v115\n"
      "v_mfma_f32_16x16x32_bf16 v[208:211], a[92:95], v[156:159], v[208:211]\n"
      "v_cmp_ne_u32 vcc, 0, v2\n"
      "v_lshlrev_b32 v116, 16, v74\n"
      "v_and_b32 v117, 0xffff0000, v74\n"
      "v_mfma_f32_16x16x32_bf16 v[212:215], a[96:99], v[156:159], v[212:215]\n"
      "v_lshlrev_b32 v118, 16, v75\n"
      "v_and_b32 v119, 0xffff0000, v75\n"
      "v_mfma_f32_16x16x32_bf16 v[216:219], a[100:103], v[156:159], v[216:219]\n"
      "ds_write_b128 v56, v[184:187] offset:2048\n"
      "s_cbranch_vccnz L_shB3_%=\n"
      "L_bhB3_%=:\n"
      "v_add_f32 v112, v112, v116\n"
      "v_add_f32 v113, v113, v117\n"
      "v_add_f32 v114, v114, v118\n"
      "v_mfma_f32_16x16x32_bf16 v[200:203], a[104:107], v[172:175], v[200:203]\n"
      "v_add_f32 v115, v115, v119\n"
      "v_cvt_pk_bf16_f32 v74, v112, v113\n"
      "v_mfma_f32_16x16x32_bf16 v[204:207], a[108:111], v[172:175], v[204:207]\n"
      "v_cvt_pk_bf16_f32 v75, v114, v115\n"
      "v_mfma_f32_16x16x32_bf16 v[208:211], a[112:115], v[172:175], v[208:211]\n"
      "global_store_dwordx2 v55, v[74:75], s[28:29] offset:32\n"
      "s_waitcnt lgkmcnt(2)\n"
      "v_mfma_f32_16x16x32_bf16 v[212:215], a[116:119], v[172:175], v[212:215]\n"
      "ds_write_b128 v56, v[188:191] offset:3072\n"
      "v_add_f32 v96, v96, v100\n"
      "v_add_f32 v97, v97, v101\n"
      "v_add_f32 v98, v98, v102\n"
      "v_add_f32 v99, v99, v103\n"
      "v_mfma_f32_16x16x32_bf16 v[216:219], a[120:123], v[172:175], v[216:219]\n"
      "v_add_f32 v104, v104, v108\n"
      "v_add_f32 v105, v105, v109\n"
      "v_mfma_f32_16x16x32_bf16 v[200:203], a[124:127], v[160:163], v[200:203]\n"
      "v_add_f32 v106, v106, v110\n"
      "v_add_f32 v107, v107, v111\n"
      "v_mfma_f32_16x16x32_bf16 v[204:207], a[128:131], v[160:163], v[204:207]\n"
      "v_add_f32 v96, v96, v104\n"
      "v_add_f32 v97, v97, v105\n"
      "v_mfma_f32_16x16x32_bf16 v[208:211], a[132:135], v[160:163], v[208:211]\n"
      "ds_write_b128 v56, v[192:195] offset:4096\n"
      "v_add_f32 v98, v98, v106\n"
      "v_add_f32 v99, v99, v107\n"
      "v_cvt_pk_bf16_f32 v96, 0, v96\n"
      "v_cvt_pk_bf16_f32 v97, 0, v97\n"
      "v_mfma_f32_16x16x32_bf16 v[212:215], a[136:139], v[160:163], v[212:215]\n"
      "v_cvt_pk_bf16_f32 v98, 0, v98\n"
      "v_cvt_pk_bf16_f32 v99, 0, v99\n"
      "v_mfma_f32_16x16x32_bf16 v[216:219], a[140:143], v[160:163], v[216:219]\n"
      "v_mul_f32 v96, v96, v88\n"
      "v_mul_f32 v97, v97, v89\n"
      "v_mfma_f32_16x16x32_bf16 v[200:203], a[144:147], v[164:167], v[200:203]\n"
      "v_mul_f32 v98, v98, v90\n"
      "v_mul_f32 v99, v99, v91\n"
      "v_mfma_f32_16x16x32_bf16 v[204:207], a[148:151], v[164:167], v[204:207]\n"
      "v_cvt_pk_bf16_f32 v96, 0, v96\n"
      "v_cvt_pk_bf16_f32 v97, 0, v97\n"
      "v_mfma_f32_16x16x32_bf16 v[208:211], a[152:155], v[164:167], v[208:211]\n"
      "v_cvt_pk_bf16_f32 v98, 0, v98\n"
      "v_cvt_pk_bf16_f32 v99, 0, v99\n"
      "v_mfma_f32_16x16x32_bf16 v[212:215], a[156:159], v[164:167], v[212:215]\n"
      "v_lshlrev_b32 v100, 16, v76\n"
      "v_and_b32 v101, 0xffff0000, v76\n"
      "v_mfma_f32_16x16x32_bf16 v[216:219], a[160:163], v[164:167], v[216:219]\n"
      "v_lshlrev_b32 v102, 16, v77\n"
      "v_and_b32 v103, 0xffff0000, v77\n"
      "v_mfma_f32_16x16x32_bf16 v[200:203], a[164:167], v[168:171], v[200:203]\n"
      "v_add_f32 v96, v96, v100\n"
      "v_add_f32 v97, v97, v101\n"
      "v_mfma_f32_16x16x32_bf16 v[200:203], a[184:187], v[64:67], v[200:203]\n"
      "v_add_f32 v98, v98, v102\n"
      "v_add_f32 v99, v99, v103\n"
      "v_mfma_f32_16x16x32_bf16 v[200:203], a[204:207], v[68:71], v[200:203]\n"
      "v_cvt_pk_bf16_f32 v76, v96, v97\n"
      "v_cvt_pk_bf16_f32 v77, v98, v99\n"
      "v_mfma_f32_16x16x32_bf16 v[204:207], a[168:171], v[168:171], v[204:207]\n"
      "global_store_dwordx2 v55, v[76:77], s[28:29] offset:64\n"
      "v_mfma_f32_16x16x32_bf16 v[204:207], a[188:191], v[64:67], v[204:207]\n"
      "v_mfma_f32_16x16x32_bf16 v[204:207], a[208:211], v[68:71], v[204:207]\n"
      "ds_write_b128 v56, v[200:203] offset:20480\n"
      "v_mfma_f32_16x16x32_bf16 v[208:211], a[172:175], v[168:171], v[208:211]\n"
      "v_mfma_f32_16x16x32_bf16 v[208:211], a[192:195], v[64:67], v[208:211]\n"
      "v_mfma_f32_16x16x32_bf16 v[208:211], a[212:215], v[68:71], v[208:211]\n"
      "ds_write_b128 v56, v[204:207] offset:21504\n"
      "v_mfma_f32_16x16x32_bf16 v[212:215], a[176:179], v[168:171], v[212:215]\n"
      "v_mfma_f32_16x16x32_bf16 v[212:215], a[196:199], v[64:67], v[212:215]\n"
      "v_mfma_f32_16x16x32_bf16 v[212:215], a[216:219], v[68:71], v[212:215]\n"
      "ds_write_b128 v56, v[208:211] offset:22528\n"
      "v_mfma_f32_16x16x32_bf16 v[216:219], a[180:183], v[168:171], v[216:219]\n"
      "v_mfma_f32_16x16x32_bf16 v[216:219], a[200:203], v[64:67], v[216:219]\n"
      "v_mfma_f32_16x16x32_bf16 v[216:219], a[220:223], v[68:71], v[216:219]\n"
      "ds_write_b128 v56, v[212:215] offset:23552\n"
      "v_xor_b32 v53, 64, v53\n"
      "ds_read_b64_tr_b16 v[64:65], v53 offset:0\n"
      "ds_read_b64_tr_b16 v[66:67], v53 offset:2048\n"
      "ds_read_b64_tr_b16 v[68:69], v53 offset:4096\n"
      "v_add_u32 v49, 0xa000, v49\n"
      "v_add_u32 v55, 81920, v55\n"
      "s_nop 0\n"
      "ds_write_b128 v56, v[216:219] offset:24576\n"
      "s_waitcnt lgkmcnt(0)\n"
      "s_barrier\n"
      "ds_read_b128 v[96:99], v49 offset:0\n"
      "ds_read_b128 v[100:103], v49 offset:5120\n"
      "ds_read_b128 v[104:107], v49 offset:10240\n"
      "ds_read_b128 v[108:111], v49 offset:15360\n"
      "ds_read_b128 v[112:115], v49 offset:1024\n"
      "ds_read_b128 v[116:119], v49 offset:6144\n"
      "ds_read_b128 v[120:123], v49 offset:11264\n"
      "ds_read_b128 v[124:127], v49 offset:16384\n"
      "ds_read_b128 v[128:131], v49 offset:2048\n"
      "ds_read_b128 v[132:135], v49 offset:7168\n"
      "ds_read_b128 v[136:139], v49 offset:12288\n"
      "ds_read_b128 v[140:143], v49 offset:17408\n"
      "v_lshlrev_b32 v144, 16, v64\n"
      "v_and_b32 v145, 0xffff0000, v64\n"
      "v_lshlrev_b32 v146, 16, v65\n"
      "v_and_b32 v147, 0xffff0000, v65\n"
      "v_lshlrev_b32 v148, 16, v66\n"
      "v_and_b32 v149, 0xffff0000, v66\n"
      "v_lshlrev_b32 v150, 16, v67\n"
      "v_and_b32 v151, 0xffff0000, v67\n"
      "v_lshlrev_b32 v152, 16, v68\n"
      "v_and_b32 v153, 0xffff0000, v68\n"
      "v_lshlrev_b32 v154, 16, v69\n"
      "v_and_b32 v155, 0xffff0000, v69\n"
      "s_waitcnt lgkmcnt(8)\n"
      "v_pk_add_f32 v[96:97], v[96:97], v[100:101]\n"
      "v_pk_add_f32 v[98:99], v[98:99], v[102:103]\n"
      "v_pk_add_f32 v[104:105], v[104:105], v[108:109]\n"
      "v_pk_add_f32 v[106:107], v[106:107], v[110:111]\n"
      "v_pk_add_f32 v[96:97], v[96:97], v[104:105]\n"
      "v_pk_add_f32 v[98:99], v[98:99], v[106:107]\n"
      "v_cvt_pk_bf16_f32 v96, 0, v96\n"
      "v_cvt_pk_bf16_f32 v97, 0, v97\n"
      "v_cvt_pk_bf16_f32 v98, 0, v98\n"
      "v_cvt_pk_bf16_f32 v99, 0, v99\n"
      "v_pk_mul_f32 v[96:97], v[96:97], v[80:81]\n"
      "v_pk_mul_f32 v[98:99], v[98:99], v[82:83]\n"
      "v_cvt_pk_bf16_f32 v96, 0, v96\n"
      "v_cvt_pk_bf16_f32 v97, 0, v97\n"
      "v_cvt_pk_bf16_f32 v98, 0, v98\n"
      "v_cvt_pk_bf16_f32 v99, 0, v99\n"
      "v_pk_add_f32 v[96:97], v[96:97], v[144:145]\n"
      "v_pk_add_f32 v[98:99], v[98:99], v[146:147]\n"
      "v_cvt_pk_bf16_f32 v96, v96, v97\n"
      "v_cvt_pk_bf16_f32 v97, v98, v99\n"
      "global_store_dwordx2 v55, v[96:97], s[28:29] offset:0\n"
      "s_waitcnt lgkmcnt(4)\n"
      "v_pk_add_f32 v[112:113], v[112:113], v[116:117]\n"
      "v_pk_add_f32 v[114:115], v[114:115], v[118:119]\n"
      "v_pk_add_f32 v[120:121], v[120:121], v[124:125]\n"
      "v_pk_add_f32 v[122:123], v[122:123], v[126:127]\n"
      "v_pk_add_f32 v[112:113], v[112:113], v[120:121]\n"
      "v_pk_add_f32 v[114:115], v[114:115], v[122:123]\n"
      "v_cvt_pk_bf16_f32 v112, 0, v112\n"
      "v_cvt_pk_bf16_f32 v113, 0, v113\n"
      "v_cvt_pk_bf16_f32 v114, 0, v114\n"
      "v_cvt_pk_bf16_f32 v115, 0, v115\n"
      "v_pk_mul_f32 v[112:113], v[112:113], v[84:85]\n"
      "v_pk_mul_f32 v[114:115], v[114:115], v[86:87]\n"
      "v_cvt_pk_bf16_f32 v112, 0, v112\n"
      "v_cvt_pk_bf16_f32 v113, 0, v113\n"
      "v_cvt_pk_bf16_f32 v114, 0, v114\n"
      "v_cvt_pk_bf16_f32 v115, 0, v115\n"
      "v_pk_add_f32 v[112:113], v[112:113], v[148:149]\n"
      "v_pk_add_f32 v[114:115], v[114:115], v[150:151]\n"
      "v_cvt_pk_bf16_f32 v112, v112, v113\n"
      "v_cvt_pk_bf16_f32 v113, v114, v115\n"
      "global_store_dwordx2 v55, v[112:113], s[28:29] offset:32\n"
      "s_waitcnt lgkmcnt(0)\n"
      "v_pk_add_f32 v[128:129], v[128:129], v[132:133]\n"
      "v_pk_add_f32 v[130:131], v[130:131], v[134:135]\n"
      "v_pk_add_f32 v[136:137], v[136:137], v[140:141]\n"
      "v_pk_add_f32 v[138:139], v[138:139], v[142:143]\n"
      "v_pk_add_f32 v[128:129], v[128:129], v[136:137]\n"
      "v_pk_add_f32 v[130:131], v[130:131], v[138:139]\n"
      "v_cvt_pk_bf16_f32 v128, 0, v128\n"
      "v_cvt_pk_bf16_f32 v129, 0, v129\n"
      "v_cvt_pk_bf16_f32 v130, 0, v130\n"
      "v_cvt_pk_bf16_f32 v131, 0, v131\n"
      "v_pk_mul_f32 v[128:129], v[128:129], v[88:89]\n"
      "v_pk_mul_f32 v[130:131], v[130:131], v[90:91]\n"
      "v_cvt_pk_bf16_f32 v128, 0, v128\n"
      "v_cvt_pk_bf16_f32 v129, 0, v129\n"
      "v_cvt_pk_bf16_f32 v130, 0, v130\n"
      "v_cvt_pk_bf16_f32 v131, 0, v131\n"
      "v_pk_add_f32 v[128:129], v[128:129], v[152:153]\n"
      "v_pk_add_f32 v[130:131], v[130:131], v[154:155]\n"
      "v_cvt_pk_bf16_f32 v128, v128, v129\n"
      "v_cvt_pk_bf16_f32 v129, v130, v131\n"
      "global_store_dwordx2 v55, v[128:129], s[28:29] offset:64\n"
      "s_or_b32 s40, s2, s60\n"
      "s_cmp_lg_u32 s40, 0\n"
      "s_cbranch_scc1 L_fin%=\n"
      "v_mov_b32 v8, s30\n"
      "v_mov_b32 v9, s31\n"
      "v_mov_b32 v10, 0\n"
      "s_mov_b64 exec, 1\n"
      "global_store_dwordx2 v10, v[8:9], s[26:27] sc1\n"
      "s_mov_b64 exec, -1\n"
      "L_fin%=:\n"
      "s_endpgm\n"
      "L_psx_%=:\n"
      "s_lshr_b32 s42, s92, 12\n"
      "s_lshr_b32 s43, s93, 12\n"
      "s_mulk_i32 s42, 4224\n"
      "s_mulk_i32 s43, 4224\n"
      "s_add_u32 s42, s42, 0x100000\n"
      "s_add_u32 s43, s43, 0x100000\n"
      "buffer_load_dwordx4 v[128:131], v54, s[32:35], s42 offen sc1\n"
      "s_add_u32 s42, s42, 1081344\n"
      "buffer_load_dwordx4 v[136:139], v54, s[32:35], s43 offen sc1\n"
      "buffer_load_dwordx4 v[132:135], v54, s[32:35], s42 offen sc1\n"
      "buffer_load_dword v140, v237, s[32:35], s70 offen sc1\n"
      "buffer_load_dword v141, v237, s[32:35], s98 offen sc1\n"
      "buffer_load_dword v142, v237, s[32:35], s71 offen sc1\n"
      "s_waitcnt vmcnt(0)\n"
      "v_add3_u32 v143, v128, v129, v130\n"
      "v_add3_u32 v143, v143, v131, v132\n"
      "v_add3_u32 v143, v143, v133, v134\n"
      "v_add3_u32 v143, v143, v135, v136\n"
      "v_add3_u32 v143, v143, v137, v138\n"
      "v_add_u32 v143, v143, v139\n"
      "v_add3_u32 v140, v140, v141, v142\n"
      "s_mul_i32 s47, s30, 3\n"
      "v_add_u32_dpp v143, v143, v143 quad_perm:[1,0,3,2] row_mask:0xf bank_mask:0xf\n"
      "s_nop 1\n"
      "v_add_u32_dpp v143, v143, v143 quad_perm:[2,3,0,1] row_mask:0xf bank_mask:0xf\n"
      "s_nop 1\n"
      "v_add_u32_dpp v143, v143, v143 row_half_mirror row_mask:0xf bank_mask:0xf\n"
      "v_add_u32 v143, v143, v140\n"
      "v_cmp_ne_u32 vcc, s47, v143\n"
      "s_sub_u32 s44, s44, 1\n"
      "s_cbranch_vccz L_pbx_%=\n"
      "s_cmp_lg_u32 s44, 0\n"
      "s_cbranch_scc1 L_psx_%=\n"
      "s_branch L_pbx_%=\n"
      "L_cv_s%=:\n"
      "buffer_load_dwordx2 v[200:201], v4, s[32:35], s80 offen offset:0 sc1\n"
      "buffer_load_dword v250, v61, s[32:35], s85 offen offset:0 sc1\n"
      "buffer_load_dwordx4 v[224:227], v57, s[32:35], s82 offen offset:0 sc1\n"
      "buffer_load_dwordx4 v[228:231], v57, s[32:35], s82 offen offset:1024 sc1\n"
      "buffer_load_dwordx4 v[192:195], v54, s[32:35], s53 offen offset:0 sc1\n"
      "buffer_load_dwordx4 v[196:199], v54, s[32:35], s54 offen offset:0 sc1\n"
      "buffer_load_dword v248, v60, s[32:35], s84 offen offset:0 sc1\n"
      "buffer_load_dword v249, v60, s[32:35], s84 offen offset:2048 sc1\n"
      "buffer_load_dwordx4 v[208:211], v55, s[32:35], 0 offen offset:0 sc1\n"
      "buffer_load_dwordx4 v[212:215], v55, s[32:35], 0 offen offset:1024 sc1\n"
      "buffer_load_dwordx4 v[216:219], v55, s[32:35], 0 offen offset:2048 sc1\n"
      "buffer_load_dwordx4 v[220:223], v55, s[32:35], 0 offen offset:3072 sc1\n"
      "buffer_load_dwordx2 v[204:205], v58, s[32:35], s83 offen offset:0 sc1\n"
      "s_waitcnt vmcnt(0)\n"
      "v_add_u32 v251, v200, v201\n"
      "v_add3_u32 v34, v192, v193, v194\n"
      "v_add3_u32 v34, v34, v195, v196\n"
      "v_add_u32_dpp v251, v251, v251 row_half_mirror row_mask:0xf bank_mask:0xf\n"
      "v_add3_u32 v34, v34, v197, v198\n"
      "v_add3_u32 v251, v251, v34, v199\n"
      "v_add3_u32 v34, v248, v249, v250\n"
      "v_add3_u32 v34, v34, v211, v215\n"
      "v_add_u32_dpp v251, v251, v251 quad_perm:[1,0,3,2] row_mask:0xf bank_mask:0xf\n"
      "v_add3_u32 v34, v34, v219, v223\n"
      "v_add3_u32 v34, v34, v227, v231\n"
      "v_add_u32_dpp v251, v251, v251 quad_perm:[2,3,0,1] row_mask:0xf bank_mask:0xf\n"
      "v_add3_u32 v251, v251, v34, v205\n"
      "v_cmp_ne_u32 vcc, s24, v251\n"
      "s_sub_u32 s44, s44, 1\n"
      "s_cbranch_vccz L_cv_r%=\n"
      "s_cmp_lg_u32 s44, 0\n"
      "s_cbranch_scc1 L_cv_s%=\n"
      "s_branch L_cv_r%=\n"
      "L_cw_s%=:\n"
      "buffer_load_dwordx2 v[252:253], v51, s[32:35], s76 offen offset:0 sc1\n"
      "s_waitcnt vmcnt(0)\n"
      "v_cmp_ne_u64 vcc, s[30:31], v[252:253]\n"
      "s_sub_u32 s44, s44, 1\n"
      "s_cbranch_vccz L_cw_r%=\n"
      "s_cmp_lg_u32 s44, 0\n"
      "s_cbranch_scc1 L_cw_s%=\n"
      "s_branch L_cw_r%=\n"
      "L_cm_s%=:\n"
      "buffer_load_dwordx2 v[254:255], v2, s[32:35], s45 offen sc1\n"
      "s_waitcnt vmcnt(0)\n"
      "v_cmp_ne_u32 vcc, s30, v255\n"
      "s_sub_u32 s44, s44, 1\n"
      "s_cbranch_vccz L_cm_r%=\n"
      "s_cmp_lg_u32 s44, 0\n"
      "s_cbranch_scc1 L_cm_s%=\n"
      "s_branch L_cm_r%=\n"
      "L_sx1_%=:\n"
      "s_mov_b32 s45, m0\n"
      "s_mov_b32 s58, s47\n"
      "s_add_u32 m0, s73, 16384\n"
      "v_add_u32 v248, 81920, v40\n"
      "global_load_lds_dwordx4 v248, s[4:5] offset:0 sc1\n"
      "global_load_lds_dwordx4 v248, s[4:5] offset:1024 sc1\n"
      "global_load_lds_dwordx4 v248, s[4:5] offset:2048 sc1\n"
      "global_load_lds_dwordx4 v248, s[4:5] offset:3072 sc1\n"
      "s_mov_b32 s47, 4\n"
      "buffer_load_dword v3, v5, s[32:35], s47 offen sc1\n"
      "s_waitcnt vmcnt(0)\n"
      "s_mov_b32 m0, s45\n"
      "s_mov_b32 s47, s58\n"
      "ds_read_b128 v[248:251], v40 offset:16384\n"
      "ds_read_b128 v[252:255], v40 offset:17408\n"
      "ds_read_b128 v[36:39], v40 offset:18432\n"
      "ds_read_b128 v[60:63], v40 offset:19456\n"
      "s_waitcnt lgkmcnt(0)\n"
      "v_add3_u32 v3, v3, v248, v249\n"
      "v_add3_u32 v3, v3, v250, v251\n"
      "v_add3_u32 v3, v3, v252, v253\n"
      "v_add3_u32 v3, v3, v254, v255\n"
      "v_add3_u32 v3, v3, v36, v37\n"
      "v_add3_u32 v3, v3, v38, v39\n"
      "v_add3_u32 v3, v3, v60, v61\n"
      "v_add3_u32 v3, v3, v62, v63\n"
      "s_nop 1\n"
      "v_add_u32_dpp v3, v3, v3 quad_perm:[1,0,3,2] row_mask:0xf bank_mask:0xf\n"
      "s_nop 1\n"
      "v_add_u32_dpp v3, v3, v3 quad_perm:[2,3,0,1] row_mask:0xf bank_mask:0xf\n"
      "s_nop 1\n"
      "v_add_u32_dpp v3, v3, v3 row_half_mirror row_mask:0xf bank_mask:0xf\n"
      "v_cmp_ne_u32 vcc, s71, v3\n"
      "s_sub_u32 s44, s44, 1\n"
      "s_cbranch_vccz L_bx1_%=\n"
      "s_cmp_lg_u32 s44, 0\n"
      "s_cbranch_scc1 L_sx1_%=\n"
      "s_branch L_bx1_%=\n"
      "L_sx2_%=:\n"
      "s_mov_b32 s45, m0\n"
      "s_mov_b32 s58, s47\n"
      "s_add_u32 m0, s73, 32768\n"
      "v_add_u32 v248, 16384, v40\n"
      "global_load_lds_dwordx4 v248, s[4:5] offset:0 sc1\n"
      "global_load_lds_dwordx4 v248, s[4:5] offset:1024 sc1\n"
      "global_load_lds_dwordx4 v248, s[4:5] offset:2048 sc1\n"
      "global_load_lds_dwordx4 v248, s[4:5] offset:3072 sc1\n"
      "s_mov_b32 s47, 32768\n"
      "buffer_load_dword v48, v5, s[32:35], s47 offen sc1\n"
      "s_waitcnt vmcnt(0)\n"
      "s_mov_b32 m0, s45\n"
      "s_mov_b32 s47, s58\n"
      "ds_read_b128 v[248:251], v40 offset:32768\n"
      "ds_read_b128 v[252:255], v40 offset:33792\n"
      "ds_read_b128 v[36:39], v40 offset:34816\n"
      "ds_read_b128 v[60:63], v40 offset:35840\n"
      "s_waitcnt lgkmcnt(0)\n"
      "v_add3_u32 v48, v48, v248, v249\n"
      "v_add3_u32 v48, v48, v250, v251\n"
      "v_add3_u32 v48, v48, v252, v253\n"
      "v_add3_u32 v48, v48, v254, v255\n"
      "v_add3_u32 v48, v48, v36, v37\n"
      "v_add3_u32 v48, v48, v38, v39\n"
      "v_add3_u32 v48, v48, v60, v61\n"
      "v_add3_u32 v48, v48, v62, v63\n"
      "s_nop 1\n"
      "v_add_u32_dpp v48, v48, v48 quad_perm:[1,0,3,2] row_mask:0xf bank_mask:0xf\n"
      "s_nop 1\n"
      "v_add_u32_dpp v48, v48, v48 quad_perm:[2,3,0,1] row_mask:0xf bank_mask:0xf\n"
      "s_nop 1\n"
      "v_add_u32_dpp v48, v48, v48 row_half_mirror row_mask:0xf bank_mask:0xf\n"
      "v_cmp_ne_u32 vcc, s71, v48\n"
      "s_sub_u32 s44, s44, 1\n"
      "s_cbranch_vccz L_bx2_%=\n"
      "s_cmp_lg_u32 s44, 0\n"
      "s_cbranch_scc1 L_sx2_%=\n"
      "s_branch L_bx2_%=\n"
      "L_sx3_%=:\n"
      "s_mov_b32 s45, m0\n"
      "s_mov_b32 s58, s47\n"
      "s_add_u32 m0, s73, 49152\n"
      "v_add_u32 v248, 32768, v40\n"
      "global_load_lds_dwordx4 v248, s[4:5] offset:0 sc1\n"
      "global_load_lds_dwordx4 v248, s[4:5] offset:1024 sc1\n"
      "global_load_lds_dwordx4 v248, s[4:5] offset:2048 sc1\n"
      "global_load_lds_dwordx4 v248, s[4:5] offset:3072 sc1\n"
      "s_mov_b32 s47, 65536\n"
      "buffer_load_dword v54, v5, s[32:35], s47 offen sc1\n"
      "s_waitcnt vmcnt(0)\n"
      "s_mov_b32 m0, s45\n"
      "s_mov_b32 s47, s58\n"
      "ds_read_b128 v[248:251], v40 offset:49152\n"
      "ds_read_b128 v[252:255], v40 offset:50176\n"
      "ds_read_b128 v[36:39], v40 offset:51200\n"
      "ds_read_b128 v[60:63], v40 offset:52224\n"
      "s_waitcnt lgkmcnt(0)\n"
      "v_add3_u32 v54, v54, v248, v249\n"
      "v_add3_u32 v54, v54, v250, v251\n"
      "v_add3_u32 v54, v54, v252, v253\n"
      "v_add3_u32 v54, v54, v254, v255\n"
      "v_add3_u32 v54, v54, v36, v37\n"
      "v_add3_u32 v54, v54, v38, v39\n"
      "v_add3_u32 v54, v54, v60, v61\n"
      "v_add3_u32 v54, v54, v62, v63\n"
      "s_nop 1\n"
      "v_add_u32_dpp v54, v54, v54 quad_perm:[1,0,3,2] row_mask:0xf bank_mask:0xf\n"
      "s_nop 1\n"
      "v_add_u32_dpp v54, v54, v54 quad_perm:[2,3,0,1] row_mask:0xf bank_mask:0xf\n"
      "s_nop 1\n"
      "v_add_u32_dpp v54, v54, v54 row_half_mirror row_mask:0xf bank_mask:0xf\n"
      "v_cmp_ne_u32 vcc, s71, v54\n"
      "s_sub_u32 s44, s44, 1\n"
      "s_cbranch_vccz L_bx3_%=\n"
      "s_cmp_lg_u32 s44, 0\n"
      "s_cbranch_scc1 L_sx3_%=\n"
      "s_branch L_bx3_%=\n"
      "L_sx4_%=:\n"
      "s_mov_b32 s45, m0\n"
      "s_mov_b32 s58, s47\n"
      "s_add_u32 m0, s73, 0\n"
      "v_add_u32 v248, 49152, v40\n"
      "global_load_lds_dwordx4 v248, s[4:5] offset:0 sc1\n"
      "global_load_lds_dwordx4 v248, s[4:5] offset:1024 sc1\n"
      "global_load_lds_dwordx4 v248, s[4:5] offset:2048 sc1\n"
      "global_load_lds_dwordx4 v248, s[4:5] offset:3072 sc1\n"
      "s_mov_b32 s47, 98304\n"
      "buffer_load_dword v56, v5, s[32:35], s47 offen sc1\n"
      "s_waitcnt vmcnt(0)\n"
      "s_mov_b32 m0, s45\n"
      "s_mov_b32 s47, s58\n"
      "ds_read_b128 v[248:251], v40 offset:0\n"
      "ds_read_b128 v[252:255], v40 offset:1024\n"
      "ds_read_b128 v[36:39], v40 offset:2048\n"
      "ds_read_b128 v[60:63], v40 offset:3072\n"
      "s_waitcnt lgkmcnt(0)\n"
      "v_add3_u32 v56, v56, v248, v249\n"
      "v_add3_u32 v56, v56, v250, v251\n"
      "v_add3_u32 v56, v56, v252, v253\n"
      "v_add3_u32 v56, v56, v254, v255\n"
      "v_add3_u32 v56, v56, v36, v37\n"
      "v_add3_u32 v56, v56, v38, v39\n"
      "v_add3_u32 v56, v56, v60, v61\n"
      "v_add3_u32 v56, v56, v62, v63\n"
      "s_nop 1\n"
      "v_add_u32_dpp v56, v56, v56 quad_perm:[1,0,3,2] row_mask:0xf bank_mask:0xf\n"
      "s_nop 1\n"
      "v_add_u32_dpp v56, v56, v56 quad_perm:[2,3,0,1] row_mask:0xf bank_mask:0xf\n"
      "s_nop 1\n"
      "v_add_u32_dpp v56, v56, v56 row_half_mirror row_mask:0xf bank_mask:0xf\n"
      "v_cmp_ne_u32 vcc, s71, v56\n"
      "s_sub_u32 s44, s44, 1\n"
      "s_cbranch_vccz L_bx4_%=\n"
      "s_cmp_lg_u32 s44, 0\n"
      "s_cbranch_scc1 L_sx4_%=\n"
      "s_branch L_bx4_%=\n"
      "L_sx5_%=:\n"
      "s_mov_b32 s45, m0\n"
      "s_mov_b32 s58, s47\n"
      "s_add_u32 m0, s73, 16384\n"
      "v_add_u32 v248, 65536, v40\n"
      "global_load_lds_dwordx4 v248, s[4:5] offset:0 sc1\n"
      "global_load_lds_dwordx4 v248, s[4:5] offset:1024 sc1\n"
      "global_load_lds_dwordx4 v248, s[4:5] offset:2048 sc1\n"
      "global_load_lds_dwordx4 v248, s[4:5] offset:3072 sc1\n"
      "s_mov_b32 s47, 131072\n"
      "buffer_load_dword v2, v5, s[32:35], s47 offen sc1\n"
      "s_waitcnt vmcnt(0)\n"
      "s_mov_b32 m0, s45\n"
      "s_mov_b32 s47, s58\n"
      "ds_read_b128 v[248:251], v40 offset:16384\n"
      "ds_read_b128 v[252:255], v40 offset:17408\n"
      "ds_read_b128 v[36:39], v40 offset:18432\n"
      "ds_read_b128 v[60:63], v40 offset:19456\n"
      "s_waitcnt lgkmcnt(0)\n"
      "v_add3_u32 v2, v2, v248, v249\n"
      "v_add3_u32 v2, v2, v250, v251\n"
      "v_add3_u32 v2, v2, v252, v253\n"
      "v_add3_u32 v2, v2, v254, v255\n"
      "v_add3_u32 v2, v2, v36, v37\n"
      "v_add3_u32 v2, v2, v38, v39\n"
      "v_add3_u32 v2, v2, v60, v61\n"
      "v_add3_u32 v2, v2, v62, v63\n"
      "s_nop 1\n"
      "v_add_u32_dpp v2, v2, v2 quad_perm:[1,0,3,2] row_mask:0xf bank_mask:0xf\n"
      "s_nop 1\n"
      "v_add_u32_dpp v2, v2, v2 quad_perm:[2,3,0,1] row_mask:0xf bank_mask:0xf\n"
      "s_nop 1\n"
      "v_add_u32_dpp v2, v2, v2 row_half_mirror row_mask:0xf bank_mask:0xf\n"
      "v_cmp_ne_u32 vcc, s71, v2\n"
      "s_sub_u32 s44, s44, 1\n"
      "s_cbranch_vccz L_bx5_%=\n"
      "s_cmp_lg_u32 s44, 0\n"
      "s_cbranch_scc1 L_sx5_%=\n"
      "s_branch L_bx5_%=\n"
      "L_sx6_%=:\n"
      "s_mov_b32 s45, m0\n"
      "s_mov_b32 s58, s47\n"
      "s_add_u32 m0, s73, 32768\n"
      "v_add_u32 v248, 98304, v40\n"
      "global_load_lds_dwordx4 v248, s[4:5] offset:0 sc1\n"
      "global_load_lds_dwordx4 v248, s[4:5] offset:1024 sc1\n"
      "global_load_lds_dwordx4 v248, s[4:5] offset:2048 sc1\n"
      "global_load_lds_dwordx4 v248, s[4:5] offset:3072 sc1\n"
      "s_mov_b32 s47, 32772\n"
      "buffer_load_dword v49, v5, s[32:35], s47 offen sc1\n"
      "s_waitcnt vmcnt(0)\n"
      "s_mov_b32 m0, s45\n"
      "s_mov_b32 s47, s58\n"
      "ds_read_b128 v[248:251], v40 offset:32768\n"
      "ds_read_b128 v[252:255], v40 offset:33792\n"
      "ds_read_b128 v[36:39], v40 offset:34816\n"
      "ds_read_b128 v[60:63], v40 offset:35840\n"
      "s_waitcnt lgkmcnt(0)\n"
      "v_add3_u32 v49, v49, v248, v249\n"
      "v_add3_u32 v49, v49, v250, v251\n"
      "v_add3_u32 v49, v49, v252, v253\n"
      "v_add3_u32 v49, v49, v254, v255\n"
      "v_add3_u32 v49, v49, v36, v37\n"
      "v_add3_u32 v49, v49, v38, v39\n"
      "v_add3_u32 v49, v49, v60, v61\n"
      "v_add3_u32 v49, v49, v62, v63\n"
      "s_nop 1\n"
      "v_add_u32_dpp v49, v49, v49 quad_perm:[1,0,3,2] row_mask:0xf bank_mask:0xf\n"
      "s_nop 1\n"
      "v_add_u32_dpp v49, v49, v49 quad_perm:[2,3,0,1] row_mask:0xf bank_mask:0xf\n"
      "s_nop 1\n"
      "v_add_u32_dpp v49, v49, v49 row_half_mirror row_mask:0xf bank_mask:0xf\n"
      "v_cmp_ne_u32 vcc, s71, v49\n"
      "s_sub_u32 s44, s44, 1\n"
      "s_cbranch_vccz L_bx6_%=\n"
      "s_cmp_lg_u32 s44, 0\n"
      "s_cbranch_scc1 L_sx6_%=\n"
      "s_branch L_bx6_%=\n"
      "L_sx7_%=:\n"
      "s_mov_b32 s45, m0\n"
      "s_mov_b32 s58, s47\n"
      "s_add_u32 m0, s73, 49152\n"
      "v_add_u32 v248, 114688, v40\n"
      "global_load_lds_dwordx4 v248, s[4:5] offset:0 sc1\n"
      "global_load_lds_dwordx4 v248, s[4:5] offset:1024 sc1\n"
      "global_load_lds_dwordx4 v248, s[4:5] offset:2048 sc1\n"
      "global_load_lds_dwordx4 v248, s[4:5] offset:3072 sc1\n"
      "s_mov_b32 s47, 65540\n"
      "buffer_load_dword v55, v5, s[32:35], s47 offen sc1\n"
      "s_waitcnt vmcnt(0)\n"
      "s_mov_b32 m0, s45\n"
      "s_mov_b32 s47, s58\n"
      "ds_read_b128 v[248:251], v40 offset:49152\n"
      "ds_read_b128 v[252:255], v40 offset:50176\n"
      "ds_read_b128 v[36:39], v40 offset:51200\n"
      "ds_read_b128 v[60:63], v40 offset:52224\n"
      "s_waitcnt lgkmcnt(0)\n"
      "v_add3_u32 v55, v55, v248, v249\n"
      "v_add3_u32 v55, v55, v250, v251\n"
      "v_add3_u32 v55, v55, v252, v253\n"
      "v_add3_u32 v55, v55, v254, v255\n"
      "v_add3_u32 v55, v55, v36, v37\n"
      "v_add3_u32 v55, v55, v38, v39\n"
      "v_add3_u32 v55, v55, v60, v61\n"
      "v_add3_u32 v55, v55, v62, v63\n"
      "s_nop 1\n"
      "v_add_u32_dpp v55, v55, v55 quad_perm:[1,0,3,2] row_mask:0xf bank_mask:0xf\n"
      "s_nop 1\n"
      "v_add_u32_dpp v55, v55, v55 quad_perm:[2,3,0,1] row_mask:0xf bank_mask:0xf\n"
      "s_nop 1\n"
      "v_add_u32_dpp v55, v55, v55 row_half_mirror row_mask:0xf bank_mask:0xf\n"
      "v_cmp_ne_u32 vcc, s71, v55\n"
      "s_sub_u32 s44, s44, 1\n"
      "s_cbranch_vccz L_bx7_%=\n"
      "s_cmp_lg_u32 s44, 0\n"
      "s_cbranch_scc1 L_sx7_%=\n"
      "s_branch L_bx7_%=\n"
      "L_sx8_%=:\n"
      "s_mov_b32 s45, m0\n"
      "s_mov_b32 s58, s47\n"
      "s_add_u32 m0, s73, 0\n"
      "v_add_u32 v248, 131072, v40\n"
      "global_load_lds_dwordx4 v248, s[4:5] offset:0 sc1\n"
      "global_load_lds_dwordx4 v248, s[4:5] offset:1024 sc1\n"
      "global_load_lds_dwordx4 v248, s[4:5] offset:2048 sc1\n"
      "global_load_lds_dwordx4 v248, s[4:5] offset:3072 sc1\n"
      "s_mov_b32 s47, 98308\n"
      "buffer_load_dword v57, v5, s[32:35], s47 offen sc1\n"
      "s_waitcnt vmcnt(0)\n"
      "s_mov_b32 m0, s45\n"
      "s_mov_b32 s47, s58\n"
      "ds_read_b128 v[248:251], v40 offset:0\n"
      "ds_read_b128 v[252:255], v40 offset:1024\n"
      "ds_read_b128 v[36:39], v40 offset:2048\n"
      "ds_read_b128 v[60:63], v40 offset:3072\n"
      "s_waitcnt lgkmcnt(0)\n"
      "v_add3_u32 v57, v57, v248, v249\n"
      "v_add3_u32 v57, v57, v250, v251\n"
      "v_add3_u32 v57, v57, v252, v253\n"
      "v_add3_u32 v57, v57, v254, v255\n"
      "v_add3_u32 v57, v57, v36, v37\n"
      "v_add3_u32 v57, v57, v38, v39\n"
      "v_add3_u32 v57, v57, v60, v61\n"
      "v_add3_u32 v57, v57, v62, v63\n"
      "s_nop 1\n"
      "v_add_u32_dpp v57, v57, v57 quad_perm:[1,0,3,2] row_mask:0xf bank_mask:0xf\n"
      "s_nop 1\n"
      "v_add_u32_dpp v57, v57, v57 quad_perm:[2,3,0,1] row_mask:0xf bank_mask:0xf\n"
      "s_nop 1\n"
      "v_add_u32_dpp v57, v57, v57 row_half_mirror row_mask:0xf bank_mask:0xf\n"
      "v_cmp_ne_u32 vcc, s71, v57\n"
      "s_sub_u32 s44, s44, 1\n"
      "s_cbranch_vccz L_bx8_%=\n"
      "s_cmp_lg_u32 s44, 0\n"
      "s_cbranch_scc1 L_sx8_%=\n"
      "s_branch L_bx8_%=\n"
      "L_sx9_%=:\n"
      "s_mov_b32 s45, m0\n"
      "s_mov_b32 s58, s47\n"
      "s_add_u32 m0, s73, 16384\n"
      "v_add_u32 v248, 147456, v40\n"
      "global_load_lds_dwordx4 v248, s[4:5] offset:0 sc1\n"
      "global_load_lds_dwordx4 v248, s[4:5] offset:1024 sc1\n"
      "global_load_lds_dwordx4 v248, s[4:5] offset:2048 sc1\n"
      "global_load_lds_dwordx4 v248, s[4:5] offset:3072 sc1\n"
      "s_mov_b32 s47, 131076\n"
      "buffer_load_dword v3, v5, s[32:35], s47 offen sc1\n"
      "s_waitcnt vmcnt(0)\n"
      "s_mov_b32 m0, s45\n"
      "s_mov_b32 s47, s58\n"
      "ds_read_b128 v[248:251], v40 offset:16384\n"
      "ds_read_b128 v[252:255], v40 offset:17408\n"
      "ds_read_b128 v[36:39], v40 offset:18432\n"
      "ds_read_b128 v[60:63], v40 offset:19456\n"
      "s_waitcnt lgkmcnt(0)\n"
      "v_add3_u32 v3, v3, v248, v249\n"
      "v_add3_u32 v3, v3, v250, v251\n"
      "v_add3_u32 v3, v3, v252, v253\n"
      "v_add3_u32 v3, v3, v254, v255\n"
      "v_add3_u32 v3, v3, v36, v37\n"
      "v_add3_u32 v3, v3, v38, v39\n"
      "v_add3_u32 v3, v3, v60, v61\n"
      "v_add3_u32 v3, v3, v62, v63\n"
      "s_nop 1\n"
      "v_add_u32_dpp v3, v3, v3 quad_perm:[1,0,3,2] row_mask:0xf bank_mask:0xf\n"
      "s_nop 1\n"
      "v_add_u32_dpp v3, v3, v3 quad_perm:[2,3,0,1] row_mask:0xf bank_mask:0xf\n"
      "s_nop 1\n"
      "v_add_u32_dpp v3, v3, v3 row_half_mirror row_mask:0xf bank_mask:0xf\n"
      "v_cmp_ne_u32 vcc, s71, v3\n"
      "s_sub_u32 s44, s44, 1\n"
      "s_cbranch_vccz L_bx9_%=\n"
      "s_cmp_lg_u32 s44, 0\n"
      "s_cbranch_scc1 L_sx9_%=\n"
      "s_branch L_bx9_%=\n"
      "L_sx0_%=:\n"
      "s_mov_b32 s45, m0\n"
      "s_mov_b32 s58, s47\n"
      "s_add_u32 m0, s73, 0\n"
      "v_add_u32 v248, 0, v40\n"
      "global_load_lds_dwordx4 v248, s[4:5] offset:0 sc1\n"
      "global_load_lds_dwordx4 v248, s[4:5] offset:1024 sc1\n"
      "global_load_lds_dwordx4 v248, s[4:5] offset:2048 sc1\n"
      "global_load_lds_dwordx4 v248, s[4:5] offset:3072 sc1\n"
      "s_mov_b32 s47, 0\n"
      "buffer_load_dword v2, v5, s[32:35], s47 offen sc1\n"
      "s_waitcnt vmcnt(0)\n"
      "s_mov_b32 m0, s45\n"
      "s_mov_b32 s47, s58\n"
      "ds_read_b128 v[248:251], v40 offset:0\n"
      "ds_read_b128 v[252:255], v40 offset:1024\n"
      "ds_read_b128 v[36:39], v40 offset:2048\n"
      "ds_read_b128 v[60:63], v40 offset:3072\n"
      "s_waitcnt lgkmcnt(0)\n"
      "v_add3_u32 v2, v2, v248, v249\n"
      "v_add3_u32 v2, v2, v250, v251\n"
      "v_add3_u32 v2, v2, v252, v253\n"
      "v_add3_u32 v2, v2, v254, v255\n"
      "v_add3_u32 v2, v2, v36, v37\n"
      "v_add3_u32 v2, v2, v38, v39\n"
      "v_add3_u32 v2, v2, v60, v61\n"
      "v_add3_u32 v2, v2, v62, v63\n"
      "s_nop 1\n"
      "v_add_u32_dpp v2, v2, v2 quad_perm:[1,0,3,2] row_mask:0xf bank_mask:0xf\n"
      "s_nop 1\n"
      "v_add_u32_dpp v2, v2, v2 quad_perm:[2,3,0,1] row_mask:0xf bank_mask:0xf\n"
      "s_nop 1\n"
      "v_add_u32_dpp v2, v2, v2 row_half_mirror row_mask:0xf bank_mask:0xf\n"
      "v_cmp_ne_u32 vcc, s71, v2\n"
      "s_sub_u32 s44, s44, 1\n"
      "s_cbranch_vccz L_bx0_%=\n"
      "s_cmp_lg_u32 s44, 0\n"
      "s_cbranch_scc1 L_sx0_%=\n"
      "s_branch L_bx0_%=\n"
      "L_sfd_%=:\n"
      "global_load_dwordx2 v[58:59], v42, s[26:27] sc1\n"
      "buffer_load_dwordx2 v[46:47], v51, s[32:35], s75 offen sc1\n"
      "s_waitcnt vmcnt(0)\n"
      "v_cmp_ne_u64 s[52:53], s[30:31], v[46:47]\n"
      "v_cmp_ne_u64 vcc, s[30:31], v[58:59]\n"
      "s_or_b64 vcc, vcc, s[52:53]\n"
      "s_sub_u32 s44, s44, 1\n"
      "s_cbranch_vccz L_bfd_%=\n"
      "s_cmp_lg_u32 s44, 0\n"
      "s_cbranch_scc1 L_sfd_%=\n"
      "s_branch L_bfd_%=\n"
      "L_sxp_%=:\n"
      "s_mov_b32 s45, m0\n"
      "s_mov_b32 s58, s47\n"
      "s_mov_b32 m0, s94\n"
      "s_nop 0\n"
      "buffer_load_dwordx4 v7, s[32:35], s67 offen offset:0 sc1 lds\n"
      "buffer_load_dwordx4 v7, s[32:35], s67 offen offset:1024 sc1 lds\n"
      "buffer_load_dwordx4 v7, s[32:35], s67 offen offset:2048 sc1 lds\n"
      "buffer_load_dword v32, v31, s[32:35], s68 offen sc1\n"
      "s_waitcnt vmcnt(0)\n"
      "s_mov_b32 m0, s45\n"
      "s_mov_b32 s47, s58\n"
      "ds_read_b128 v[248:251], v50 offset:0\n"
      "ds_read_b128 v[252:255], v50 offset:1024\n"
      "ds_read_b128 v[36:39], v50 offset:2048\n"
      "s_waitcnt lgkmcnt(0)\n"
      "v_add3_u32 v32, v32, v248, v249\n"
      "v_add3_u32 v32, v32, v250, v251\n"
      "v_add3_u32 v32, v32, v252, v253\n"
      "v_add3_u32 v32, v32, v254, v255\n"
      "v_add3_u32 v32, v32, v36, v37\n"
      "v_add3_u32 v32, v32, v38, v39\n"
      "s_nop 1\n"
      "v_add_u32_dpp v32, v32, v32 quad_perm:[1,0,3,2] row_mask:0xf bank_mask:0xf\n"
      "s_nop 1\n"
      "v_add_u32_dpp v32, v32, v32 quad_perm:[2,3,0,1] row_mask:0xf bank_mask:0xf\n"
      "s_nop 1\n"
      "v_add_u32_dpp v32, v32, v32 row_half_mirror row_mask:0xf bank_mask:0xf\n"
      "v_cmp_ne_u32 vcc, s72, v32\n"
      "s_sub_u32 s44, s44, 1\n"
      "s_cbranch_vccz L_bxp_%=\n"
      "s_cmp_lg_u32 s44, 0\n"
      "s_cbranch_scc1 L_sxp_%=\n"
      "s_branch L_bxp_%=\n"
      "L_sg_%=:\n"
      "buffer_load_dwordx4 v[8:11], v50, s[32:35], s93 offen offset:0 sc1\n"
      "buffer_load_dwordx4 v[12:15], v50, s[32:35], s93 offen offset:1024 sc1\n"
      "buffer_load_dwordx4 v[16:19], v50, s[32:35], s93 offen offset:2048 sc1\n"
      "buffer_load_dwordx4 v[20:23], v50, s[32:35], s93 offen offset:3072 sc1\n"
      "s_waitcnt vmcnt(0)\n"
      "v_add3_u32 v10, v10, v14, v18\n"
      "v_add_u32 v10, v10, v22\n"
      "v_cmp_ne_u32 vcc, s71, v10\n"
      "s_sub_u32 s44, s44, 1\n"
      "s_cbranch_vccz L_bg_%=\n"
      "s_cmp_lg_u32 s44, 0\n"
      "s_cbranch_scc1 L_sg_%=\n"
      "s_branch L_bg_%=\n"
      "L_shA0_%=:\n"
      "v_add_u32 v2, 4096, v7\n"
      "global_load_dwordx4 v[8:11], v2, s[10:11] offset:-4096 sc1\n"
      "global_load_dwordx4 v[16:19], v2, s[10:11] offset:-3072 sc1\n"
      "global_load_dwordx4 v[128:131], v2, s[10:11] offset:-2048 sc1\n"
      "v_add_u32 v2, 4096, v7\n"
      "global_load_dwordx4 v[12:15], v2, s[12:13] offset:-4096 sc1\n"
      "global_load_dwordx4 v[20:23], v2, s[12:13] offset:-3072 sc1\n"
      "global_load_dwordx4 v[132:135], v2, s[12:13] offset:-2048 sc1\n"
      "buffer_load_dword v36, v42, s[32:35], s63 offen offset:0 sc1\n"
      "buffer_load_dword v37, v42, s[32:35], s63 offen offset:256 sc1\n"
      "s_waitcnt vmcnt(0)\n"
      "v_add3_u32 v2, v8, v9, s30\n"
      "v_add3_u32 v2, v2, v10, v11\n"
      "v_add3_u32 v2, v2, v16, v17\n"
      "v_add3_u32 v2, v2, v18, v19\n"
      "v_add3_u32 v2, v2, v128, v129\n"
      "v_add3_u32 v2, v2, v130, v131\n"
      "v_xor_b32 v2, v2, v36\n"
      "v_add3_u32 v3, v12, v13, s30\n"
      "v_add3_u32 v3, v3, v14, v15\n"
      "v_add3_u32 v3, v3, v20, v21\n"
      "v_add3_u32 v3, v3, v22, v23\n"
      "v_add3_u32 v3, v3, v132, v133\n"
      "v_add3_u32 v3, v3, v134, v135\n"
      "v_xor_b32 v3, v3, v37\n"
      "v_or_b32 v2, v2, v3\n"
      "v_cmp_ne_u32 vcc, 0, v2\n"
      "s_sub_u32 s44, s44, 1\n"
      "s_cbranch_vccz L_bhA0_%=\n"
      "s_cmp_lg_u32 s44, 0\n"
      "s_cbranch_scc1 L_shA0_%=\n"
      "s_branch L_bhA0_%=\n"
      "L_shA1_%=:\n"
      "v_add_u32 v2, 4096, v7\n"
      "global_load_dwordx4 v[136:139], v2, s[10:11] offset:-1024 sc1\n"
      "global_load_dwordx4 v[144:147], v2, s[10:11] offset:0 sc1\n"
      "global_load_dwordx4 v[152:155], v2, s[10:11] offset:1024 sc1\n"
      "v_add_u32 v2, 4096, v7\n"
      "global_load_dwordx4 v[140:143], v2, s[12:13] offset:-1024 sc1\n"
      "global_load_dwordx4 v[148:151], v2, s[12:13] offset:0 sc1\n"
      "global_load_dwordx4 v[156:159], v2, s[12:13] offset:1024 sc1\n"
      "buffer_load_dword v38, v42, s[32:35], s63 offen offset:512 sc1\n"
      "buffer_load_dword v39, v42, s[32:35], s63 offen offset:768 sc1\n"
      "s_waitcnt vmcnt(0)\n"
      "v_add3_u32 v2, v136, v137, s30\n"
      "v_add3_u32 v2, v2, v138, v139\n"
      "v_add3_u32 v2, v2, v144, v145\n"
      "v_add3_u32 v2, v2, v146, v147\n"
      "v_add3_u32 v2, v2, v152, v153\n"
      "v_add3_u32 v2, v2, v154, v155\n"
      "v_xor_b32 v2, v2, v38\n"
      "v_add3_u32 v3, v140, v141, s30\n"
      "v_add3_u32 v3, v3, v142, v143\n"
      "v_add3_u32 v3, v3, v148, v149\n"
      "v_add3_u32 v3, v3, v150, v151\n"
      "v_add3_u32 v3, v3, v156, v157\n"
      "v_add3_u32 v3, v3, v158, v159\n"
      "v_xor_b32 v3, v3, v39\n"
      "v_or_b32 v2, v2, v3\n"
      "v_cmp_ne_u32 vcc, 0, v2\n"
      "s_sub_u32 s44, s44, 1\n"
      "s_cbranch_vccz L_bhA1_%=\n"
      "s_cmp_lg_u32 s44, 0\n"
      "s_cbranch_scc1 L_shA1_%=\n"
      "s_branch L_bhA1_%=\n"
      "L_shA2_%=:\n"
      "v_add_u32 v2, 10240, v7\n"
      "global_load_dwordx4 v[172:175], v2, s[10:11] offset:-4096 sc1\n"
      "global_load_dwordx4 v[164:167], v2, s[10:11] offset:-3072 sc1\n"
      "global_load_dwordx4 v[64:67], v2, s[10:11] offset:-2048 sc1\n"
      "v_add_u32 v2, 10240, v7\n"
      "global_load_dwordx4 v[160:163], v2, s[12:13] offset:-4096 sc1\n"
      "global_load_dwordx4 v[168:171], v2, s[12:13] offset:-3072 sc1\n"
      "global_load_dwordx4 v[68:71], v2, s[12:13] offset:-2048 sc1\n"
      "buffer_load_dword v60, v42, s[32:35], s63 offen offset:1024 sc1\n"
      "buffer_load_dword v61, v42, s[32:35], s63 offen offset:1280 sc1\n"
      "s_waitcnt vmcnt(0)\n"
      "v_add3_u32 v2, v172, v173, s30\n"
      "v_add3_u32 v2, v2, v174, v175\n"
      "v_add3_u32 v2, v2, v164, v165\n"
      "v_add3_u32 v2, v2, v166, v167\n"
      "v_add3_u32 v2, v2, v64, v65\n"
      "v_add3_u32 v2, v2, v66, v67\n"
      "v_xor_b32 v2, v2, v60\n"
      "v_add3_u32 v3, v160, v161, s30\n"
      "v_add3_u32 v3, v3, v162, v163\n"
      "v_add3_u32 v3, v3, v168, v169\n"
      "v_add3_u32 v3, v3, v170, v171\n"
      "v_add3_u32 v3, v3, v68, v69\n"
      "v_add3_u32 v3, v3, v70, v71\n"
      "v_xor_b32 v3, v3, v61\n"
      "v_or_b32 v2, v2, v3\n"
      "v_cmp_ne_u32 vcc, 0, v2\n"
      "s_sub_u32 s44, s44, 1\n"
      "s_cbranch_vccz L_bhA2_%=\n"
      "s_cmp_lg_u32 s44, 0\n"
      "s_cbranch_scc1 L_shA2_%=\n"
      "s_branch L_bhA2_%=\n"
      "L_shA3_%=:\n"
      "v_add_u32 v2, 10240, v7\n"
      "global_load_dwordx4 v[72:75], v2, s[10:11] offset:-1024 sc1\n"
      "global_load_dwordx4 v[80:83], v2, s[10:11] offset:0 sc1\n"
      "global_load_dwordx4 v[88:91], v2, s[10:11] offset:1024 sc1\n"
      "v_add_u32 v2, 10240, v7\n"
      "global_load_dwordx4 v[76:79], v2, s[12:13] offset:-1024 sc1\n"
      "global_load_dwordx4 v[84:87], v2, s[12:13] offset:0 sc1\n"
      "global_load_dwordx4 v[92:95], v2, s[12:13] offset:1024 sc1\n"
      "buffer_load_dword v62, v42, s[32:35], s63 offen offset:1536 sc1\n"
      "buffer_load_dword v63, v42, s[32:35], s63 offen offset:1792 sc1\n"
      "s_waitcnt vmcnt(0)\n"
      "v_add3_u32 v2, v72, v73, s30\n"
      "v_add3_u32 v2, v2, v74, v75\n"
      "v_add3_u32 v2, v2, v80, v81\n"
      "v_add3_u32 v2, v2, v82, v83\n"
      "v_add3_u32 v2, v2, v88, v89\n"
      "v_add3_u32 v2, v2, v90, v91\n"
      "v_xor_b32 v2, v2, v62\n"
      "v_add3_u32 v3, v76, v77, s30\n"
      "v_add3_u32 v3, v3, v78, v79\n"
      "v_add3_u32 v3, v3, v84, v85\n"
      "v_add3_u32 v3, v3, v86, v87\n"
      "v_add3_u32 v3, v3, v92, v93\n"
      "v_add3_u32 v3, v3, v94, v95\n"
      "v_xor_b32 v3, v3, v63\n"
      "v_or_b32 v2, v2, v3\n"
      "v_cmp_ne_u32 vcc, 0, v2\n"
      "s_sub_u32 s44, s44, 1\n"
      "s_cbranch_vccz L_bhA3_%=\n"
      "s_cmp_lg_u32 s44, 0\n"
      "s_cbranch_scc1 L_shA3_%=\n"
      "s_branch L_bhA3_%=\n"
      "L_shB0_%=:\n"
      "v_add_u32 v2, 102400, v7\n"
      "global_load_dwordx4 v[224:227], v2, s[10:11] offset:-4096 sc1\n"
      "global_load_dwordx4 v[228:231], v2, s[10:11] offset:-3072 sc1\n"
      "global_load_dwordx4 v[232:235], v2, s[10:11] offset:-2048 sc1\n"
      "v_add_u32 v2, 102400, v7\n"
      "global_load_dwordx4 v[236:239], v2, s[10:11] offset:-1024 sc1\n"
      "global_load_dwordx4 v[240:243], v2, s[10:11] offset:0 sc1\n"
      "global_load_dwordx4 v[244:247], v2, s[10:11] offset:1024 sc1\n"
      "buffer_load_dword v32, v42, s[32:35], s63 offen offset:2048 sc1\n"
      "buffer_load_dword v33, v42, s[32:35], s63 offen offset:2304 sc1\n"
      "s_waitcnt vmcnt(0)\n"
      "v_add3_u32 v2, v224, v225, s30\n"
      "v_add3_u32 v2, v2, v226, v227\n"
      "v_add3_u32 v2, v2, v228, v229\n"
      "v_add3_u32 v2, v2, v230, v231\n"
      "v_add3_u32 v2, v2, v232, v233\n"
      "v_add3_u32 v2, v2, v234, v235\n"
      "v_xor_b32 v2, v2, v32\n"
      "v_add3_u32 v3, v236, v237, s30\n"
      "v_add3_u32 v3, v3, v238, v239\n"
      "v_add3_u32 v3, v3, v240, v241\n"
      "v_add3_u32 v3, v3, v242, v243\n"
      "v_add3_u32 v3, v3, v244, v245\n"
      "v_add3_u32 v3, v3, v246, v247\n"
      "v_xor_b32 v3, v3, v33\n"
      "v_or_b32 v2, v2, v3\n"
      "v_cmp_ne_u32 vcc, 0, v2\n"
      "s_sub_u32 s44, s44, 1\n"
      "s_cbranch_vccz L_bhB0_%=\n"
      "s_cmp_lg_u32 s44, 0\n"
      "s_cbranch_scc1 L_shB0_%=\n"
      "s_branch L_bhB0_%=\n"
      "L_shB1_%=:\n"
      "v_add_u32 v2, 108544, v7\n"
      "global_load_dwordx4 v[8:11], v2, s[10:11] offset:-4096 sc1\n"
      "global_load_dwordx4 v[12:15], v2, s[10:11] offset:-3072 sc1\n"
      "global_load_dwordx4 v[16:19], v2, s[10:11] offset:-2048 sc1\n"
      "v_add_u32 v2, 108544, v7\n"
      "global_load_dwordx4 v[20:23], v2, s[10:11] offset:-1024 sc1\n"
      "global_load_dwordx4 v[128:131], v2, s[10:11] offset:0 sc1\n"
      "global_load_dwordx4 v[132:135], v2, s[10:11] offset:1024 sc1\n"
      "buffer_load_dword v34, v42, s[32:35], s63 offen offset:2560 sc1\n"
      "buffer_load_dword v35, v42, s[32:35], s63 offen offset:2816 sc1\n"
      "s_waitcnt vmcnt(0)\n"
      "v_add3_u32 v2, v8, v9, s30\n"
      "v_add3_u32 v2, v2, v10, v11\n"
      "v_add3_u32 v2, v2, v12, v13\n"
      "v_add3_u32 v2, v2, v14, v15\n"
      "v_add3_u32 v2, v2, v16, v17\n"
      "v_add3_u32 v2, v2, v18, v19\n"
      "v_xor_b32 v2, v2, v34\n"
      "v_add3_u32 v3, v20, v21, s30\n"
      "v_add3_u32 v3, v3, v22, v23\n"
      "v_add3_u32 v3, v3, v128, v129\n"
      "v_add3_u32 v3, v3, v130, v131\n"
      "v_add3_u32 v3, v3, v132, v133\n"
      "v_add3_u32 v3, v3, v134, v135\n"
      "v_xor_b32 v3, v3, v35\n"
      "v_or_b32 v2, v2, v3\n"
      "v_cmp_ne_u32 vcc, 0, v2\n"
      "s_sub_u32 s44, s44, 1\n"
      "s_cbranch_vccz L_bhB1_%=\n"
      "s_cmp_lg_u32 s44, 0\n"
      "s_cbranch_scc1 L_shB1_%=\n"
      "s_branch L_bhB1_%=\n"
      "L_shB2_%=:\n"
      "v_add_u32 v2, 102400, v7\n"
      "global_load_dwordx4 v[136:139], v2, s[12:13] offset:-4096 sc1\n"
      "global_load_dwordx4 v[140:143], v2, s[12:13] offset:-3072 sc1\n"
      "global_load_dwordx4 v[144:147], v2, s[12:13] offset:-2048 sc1\n"
      "v_add_u32 v2, 102400, v7\n"
      "global_load_dwordx4 v[148:151], v2, s[12:13] offset:-1024 sc1\n"
      "global_load_dwordx4 v[152:155], v2, s[12:13] offset:0 sc1\n"
      "global_load_dwordx4 v[156:159], v2, s[12:13] offset:1024 sc1\n"
      "buffer_load_dword v24, v42, s[32:35], s63 offen offset:3072 sc1\n"
      "buffer_load_dword v25, v42, s[32:35], s63 offen offset:3328 sc1\n"
      "s_waitcnt vmcnt(0)\n"
      "v_add3_u32 v2, v136, v137, s30\n"
      "v_add3_u32 v2, v2, v138, v139\n"
      "v_add3_u32 v2, v2, v140, v141\n"
      "v_add3_u32 v2, v2, v142, v143\n"
      "v_add3_u32 v2, v2, v144, v145\n"
      "v_add3_u32 v2, v2, v146, v147\n"
      "v_xor_b32 v2, v2, v24\n"
      "v_add3_u32 v3, v148, v149, s30\n"
      "v_add3_u32 v3, v3, v150, v151\n"
      "v_add3_u32 v3, v3, v152, v153\n"
      "v_add3_u32 v3, v3, v154, v155\n"
      "v_add3_u32 v3, v3, v156, v157\n"
      "v_add3_u32 v3, v3, v158, v159\n"
      "v_xor_b32 v3, v3, v25\n"
      "v_or_b32 v2, v2, v3\n"
      "v_cmp_ne_u32 vcc, 0, v2\n"
      "s_sub_u32 s44, s44, 1\n"
      "s_cbranch_vccz L_bhB2_%=\n"
      "s_cmp_lg_u32 s44, 0\n"
      "s_cbranch_scc1 L_shB2_%=\n"
      "s_branch L_bhB2_%=\n"
      "L_shB3_%=:\n"
      "v_add_u32 v2, 108544, v7\n"
      "global_load_dwordx4 v[172:175], v2, s[12:13] offset:-4096 sc1\n"
      "global_load_dwordx4 v[160:163], v2, s[12:13] offset:-3072 sc1\n"
      "global_load_dwordx4 v[164:167], v2, s[12:13] offset:-2048 sc1\n"
      "v_add_u32 v2, 108544, v7\n"
      "global_load_dwordx4 v[168:171], v2, s[12:13] offset:-1024 sc1\n"
      "global_load_dwordx4 v[64:67], v2, s[12:13] offset:0 sc1\n"
      "global_load_dwordx4 v[68:71], v2, s[12:13] offset:1024 sc1\n"
      "buffer_load_dword v26, v42, s[32:35], s63 offen offset:3584 sc1\n"
      "buffer_load_dword v27, v42, s[32:35], s63 offen offset:3840 sc1\n"
      "s_waitcnt vmcnt(0)\n"
      "v_add3_u32 v2, v172, v173, s30\n"
      "v_add3_u32 v2, v2, v174, v175\n"
      "v_add3_u32 v2, v2, v160, v161\n"
      "v_add3_u32 v2, v2, v162, v163\n"
      "v_add3_u32 v2, v2, v164, v165\n"
      "v_add3_u32 v2, v2, v166, v167\n"
      "v_xor_b32 v2, v2, v26\n"
      "v_add3_u32 v3, v168, v169, s30\n"
      "v_add3_u32 v3, v3, v170, v171\n"
      "v_add3_u32 v3, v3, v64, v65\n"
      "v_add3_u32 v3, v3, v66, v67\n"
      "v_add3_u32 v3, v3, v68, v69\n"
      "v_add3_u32 v3, v3, v70, v71\n"
      "v_xor_b32 v3, v3, v27\n"
      "v_or_b32 v2, v2, v3\n"
      "v_cmp_ne_u32 vcc, 0, v2\n"
      "s_sub_u32 s44, s44, 1\n"
      "s_cbranch_vccz L_bhB3_%=\n"
      "s_cmp_lg_u32 s44, 0\n"
      "s_cbranch_scc1 L_shB3_%=\n"
      "s_branch L_bhB3_%=\n"
      "L_sgb_%=:\n"
      "buffer_load_dwordx4 v[244:247], v53, s[32:35], s66 offen sc1\n"
      "s_waitcnt vmcnt(0)\n"
      "v_add_u32 v245, v245, v247\n"
      "v_cmp_ne_u32 vcc, s54, v245\n"
      "s_sub_u32 s44, s44, 1\n"
      "s_cbranch_vccz L_bgb_%=\n"
      "s_cmp_lg_u32 s44, 0\n"
      "s_cbranch_scc1 L_sgb_%=\n"
      "s_branch L_bgb_%=\n"
      : STATE, "+{s[32:35]}"(s32), "+{v[8:23]}"(v8), "+{v[24:27]}"(v24), "+{v[52:55]}"(v52), "+{v[112:127]}"(v112), "+{v[192:195]}"(t[0]), "+{v[240:243]}"(t[1]), "+{v[196:199]}"(t[2]), "+{v[200:203]}"(t[3]), "+{v[204:207]}"(t[4]), "+{v[208:211]}"(t[5]), "+{v[212:215]}"(t[6]), "+{v[216:219]}"(t[7]), "+{v[220:223]}"(t[8]), "+{v[224:227]}"(t[9]), "+{v[236:239]}"(t[10]), "+{v[40:43]}"(t[11]), "+{v[44:47]}"(t[12]), "+{v[48:51]}"(t[13]), "+{v[56:59]}"(t[14]), "+{v[60:63]}"(t[15]), "+{v[28:31]}"(t[16]), "+{v[4:7]}"(t[17])
      : "{s2}"(blockIdx.x)
      : "memory", "scc", "vcc", "s40", "s41", "s42", "s43", "s44", "s45", "s46", "s47", "s48", "s49", "s50", "s51", "v2", "v3", "v32", "v33", "v34", "v35", "v36", "v37", "v38", "v39", "v64", "v65", "v66", "v67", "v68", "v69", "v70", "v71", "v72", "v73", "v74", "v75", "v76", "v77", "v78", "v79", "v80", "v81", "v82", "v83", "v84", "v85", "v86", "v87", "v88", "v89", "v90", "v91", "v92", "v93", "v94", "v95", "v96", "v97", "v98", "v99", "v100", "v101", "v102", "v103", "v104", "v105", "v106", "v107", "v108", "v109", "v110", "v111", "v128", "v129", "v130", "v131", "v132", "v133", "v134", "v135", "v136", "v137", "v138", "v139", "v140", "v141", "v142", "v143", "v144", "v145", "v146", "v147", "v148", "v149", "v150", "v151", "v152", "v153", "v154", "v155", "v156", "v157", "v158", "v159", "v160", "v161", "v162", "v163", "v164", "v165", "v166", "v167", "v168", "v169", "v170", "v171", "v172", "v173", "v174", "v175", "v176", "v177", "v178", "v179", "v180", "v181", "v182", "v183", "v184", "v185", "v186", "v187", "v188", "v189", "v190", "v191", "v228", "v229", "v230", "v231", "v244", "v245", "v246", "v247", "v248", "v249", "v250", "v251", "v252", "v253", "v254", "v255", "a0", "a1", "a2", "a3", "a4", "a5", "a6", "a7", "a8", "a9", "a10", "a11", "a12", "a13", "a14", "a15", "a16", "a17", "a18", "a19", "a20", "a21", "a22", "a23", "a24", "a25", "a26", "a27", "a28", "a29", "a30", "a31", "a32", "a33", "a34", "a35", "a36", "a37", "a38", "a39", "a40", "a41", "a42", "a43", "a44", "a45", "a46", "a47", "a48", "a49", "a50", "a51", "a52", "a53", "a54", "a55", "a56", "a57", "a58", "a59", "a60", "a61", "a62", "a63", "a64", "a65", "a66", "a67", "a68", "a69", "a70", "a71", "a72", "a73", "a74", "a75", "a76", "a77", "a78", "a79", "a80", "a81", "a82", "a83", "a84", "a85", "a86", "a87", "a88", "a89", "a90", "a91", "a92", "a93", "a94", "a95", "a96", "a97", "a98", "a99", "a100", "a101", "a102", "a103", "a104", "a105", "a106", "a107", "a108", "a109", "a110", "a111", "a112", "a113", "a114", "a115", "a116", "a117", "a118", "a119", "a120", "a121", "a122", "a123", "a124", "a125", "a126", "a127", "a128", "a129", "a130", "a131", "a132", "a133", "a134", "a135", "a136", "a137", "a138", "a139", "a140", "a141", "a142", "a143", "a144", "a145", "a146", "a147", "a148", "a149", "a150", "a151", "a152", "a153", "a154", "a155", "a156", "a157", "a158", "a159", "a160", "a161", "a162", "a163", "a164", "a165", "a166", "a167", "a168", "a169", "a170", "a171", "a172", "a173", "a174", "a175", "a176", "a177", "a178", "a179", "a180", "a181", "a182", "a183", "a184", "a185", "a186", "a187", "a188", "a189", "a190", "a191", "a192", "a193", "a194", "a195", "a196", "a197", "a198", "a199", "a200", "a201", "a202", "a203", "a204", "a205", "a206", "a207", "a208", "a209", "a210", "a211", "a212", "a213", "a214", "a215", "a216", "a217", "a218", "a219", "a220", "a221", "a222", "a223", "a224", "a225", "a226", "a227", "a228", "a229", "a230", "a231", "a232", "a233", "a234", "a235", "a236", "a237", "a238", "a239", "a240", "a241", "a242", "a243", "a244", "a245", "a246", "a247", "a248", "a249", "a250", "a251", "a252", "a253", "a254", "a255");
}

extern "C" void kernel_launch(void* const* d_in, const int* in_sizes, int n_in, void* d_out, int out_size,
                              void* d_ws, size_t ws_size, hipStream_t stream) {
  mega<<<256, 256, 0, stream>>>((const u16*)d_in[0], (const u16*)d_in[1], (const u16*)d_in[2], (const u16*)d_in[3],
                                (const u16*)d_in[4], (const u16*)d_in[5], (const u16*)d_in[7], (const u16*)d_in[8],
                                (const u16*)d_in[9], (const u16*)d_in[10], (const u16*)d_in[11], (char*)d_ws, (u16*)d_out);
}
```

```cpp
#include <hip/hip_runtime.h>
#include <stdint.h>

typedef unsigned short u16;
typedef __attribute__((ext_vector_type(4))) unsigned int u32x4;
typedef __attribute__((ext_vector_type(8))) unsigned int u32x8;
typedef __attribute__((ext_vector_type(16))) unsigned int u32x16;
static constexpr int D_ = 640, F_ = 1536, RS_ = 4224;

static constexpr unsigned WS_FW = 20480;
static constexpr unsigned WS_FD = WS_FW + 61440;
static constexpr unsigned WS_STAT = 147456;
static constexpr unsigned WS_PART = 262144;
static constexpr unsigned WS_MOD = 786432;
static constexpr unsigned WS_KVT = 1u << 20;
static constexpr unsigned WS_QT = 4u << 20;
static constexpr unsigned WS_X1T = 7u << 20;
static constexpr unsigned WS_CK = 2752512;
static constexpr unsigned WS_WP = 10u << 20;
static constexpr unsigned WS_WD = 14u << 20;
static constexpr unsigned WS_HH = 16u << 20;
static constexpr unsigned WS_HC = 22u << 20;
static constexpr unsigned WS_ST2 = WS_HC + (512u << 10);
static constexpr unsigned WS_X1R = 23u << 20;
static constexpr unsigned WS_DUMMY = WS_HC + (768u << 10);

static constexpr int KCP = 4160, KCH = 4 * KCP;
static constexpr int XR_CH = 7680;
static constexpr int L_XI = 3 * KCH;
static constexpr int L_EX = 72960;
static constexpr int L_KV = L_EX, KVW = 6400;
static constexpr int L_KT = L_KV + 4 * KVW;
static constexpr int L_QT = L_KT + 84 * 144;
static constexpr int L_WR = L_QT + 10240;
static constexpr int L_GW = L_WR + 16384;
static constexpr int L_GS = L_GW + 20480;
static constexpr int L_SC = L_GS + 320;
static constexpr int L_XR = 110592, L_RS = 162304;
static constexpr int L_WD1 = 122880;
static_assert(L_KT == 98560 && L_QT == 110656 && L_WR == 120896 && L_GW == 137280, "");

struct __attribute__((aligned(256))) Tab { unsigned s[1024][64]; };
static constexpr Tab make_tab() {
  Tab t = {};
  for (int i = 0; i < 1024; ++i) {
    const int b = i >> 2, w = i & 3, xcd = b & 7, idx = b >> 3, L = xcd * 32 + idx, side = L & 1;
    const int ct = L & 7, rt = L >> 3, srow = xcd * 256 + idx * 8;
    const int chA = 128 + L, chH = L >> 1, rH = 512 * (side + (3 - 2 * side) * (w >> 1)) + 256 * (w & 1);
    const int id = idx < 29 ? idx : 29, gj = xcd * 30 + id, j_ks = 5 * (xcd >> 1) + (id * 4 + w) / 24, j_b = 24 * (xcd & 1) + (id * 4 + w) % 24;
    const int d_ks = gj / 5, d_cb = 4 * (gj % 5) + w;
    const unsigned dummy = WS_DUMMY + (i & 63) * 1024;
    const int bm = (xcd >> 2) * 8 + (idx >> 2), bn = (xcd & 3) * 4 + (idx & 3), bm2 = (xcd >> 1) * 8 + (idx >> 2), bn2 = (xcd & 1) * 4 + (idx & 3);
    t.s[i][0] = WS_KVT + (1984 - 64 * rt) * 2 + ct * 80 * RS_;
    t.s[i][1] = WS_KVT + WS_CK + (31 - rt) * RS_ + ct * 512;
    t.s[i][2] = idx < 30 ? WS_MOD + (gj >> 2) * 2048 + (gj & 3) * 64 : dummy;
    t.s[i][3] = 32 * j_ks * (F_ * 2) + (j_b - w) * 64;
    t.s[i][4] = WS_QT + rt * 128 + ct * 80 * RS_;
    t.s[i][5] = WS_QT + WS_CK + (rt * 40 + ct * 5) * 128;
    t.s[i][6] = srow * 1280;
    t.s[i][7] = WS_STAT + (srow >> 9) * 4096 + ((srow >> 3) & 63) * 16;
    t.s[i][8] = WS_MOD + (1920 + 640 * (w >> 1) + 32 * j_ks + 16 * (w & 1)) * 32;
    t.s[i][9] = idx < 30 ? WS_PART + j_ks * 24576 + j_b * 512 : dummy;
    t.s[i][10] = WS_KVT + WS_CK + chA / 20 * 128 + chA % 20 * 4;
    t.s[i][11] = WS_KVT + WS_CK + chH / 20 * 128 + chH % 20 * 4;
    t.s[i][12] = idx < 30 ? WS_WP + (4 * j_b + 0) / 3 * 61440 + (j_ks * 3 + (4 * j_b + 0) % 3) * 1024 : dummy;
    t.s[i][13] = idx < 30 ? WS_WP + (4 * j_b + 1) / 3 * 61440 + (j_ks * 3 + (4 * j_b + 1) % 3) * 1024 : dummy;
    t.s[i][14] = idx < 30 ? WS_WP + (4 * j_b + 2) / 3 * 61440 + (j_ks * 3 + (4 * j_b + 2) % 3) * 1024 : dummy;
    t.s[i][15] = idx < 30 ? WS_WP + (4 * j_b + 3) / 3 * 61440 + (j_ks * 3 + (4 * j_b + 3) % 3) * 1024 : dummy;
    t.s[i][16] = WS_FW + (3 * (bn * 4 + w) / 4 < 46 ? 3 * (bn * 4 + w) / 4 : 46) * 1280;
    t.s[i][17] = WS_X1T + WS_CK + chH / 2 * 512 + chH % 2 * 64 + (rH >> 9) * 128 + ((rH >> 6) & 7) * 8;
    t.s[i][18] = WS_X1T + ((rH >> 6) * D_ + chH) * 128;
    t.s[i][19] = idx < 30 ? WS_FW + (j_b * 20 + j_ks) * 64 : dummy;
    t.s[i][20] = WS_QT + chH * RS_ + rH * 2;
    t.s[i][21] = WS_X1T + WS_CK + chA / 2 * 512 + chA % 2 * 64;
    t.s[i][22] = WS_STAT + (rH >> 9) * 4096 + ((rH >> 3) & 63) * 16;
    t.s[i][23] = WS_MOD + chH / 8 * 256 + chH % 8 * 8;
    t.s[i][24] = WS_QT + WS_CK + chA / 16 * 128 + chA % 16 * 4;
    t.s[i][25] = WS_QT + WS_CK + (rH >> 6) * 5120 + chH / 16 * 128 + chH % 16 * 4;
    t.s[i][26] = idx < 30 ? WS_WD + ((2 * d_cb / 5 * 48 + d_ks) * 5 + 2 * d_cb % 5) * 1024 : dummy;
    t.s[i][27] = idx < 30 ? WS_WD + (((2 * d_cb + 1) / 5 * 48 + d_ks) * 5 + (2 * d_cb + 1) % 5) * 1024 : dummy;
    t.s[i][28] = idx < 30 ? WS_FD + (d_cb * 48 + d_ks) * 64 : dummy;
    t.s[i][29] = WS_X1T + chA * 128;
    t.s[i][30] = WS_X1T + WS_CK + bm / 4 * 128 + bm % 4 * 16 + 4;
    t.s[i][31] = WS_WP + (bn * 4 + w) * 61440;
    t.s[i][32] = chA * 4096;
    t.s[i][33] = chH * 4096;
    t.s[i][34] = ((1792 - rH) >> 5) * 96;
    t.s[i][35] = (96 * (d_ks / 3) + 16 * (d_ks % 3 >> 1) + 2 * (d_ks % 3 & 1)) * (D_ * 2) + d_cb * 64;
    t.s[i][36] = (d_ks % 3 == 1 ? 30 : 18) * (D_ * 2) + ((chA / 8 * 256 + chA % 8 * 8 - chH / 8 * 256 - chH % 8 * 8) << 16);
    t.s[i][37] = ((chA >> 1) & 3) * 32;
    t.s[i][38] = WS_KVT + WS_CK + (chA + 256) / 20 * 128 + (chA + 256) % 20 * 4;
    t.s[i][39] = side;
    t.s[i][40] = WS_ST2 + (bm2 * 8 + bn2) * 1024 + w * 256;
    t.s[i][41] = WS_ST2 + (2 * bm + (w >> 1)) * 8192 + (w & 1) * 512;
    t.s[i][42] = L_XR + (w < 3 ? 20 * w : 56) * 128;
    t.s[i][43] = L_RS + (w & 1) * 512 + (w >> 1) * 256;
    t.s[i][48] = w;
    t.s[i][49] = WS_HH + (((2 * bm + (w >> 1)) * 4 + (w & 1)) * 48 + 3 * bn) * 1024;
    t.s[i][50] = WS_HC + (((2 * bm + (w >> 1)) * 4 + (bn >> 2)) * 8 + (bn & 3)) * 512 + (w & 1) * 256;
    t.s[i][51] = WS_HC + (bm2 * 4 + w) * 4096;
    t.s[i][52] = WS_FD + (((5 * bn2) >> 1) * 48 + 12 * w) * 64;
    t.s[i][53] = WS_PART + (bn * 4 + w) * 384;
    t.s[i][54] = WS_MOD + (3200 + bn2 * 80 + (w < 3 ? 16 * w : 48)) * 32;
    t.s[i][55] = WS_X1R + (bm2 * D_ + bn2 * 80 + (w < 3 ? 20 * w : 56)) * 128;
    t.s[i][56] = WS_X1T + WS_CK + (bn2 * 80 + (w < 3 ? 20 * w : 56)) * 256 + bm2 / 8 * 128 + bm2 % 8 * 8 + bm2 % 2 * 4;
    t.s[i][57] = WS_HH + (((2 * bm + (w >> 1)) * 4 + 2 + (w & 1)) * 48 + 3 * bn) * 1024;
    t.s[i][58] = WS_HC + (((2 * bm + (w >> 1)) * 4 + (bn >> 2)) * 8 + 4) * 512 + (4 * (w & 1) + (bn & 3)) * 256;
    t.s[i][59] = WS_X1T + bm * 163840;
    t.s[i][60] = WS_WD + (bn2 * 48 + 12 * w) * 5120;
    t.s[i][61] = w * 4096;
    t.s[i][62] = ((bm2 >> 1) * 128 + (bm2 & 1) * 32 + (w & 1) * 16) * (D_ * 2) + bn2 * 160 + (w >> 1) * 64;
    t.s[i][63] = WS_HH + (bm2 * 192 + 12 * w) * 1024;
  }
  return t;
}
static __device__ const Tab TAB = make_tab();

__global__ __launch_bounds__(256, 1) __attribute__((aligned(4096))) void mega(const u16* q, const u16* k, const u16* v, const u16* shift_k, const u16* ssm_k, const u16* ssm_D, const u16* w_gate,
                                               const u16* w_up, const u16* w_down, const u16* c, const u16* w_mod, char* ws, u16* out) {
  __shared__ __attribute__((aligned(16))) char lds[163840];
  int w = (int)(size_t)lds, tid = threadIdx.x, v1;
  u32x16 s4, s60, s76;
  u32x8 s20, s52, s92;
  u32x4 s28, s36, v232;
  asm volatile(
      "s_load_dwordx16 s[4:19], s[0:1], 0x0\n"
      "s_load_dwordx8 s[20:27], s[0:1], 0x40\n"
      "s_load_dwordx2 s[28:29], s[0:1], 0x60\n"
      "v_readfirstlane_b32 s3, v0\n"
      "v_and_b32 v2, 63, v0\n"
      "s_lshl_b32 s42, s2, 4\n"
      "s_and_b32 s43, s2, 7\n"
      "v_mul_u32_u24 v3, 205, v2\n"
      "s_and_b32 s52, s2, 0xc0\n"
      "s_lshr_b32 s3, s3, 6\n"
      "v_lshlrev_b32 v8, 4, v2\n"
      "s_cmp_lg_u32 s3, 3\n"
      "s_cbranch_scc1 L_pf%=\n"
      "s_and_b32 s47, s2, 0xf8\n"
      "s_lshl_b32 s47, s47, 10\n"
      "s_cmp_ge_u32 s47, 39168\n"
      "s_cbranch_scc1 L_pf%=\n"
      "s_getpc_b64 s[54:55]\n"
      "v_lshl_add_u32 v9, v8, 3, s47\n"
      "s_and_b32 s54, s54, 0xffffff00\n"
      "s_mov_b32 m0, 160512\n"
      "v_min_u32 v9, 39104, v9\n"
      "global_load_lds_dword v9, s[54:55]\n"
      "L_pf%=:\n"
      "s_lshl_b32 s47, s43, 8\n"
      "v_add_u32 v9, 13120, v3\n"
      "v_add_u32 v10, 26240, v3\n"
      "s_add_u32 s52, s52, s47\n"
      "s_lshl_b32 s47, s3, 4\n"
      "v_lshrrev_b32 v3, 11, v3\n"
      "v_lshrrev_b32 v9, 11, v9\n"
      "s_add_u32 s52, s52, s47\n"
      "s_movk_i32 s47, 1120\n"
      "v_lshrrev_b32 v10, 11, v10\n"
      "s_mulk_i32 s52, 1280\n"
      "s_bfe_u32 s54, s2, 0x30003\n"
      "v_mad_u32_u24 v232, v3, s47, v8\n"
      "v_mad_u32_u24 v233, v9, s47, v8\n"
      "v_mad_u32_u24 v234, v10, s47, v8\n"
      "s_mul_i32 s47, s54, 160\n"
      "s_mul_i32 s53, s3, 6400\n"
      "s_add_u32 s52, s52, s47\n"
      "v_subrev_u32 v233, 6656, v233\n"
      "v_subrev_u32 v234, 5632, v234\n"
      "s_sub_u32 s47, s52, 3840\n"
      "s_add_u32 s53, s53, 72960\n"
      "v_add_u32 v3, s47, v232\n"
      "s_add_u32 s47, s52, 3840\n"
      "s_add_u32 s55, s52, 7680\n"
      "s_mov_b32 s38, 2621440\n"
      "s_mov_b32 s39, 0x20000\n"
      "s_mulk_i32 s54, 640\n"
      "s_waitcnt lgkmcnt(0)\n"
      "s_mov_b32 m0, s53\n"
      "s_mov_b32 s36, s6\n"
      "s_and_b32 s37, s7, 0xffff\n"
      "buffer_load_dwordx4 v3, s[36:39], 0 offen lds\n"
      "s_add_u32 m0, s53, 1024\n"
      "s_nop 0\n"
      "buffer_load_dwordx4 v233, s[36:39], s47 offen lds\n"
      "s_add_u32 m0, s53, 2048\n"
      "v_add_u32 v9, s54, v8\n"
      "buffer_load_dwordx4 v234, s[36:39], s47 offen lds\n"
      "s_add_u32 m0, s53, 3072\n"
      "s_mov_b32 s36, s8\n"
      "s_and_b32 s37, s9, 0xffff\n"
      "buffer_load_dwordx4 v232, s[36:39], s52 offen lds\n"
      "s_add_u32 m0, s53, 4096\n"
      "s_nop 0\n"
      "buffer_load_dwordx4 v233, s[36:39], s55 offen lds\n"
      "s_add_u32 m0, s53, 5120\n"
      "s_mov_b32 exec_hi, 0\n"
      "buffer_load_dwordx4 v234, s[36:39], s55 offen lds\n"
      "s_add_u32 m0, s53, 5632\n"
      "s_mov_b32 exec_hi, 0xff\n"
      "global_load_lds_dwordx4 v9, s[10:11]\n"
      "s_mov_b32 exec_hi, -1\n"
      "s_load_dwordx2 s[30:31], s[26:27], 0x0\n"
      "s_lshl_b32 s51, s3, 2\n"
      "s_add_u32 s42, s42, s51\n"
      "s_lshl_b32 s42, s42, 6\n"
      "s_add_u32 s56, s40, s42\n"
      "s_addc_u32 s57, s41, 0\n"
      "s_load_dwordx16 s[60:75], s[56:57], 0x0\n"
      "s_load_dwordx16 s[76:91], s[56:57], 0x40\n"
      "s_load_dwordx8 s[92:99], s[56:57], 0x80\n"
      : "+{s3}"(w), "+{v0}"(tid), "=&{s[4:19]}"(s4), "=&{s[20:27]}"(s20), "=&{s[28:31]}"(s28), "=&{s[36:39]}"(s36), "=&{s[52:59]}"(s52), "=&{s[60:75]}"(s60), "=&{s[76:91]}"(s76),
        "=&{s[92:99]}"(s92), "=&{v[232:235]}"(v232), "=&{v1}"(v1)
      : "{s[0:1]}"(__builtin_amdgcn_kernarg_segment_ptr()), "{s2}"(blockIdx.x), "{s[40:41]}"(&TAB)
      : "memory", "scc", "s42", "s43", "s47", "s48", "s49", "s50", "s51", "v2", "v3", "v4", "v5", "v8", "v9", "v10", "v11");
#define STATE "+{s3}"(w), "+{v0}"(tid), "+{s[4:19]}"(s4), "+{s[20:27]}"(s20), "+{s[28:31]}"(s28), "+{s[36:39]}"(s36), "+{s[52:59]}"(s52), "+{s[60:75]}"(s60), "+{s[76:91]}"(s76), \
              "+{s[92:99]}"(s92), "+{v[232:235]}"(v232), "+{v1}"(v1)
  u32x4 t[18], s32, v24, v52;
  u32x16 v8, v112;
  {
    const int lane = tid & 63, ip = lane & 15, fq = lane >> 4, q4 = ip >> 2, p4 = lane & 3;
    const int ck = 40 * w + (lane < 40 ? lane : 39);
    const bool dg = fq == q4;
    const unsigned msk = dg ? ((ip & 1) ? 0xFFFF0000u : 0xFFFFu) : 0u, mk0 = (ip & 2) ? 0u : msk, mk1 = (ip & 2) ? msk : 0u;
    const int fo = (ip & 3) * KCP + 2 * ((ip & 12) + 8 * (fq & 1) + 256 * (fq >> 1)), go = L_XI + (15 - ip + 8 * (fq >> 1)) * 96 + 16 * (fq & 1);
    const int exw = L_EX + ip * 128 + (fq ^ (ip & 7)) * 16, z7 = ((tid < 84 ? tid : 83) * 37) >> 8, rowA = 8 * tid;
    t[0][0] = dg && p4 == 0 ? 0x3F803F80u : 0u;
    t[0][1] = dg && p4 == 1 ? 0x3F803F80u : 0u;
    t[0][2] = dg && p4 == 2 ? 0x3F803F80u : 0u;
    t[0][3] = dg && p4 == 3 ? 0x3F803F80u : 0u;
    t[1][0] = (mk0 & 0x0D0C0D0Cu) ^ 0x0C0C0C0Cu;
    t[1][1] = (mk1 & 0x0D0C0D0Cu) ^ 0x0C0C0C0Cu;
    t[1][2] = (mk0 & 0x0F0E0F0Eu) ^ 0x0C0C0C0Cu;
    t[1][3] = (mk1 & 0x0F0E0F0Eu) ^ 0x0C0C0C0Cu;
    t[2][0] = L_KV + w * KVW + ip * 160 + fq * 8;
    t[2][1] = L_KV + w * KVW + 5632 + ip * 8;
    t[2][2] = mk0;
    t[2][3] = mk1;
    t[3][0] = L_KT + ip * 144 + (60 - 16 * w - 4 * fq) * 2;
    t[3][1] = L_KT + (20 * w + (lane >> 3)) * 144 + (lane & 7) * 16;
    t[3][2] = (20 * w + (lane >> 3)) * RS_ + (lane & 7) * 16;
    t[3][3] = lane & 4 ? 0x80000000u : w * 128 + (8 * (lane & 7) + (lane >> 3)) * 4;
    t[4][0] = L_GS + ((ck & ~31) + ((ck >> 2) & 3) * 8 + ((ck >> 4) & 1) * 4 + (ck & 3)) * 2;
    t[4][1] = L_GW + (4 * fq + q4) * 128 + p4 * 8 + ((w ^ (2 * fq + (q4 >> 1))) & 3) * 32;
    t[4][2] = L_GS + fq * 16;
    t[4][3] = lane < 16 ? (2 * w + (lane >> 3)) * 256 + (lane & 7) * 8 : 0x80000000u;
    t[5][0] = (lane & 12) == 4 ? 1 << 20 : 0;
    t[5][1] = lane & 8 ? 0u : ~0u;
    t[5][2] = (8 * w + fq) * (F_ * 2) + (ip ^ (2 * fq)) * 16;
    t[5][3] = (2 * w + (lane >> 5)) * 1280 + (lane & 31) * 16;
    t[6][0] = p4 * 64 + (lane >> 5) * 256 + ((lane >> 2) & 7) * 8;
    t[6][1] = L_WR + (fq >> 1) * 4096 + (fq & 1) * 1024 + q4 * 256 + (((2 * w + 1) ^ q4) & 7) * 32 + p4 * 8;
    t[6][2] = lane == 0 ? w * 1024 : 0x80000000u;
    t[6][3] = ((fq & 1 ? w * 64 + 512 : fq * 32 + 128 * w) + ip * 4) | ((unsigned)(fq & (fq >> 1 | w >> 1)) << 31);
    t[7][0] = (ip < 8 ? ip : 8 + p4) * KCP + 56;
    t[7][1] = z7 * (KCP - 56) + (tid < 84 ? tid : 83) * 8;
    t[7][2] = L_XI + 64 * 96 + (tid < 90 ? tid : 89) * 16;
    t[7][3] = L_WR + (fq >> 1) * 4096 + (fq & 1) * 1024 + q4 * 256 + (((2 * w) ^ q4) & 7) * 32 + p4 * 8;
    t[8][0] = L_SC + w * 32 + (lane >> 2) * 2;
    t[8][1] = (2 * (tid >> 4) + ((tid >> 2) & 1)) * (D_ * 128) + (2 * ((tid >> 3) & 1) + ((tid >> 1) & 1)) * 32 + (tid & 1) * 16;
    t[8][2] = (4 * fq + ((lane >> 2) & 1)) * (D_ * 2) + p4 * 16;
    t[8][3] = ((lane >> 3) ^ (lane >> 4)) & 1;
  }
  asm volatile(
      "v_and_b32 v2, 63, v0\n"
      "s_and_b32 s43, s2, 7\n"
      "v_mul_u32_u24 v3, 205, v2\n"
      "v_lshlrev_b32 v8, 4, v2\n"
      "s_lshr_b32 s48, s2, 3\n"
      "s_min_u32 s48, s48, 29\n"
      "s_mul_i32 s49, s43, 30\n"
      "v_lshrrev_b32 v4, 3, v2\n"
      "s_add_u32 s48, s48, s49\n"
      "v_xor_b32 v5, v4, v2\n"
      "s_and_b32 s49, s48, 3\n"
      "v_and_b32 v11, 1, v2\n"
      "s_lshl_b32 s49, s49, 2\n"
      "v_and_or_b32 v5, v5, 6, v11\n"
      "s_add_u32 s49, s49, s3\n"
      "v_mul_u32_u24 v4, 7680, v4\n"
      "s_lshr_b32 s48, s48, 2\n"
      "s_mul_i32 s50, s49, 307200\n"
      "s_lshl_b32 s48, s48, 7\n"
      "v_lshl_add_u32 v4, v5, 4, v4\n"
      "s_add_u32 s48, s48, s50\n"
      "s_mul_i32 s50, s3, 5120\n"
      "s_mulk_i32 s49, 80\n"
      "v_min_u32 v5, 39, v2\n"
      "v_add_u32 v4, s48, v4\n"
      "v_lshl_add_u32 v5, v5, 1, s49\n"
      "s_add_u32 m0, s50, 137280\n"
      "global_load_ushort v235, v5, s[22:23]\n"
      "global_load_lds_dwordx4 v4, s[24:25]\n"
      "s_add_u32 m0, m0, 1024\n"
      "v_add_u32 v4, 61440, v4\n"
      "global_load_lds_dwordx4 v4, s[24:25]\n"
      "s_add_u32 m0, m0, 1024\n"
      "v_add_u32 v4, 61440, v4\n"
      "global_load_lds_dwordx4 v4, s[24:25]\n"
      "s_add_u32 m0, m0, 1024\n"
      "v_add_u32 v4, 61440, v4\n"
      "global_load_lds_dwordx4 v4, s[24:25]\n"
      "s_add_u32 m0, m0, 1024\n"
      "v_add_u32 v4, 61440, v4\n"
      "global_load_lds_dwordx4 v4, s[24:25]\n"
      "s_lshr_b32 s42, s2, 3\n"
      "s_lshl_b32 s47, s43, 5\n"
      "s_add_u32 s42, s47, s42\n"
      "s_and_b32 s54, s42, -4\n"
      "s_lshl_b32 s47, s42, 1\n"
      "s_and_b32 s47, s47, -4\n"
      "s_load_dword s53, s[14:15], s54\n"
      "s_load_dword s58, s[14:15], s47 offset:0x100\n"
      "s_load_dword s59, s[14:15], s47 offset:0x300\n"
      "s_min_u32 s54, s3, 2\n"
      "s_lshl_b32 s54, s54, 10\n"
      "s_lshr_b32 s10, s42, 1\n"
      "s_lshl_b32 s47, s42, 12\n"
      "s_lshl_b32 s10, s10, 12\n"
      "s_add_u32 s47, s47, s54\n"
      "s_add_u32 s54, s54, s10\n"
      "s_load_dwordx2 s[6:7], s[12:13], s47 offset:0x80400\n"
      "s_add_u32 s47, s47, 0x100000\n"
      "s_load_dwordx2 s[10:11], s[12:13], s54 offset:0x400\n"
      "s_load_dwordx2 s[8:9], s[12:13], s47 offset:0x80400\n"
      "s_mov_b32 s32, s26\n"
      "s_and_b32 s33, s27, 0xffff\n"
      "s_mov_b32 s34, 27262976\n"
      "s_mov_b32 s35, 0x20000\n"
      "v_lshlrev_b32 v54, 4, v0\n"
      "s_sub_u32 s18, s18, 2048\n"
      "s_subb_u32 s19, s19, 0\n"
      "s_add_u32 s22, s20, 60416\n"
      "s_addc_u32 s23, s21, 0\n"
      "v_mov_b32 v66, 0\n"
      "v_mov_b32 v67, 0\n"
      "v_mov_b32 v72, 0\n"
      "v_mov_b32 v73, 0\n"
      "v_mov_b32 v76, 0\n"
      "v_mov_b32 v77, 0\n"
      "v_mov_b32 v82, 0\n"
      "v_mov_b32 v83, 0\n"
      "v_mov_b32 v88, 0\n"
      "v_mov_b32 v89, 0\n"
      "v_mov_b32 v92, 0\n"
      "v_mov_b32 v93, 0\n"
      "v_mov_b32 v98, 0\n"
      "v_mov_b32 v99, 0\n"
      "v_mov_b32 v104, 0\n"
      "v_mov_b32 v105, 0\n"
      "v_mov_b32 v108, 0\n"
      "v_mov_b32 v109, 0\n"
      "v_mov_b32 v114, 0\n"
      "v_mov_b32 v115, 0\n"
      "v_mov_b32 v120, 0\n"
      "v_mov_b32 v121, 0\n"
      "v_mov_b32 v124, 0\n"
      "v_mov_b32 v125, 0\n"
      "v_mov_b32 v34, 0\n"
      "v_mov_b32 v35, 0\n"
      "v_and_b32 v32, 0x3f803f80, v198\n"
      "v_and_b32 v33, 0x3f803f80, v199\n"
      "v_and_b32 v36, 0x3f803f80, v198\n"
      "v_and_b32 v37, 0x3f803f80, v199\n"
      "s_mov_b32 s48, 0xaaaaaaaa\n"
      "s_mov_b32 s49, s48\n"
      "s_mov_b32 s50, 0xcccccccc\n"
      "s_mov_b32 s51, s50\n"
      "s_waitcnt lgkmcnt(0)\n"
      "s_add_u32 s30, s30, 1\n"
      "s_addc_u32 s31, s31, 0\n"
      "s_mul_i32 s24, s30, 10\n"
      "v_mov_b32 v52, s30\n"
      "v_mov_b32 v53, s31\n"
      "s_and_b32 s42, s96, 0xffff\n"
      "s_lshl_b32 s43, s3, 11\n"
      "v_mad_u32_u24 v222, v223, s42, v222\n"
      "s_cmp_eq_u32 s3, 3\n"
      "s_cmov_b32 s43, 7680\n"
      "v_add_u32 v222, s95, v222\n"
      "s_add_u32 s43, s43, 49920\n"
      "s_mov_b32 s36, s12\n"
      "s_and_b32 s37, s13, 0xffff\n"
      "s_add_u32 s40, s60, 33792\n"
      "s_add_u32 s41, s60, 67584\n"
      "s_add_u32 s47, s92, 0x100000\n"
      "s_sub_u32 s54, s92, s93\n"
      "s_lshl_b32 s45, s3, 12\n"
      "v_and_b32 v39, s54, v209\n"
      "v_add_u32 v210, s63, v210\n"
      "v_add_u32 v208, v39, v208\n"
      "v_add_u32 v39, 11264, v210\n"
      "s_waitcnt vmcnt(6)\n"
      "ds_read_b64 v[244:245], v197 offset:0\n"
      "ds_read_b64 v[246:247], v197 offset:128\n"
      "ds_read_b64 v[248:249], v197 offset:256\n"
      "ds_read_b64 v[250:251], v197 offset:384\n"
      "ds_read_b64 v[252:253], v197 offset:512\n"
      "ds_read_b64 a[0:1], v196 offset:0\n"
      "ds_read_b64 a[2:3], v196 offset:32\n"
      "ds_read_b64 a[4:5], v196 offset:64\n"
      "ds_read_b64 a[6:7], v196 offset:96\n"
      "ds_read_b64 a[8:9], v196 offset:128\n"
      "ds_read_b64 a[10:11], v196 offset:160\n"
      "ds_read_b64 a[12:13], v196 offset:192\n"
      "ds_read_b64 a[14:15], v196 offset:224\n"
      "ds_read_b64 a[16:17], v196 offset:256\n"
      "ds_read_b64 a[18:19], v196 offset:288\n"
      "s_waitcnt lgkmcnt(10)\n"
      "ds_read_b64 a[20:21], v196 offset:320\n"
      "ds_read_b64 a[22:23], v196 offset:352\n"
      "ds_read_b64 a[24:25], v196 offset:384\n"
      "v_perm_b32 v64, v245, v245, v242\n"
      "v_perm_b32 v65, v245, v245, v243\n"
      "v_perm_b32 v80, v245, v245, v240\n"
      "v_perm_b32 v81, v245, v245, v241\n"
      "ds_read_b64 a[26:27], v196 offset:416\n"
      "ds_read_b64 a[28:29], v196 offset:448\n"
      "ds_read_b64 a[30:31], v196 offset:480\n"
      "v_perm_b32 v68, v247, v247, v242\n"
      "v_perm_b32 v69, v247, v247, v243\n"
      "v_perm_b32 v84, v247, v247, v240\n"
      "v_perm_b32 v85, v247, v247, v241\n"
      "ds_read_b64 a[32:33], v196 offset:512\n"
      "ds_read_b64 a[34:35], v196 offset:544\n"
      "ds_read_b64 a[36:37], v196 offset:576\n"
      "v_perm_b32 v70, v249, v249, v242\n"
      "v_perm_b32 v71, v249, v249, v243\n"
      "v_perm_b32 v86, v249, v249, v240\n"
      "v_perm_b32 v87, v249, v249, v241\n"
      "ds_read_b64 a[38:39], v196 offset:608\n"
      "ds_read_b64 a[40:41], v196 offset:3072\n"
      "ds_read_b64 a[42:43], v196 offset:3104\n"
      "v_perm_b32 v74, v251, v251, v242\n"
      "v_perm_b32 v75, v251, v251, v243\n"
      "v_perm_b32 v90, v251, v251, v240\n"
      "v_perm_b32 v91, v251, v251, v241\n"
      "ds_read_b64 a[44:45], v196 offset:3136\n"
      "ds_read_b64 a[46:47], v196 offset:3168\n"
      "ds_read_b64 a[48:49], v196 offset:3200\n"
      "v_perm_b32 v78, v253, v253, v242\n"
      "v_perm_b32 v79, v253, v253, v243\n"
      "v_perm_b32 v94, v253, v253, v240\n"
      "v_perm_b32 v95, v253, v253, v241\n"
      "s_waitcnt lgkmcnt(14)\n"
      "v_mfma_f32_16x16x32_bf16 v[128:131], a[0:3], v[64:67], 0\n"
      "v_perm_b32 v96, v244, v244, v242\n"
      "v_perm_b32 v97, v244, v244, v243\n"
      "v_mfma_f32_16x16x32_bf16 v[132:135], a[10:13], v[80:83], 0\n"
      "buffer_load_dwordx4 v[8:11], v54, s[36:39], s92 offen\n"
      "v_perm_b32 v112, v244, v244, v240\n"
      "v_perm_b32 v113, v244, v244, v241\n"
      "v_mfma_f32_16x16x32_bf16 v[140:143], a[0:3], v[66:69], 0\n"
      "v_perm_b32 v100, v246, v246, v242\n"
      "v_perm_b32 v101, v246, v246, v243\n"
      "v_mfma_f32_16x16x32_bf16 v[144:147], a[10:13], v[82:85], 0\n"
      "buffer_load_dwordx4 v[14:17], v54, s[36:39], s47 offen\n"
      "v_perm_b32 v116, v246, v246, v240\n"
      "v_perm_b32 v117, v246, v246, v241\n"
      "v_mfma_f32_16x16x32_bf16 v[152:155], a[4:7], v[70:73], 0\n"
      "v_perm_b32 v102, v248, v248, v242\n"
      "v_perm_b32 v103, v248, v248, v243\n"
      "v_mfma_f32_16x16x32_bf16 v[156:159], a[14:17], v[86:89], 0\n"
      "buffer_load_dwordx4 v[20:23], v54, s[36:39], s93 offen\n"
      "v_perm_b32 v118, v248, v248, v240\n"
      "v_perm_b32 v119, v248, v248, v241\n"
      "v_mfma_f32_16x16x32_bf16 v[164:167], a[4:7], v[72:75], 0\n"
      "v_perm_b32 v106, v250, v250, v242\n"
      "v_perm_b32 v107, v250, v250, v243\n"
      "v_mfma_f32_16x16x32_bf16 v[168:171], a[14:17], v[88:91], 0\n"
      "buffer_load_dwordx2 v[26:27], v208, s[36:39], s93 offen\n"
      "v_perm_b32 v122, v250, v250, v240\n"
      "v_perm_b32 v123, v250, v250, v241\n"
      "v_mfma_f32_16x16x32_bf16 v[176:179], a[6:9], v[76:79], 0\n"
      "v_perm_b32 v110, v252, v252, v242\n"
      "v_perm_b32 v111, v252, v252, v243\n"
      "v_mfma_f32_16x16x32_bf16 v[180:183], a[16:19], v[92:95], 0\n"
      "v_perm_b32 v126, v252, v252, v240\n"
      "v_perm_b32 v127, v252, v252, v241\n"
      "s_waitcnt lgkmcnt(0)\n"
      "v_cvt_pk_bf16_f32 v136, v128, v132\n"
      "v_cvt_pk_bf16_f32 v137, v129, v133\n"
      "v_cvt_pk_bf16_f32 v138, v130, v134\n"
      "v_cvt_pk_bf16_f32 v139, v131, v135\n"
      "v_cvt_pk_bf16_f32 v148, v140, v144\n"
      "v_cvt_pk_bf16_f32 v149, v141, v145\n"
      "v_mfma_f32_16x16x32_bf16 v[128:131], v[192:195], v[136:139], 0\n"
      "v_cvt_pk_bf16_f32 v150, v142, v146\n"
      "v_cvt_pk_bf16_f32 v151, v143, v147\n"
      "v_mfma_f32_16x16x32_bf16 v[132:135], a[20:23], v[96:99], 0\n"
      "s_add_u32 m0, s45, 120896\n"
      "s_nop 0\n"
      "global_load_lds_dwordx4 v210, s[16:17]\n"
      "v_cvt_pk_bf16_f32 v160, v152, v156\n"
      "v_cvt_pk_bf16_f32 v161, v153, v157\n"
      "v_mfma_f32_16x16x32_bf16 v[140:143], v[192:195], v[148:151], 0\n"
      "v_cvt_pk_bf16_f32 v162, v154, v158\n"
      "v_cvt_pk_bf16_f32 v163, v155, v159\n"
      "v_mfma_f32_16x16x32_bf16 v[144:147], a[20:23], v[98:101], 0\n"
      "global_load_lds_dwordx4 v39, s[16:17] offset:1024\n"
      "v_cvt_pk_bf16_f32 v172, v164, v168\n"
      "v_cvt_pk_bf16_f32 v173, v165, v169\n"
      "v_mfma_f32_16x16x32_bf16 v[152:155], v[192:195], v[160:163], 0\n"
      "v_cvt_pk_bf16_f32 v174, v166, v170\n"
      "v_cvt_pk_bf16_f32 v175, v167, v171\n"
      "v_mfma_f32_16x16x32_bf16 v[156:159], a[24:27], v[102:105], 0\n"
      "global_load_lds_dwordx4 v210, s[18:19] offset:2048\n"
      "v_cvt_pk_bf16_f32 v184, v176, v180\n"
      "v_cvt_pk_bf16_f32 v185, v177, v181\n"
      "v_mfma_f32_16x16x32_bf16 v[164:167], v[192:195], v[172:175], 0\n"
      "v_cvt_pk_bf16_f32 v186, v178, v182\n"
      "v_cvt_pk_bf16_f32 v187, v179, v183\n"
      "v_mfma_f32_16x16x32_bf16 v[168:171], a[24:27], v[104:107], 0\n"
      "global_load_lds_dwordx4 v39, s[18:19] offset:3072\n"
      "v_cvt_pk_bf16_f32 v136, v128, v132\n"
      "v_cvt_pk_bf16_f32 v137, v129, v133\n"
      "v_mfma_f32_16x16x32_bf16 v[176:179], v[192:195], v[184:187], 0\n"
      "v_cvt_pk_bf16_f32 v138, v130, v134\n"
      "v_cvt_pk_bf16_f32 v139, v131, v135\n"
      "v_mfma_f32_16x16x32_bf16 v[180:183], a[26:29], v[108:111], 0\n"
      "v_cvt_pk_bf16_f32 v148, v140, v144\n"
      "v_cvt_pk_bf16_f32 v149, v141, v145\n"
      "v_mfma_f32_16x16x32_bf16 v[128:131], v[192:195], v[136:139], 0\n"
      "v_cvt_pk_bf16_f32 v150, v142, v146\n"
      "v_cvt_pk_bf16_f32 v151, v143, v147\n"
      "v_mfma_f32_16x16x32_bf16 v[132:135], a[30:33], v[112:115], 0\n"
      "v_cvt_pk_bf16_f32 v160, v152, v156\n"
      "v_cvt_pk_bf16_f32 v161, v153, v157\n"
      "v_mfma_f32_16x16x32_bf16 v[140:143], v[192:195], v[148:151], 0\n"
      "v_cvt_pk_bf16_f32 v162, v154, v158\n"
      "v_cvt_pk_bf16_f32 v163, v155, v159\n"
      "v_mfma_f32_16x16x32_bf16 v[144:147], a[30:33], v[114:117], 0\n"
      "v_cvt_pk_bf16_f32 v172, v164, v168\n"
      "v_cvt_pk_bf16_f32 v173, v165, v169\n"
      "v_mfma_f32_16x16x32_bf16 v[152:155], v[192:195], v[160:163], 0\n"
      "v_cvt_pk_bf16_f32 v174, v166, v170\n"
      "v_cvt_pk_bf16_f32 v175, v167, v171\n"
      "v_mfma_f32_16x16x32_bf16 v[156:159], a[34:37], v[118:121], 0\n"
      "v_cvt_pk_bf16_f32 v184, v176, v180\n"
      "v_cvt_pk_bf16_f32 v185, v177, v181\n"
      "v_mfma_f32_16x16x32_bf16 v[164:167], v[192:195], v[172:175], 0\n"
      "v_cvt_pk_bf16_f32 v186, v178, v182\n"
      "v_cvt_pk_bf16_f32 v187, v179, v183\n"
      "v_mfma_f32_16x16x32_bf16 v[168:171], a[34:37], v[120:123], 0\n"
      "v_cvt_pk_bf16_f32 v136, v128, v132\n"
      "v_cvt_pk_bf16_f32 v137, v129, v133\n"
      "v_mfma_f32_16x16x32_bf16 v[176:179], v[192:195], v[184:187], 0\n"
      "v_cvt_pk_bf16_f32 v138, v130, v134\n"
      "v_cvt_pk_bf16_f32 v139, v131, v135\n"
      "v_mfma_f32_16x16x32_bf16 v[180:183], a[36:39], v[124:127], 0\n"
      "v_cvt_pk_bf16_f32 v148, v140, v144\n"
      "v_cvt_pk_bf16_f32 v149, v141, v145\n"
      "v_mfma_f32_16x16x32_bf16 v[128:131], v[192:195], v[136:139], 0\n"
      "v_cvt_pk_bf16_f32 v150, v142, v146\n"
      "v_cvt_pk_bf16_f32 v151, v143, v147\n"
      "v_mfma_f32_16x16x32_bf16 v[132:135], a[40:43], v[32:35], 0\n"
      "v_cvt_pk_bf16_f32 v160, v152, v156\n"
      "v_cvt_pk_bf16_f32 v161, v153, v157\n"
      "v_mfma_f32_16x16x32_bf16 v[140:143], v[192:195], v[148:151], 0\n"
      "v_cvt_pk_bf16_f32 v162, v154, v158\n"
      "v_cvt_pk_bf16_f32 v163, v155, v159\n"
      "v_mfma_f32_16x16x32_bf16 v[144:147], a[40:43], v[34:37], 0\n"
      "v_cvt_pk_bf16_f32 v172, v164, v168\n"
      "v_cvt_pk_bf16_f32 v173, v165, v169\n"
      "v_mfma_f32_16x16x32_bf16 v[152:155], v[192:195], v[160:163], 0\n"
      "v_cvt_pk_bf16_f32 v174, v166, v170\n"
      "v_cvt_pk_bf16_f32 v175, v167, v171\n"
      "v_mfma_f32_16x16x32_bf16 v[156:159], a[44:47], v[32:35], 0\n"
      "v_cvt_pk_bf16_f32 v184, v176, v180\n"
      "v_cvt_pk_bf16_f32 v185, v177, v181\n"
      "v_mfma_f32_16x16x32_bf16 v[164:167], v[192:195], v[172:175], 0\n"
      "v_cvt_pk_bf16_f32 v186, v178, v182\n"
      "v_cvt_pk_bf16_f32 v187, v179, v183\n"
      "v_mfma_f32_16x16x32_bf16 v[168:171], a[44:47], v[34:37], 0\n"
      "v_cvt_pk_bf16_f32 v128, 0, v128\n"
      "v_cvt_pk_bf16_f32 v129, 0, v129\n"
      "v_mfma_f32_16x16x32_bf16 v[176:179], v[192:195], v[184:187], 0\n"
      "v_cvt_pk_bf16_f32 v130, 0, v130\n"
      "v_cvt_pk_bf16_f32 v131, 0, v131\n"
      "v_mfma_f32_16x16x32_bf16 v[180:183], a[46:49], v[34:37], 0\n"
      "v_mul_f32 v128, v128, v132\n"
      "v_mul_f32 v129, v129, v133\n"
      "v_mul_f32 v130, v130, v134\n"
      "v_mul_f32 v131, v131, v135\n"
      "v_cvt_pk_bf16_f32 v136, v131, v130\n"
      "v_cvt_pk_bf16_f32 v137, v129, v128\n"
      "v_cvt_pk_bf16_f32 v140, 0, v140\n"
      "v_cvt_pk_bf16_f32 v141, 0, v141\n"
      "v_cvt_pk_bf16_f32 v142, 0, v142\n"
      "v_cvt_pk_bf16_f32 v143, 0, v143\n"
      "ds_write_b64 v200, v[136:137] offset:0\n"
      "v_mul_f32 v140, v140, v144\n"
      "v_mul_f32 v141, v141, v145\n"
      "v_mul_f32 v142, v142, v146\n"
      "v_mul_f32 v143, v143, v147\n"
      "v_cvt_pk_bf16_f32 v148, v143, v142\n"
      "v_cvt_pk_bf16_f32 v149, v141, v140\n"
      "v_cvt_pk_bf16_f32 v152, 0, v152\n"
      "v_cvt_pk_bf16_f32 v153, 0, v153\n"
      "v_cvt_pk_bf16_f32 v154, 0, v154\n"
      "v_cvt_pk_bf16_f32 v155, 0, v155\n"
      "ds_write_b64 v200, v[148:149] offset:2304\n"
      "v_mul_f32 v152, v152, v156\n"
      "v_mul_f32 v153, v153, v157\n"
      "v_mul_f32 v154, v154, v158\n"
      "v_mul_f32 v155, v155, v159\n"
      "v_cvt_pk_bf16_f32 v160, v155, v154\n"
      "v_cvt_pk_bf16_f32 v161, v153, v152\n"
      "v_cvt_pk_bf16_f32 v164, 0, v164\n"
      "v_cvt_pk_bf16_f32 v165, 0, v165\n"
      "v_cvt_pk_bf16_f32 v166, 0, v166\n"
      "v_cvt_pk_bf16_f32 v167, 0, v167\n"
      "ds_write_b64 v200, v[160:161] offset:4608\n"
      "v_mul_f32 v164, v164, v168\n"
      "v_mul_f32 v165, v165, v169\n"
      "v_mul_f32 v166, v166, v170\n"
      "v_mul_f32 v167, v167, v171\n"
      "v_cvt_pk_bf16_f32 v172, v167, v166\n"
      "v_cvt_pk_bf16_f32 v173, v165, v164\n"
      "v_cvt_pk_bf16_f32 v176, 0, v176\n"
      "v_cvt_pk_bf16_f32 v177, 0, v177\n"
      "v_cvt_pk_bf16_f32 v178, 0, v178\n"
      "v_cvt_pk_bf16_f32 v179, 0, v179\n"
      "ds_write_b64 v200, v[172:173] offset:6912\n"
      "v_mul_f32 v176, v176, v180\n"
      "v_mul_f32 v177, v177, v181\n"
      "v_mul_f32 v178, v178, v182\n"
      "v_mul_f32 v179, v179, v183\n"
      "v_cvt_pk_bf16_f32 v184, v179, v178\n"
      "v_cvt_pk_bf16_f32 v185, v177, v176\n"
      "ds_write_b64 v200, v[184:185] offset:9216\n"
      "s_waitcnt lgkmcnt(0)\n"
      "s_barrier\n"
      "ds_read_b128 v[64:67], v201 offset:0\n"
      "ds_read_b128 v[68:71], v201 offset:1152\n"
      "ds_read_b128 v[72:75], v201 offset:2304\n"
      "s_waitcnt lgkmcnt(2)\n"
      "buffer_store_dwordx4 v[64:67], v202, s[32:35], s60 offen offset:0 sc1\n"
      "v_add3_u32 v76, v64, v65, v66\n"
      "v_add_u32 v76, v76, v67\n"
      "s_waitcnt lgkmcnt(1)\n"
      "buffer_store_dwordx4 v[68:71], v202, s[32:35], s40 offen offset:0 sc1\n"
      "v_add3_u32 v77, v68, v69, v70\n"
      "v_add_u32 v77, v77, v71\n"
      "s_waitcnt lgkmcnt(0)\n"
      "s_mov_b32 exec_hi, 0\n"
      "buffer_store_dwordx4 v[72:75], v202, s[32:35], s41 offen offset:0 sc1\n"
      "s_mov_b32 exec_hi, -1\n"
      "v_add3_u32 v78, v72, v73, v74\n"
      "v_add_u32 v78, v78, v75\n"
      "v_add_u32_dpp v76, v76, v76 quad_perm:[1,0,3,2] row_mask:0xf bank_mask:0xf\n"
      "v_add_u32_dpp v77, v77, v77 quad_perm:[1,0,3,2] row_mask:0xf bank_mask:0xf\n"
      "v_add_u32_dpp v78, v78, v78 quad_perm:[1,0,3,2] row_mask:0xf bank_mask:0xf\n"
      "v_add_u32_dpp v76, v76, v76 quad_perm:[2,3,0,1] row_mask:0xf bank_mask:0xf\n"
      "v_add_u32_dpp v77, v77, v77 quad_perm:[2,3,0,1] row_mask:0xf bank_mask:0xf\n"
      "v_add_u32_dpp v78, v78, v78 quad_perm:[2,3,0,1] row_mask:0xf bank_mask:0xf\n"
      "v_add_u32_dpp v76, v76, v76 row_half_mirror row_mask:0xf bank_mask:0xf\n"
      "v_add_u32_dpp v77, v77, v77 row_half_mirror row_mask:0xf bank_mask:0xf\n"
      "v_add_u32_dpp v78, v78, v78 row_half_mirror row_mask:0xf bank_mask:0xf\n"
      "v_cndmask_b32 v76, v76, v77, s[48:49]\n"
      "v_cndmask_b32 v76, v76, v78, s[50:51]\n"
      "v_sub_u32 v76, s30, v76\n"
      "buffer_store_dword v76, v203, s[32:35], s61 offen offset:0 sc1\n"
      "s_waitcnt vmcnt(12)\n"
      "v_lshlrev_b32 v38, 16, v235\n"
      "v_mul_f32 v172, 0xbfb8aa3b, v38\n"
      "v_exp_f32 v172, v172\n"
      "s_nop 0\n"
      "v_add_f32 v172, 1.0, v172\n"
      "v_rcp_f32 v172, v172\n"
      "s_nop 0\n"
      "v_mul_f32 v38, v38, v172\n"
      "v_cvt_pk_bf16_f32 v38, v38, v38\n"
      "ds_write_b16 v204, v38\n"
      "s_waitcnt lgkmcnt(0)\n"
      "s_barrier\n"
      "ds_read_b128 v[144:147], v206 offset:0\n"
      "ds_read_b64_tr_b16 v[148:149], v205 offset:0\n"
      "ds_read_b64_tr_b16 v[150:151], v205 offset:2048\n"
      "ds_read_b128 v[152:155], v206 offset:64\n"
      "ds_read_b64_tr_b16 v[156:157], v205 offset:4096\n"
      "ds_read_b64_tr_b16 v[158:159], v205 offset:6144\n"
      "ds_read_b128 v[160:163], v206 offset:128\n"
      "ds_read_b64_tr_b16 v[164:165], v205 offset:8192\n"
      "ds_read_b64_tr_b16 v[166:167], v205 offset:10240\n"
      "ds_read_b128 v[168:171], v206 offset:192\n"
      "ds_read_b64_tr_b16 v[172:173], v205 offset:12288\n"
      "ds_read_b64_tr_b16 v[174:175], v205 offset:14336\n"
      "ds_read_b128 v[176:179], v206 offset:256\n"
      "ds_read_b64_tr_b16 v[180:181], v205 offset:16384\n"
      "ds_read_b64_tr_b16 v[182:183], v205 offset:18432\n"
      "s_waitcnt lgkmcnt(12)\n"
      "v_mfma_f32_16x16x32_bf16 v[184:187], v[144:147], v[148:151], 0\n"
      "s_waitcnt lgkmcnt(9)\n"
      "v_mfma_f32_16x16x32_bf16 v[184:187], v[152:155], v[156:159], v[184:187]\n"
      "s_waitcnt lgkmcnt(6)\n"
      "v_mfma_f32_16x16x32_bf16 v[184:187], v[160:163], v[164:167], v[184:187]\n"
      "s_waitcnt lgkmcnt(3)\n"
      "v_mfma_f32_16x16x32_bf16 v[184:187], v[168:171], v[172:175], v[184:187]\n"
      "s_waitcnt lgkmcnt(0)\n"
      "v_mfma_f32_16x16x32_bf16 v[184:187], v[176:179], v[180:183], v[184:187]\n"
      "s_nop 7\n"
      "v_mov_b32 v185, s30\n"
      "buffer_store_dwordx2 v[184:185], v207, s[32:35], s62 offen offset:0 sc1\n"
      "s_mul_i32 s42, s3, 2560\n"
      "s_add_u32 m0, s42, 110656\n"
      "s_mov_b32 s36, s4\n"
      "s_and_b32 s37, s5, 0xffff\n"
      "buffer_load_dwordx4 v232, s[36:39], s52 offen lds\n"
      "s_add_u32 m0, m0, 1024\n"
      "s_nop 0\n"
      "buffer_load_dwordx4 v233, s[36:39], s55 offen lds\n"
      "s_add_u32 m0, m0, 1024\n"
      "s_mov_b32 exec_hi, 0\n"
      "buffer_load_dwordx4 v234, s[36:39], s55 offen lds\n"
      "s_mov_b32 exec_hi, -1\n"
      "buffer_load_dwordx4 v[112:115], v211, s[36:39], s66 offen\n"
      "buffer_load_dwordx4 v[116:119], v211, s[36:39], s66 offen offset:512\n"
      "v_bfe_u32 v2, v54, 1, 8\n"
      "v_sub_u32 v2, v211, v2\n"
      "buffer_load_dwordx2 v[120:121], v2, s[36:39], s66 offen offset:1024\n"
      : STATE, "=&{s[32:35]}"(s32), "=&{v[8:23]}"(v8), "=&{v[24:27]}"(v24), "=&{v[52:55]}"(v52), "=&{v[112:127]}"(v112), "+{v[192:195]}"(t[0]), "+{v[240:243]}"(t[1]), "+{v[196:199]}"(t[2]), "+{v[200:203]}"(t[3]), "+{v[204:207]}"(t[4]), "+{v[208:211]}"(t[5]), "+{v[212:215]}"(t[6]), "+{v[216:219]}"(t[7]), "+{v[220:223]}"(t[8])
      : "{s2}"(blockIdx.x)
      : "memory", "scc", "vcc", "s40", "s41", "s42", "s43", "s44", "s45", "s46", "s47", "s48", "s49", "s50", "s51", "v2", "v3", "v4", "v5", "v6", "v7", "v28", "v29", "v30", "v31", "v32", "v33", "v34", "v35", "v36", "v37", "v38", "v39", "v40", "v41", "v42", "v43", "v44", "v45", "v46", "v47", "v48", "v49", "v50", "v51", "v56", "v57", "v58", "v59", "v60", "v61", "v62", "v63", "v64", "v65", "v66", "v67", "v68", "v69", "v70", "v71", "v72", "v73", "v74", "v75", "v76", "v77", "v78", "v79", "v80", "v81", "v82", "v83", "v84", "v85", "v86", "v87", "v88", "v89", "v90", "v91", "v92", "v93", "v94", "v95", "v96", "v97", "v98", "v99", "v100", "v101", "v102", "v103", "v104", "v105", "v106", "v107", "v108", "v109", "v110", "v111", "v128", "v129", "v130", "v131", "v132", "v133", "v134", "v135", "v136", "v137", "v138", "v139", "v140", "v141", "v142", "v143", "v144", "v145", "v146", "v147", "v148", "v149", "v150", "v151", "v152", "v153", "v154", "v155", "v156", "v157", "v158", "v159", "v160", "v161", "v162", "v163", "v164", "v165", "v166", "v167", "v168", "v169", "v170", "v171", "v172", "v173", "v174", "v175", "v176", "v177", "v178", "v179", "v180", "v181", "v182", "v183", "v184", "v185", "v186", "v187", "v188", "v189", "v190", "v191", "v224", "v225", "v226", "v227", "v228", "v229", "v230", "v231", "v236", "v237", "v238", "v239", "v244", "v245", "v246", "v247", "v248", "v249", "v250", "v251", "v252", "v253", "v254", "v255", "a0", "a1", "a2", "a3", "a4", "a5", "a6", "a7", "a8", "a9", "a10", "a11", "a12", "a13", "a14", "a15", "a16", "a17", "a18", "a19", "a20", "a21", "a22", "a23", "a24", "a25", "a26", "a27", "a28", "a29", "a30", "a31", "a32", "a33", "a34", "a35", "a36", "a37", "a38", "a39", "a40", "a41", "a42", "a43", "a44", "a45", "a46", "a47", "a48", "a49", "a50", "a51", "a52", "a53", "a54", "a55", "a56", "a57", "a58", "a59", "a60", "a61", "a62", "a63", "a64", "a65", "a66", "a67", "a68", "a69", "a70", "a71", "a72", "a73", "a74", "a75", "a76", "a77", "a78", "a79", "a80", "a81", "a82", "a83", "a84", "a85", "a86", "a87", "a88", "a89", "a90", "a91", "a92", "a93", "a94", "a95", "a96", "a97", "a98", "a99", "a100", "a101", "a102", "a103", "a104", "a105", "a106", "a107", "a108", "a109", "a110", "a111", "a112", "a113", "a114", "a115", "a116", "a117", "a118", "a119", "a120", "a121", "a122", "a123", "a124", "a125", "a126", "a127", "a128", "a129", "a130", "a131", "a132", "a133", "a134", "a135", "a136", "a137", "a138", "a139", "a140", "a141", "a142", "a143", "a144", "a145", "a146", "a147", "a148", "a149", "a150", "a151", "a152", "a153", "a154", "a155", "a156", "a157", "a158", "a159", "a160", "a161", "a162", "a163", "a164", "a165", "a166", "a167", "a168", "a169", "a170", "a171", "a172", "a173", "a174", "a175", "a176", "a177", "a178", "a179", "a180", "a181", "a182", "a183", "a184", "a185", "a186", "a187", "a188", "a189", "a190", "a191", "a192", "a193", "a194", "a195", "a196", "a197", "a198", "a199", "a200", "a201", "a202", "a203", "a204", "a205", "a206", "a207", "a208", "a209", "a210", "a211", "a212", "a213", "a214", "a215", "a216", "a217", "a218", "a219", "a220", "a221", "a222", "a223", "a224", "a225", "a226", "a227", "a228", "a229", "a230", "a231", "a232", "a233", "a234", "a235", "a236", "a237", "a238", "a239", "a240", "a241", "a242", "a243", "a244", "a245", "a246", "a247", "a248", "a249", "a250", "a251", "a252", "a253", "a254", "a255");
  {
    const int lane = tid & 63, ip = lane & 15, fq = lane >> 4, q4 = ip >> 2, p4 = lane & 3;
    const int ck = 40 * w + (lane < 40 ? lane : 39);
    const bool dg = fq == q4;
    const unsigned msk = dg ? ((ip & 1) ? 0xFFFF0000u : 0xFFFFu) : 0u, mk0 = (ip & 2) ? 0u : msk, mk1 = (ip & 2) ? msk : 0u;
    const int fo = (ip & 3) * KCP + 2 * ((ip & 12) + 8 * (fq & 1) + 256 * (fq >> 1)), go = L_XI + (15 - ip + 8 * (fq >> 1)) * 96 + 16 * (fq & 1);
    const int exw = L_EX + ip * 128 + (fq ^ (ip & 7)) * 16, z7 = ((tid < 84 ? tid : 83) * 37) >> 8, rowA = 8 * tid;
    t[9][0] = L_QT + (8 * fq + q4) * 160 + p4 * 8 + w * 32;
    t[9][1] = L_QT + (8 * fq + q4) * 160 + p4 * 8 + 128 + (w & 1) * 5120;
    t[9][2] = (16 * w + ip) * RS_ + fq * 16;
    t[9][3] = w < 2 ? (64 + ip) * RS_ + fq * 16 + w * 64 : 0x80000000u;
    t[10][0] = L_SC + (ip & 4 ? 0 : 64) + fq * 8;
    t[10][1] = (tid >> 3) * RS_;
    t[10][2] = L_XI + (tid >> 2) * 96 + (tid & 3) * 16;
    t[10][3] = (fq >> 1) * 256 + (fq & 1) * 64 + ip * 4;
    t[11][0] = fo + (w >> 1) * KCH + (w & 1) * 256;
    t[11][1] = go + (w >> 1) * XR_CH + (w & 1) * 384;
    t[11][2] = fo + 2 * KCH + w * 128;
    t[11][3] = go + 2 * XR_CH + w * 192;
    t[12][0] = exw + w * 8192;
    t[12][1] = L_EX + 32768 + w * 1024 + (lane >> 3) * 128 + ((lane & 7) ^ (lane >> 3)) * 16;
    t[12][2] = L_XI + ((2040 - rowA) >> 5) * 96 + ((2040 - rowA) & 31) * 2;
    t[12][3] = L_XI + 2 * XR_CH + ((252 - 4 * lane) >> 5) * 96 + ((252 - 4 * lane) & 31) * 2;
    t[13][0] = lane < 8 ? lane * 8 : 0x80000000u;
    t[13][1] = (ip >> 1) == 4 * (fq & 1) ? fq * 8 + (ip & 1) * 4 : 0x80000000u;
    t[13][2] = fq * D_ * 128 + q4 * 32 + p4 * 8;
    t[13][3] = (lane < 40 ? lane : 39) * 64;
    t[14][0] = exw + 32768 + w * 4096;
    t[14][1] = (lane & 1) * 2048 + (lane >> 1) * 16;
    t[14][2] = p4 * 64 + (fq == 1 ? 256 * 32 : 0) + (q4 < 3 ? q4 : 4) * 20480;
    t[14][3] = (exw + w * 8192) ^ 64;
    t[15][0] = (tid >> 3) * 5120 + ((tid >> 2) & 1) * 64;
    t[15][1] = fq * 5120 + ((lane >> 3) & 1) * 64;
    t[15][2] = (exw + 32768 + w * 4096) ^ 64;
    t[15][3] = (2 * (lane >> 5) + (q4 >> 1)) * (D_ * 128) + (2 * (fq & 1) + (q4 & 1)) * 32 + p4 * 8;
    t[16][0] = L_GW + w * 2048 + (4 * fq + q4) * 64 + p4 * 8;
    t[16][1] = (rowA >> 6) * D_ * 128 + ((lane >> 1) & 3) * 32 + (lane & 1) * 16;
    t[16][2] = ((lane >> 1) & 3) == 2 * ((lane >> 3) & 1) ? w * 128 + (lane >> 3) * 8 + (lane & 1) * 4 : 0x80000000u;
    t[16][3] = L_EX + w * 2048 + (lane >> 2) * 128 + ((2 * p4) ^ ((lane >> 2) & 7)) * 16;
    t[17][0] = lane * 8;
    t[17][1] = lane & 4 ? 0x80000000u : (16 * w + 4 * (lane & 7) + fq) * 512 + ((lane >> 3) & 1) * 64;
    t[17][2] = (4 * fq + q4) * 128 + ((2 * fq + (q4 >> 1)) & 3) * 32 + p4 * 8;
    t[17][3] = lane * 16;
  }
  asm volatile(
      "v_mov_b32 v34, 0\n"
      "v_mov_b32 v35, 0\n"
      "v_mov_b32 v228, 0\n"
      "v_mov_b32 v229, 0\n"
      "v_mov_b32 v230, 0\n"
      "v_mov_b32 v231, 0\n"
      "s_mov_b32 s44, 0x40000\n"
      "s_mov_b32 s46, 0\n"
      "s_lshl_b32 s42, s3, 12\n"
      "s_add_u32 s42, s42, 147456\n"
      "v_add_u32 v55, s42, v7\n"
      "ds_write_b64 v217, v[228:229]\n"
      "ds_write_b128 v218, v[228:231] offset:0\n"
      "ds_write_b128 v218, v[228:231] offset:7680\n"
      "ds_write_b128 v218, v[228:231] offset:15360\n"
      "s_lshr_b32 s43, s96, 16\n"
      "s_lshr_b32 s42, s93, 8\n"
      "s_and_b32 s42, s42, 0x60\n"
      "v_add_u32 v47, s94, v47\n"
      "s_mov_b32 exec_hi, 0\n"
      "v_add_u32 v58, s43, v58\n"
      "s_mov_b32 exec_hi, -1\n"
      "v_xor_b32 v50, s42, v50\n"
      "v_xor_b32 v29, s97, v29\n"
      "v_add_u32 v5, s90, v5\n"
      "s_lshl_b32 s42, s99, 4\n"
      "s_lshr_b32 s58, s58, s42\n"
      "s_lshr_b32 s59, s59, s42\n"
      "s_lshr_b32 s42, s93, 8\n"
      "s_and_b32 s42, s42, 16\n"
      "s_lshr_b32 s25, s53, s42\n"
      "s_lshl_b32 s14, s58, 16\n"
      "s_lshl_b32 s15, s59, 16\n"
      "s_lshl_b32 s25, s25, 16\n"
      "s_mov_b64 exec, 0\n"
      "s_cmp_eq_u32 s3, 0\n"
      "s_cbranch_scc1 L_pg0_%=\n"
      "s_cmp_eq_u32 s3, 1\n"
      "s_cbranch_scc1 L_pg1_%=\n"
      "s_cmp_eq_u32 s3, 2\n"
      "s_cbranch_scc1 L_pg2_%=\n"
      "s_branch L_pg3_%=\n"
      "L_pgr_%=:\n"
      "s_mov_b64 exec, -1\n"
      "s_waitcnt vmcnt(0)\n"
      "s_barrier\n"
      "ds_read_b64_tr_b16 v[64:65], v224 offset:0\n"
      "ds_read_b64_tr_b16 v[66:67], v224 offset:640\n"
      "ds_read_b64_tr_b16 v[68:69], v224 offset:5120\n"
      "ds_read_b64_tr_b16 v[70:71], v224 offset:5760\n"
      "ds_read_b64_tr_b16 v[72:73], v225 offset:0\n"
      "ds_read_b64_tr_b16 v[74:75], v225 offset:640\n"
      "ds_read_b64_tr_b16 v[144:145], v219 offset:0\n"
      "ds_read_b64_tr_b16 v[146:147], v219 offset:8192\n"
      "ds_read_b64_tr_b16 v[148:149], v219 offset:2048\n"
      "ds_read_b64_tr_b16 v[150:151], v219 offset:10240\n"
      "ds_read_b64_tr_b16 v[152:153], v213 offset:0\n"
      "ds_read_b64_tr_b16 v[154:155], v213 offset:8192\n"
      "ds_read_b64_tr_b16 v[156:157], v213 offset:2048\n"
      "ds_read_b64_tr_b16 v[158:159], v213 offset:10240\n"
      "v_mov_b32 v12, s6\n"
      "v_mov_b32 v13, s7\n"
      "v_mov_b32 v18, s8\n"
      "v_mov_b32 v19, s9\n"
      "v_mov_b32 v24, s10\n"
      "v_mov_b32 v25, s11\n"
      "v_and_b32 v188, 48, v7\n"
      "v_xor_b32 v188, 48, v188\n"
      "v_lshlrev_b64 v[26:27], v188, v[26:27]\n"
      "v_mov_b32_dpp v12, v8 wave_shl:1 row_mask:0xf bank_mask:0xf\n"
      "v_mov_b32_dpp v13, v9 wave_shl:1 row_mask:0xf bank_mask:0xf\n"
      "v_mov_b32_dpp v18, v14 wave_shl:1 row_mask:0xf bank_mask:0xf\n"
      "v_mov_b32_dpp v19, v15 wave_shl:1 row_mask:0xf bank_mask:0xf\n"
      "v_mov_b32_dpp v24, v20 wave_shl:1 row_mask:0xf bank_mask:0xf\n"
      "v_mov_b32_dpp v25, v21 wave_shl:1 row_mask:0xf bank_mask:0xf\n"
      "v_mov_b32 v122, 0\n"
      "v_mov_b32 v123, 0\n"
      "v_mov_b32 v96, 0x3f803f80\n"
      "v_dot2c_f32_bf16 v122, v112, v96\n"
      "v_dot2c_f32_bf16 v123, v112, v112\n"
      "v_dot2c_f32_bf16 v122, v113, v96\n"
      "v_dot2c_f32_bf16 v123, v113, v113\n"
      "v_dot2c_f32_bf16 v122, v114, v96\n"
      "v_dot2c_f32_bf16 v123, v114, v114\n"
      "v_dot2c_f32_bf16 v122, v115, v96\n"
      "v_dot2c_f32_bf16 v123, v115, v115\n"
      "v_dot2c_f32_bf16 v122, v116, v96\n"
      "v_dot2c_f32_bf16 v123, v116, v116\n"
      "v_dot2c_f32_bf16 v122, v117, v96\n"
      "v_dot2c_f32_bf16 v123, v117, v117\n"
      "v_dot2c_f32_bf16 v122, v118, v96\n"
      "v_dot2c_f32_bf16 v123, v118, v118\n"
      "v_dot2c_f32_bf16 v122, v119, v96\n"
      "v_dot2c_f32_bf16 v123, v119, v119\n"
      "v_dot2c_f32_bf16 v122, v120, v96\n"
      "v_dot2c_f32_bf16 v123, v120, v120\n"
      "v_dot2c_f32_bf16 v122, v121, v96\n"
      "v_dot2c_f32_bf16 v123, v121, v121\n"
      "s_lshr_b32 s42, s92, 12\n"
      "s_lshr_b32 s43, s93, 12\n"
      "s_mulk_i32 s42, 4224\n"
      "s_mulk_i32 s43, 4224\n"
      "s_add_u32 s42, s42, 0x100000\n"
      "s_add_u32 s43, s43, 0x100000\n"
      "buffer_load_dwordx4 v[128:131], v54, s[32:35], s42 offen sc1\n"
      "s_add_u32 s42, s42, 1081344\n"
      "buffer_load_dwordx4 v[136:139], v54, s[32:35], s43 offen sc1\n"
      "buffer_load_dwordx4 v[132:135], v54, s[32:35], s42 offen sc1\n"
      "buffer_load_dword v140, v237, s[32:35], s70 offen sc1\n"
      "buffer_load_dword v141, v237, s[32:35], s98 offen sc1\n"
      "buffer_load_dword v142, v237, s[32:35], s71 offen sc1\n"
      "s_waitcnt lgkmcnt(0)\n"
      "ds_write_b128 v54, v[10:13] offset:12544\n"
      "v_alignbit_b32 v160, v9, v8, 16\n"
      "v_alignbit_b32 v161, v10, v9, 16\n"
      "v_alignbit_b32 v162, v11, v10, 16\n"
      "v_alignbit_b32 v163, v12, v11, 16\n"
      "s_mov_b32 vcc_lo, 0x33333333\n"
      "s_mov_b32 vcc_hi, 0x33333333\n"
      "v_cndmask_b32_dpp v80, v148, v144, vcc quad_perm:[0,1,0,1] row_mask:0xf bank_mask:0xf\n"
      "v_cndmask_b32_dpp v81, v149, v145, vcc quad_perm:[0,1,0,1] row_mask:0xf bank_mask:0xf\n"
      "v_cndmask_b32_dpp v82, v150, v146, vcc quad_perm:[0,1,0,1] row_mask:0xf bank_mask:0xf\n"
      "v_cndmask_b32_dpp v83, v151, v147, vcc quad_perm:[0,1,0,1] row_mask:0xf bank_mask:0xf\n"
      "ds_write_b128 v54, v[160:163] offset:64\n"
      "v_alignbit_b32 v164, v10, v9, 16\n"
      "v_alignbit_b32 v165, v11, v10, 16\n"
      "v_alignbit_b32 v166, v12, v11, 16\n"
      "v_alignbit_b32 v167, v13, v12, 16\n"
      "v_cndmask_b32_dpp v88, v156, v152, vcc quad_perm:[0,1,0,1] row_mask:0xf bank_mask:0xf\n"
      "v_cndmask_b32_dpp v89, v157, v153, vcc quad_perm:[0,1,0,1] row_mask:0xf bank_mask:0xf\n"
      "v_cndmask_b32_dpp v90, v158, v154, vcc quad_perm:[0,1,0,1] row_mask:0xf bank_mask:0xf\n"
      "v_cndmask_b32_dpp v91, v159, v155, vcc quad_perm:[0,1,0,1] row_mask:0xf bank_mask:0xf\n"
      "s_not_b64 vcc, vcc\n"
      "v_cndmask_b32_dpp v84, v144, v148, vcc quad_perm:[2,3,2,3] row_mask:0xf bank_mask:0xf\n"
      "ds_write_b128 v54, v[164:167] offset:8384\n"
      "v_mov_b32 v168, v9\n"
      "v_mov_b32 v169, v10\n"
      "v_mov_b32 v170, v11\n"
      "v_mov_b32 v171, v12\n"
      "v_cndmask_b32_dpp v85, v145, v149, vcc quad_perm:[2,3,2,3] row_mask:0xf bank_mask:0xf\n"
      "v_cndmask_b32_dpp v86, v146, v150, vcc quad_perm:[2,3,2,3] row_mask:0xf bank_mask:0xf\n"
      "v_cndmask_b32_dpp v87, v147, v151, vcc quad_perm:[2,3,2,3] row_mask:0xf bank_mask:0xf\n"
      "v_cndmask_b32_dpp v92, v152, v156, vcc quad_perm:[2,3,2,3] row_mask:0xf bank_mask:0xf\n"
      "v_cndmask_b32_dpp v93, v153, v157, vcc quad_perm:[2,3,2,3] row_mask:0xf bank_mask:0xf\n"
      "v_cndmask_b32_dpp v94, v154, v158, vcc quad_perm:[2,3,2,3] row_mask:0xf bank_mask:0xf\n"
      "ds_write_b128 v54, v[168:171] offset:4224\n"
      "v_cndmask_b32_dpp v95, v155, v159, vcc quad_perm:[2,3,2,3] row_mask:0xf bank_mask:0xf\n"
      "buffer_store_dwordx4 v[80:83], v7, s[32:35], s72 offen offset:0 sc1\n"
      "buffer_store_dwordx4 v[84:87], v7, s[32:35], s73 offen offset:0 sc1\n"
      "buffer_store_dwordx4 v[88:91], v7, s[32:35], s74 offen offset:0 sc1\n"
      "buffer_store_dwordx4 v[92:95], v7, s[32:35], s75 offen offset:0 sc1\n"
      "v_add3_u32 v76, v64, v65, v66\n"
      "ds_write_b128 v54, v[16:19] offset:29184\n"
      "v_alignbit_b32 v172, v15, v14, 16\n"
      "v_alignbit_b32 v173, v16, v15, 16\n"
      "v_alignbit_b32 v174, v17, v16, 16\n"
      "v_alignbit_b32 v175, v18, v17, 16\n"
      "v_add_u32 v76, v76, v67\n"
      "v_add3_u32 v77, v68, v69, v70\n"
      "v_add_f32_dpp v122, v122, v122 quad_perm:[1,0,3,2] row_mask:0xf bank_mask:0xf\n"
      "v_add_u32 v77, v77, v71\n"
      "v_add_f32_dpp v123, v123, v123 quad_perm:[1,0,3,2] row_mask:0xf bank_mask:0xf\n"
      "v_add3_u32 v78, v72, v73, v74\n"
      "ds_write_b128 v54, v[172:175] offset:16704\n"
      "v_alignbit_b32 v176, v16, v15, 16\n"
      "v_alignbit_b32 v177, v17, v16, 16\n"
      "v_alignbit_b32 v178, v18, v17, 16\n"
      "v_alignbit_b32 v179, v19, v18, 16\n"
      "v_add_f32_dpp v122, v122, v122 quad_perm:[2,3,0,1] row_mask:0xf bank_mask:0xf\n"
      "v_add_u32 v78, v78, v75\n"
      "v_add_f32_dpp v123, v123, v123 quad_perm:[2,3,0,1] row_mask:0xf bank_mask:0xf\n"
      "v_mov_b32 v79, v78\n"
      "v_add_f32_dpp v122, v122, v122 row_half_mirror row_mask:0xf bank_mask:0xf\n"
      "v_permlane32_swap_b32 v76, v77\n"
      "ds_write_b128 v54, v[176:179] offset:25024\n"
      "v_mov_b32 v180, v15\n"
      "v_mov_b32 v181, v16\n"
      "v_mov_b32 v182, v17\n"
      "v_mov_b32 v183, v18\n"
      "v_add_f32_dpp v123, v123, v123 row_half_mirror row_mask:0xf bank_mask:0xf\n"
      "v_permlane32_swap_b32 v78, v79\n"
      "v_add_f32_dpp v122, v122, v122 row_mirror row_mask:0xf bank_mask:0xf\n"
      "v_add_u32 v76, v76, v77\n"
      "v_add_f32_dpp v123, v123, v123 row_mirror row_mask:0xf bank_mask:0xf\n"
      "v_add_u32 v78, v78, v79\n"
      "ds_write_b128 v54, v[180:183] offset:20864\n"
      "v_mov_b32 v96, v122\n"
      "v_permlane16_swap_b32 v76, v78\n"
      "v_mov_b32 v97, v123\n"
      "v_add_u32 v76, v76, v78\n"
      "v_permlane16_swap_b32 v122, v96\n"
      "v_sub_u32 v76, s30, v76\n"
      "ds_write_b128 v54, v[22:25] offset:45824\n"
      "v_alignbit_b32 v160, v21, v20, 16\n"
      "v_alignbit_b32 v161, v22, v21, 16\n"
      "v_alignbit_b32 v162, v23, v22, 16\n"
      "v_alignbit_b32 v163, v24, v23, 16\n"
      "v_permlane16_swap_b32 v123, v97\n"
      "v_add_f32 v122, v122, v96\n"
      "v_add_f32 v123, v123, v97\n"
      "v_mul_f32 v122, 0x3acccccd, v122\n"
      "v_mul_f32 v123, 0x3acccccd, v123\n"
      "v_fma_f32 v123, -v122, v122, v123\n"
      "ds_write_b128 v54, v[160:163] offset:33344\n"
      "v_alignbit_b32 v164, v22, v21, 16\n"
      "v_alignbit_b32 v165, v23, v22, 16\n"
      "v_alignbit_b32 v166, v24, v23, 16\n"
      "v_alignbit_b32 v167, v25, v24, 16\n"
      "v_max_f32 v123, 0, v123\n"
      "v_cvt_pk_bf16_f32 v123, 0, v123\n"
      "v_add_f32 v123, 0x3727c5ac, v123\n"
      "v_cvt_pk_bf16_f32 v123, 0, v123\n"
      "v_sqrt_f32 v123, v123\n"
      "v_readlane_b32 s42, v122, 32\n"
      "ds_write_b128 v54, v[164:167] offset:41664\n"
      "v_mov_b32 v168, v21\n"
      "v_mov_b32 v169, v22\n"
      "v_mov_b32 v170, v23\n"
      "v_mov_b32 v171, v24\n"
      "v_cvt_pk_bf16_f32 v123, 0, v123\n"
      "v_rcp_f32 v96, v123\n"
      "v_mov_b32 v99, s30\n"
      "v_cvt_pk_bf16_f32 v98, v122, s42\n"
      "v_readlane_b32 s42, v96, 32\n"
      "s_nop 1\n"
      "v_mov_b32 v97, s42\n"
      "ds_write_b128 v54, v[168:171] offset:37504\n"
      "buffer_store_dwordx4 v[96:99], v214, s[32:35], s67 offen offset:0 sc1\n"
      "buffer_store_dwordx4 v[64:67], v226, s[32:35], s64 offen offset:0 sc1\n"
      "buffer_store_dwordx4 v[68:71], v226, s[32:35], s64 offen offset:64 sc1\n"
      "buffer_store_dwordx4 v[72:75], v227, s[32:35], s64 offen offset:0 sc1\n"
      "buffer_store_dword v76, v215, s[32:35], s65 offen offset:0 sc1\n"
      "ds_write_b64 v216, v[26:27]\n"
      "v_mov_b64 v[8:9], v[80:81]\n"
      "v_mov_b64 v[10:11], v[82:83]\n"
      "v_mov_b64 v[12:13], v[84:85]\n"
      "v_mov_b64 v[14:15], v[86:87]\n"
      "v_mov_b64 v[16:17], v[88:89]\n"
      "v_mov_b64 v[18:19], v[90:91]\n"
      "v_mov_b64 v[20:21], v[92:93]\n"
      "v_mov_b64 v[22:23], v[94:95]\n"
      "v_mov_b32 v2, v212\n"
      "v_mov_b32 v3, v220\n"
      "s_mov_b32 s45, s68\n"
      "s_lshr_b32 s42, s93, 8\n"
      "s_and_b32 s42, s42, 0x60\n"
      "v_xor_b32 v36, s97, v221\n"
      "v_xor_b32 v37, s42, v63\n"
      "s_add_u32 s55, s78, 0x1000000\n"
      "s_mov_b32 s59, s69\n"
      "s_lshr_b32 s53, s92, 12\n"
      "s_mulk_i32 s53, 4224\n"
      "s_add_u32 s53, s53, 0x400000\n"
      "s_add_u32 s54, s53, 1081344\n"
      "s_waitcnt vmcnt(9)\n"
      "v_add3_u32 v143, v128, v129, v130\n"
      "v_add3_u32 v143, v143, v131, v132\n"
      "v_add3_u32 v143, v143, v133, v134\n"
      "v_add3_u32 v143, v143, v135, v136\n"
      "v_add3_u32 v143, v143, v137, v138\n"
      "v_add_u32 v143, v143, v139\n"
      "v_add3_u32 v140, v140, v141, v142\n"
      "s_mul_i32 s47, s30, 3\n"
      "v_add_u32_dpp v143, v143, v143 quad_perm:[1,0,3,2] row_mask:0xf bank_mask:0xf\n"
      "s_nop 1\n"
      "v_add_u32_dpp v143, v143, v143 quad_perm:[2,3,0,1] row_mask:0xf bank_mask:0xf\n"
      "s_nop 1\n"
      "v_add_u32_dpp v143, v143, v143 row_half_mirror row_mask:0xf bank_mask:0xf\n"
      "v_add_u32 v143, v143, v140\n"
      "v_cmp_ne_u32 vcc, s47, v143\n"
      "s_cbranch_vccnz L_psx_%=\n"
      "L_pbx_%=:\n"
      "ds_write_b128 v238, v[128:131] offset:0\n"
      "ds_write_b128 v238, v[132:135] offset:7680\n"
      "ds_write_b128 v238, v[136:139] offset:15360\n"
      "s_waitcnt vmcnt(5)\n"
      "buffer_store_dwordx2 v[52:53], v48, s[32:35], s79 offen offset:0 sc1\n"
      "s_mul_i32 s42, s3, 2048\n"
      "s_add_u32 m0, s42, 137280\n"
      "s_waitcnt lgkmcnt(0)\n"
      "s_barrier\n"
      "s_cmp_lg_u32 s99, 0\n"
      "s_cbranch_scc1 L_cv_b%=\n"
      "ds_read_b128 v[160:163], v43 offset:4608\n"
      "ds_read_b64 v[112:113], v42 offset:0\n"
      "ds_read_b64 v[114:115], v42 offset:8\n"
      "ds_read_b64 v[148:149], v42 offset:3072\n"
      "ds_read_b64 v[150:151], v42 offset:3080\n"
      "ds_read_b128 v[172:175], v43 offset:0\n"
      "ds_read_b128 v[168:171], v43 offset:1536\n"
      "ds_read_b64 v[124:125], v42 offset:1024\n"
      "ds_read_b64 v[126:127], v42 offset:1032\n"
      "ds_read_b128 v[164:167], v43 offset:3072\n"
      "ds_read_b64 v[136:137], v42 offset:2048\n"
      "ds_read_b64 v[138:139], v42 offset:2056\n"
      "ds_read_b64 v[116:117], v42 offset:32\n"
      "ds_read_b64 v[118:119], v42 offset:40\n"
      "ds_read_b64 v[128:129], v42 offset:1056\n"
      "ds_read_b64 v[130:131], v42 offset:1064\n"
      "ds_read_b64 v[140:141], v42 offset:2080\n"
      "ds_read_b64 v[142:143], v42 offset:2088\n"
      "ds_read_b64 v[152:153], v42 offset:3104\n"
      "ds_read_b64 v[154:155], v42 offset:3112\n"
      "s_waitcnt lgkmcnt(8)\n"
      "v_mfma_f32_16x16x32_bf16 v[96:99], v[112:115], v[160:163], 0\n"
      "ds_read_b128 v[176:179], v43 offset:4640\n"
      "ds_read_b128 v[180:183], v43 offset:3104\n"
      "v_mfma_f32_16x16x32_bf16 v[104:107], v[148:151], v[160:163], 0\n"
      "ds_read_b128 v[184:187], v43 offset:1568\n"
      "v_mfma_f32_16x16x32_bf16 v[104:107], v[112:115], v[172:175], v[104:107]\n"
      "ds_read_b128 v[188:191], v43 offset:32\n"
      "v_mfma_f32_16x16x32_bf16 v[104:107], v[124:127], v[168:171], v[104:107]\n"
      "ds_read_b64 v[120:121], v42 offset:64\n"
      "ds_read_b64 v[122:123], v42 offset:72\n"
      "v_mfma_f32_16x16x32_bf16 v[104:107], v[136:139], v[164:167], v[104:107]\n"
      "ds_read_b64 v[132:133], v42 offset:1088\n"
      "s_waitcnt lgkmcnt(7)\n"
      "v_mfma_f32_16x16x32_bf16 v[100:103], v[116:119], v[160:163], 0\n"
      "ds_read_b64 v[134:135], v42 offset:1096\n"
      "v_mfma_f32_16x16x32_bf16 v[108:111], v[152:155], v[160:163], 0\n"
      "ds_read_b64 v[144:145], v42 offset:2112\n"
      "v_mfma_f32_16x16x32_bf16 v[108:111], v[116:119], v[172:175], v[108:111]\n"
      "ds_read_b64 v[146:147], v42 offset:2120\n"
      "v_mfma_f32_16x16x32_bf16 v[108:111], v[128:131], v[168:171], v[108:111]\n"
      "ds_read_b64 v[156:157], v42 offset:3136\n"
      "v_mfma_f32_16x16x32_bf16 v[108:111], v[140:143], v[164:167], v[108:111]\n"
      "ds_read_b64 v[158:159], v42 offset:3144\n"
      "s_waitcnt lgkmcnt(8)\n"
      "v_mfma_f32_16x16x32_bf16 v[96:99], v[116:119], v[176:179], v[96:99]\n"
      "ds_read_b128 v[160:163], v43 offset:4704\n"
      "ds_read_b128 v[164:167], v43 offset:3168\n"
      "v_mfma_f32_16x16x32_bf16 v[104:107], v[152:155], v[176:179], v[104:107]\n"
      "ds_read_b128 v[168:171], v43 offset:1632\n"
      "v_mfma_f32_16x16x32_bf16 v[104:107], v[116:119], v[188:191], v[104:107]\n"
      "ds_read_b128 v[172:175], v43 offset:96\n"
      "v_mfma_f32_16x16x32_bf16 v[104:107], v[128:131], v[184:187], v[104:107]\n"
      "ds_read_b64 v[112:113], v42 offset:96\n"
      "ds_read_b64 v[114:115], v42 offset:104\n"
      "v_mfma_f32_16x16x32_bf16 v[104:107], v[140:143], v[180:183], v[104:107]\n"
      "ds_read_b64 v[124:125], v42 offset:1120\n"
      "s_waitcnt lgkmcnt(7)\n"
      "v_mfma_f32_16x16x32_bf16 v[100:103], v[120:123], v[176:179], v[100:103]\n"
      "ds_read_b64 v[126:127], v42 offset:1128\n"
      "v_mfma_f32_16x16x32_bf16 v[108:111], v[156:159], v[176:179], v[108:111]\n"
      "ds_read_b64 v[136:137], v42 offset:2144\n"
      "v_mfma_f32_16x16x32_bf16 v[108:111], v[120:123], v[188:191], v[108:111]\n"
      "ds_read_b64 v[138:139], v42 offset:2152\n"
      "v_mfma_f32_16x16x32_bf16 v[108:111], v[132:135], v[184:187], v[108:111]\n"
      "ds_read_b64 v[148:149], v42 offset:3168\n"
      "v_mfma_f32_16x16x32_bf16 v[108:111], v[144:147], v[180:183], v[108:111]\n"
      "ds_read_b64 v[150:151], v42 offset:3176\n"
      "s_waitcnt lgkmcnt(8)\n"
      "v_mfma_f32_16x16x32_bf16 v[96:99], v[120:123], v[160:163], v[96:99]\n"
      "ds_read_b128 v[176:179], v43 offset:4736\n"
      "ds_read_b128 v[180:183], v43 offset:3200\n"
      "v_mfma_f32_16x16x32_bf16 v[104:107], v[156:159], v[160:163], v[104:107]\n"
      "ds_read_b128 v[184:187], v43 offset:1664\n"
      "v_mfma_f32_16x16x32_bf16 v[104:107], v[120:123], v[172:175], v[104:107]\n"
      "ds_read_b128 v[188:191], v43 offset:128\n"
      "v_mfma_f32_16x16x32_bf16 v[104:107], v[132:135], v[168:171], v[104:107]\n"
      "ds_read_b64 v[116:117], v42 offset:128\n"
      "ds_read_b64 v[118:119], v42 offset:136\n"
      "v_mfma_f32_16x16x32_bf16 v[104:107], v[144:147], v[164:167], v[104:107]\n"
      "ds_read_b64 v[128:129], v42 offset:1152\n"
      "s_waitcnt lgkmcnt(7)\n"
      "v_mfma_f32_16x16x32_bf16 v[100:103], v[112:115], v[160:163], v[100:103]\n"
      "ds_read_b64 v[130:131], v42 offset:1160\n"
      "v_mfma_f32_16x16x32_bf16 v[108:111], v[148:151], v[160:163], v[108:111]\n"
      "ds_read_b64 v[140:141], v42 offset:2176\n"
      "v_mfma_f32_16x16x32_bf16 v[108:111], v[112:115], v[172:175], v[108:111]\n"
      "ds_read_b64 v[142:143], v42 offset:2184\n"
      "v_mfma_f32_16x16x32_bf16 v[108:111], v[124:127], v[168:171], v[108:111]\n"
      "ds_read_b64 v[152:153], v42 offset:3200\n"
      "L_pg0_%=:\n"
      "s_cbranch_execz L_pg4_%=\n"
      "v_mfma_f32_16x16x32_bf16 v[108:111], v[136:139], v[164:167], v[108:111]\n"
      "ds_read_b64 v[154:155], v42 offset:3208\n"
      "s_waitcnt lgkmcnt(8)\n"
      "v_mfma_f32_16x16x32_bf16 v[96:99], v[112:115], v[176:179], v[96:99]\n"
      "ds_read_b128 v[160:163], v41 offset:4608\n"
      "ds_read_b64 v[120:121], v40 offset:0\n"
      "v_mfma_f32_16x16x32_bf16 v[104:107], v[148:151], v[176:179], v[104:107]\n"
      "ds_read_b64 v[122:123], v40 offset:8\n"
      "ds_read_b128 v[164:167], v41 offset:3072\n"
      "v_mfma_f32_16x16x32_bf16 v[104:107], v[112:115], v[188:191], v[104:107]\n"
      "ds_read_b64 v[132:133], v40 offset:1024\n"
      "ds_read_b64 v[134:135], v40 offset:1032\n"
      "v_mfma_f32_16x16x32_bf16 v[104:107], v[124:127], v[184:187], v[104:107]\n"
      "ds_read_b128 v[168:171], v41 offset:1536\n"
      "ds_read_b64 v[144:145], v40 offset:2048\n"
      "v_mfma_f32_16x16x32_bf16 v[104:107], v[136:139], v[180:183], v[104:107]\n"
      "ds_read_b64 v[146:147], v40 offset:2056\n"
      "ds_read_b128 v[172:175], v41 offset:0\n"
      "s_waitcnt lgkmcnt(10)\n"
      "v_mfma_f32_16x16x32_bf16 v[100:103], v[116:119], v[176:179], v[100:103]\n"
      "ds_read_b64 v[156:157], v40 offset:3072\n"
      "v_mfma_f32_16x16x32_bf16 v[108:111], v[152:155], v[176:179], v[108:111]\n"
      "ds_read_b64 v[158:159], v40 offset:3080\n"
      "v_mfma_f32_16x16x32_bf16 v[108:111], v[116:119], v[188:191], v[108:111]\n"
      "v_mfma_f32_16x16x32_bf16 v[108:111], v[128:131], v[184:187], v[108:111]\n"
      "v_mfma_f32_16x16x32_bf16 v[108:111], v[140:143], v[180:183], v[108:111]\n"
      "s_waitcnt lgkmcnt(0)\n"
      "s_branch L_cv_e%=\n"
      "L_cv_b%=:\n"
      "ds_read_b128 v[164:167], v43 offset:3072\n"
      "ds_read_b64 v[112:113], v42 offset:0\n"
      "ds_read_b64 v[114:115], v42 offset:8\n"
      "ds_read_b64 v[124:125], v42 offset:1024\n"
      "ds_read_b64 v[126:127], v42 offset:1032\n"
      "ds_read_b128 v[160:163], v43 offset:4608\n"
      "ds_read_b64 v[136:137], v42 offset:2048\n"
      "ds_read_b64 v[138:139], v42 offset:2056\n"
      "ds_read_b128 v[168:171], v43 offset:1536\n"
      "ds_read_b64 v[116:117], v42 offset:32\n"
      "ds_read_b64 v[118:119], v42 offset:40\n"
      "ds_read_b64 v[128:129], v42 offset:1056\n"
      "ds_read_b64 v[130:131], v42 offset:1064\n"
      "ds_read_b64 v[140:141], v42 offset:2080\n"
      "ds_read_b64 v[142:143], v42 offset:2088\n"
      "s_waitcnt lgkmcnt(6)\n"
      "v_mfma_f32_16x16x32_bf16 v[96:99], v[112:115], v[164:167], 0\n"
      "ds_read_b128 v[176:179], v43 offset:4640\n"
      "v_mfma_f32_16x16x32_bf16 v[104:107], v[124:127], v[164:167], 0\n"
      "ds_read_b128 v[180:183], v43 offset:3104\n"
      "v_mfma_f32_16x16x32_bf16 v[96:99], v[124:127], v[160:163], v[96:99]\n"
      "ds_read_b128 v[184:187], v43 offset:1568\n"
      "v_mfma_f32_16x16x32_bf16 v[104:107], v[136:139], v[160:163], v[104:107]\n"
      "ds_read_b64 v[120:121], v42 offset:64\n"
      "v_mfma_f32_16x16x32_bf16 v[104:107], v[112:115], v[168:171], v[104:107]\n"
      "ds_read_b64 v[122:123], v42 offset:72\n"
      "s_waitcnt lgkmcnt(5)\n"
      "v_mfma_f32_16x16x32_bf16 v[100:103], v[116:119], v[164:167], 0\n"
      "ds_read_b64 v[132:133], v42 offset:1088\n"
      "v_mfma_f32_16x16x32_bf16 v[108:111], v[128:131], v[164:167], 0\n"
      "ds_read_b64 v[134:135], v42 offset:1096\n"
      "v_mfma_f32_16x16x32_bf16 v[100:103], v[128:131], v[160:163], v[100:103]\n"
      "ds_read_b64 v[144:145], v42 offset:2112\n"
      "v_mfma_f32_16x16x32_bf16 v[108:111], v[140:143], v[160:163], v[108:111]\n"
      "ds_read_b64 v[146:147], v42 offset:2120\n"
      "v_mfma_f32_16x16x32_bf16 v[108:111], v[116:119], v[168:171], v[108:111]\n"
      "s_waitcnt lgkmcnt(6)\n"
      "v_mfma_f32_16x16x32_bf16 v[96:99], v[116:119], v[180:183], v[96:99]\n"
      "ds_read_b128 v[160:163], v43 offset:4704\n"
      "v_mfma_f32_16x16x32_bf16 v[104:107], v[128:131], v[180:183], v[104:107]\n"
      "ds_read_b128 v[164:167], v43 offset:3168\n"
      "v_mfma_f32_16x16x32_bf16 v[96:99], v[128:131], v[176:179], v[96:99]\n"
      "ds_read_b128 v[168:171], v43 offset:1632\n"
      "v_mfma_f32_16x16x32_bf16 v[104:107], v[140:143], v[176:179], v[104:107]\n"
      "ds_read_b64 v[112:113], v42 offset:96\n"
      "v_mfma_f32_16x16x32_bf16 v[104:107], v[116:119], v[184:187], v[104:107]\n"
      "ds_read_b64 v[114:115], v42 offset:104\n"
      "s_waitcnt lgkmcnt(5)\n"
      "v_mfma_f32_16x16x32_bf16 v[100:103], v[120:123], v[180:183], v[100:103]\n"
      "ds_read_b64 v[124:125], v42 offset:1120\n"
      "v_mfma_f32_16x16x32_bf16 v[108:111], v[132:135], v[180:183], v[108:111]\n"
      "ds_read_b64 v[126:127], v42 offset:1128\n"
      "v_mfma_f32_16x16x32_bf16 v[100:103], v[132:135], v[176:179], v[100:103]\n"
      "ds_read_b64 v[136:137], v42 offset:2144\n"
      "v_mfma_f32_16x16x32_bf16 v[108:111], v[144:147], v[176:179], v[108:111]\n"
      "ds_read_b64 v[138:139], v42 offset:2152\n"
      "v_mfma_f32_16x16x32_bf16 v[108:111], v[120:123], v[184:187], v[108:111]\n"
      "s_waitcnt lgkmcnt(6)\n"
      "v_mfma_f32_16x16x32_bf16 v[96:99], v[120:123], v[164:167], v[96:99]\n"
      "ds_read_b128 v[176:179], v43 offset:4736\n"
      "v_mfma_f32_16x16x32_bf16 v[104:107], v[132:135], v[164:167], v[104:107]\n"
      "ds_read_b128 v[180:183], v43 offset:3200\n"
      "v_mfma_f32_16x16x32_bf16 v[96:99], v[132:135], v[160:163], v[96:99]\n"
      "ds_read_b128 v[184:187], v43 offset:1664\n"
      "v_mfma_f32_16x16x32_bf16 v[104:107], v[144:147], v[160:163], v[104:107]\n"
      "ds_read_b64 v[116:117], v42 offset:128\n"
      "v_mfma_f32_16x16x32_bf16 v[104:107], v[120:123], v[168:171], v[104:107]\n"
      "ds_read_b64 v[118:119], v42 offset:136\n"
      "s_waitcnt lgkmcnt(5)\n"
      "v_mfma_f32_16x16x32_bf16 v[100:103], v[112:115], v[164:167], v[100:103]\n"
      "ds_read_b64 v[128:129], v42 offset:1152\n"
      "v_mfma_f32_16x16x32_bf16 v[108:111], v[124:127], v[164:167], v[108:111]\n"
      "ds_read_b64 v[130:131], v42 offset:1160\n"
      "v_mfma_f32_16x16x32_bf16 v[100:103], v[124:127], v[160:163], v[100:103]\n"
      "ds_read_b64 v[140:141], v42 offset:2176\n"
      "v_mfma_f32_16x16x32_bf16 v[108:111], v[136:139], v[160:163], v[108:111]\n"
      "ds_read_b64 v[142:143], v42 offset:2184\n"
      "v_mfma_f32_16x16x32_bf16 v[108:111], v[112:115], v[168:171], v[108:111]\n"
      "s_waitcnt lgkmcnt(6)\n"
      "v_mfma_f32_16x16x32_bf16 v[96:99], v[112:115], v[180:183], v[96:99]\n"
      "ds_read_b128 v[160:163], v41 offset:4608\n"
      "ds_read_b64 v[120:121], v40 offset:0\n"
      "v_mfma_f32_16x16x32_bf16 v[104:107], v[124:127], v[180:183], v[104:107]\n"
      "ds_read_b64 v[122:123], v40 offset:8\n"
      "ds_read_b128 v[164:167], v41 offset:3072\n"
      "v_mfma_f32_16x16x32_bf16 v[96:99], v[124:127], v[176:179], v[96:99]\n"
      "ds_read_b64 v[132:133], v40 offset:1024\n"
      "ds_read_b64 v[134:135], v40 offset:1032\n"
      "v_mfma_f32_16x16x32_bf16 v[104:107], v[136:139], v[176:179], v[104:107]\n"
      "ds_read_b128 v[168:171], v41 offset:1536\n"
      "ds_read_b64 v[144:145], v40 offset:2048\n"
      "v_mfma_f32_16x16x32_bf16 v[104:107], v[112:115], v[184:187], v[104:107]\n"
      "ds_read_b64 v[146:147], v40 offset:2056\n"
      "ds_read_b128 v[172:175], v41 offset:0\n"
      "s_waitcnt lgkmcnt(10)\n"
      "v_mfma_f32_16x16x32_bf16 v[100:103], v[116:119], v[180:183], v[100:103]\n"
      "ds_read_b64 v[156:157], v40 offset:3072\n"
      "v_mfma_f32_16x16x32_bf16 v[108:111], v[128:131], v[180:183], v[108:111]\n"
      "ds_read_b64 v[158:159], v40 offset:3080\n"
      "v_mfma_f32_16x16x32_bf16 v[100:103], v[128:131], v[176:179], v[100:103]\n"
      "v_mfma_f32_16x16x32_bf16 v[108:111], v[140:143], v[176:179], v[108:111]\n"
      "v_mfma_f32_16x16x32_bf16 v[108:111], v[116:119], v[184:187], v[108:111]\n"
      "s_waitcnt lgkmcnt(0)\n"
      "L_cv_e%=:\n"
      "ds_read_b64 v[112:113], v40 offset:32\n"
      "ds_read_b64 v[114:115], v40 offset:40\n"
      "s_add_u32 s8, s26, s91\n"
      "s_addc_u32 s9, s27, 0\n"
      "v_mfma_f32_16x16x32_bf16 v[64:67], v[120:123], v[160:163], 0\n"
      "global_load_lds_dwordx4 v222, s[20:21]\n"
      "ds_read_b64 v[124:125], v40 offset:1056\n"
      "v_mfma_f32_16x16x32_bf16 v[72:75], v[120:123], v[164:167], 0\n"
      "ds_read_b64 v[126:127], v40 offset:1064\n"
      "ds_read_b64 v[136:137], v40 offset:2080\n"
      "v_mfma_f32_16x16x32_bf16 v[80:83], v[120:123], v[168:171], 0\n"
      "ds_read_b64 v[138:139], v40 offset:2088\n"
      "v_mfma_f32_16x16x32_bf16 v[88:91], v[120:123], v[172:175], 0\n"
      "ds_read_b64 v[148:149], v40 offset:3104\n"
      "v_mfma_f32_16x16x32_bf16 v[72:75], v[132:135], v[160:163], v[72:75]\n"
      "ds_read_b64 v[150:151], v40 offset:3112\n"
      "v_mfma_f32_16x16x32_bf16 v[80:83], v[132:135], v[164:167], v[80:83]\n"
      "ds_write_b128 v56, v[96:99]\n"
      "ds_read_b128 v[176:179], v41 offset:4640\n"
      "ds_read_b128 v[180:183], v41 offset:3104\n"
      "ds_read_b128 v[184:187], v41 offset:1568\n"
      "v_mfma_f32_16x16x32_bf16 v[88:91], v[132:135], v[168:171], v[88:91]\n"
      "ds_read_b128 v[188:191], v41 offset:32\n"
      "v_mfma_f32_16x16x32_bf16 v[80:83], v[144:147], v[160:163], v[80:83]\n"
      "ds_read_b64 v[116:117], v40 offset:64\n"
      "v_mfma_f32_16x16x32_bf16 v[88:91], v[144:147], v[164:167], v[88:91]\n"
      "global_load_lds_dwordx4 v222, s[22:23] offset:1024\n"
      "v_mfma_f32_16x16x32_bf16 v[88:91], v[156:159], v[160:163], v[88:91]\n"
      "ds_write_b128 v56, v[104:107] offset:2048\n"
      "ds_read_b64 v[118:119], v40 offset:72\n"
      "ds_read_b64 v[128:129], v40 offset:1088\n"
      "ds_read_b64 v[130:131], v40 offset:1096\n"
      "s_waitcnt lgkmcnt(10)\n"
      "v_mfma_f32_16x16x32_bf16 v[68:71], v[112:115], v[160:163], 0\n"
      "ds_read_b64 v[140:141], v40 offset:2112\n"
      "v_mfma_f32_16x16x32_bf16 v[76:79], v[112:115], v[164:167], 0\n"
      "ds_read_b64 v[142:143], v40 offset:2120\n"
      "v_mfma_f32_16x16x32_bf16 v[84:87], v[112:115], v[168:171], 0\n"
      "ds_read_b64 v[152:153], v40 offset:3136\n"
      "s_load_dwordx16 s[60:75], s[56:57], 0xc0\n"
      "v_mfma_f32_16x16x32_bf16 v[92:95], v[112:115], v[172:175], 0\n"
      "ds_write_b128 v62, v[100:103]\n"
      "ds_read_b64 v[154:155], v40 offset:3144\n"
      "s_load_dwordx4 s[92:95], s[56:57], 0xa0\n"
      "v_mfma_f32_16x16x32_bf16 v[76:79], v[124:127], v[160:163], v[76:79]\n"
      "v_mfma_f32_16x16x32_bf16 v[84:87], v[124:127], v[164:167], v[84:87]\n"
      "v_mfma_f32_16x16x32_bf16 v[92:95], v[124:127], v[168:171], v[92:95]\n"
      "v_mfma_f32_16x16x32_bf16 v[84:87], v[136:139], v[160:163], v[84:87]\n"
      "ds_write_b128 v62, v[108:111] offset:2048\n"
      "v_mfma_f32_16x16x32_bf16 v[92:95], v[136:139], v[164:167], v[92:95]\n"
      "v_mfma_f32_16x16x32_bf16 v[92:95], v[148:151], v[160:163], v[92:95]\n"
      "s_waitcnt lgkmcnt(11)\n"
      "v_mfma_f32_16x16x32_bf16 v[64:67], v[112:115], v[176:179], v[64:67]\n"
      "buffer_load_dwordx2 v[254:255], v2, s[32:35], s45 offen sc1\n"
      "v_mfma_f32_16x16x32_bf16 v[72:75], v[112:115], v[180:183], v[72:75]\n"
      "ds_read_b128 v[160:163], v41 offset:4704\n"
      "v_mfma_f32_16x16x32_bf16 v[80:83], v[112:115], v[184:187], v[80:83]\n"
      "ds_read_b128 v[164:167], v41 offset:3168\n"
      "v_mfma_f32_16x16x32_bf16 v[88:91], v[112:115], v[188:191], v[88:91]\n"
      "ds_read_b128 v[168:171], v41 offset:1632\n"
      "v_mfma_f32_16x16x32_bf16 v[72:75], v[124:127], v[176:179], v[72:75]\n"
      "ds_read_b128 v[172:175], v41 offset:96\n"
      "v_mfma_f32_16x16x32_bf16 v[80:83], v[124:127], v[180:183], v[80:83]\n"
      "ds_read_b64 v[120:121], v40 offset:96\n"
      "v_mfma_f32_16x16x32_bf16 v[88:91], v[124:127], v[184:187], v[88:91]\n"
      "s_waitcnt lgkmcnt(5)\n"
      "s_barrier\n"
      "v_mfma_f32_16x16x32_bf16 v[80:83], v[136:139], v[176:179], v[80:83]\n"
      "ds_read_b128 v[96:99], v45 offset:0\n"
      "v_mfma_f32_16x16x32_bf16 v[88:91], v[136:139], v[180:183], v[88:91]\n"
      "ds_read_b128 v[100:103], v45 offset:4096\n"
      "v_mfma_f32_16x16x32_bf16 v[88:91], v[148:151], v[176:179], v[88:91]\n"
      "ds_read_b128 v[104:107], v45 offset:8192\n"
      "v_mfma_f32_16x16x32_bf16 v[68:71], v[116:119], v[176:179], v[68:71]\n"
      "ds_read_b128 v[108:111], v45 offset:12288\n"
      "v_mfma_f32_16x16x32_bf16 v[76:79], v[116:119], v[180:183], v[76:79]\n"
      "ds_read_b64 v[202:203], v47\n"
      "v_mfma_f32_16x16x32_bf16 v[84:87], v[116:119], v[184:187], v[84:87]\n"
      "ds_read_b64 v[122:123], v40 offset:104\n"
      "v_mfma_f32_16x16x32_bf16 v[92:95], v[116:119], v[188:191], v[92:95]\n"
      "ds_read_b64 v[132:133], v40 offset:1120\n"
      "v_mfma_f32_16x16x32_bf16 v[76:79], v[128:131], v[176:179], v[76:79]\n"
      "ds_read_b64 v[134:135], v40 offset:1128\n"
      "v_mfma_f32_16x16x32_bf16 v[84:87], v[128:131], v[180:183], v[84:87]\n"
      "ds_read_b64 v[144:145], v40 offset:2144\n"
      "v_mfma_f32_16x16x32_bf16 v[92:95], v[128:131], v[184:187], v[92:95]\n"
      "ds_read_b64 v[146:147], v40 offset:2152\n"
      "s_waitcnt lgkmcnt(5)\n"
      "v_mfma_f32_16x16x32_bf16 v[84:87], v[140:143], v[176:179], v[84:87]\n"
      "ds_read_b64 v[156:157], v40 offset:3168\n"
      "v_add_f32 v96, v96, v100\n"
      "v_mfma_f32_16x16x32_bf16 v[92:95], v[140:143], v[180:183], v[92:95]\n"
      "ds_read_b64 v[158:159], v40 offset:3176\n"
      "v_add_f32 v97, v97, v101\n"
      "v_mfma_f32_16x16x32_bf16 v[92:95], v[152:155], v[176:179], v[92:95]\n"
      "v_add_f32 v98, v98, v102\n"
      "v_mfma_f32_16x16x32_bf16 v[64:67], v[116:119], v[160:163], v[64:67]\n"
      "ds_read_b128 v[176:179], v41 offset:4736\n"
      "v_add_f32 v99, v99, v103\n"
      "v_mfma_f32_16x16x32_bf16 v[72:75], v[116:119], v[164:167], v[72:75]\n"
      "ds_read_b128 v[180:183], v41 offset:3200\n"
      "v_add_f32 v104, v104, v108\n"
      "v_mfma_f32_16x16x32_bf16 v[80:83], v[116:119], v[168:171], v[80:83]\n"
      "ds_read_b128 v[184:187], v41 offset:1664\n"
      "v_add_f32 v105, v105, v109\n"
      "v_mfma_f32_16x16x32_bf16 v[88:91], v[116:119], v[172:175], v[88:91]\n"
      "ds_read_b128 v[188:191], v41 offset:128\n"
      "v_add_f32 v106, v106, v110\n"
      "v_mfma_f32_16x16x32_bf16 v[72:75], v[128:131], v[160:163], v[72:75]\n"
      "ds_read_b64 v[112:113], v40 offset:128\n"
      "v_add_f32 v107, v107, v111\n"
      "v_mfma_f32_16x16x32_bf16 v[80:83], v[128:131], v[164:167], v[80:83]\n"
      "ds_read_b64 v[114:115], v40 offset:136\n"
      "v_add_f32 v96, v96, v104\n"
      "v_mfma_f32_16x16x32_bf16 v[88:91], v[128:131], v[168:171], v[88:91]\n"
      "buffer_load_dwordx2 v[200:201], v4, s[32:35], s80 offen offset:0 sc1\n"
      "v_add_f32 v97, v97, v105\n"
      "v_mfma_f32_16x16x32_bf16 v[80:83], v[140:143], v[160:163], v[80:83]\n"
      "ds_read_b64 v[124:125], v40 offset:1152\n"
      "v_add_f32 v98, v98, v106\n"
      "v_mfma_f32_16x16x32_bf16 v[88:91], v[140:143], v[164:167], v[88:91]\n"
      "buffer_load_dword v250, v61, s[32:35], s85 offen offset:0 sc1\n"
      "v_add_f32 v99, v99, v107\n"
      "v_mfma_f32_16x16x32_bf16 v[88:91], v[152:155], v[160:163], v[88:91]\n"
      "ds_read_b64 v[126:127], v40 offset:1160\n"
      "s_waitcnt lgkmcnt(8)\n"
      "v_mfma_f32_16x16x32_bf16 v[68:71], v[120:123], v[160:163], v[68:71]\n"
      "buffer_load_dwordx4 v[224:227], v57, s[32:35], s82 offen offset:0 sc1\n"
      "v_cvt_pk_bf16_f32 v96, 0, v96\n"
      "v_mfma_f32_16x16x32_bf16 v[76:79], v[120:123], v[164:167], v[76:79]\n"
      "ds_read_b64 v[136:137], v40 offset:2176\n"
      "v_cvt_pk_bf16_f32 v97, 0, v97\n"
      "v_mfma_f32_16x16x32_bf16 v[84:87], v[120:123], v[168:171], v[84:87]\n"
      "ds_read_b64 v[138:139], v40 offset:2184\n"
      "v_cvt_pk_bf16_f32 v98, 0, v98\n"
      "v_mfma_f32_16x16x32_bf16 v[92:95], v[120:123], v[172:175], v[92:95]\n"
      "ds_read_b64 v[148:149], v40 offset:3200\n"
      "v_cvt_pk_bf16_f32 v99, 0, v99\n"
      "v_mfma_f32_16x16x32_bf16 v[76:79], v[132:135], v[160:163], v[76:79]\n"
      "buffer_load_dwordx4 v[228:231], v57, s[32:35], s82 offen offset:1024 sc1\n"
      "v_and_b32 v100, 0xffff0000, v203\n"
      "v_mfma_f32_16x16x32_bf16 v[84:87], v[132:135], v[164:167], v[84:87]\n"
      "ds_read_b64 v[150:151], v40 offset:3208\n"
      "v_lshlrev_b32 v101, 16, v203\n"
      "v_mfma_f32_16x16x32_bf16 v[92:95], v[132:135], v[168:171], v[92:95]\n"
      "s_lshr_b32 s58, s3, 1\n"
      "s_mul_i32 s58, s58, 0x3f800000\n"
      "v_mfma_f32_16x16x32_bf16 v[84:87], v[144:147], v[160:163], v[84:87]\n"
      "v_and_b32 v102, 0xffff0000, v202\n"
      "v_lshlrev_b32 v103, 16, v202\n"
      "v_mfma_f32_16x16x32_bf16 v[92:95], v[144:147], v[164:167], v[92:95]\n"
      "buffer_load_dwordx4 v[192:195], v54, s[32:35], s53 offen offset:0 sc1\n"
      "v_mul_f32 v100, s25, v100\n"
      "v_mfma_f32_16x16x32_bf16 v[92:95], v[156:159], v[160:163], v[92:95]\n"
      "v_mul_f32 v101, s25, v101\n"
      "s_waitcnt lgkmcnt(8)\n"
      "v_mfma_f32_16x16x32_bf16 v[64:67], v[120:123], v[176:179], v[64:67]\n"
      "s_waitcnt vmcnt(5)\n"
      "v_cmp_ne_u32 vcc, s30, v255\n"
      "v_mfma_f32_16x16x32_bf16 v[72:75], v[120:123], v[180:183], v[72:75]\n"
      "ds_read_b128 v[160:163], v41 offset:4800\n"
      "v_mul_f32 v102, s25, v102\n"
      "v_mfma_f32_16x16x32_bf16 v[80:83], v[120:123], v[184:187], v[80:83]\n"
      "buffer_load_dwordx4 v[196:199], v54, s[32:35], s54 offen offset:0 sc1\n"
      "s_cbranch_vccnz L_cm_s%=\n"
      "L_cm_r%=:\n"
      "v_add_f32_dpp v254, v254, v254 quad_perm:[1,0,3,2] row_mask:0xf bank_mask:0xf\n"
      "v_mfma_f32_16x16x32_bf16 v[88:91], v[120:123], v[188:191], v[88:91]\n"
      "ds_read_b128 v[164:167], v41 offset:3264\n"
      "v_mul_f32 v103, s25, v103\n"
      "v_mfma_f32_16x16x32_bf16 v[72:75], v[132:135], v[176:179], v[72:75]\n"
      "ds_read_b128 v[168:171], v41 offset:1728\n"
      "v_add_f32_dpp v254, v254, v254 quad_perm:[2,3,0,1] row_mask:0xf bank_mask:0xf\n"
      "v_mfma_f32_16x16x32_bf16 v[80:83], v[132:135], v[180:183], v[80:83]\n"
      "ds_read_b128 v[172:175], v41 offset:192\n"
      "v_cvt_pk_bf16_f32 v254, 0, v254\n"
      "v_mfma_f32_16x16x32_bf16 v[88:91], v[132:135], v[184:187], v[88:91]\n"
      "buffer_load_dword v248, v60, s[32:35], s84 offen offset:0 sc1\n"
      "v_add_f32 v254, s58, v254\n"
      "v_mfma_f32_16x16x32_bf16 v[80:83], v[144:147], v[176:179], v[80:83]\n"
      "ds_read_b64 v[116:117], v40 offset:160\n"
      "v_cvt_pk_bf16_f32 v254, v254, v254\n"
      "v_mfma_f32_16x16x32_bf16 v[88:91], v[144:147], v[180:183], v[88:91]\n"
      "ds_write_b16 v3, v254\n"
      "ds_read_b64 v[118:119], v40 offset:168\n"
      "v_cvt_pk_bf16_f32 v100, 0, v100\n"
      "v_mfma_f32_16x16x32_bf16 v[88:91], v[156:159], v[176:179], v[88:91]\n"
      "ds_read_b64 v[128:129], v40 offset:1184\n"
      "s_waitcnt lgkmcnt(8)\n"
      "v_mfma_f32_16x16x32_bf16 v[68:71], v[112:115], v[176:179], v[68:71]\n"
      "buffer_load_dword v249, v60, s[32:35], s84 offen offset:2048 sc1\n"
      "v_cvt_pk_bf16_f32 v101, 0, v101\n"
      "v_mfma_f32_16x16x32_bf16 v[76:79], v[112:115], v[180:183], v[76:79]\n"
      "ds_read_b64 v[130:131], v40 offset:1192\n"
      "v_cvt_pk_bf16_f32 v102, 0, v102\n"
      "v_mfma_f32_16x16x32_bf16 v[84:87], v[112:115], v[184:187], v[84:87]\n"
      "ds_read_b64 v[140:141], v40 offset:2208\n"
      "v_cvt_pk_bf16_f32 v103, 0, v103\n"
      "v_mfma_f32_16x16x32_bf16 v[92:95], v[112:115], v[188:191], v[92:95]\n"
      "ds_read_b64 v[142:143], v40 offset:2216\n"
      "s_waitcnt lgkmcnt(5)\n"
      "s_barrier\n"
      "v_mfma_f32_16x16x32_bf16 v[76:79], v[124:127], v[176:179], v[76:79]\n"
      "buffer_load_dwordx2 v[252:253], v51, s[32:35], s76 offen offset:0 sc1\n"
      "ds_read2_b64 v[24:27], v236 offset0:0 offset1:4\n"
      "v_add_f32 v96, v96, v100\n"
      "v_mfma_f32_16x16x32_bf16 v[84:87], v[124:127], v[180:183], v[84:87]\n"
      "ds_read_b64 v[152:153], v40 offset:3232\n"
      "v_add_f32 v97, v97, v101\n"
      "v_mfma_f32_16x16x32_bf16 v[92:95], v[124:127], v[184:187], v[92:95]\n"
      "ds_read_b64 v[154:155], v40 offset:3240\n"
      "v_add_f32 v98, v98, v102\n"
      "v_mfma_f32_16x16x32_bf16 v[84:87], v[136:139], v[176:179], v[84:87]\n"
      "s_mov_b32 s48, 0\n"
      "s_mov_b32 s49, -1\n"
      "v_mfma_f32_16x16x32_bf16 v[92:95], v[136:139], v[180:183], v[92:95]\n"
      "v_add_f32 v99, v99, v103\n"
      "v_cvt_pk_bf16_f32 v96, 0, v96\n"
      "v_mfma_f32_16x16x32_bf16 v[92:95], v[148:151], v[176:179], v[92:95]\n"
      "s_waitcnt lgkmcnt(2)\n"
      "v_mfma_f32_16x16x32_bf16 v[64:67], v[112:115], v[160:163], v[64:67]\n"
      "buffer_load_dwordx4 v[208:211], v55, s[32:35], 0 offen offset:0 sc1\n"
      "v_mfma_f32_16x16x32_bf16 v[232:235], v[24:27], v[8:11], 0\n"
      "v_mfma_f32_16x16x32_bf16 v[72:75], v[112:115], v[164:167], v[72:75]\n"
      "ds_read_b128 v[176:179], v41 offset:4832\n"
      "v_mfma_f32_16x16x32_bf16 v[8:11], v[24:27], v[12:15], 0\n"
      "v_mfma_f32_16x16x32_bf16 v[80:83], v[112:115], v[168:171], v[80:83]\n"
      "ds_read_b128 v[180:183], v41 offset:3296\n"
      "v_mfma_f32_16x16x32_bf16 v[12:15], v[24:27], v[16:19], 0\n"
      "v_mfma_f32_16x16x32_bf16 v[88:91], v[112:115], v[172:175], v[88:91]\n"
      "ds_read_b128 v[184:187], v41 offset:1760\n"
      "v_mfma_f32_16x16x32_bf16 v[16:19], v[24:27], v[20:23], 0\n"
      "v_mfma_f32_16x16x32_bf16 v[72:75], v[124:127], v[160:163], v[72:75]\n"
      "buffer_load_dwordx4 v[212:215], v55, s[32:35], 0 offen offset:1024 sc1\n"
      "v_cvt_pk_bf16_f32 v97, 0, v97\n"
      "v_mfma_f32_16x16x32_bf16 v[80:83], v[124:127], v[164:167], v[80:83]\n"
      "ds_read_b128 v[188:191], v41 offset:224\n"
      "v_cvt_pk_bf16_f32 v98, 0, v98\n"
      "v_mfma_f32_16x16x32_bf16 v[88:91], v[124:127], v[168:171], v[88:91]\n"
      "v_cndmask_b32 v206, v232, v12, s[48:49]\n"
      "v_cndmask_b32 v207, v8, v16, s[48:49]\n"
      "v_mfma_f32_16x16x32_bf16 v[80:83], v[136:139], v[160:163], v[80:83]\n"
      "ds_read_b64 v[120:121], v40 offset:192\n"
      "v_cvt_pk_bf16_f32 v99, 0, v99\n"
      "v_mfma_f32_16x16x32_bf16 v[88:91], v[136:139], v[164:167], v[88:91]\n"
      "buffer_load_dwordx4 v[216:219], v55, s[32:35], 0 offen offset:2048 sc1\n"
      "v_mfma_f32_16x16x32_bf16 v[88:91], v[148:151], v[160:163], v[88:91]\n"
      "ds_read_b64 v[122:123], v40 offset:200\n"
      "s_waitcnt lgkmcnt(6)\n"
      "v_mfma_f32_16x16x32_bf16 v[68:71], v[116:119], v[160:163], v[68:71]\n"
      "ds_read_b64 v[132:133], v40 offset:1216\n"
      "v_mfma_f32_16x16x32_bf16 v[76:79], v[116:119], v[164:167], v[76:79]\n"
      "ds_read_b64 v[134:135], v40 offset:1224\n"
      "v_mfma_f32_16x16x32_bf16 v[84:87], v[116:119], v[168:171], v[84:87]\n"
      "buffer_load_dwordx4 v[220:223], v55, s[32:35], 0 offen offset:3072 sc1\n"
      "v_mfma_f32_16x16x32_bf16 v[92:95], v[116:119], v[172:175], v[92:95]\n"
      "ds_read_b64 v[144:145], v40 offset:2240\n"
      "v_mfma_f32_16x16x32_bf16 v[76:79], v[128:131], v[160:163], v[76:79]\n"
      "ds_read_b64 v[146:147], v40 offset:2248\n"
      "v_mfma_f32_16x16x32_bf16 v[84:87], v[128:131], v[164:167], v[84:87]\n"
      "ds_read_b64 v[156:157], v40 offset:3264\n"
      "v_mfma_f32_16x16x32_bf16 v[92:95], v[128:131], v[168:171], v[92:95]\n"
      "buffer_load_dwordx2 v[204:205], v58, s[32:35], s83 offen offset:0 sc1\n"
      "v_mfma_f32_16x16x32_bf16 v[84:87], v[140:143], v[160:163], v[84:87]\n"
      "ds_read_b64 v[158:159], v40 offset:3272\n"
      "v_mfma_f32_16x16x32_bf16 v[92:95], v[140:143], v[164:167], v[92:95]\n"
      "v_mfma_f32_16x16x32_bf16 v[92:95], v[152:155], v[160:163], v[92:95]\n"
      "s_waitcnt lgkmcnt(8)\n"
      "v_mfma_f32_16x16x32_bf16 v[64:67], v[116:119], v[176:179], v[64:67]\n"
      "ds_read_b128 v[160:163], v41 offset:4896\n"
      "v_mfma_f32_16x16x32_bf16 v[72:75], v[116:119], v[180:183], v[72:75]\n"
      "ds_read_b128 v[164:167], v41 offset:3360\n"
      "v_mfma_f32_16x16x32_bf16 v[80:83], v[116:119], v[184:187], v[80:83]\n"
      "ds_read_b128 v[168:171], v41 offset:1824\n"
      "v_mfma_f32_16x16x32_bf16 v[88:91], v[116:119], v[188:191], v[88:91]\n"
      "ds_read_b128 v[172:175], v41 offset:288\n"
      "v_mfma_f32_16x16x32_bf16 v[72:75], v[128:131], v[176:179], v[72:75]\n"
      "ds_read_b64 v[112:113], v40 offset:224\n"
      "v_mfma_f32_16x16x32_bf16 v[80:83], v[128:131], v[180:183], v[80:83]\n"
      "ds_read_b64 v[114:115], v40 offset:232\n"
      "v_mfma_f32_16x16x32_bf16 v[88:91], v[128:131], v[184:187], v[88:91]\n"
      "ds_read_b64 v[124:125], v40 offset:1248\n"
      "s_waitcnt vmcnt(5)\n"
      "v_mfma_f32_16x16x32_bf16 v[80:83], v[140:143], v[176:179], v[80:83]\n"
      "ds_read_b64 v[126:127], v40 offset:1256\n"
      "v_cmp_ne_u64 vcc, s[30:31], v[252:253]\n"
      "v_mfma_f32_16x16x32_bf16 v[88:91], v[140:143], v[180:183], v[88:91]\n"
      "ds_read_b64 v[136:137], v40 offset:2272\n"
      "v_mfma_f32_16x16x32_bf16 v[88:91], v[152:155], v[176:179], v[88:91]\n"
      "s_cbranch_vccnz L_cw_s%=\n"
      "L_cw_r%=:\n"
      "s_waitcnt lgkmcnt(9)\n"
      "v_mfma_f32_16x16x32_bf16 v[68:71], v[120:123], v[176:179], v[68:71]\n"
      "ds_read_b64 v[138:139], v40 offset:2280\n"
      "v_add_u32 v35, 4096, v7\n"
      "v_mfma_f32_16x16x32_bf16 v[76:79], v[120:123], v[180:183], v[76:79]\n"
      "ds_read_b64 v[148:149], v40 offset:3296\n"
      "v_mfma_f32_16x16x32_bf16 v[84:87], v[120:123], v[184:187], v[84:87]\n"
      "global_load_dwordx4 a[0:3], v35, s[8:9] offset:-4096 sc1\n"
      "v_mfma_f32_16x16x32_bf16 v[92:95], v[120:123], v[188:191], v[92:95]\n"
      "ds_read_b64 v[150:151], v40 offset:3304\n"
      "v_mfma_f32_16x16x32_bf16 v[76:79], v[132:135], v[176:179], v[76:79]\n"
      "v_mfma_f32_16x16x32_bf16 v[84:87], v[132:135], v[180:183], v[84:87]\n"
      "v_mfma_f32_16x16x32_bf16 v[92:95], v[132:135], v[184:187], v[92:95]\n"
      "global_load_dwordx4 a[4:7], v35, s[8:9] offset:-3072 sc1\n"
      "v_mfma_f32_16x16x32_bf16 v[84:87], v[144:147], v[176:179], v[84:87]\n"
      "v_mfma_f32_16x16x32_bf16 v[92:95], v[144:147], v[180:183], v[92:95]\n"
      "s_waitcnt vmcnt(2)\n"
      "v_add_u32 v251, v200, v201\n"
      "v_mfma_f32_16x16x32_bf16 v[92:95], v[156:159], v[176:179], v[92:95]\n"
      "v_add3_u32 v34, v192, v193, v194\n"
      "v_add3_u32 v34, v34, v195, v196\n"
      "v_add_u32_dpp v251, v251, v251 row_half_mirror row_mask:0xf bank_mask:0xf\n"
      "v_add3_u32 v34, v34, v197, v198\n"
      "s_waitcnt lgkmcnt(8)\n"
      "v_mfma_f32_16x16x32_bf16 v[64:67], v[120:123], v[160:163], v[64:67]\n"
      "global_load_dwordx4 a[8:11], v35, s[8:9] offset:-2048 sc1\n"
      "v_add3_u32 v251, v251, v34, v199\n"
      "v_add3_u32 v34, v248, v249, v250\n"
      "v_add3_u32 v34, v34, v211, v215\n"
      "v_add_u32_dpp v251, v251, v251 quad_perm:[1,0,3,2] row_mask:0xf bank_mask:0xf\n"
      "v_mfma_f32_16x16x32_bf16 v[72:75], v[120:123], v[164:167], v[72:75]\n"
      "ds_read_b128 v[176:179], v41 offset:4928\n"
      "v_add3_u32 v34, v34, v219, v223\n"
      "v_add3_u32 v34, v34, v227, v231\n"
      "v_add_u32_dpp v251, v251, v251 quad_perm:[2,3,0,1] row_mask:0xf bank_mask:0xf\n"
      "v_add3_u32 v251, v251, v34, v205\n"
      "v_mfma_f32_16x16x32_bf16 v[80:83], v[120:123], v[168:171], v[80:83]\n"
      "ds_read_b128 v[180:183], v41 offset:3392\n"
      "v_cmp_ne_u32 vcc, s24, v251\n"
      "v_mfma_f32_16x16x32_bf16 v[88:91], v[120:123], v[172:175], v[88:91]\n"
      "ds_read_b128 v[184:187], v41 offset:1856\n"
      "v_mfma_f32_16x16x32_bf16 v[72:75], v[132:135], v[160:163], v[72:75]\n"
      "global_load_dwordx4 a[12:15], v35, s[8:9] offset:-1024 sc1\n"
      "s_cbranch_vccnz L_cv_s%=\n"
      "L_cv_r%=:\n"
      "v_add_f32_dpp v204, v204, v204 quad_perm:[1,0,3,2] row_mask:0xf bank_mask:0xf\n"
      "v_lshlrev_b32 v100, 16, v200\n"
      "v_and_b32 v101, 0xffff0000, v200\n"
      "v_add_f32_dpp v204, v204, v204 quad_perm:[2,3,0,1] row_mask:0xf bank_mask:0xf\n"
      "v_mfma_f32_16x16x32_bf16 v[80:83], v[132:135], v[164:167], v[80:83]\n"
      "ds_read_b128 v[188:191], v41 offset:320\n"
      "v_cvt_pk_bf16_f32 v204, 0, v204\n"
      "v_lshlrev_b32 v102, 16, v201\n"
      "v_add_f32 v205, 1.0, v204\n"
      "v_mfma_f32_16x16x32_bf16 v[88:91], v[132:135], v[168:171], v[88:91]\n"
      "ds_read_b64 v[116:117], v40 offset:256\n"
      "v_and_b32 v103, 0xffff0000, v201\n"
      "v_cvt_pk_bf16_f32 v205, 0, v205\n"
      "v_lshlrev_b32 v104, 16, v226\n"
      "v_mfma_f32_16x16x32_bf16 v[80:83], v[144:147], v[160:163], v[80:83]\n"
      "ds_read_b64 v[118:119], v40 offset:264\n"
      "v_readlane_b32 s40, v204, 32\n"
      "v_and_b32 v105, 0xffff0000, v226\n"
      "v_readlane_b32 s41, v205, 36\n"
      "v_lshlrev_b32 v106, 16, v230\n"
      "v_mfma_f32_16x16x32_bf16 v[88:91], v[144:147], v[164:167], v[88:91]\n"
      "global_load_dwordx4 a[16:19], v35, s[8:9] offset:0 sc1\n"
      "ds_read_b64_tr_b16 v[240:241], v28 offset:0\n"
      "ds_read_b64_tr_b16 v[242:243], v28 offset:1024\n"
      "ds_read_b64_tr_b16 v[244:245], v28 offset:32\n"
      "ds_read_b64_tr_b16 v[246:247], v28 offset:1056\n"
      "v_readlane_b32 s42, v204, 40\n"
      "v_and_b32 v107, 0xffff0000, v230\n"
      "v_readlane_b32 s43, v205, 44\n"
      "v_mfma_f32_16x16x32_bf16 v[88:91], v[156:159], v[160:163], v[88:91]\n"
      "ds_read_b64 v[128:129], v40 offset:1280\n"
      "v_sub_f32 v104, v100, v104\n"
      "v_sub_f32 v105, v101, v105\n"
      "v_sub_f32 v106, v102, v106\n"
      "s_waitcnt lgkmcnt(11)\n"
      "v_mfma_f32_16x16x32_bf16 v[68:71], v[112:115], v[160:163], v[68:71]\n"
      "ds_read_b64 v[130:131], v40 offset:1288\n"
      "v_sub_f32 v107, v103, v107\n"
      "v_cvt_pk_bf16_f32 v104, 0, v104\n"
      "v_cvt_pk_bf16_f32 v105, 0, v105\n"
      "v_mfma_f32_16x16x32_bf16 v[76:79], v[112:115], v[164:167], v[76:79]\n"
      "ds_read_b64 v[140:141], v40 offset:2304\n"
      "v_cvt_pk_bf16_f32 v106, 0, v106\n"
      "v_cvt_pk_bf16_f32 v107, 0, v107\n"
      "v_mul_f32 v104, v104, v224\n"
      "v_mul_f32 v105, v105, v225\n"
      "v_mfma_f32_16x16x32_bf16 v[84:87], v[112:115], v[168:171], v[84:87]\n"
      "global_load_dwordx4 a[20:23], v35, s[8:9] offset:1024 sc1\n"
      "ds_read_b64 v[142:143], v40 offset:2312\n"
      "v_mul_f32 v106, v106, v228\n"
      "v_mul_f32 v107, v107, v229\n"
      "v_cvt_pk_bf16_f32 v104, 0, v104\n"
      "v_mfma_f32_16x16x32_bf16 v[92:95], v[112:115], v[172:175], v[92:95]\n"
      "ds_read_b64 v[152:153], v40 offset:3328\n"
      "v_cvt_pk_bf16_f32 v105, 0, v105\n"
      "v_cvt_pk_bf16_f32 v106, 0, v106\n"
      "v_cvt_pk_bf16_f32 v107, 0, v107\n"
      "v_mfma_f32_16x16x32_bf16 v[76:79], v[124:127], v[160:163], v[76:79]\n"
      "ds_read_b64 v[154:155], v40 offset:3336\n"
      "v_mul_f32 v104, s41, v104\n"
      "v_mul_f32 v105, s41, v105\n"
      "v_mul_f32 v106, s41, v106\n"
      "v_mul_f32 v107, s41, v107\n"
      "v_mfma_f32_16x16x32_bf16 v[84:87], v[124:127], v[164:167], v[84:87]\n"
      "v_cvt_pk_bf16_f32 v104, 0, v104\n"
      "v_cvt_pk_bf16_f32 v105, 0, v105\n"
      "v_cvt_pk_bf16_f32 v106, 0, v106\n"
      "v_mfma_f32_16x16x32_bf16 v[92:95], v[124:127], v[168:171], v[92:95]\n"
      "global_load_dwordx4 a[24:27], v35, s[8:9] offset:2048 sc1\n"
      "v_cvt_pk_bf16_f32 v107, 0, v107\n"
      "v_add_f32 v104, s40, v104\n"
      "v_add_f32 v105, s40, v105\n"
      "s_waitcnt lgkmcnt(6)\n"
      "v_mfma_f32_16x16x32_bf16 v[84:87], v[136:139], v[160:163], v[84:87]\n"
      "v_add_f32 v106, s40, v106\n"
      "v_add_f32 v107, s40, v107\n"
      "v_cvt_pk_bf16_f32 v104, 0, v104\n"
      "v_mfma_f32_16x16x32_bf16 v[92:95], v[136:139], v[164:167], v[92:95]\n"
      "v_cvt_pk_bf16_f32 v105, 0, v105\n"
      "v_cvt_pk_bf16_f32 v106, 0, v106\n"
      "v_cvt_pk_bf16_f32 v107, 0, v107\n"
      "v_mul_f32 v104, v104, v96\n"
      "v_mfma_f32_16x16x32_bf16 v[92:95], v[148:151], v[160:163], v[92:95]\n"
      "v_mul_f32 v105, v105, v97\n"
      "v_mul_f32 v106, v106, v98\n"
      "v_mul_f32 v107, v107, v99\n"
      "s_waitcnt lgkmcnt(0)\n"
      "v_mfma_f32_16x16x32_bf16 v[64:67], v[112:115], v[176:179], v[64:67]\n"
      "buffer_store_dwordx4 v[240:243], v7, s[32:35], s86 offen sc1\n"
      "v_cvt_pk_bf16_f32 v104, 0, v104\n"
      "v_cvt_pk_bf16_f32 v105, 0, v105\n"
      "v_cvt_pk_bf16_f32 v106, 0, v106\n"
      "v_mfma_f32_16x16x32_bf16 v[68:71], v[116:119], v[176:179], v[68:71]\n"
      "v_cvt_pk_bf16_f32 v107, 0, v107\n"
      "v_mul_f32 v104, s42, v104\n"
      "L_pg1_%=:\n"
      "s_cbranch_execz L_pg5_%=\n"
      "v_mul_f32 v105, s42, v105\n"
      "v_mul_f32 v106, s42, v106\n"
      "v_mfma_f32_16x16x32_bf16 v[72:75], v[112:115], v[180:183], v[72:75]\n"
      "v_mul_f32 v107, s42, v107\n"
      "v_cvt_pk_bf16_f32 v104, 0, v104\n"
      "v_cvt_pk_bf16_f32 v105, 0, v105\n"
      "v_mfma_f32_16x16x32_bf16 v[72:75], v[124:127], v[176:179], v[72:75]\n"
      "ds_write_b128 v44, v[64:67] offset:0\n"
      "v_cvt_pk_bf16_f32 v106, 0, v106\n"
      "v_cvt_pk_bf16_f32 v107, 0, v107\n"
      "v_add_f32 v104, v104, v100\n"
      "v_add_f32 v105, v105, v101\n"
      "v_mfma_f32_16x16x32_bf16 v[76:79], v[116:119], v[180:183], v[76:79]\n"
      "buffer_store_dwordx4 v[244:247], v7, s[32:35], s87 offen sc1\n"
      "global_load_dwordx4 a[28:31], v35, s[8:9] offset:3072 sc1\n"
      "ds_write_b128 v59, v[68:71] offset:0\n"
      "v_add_f32 v106, v106, v102\n"
      "v_add_f32 v107, v107, v103\n"
      "v_cvt_pk_bf16_f32 v104, 0, v104\n"
      "v_cvt_pk_bf16_f32 v105, 0, v105\n"
      "v_mfma_f32_16x16x32_bf16 v[76:79], v[128:131], v[176:179], v[76:79]\n"
      "v_cvt_pk_bf16_f32 v106, 0, v106\n"
      "v_cvt_pk_bf16_f32 v107, 0, v107\n"
      "v_mfma_f32_16x16x32_bf16 v[80:83], v[112:115], v[184:187], v[80:83]\n"
      "buffer_store_dword v206, v239, s[32:35], s59 offen offset:0 sc1\n"
      "buffer_store_dword v207, v239, s[32:35], s59 offen offset:128 sc1\n"
      "ds_write_b128 v44, v[72:75] offset:2048\n"
      "v_cvt_pk_bf16_f32 v108, v104, v105\n"
      "v_cvt_pk_bf16_f32 v109, v106, v107\n"
      "v_mul_f32 v104, s43, v104\n"
      "v_add_u32 v35, 12288, v7\n"
      "v_mfma_f32_16x16x32_bf16 v[80:83], v[124:127], v[180:183], v[80:83]\n"
      "global_load_dwordx4 a[32:35], v35, s[8:9] offset:-4096 sc1\n"
      "v_mul_f32 v105, s43, v105\n"
      "v_mul_f32 v106, s43, v106\n"
      "v_mul_f32 v107, s43, v107\n"
      "v_mfma_f32_16x16x32_bf16 v[80:83], v[136:139], v[176:179], v[80:83]\n"
      "ds_write_b128 v59, v[76:79] offset:2048\n"
      "ds_read_b128 v[232:235], v46\n"
      "v_cvt_pk_bf16_f32 v110, v104, v105\n"
      "v_cvt_pk_bf16_f32 v111, v106, v107\n"
      "v_add_u32 v96, v108, v109\n"
      "v_mfma_f32_16x16x32_bf16 v[84:87], v[116:119], v[184:187], v[84:87]\n"
      "ds_read_b128 v[236:239], v46 offset:7680\n"
      "v_add_u32 v97, v110, v111\n"
      "v_mfma_f32_16x16x32_bf16 v[84:87], v[128:131], v[180:183], v[84:87]\n"
      "buffer_store_dwordx2 v[110:111], v50, s[32:35], s78 offen sc1\n"
      "global_load_dwordx4 a[36:39], v35, s[8:9] offset:-3072 sc1\n"
      "v_add_u32_dpp v96, v96, v96 quad_perm:[1,0,3,2] row_mask:0xf bank_mask:0xf\n"
      "v_add_u32_dpp v97, v97, v97 quad_perm:[1,0,3,2] row_mask:0xf bank_mask:0xf\n"
      "s_add_u32 s4, s26, s71\n"
      "s_addc_u32 s5, s27, 0\n"
      "v_mfma_f32_16x16x32_bf16 v[84:87], v[140:143], v[176:179], v[84:87]\n"
      "ds_write_b128 v44, v[80:83] offset:4096\n"
      "v_add_u32_dpp v96, v96, v96 quad_perm:[2,3,0,1] row_mask:0xf bank_mask:0xf\n"
      "v_add_u32_dpp v97, v97, v97 quad_perm:[2,3,0,1] row_mask:0xf bank_mask:0xf\n"
      "s_add_u32 s6, s26, s72\n"
      "s_addc_u32 s7, s27, 0\n"
      "v_mfma_f32_16x16x32_bf16 v[88:91], v[112:115], v[188:191], v[88:91]\n"
      "v_add_u32_dpp v96, v96, v96 row_half_mirror row_mask:0xf bank_mask:0xf\n"
      "v_add_u32_dpp v97, v97, v97 row_half_mirror row_mask:0xf bank_mask:0xf\n"
      "v_mfma_f32_16x16x32_bf16 v[88:91], v[124:127], v[184:187], v[88:91]\n"
      "buffer_store_dwordx2 v[108:109], v37, s[32:35], s55 offen sc1\n"
      "global_load_dwordx4 a[40:43], v35, s[8:9] offset:-2048 sc1\n"
      "v_mov_b32 v98, v96\n"
      "v_add_u32_dpp v97, v97, v97 row_mirror row_mask:0xf bank_mask:0xf\n"
      "v_mfma_f32_16x16x32_bf16 v[88:91], v[136:139], v[180:183], v[88:91]\n"
      "ds_write_b128 v59, v[84:87] offset:4096\n"
      "s_mov_b32 s42, 0xaaaa5555\n"
      "s_mov_b32 s43, s42\n"
      "s_add_u32 s10, s26, s75\n"
      "s_addc_u32 s11, s27, 0\n"
      "v_mfma_f32_16x16x32_bf16 v[88:91], v[148:151], v[176:179], v[88:91]\n"
      "v_permlane16_swap_b32 v96, v98\n"
      "v_add_u32 v96, v96, v98\n"
      "v_mfma_f32_16x16x32_bf16 v[92:95], v[116:119], v[188:191], v[92:95]\n"
      "global_load_dwordx4 a[44:47], v35, s[8:9] offset:-1024 sc1\n"
      "v_cndmask_b32 v96, v97, v96, s[42:43]\n"
      "v_sub_u32 v96, v52, v96\n"
      "s_add_u32 s12, s10, 49152\n"
      "s_addc_u32 s13, s11, 0\n"
      "v_mfma_f32_16x16x32_bf16 v[92:95], v[128:131], v[184:187], v[92:95]\n"
      "buffer_store_dword v96, v49, s[32:35], s77 offen sc1\n"
      "s_lshl_b32 s71, s30, 2\n"
      "v_mfma_f32_16x16x32_bf16 v[92:95], v[140:143], v[180:183], v[92:95]\n"
      "ds_write_b128 v44, v[88:91] offset:6144\n"
      "v_mfma_f32_16x16x32_bf16 v[92:95], v[152:155], v[176:179], v[92:95]\n"
      "global_load_dwordx4 a[48:51], v35, s[8:9] offset:0 sc1\n"
      "s_nop 7\n"
      "ds_write_b128 v59, v[92:95] offset:6144\n"
      "v_readlane_b32 s40, v204, 0\n"
      "v_readlane_b32 s42, v205, 4\n"
      "v_xor_b32 v33, 16, v31\n"
      "v_readlane_b32 s58, v204, 8\n"
      "v_readlane_b32 s48, v204, 16\n"
      "v_readlane_b32 s50, v205, 20\n"
      "v_readlane_b32 s52, v204, 24\n"
      "v_readlane_b32 s41, v205, 12\n"
      "v_readlane_b32 s49, v205, 28\n"
      "s_mov_b32 vcc_lo, 0xaa55aa55\n"
      "s_mov_b32 vcc_hi, 0xaa55aa55\n"
      "v_add_u32 v40, s73, v7\n"
      "v_lshlrev_b32 v96, 16, v210\n"
      "v_and_b32 v97, 0xffff0000, v210\n"
      "v_lshlrev_b32 v98, 16, v214\n"
      "v_and_b32 v99, 0xffff0000, v214\n"
      "v_lshlrev_b32 v100, 16, v218\n"
      "v_and_b32 v101, 0xffff0000, v218\n"
      "v_lshlrev_b32 v102, 16, v222\n"
      "v_and_b32 v103, 0xffff0000, v222\n"
      "s_waitcnt lgkmcnt(0)\n"
      "s_barrier\n"
      "ds_read_b128 v[64:67], v31 offset:0\n"
      "ds_read_b128 v[68:71], v33 offset:0\n"
      "ds_read_b128 v[72:75], v31 offset:8192\n"
      "ds_read_b128 v[76:79], v33 offset:8192\n"
      "ds_read_b128 v[80:83], v31 offset:16384\n"
      "ds_read_b128 v[84:87], v33 offset:16384\n"
      "ds_read_b128 v[88:91], v31 offset:24576\n"
      "ds_read_b128 v[92:95], v33 offset:24576\n"
      "v_add_u32 v34, 12288, v7\n"
      "global_load_dwordx4 a[52:55], v34, s[8:9] offset:1024 sc1\n"
      "v_lshlrev_b32 v112, 16, v192\n"
      "v_and_b32 v113, 0xffff0000, v192\n"
      "v_lshlrev_b32 v114, 16, v193\n"
      "v_and_b32 v115, 0xffff0000, v193\n"
      "v_lshlrev_b32 v116, 16, v194\n"
      "v_and_b32 v117, 0xffff0000, v194\n"
      "v_lshlrev_b32 v118, 16, v195\n"
      "v_and_b32 v119, 0xffff0000, v195\n"
      "v_pk_add_f32 v[104:105], v[112:113], v[96:97] neg_lo:[0,1] neg_hi:[0,1]\n"
      "v_pk_add_f32 v[106:107], v[114:115], v[98:99] neg_lo:[0,1] neg_hi:[0,1]\n"
      "v_pk_add_f32 v[108:109], v[116:117], v[100:101] neg_lo:[0,1] neg_hi:[0,1]\n"
      "v_pk_add_f32 v[110:111], v[118:119], v[102:103] neg_lo:[0,1] neg_hi:[0,1]\n"
      "global_load_dwordx4 a[56:59], v34, s[8:9] offset:2048 sc1\n"
      "v_cvt_pk_bf16_f32 v104, 0, v104\n"
      "v_cvt_pk_bf16_f32 v105, 0, v105\n"
      "v_cvt_pk_bf16_f32 v106, 0, v106\n"
      "v_cvt_pk_bf16_f32 v107, 0, v107\n"
      "v_cvt_pk_bf16_f32 v108, 0, v108\n"
      "v_cvt_pk_bf16_f32 v109, 0, v109\n"
      "v_cvt_pk_bf16_f32 v110, 0, v110\n"
      "v_cvt_pk_bf16_f32 v111, 0, v111\n"
      "v_pk_mul_f32 v[104:105], v[104:105], v[208:209]\n"
      "v_pk_mul_f32 v[106:107], v[106:107], v[212:213]\n"
      "v_pk_mul_f32 v[108:109], v[108:109], v[216:217]\n"
      "v_pk_mul_f32 v[110:111], v[110:111], v[220:221]\n"
      "global_load_dwordx4 a[60:63], v34, s[8:9] offset:3072 sc1\n"
      "v_cvt_pk_bf16_f32 v104, 0, v104\n"
      "v_cvt_pk_bf16_f32 v105, 0, v105\n"
      "v_cvt_pk_bf16_f32 v106, 0, v106\n"
      "v_cvt_pk_bf16_f32 v107, 0, v107\n"
      "v_cvt_pk_bf16_f32 v108, 0, v108\n"
      "v_cvt_pk_bf16_f32 v109, 0, v109\n"
      "v_cvt_pk_bf16_f32 v110, 0, v110\n"
      "v_cvt_pk_bf16_f32 v111, 0, v111\n"
      "v_pk_mul_f32 v[104:105], v[104:105], s[42:43] op_sel_hi:[1,0]\n"
      "v_pk_mul_f32 v[106:107], v[106:107], s[42:43] op_sel_hi:[1,0]\n"
      "v_pk_mul_f32 v[108:109], v[108:109], s[42:43] op_sel_hi:[1,0]\n"
      "v_pk_mul_f32 v[110:111], v[110:111], s[42:43] op_sel_hi:[1,0]\n"
      "v_add_u32 v34, 20480, v7\n"
      "global_load_dwordx4 a[64:67], v34, s[8:9] offset:-4096 sc1\n"
      "v_cvt_pk_bf16_f32 v104, 0, v104\n"
      "v_cvt_pk_bf16_f32 v105, 0, v105\n"
      "v_cvt_pk_bf16_f32 v106, 0, v106\n"
      "v_cvt_pk_bf16_f32 v107, 0, v107\n"
      "v_cvt_pk_bf16_f32 v108, 0, v108\n"
      "v_cvt_pk_bf16_f32 v109, 0, v109\n"
      "v_cvt_pk_bf16_f32 v110, 0, v110\n"
      "v_cvt_pk_bf16_f32 v111, 0, v111\n"
      "v_pk_add_f32 v[104:105], v[104:105], s[40:41] op_sel_hi:[1,0]\n"
      "v_pk_add_f32 v[106:107], v[106:107], s[40:41] op_sel_hi:[1,0]\n"
      "v_pk_add_f32 v[108:109], v[108:109], s[40:41] op_sel_hi:[1,0]\n"
      "v_pk_add_f32 v[110:111], v[110:111], s[40:41] op_sel_hi:[1,0]\n"
      "global_load_dwordx4 a[68:71], v34, s[8:9] offset:-3072 sc1\n"
      "v_cvt_pk_bf16_f32 v104, 0, v104\n"
      "v_cvt_pk_bf16_f32 v105, 0, v105\n"
      "v_cvt_pk_bf16_f32 v106, 0, v106\n"
      "v_cvt_pk_bf16_f32 v107, 0, v107\n"
      "v_cvt_pk_bf16_f32 v108, 0, v108\n"
      "v_cvt_pk_bf16_f32 v109, 0, v109\n"
      "v_cvt_pk_bf16_f32 v110, 0, v110\n"
      "v_cvt_pk_bf16_f32 v111, 0, v111\n"
      "v_and_b32 v120, 0xffff0000, v235\n"
      "v_lshlrev_b32 v121, 16, v235\n"
      "v_and_b32 v122, 0xffff0000, v234\n"
      "v_lshlrev_b32 v123, 16, v234\n"
      "global_load_dwordx4 a[72:75], v34, s[8:9] offset:-2048 sc1\n"
      "v_and_b32 v124, 0xffff0000, v233\n"
      "v_lshlrev_b32 v125, 16, v233\n"
      "v_and_b32 v126, 0xffff0000, v232\n"
      "v_lshlrev_b32 v127, 16, v232\n"
      "v_pk_mul_f32 v[120:121], v[120:121], s[14:15] op_sel_hi:[1,0]\n"
      "v_pk_mul_f32 v[122:123], v[122:123], s[14:15] op_sel_hi:[1,0]\n"
      "v_pk_mul_f32 v[124:125], v[124:125], s[14:15] op_sel_hi:[1,0]\n"
      "v_pk_mul_f32 v[126:127], v[126:127], s[14:15] op_sel_hi:[1,0]\n"
      "v_cvt_pk_bf16_f32 v120, 0, v120\n"
      "v_cvt_pk_bf16_f32 v121, 0, v121\n"
      "v_cvt_pk_bf16_f32 v122, 0, v122\n"
      "v_cvt_pk_bf16_f32 v123, 0, v123\n"
      "global_load_dwordx4 a[76:79], v34, s[8:9] offset:-1024 sc1\n"
      "v_cvt_pk_bf16_f32 v124, 0, v124\n"
      "v_cvt_pk_bf16_f32 v125, 0, v125\n"
      "v_cvt_pk_bf16_f32 v126, 0, v126\n"
      "v_cvt_pk_bf16_f32 v127, 0, v127\n"
      "s_add_u32 s43, s89, 32768\n"
      "s_add_u32 s53, s89, 0x1000000\n"
      "s_add_u32 s54, s43, 0x1000000\n"
      "s_add_u32 s55, s79, 176128\n"
      "s_add_u32 s45, s81, 65536\n"
      "s_waitcnt lgkmcnt(0)\n"
      "v_pk_add_f32 v[64:65], v[64:65], v[72:73]\n"
      "v_pk_add_f32 v[66:67], v[66:67], v[74:75]\n"
      "global_load_dwordx4 a[80:83], v34, s[8:9] offset:0 sc1\n"
      "v_pk_add_f32 v[68:69], v[68:69], v[76:77]\n"
      "v_pk_add_f32 v[70:71], v[70:71], v[78:79]\n"
      "v_cvt_pk_bf16_f32 v64, 0, v64\n"
      "v_cvt_pk_bf16_f32 v65, 0, v65\n"
      "v_cvt_pk_bf16_f32 v66, 0, v66\n"
      "v_cvt_pk_bf16_f32 v67, 0, v67\n"
      "v_cvt_pk_bf16_f32 v68, 0, v68\n"
      "v_cvt_pk_bf16_f32 v69, 0, v69\n"
      "v_cvt_pk_bf16_f32 v70, 0, v70\n"
      "v_cvt_pk_bf16_f32 v71, 0, v71\n"
      "v_pk_add_f32 v[120:121], v[120:121], v[64:65]\n"
      "v_pk_add_f32 v[122:123], v[122:123], v[66:67]\n"
      "global_load_dwordx4 a[84:87], v34, s[8:9] offset:1024 sc1\n"
      "v_pk_add_f32 v[124:125], v[124:125], v[68:69]\n"
      "v_pk_add_f32 v[126:127], v[126:127], v[70:71]\n"
      "v_cvt_pk_bf16_f32 v120, 0, v120\n"
      "v_cvt_pk_bf16_f32 v121, 0, v121\n"
      "v_cvt_pk_bf16_f32 v122, 0, v122\n"
      "v_cvt_pk_bf16_f32 v123, 0, v123\n"
      "v_cvt_pk_bf16_f32 v124, 0, v124\n"
      "v_cvt_pk_bf16_f32 v125, 0, v125\n"
      "v_cvt_pk_bf16_f32 v126, 0, v126\n"
      "v_cvt_pk_bf16_f32 v127, 0, v127\n"
      "v_pk_mul_f32 v[104:105], v[104:105], v[120:121]\n"
      "v_pk_mul_f32 v[106:107], v[106:107], v[122:123]\n"
      "global_load_dwordx4 a[88:91], v34, s[8:9] offset:2048 sc1\n"
      "v_pk_mul_f32 v[108:109], v[108:109], v[124:125]\n"
      "v_pk_mul_f32 v[110:111], v[110:111], v[126:127]\n"
      "v_cvt_pk_bf16_f32 v104, 0, v104\n"
      "v_cvt_pk_bf16_f32 v105, 0, v105\n"
      "v_cvt_pk_bf16_f32 v106, 0, v106\n"
      "v_cvt_pk_bf16_f32 v107, 0, v107\n"
      "v_cvt_pk_bf16_f32 v108, 0, v108\n"
      "v_cvt_pk_bf16_f32 v109, 0, v109\n"
      "v_cvt_pk_bf16_f32 v110, 0, v110\n"
      "v_cvt_pk_bf16_f32 v111, 0, v111\n"
      "v_pk_mul_f32 v[104:105], v[104:105], s[58:59] op_sel_hi:[1,0]\n"
      "v_pk_mul_f32 v[106:107], v[106:107], s[58:59] op_sel_hi:[1,0]\n"
      "global_load_dwordx4 a[92:95], v34, s[8:9] offset:3072 sc1\n"
      "v_pk_mul_f32 v[108:109], v[108:109], s[58:59] op_sel_hi:[1,0]\n"
      "v_pk_mul_f32 v[110:111], v[110:111], s[58:59] op_sel_hi:[1,0]\n"
      "v_cvt_pk_bf16_f32 v104, 0, v104\n"
      "v_cvt_pk_bf16_f32 v105, 0, v105\n"
      "v_cvt_pk_bf16_f32 v106, 0, v106\n"
      "v_cvt_pk_bf16_f32 v107, 0, v107\n"
      "v_cvt_pk_bf16_f32 v108, 0, v108\n"
      "v_cvt_pk_bf16_f32 v109, 0, v109\n"
      "v_cvt_pk_bf16_f32 v110, 0, v110\n"
      "v_cvt_pk_bf16_f32 v111, 0, v111\n"
      "v_pk_add_f32 v[104:105], v[104:105], v[112:113]\n"
      "v_pk_add_f32 v[106:107], v[106:107], v[114:115]\n"
      "v_add_u32 v34, 28672, v7\n"
      "global_load_dwordx4 a[96:99], v34, s[8:9] offset:-4096 sc1\n"
      "v_pk_add_f32 v[108:109], v[108:109], v[116:117]\n"
      "v_pk_add_f32 v[110:111], v[110:111], v[118:119]\n"
      "v_cvt_pk_bf16_f32 v104, 0, v104\n"
      "v_cvt_pk_bf16_f32 v105, 0, v105\n"
      "v_cvt_pk_bf16_f32 v106, 0, v106\n"
      "v_cvt_pk_bf16_f32 v107, 0, v107\n"
      "v_cvt_pk_bf16_f32 v108, 0, v108\n"
      "v_cvt_pk_bf16_f32 v109, 0, v109\n"
      "v_cvt_pk_bf16_f32 v110, 0, v110\n"
      "v_cvt_pk_bf16_f32 v111, 0, v111\n"
      "v_pk_mul_f32 v[120:121], v[104:105], s[40:41] op_sel:[0,1] op_sel_hi:[1,1]\n"
      "v_pk_mul_f32 v[122:123], v[106:107], s[40:41] op_sel:[0,1] op_sel_hi:[1,1]\n"
      "v_pk_mul_f32 v[124:125], v[108:109], s[40:41] op_sel:[0,1] op_sel_hi:[1,1]\n"
      "v_pk_mul_f32 v[126:127], v[110:111], s[40:41] op_sel:[0,1] op_sel_hi:[1,1]\n"
      "v_cvt_pk_bf16_f32 v128, v120, v121\n"
      "v_cvt_pk_bf16_f32 v129, v122, v123\n"
      "v_cvt_pk_bf16_f32 v130, v124, v125\n"
      "v_cvt_pk_bf16_f32 v131, v126, v127\n"
      "buffer_store_dwordx4 v[128:131], v29, s[32:35], s89 offen sc1\n"
      "v_cvt_pk_bf16_f32 v132, v104, v105\n"
      "v_cvt_pk_bf16_f32 v133, v106, v107\n"
      "v_cvt_pk_bf16_f32 v134, v108, v109\n"
      "v_cvt_pk_bf16_f32 v135, v110, v111\n"
      "v_add3_u32 v35, v128, v129, v130\n"
      "v_add_u32 v35, v35, v131\n"
      "buffer_store_dwordx4 v[132:135], v36, s[32:35], s53 offen sc1\n"
      "v_add3_u32 v38, v132, v133, v134\n"
      "v_add_u32 v38, v38, v135\n"
      "v_lshlrev_b32 v112, 16, v196\n"
      "v_add_u32_dpp v35, v35, v35 quad_perm:[1,0,3,2] row_mask:0xf bank_mask:0xf\n"
      "v_and_b32 v113, 0xffff0000, v196\n"
      "v_add_u32_dpp v38, v38, v38 quad_perm:[1,0,3,2] row_mask:0xf bank_mask:0xf\n"
      "v_lshlrev_b32 v114, 16, v197\n"
      "v_add_u32_dpp v35, v35, v35 quad_perm:[2,3,0,1] row_mask:0xf bank_mask:0xf\n"
      "v_and_b32 v115, 0xffff0000, v197\n"
      "v_add_u32_dpp v38, v38, v38 quad_perm:[2,3,0,1] row_mask:0xf bank_mask:0xf\n"
      "v_lshlrev_b32 v116, 16, v198\n"
      "v_add_u32_dpp v35, v35, v35 row_half_mirror row_mask:0xf bank_mask:0xf\n"
      "v_and_b32 v117, 0xffff0000, v198\n"
      "v_add_u32_dpp v38, v38, v38 row_ror:8 row_mask:0xf bank_mask:0xf\n"
      "v_lshlrev_b32 v118, 16, v199\n"
      "v_cndmask_b32 v35, v35, v38, vcc\n"
      "v_and_b32 v119, 0xffff0000, v199\n"
      "v_sub_u32 v35, s30, v35\n"
      "v_pk_add_f32 v[104:105], v[112:113], v[96:97] neg_lo:[0,1] neg_hi:[0,1]\n"
      "buffer_store_dword v35, v30, s[32:35], s81 offen sc1\n"
      "s_waitcnt vmcnt(23)\n"
      "buffer_store_dwordx2 v[52:53], v48, s[32:35], s88 offen sc1\n"
      "v_pk_add_f32 v[106:107], v[114:115], v[98:99] neg_lo:[0,1] neg_hi:[0,1]\n"
      "v_pk_add_f32 v[108:109], v[116:117], v[100:101] neg_lo:[0,1] neg_hi:[0,1]\n"
      "v_pk_add_f32 v[110:111], v[118:119], v[102:103] neg_lo:[0,1] neg_hi:[0,1]\n"
      "v_cvt_pk_bf16_f32 v104, 0, v104\n"
      "v_cvt_pk_bf16_f32 v105, 0, v105\n"
      "v_cvt_pk_bf16_f32 v106, 0, v106\n"
      "v_cvt_pk_bf16_f32 v107, 0, v107\n"
      "v_cvt_pk_bf16_f32 v108, 0, v108\n"
      "v_cvt_pk_bf16_f32 v109, 0, v109\n"
      "v_cvt_pk_bf16_f32 v110, 0, v110\n"
      "v_cvt_pk_bf16_f32 v111, 0, v111\n"
      "buffer_store_dwordx2 v[52:53], v48, s[32:35], s55 offen sc1\n"
      "v_pk_mul_f32 v[104:105], v[104:105], v[208:209]\n"
      "v_pk_mul_f32 v[106:107], v[106:107], v[212:213]\n"
      "v_pk_mul_f32 v[108:109], v[108:109], v[216:217]\n"
      "v_pk_mul_f32 v[110:111], v[110:111], v[220:221]\n"
      "v_cvt_pk_bf16_f32 v104, 0, v104\n"
      "v_cvt_pk_bf16_f32 v105, 0, v105\n"
      "v_cvt_pk_bf16_f32 v106, 0, v106\n"
      "v_cvt_pk_bf16_f32 v107, 0, v107\n"
      "v_cvt_pk_bf16_f32 v108, 0, v108\n"
      "v_cvt_pk_bf16_f32 v109, 0, v109\n"
      "v_cvt_pk_bf16_f32 v110, 0, v110\n"
      "global_load_dwordx4 a[100:103], v34, s[8:9] offset:-3072 sc1\n"
      "v_cvt_pk_bf16_f32 v111, 0, v111\n"
      "v_pk_mul_f32 v[104:105], v[104:105], s[50:51] op_sel_hi:[1,0]\n"
      "v_pk_mul_f32 v[106:107], v[106:107], s[50:51] op_sel_hi:[1,0]\n"
      "v_pk_mul_f32 v[108:109], v[108:109], s[50:51] op_sel_hi:[1,0]\n"
      "v_pk_mul_f32 v[110:111], v[110:111], s[50:51] op_sel_hi:[1,0]\n"
      "v_cvt_pk_bf16_f32 v104, 0, v104\n"
      "v_cvt_pk_bf16_f32 v105, 0, v105\n"
      "v_cvt_pk_bf16_f32 v106, 0, v106\n"
      "v_cvt_pk_bf16_f32 v107, 0, v107\n"
      "v_cvt_pk_bf16_f32 v108, 0, v108\n"
      "v_cvt_pk_bf16_f32 v109, 0, v109\n"
      "s_add_u32 m0, s73, 0\n"
      "v_add_u32 v43, 0, v40\n"
      "global_load_lds_dwordx4 v43, s[4:5] offset:0 sc1\n"
      "v_cvt_pk_bf16_f32 v110, 0, v110\n"
      "v_cvt_pk_bf16_f32 v111, 0, v111\n"
      "v_pk_add_f32 v[104:105], v[104:105], s[48:49] op_sel_hi:[1,0]\n"
      "v_pk_add_f32 v[106:107], v[106:107], s[48:49] op_sel_hi:[1,0]\n"
      "v_pk_add_f32 v[108:109], v[108:109], s[48:49] op_sel_hi:[1,0]\n"
      "v_pk_add_f32 v[110:111], v[110:111], s[48:49] op_sel_hi:[1,0]\n"
      "v_cvt_pk_bf16_f32 v104, 0, v104\n"
      "v_cvt_pk_bf16_f32 v105, 0, v105\n"
      "v_cvt_pk_bf16_f32 v106, 0, v106\n"
      "v_cvt_pk_bf16_f32 v107, 0, v107\n"
      "v_cvt_pk_bf16_f32 v108, 0, v108\n"
      "global_load_lds_dwordx4 v43, s[4:5] offset:1024 sc1\n"
      "v_cvt_pk_bf16_f32 v109, 0, v109\n"
      "v_cvt_pk_bf16_f32 v110, 0, v110\n"
      "v_cvt_pk_bf16_f32 v111, 0, v111\n"
      "v_and_b32 v120, 0xffff0000, v239\n"
      "v_lshlrev_b32 v121, 16, v239\n"
      "v_and_b32 v122, 0xffff0000, v238\n"
      "v_lshlrev_b32 v123, 16, v238\n"
      "v_and_b32 v124, 0xffff0000, v237\n"
      "v_lshlrev_b32 v125, 16, v237\n"
      "v_and_b32 v126, 0xffff0000, v236\n"
      "v_lshlrev_b32 v127, 16, v236\n"
      "global_load_lds_dwordx4 v43, s[4:5] offset:2048 sc1\n"
      "v_pk_mul_f32 v[120:121], v[120:121], s[14:15] op_sel:[0,1] op_sel_hi:[1,1]\n"
      "v_pk_mul_f32 v[122:123], v[122:123], s[14:15] op_sel:[0,1] op_sel_hi:[1,1]\n"
      "v_pk_mul_f32 v[124:125], v[124:125], s[14:15] op_sel:[0,1] op_sel_hi:[1,1]\n"
      "v_pk_mul_f32 v[126:127], v[126:127], s[14:15] op_sel:[0,1] op_sel_hi:[1,1]\n"
      "v_cvt_pk_bf16_f32 v120, 0, v120\n"
      "v_cvt_pk_bf16_f32 v121, 0, v121\n"
      "v_cvt_pk_bf16_f32 v122, 0, v122\n"
      "v_cvt_pk_bf16_f32 v123, 0, v123\n"
      "v_cvt_pk_bf16_f32 v124, 0, v124\n"
      "v_cvt_pk_bf16_f32 v125, 0, v125\n"
      "v_cvt_pk_bf16_f32 v126, 0, v126\n"
      "global_load_lds_dwordx4 v43, s[4:5] offset:3072 sc1\n"
      "v_cvt_pk_bf16_f32 v127, 0, v127\n"
      "v_pk_add_f32 v[80:81], v[80:81], v[88:89]\n"
      "v_pk_add_f32 v[82:83], v[82:83], v[90:91]\n"
      "v_pk_add_f32 v[84:85], v[84:85], v[92:93]\n"
      "v_pk_add_f32 v[86:87], v[86:87], v[94:95]\n"
      "v_cvt_pk_bf16_f32 v80, 0, v80\n"
      "v_cvt_pk_bf16_f32 v81, 0, v81\n"
      "v_cvt_pk_bf16_f32 v82, 0, v82\n"
      "v_cvt_pk_bf16_f32 v83, 0, v83\n"
      "v_cvt_pk_bf16_f32 v84, 0, v84\n"
      "v_cvt_pk_bf16_f32 v85, 0, v85\n"
      "s_mov_b32 s47, 0\n"
      "buffer_load_dwordx2 v[2:3], v5, s[32:35], s47 offen sc1\n"
      "v_cvt_pk_bf16_f32 v86, 0, v86\n"
      "v_cvt_pk_bf16_f32 v87, 0, v87\n"
      "v_pk_add_f32 v[120:121], v[120:121], v[80:81]\n"
      "v_pk_add_f32 v[122:123], v[122:123], v[82:83]\n"
      "v_pk_add_f32 v[124:125], v[124:125], v[84:85]\n"
      "v_pk_add_f32 v[126:127], v[126:127], v[86:87]\n"
      "v_cvt_pk_bf16_f32 v120, 0, v120\n"
      "v_cvt_pk_bf16_f32 v121, 0, v121\n"
      "v_cvt_pk_bf16_f32 v122, 0, v122\n"
      "v_cvt_pk_bf16_f32 v123, 0, v123\n"
      "v_cvt_pk_bf16_f32 v124, 0, v124\n"
      "s_add_u32 m0, s73, 16384\n"
      "v_add_u32 v43, 81920, v40\n"
      "global_load_lds_dwordx4 v43, s[4:5] offset:0 sc1\n"
      "v_cvt_pk_bf16_f32 v125, 0, v125\n"
      "v_cvt_pk_bf16_f32 v126, 0, v126\n"
      "v_cvt_pk_bf16_f32 v127, 0, v127\n"
      "v_pk_mul_f32 v[104:105], v[104:105], v[120:121]\n"
      "v_pk_mul_f32 v[106:107], v[106:107], v[122:123]\n"
      "v_pk_mul_f32 v[108:109], v[108:109], v[124:125]\n"
      "v_pk_mul_f32 v[110:111], v[110:111], v[126:127]\n"
      "v_cvt_pk_bf16_f32 v104, 0, v104\n"
      "v_cvt_pk_bf16_f32 v105, 0, v105\n"
      "v_cvt_pk_bf16_f32 v106, 0, v106\n"
      "v_cvt_pk_bf16_f32 v107, 0, v107\n"
      "global_load_lds_dwordx4 v43, s[4:5] offset:1024 sc1\n"
      "v_cvt_pk_bf16_f32 v108, 0, v108\n"
      "v_cvt_pk_bf16_f32 v109, 0, v109\n"
      "v_cvt_pk_bf16_f32 v110, 0, v110\n"
      "v_cvt_pk_bf16_f32 v111, 0, v111\n"
      "v_pk_mul_f32 v[104:105], v[104:105], s[52:53] op_sel_hi:[1,0]\n"
      "v_pk_mul_f32 v[106:107], v[106:107], s[52:53] op_sel_hi:[1,0]\n"
      "v_pk_mul_f32 v[108:109], v[108:109], s[52:53] op_sel_hi:[1,0]\n"
      "v_pk_mul_f32 v[110:111], v[110:111], s[52:53] op_sel_hi:[1,0]\n"
      "v_cvt_pk_bf16_f32 v104, 0, v104\n"
      "v_cvt_pk_bf16_f32 v105, 0, v105\n"
      "v_cvt_pk_bf16_f32 v106, 0, v106\n"
      "global_load_lds_dwordx4 v43, s[4:5] offset:2048 sc1\n"
      "v_cvt_pk_bf16_f32 v107, 0, v107\n"
      "v_cvt_pk_bf16_f32 v108, 0, v108\n"
      "v_cvt_pk_bf16_f32 v109, 0, v109\n"
      "v_cvt_pk_bf16_f32 v110, 0, v110\n"
      "v_cvt_pk_bf16_f32 v111, 0, v111\n"
      "v_pk_add_f32 v[104:105], v[104:105], v[112:113]\n"
      "v_pk_add_f32 v[106:107], v[106:107], v[114:115]\n"
      "v_pk_add_f32 v[108:109], v[108:109], v[116:117]\n"
      "v_pk_add_f32 v[110:111], v[110:111], v[118:119]\n"
      "v_cvt_pk_bf16_f32 v104, 0, v104\n"
      "v_cvt_pk_bf16_f32 v105, 0, v105\n"
      "global_load_lds_dwordx4 v43, s[4:5] offset:3072 sc1\n"
      "v_cvt_pk_bf16_f32 v106, 0, v106\n"
      "v_cvt_pk_bf16_f32 v107, 0, v107\n"
      "v_cvt_pk_bf16_f32 v108, 0, v108\n"
      "v_cvt_pk_bf16_f32 v109, 0, v109\n"
      "v_cvt_pk_bf16_f32 v110, 0, v110\n"
      "v_cvt_pk_bf16_f32 v111, 0, v111\n"
      "v_pk_mul_f32 v[120:121], v[104:105], s[48:49] op_sel:[0,1] op_sel_hi:[1,1]\n"
      "v_pk_mul_f32 v[122:123], v[106:107], s[48:49] op_sel:[0,1] op_sel_hi:[1,1]\n"
      "v_pk_mul_f32 v[124:125], v[108:109], s[48:49] op_sel:[0,1] op_sel_hi:[1,1]\n"
      "v_pk_mul_f32 v[126:127], v[110:111], s[48:49] op_sel:[0,1] op_sel_hi:[1,1]\n"
      "v_cvt_pk_bf16_f32 v136, v120, v121\n"
      "v_cvt_pk_bf16_f32 v137, v122, v123\n"
      "v_cvt_pk_bf16_f32 v138, v124, v125\n"
      "v_cvt_pk_bf16_f32 v139, v126, v127\n"
      "buffer_store_dwordx4 v[136:139], v29, s[32:35], s43 offen sc1\n"
      "v_cvt_pk_bf16_f32 v140, v104, v105\n"
      "v_cvt_pk_bf16_f32 v141, v106, v107\n"
      "v_cvt_pk_bf16_f32 v142, v108, v109\n"
      "v_cvt_pk_bf16_f32 v143, v110, v111\n"
      "v_add3_u32 v35, v136, v137, v138\n"
      "v_add_u32 v35, v35, v139\n"
      "buffer_store_dwordx4 v[140:143], v36, s[32:35], s54 offen sc1\n"
      "v_add3_u32 v38, v140, v141, v142\n"
      "v_add_u32 v38, v38, v143\n"
      "v_add_u32_dpp v35, v35, v35 quad_perm:[1,0,3,2] row_mask:0xf bank_mask:0xf\n"
      "s_nop 0\n"
      "v_add_u32_dpp v38, v38, v38 quad_perm:[1,0,3,2] row_mask:0xf bank_mask:0xf\n"
      "v_add_u32_dpp v35, v35, v35 quad_perm:[2,3,0,1] row_mask:0xf bank_mask:0xf\n"
      "s_nop 0\n"
      "v_add_u32_dpp v38, v38, v38 quad_perm:[2,3,0,1] row_mask:0xf bank_mask:0xf\n"
      "v_add_u32_dpp v35, v35, v35 row_half_mirror row_mask:0xf bank_mask:0xf\n"
      "s_nop 0\n"
      "v_add_u32_dpp v38, v38, v38 row_ror:8 row_mask:0xf bank_mask:0xf\n"
      "v_cndmask_b32 v35, v35, v38, vcc\n"
      "v_sub_u32 v35, s30, v35\n"
      "buffer_store_dword v35, v30, s[32:35], s45 offen sc1\n"
      "global_load_dwordx4 a[104:107], v34, s[8:9] offset:-2048 sc1\n"
      "global_load_dwordx4 a[108:111], v34, s[8:9] offset:-1024 sc1\n"
      "global_load_dwordx4 a[112:115], v34, s[8:9] offset:0 sc1\n"
      "global_load_dwordx4 a[116:119], v34, s[8:9] offset:1024 sc1\n"
      "global_load_dwordx4 a[120:123], v34, s[8:9] offset:2048 sc1\n"
      "global_load_dwordx4 a[124:127], v34, s[8:9] offset:3072 sc1\n"
      "v_add_u32 v34, 36864, v7\n"
      "global_load_dwordx4 a[128:131], v34, s[8:9] offset:-4096 sc1\n"
      "global_load_dwordx4 a[132:135], v34, s[8:9] offset:-3072 sc1\n"
      "global_load_dwordx4 a[136:139], v34, s[8:9] offset:-2048 sc1\n"
      "global_load_dwordx4 a[140:143], v34, s[8:9] offset:-1024 sc1\n"
      "global_load_dwordx4 a[144:147], v34, s[8:9] offset:0 sc1\n"
      "global_load_dwordx4 a[148:151], v34, s[8:9] offset:1024 sc1\n"
      "global_load_dwordx4 a[152:155], v34, s[8:9] offset:2048 sc1\n"
      "global_load_dwordx4 a[156:159], v34, s[8:9] offset:3072 sc1\n"
      "v_xor_b32 v33, 32, v6\n"
      "v_xor_b32 v34, 64, v6\n"
      "v_xor_b32 v35, 96, v6\n"
      "v_add_u32 v45, 45056, v7\n"
      "s_waitcnt vmcnt(21)\n"
      "ds_read_b128 v[248:251], v40 offset:0\n"
      "ds_read_b128 v[252:255], v40 offset:1024\n"
      "ds_read_b128 v[36:39], v40 offset:2048\n"
      "ds_read_b128 v[60:63], v40 offset:3072\n"
      "s_waitcnt lgkmcnt(3)\n"
      "v_add3_u32 v2, v2, v248, v249\n"
      "v_add3_u32 v2, v2, v250, v251\n"
      "s_waitcnt lgkmcnt(2)\n"
      "v_add3_u32 v2, v2, v252, v253\n"
      "v_add3_u32 v2, v2, v254, v255\n"
      "s_waitcnt lgkmcnt(1)\n"
      "v_add3_u32 v2, v2, v36, v37\n"
      "v_add3_u32 v2, v2, v38, v39\n"
      "s_waitcnt lgkmcnt(0)\n"
      "v_add3_u32 v2, v2, v60, v61\n"
      "v_add3_u32 v2, v2, v62, v63\n"
      "s_nop 1\n"
      "v_add_u32_dpp v2, v2, v2 quad_perm:[1,0,3,2] row_mask:0xf bank_mask:0xf\n"
      "s_nop 1\n"
      "v_add_u32_dpp v2, v2, v2 quad_perm:[2,3,0,1] row_mask:0xf bank_mask:0xf\n"
      "s_nop 1\n"
      "v_add_u32_dpp v2, v2, v2 row_half_mirror row_mask:0xf bank_mask:0xf\n"
      "v_cmp_ne_u32 vcc, s71, v2\n"
      "s_cbranch_vccnz L_sx0_%=\n"
      "L_bx0_%=:\n"
      "s_barrier\n"
      "ds_read_b64_tr_b16 v[64:65], v6 offset:0\n"
      "ds_read_b64_tr_b16 v[66:67], v6 offset:2048\n"
      "ds_read_b64_tr_b16 v[68:69], v33 offset:0\n"
      "ds_read_b64_tr_b16 v[70:71], v33 offset:2048\n"
      "ds_read_b64_tr_b16 v[72:73], v34 offset:0\n"
      "ds_read_b64_tr_b16 v[74:75], v34 offset:2048\n"
      "ds_read_b64_tr_b16 v[76:77], v35 offset:0\n"
      "ds_read_b64_tr_b16 v[78:79], v35 offset:2048\n"
      "ds_read_b64_tr_b16 v[80:81], v6 offset:4096\n"
      "ds_read_b64_tr_b16 v[82:83], v6 offset:6144\n"
      "ds_read_b64_tr_b16 v[84:85], v33 offset:4096\n"
      "ds_read_b64_tr_b16 v[86:87], v33 offset:6144\n"
      "ds_read_b64_tr_b16 v[88:89], v34 offset:4096\n"
      "ds_read_b64_tr_b16 v[90:91], v34 offset:6144\n"
      "ds_read_b64_tr_b16 v[92:93], v35 offset:4096\n"
      "ds_read_b64_tr_b16 v[94:95], v35 offset:6144\n"
      "s_waitcnt lgkmcnt(0)\n"
      "v_mfma_f32_16x16x32_bf16 v[128:131], a[0:3], v[64:67], 0\n"
      "global_load_dwordx4 a[160:163], v45, s[8:9] offset:-4096 sc1\n"
      "L_pg2_%=:\n"
      "s_cbranch_execz L_pg6_%=\n"
      "v_lshrrev_b32 v42, 8, v7\n"
      "v_mfma_f32_16x16x32_bf16 v[144:147], a[4:7], v[64:67], 0\n"
      "ds_read_b64_tr_b16 v[96:97], v6 offset:8192\n"
      "v_bfe_u32 v41, v7, 4, 4\n"
      "v_mfma_f32_16x16x32_bf16 v[160:163], a[8:11], v[64:67], 0\n"
      "ds_read_b64_tr_b16 v[98:99], v6 offset:10240\n"
      "s_waitcnt vmcnt(18)\n"
      "v_mfma_f32_16x16x32_bf16 v[132:135], a[0:3], v[68:71], 0\n"
      "ds_read_b128 v[248:251], v40 offset:16384\n"
      "v_min_u32 v42, 2, v42\n"
      "v_mfma_f32_16x16x32_bf16 v[148:151], a[4:7], v[68:71], 0\n"
      "global_load_dwordx4 a[164:167], v45, s[8:9] offset:-3072 sc1\n"
      "ds_read_b128 v[252:255], v40 offset:17408\n"
      "ds_read_b64_tr_b16 v[100:101], v33 offset:8192\n"
      "v_min_u32 v41, 11, v41\n"
      "v_mfma_f32_16x16x32_bf16 v[164:167], a[8:11], v[68:71], 0\n"
      "ds_read_b128 v[36:39], v40 offset:18432\n"
      "ds_read_b64_tr_b16 v[102:103], v33 offset:10240\n"
      "v_mad_u32_u24 v42, v42, 48, v41\n"
      "v_mfma_f32_16x16x32_bf16 v[136:139], a[0:3], v[72:75], 0\n"
      "ds_read_b128 v[60:63], v40 offset:19456\n"
      "ds_read_b64_tr_b16 v[104:105], v34 offset:8192\n"
      "v_lshl_add_u32 v42, v42, 6, s64\n"
      "v_mfma_f32_16x16x32_bf16 v[152:155], a[4:7], v[72:75], 0\n"
      "ds_read_b64_tr_b16 v[106:107], v34 offset:10240\n"
      "s_add_u32 s75, s76, 176128\n"
      "v_mfma_f32_16x16x32_bf16 v[168:171], a[8:11], v[72:75], 0\n"
      "global_load_dwordx4 a[168:171], v45, s[8:9] offset:-2048 sc1\n"
      "ds_read_b64_tr_b16 v[108:109], v35 offset:8192\n"
      "v_mfma_f32_16x16x32_bf16 v[140:143], a[0:3], v[76:79], 0\n"
      "ds_read_b64_tr_b16 v[110:111], v35 offset:10240\n"
      "v_mfma_f32_16x16x32_bf16 v[156:159], a[4:7], v[76:79], 0\n"
      "ds_read_b64_tr_b16 v[112:113], v6 offset:12288\n"
      "s_waitcnt lgkmcnt(5)\n"
      "v_add3_u32 v3, v3, v248, v249\n"
      "v_add3_u32 v3, v3, v250, v251\n"
      "v_mfma_f32_16x16x32_bf16 v[172:175], a[8:11], v[76:79], 0\n"
      "ds_read_b64_tr_b16 v[114:115], v6 offset:14336\n"
      "v_add3_u32 v3, v3, v252, v253\n"
      "v_add3_u32 v3, v3, v254, v255\n"
      "v_mfma_f32_16x16x32_bf16 v[128:131], a[12:15], v[80:83], v[128:131]\n"
      "global_load_dwordx4 a[172:175], v45, s[8:9] offset:-1024 sc1\n"
      "v_add3_u32 v3, v3, v36, v37\n"
      "v_add3_u32 v3, v3, v38, v39\n"
      "v_mfma_f32_16x16x32_bf16 v[144:147], a[16:19], v[80:83], v[144:147]\n"
      "ds_read_b64_tr_b16 v[116:117], v33 offset:12288\n"
      "v_add3_u32 v3, v3, v60, v61\n"
      "v_mfma_f32_16x16x32_bf16 v[160:163], a[20:23], v[80:83], v[160:163]\n"
      "ds_read_b64_tr_b16 v[118:119], v33 offset:14336\n"
      "v_add3_u32 v3, v3, v62, v63\n"
      "v_mfma_f32_16x16x32_bf16 v[132:135], a[12:15], v[84:87], v[132:135]\n"
      "ds_read_b64_tr_b16 v[120:121], v34 offset:12288\n"
      "v_mfma_f32_16x16x32_bf16 v[148:151], a[16:19], v[84:87], v[148:151]\n"
      "global_load_dwordx4 a[176:179], v45, s[8:9] offset:0 sc1\n"
      "ds_read_b64_tr_b16 v[122:123], v34 offset:14336\n"
      "v_add_u32_dpp v3, v3, v3 quad_perm:[1,0,3,2] row_mask:0xf bank_mask:0xf\n"
      "v_mfma_f32_16x16x32_bf16 v[164:167], a[20:23], v[84:87], v[164:167]\n"
      "ds_read_b64_tr_b16 v[124:125], v35 offset:12288\n"
      "v_mfma_f32_16x16x32_bf16 v[136:139], a[12:15], v[88:91], v[136:139]\n"
      "ds_read_b64_tr_b16 v[126:127], v35 offset:14336\n"
      "v_add_u32_dpp v3, v3, v3 quad_perm:[2,3,0,1] row_mask:0xf bank_mask:0xf\n"
      "v_mfma_f32_16x16x32_bf16 v[152:155], a[16:19], v[88:91], v[152:155]\n"
      "v_mfma_f32_16x16x32_bf16 v[168:171], a[20:23], v[88:91], v[168:171]\n"
      "global_load_dwordx4 a[180:183], v45, s[8:9] offset:1024 sc1\n"
      "s_add_u32 m0, s73, 32768\n"
      "v_add_u32 v43, 16384, v40\n"
      "v_add_u32_dpp v3, v3, v3 row_half_mirror row_mask:0xf bank_mask:0xf\n"
      "v_cmp_ne_u32 vcc, s71, v3\n"
      "v_mfma_f32_16x16x32_bf16 v[140:143], a[12:15], v[92:95], v[140:143]\n"
      "v_mfma_f32_16x16x32_bf16 v[156:159], a[16:19], v[92:95], v[156:159]\n"
      "global_load_lds_dwordx4 v43, s[4:5] offset:0 sc1\n"
      "s_cbranch_vccnz L_sx1_%=\n"
      "L_bx1_%=:\n"
      "v_mfma_f32_16x16x32_bf16 v[172:175], a[20:23], v[92:95], v[172:175]\n"
      "s_waitcnt lgkmcnt(0)\n"
      "s_barrier\n"
      "v_mfma_f32_16x16x32_bf16 v[128:131], a[24:27], v[96:99], v[128:131]\n"
      "ds_read_b64_tr_b16 v[64:65], v6 offset:16384\n"
      "v_and_b32 v41, 0x1f0, v7\n"
      "v_mfma_f32_16x16x32_bf16 v[144:147], a[28:31], v[96:99], v[144:147]\n"
      "ds_read_b64_tr_b16 v[66:67], v6 offset:18432\n"
      "v_min_u32 v41, 0x170, v41\n"
      "v_mfma_f32_16x16x32_bf16 v[160:163], a[32:35], v[96:99], v[160:163]\n"
      "global_load_lds_dwordx4 v43, s[4:5] offset:1024 sc1\n"
      "v_lshrrev_b32 v52, 9, v7\n"
      "v_mfma_f32_16x16x32_bf16 v[132:135], a[24:27], v[100:103], v[132:135]\n"
      "ds_read_b64_tr_b16 v[68:69], v34 offset:16384\n"
      "v_mul_u32_u24 v52, 0x3c000, v52\n"
      "v_mfma_f32_16x16x32_bf16 v[148:151], a[28:31], v[100:103], v[148:151]\n"
      "ds_read_b64_tr_b16 v[70:71], v34 offset:18432\n"
      "v_add3_u32 v53, v52, v41, s65\n"
      "v_mfma_f32_16x16x32_bf16 v[164:167], a[32:35], v[100:103], v[164:167]\n"
      "ds_read_b64_tr_b16 v[72:73], v33 offset:16384\n"
      "s_mul_i32 s96, s60, 384\n"
      "v_mfma_f32_16x16x32_bf16 v[136:139], a[24:27], v[104:107], v[136:139]\n"
      "global_load_lds_dwordx4 v43, s[4:5] offset:2048 sc1\n"
      "s_add_u32 s96, s96, 160768\n"
      "v_mfma_f32_16x16x32_bf16 v[152:155], a[28:31], v[104:107], v[152:155]\n"
      "ds_read_b64_tr_b16 v[74:75], v33 offset:18432\n"
      "v_lshrrev_b32 v52, 4, v7\n"
      "v_mfma_f32_16x16x32_bf16 v[168:171], a[32:35], v[104:107], v[168:171]\n"
      "global_load_dwordx2 v[58:59], v42, s[26:27] sc1\n"
      "ds_read_b64_tr_b16 v[76:77], v35 offset:16384\n"
      "v_and_b32 v52, 0x30, v52\n"
      "v_mfma_f32_16x16x32_bf16 v[140:143], a[24:27], v[108:111], v[140:143]\n"
      "ds_read_b64_tr_b16 v[78:79], v35 offset:18432\n"
      "v_add_u32 v52, s96, v52\n"
      "v_mfma_f32_16x16x32_bf16 v[156:159], a[28:31], v[108:111], v[156:159]\n"
      "global_load_lds_dwordx4 v43, s[4:5] offset:3072 sc1\n"
      "s_mov_b32 s47, 32768\n"
      "v_mfma_f32_16x16x32_bf16 v[172:175], a[32:35], v[108:111], v[172:175]\n"
      "ds_read_b64_tr_b16 v[80:81], v6 offset:20480\n"
      "s_mul_i32 s40, s60, 9216\n"
      "v_mfma_f32_16x16x32_bf16 v[128:131], a[36:39], v[112:115], v[128:131]\n"
      "buffer_load_dwordx2 v[48:49], v5, s[32:35], s47 offen sc1\n"
      "ds_read_b64_tr_b16 v[82:83], v6 offset:22528\n"
      "s_add_u32 s40, s40, 122880\n"
      "v_mfma_f32_16x16x32_bf16 v[144:147], a[40:43], v[112:115], v[144:147]\n"
      "ds_read_b64_tr_b16 v[84:85], v34 offset:20480\n"
      "v_bfe_u32 v30, v7, 4, 3\n"
      "v_mfma_f32_16x16x32_bf16 v[160:163], a[44:47], v[112:115], v[160:163]\n"
      "ds_read_b64_tr_b16 v[86:87], v34 offset:22528\n"
      "v_lshrrev_b32 v31, 8, v7\n"
      "v_mfma_f32_16x16x32_bf16 v[132:135], a[36:39], v[116:119], v[132:135]\n"
      "ds_read_b64_tr_b16 v[88:89], v33 offset:20480\n"
      "v_lshl_add_u32 v31, v30, 2, v31\n"
      "v_mfma_f32_16x16x32_bf16 v[148:151], a[40:43], v[116:119], v[148:151]\n"
      "ds_read_b64_tr_b16 v[90:91], v33 offset:22528\n"
      "v_sub_u32 v30, 2, v30\n"
      "v_mfma_f32_16x16x32_bf16 v[164:167], a[44:47], v[116:119], v[164:167]\n"
      "ds_read_b64_tr_b16 v[92:93], v35 offset:20480\n"
      "s_add_u32 m0, s73, 49152\n"
      "v_add_u32 v43, 32768, v40\n"
      "v_mfma_f32_16x16x32_bf16 v[136:139], a[36:39], v[120:123], v[136:139]\n"
      "buffer_load_dwordx2 v[46:47], v51, s[32:35], s75 offen sc1\n"
      "global_load_lds_dwordx4 v43, s[4:5] offset:0 sc1\n"
      "v_and_b32 v29, 64, v4\n"
      "v_mfma_f32_16x16x32_bf16 v[152:155], a[40:43], v[120:123], v[152:155]\n"
      "ds_read_b64_tr_b16 v[94:95], v35 offset:22528\n"
      "v_lshl_add_u32 v31, v31, 9, v29\n"
      "v_mfma_f32_16x16x32_bf16 v[168:171], a[44:47], v[120:123], v[168:171]\n"
      "v_and_b32 v30, 0x80000000, v30\n"
      "v_or_b32 v31, v31, v30\n"
      "v_mfma_f32_16x16x32_bf16 v[140:143], a[36:39], v[124:127], v[140:143]\n"
      "v_add_u32 v50, s94, v7\n"
      "s_mul_i32 s72, s30, 3\n"
      "v_mfma_f32_16x16x32_bf16 v[156:159], a[40:43], v[124:127], v[156:159]\n"
      "global_load_lds_dwordx4 v43, s[4:5] offset:1024 sc1\n"
      "s_lshl_b32 s42, s60, 5\n"
      "v_mfma_f32_16x16x32_bf16 v[172:175], a[44:47], v[124:127], v[172:175]\n"
      "v_xor_b32 v27, s42, v6\n"
      "s_waitcnt lgkmcnt(0)\n"
      "v_mfma_f32_16x16x32_bf16 v[176:179], a[0:3], v[64:67], 0\n"
      "ds_read_b64_tr_b16 v[96:97], v6 offset:24576\n"
      "v_add_u32 v27, 110592, v27\n"
      "v_mfma_f32_16x16x32_bf16 v[184:187], a[4:7], v[64:67], 0\n"
      "ds_read_b64_tr_b16 v[98:99], v6 offset:26624\n"
      "v_mov_b32 v18, 0\n"
      "v_mfma_f32_16x16x32_bf16 v[192:195], a[8:11], v[64:67], 0\n"
      "global_load_lds_dwordx4 v43, s[4:5] offset:2048 sc1\n"
      "s_waitcnt vmcnt(4)\n"
      "v_mfma_f32_16x16x32_bf16 v[180:183], a[0:3], v[68:71], 0\n"
      "ds_read_b128 v[248:251], v40 offset:32768\n"
      "v_mov_b32 v19, 0\n"
      "v_mfma_f32_16x16x32_bf16 v[188:191], a[4:7], v[68:71], 0\n"
      "ds_read_b128 v[252:255], v40 offset:33792\n"
      "ds_read_b64_tr_b16 v[100:101], v34 offset:24576\n"
      "v_mov_b32 v28, 0x3f803f80\n"
      "v_mov_b32 v29, 0x3f803f80\n"
      "v_mov_b32 v30, 0x3f803f80\n"
      "v_mfma_f32_16x16x32_bf16 v[196:199], a[8:11], v[68:71], 0\n"
      "global_load_lds_dwordx4 v43, s[4:5] offset:3072 sc1\n"
      "ds_read_b128 v[36:39], v40 offset:34816\n"
      "ds_read_b64_tr_b16 v[102:103], v34 offset:26624\n"
      "s_mov_b32 s47, 65536\n"
      "s_lshl_b32 s41, s60, 13\n"
      "v_mfma_f32_16x16x32_bf16 v[200:203], a[0:3], v[72:75], 0\n"
      "buffer_load_dwordx2 v[54:55], v5, s[32:35], s47 offen sc1\n"
      "ds_read_b128 v[60:63], v40 offset:35840\n"
      "ds_read_b64_tr_b16 v[104:105], v33 offset:24576\n"
      "s_mov_b32 m0, s94\n"
      "v_mfma_f32_16x16x32_bf16 v[208:211], a[4:7], v[72:75], 0\n"
      "buffer_load_dwordx4 v7, s[32:35], s67 offen offset:0 sc1 lds\n"
      "ds_read_b64_tr_b16 v[106:107], v33 offset:26624\n"
      "s_add_u32 s41, s41, 65536\n"
      "v_mfma_f32_16x16x32_bf16 v[216:219], a[8:11], v[72:75], 0\n"
      "ds_read_b64_tr_b16 v[108:109], v35 offset:24576\n"
      "s_mov_b32 s48, 0xaaaaaaaa\n"
      "v_mfma_f32_16x16x32_bf16 v[204:207], a[0:3], v[76:79], 0\n"
      "ds_read_b64_tr_b16 v[110:111], v35 offset:26624\n"
      "s_mov_b32 s49, s48\n"
      "v_mfma_f32_16x16x32_bf16 v[212:215], a[4:7], v[76:79], 0\n"
      "ds_read_b64_tr_b16 v[112:113], v6 offset:28672\n"
      "s_waitcnt lgkmcnt(5)\n"
      "v_add3_u32 v48, v48, v248, v249\n"
      "v_add3_u32 v48, v48, v250, v251\n"
      "v_mfma_f32_16x16x32_bf16 v[220:223], a[8:11], v[76:79], 0\n"
      "buffer_load_dwordx4 v7, s[32:35], s67 offen offset:1024 sc1 lds\n"
      "v_add3_u32 v48, v48, v252, v253\n"
      "v_add3_u32 v48, v48, v254, v255\n"
      "v_mfma_f32_16x16x32_bf16 v[176:179], a[12:15], v[80:83], v[176:179]\n"
      "ds_read_b64_tr_b16 v[114:115], v6 offset:30720\n"
      "v_add3_u32 v48, v48, v36, v37\n"
      "v_add3_u32 v48, v48, v38, v39\n"
      "v_mfma_f32_16x16x32_bf16 v[184:187], a[16:19], v[80:83], v[184:187]\n"
      "ds_read_b64_tr_b16 v[116:117], v34 offset:28672\n"
      "v_add3_u32 v48, v48, v60, v61\n"
      "v_mfma_f32_16x16x32_bf16 v[192:195], a[20:23], v[80:83], v[192:195]\n"
      "ds_read_b64_tr_b16 v[118:119], v34 offset:30720\n"
      "v_add3_u32 v48, v48, v62, v63\n"
      "s_mov_b32 s50, 0xcccccccc\n"
      "v_mfma_f32_16x16x32_bf16 v[180:183], a[12:15], v[84:87], v[180:183]\n"
      "buffer_load_dwordx4 v7, s[32:35], s67 offen offset:2048 sc1 lds\n"
      "ds_read_b64_tr_b16 v[120:121], v33 offset:28672\n"
      "s_mov_b32 s51, s50\n"
      "v_mfma_f32_16x16x32_bf16 v[188:191], a[16:19], v[84:87], v[188:191]\n"
      "ds_read_b64_tr_b16 v[122:123], v33 offset:30720\n"
      "v_add_u32_dpp v48, v48, v48 quad_perm:[1,0,3,2] row_mask:0xf bank_mask:0xf\n"
      "v_mfma_f32_16x16x32_bf16 v[196:199], a[20:23], v[84:87], v[196:199]\n"
      "ds_read_b64_tr_b16 v[124:125], v35 offset:28672\n"
      "s_lshl_b32 s59, s60, 31\n"
      "v_mfma_f32_16x16x32_bf16 v[200:203], a[12:15], v[88:91], v[200:203]\n"
      "ds_read_b64_tr_b16 v[126:127], v35 offset:30720\n"
      "v_add_u32_dpp v48, v48, v48 quad_perm:[2,3,0,1] row_mask:0xf bank_mask:0xf\n"
      "v_mfma_f32_16x16x32_bf16 v[208:211], a[16:19], v[88:91], v[208:211]\n"
      "buffer_load_dword v32, v31, s[32:35], s68 offen sc1\n"
      "s_lshl_b32 s54, s30, 1\n"
      "v_mfma_f32_16x16x32_bf16 v[216:219], a[20:23], v[88:91], v[216:219]\n"
      "v_add_u32_dpp v48, v48, v48 row_half_mirror row_mask:0xf bank_mask:0xf\n"
      "v_cmp_ne_u32 vcc, s71, v48\n"
      "v_mfma_f32_16x16x32_bf16 v[204:207], a[12:15], v[92:95], v[204:207]\n"
      "s_lshl_b32 s55, s60, 6\n"
      "s_add_u32 s55, s55, 163328\n"
      "v_mfma_f32_16x16x32_bf16 v[212:215], a[16:19], v[92:95], v[212:215]\n"
      "s_cbranch_vccnz L_sx2_%=\n"
      "L_bx2_%=:\n"
      "v_mfma_f32_16x16x32_bf16 v[220:223], a[20:23], v[92:95], v[220:223]\n"
      "s_waitcnt lgkmcnt(0)\n"
      "s_barrier\n"
      "v_mfma_f32_16x16x32_bf16 v[176:179], a[24:27], v[96:99], v[176:179]\n"
      "ds_read_b64_tr_b16 v[64:65], v6 offset:32768\n"
      "v_mfma_f32_16x16x32_bf16 v[184:187], a[28:31], v[96:99], v[184:187]\n"
      "ds_read_b64_tr_b16 v[66:67], v6 offset:34816\n"
      "v_mfma_f32_16x16x32_bf16 v[192:195], a[32:35], v[96:99], v[192:195]\n"
      "ds_read_b64_tr_b16 v[68:69], v33 offset:32768\n"
      "s_waitcnt vmcnt(9)\n"
      "v_cmp_ne_u64 s[52:53], s[30:31], v[46:47]\n"
      "v_mfma_f32_16x16x32_bf16 v[180:183], a[24:27], v[100:103], v[180:183]\n"
      "ds_read_b64_tr_b16 v[70:71], v33 offset:34816\n"
      "s_add_u32 m0, s73, 0\n"
      "v_add_u32 v43, 49152, v40\n"
      "v_add_u32 v44, 4096, v7\n"
      "v_cmp_ne_u64 vcc, s[30:31], v[58:59]\n"
      "v_mfma_f32_16x16x32_bf16 v[188:191], a[28:31], v[100:103], v[188:191]\n"
      "ds_read_b64_tr_b16 v[72:73], v34 offset:32768\n"
      "s_waitcnt vmcnt(4)\n"
      "v_mfma_f32_16x16x32_bf16 v[196:199], a[32:35], v[100:103], v[196:199]\n"
      "global_load_lds_dwordx4 v43, s[4:5] offset:0 sc1\n"
      "ds_read_b128 v[248:251], v40 offset:49152\n"
      "s_or_b64 vcc, vcc, s[52:53]\n"
      "v_mfma_f32_16x16x32_bf16 v[200:203], a[24:27], v[104:107], v[200:203]\n"
      "ds_read_b128 v[252:255], v40 offset:50176\n"
      "s_cbranch_vccnz L_sfd_%=\n"
      "L_bfd_%=:\n"
      "v_mfma_f32_16x16x32_bf16 v[208:211], a[28:31], v[104:107], v[208:211]\n"
      "ds_read_b128 v[36:39], v40 offset:51200\n"
      "ds_read_b64_tr_b16 v[74:75], v34 offset:34816\n"
      "v_add_u32 v51, s96, v41\n"
      "v_mfma_f32_16x16x32_bf16 v[216:219], a[32:35], v[104:107], v[216:219]\n"
      "ds_read_b128 v[60:63], v40 offset:52224\n"
      "ds_read_b64_tr_b16 v[76:77], v35 offset:32768\n"
      "v_lshrrev_b32 v42, 2, v7\n"
      "v_mfma_f32_16x16x32_bf16 v[204:207], a[24:27], v[108:111], v[204:207]\n"
      "global_load_lds_dwordx4 v43, s[4:5] offset:1024 sc1\n"
      "ds_read_b64_tr_b16 v[78:79], v35 offset:34816\n"
      "s_lshl_b32 s97, s60, 10\n"
      "v_mfma_f32_16x16x32_bf16 v[212:215], a[28:31], v[108:111], v[212:215]\n"
      "ds_read_b64_tr_b16 v[80:81], v6 offset:36864\n"
      "s_add_u32 s96, s97, 98304\n"
      "v_mfma_f32_16x16x32_bf16 v[220:223], a[32:35], v[108:111], v[220:223]\n"
      "ds_read_b64_tr_b16 v[82:83], v6 offset:38912\n"
      "v_add_u32 v47, s96, v7\n"
      "v_mfma_f32_16x16x32_bf16 v[176:179], a[36:39], v[112:115], v[176:179]\n"
      "ds_read_b64_tr_b16 v[84:85], v33 offset:36864\n"
      "s_waitcnt lgkmcnt(5)\n"
      "v_add3_u32 v54, v54, v248, v249\n"
      "v_add3_u32 v54, v54, v250, v251\n"
      "v_mfma_f32_16x16x32_bf16 v[184:187], a[40:43], v[112:115], v[184:187]\n"
      "global_load_lds_dwordx4 v43, s[4:5] offset:2048 sc1\n"
      "v_add3_u32 v54, v54, v252, v253\n"
      "v_add3_u32 v54, v54, v254, v255\n"
      "v_mfma_f32_16x16x32_bf16 v[192:195], a[44:47], v[112:115], v[192:195]\n"
      "ds_read_b64_tr_b16 v[86:87], v33 offset:38912\n"
      "v_add3_u32 v54, v54, v36, v37\n"
      "v_add3_u32 v54, v54, v38, v39\n"
      "v_mfma_f32_16x16x32_bf16 v[180:183], a[36:39], v[116:119], v[180:183]\n"
      "ds_read_b64_tr_b16 v[88:89], v34 offset:36864\n"
      "v_add3_u32 v54, v54, v60, v61\n"
      "v_mfma_f32_16x16x32_bf16 v[188:191], a[40:43], v[116:119], v[188:191]\n"
      "ds_read_b64_tr_b16 v[90:91], v34 offset:38912\n"
      "v_add3_u32 v54, v54, v62, v63\n"
      "v_mfma_f32_16x16x32_bf16 v[196:199], a[44:47], v[116:119], v[196:199]\n"
      "global_load_lds_dwordx4 v43, s[4:5] offset:3072 sc1\n"
      "ds_read_b64_tr_b16 v[92:93], v35 offset:36864\n"
      "s_mov_b32 s47, 98304\n"
      "v_mfma_f32_16x16x32_bf16 v[200:203], a[36:39], v[120:123], v[200:203]\n"
      "ds_read_b64_tr_b16 v[94:95], v35 offset:38912\n"
      "v_add_u32_dpp v54, v54, v54 quad_perm:[1,0,3,2] row_mask:0xf bank_mask:0xf\n"
      "v_mfma_f32_16x16x32_bf16 v[208:211], a[40:43], v[120:123], v[208:211]\n"
      "v_bfe_u32 v46, v7, 8, 1\n"
      "v_xor_b32 v46, s60, v46\n"
      "v_mfma_f32_16x16x32_bf16 v[216:219], a[44:47], v[120:123], v[216:219]\n"
      "v_add_u32_dpp v54, v54, v54 quad_perm:[2,3,0,1] row_mask:0xf bank_mask:0xf\n"
      "v_lshl_add_u32 v46, v46, 2, v7\n"
      "v_mfma_f32_16x16x32_bf16 v[204:207], a[36:39], v[124:127], v[204:207]\n"
      "buffer_load_dwordx2 v[56:57], v5, s[32:35], s47 offen sc1\n"
      "v_add_u32 v46, 98304, v46\n"
      "v_mfma_f32_16x16x32_bf16 v[212:215], a[40:43], v[124:127], v[212:215]\n"
      "v_add_u32_dpp v54, v54, v54 row_half_mirror row_mask:0xf bank_mask:0xf\n"
      "v_cmp_ne_u32 vcc, s71, v54\n"
      "v_mfma_f32_16x16x32_bf16 v[220:223], a[44:47], v[124:127], v[220:223]\n"
      "s_waitcnt lgkmcnt(0)\n"
      "v_mfma_f32_16x16x32_bf16 v[128:131], a[48:51], v[64:67], v[128:131]\n"
      "ds_read_b64_tr_b16 v[96:97], v6 offset:40960\n"
      "s_cbranch_vccnz L_sx3_%=\n"
      "L_bx3_%=:\n"
      "v_and_b32 v41, 0x78, v4\n"
      "v_mfma_f32_16x16x32_bf16 v[144:147], a[52:55], v[64:67], v[144:147]\n"
      "ds_read_b64_tr_b16 v[98:99], v6 offset:43008\n"
      "v_add_u32 v41, 162304, v41\n"
      "v_mfma_f32_16x16x32_bf16 v[160:163], a[56:59], v[64:67], v[160:163]\n"
      "ds_read_b64_tr_b16 v[100:101], v33 offset:40960\n"
      "s_waitcnt vmcnt(5)\n"
      "v_mfma_f32_16x16x32_bf16 v[132:135], a[48:51], v[68:71], v[132:135]\n"
      "ds_read_b128 v[248:251], v50 offset:0\n"
      "s_add_u32 m0, s73, 16384\n"
      "v_add_u32 v43, 65536, v40\n"
      "v_mfma_f32_16x16x32_bf16 v[148:151], a[52:55], v[68:71], v[148:151]\n"
      "ds_read_b128 v[252:255], v50 offset:1024\n"
      "v_mfma_f32_16x16x32_bf16 v[164:167], a[56:59], v[68:71], v[164:167]\n"
      "global_load_dwordx4 v[224:227], v53, s[26:27] sc1\n"
      "ds_read_b64_tr_b16 v[102:103], v33 offset:43008\n"
      "v_mfma_f32_16x16x32_bf16 v[136:139], a[48:51], v[72:75], v[136:139]\n"
      "ds_read_b128 v[36:39], v50 offset:2048\n"
      "ds_read_b64_tr_b16 v[104:105], v34 offset:40960\n"
      "v_add_u32 v53, 0x6000, v53\n"
      "v_mfma_f32_16x16x32_bf16 v[152:155], a[52:55], v[72:75], v[152:155]\n"
      "ds_read_b64_tr_b16 v[106:107], v34 offset:43008\n"
      "v_mfma_f32_16x16x32_bf16 v[168:171], a[56:59], v[72:75], v[168:171]\n"
      "global_load_dwordx4 v[228:231], v53, s[26:27] sc1\n"
      "ds_read_b64_tr_b16 v[108:109], v35 offset:40960\n"
      "v_mfma_f32_16x16x32_bf16 v[140:143], a[48:51], v[76:79], v[140:143]\n"
      "ds_read_b64_tr_b16 v[110:111], v35 offset:43008\n"
      "v_add_u32 v53, 0x6000, v53\n"
      "v_mfma_f32_16x16x32_bf16 v[156:159], a[52:55], v[76:79], v[156:159]\n"
      "ds_read_b64_tr_b16 v[112:113], v6 offset:45056\n"
      "s_waitcnt lgkmcnt(5)\n"
      "v_add3_u32 v32, v32, v248, v249\n"
      "v_add3_u32 v32, v32, v250, v251\n"
      "v_mfma_f32_16x16x32_bf16 v[172:175], a[56:59], v[76:79], v[172:175]\n"
      "global_load_dwordx4 v[232:235], v53, s[26:27] sc1\n"
      "v_add3_u32 v32, v32, v252, v253\n"
      "v_mfma_f32_16x16x32_bf16 v[128:131], a[60:63], v[80:83], v[128:131]\n"
      "ds_read_b64_tr_b16 v[114:115], v6 offset:47104\n"
      "v_add_u32 v53, 0x6000, v53\n"
      "v_add3_u32 v32, v32, v254, v255\n"
      "v_mfma_f32_16x16x32_bf16 v[144:147], a[64:67], v[80:83], v[144:147]\n"
      "ds_read_b64_tr_b16 v[116:117], v33 offset:45056\n"
      "v_add3_u32 v32, v32, v36, v37\n"
      "v_mfma_f32_16x16x32_bf16 v[160:163], a[68:71], v[80:83], v[160:163]\n"
      "global_load_dwordx4 v[236:239], v53, s[26:27] sc1\n"
      "ds_read_b64_tr_b16 v[118:119], v33 offset:47104\n"
      "v_add3_u32 v32, v32, v38, v39\n"
      "v_mfma_f32_16x16x32_bf16 v[132:135], a[60:63], v[84:87], v[132:135]\n"
      "ds_read_b64_tr_b16 v[120:121], v34 offset:45056\n"
      "v_add_u32 v53, 0x6000, v53\n"
      "v_mfma_f32_16x16x32_bf16 v[148:151], a[64:67], v[84:87], v[148:151]\n"
      "ds_read_b64_tr_b16 v[122:123], v34 offset:47104\n"
      "v_add_u32_dpp v32, v32, v32 quad_perm:[1,0,3,2] row_mask:0xf bank_mask:0xf\n"
      "v_mfma_f32_16x16x32_bf16 v[164:167], a[68:71], v[84:87], v[164:167]\n"
      "global_load_dwordx4 v[240:243], v53, s[26:27] sc1\n"
      "ds_read_b64_tr_b16 v[124:125], v35 offset:45056\n"
      "v_mfma_f32_16x16x32_bf16 v[136:139], a[60:63], v[88:91], v[136:139]\n"
      "ds_read_b64_tr_b16 v[126:127], v35 offset:47104\n"
      "v_add_u32 v53, 0x6000, v53\n"
      "v_add_u32_dpp v32, v32, v32 quad_perm:[2,3,0,1] row_mask:0xf bank_mask:0xf\n"
      "v_mfma_f32_16x16x32_bf16 v[152:155], a[64:67], v[88:91], v[152:155]\n"
      "v_mfma_f32_16x16x32_bf16 v[168:171], a[68:71], v[88:91], v[168:171]\n"
      "global_load_dwordx4 v[244:247], v53, s[26:27] sc1\n"
      "v_add_u32_dpp v32, v32, v32 row_half_mirror row_mask:0xf bank_mask:0xf\n"
      "v_cmp_ne_u32 vcc, s72, v32\n"
      "v_mfma_f32_16x16x32_bf16 v[140:143], a[60:63], v[92:95], v[140:143]\n"
      "v_mfma_f32_16x16x32_bf16 v[156:159], a[64:67], v[92:95], v[156:159]\n"
      "s_cbranch_vccnz L_sxp_%=\n"
      "L_bxp_%=:\n"
      "v_mfma_f32_16x16x32_bf16 v[172:175], a[68:71], v[92:95], v[172:175]\n"
      "global_load_lds_dwordx4 v43, s[4:5] offset:0 sc1\n"
      "s_waitcnt lgkmcnt(0)\n"
      "s_barrier\n"
      "v_mfma_f32_16x16x32_bf16 v[128:131], a[72:75], v[96:99], v[128:131]\n"
      "ds_read_b64_tr_b16 v[8:9], v27 offset:0\n"
      "v_mfma_f32_16x16x32_bf16 v[144:147], a[76:79], v[96:99], v[144:147]\n"
      "ds_read_b64_tr_b16 v[10:11], v27 offset:2048\n"
      "v_mfma_f32_16x16x32_bf16 v[160:163], a[80:83], v[96:99], v[160:163]\n"
      "ds_read_b64_tr_b16 v[12:13], v27 offset:4096\n"
      "ds_read_b64_tr_b16 v[64:65], v6 offset:49152\n"
      "v_mfma_f32_16x16x32_bf16 v[132:135], a[72:75], v[100:103], v[132:135]\n"
      "global_load_lds_dwordx4 v43, s[4:5] offset:1024 sc1\n"
      "ds_read_b64_tr_b16 v[14:15], v27 offset:6144\n"
      "ds_read_b64_tr_b16 v[66:67], v6 offset:51200\n"
      "v_mfma_f32_16x16x32_bf16 v[148:151], a[76:79], v[100:103], v[148:151]\n"
      "ds_read_b64_tr_b16 v[16:17], v27 offset:8192\n"
      "v_mov_b32 v31, 0x3f803f80\n"
      "v_mfma_f32_16x16x32_bf16 v[164:167], a[80:83], v[100:103], v[164:167]\n"
      "ds_read_b64_tr_b16 v[68:69], v33 offset:49152\n"
      "v_and_b32 v50, 0xf0, v7\n"
      "v_mfma_f32_16x16x32_bf16 v[136:139], a[72:75], v[104:107], v[136:139]\n"
      "ds_read_b64_tr_b16 v[70:71], v33 offset:51200\n"
      "s_mov_b32 s42, 0x00f0000f\n"
      "v_mfma_f32_16x16x32_bf16 v[152:155], a[76:79], v[104:107], v[152:155]\n"
      "global_load_lds_dwordx4 v43, s[4:5] offset:2048 sc1\n"
      "s_mov_b32 s43, 0xf0000f00\n"
      "v_mfma_f32_16x16x32_bf16 v[168:171], a[80:83], v[104:107], v[168:171]\n"
      "ds_read_b64_tr_b16 v[72:73], v34 offset:49152\n"
      "v_bfrev_b32 v32, 1\n"
      "v_mfma_f32_16x16x32_bf16 v[140:143], a[72:75], v[108:111], v[140:143]\n"
      "ds_read_b64_tr_b16 v[74:75], v34 offset:51200\n"
      "v_cndmask_b32 v50, v32, v50, s[42:43]\n"
      "v_mfma_f32_16x16x32_bf16 v[156:159], a[76:79], v[108:111], v[156:159]\n"
      "ds_read_b64_tr_b16 v[76:77], v35 offset:49152\n"
      "v_mfma_f32_16x16x32_bf16 v[172:175], a[80:83], v[108:111], v[172:175]\n"
      "global_load_lds_dwordx4 v43, s[4:5] offset:3072 sc1\n"
      "s_waitcnt lgkmcnt(5)\n"
      "v_mfma_f32_16x16x32_bf16 v[20:23], v[28:31], v[8:11], 0\n"
      "ds_read_b64_tr_b16 v[78:79], v35 offset:51200\n"
      "s_mov_b32 s47, 131072\n"
      "v_mfma_f32_16x16x32_bf16 v[24:27], v[8:11], v[8:11], 0\n"
      "ds_read_b64_tr_b16 v[80:81], v6 offset:53248\n"
      "v_mfma_f32_16x16x32_bf16 v[20:23], v[28:31], v[12:15], v[20:23]\n"
      "ds_read_b64_tr_b16 v[82:83], v6 offset:55296\n"
      "v_mfma_f32_16x16x32_bf16 v[24:27], v[12:15], v[12:15], v[24:27]\n"
      "buffer_load_dwordx2 v[2:3], v5, s[32:35], s47 offen sc1\n"
      "v_mfma_f32_16x16x32_bf16 v[20:23], v[28:31], v[16:19], v[20:23]\n"
      "ds_read_b64_tr_b16 v[84:85], v33 offset:53248\n"
      "v_mfma_f32_16x16x32_bf16 v[24:27], v[16:19], v[16:19], v[24:27]\n"
      "ds_read_b64_tr_b16 v[86:87], v33 offset:55296\n"
      "v_mfma_f32_16x16x32_bf16 v[128:131], a[84:87], v[112:115], v[128:131]\n"
      "ds_read_b64_tr_b16 v[88:89], v34 offset:53248\n"
      "v_mfma_f32_16x16x32_bf16 v[144:147], a[88:91], v[112:115], v[144:147]\n"
      "ds_read_b64_tr_b16 v[90:91], v34 offset:55296\n"
      "v_mfma_f32_16x16x32_bf16 v[160:163], a[92:95], v[112:115], v[160:163]\n"
      "ds_read_b64_tr_b16 v[92:93], v35 offset:53248\n"
      "v_cndmask_b32 v24, v24, v25, s[48:49]\n"
      "v_cndmask_b32 v26, v26, v27, s[48:49]\n"
      "v_mfma_f32_16x16x32_bf16 v[132:135], a[84:87], v[116:119], v[132:135]\n"
      "ds_read_b64_tr_b16 v[94:95], v35 offset:55296\n"
      "s_add_u32 m0, s73, 32768\n"
      "v_add_u32 v43, 98304, v40\n"
      "v_mov_b32 v22, s30\n"
      "v_cndmask_b32 v21, v24, v26, s[50:51]\n"
      "v_mfma_f32_16x16x32_bf16 v[148:151], a[88:91], v[116:119], v[148:151]\n"
      "v_mov_b32 v23, s31\n"
      "v_mfma_f32_16x16x32_bf16 v[164:167], a[92:95], v[116:119], v[164:167]\n"
      "buffer_store_dwordx4 v[20:23], v50, s[32:35], s92 offen sc1\n"
      "v_and_b32 v32, 0x200, v7\n"
      "v_mfma_f32_16x16x32_bf16 v[136:139], a[84:87], v[120:123], v[136:139]\n"
      "v_and_b32 v50, 0x1f0, v7\n"
      "v_lshl_add_u32 v50, v32, 3, v50\n"
      "v_mfma_f32_16x16x32_bf16 v[152:155], a[88:91], v[120:123], v[152:155]\n"
      "global_load_dwordx4 a[184:187], v45, s[8:9] offset:2048 sc1\n"
      "v_and_b32 v32, 0xf8, v4\n"
      "v_mfma_f32_16x16x32_bf16 v[168:171], a[92:95], v[120:123], v[168:171]\n"
      "v_add_u32 v32, s95, v32\n"
      "v_mfma_f32_16x16x32_bf16 v[140:143], a[84:87], v[124:127], v[140:143]\n"
      "v_mfma_f32_16x16x32_bf16 v[156:159], a[88:91], v[124:127], v[156:159]\n"
      "v_mfma_f32_16x16x32_bf16 v[172:175], a[92:95], v[124:127], v[172:175]\n"
      "global_load_dwordx4 a[188:191], v45, s[8:9] offset:3072 sc1\n"
      "s_waitcnt lgkmcnt(0)\n"
      "v_mfma_f32_16x16x32_bf16 v[128:131], a[96:99], v[64:67], v[128:131]\n"
      "ds_read_b64_tr_b16 v[96:97], v6 offset:57344\n"
      "v_mfma_f32_16x16x32_bf16 v[144:147], a[100:103], v[64:67], v[144:147]\n"
      "ds_read_b64_tr_b16 v[98:99], v6 offset:59392\n"
      "v_add_u32 v45, 53248, v7\n"
      "v_mfma_f32_16x16x32_bf16 v[160:163], a[104:107], v[64:67], v[160:163]\n"
      "ds_read_b64_tr_b16 v[100:101], v33 offset:57344\n"
      "s_waitcnt vmcnt(14)\n"
      "v_mfma_f32_16x16x32_bf16 v[132:135], a[96:99], v[68:71], v[132:135]\n"
      "global_load_dwordx4 a[192:195], v45, s[8:9] offset:-4096 sc1\n"
      "ds_read_b128 v[248:251], v40 offset:0\n"
      "v_mfma_f32_16x16x32_bf16 v[148:151], a[100:103], v[68:71], v[148:151]\n"
      "ds_read_b128 v[252:255], v40 offset:1024\n"
      "v_mfma_f32_16x16x32_bf16 v[164:167], a[104:107], v[68:71], v[164:167]\n"
      "ds_read_b128 v[36:39], v40 offset:2048\n"
      "ds_read_b64_tr_b16 v[102:103], v33 offset:59392\n"
      "v_mfma_f32_16x16x32_bf16 v[136:139], a[96:99], v[72:75], v[136:139]\n"
      "ds_read_b128 v[60:63], v40 offset:3072\n"
      "ds_read_b64_tr_b16 v[104:105], v34 offset:57344\n"
      "v_mfma_f32_16x16x32_bf16 v[152:155], a[100:103], v[72:75], v[152:155]\n"
      "global_load_dwordx4 a[196:199], v45, s[8:9] offset:-3072 sc1\n"
      "ds_read_b64_tr_b16 v[106:107], v34 offset:59392\n"
      "v_mfma_f32_16x16x32_bf16 v[168:171], a[104:107], v[72:75], v[168:171]\n"
      "ds_read_b64_tr_b16 v[108:109], v35 offset:57344\n"
      "v_mfma_f32_16x16x32_bf16 v[140:143], a[96:99], v[76:79], v[140:143]\n"
      "ds_read_b64_tr_b16 v[110:111], v35 offset:59392\n"
      "v_mfma_f32_16x16x32_bf16 v[156:159], a[100:103], v[76:79], v[156:159]\n"
      "ds_read_b64_tr_b16 v[112:113], v6 offset:61440\n"
      "s_waitcnt lgkmcnt(5)\n"
      "v_add3_u32 v56, v56, v248, v249\n"
      "v_add3_u32 v56, v56, v250, v251\n"
      "v_mfma_f32_16x16x32_bf16 v[172:175], a[104:107], v[76:79], v[172:175]\n"
      "global_load_dwordx4 a[200:203], v45, s[8:9] offset:-2048 sc1\n"
      "v_add3_u32 v56, v56, v252, v253\n"
      "v_add3_u32 v56, v56, v254, v255\n"
      "v_mfma_f32_16x16x32_bf16 v[128:131], a[108:111], v[80:83], v[128:131]\n"
      "ds_read_b64_tr_b16 v[114:115], v6 offset:63488\n"
      "v_add3_u32 v56, v56, v36, v37\n"
      "v_add3_u32 v56, v56, v38, v39\n"
      "v_mfma_f32_16x16x32_bf16 v[144:147], a[112:115], v[80:83], v[144:147]\n"
      "ds_read_b64_tr_b16 v[116:117], v33 offset:61440\n"
      "v_add3_u32 v56, v56, v60, v61\n"
      "v_mfma_f32_16x16x32_bf16 v[160:163], a[116:119], v[80:83], v[160:163]\n"
      "ds_read_b64_tr_b16 v[118:119], v33 offset:63488\n"
      "v_add3_u32 v56, v56, v62, v63\n"
      "v_mfma_f32_16x16x32_bf16 v[132:135], a[108:111], v[84:87], v[132:135]\n"
      "global_load_dwordx4 a[204:207], v45, s[8:9] offset:-1024 sc1\n"
      "ds_read_b64_tr_b16 v[120:121], v34 offset:61440\n"
      "v_mfma_f32_16x16x32_bf16 v[148:151], a[112:115], v[84:87], v[148:151]\n"
      "ds_read_b64_tr_b16 v[122:123], v34 offset:63488\n"
      "v_add_u32_dpp v56, v56, v56 quad_perm:[1,0,3,2] row_mask:0xf bank_mask:0xf\n"
      "v_mfma_f32_16x16x32_bf16 v[164:167], a[116:119], v[84:87], v[164:167]\n"
      "ds_read_b64_tr_b16 v[124:125], v35 offset:61440\n"
      "s_waitcnt vmcnt(12)\n"
      "v_mfma_f32_16x16x32_bf16 v[136:139], a[108:111], v[88:91], v[136:139]\n"
      "ds_read_b64_tr_b16 v[126:127], v35 offset:63488\n"
      "v_add_u32_dpp v56, v56, v56 quad_perm:[2,3,0,1] row_mask:0xf bank_mask:0xf\n"
      "v_mfma_f32_16x16x32_bf16 v[152:155], a[112:115], v[88:91], v[152:155]\n"
      "global_load_dwordx4 a[208:211], v45, s[8:9] offset:0 sc1\n"
      "v_add_f32 v224, v224, v228\n"
      "v_mfma_f32_16x16x32_bf16 v[168:171], a[116:119], v[88:91], v[168:171]\n"
      "v_add_u32_dpp v56, v56, v56 row_half_mirror row_mask:0xf bank_mask:0xf\n"
      "v_cmp_ne_u32 vcc, s71, v56\n"
      "v_add_f32 v225, v225, v229\n"
      "v_mfma_f32_16x16x32_bf16 v[140:143], a[108:111], v[92:95], v[140:143]\n"
      "v_add_f32 v226, v226, v230\n"
      "v_add_f32 v227, v227, v231\n"
      "v_mfma_f32_16x16x32_bf16 v[156:159], a[112:115], v[92:95], v[156:159]\n"
      "s_cbranch_vccnz L_sx4_%=\n"
      "L_bx4_%=:\n"
      "v_add_f32 v232, v232, v236\n"
      "v_mfma_f32_16x16x32_bf16 v[172:175], a[116:119], v[92:95], v[172:175]\n"
      "global_load_dwordx4 a[212:215], v45, s[8:9] offset:1024 sc1\n"
      "s_waitcnt lgkmcnt(0)\n"
      "s_barrier\n"
      "v_mfma_f32_16x16x32_bf16 v[128:131], a[120:123], v[96:99], v[128:131]\n"
      "ds_read_b64_tr_b16 v[64:65], v6 offset:0\n"
      "v_add_f32 v233, v233, v237\n"
      "L_pg3_%=:\n"
      "s_cbranch_execz L_pgr_%=\n"
      "v_mfma_f32_16x16x32_bf16 v[144:147], a[124:127], v[96:99], v[144:147]\n"
      "ds_read_b64_tr_b16 v[66:67], v6 offset:2048\n"
      "v_add_f32 v234, v234, v238\n"
      "v_mfma_f32_16x16x32_bf16 v[160:163], a[128:131], v[96:99], v[160:163]\n"
      "ds_read_b64_tr_b16 v[68:69], v33 offset:0\n"
      "v_add_f32 v235, v235, v239\n"
      "v_mfma_f32_16x16x32_bf16 v[132:135], a[120:123], v[100:103], v[132:135]\n"
      "global_load_lds_dwordx4 v43, s[4:5] offset:0 sc1\n"
      "v_add_f32 v240, v240, v244\n"
      "v_mfma_f32_16x16x32_bf16 v[148:151], a[124:127], v[100:103], v[148:151]\n"
      "ds_read_b64_tr_b16 v[70:71], v33 offset:2048\n"
      "v_add_f32 v241, v241, v245\n"
      "v_mfma_f32_16x16x32_bf16 v[164:167], a[128:131], v[100:103], v[164:167]\n"
      "ds_read_b64_tr_b16 v[72:73], v34 offset:0\n"
      "v_add_f32 v242, v242, v246\n"
      "v_mfma_f32_16x16x32_bf16 v[136:139], a[120:123], v[104:107], v[136:139]\n"
      "ds_read_b64_tr_b16 v[74:75], v34 offset:2048\n"
      "v_add_f32 v243, v243, v247\n"
      "v_mfma_f32_16x16x32_bf16 v[152:155], a[124:127], v[104:107], v[152:155]\n"
      "global_load_dwordx4 a[216:219], v45, s[8:9] offset:2048 sc1\n"
      "v_add_f32 v224, v224, v232\n"
      "v_mfma_f32_16x16x32_bf16 v[168:171], a[128:131], v[104:107], v[168:171]\n"
      "ds_read_b64_tr_b16 v[76:77], v35 offset:0\n"
      "v_add_f32 v225, v225, v233\n"
      "v_mfma_f32_16x16x32_bf16 v[140:143], a[120:123], v[108:111], v[140:143]\n"
      "ds_read_b64_tr_b16 v[78:79], v35 offset:2048\n"
      "v_add_f32 v226, v226, v234\n"
      "v_mfma_f32_16x16x32_bf16 v[156:159], a[124:127], v[108:111], v[156:159]\n"
      "ds_read_b64_tr_b16 v[80:81], v6 offset:4096\n"
      "v_add_f32 v227, v227, v235\n"
      "v_mfma_f32_16x16x32_bf16 v[172:175], a[128:131], v[108:111], v[172:175]\n"
      "global_load_lds_dwordx4 v43, s[4:5] offset:1024 sc1\n"
      "v_add_f32 v224, v224, v240\n"
      "v_mfma_f32_16x16x32_bf16 v[128:131], a[132:135], v[112:115], v[128:131]\n"
      "ds_read_b64_tr_b16 v[82:83], v6 offset:6144\n"
      "v_add_f32 v225, v225, v241\n"
      "v_mfma_f32_16x16x32_bf16 v[144:147], a[136:139], v[112:115], v[144:147]\n"
      "ds_read_b64_tr_b16 v[84:85], v33 offset:4096\n"
      "v_add_f32 v226, v226, v242\n"
      "v_mfma_f32_16x16x32_bf16 v[160:163], a[140:143], v[112:115], v[160:163]\n"
      "ds_read_b64_tr_b16 v[86:87], v33 offset:6144\n"
      "v_add_f32 v227, v227, v243\n"
      "v_add_u32 v53, 0x6000, v53\n"
      "v_mfma_f32_16x16x32_bf16 v[132:135], a[132:135], v[116:119], v[132:135]\n"
      "global_load_dwordx4 v[228:231], v53, s[26:27] sc1\n"
      "ds_read_b64_tr_b16 v[88:89], v34 offset:4096\n"
      "v_mfma_f32_16x16x32_bf16 v[148:151], a[136:139], v[116:119], v[148:151]\n"
      "ds_read_b64_tr_b16 v[90:91], v34 offset:6144\n"
      "v_add_u32 v53, 0x6000, v53\n"
      "v_mfma_f32_16x16x32_bf16 v[164:167], a[140:143], v[116:119], v[164:167]\n"
      "ds_read_b64_tr_b16 v[92:93], v35 offset:4096\n"
      "v_mfma_f32_16x16x32_bf16 v[136:139], a[132:135], v[120:123], v[136:139]\n"
      "global_load_dwordx4 v[232:235], v53, s[26:27] sc1\n"
      "v_add_u32 v53, 0x6000, v53\n"
      "v_mfma_f32_16x16x32_bf16 v[152:155], a[136:139], v[120:123], v[152:155]\n"
      "global_load_dwordx4 v[236:239], v53, s[26:27] sc1\n"
      "ds_read_b64_tr_b16 v[94:95], v35 offset:6144\n"
      "v_add_u32 v53, 0x6000, v53\n"
      "v_mfma_f32_16x16x32_bf16 v[168:171], a[140:143], v[120:123], v[168:171]\n"
      "global_load_dwordx4 a[220:223], v45, s[8:9] offset:3072 sc1\n"
      "global_load_dwordx4 v[240:243], v53, s[26:27] sc1\n"
      "v_and_b32 v53, 0x300, v7\n"
      "v_mfma_f32_16x16x32_bf16 v[140:143], a[132:135], v[124:127], v[140:143]\n"
      "v_and_b32 v59, 0x30, v7\n"
      "v_lshl_add_u32 v53, v59, 2, v53\n"
      "v_mfma_f32_16x16x32_bf16 v[156:159], a[136:139], v[124:127], v[156:159]\n"
      "v_bfe_u32 v59, v7, 6, 2\n"
      "v_lshl_add_u32 v53, v59, 4, v53\n"
      "v_lshrrev_b32 v59, 6, v7\n"
      "v_mfma_f32_16x16x32_bf16 v[172:175], a[140:143], v[124:127], v[172:175]\n"
      "global_load_lds_dwordx4 v43, s[4:5] offset:2048 sc1\n"
      "v_add_u32 v45, 61440, v7\n"
      "s_waitcnt lgkmcnt(0)\n"
      "v_mfma_f32_16x16x32_bf16 v[128:131], a[144:147], v[64:67], v[128:131]\n"
      "global_load_dwordx4 a[224:227], v45, s[8:9] offset:-4096 sc1\n"
      "buffer_load_dwordx4 v[244:247], v53, s[32:35], s66 offen sc1\n"
      "v_mfma_f32_16x16x32_bf16 v[144:147], a[148:151], v[64:67], v[144:147]\n"
      "ds_read_b64_tr_b16 v[96:97], v6 offset:8192\n"
      "v_mfma_f32_16x16x32_bf16 v[160:163], a[152:155], v[64:67], v[160:163]\n"
      "ds_read_b64_tr_b16 v[98:99], v6 offset:10240\n"
      "s_waitcnt vmcnt(20)\n"
      "v_mfma_f32_16x16x32_bf16 v[132:135], a[144:147], v[68:71], v[132:135]\n"
      "global_load_lds_dwordx4 v43, s[4:5] offset:3072 sc1\n"
      "ds_read_b128 v[248:251], v40 offset:16384\n"
      "v_mfma_f32_16x16x32_bf16 v[148:151], a[148:151], v[68:71], v[148:151]\n"
      "global_load_dwordx4 a[228:231], v45, s[8:9] offset:-3072 sc1\n"
      "ds_read_b128 v[252:255], v40 offset:17408\n"
      "ds_read_b64_tr_b16 v[100:101], v33 offset:8192\n"
      "v_mfma_f32_16x16x32_bf16 v[164:167], a[152:155], v[68:71], v[164:167]\n"
      "ds_read_b128 v[36:39], v40 offset:18432\n"
      "ds_read_b64_tr_b16 v[102:103], v33 offset:10240\n"
      "v_mfma_f32_16x16x32_bf16 v[136:139], a[144:147], v[72:75], v[136:139]\n"
      "ds_read_b128 v[60:63], v40 offset:19456\n"
      "ds_read_b64_tr_b16 v[104:105], v34 offset:8192\n"
      "v_mfma_f32_16x16x32_bf16 v[152:155], a[148:151], v[72:75], v[152:155]\n"
      "ds_read_b64_tr_b16 v[106:107], v34 offset:10240\n"
      "v_mfma_f32_16x16x32_bf16 v[168:171], a[152:155], v[72:75], v[168:171]\n"
      "buffer_load_dwordx4 v[8:11], v50, s[32:35], s93 offen offset:0 sc1\n"
      "global_load_dwordx4 a[232:235], v45, s[8:9] offset:-2048 sc1\n"
      "ds_read_b64_tr_b16 v[108:109], v35 offset:8192\n"
      "v_mfma_f32_16x16x32_bf16 v[140:143], a[144:147], v[76:79], v[140:143]\n"
      "ds_read_b64_tr_b16 v[110:111], v35 offset:10240\n"
      "v_mfma_f32_16x16x32_bf16 v[156:159], a[148:151], v[76:79], v[156:159]\n"
      "ds_read_b64_tr_b16 v[112:113], v6 offset:12288\n"
      "s_waitcnt lgkmcnt(5)\n"
      "v_add3_u32 v2, v2, v248, v249\n"
      "v_add3_u32 v2, v2, v250, v251\n"
      "v_mfma_f32_16x16x32_bf16 v[172:175], a[152:155], v[76:79], v[172:175]\n"
      "ds_read_b64_tr_b16 v[114:115], v6 offset:14336\n"
      "v_add3_u32 v2, v2, v252, v253\n"
      "v_add3_u32 v2, v2, v254, v255\n"
      "v_mfma_f32_16x16x32_bf16 v[128:131], a[156:159], v[80:83], v[128:131]\n"
      "buffer_load_dwordx4 v[12:15], v50, s[32:35], s93 offen offset:1024 sc1\n"
      "global_load_dwordx4 a[236:239], v45, s[8:9] offset:-1024 sc1\n"
      "v_add3_u32 v2, v2, v36, v37\n"
      "v_add3_u32 v2, v2, v38, v39\n"
      "v_mfma_f32_16x16x32_bf16 v[144:147], a[160:163], v[80:83], v[144:147]\n"
      "ds_read_b64_tr_b16 v[116:117], v33 offset:12288\n"
      "v_add3_u32 v2, v2, v60, v61\n"
      "v_mfma_f32_16x16x32_bf16 v[160:163], a[164:167], v[80:83], v[160:163]\n"
      "ds_read_b64_tr_b16 v[118:119], v33 offset:14336\n"
      "v_add3_u32 v2, v2, v62, v63\n"
      "v_mfma_f32_16x16x32_bf16 v[132:135], a[156:159], v[84:87], v[132:135]\n"
      "ds_read_b64_tr_b16 v[120:121], v34 offset:12288\n"
      "v_mfma_f32_16x16x32_bf16 v[148:151], a[160:163], v[84:87], v[148:151]\n"
      "buffer_load_dwordx4 v[16:19], v50, s[32:35], s93 offen offset:2048 sc1\n"
      "ds_read_b64_tr_b16 v[122:123], v34 offset:14336\n"
      "v_add_u32_dpp v2, v2, v2 quad_perm:[1,0,3,2] row_mask:0xf bank_mask:0xf\n"
      "v_mfma_f32_16x16x32_bf16 v[164:167], a[164:167], v[84:87], v[164:167]\n"
      "ds_read_b64_tr_b16 v[124:125], v35 offset:12288\n"
      "v_mfma_f32_16x16x32_bf16 v[136:139], a[156:159], v[88:91], v[136:139]\n"
      "ds_read_b64_tr_b16 v[126:127], v35 offset:14336\n"
      "v_add_u32_dpp v2, v2, v2 quad_perm:[2,3,0,1] row_mask:0xf bank_mask:0xf\n"
      "v_mfma_f32_16x16x32_bf16 v[152:155], a[160:163], v[88:91], v[152:155]\n"
      "v_mfma_f32_16x16x32_bf16 v[168:171], a[164:167], v[88:91], v[168:171]\n"
      "buffer_load_dwordx4 v[20:23], v50, s[32:35], s93 offen offset:3072 sc1\n"
      "v_add_u32_dpp v2, v2, v2 row_half_mirror row_mask:0xf bank_mask:0xf\n"
      "v_cmp_ne_u32 vcc, s71, v2\n"
      "v_mfma_f32_16x16x32_bf16 v[140:143], a[156:159], v[92:95], v[140:143]\n"
      "v_mfma_f32_16x16x32_bf16 v[156:159], a[160:163], v[92:95], v[156:159]\n"
      "s_cbranch_vccnz L_sx5_%=\n"
      "L_bx5_%=:\n"
      "v_mfma_f32_16x16x32_bf16 v[172:175], a[164:167], v[92:95], v[172:175]\n"
      "s_waitcnt vmcnt(25)\n"
      "s_waitcnt lgkmcnt(0)\n"
      "s_barrier\n"
      "v_mfma_f32_16x16x32_bf16 v[128:131], a[168:171], v[96:99], v[128:131]\n"
      "ds_read_b64_tr_b16 v[64:65], v6 offset:16384\n"
      "v_mfma_f32_16x16x32_bf16 v[144:147], a[172:175], v[96:99], v[144:147]\n"
      "ds_read_b64_tr_b16 v[66:67], v6 offset:18432\n"
      "v_mfma_f32_16x16x32_bf16 v[160:163], a[176:179], v[96:99], v[160:163]\n"
      "ds_read_b64_tr_b16 v[68:69], v33 offset:16384\n"
      "s_add_u32 m0, s73, 49152\n"
      "v_add_u32 v43, 114688, v40\n"
      "v_mfma_f32_16x16x32_bf16 v[132:135], a[168:171], v[100:103], v[132:135]\n"
      "ds_read_b64_tr_b16 v[70:71], v33 offset:18432\n"
      "v_mfma_f32_16x16x32_bf16 v[148:151], a[172:175], v[100:103], v[148:151]\n"
      "global_load_lds_dwordx4 v43, s[4:5] offset:0 sc1\n"
      "v_mfma_f32_16x16x32_bf16 v[164:167], a[176:179], v[100:103], v[164:167]\n"
      "ds_read_b64_tr_b16 v[72:73], v34 offset:16384\n"
      "v_mfma_f32_16x16x32_bf16 v[136:139], a[168:171], v[104:107], v[136:139]\n"
      "ds_read_b64_tr_b16 v[74:75], v34 offset:18432\n"
      "v_mfma_f32_16x16x32_bf16 v[152:155], a[172:175], v[104:107], v[152:155]\n"
      "ds_read_b64_tr_b16 v[76:77], v35 offset:16384\n"
      "v_mfma_f32_16x16x32_bf16 v[168:171], a[176:179], v[104:107], v[168:171]\n"
      "global_load_lds_dwordx4 v43, s[4:5] offset:1024 sc1\n"
      "v_mfma_f32_16x16x32_bf16 v[140:143], a[168:171], v[108:111], v[140:143]\n"
      "ds_read_b64_tr_b16 v[78:79], v35 offset:18432\n"
      "v_mfma_f32_16x16x32_bf16 v[156:159], a[172:175], v[108:111], v[156:159]\n"
      "ds_read_b64_tr_b16 v[80:81], v6 offset:20480\n"
      "v_mfma_f32_16x16x32_bf16 v[172:175], a[176:179], v[108:111], v[172:175]\n"
      "ds_read_b64_tr_b16 v[82:83], v6 offset:22528\n"
      "v_mfma_f32_16x16x32_bf16 v[128:131], a[180:183], v[112:115], v[128:131]\n"
      "global_load_lds_dwordx4 v43, s[4:5] offset:2048 sc1\n"
      "s_waitcnt vmcnt(11)\n"
      "v_mfma_f32_16x16x32_bf16 v[144:147], a[184:187], v[112:115], v[144:147]\n"
      "ds_read_b64_tr_b16 v[84:85], v33 offset:20480\n"
      "v_add_f32 v228, v228, v232\n"
      "v_mfma_f32_16x16x32_bf16 v[160:163], a[188:191], v[112:115], v[160:163]\n"
      "ds_read_b64_tr_b16 v[86:87], v33 offset:22528\n"
      "v_add_f32 v229, v229, v233\n"
      "v_mfma_f32_16x16x32_bf16 v[132:135], a[180:183], v[116:119], v[132:135]\n"
      "ds_read_b64_tr_b16 v[88:89], v34 offset:20480\n"
      "v_add_f32 v230, v230, v234\n"
      "v_mfma_f32_16x16x32_bf16 v[148:151], a[184:187], v[116:119], v[148:151]\n"
      "global_load_lds_dwordx4 v43, s[4:5] offset:3072 sc1\n"
      "ds_read_b64_tr_b16 v[90:91], v34 offset:22528\n"
      "v_add_f32 v231, v231, v235\n"
      "v_mfma_f32_16x16x32_bf16 v[164:167], a[188:191], v[116:119], v[164:167]\n"
      "ds_read_b64_tr_b16 v[92:93], v35 offset:20480\n"
      "v_add_f32 v236, v236, v240\n"
      "v_mfma_f32_16x16x32_bf16 v[136:139], a[180:183], v[120:123], v[136:139]\n"
      "ds_read_b64_tr_b16 v[94:95], v35 offset:22528\n"
      "s_add_u32 m0, s73, 0\n"
      "v_add_u32 v43, 131072, v40\n"
      "v_mfma_f32_16x16x32_bf16 v[152:155], a[184:187], v[120:123], v[152:155]\n"
      "v_add_f32 v237, v237, v241\n"
      "v_add_f32 v238, v238, v242\n"
      "v_mfma_f32_16x16x32_bf16 v[168:171], a[188:191], v[120:123], v[168:171]\n"
      "global_load_dwordx4 a[240:243], v44, s[6:7] offset:-4096 sc1\n"
      "v_add_f32 v239, v239, v243\n"
      "v_mfma_f32_16x16x32_bf16 v[140:143], a[180:183], v[124:127], v[140:143]\n"
      "v_add_f32 v224, v224, v228\n"
      "v_add_f32 v225, v225, v229\n"
      "v_mfma_f32_16x16x32_bf16 v[156:159], a[184:187], v[124:127], v[156:159]\n"
      "v_add_f32 v226, v226, v230\n"
      "v_add_f32 v227, v227, v231\n"
      "v_mfma_f32_16x16x32_bf16 v[172:175], a[188:191], v[124:127], v[172:175]\n"
      "s_waitcnt lgkmcnt(0)\n"
      "v_mfma_f32_16x16x32_bf16 v[128:131], a[192:195], v[64:67], v[128:131]\n"
      "global_load_dwordx4 a[244:247], v44, s[6:7] offset:-3072 sc1\n"
      "v_add_f32 v224, v224, v236\n"
      "v_mfma_f32_16x16x32_bf16 v[144:147], a[196:199], v[64:67], v[144:147]\n"
      "ds_read_b64_tr_b16 v[96:97], v6 offset:24576\n"
      "v_add_f32 v225, v225, v237\n"
      "v_mfma_f32_16x16x32_bf16 v[160:163], a[200:203], v[64:67], v[160:163]\n"
      "ds_read_b64_tr_b16 v[98:99], v6 offset:26624\n"
      "s_waitcnt vmcnt(13)\n"
      "v_mfma_f32_16x16x32_bf16 v[132:135], a[192:195], v[68:71], v[132:135]\n"
      "ds_read_b128 v[248:251], v40 offset:32768\n"
      "v_add_f32 v226, v226, v238\n"
      "v_mfma_f32_16x16x32_bf16 v[148:151], a[196:199], v[68:71], v[148:151]\n"
      "global_load_dwordx4 a[248:251], v44, s[6:7] offset:-2048 sc1\n"
      "ds_read_b128 v[252:255], v40 offset:33792\n"
      "ds_read_b64_tr_b16 v[100:101], v33 offset:24576\n"
      "v_add_f32 v227, v227, v239\n"
      "v_mfma_f32_16x16x32_bf16 v[164:167], a[200:203], v[68:71], v[164:167]\n"
      "ds_read_b128 v[36:39], v40 offset:34816\n"
      "ds_read_b64_tr_b16 v[102:103], v33 offset:26624\n"
      "v_mov_b32 v228, v224\n"
      "v_mfma_f32_16x16x32_bf16 v[136:139], a[192:195], v[72:75], v[136:139]\n"
      "ds_read_b128 v[60:63], v40 offset:35840\n"
      "ds_read_b64_tr_b16 v[104:105], v34 offset:24576\n"
      "v_mov_b32 v229, v225\n"
      "v_mfma_f32_16x16x32_bf16 v[152:155], a[196:199], v[72:75], v[152:155]\n"
      "ds_read_b64_tr_b16 v[106:107], v34 offset:26624\n"
      "v_mov_b32 v230, v226\n"
      "v_mfma_f32_16x16x32_bf16 v[168:171], a[200:203], v[72:75], v[168:171]\n"
      "global_load_lds_dwordx4 v43, s[4:5] offset:0 sc1\n"
      "ds_read_b64_tr_b16 v[108:109], v35 offset:24576\n"
      "v_mov_b32 v231, v227\n"
      "v_mfma_f32_16x16x32_bf16 v[140:143], a[192:195], v[76:79], v[140:143]\n"
      "ds_read_b64_tr_b16 v[110:111], v35 offset:26624\n"
      "v_lshl_add_u32 v59, v59, 3, s55\n"
      "v_mfma_f32_16x16x32_bf16 v[156:159], a[196:199], v[76:79], v[156:159]\n"
      "ds_read_b64_tr_b16 v[112:113], v6 offset:28672\n"
      "s_waitcnt lgkmcnt(5)\n"
      "v_add3_u32 v49, v49, v248, v249\n"
      "v_add3_u32 v49, v49, v250, v251\n"
      "v_mfma_f32_16x16x32_bf16 v[172:175], a[200:203], v[76:79], v[172:175]\n"
      "ds_read_b64_tr_b16 v[114:115], v6 offset:30720\n"
      "v_add3_u32 v49, v49, v252, v253\n"
      "v_add3_u32 v49, v49, v254, v255\n"
      "v_mfma_f32_16x16x32_bf16 v[128:131], a[204:207], v[80:83], v[128:131]\n"
      "global_load_dwordx4 a[252:255], v44, s[6:7] offset:-1024 sc1\n"
      "v_add3_u32 v49, v49, v36, v37\n"
      "v_add3_u32 v49, v49, v38, v39\n"
      "v_mfma_f32_16x16x32_bf16 v[144:147], a[208:211], v[80:83], v[144:147]\n"
      "ds_read_b64_tr_b16 v[116:117], v33 offset:28672\n"
      "v_add3_u32 v49, v49, v60, v61\n"
      "v_mfma_f32_16x16x32_bf16 v[160:163], a[212:215], v[80:83], v[160:163]\n"
      "ds_read_b64_tr_b16 v[118:119], v33 offset:30720\n"
      "v_add3_u32 v49, v49, v62, v63\n"
      "v_permlane32_swap_b32 v224, v228\n"
      "v_mfma_f32_16x16x32_bf16 v[132:135], a[204:207], v[84:87], v[132:135]\n"
      "ds_read_b64_tr_b16 v[120:121], v34 offset:28672\n"
      "v_permlane32_swap_b32 v225, v229\n"
      "v_mfma_f32_16x16x32_bf16 v[148:151], a[208:211], v[84:87], v[148:151]\n"
      "global_load_lds_dwordx4 v43, s[4:5] offset:1024 sc1\n"
      "ds_read_b64_tr_b16 v[122:123], v34 offset:30720\n"
      "v_add_u32_dpp v49, v49, v49 quad_perm:[1,0,3,2] row_mask:0xf bank_mask:0xf\n"
      "v_permlane32_swap_b32 v226, v230\n"
      "v_mfma_f32_16x16x32_bf16 v[164:167], a[212:215], v[84:87], v[164:167]\n"
      "ds_read_b64_tr_b16 v[124:125], v35 offset:28672\n"
      "v_permlane32_swap_b32 v227, v231\n"
      "v_mfma_f32_16x16x32_bf16 v[136:139], a[204:207], v[88:91], v[136:139]\n"
      "ds_read_b64_tr_b16 v[126:127], v35 offset:30720\n"
      "v_add_u32_dpp v49, v49, v49 quad_perm:[2,3,0,1] row_mask:0xf bank_mask:0xf\n"
      "v_add_f32 v224, v224, v228\n"
      "v_mfma_f32_16x16x32_bf16 v[152:155], a[208:211], v[88:91], v[152:155]\n"
      "v_add_f32 v225, v225, v229\n"
      "v_add_f32 v226, v226, v230\n"
      "v_mfma_f32_16x16x32_bf16 v[168:171], a[212:215], v[88:91], v[168:171]\n"
      "global_load_dwordx4 a[0:3], v44, s[6:7] offset:0 sc1\n"
      "v_add_u32_dpp v49, v49, v49 row_half_mirror row_mask:0xf bank_mask:0xf\n"
      "v_cmp_ne_u32 vcc, s71, v49\n"
      "v_mfma_f32_16x16x32_bf16 v[140:143], a[204:207], v[92:95], v[140:143]\n"
      "v_add_f32 v227, v227, v231\n"
      "v_mfma_f32_16x16x32_bf16 v[156:159], a[208:211], v[92:95], v[156:159]\n"
      "ds_write_b128 v51, v[224:227]\n"
      "s_cbranch_vccnz L_sx6_%=\n"
      "L_bx6_%=:\n"
      "v_mfma_f32_16x16x32_bf16 v[172:175], a[212:215], v[92:95], v[172:175]\n"
      "s_waitcnt vmcnt(13)\n"
      "s_waitcnt lgkmcnt(1)\n"
      "s_barrier\n"
      "v_mfma_f32_16x16x32_bf16 v[128:131], a[216:219], v[96:99], v[128:131]\n"
      "global_load_lds_dwordx4 v43, s[4:5] offset:2048 sc1\n"
      "v_mfma_f32_16x16x32_bf16 v[144:147], a[220:223], v[96:99], v[144:147]\n"
      "ds_read_b64_tr_b16 v[64:65], v6 offset:32768\n"
      "v_mfma_f32_16x16x32_bf16 v[160:163], a[224:227], v[96:99], v[160:163]\n"
      "ds_read_b64_tr_b16 v[66:67], v6 offset:34816\n"
      "s_waitcnt vmcnt(12)\n"
      "v_add3_u32 v10, v10, v14, v18\n"
      "v_mfma_f32_16x16x32_bf16 v[132:135], a[216:219], v[100:103], v[132:135]\n"
      "ds_read_b64_tr_b16 v[68:69], v34 offset:32768\n"
      "v_add_u32 v10, v10, v22\n"
      "v_cmp_ne_u32 vcc, s71, v10\n"
      "v_mfma_f32_16x16x32_bf16 v[148:151], a[220:223], v[100:103], v[148:151]\n"
      "global_load_dwordx4 a[4:7], v44, s[6:7] offset:1024 sc1\n"
      "v_mfma_f32_16x16x32_bf16 v[164:167], a[224:227], v[100:103], v[164:167]\n"
      "global_load_lds_dwordx4 v43, s[4:5] offset:3072 sc1\n"
      "ds_read_b64_tr_b16 v[70:71], v34 offset:34816\n"
      "s_cbranch_vccnz L_sg_%=\n"
      "L_bg_%=:\n"
      "v_add_f32 v8, v8, v12\n"
      "v_add_f32 v9, v9, v13\n"
      "v_mfma_f32_16x16x32_bf16 v[136:139], a[216:219], v[104:107], v[136:139]\n"
      "ds_read_b64_tr_b16 v[72:73], v33 offset:32768\n"
      "v_add_f32 v16, v16, v20\n"
      "v_add_f32 v17, v17, v21\n"
      "v_mfma_f32_16x16x32_bf16 v[152:155], a[220:223], v[104:107], v[152:155]\n"
      "ds_read_b64_tr_b16 v[74:75], v33 offset:34816\n"
      "v_add_f32 v8, v8, v16\n"
      "v_add_u32 v245, v245, v247\n"
      "v_mfma_f32_16x16x32_bf16 v[168:171], a[224:227], v[104:107], v[168:171]\n"
      "global_load_dwordx4 a[8:11], v44, s[6:7] offset:2048 sc1\n"
      "ds_read_b64_tr_b16 v[76:77], v35 offset:32768\n"
      "v_add_f32 v9, v9, v17\n"
      "v_mov_b32 v10, v8\n"
      "v_cmp_ne_u32 vcc, s54, v245\n"
      "v_mfma_f32_16x16x32_bf16 v[140:143], a[216:219], v[108:111], v[140:143]\n"
      "ds_read_b64_tr_b16 v[78:79], v35 offset:34816\n"
      "v_mov_b32 v11, v9\n"
      "s_nop 0\n"
      "v_mfma_f32_16x16x32_bf16 v[156:159], a[220:223], v[108:111], v[156:159]\n"
      "ds_read_b64_tr_b16 v[80:81], v6 offset:36864\n"
      "v_permlane32_swap_b32 v8, v10\n"
      "v_permlane32_swap_b32 v9, v11\n"
      "s_cbranch_vccnz L_sgb_%=\n"
      "L_bgb_%=:\n"
      "v_add_f32 v8, v8, v10\n"
      "v_mfma_f32_16x16x32_bf16 v[172:175], a[224:227], v[108:111], v[172:175]\n"
      "ds_read_b64_tr_b16 v[82:83], v6 offset:38912\n"
      "v_add_f32_dpp v244, v244, v244 quad_perm:[1,0,3,2] row_mask:0xf bank_mask:0xf\n"
      "v_add_f32_dpp v246, v246, v246 quad_perm:[1,0,3,2] row_mask:0xf bank_mask:0xf\n"
      "v_mfma_f32_16x16x32_bf16 v[128:131], a[228:231], v[112:115], v[128:131]\n"
      "global_load_dwordx4 a[12:15], v44, s[6:7] offset:3072 sc1\n"
      "v_add_f32 v9, v9, v11\n"
      "v_mul_f32 v8, 0x3acccccd, v8\n"
      "v_add_f32_dpp v244, v244, v244 quad_perm:[2,3,0,1] row_mask:0xf bank_mask:0xf\n"
      "v_add_f32_dpp v246, v246, v246 quad_perm:[2,3,0,1] row_mask:0xf bank_mask:0xf\n"
      "v_mfma_f32_16x16x32_bf16 v[144:147], a[232:235], v[112:115], v[144:147]\n"
      "ds_read_b64_tr_b16 v[84:85], v34 offset:36864\n"
      "v_mul_f32 v9, 0x3acccccd, v9\n"
      "v_fma_f32 v9, -v8, v8, v9\n"
      "s_add_u32 m0, s40, 0\n"
      "v_add_u32 v43, 40960, v7\n"
      "v_mfma_f32_16x16x32_bf16 v[160:163], a[236:239], v[112:115], v[160:163]\n"
      "global_load_lds_dwordx4 v43, s[6:7] offset:0 sc1\n"
      "global_load_lds_dwordx4 v43, s[6:7] offset:1024 sc1\n"
      "global_load_lds_dwordx4 v43, s[6:7] offset:2048 sc1\n"
      "global_load_lds_dwordx4 v43, s[6:7] offset:3072 sc1\n"
      "ds_read_b64_tr_b16 v[86:87], v34 offset:38912\n"
      "v_max_f32 v9, 0, v9\n"
      "s_add_u32 m0, s73, 16384\n"
      "v_add_u32 v43, 147456, v40\n"
      "v_cvt_pk_bf16_f32 v244, 0, v244\n"
      "v_cvt_pk_bf16_f32 v245, 0, v246\n"
      "v_mfma_f32_16x16x32_bf16 v[132:135], a[228:231], v[116:119], v[132:135]\n"
      "global_load_lds_dwordx4 v43, s[4:5] offset:0 sc1\n"
      "ds_read_b64_tr_b16 v[88:89], v33 offset:36864\n"
      "ds_write_b64 v59, v[244:245]\n"
      "v_cvt_pk_bf16_f32 v9, 0, v9\n"
      "v_add_f32 v9, 0x3727c5ac, v9\n"
      "v_add_u32 v44, 12288, v7\n"
      "v_mfma_f32_16x16x32_bf16 v[148:151], a[232:235], v[116:119], v[148:151]\n"
      "global_load_dwordx4 a[16:19], v44, s[6:7] offset:-4096 sc1\n"
      "ds_read_b64_tr_b16 v[90:91], v33 offset:38912\n"
      "ds_read_b128 v[224:227], v52 offset:0\n"
      "v_cvt_pk_bf16_f32 v9, 0, v9\n"
      "v_mfma_f32_16x16x32_bf16 v[164:167], a[236:239], v[116:119], v[164:167]\n"
      "ds_read_b64_tr_b16 v[92:93], v35 offset:36864\n"
      "ds_read_b128 v[228:231], v52 offset:128\n"
      "v_sqrt_f32 v9, v9\n"
      "v_sub_f32 v8, 0, v8\n"
      "v_cvt_pk_bf16_f32 v9, 0, v9\n"
      "v_mfma_f32_16x16x32_bf16 v[136:139], a[228:231], v[120:123], v[136:139]\n"
      "ds_read_b128 v[232:235], v52 offset:256\n"
      "v_rcp_f32 v9, v9\n"
      "v_cvt_pk_bf16_f32 v8, 0, v8\n"
      "v_mfma_f32_16x16x32_bf16 v[152:155], a[232:235], v[120:123], v[152:155]\n"
      "global_load_lds_dwordx4 v43, s[4:5] offset:1024 sc1\n"
      "ds_write_b64 v32, v[8:9]\n"
      "ds_read_b64_tr_b16 v[94:95], v35 offset:38912\n"
      "v_mfma_f32_16x16x32_bf16 v[168:171], a[236:239], v[120:123], v[168:171]\n"
      "global_load_dwordx4 a[20:23], v44, s[6:7] offset:-3072 sc1\n"
      "ds_read_b128 v[236:239], v52 offset:64\n"
      "v_mfma_f32_16x16x32_bf16 v[140:143], a[228:231], v[124:127], v[140:143]\n"
      "ds_read_b128 v[240:243], v52 offset:192\n"
      "v_mfma_f32_16x16x32_bf16 v[156:159], a[232:235], v[124:127], v[156:159]\n"
      "ds_read_b128 v[244:247], v52 offset:320\n"
      "v_mfma_f32_16x16x32_bf16 v[172:175], a[236:239], v[124:127], v[172:175]\n"
      "global_load_lds_dwordx4 v43, s[4:5] offset:2048 sc1\n"
      "s_waitcnt lgkmcnt(3)\n"
      "s_barrier\n"
      "v_mfma_f32_16x16x32_bf16 v[176:179], a[48:51], v[64:67], v[176:179]\n"
      "global_load_dwordx4 a[24:27], v44, s[6:7] offset:-2048 sc1\n"
      "ds_read_b64 v[16:17], v41 offset:0\n"
      "v_mfma_f32_16x16x32_bf16 v[184:187], a[52:55], v[64:67], v[184:187]\n"
      "ds_read_b64 v[18:19], v41 offset:128\n"
      "v_mfma_f32_16x16x32_bf16 v[192:195], a[56:59], v[64:67], v[192:195]\n"
      "ds_read_b64 v[20:21], v41 offset:256\n"
      "ds_read_b64_tr_b16 v[96:97], v6 offset:40960\n"
      "s_waitcnt vmcnt(22)\n"
      "v_mfma_f32_16x16x32_bf16 v[180:183], a[48:51], v[68:71], v[180:183]\n"
      "global_load_lds_dwordx4 v43, s[4:5] offset:3072 sc1\n"
      "ds_read_b128 v[248:251], v40 offset:49152\n"
      "ds_read_b64 v[22:23], v41 offset:384\n"
      "ds_read_b64_tr_b16 v[98:99], v6 offset:43008\n"
      "v_mfma_f32_16x16x32_bf16 v[188:191], a[52:55], v[68:71], v[188:191]\n"
      "global_load_dwordx4 a[28:31], v44, s[6:7] offset:-1024 sc1\n"
      "ds_read_b128 v[252:255], v40 offset:50176\n"
      "ds_read_b64_tr_b16 v[100:101], v34 offset:40960\n"
      "v_mfma_f32_16x16x32_bf16 v[196:199], a[56:59], v[68:71], v[196:199]\n"
      "ds_read_b128 v[36:39], v40 offset:51200\n"
      "ds_read_b64_tr_b16 v[102:103], v34 offset:43008\n"
      "v_mfma_f32_16x16x32_bf16 v[200:203], a[48:51], v[72:75], v[200:203]\n"
      "ds_read_b128 v[60:63], v40 offset:52224\n"
      "ds_read_b64_tr_b16 v[104:105], v33 offset:40960\n"
      "s_waitcnt lgkmcnt(7)\n"
      "v_mfma_f32_16x16x32_bf16 v[208:211], a[52:55], v[72:75], v[208:211]\n"
      "ds_read_b64_tr_b16 v[106:107], v33 offset:43008\n"
      "v_fmac_f32 v128, v16, v224\n"
      "v_fmac_f32 v129, v16, v225\n"
      "v_mfma_f32_16x16x32_bf16 v[216:219], a[56:59], v[72:75], v[216:219]\n"
      "global_load_dwordx4 a[32:35], v44, s[6:7] offset:0 sc1\n"
      "ds_read_b64_tr_b16 v[108:109], v35 offset:40960\n"
      "v_fmac_f32 v130, v16, v226\n"
      "v_fmac_f32 v131, v16, v227\n"
      "v_mfma_f32_16x16x32_bf16 v[204:207], a[48:51], v[76:79], v[204:207]\n"
      "ds_read_b64_tr_b16 v[110:111], v35 offset:43008\n"
      "v_fma_f32 v128, v128, v17, v236\n"
      "v_fma_f32 v129, v129, v17, v237\n"
      "v_fma_f32 v130, v130, v17, v238\n"
      "v_mfma_f32_16x16x32_bf16 v[212:215], a[52:55], v[76:79], v[212:215]\n"
      "ds_read_b64_tr_b16 v[112:113], v6 offset:45056\n"
      "s_waitcnt lgkmcnt(5)\n"
      "v_add3_u32 v55, v55, v248, v249\n"
      "v_add3_u32 v55, v55, v250, v251\n"
      "v_fma_f32 v131, v131, v17, v239\n"
      "v_mul_f32 v128, v128, v130\n"
      "v_mfma_f32_16x16x32_bf16 v[220:223], a[56:59], v[76:79], v[220:223]\n"
      "ds_read_b64_tr_b16 v[114:115], v6 offset:47104\n"
      "v_add3_u32 v55, v55, v252, v253\n"
      "v_add3_u32 v55, v55, v254, v255\n"
      "v_mul_f32 v129, v129, v131\n"
      "v_cvt_pk_bf16_f32 v128, v128, v129\n"
      "v_mfma_f32_16x16x32_bf16 v[176:179], a[60:63], v[80:83], v[176:179]\n"
      "global_load_dwordx4 a[36:39], v44, s[6:7] offset:1024 sc1\n"
      "ds_write_b32 v46, v128 offset:0\n"
      "v_add3_u32 v55, v55, v36, v37\n"
      "v_add3_u32 v55, v55, v38, v39\n"
      "v_fmac_f32 v144, v16, v228\n"
      "v_fmac_f32 v145, v16, v229\n"
      "v_mfma_f32_16x16x32_bf16 v[184:187], a[64:67], v[80:83], v[184:187]\n"
      "ds_read_b64_tr_b16 v[116:117], v34 offset:45056\n"
      "v_add3_u32 v55, v55, v60, v61\n"
      "v_fmac_f32 v146, v16, v230\n"
      "v_fmac_f32 v147, v16, v231\n"
      "v_mfma_f32_16x16x32_bf16 v[192:195], a[68:71], v[80:83], v[192:195]\n"
      "ds_read_b64_tr_b16 v[118:119], v34 offset:47104\n"
      "v_add3_u32 v55, v55, v62, v63\n"
      "v_fma_f32 v144, v144, v17, v240\n"
      "v_fma_f32 v145, v145, v17, v241\n"
      "v_mfma_f32_16x16x32_bf16 v[180:183], a[60:63], v[84:87], v[180:183]\n"
      "ds_read_b64_tr_b16 v[120:121], v33 offset:45056\n"
      "v_fma_f32 v146, v146, v17, v242\n"
      "v_fma_f32 v147, v147, v17, v243\n"
      "v_mul_f32 v144, v144, v146\n"
      "v_mfma_f32_16x16x32_bf16 v[188:191], a[64:67], v[84:87], v[188:191]\n"
      "global_load_dwordx4 a[40:43], v44, s[6:7] offset:2048 sc1\n"
      "ds_read_b64_tr_b16 v[122:123], v33 offset:47104\n"
      "v_add_u32_dpp v55, v55, v55 quad_perm:[1,0,3,2] row_mask:0xf bank_mask:0xf\n"
      "v_mul_f32 v145, v145, v147\n"
      "v_cvt_pk_bf16_f32 v144, v144, v145\n"
      "v_mfma_f32_16x16x32_bf16 v[196:199], a[68:71], v[84:87], v[196:199]\n"
      "ds_write_b32 v46, v144 offset:4096\n"
      "ds_read_b64_tr_b16 v[124:125], v35 offset:45056\n"
      "v_fmac_f32 v160, v16, v232\n"
      "v_mfma_f32_16x16x32_bf16 v[200:203], a[60:63], v[88:91], v[200:203]\n"
      "ds_read_b64_tr_b16 v[126:127], v35 offset:47104\n"
      "v_add_u32_dpp v55, v55, v55 quad_perm:[2,3,0,1] row_mask:0xf bank_mask:0xf\n"
      "v_fmac_f32 v161, v16, v233\n"
      "v_fmac_f32 v162, v16, v234\n"
      "v_fmac_f32 v163, v16, v235\n"
      "v_mfma_f32_16x16x32_bf16 v[208:211], a[64:67], v[88:91], v[208:211]\n"
      "v_fma_f32 v160, v160, v17, v244\n"
      "v_fma_f32 v161, v161, v17, v245\n"
      "v_mfma_f32_16x16x32_bf16 v[216:219], a[68:71], v[88:91], v[216:219]\n"
      "global_load_dwordx4 a[44:47], v44, s[6:7] offset:3072 sc1\n"
      "v_add_u32_dpp v55, v55, v55 row_half_mirror row_mask:0xf bank_mask:0xf\n"
      "v_cmp_ne_u32 vcc, s71, v55\n"
      "v_fma_f32 v162, v162, v17, v246\n"
      "v_fma_f32 v163, v163, v17, v247\n"
      "v_mul_f32 v160, v160, v162\n"
      "v_mfma_f32_16x16x32_bf16 v[204:207], a[60:63], v[92:95], v[204:207]\n"
      "v_mul_f32 v161, v161, v163\n"
      "v_cvt_pk_bf16_f32 v160, v160, v161\n"
      "v_mfma_f32_16x16x32_bf16 v[212:215], a[64:67], v[92:95], v[212:215]\n"
      "ds_write_b32 v46, v160 offset:8192\n"
      "s_cbranch_vccnz L_sx7_%=\n"
      "L_bx7_%=:\n"
      "v_fmac_f32 v132, v18, v224\n"
      "v_mfma_f32_16x16x32_bf16 v[220:223], a[68:71], v[92:95], v[220:223]\n"
      "v_fmac_f32 v133, v18, v225\n"
      "v_fmac_f32 v134, v18, v226\n"
      "v_fmac_f32 v135, v18, v227\n"
      "v_add_u32 v44, 20480, v7\n"
      "s_waitcnt lgkmcnt(1)\n"
      "s_barrier\n"
      "v_mfma_f32_16x16x32_bf16 v[176:179], a[72:75], v[96:99], v[176:179]\n"
      "global_load_dwordx4 a[48:51], v44, s[6:7] offset:-4096 sc1\n"
      "v_fma_f32 v132, v132, v19, v236\n"
      "v_fma_f32 v133, v133, v19, v237\n"
      "v_mfma_f32_16x16x32_bf16 v[184:187], a[76:79], v[96:99], v[184:187]\n"
      "ds_read_b64_tr_b16 v[64:65], v6 offset:49152\n"
      "v_fma_f32 v134, v134, v19, v238\n"
      "v_fma_f32 v135, v135, v19, v239\n"
      "v_mfma_f32_16x16x32_bf16 v[192:195], a[80:83], v[96:99], v[192:195]\n"
      "ds_read_b64_tr_b16 v[66:67], v6 offset:51200\n"
      "v_mul_f32 v132, v132, v134\n"
      "v_mul_f32 v133, v133, v135\n"
      "v_cvt_pk_bf16_f32 v132, v132, v133\n"
      "v_mfma_f32_16x16x32_bf16 v[180:183], a[72:75], v[100:103], v[180:183]\n"
      "ds_write_b32 v46, v132 offset:1024\n"
      "ds_read_b64_tr_b16 v[68:69], v34 offset:49152\n"
      "v_fmac_f32 v148, v18, v228\n"
      "v_mfma_f32_16x16x32_bf16 v[188:191], a[76:79], v[100:103], v[188:191]\n"
      "global_load_dwordx4 a[52:55], v44, s[6:7] offset:-3072 sc1\n"
      "s_waitcnt vmcnt(20)\n"
      "v_fmac_f32 v149, v18, v229\n"
      "v_fmac_f32 v150, v18, v230\n"
      "v_mfma_f32_16x16x32_bf16 v[196:199], a[80:83], v[100:103], v[196:199]\n"
      "ds_read_b128 v[248:251], v40 offset:0\n"
      "ds_read_b64_tr_b16 v[70:71], v34 offset:51200\n"
      "v_fmac_f32 v151, v18, v231\n"
      "v_fma_f32 v148, v148, v19, v240\n"
      "v_fma_f32 v149, v149, v19, v241\n"
      "v_mfma_f32_16x16x32_bf16 v[200:203], a[72:75], v[104:107], v[200:203]\n"
      "ds_read_b128 v[252:255], v40 offset:1024\n"
      "ds_read_b64_tr_b16 v[72:73], v33 offset:49152\n"
      "v_fma_f32 v150, v150, v19, v242\n"
      "v_fma_f32 v151, v151, v19, v243\n"
      "v_mfma_f32_16x16x32_bf16 v[208:211], a[76:79], v[104:107], v[208:211]\n"
      "ds_read_b128 v[36:39], v40 offset:2048\n"
      "ds_read_b64_tr_b16 v[74:75], v33 offset:51200\n"
      "v_mul_f32 v148, v148, v150\n"
      "v_mul_f32 v149, v149, v151\n"
      "v_cvt_pk_bf16_f32 v148, v148, v149\n"
      "v_mfma_f32_16x16x32_bf16 v[216:219], a[80:83], v[104:107], v[216:219]\n"
      "global_load_dwordx4 a[56:59], v44, s[6:7] offset:-2048 sc1\n"
      "ds_read_b128 v[60:63], v40 offset:3072\n"
      "ds_write_b32 v46, v148 offset:5120\n"
      "ds_read_b64_tr_b16 v[76:77], v35 offset:49152\n"
      "v_fmac_f32 v164, v18, v232\n"
      "v_mfma_f32_16x16x32_bf16 v[204:207], a[72:75], v[108:111], v[204:207]\n"
      "ds_read_b64_tr_b16 v[78:79], v35 offset:51200\n"
      "v_fmac_f32 v165, v18, v233\n"
      "v_fmac_f32 v166, v18, v234\n"
      "v_mfma_f32_16x16x32_bf16 v[212:215], a[76:79], v[108:111], v[212:215]\n"
      "ds_read_b64_tr_b16 v[80:81], v6 offset:53248\n"
      "v_fmac_f32 v167, v18, v235\n"
      "v_fma_f32 v164, v164, v19, v244\n"
      "v_fma_f32 v165, v165, v19, v245\n"
      "v_mfma_f32_16x16x32_bf16 v[220:223], a[80:83], v[108:111], v[220:223]\n"
      "ds_read_b64_tr_b16 v[82:83], v6 offset:55296\n"
      "v_fma_f32 v166, v166, v19, v246\n"
      "v_fma_f32 v167, v167, v19, v247\n"
      "v_mfma_f32_16x16x32_bf16 v[176:179], a[84:87], v[112:115], v[176:179]\n"
      "global_load_dwordx4 a[60:63], v44, s[6:7] offset:-1024 sc1\n"
      "s_waitcnt lgkmcnt(5)\n"
      "v_add3_u32 v57, v57, v248, v249\n"
      "L_pg4_%=:\n"
      "s_cbranch_execz L_pgr_%=\n"
      "v_add3_u32 v57, v57, v250, v251\n"
      "v_mul_f32 v164, v164, v166\n"
      "v_mul_f32 v165, v165, v167\n"
      "v_cvt_pk_bf16_f32 v164, v164, v165\n"
      "v_mfma_f32_16x16x32_bf16 v[184:187], a[88:91], v[112:115], v[184:187]\n"
      "ds_write_b32 v46, v164 offset:9216\n"
      "ds_read_b64_tr_b16 v[84:85], v34 offset:53248\n"
      "v_add3_u32 v57, v57, v252, v253\n"
      "v_add3_u32 v57, v57, v254, v255\n"
      "v_fmac_f32 v136, v20, v224\n"
      "v_mfma_f32_16x16x32_bf16 v[192:195], a[92:95], v[112:115], v[192:195]\n"
      "ds_read_b64_tr_b16 v[86:87], v34 offset:55296\n"
      "v_add3_u32 v57, v57, v36, v37\n"
      "v_add3_u32 v57, v57, v38, v39\n"
      "v_fmac_f32 v137, v20, v225\n"
      "v_fmac_f32 v138, v20, v226\n"
      "v_mfma_f32_16x16x32_bf16 v[180:183], a[84:87], v[116:119], v[180:183]\n"
      "ds_read_b64_tr_b16 v[88:89], v33 offset:53248\n"
      "v_add3_u32 v57, v57, v60, v61\n"
      "v_fmac_f32 v139, v20, v227\n"
      "v_fma_f32 v136, v136, v21, v236\n"
      "v_mfma_f32_16x16x32_bf16 v[188:191], a[88:91], v[116:119], v[188:191]\n"
      "global_load_dwordx4 a[64:67], v44, s[6:7] offset:0 sc1\n"
      "ds_read_b64_tr_b16 v[90:91], v33 offset:55296\n"
      "v_add3_u32 v57, v57, v62, v63\n"
      "v_fma_f32 v137, v137, v21, v237\n"
      "v_fma_f32 v138, v138, v21, v238\n"
      "v_fma_f32 v139, v139, v21, v239\n"
      "v_mfma_f32_16x16x32_bf16 v[196:199], a[92:95], v[116:119], v[196:199]\n"
      "ds_read_b64_tr_b16 v[92:93], v35 offset:53248\n"
      "v_mul_f32 v136, v136, v138\n"
      "v_mul_f32 v137, v137, v139\n"
      "v_cvt_pk_bf16_f32 v136, v136, v137\n"
      "v_mfma_f32_16x16x32_bf16 v[200:203], a[84:87], v[120:123], v[200:203]\n"
      "ds_write_b32 v46, v136 offset:2048\n"
      "ds_read_b64_tr_b16 v[94:95], v35 offset:55296\n"
      "v_add_u32_dpp v57, v57, v57 quad_perm:[1,0,3,2] row_mask:0xf bank_mask:0xf\n"
      "v_mfma_f32_16x16x32_bf16 v[208:211], a[88:91], v[120:123], v[208:211]\n"
      "v_fmac_f32 v152, v20, v228\n"
      "v_fmac_f32 v153, v20, v229\n"
      "v_fmac_f32 v154, v20, v230\n"
      "v_mfma_f32_16x16x32_bf16 v[216:219], a[92:95], v[120:123], v[216:219]\n"
      "global_load_dwordx4 a[68:71], v44, s[6:7] offset:1024 sc1\n"
      "v_add_u32_dpp v57, v57, v57 quad_perm:[2,3,0,1] row_mask:0xf bank_mask:0xf\n"
      "v_fmac_f32 v155, v20, v231\n"
      "v_fma_f32 v152, v152, v21, v240\n"
      "v_mfma_f32_16x16x32_bf16 v[204:207], a[84:87], v[124:127], v[204:207]\n"
      "v_fma_f32 v153, v153, v21, v241\n"
      "v_fma_f32 v154, v154, v21, v242\n"
      "v_fma_f32 v155, v155, v21, v243\n"
      "v_mfma_f32_16x16x32_bf16 v[212:215], a[88:91], v[124:127], v[212:215]\n"
      "v_add_u32_dpp v57, v57, v57 row_half_mirror row_mask:0xf bank_mask:0xf\n"
      "v_cmp_ne_u32 vcc, s71, v57\n"
      "v_mul_f32 v152, v152, v154\n"
      "v_mul_f32 v153, v153, v155\n"
      "v_cvt_pk_bf16_f32 v152, v152, v153\n"
      "v_mfma_f32_16x16x32_bf16 v[220:223], a[92:95], v[124:127], v[220:223]\n"
      "ds_write_b32 v46, v152 offset:6144\n"
      "v_fmac_f32 v168, v20, v232\n"
      "s_waitcnt lgkmcnt(1)\n"
      "v_mfma_f32_16x16x32_bf16 v[176:179], a[96:99], v[64:67], v[176:179]\n"
      "global_load_dwordx4 a[72:75], v44, s[6:7] offset:2048 sc1\n"
      "s_cbranch_vccnz L_sx8_%=\n"
      "L_bx8_%=:\n"
      "v_fmac_f32 v169, v20, v233\n"
      "v_fmac_f32 v170, v20, v234\n"
      "s_add_u32 m0, s40, 4096\n"
      "v_add_u32 v43, 45056, v7\n"
      "v_mfma_f32_16x16x32_bf16 v[184:187], a[100:103], v[64:67], v[184:187]\n"
      "global_load_lds_dwordx4 v43, s[6:7] offset:0 sc1\n"
      "v_fmac_f32 v171, v20, v235\n"
      "v_fma_f32 v168, v168, v21, v244\n"
      "v_mfma_f32_16x16x32_bf16 v[192:195], a[104:107], v[64:67], v[192:195]\n"
      "ds_read_b64_tr_b16 v[96:97], v6 offset:57344\n"
      "s_waitcnt vmcnt(13)\n"
      "v_fma_f32 v169, v169, v21, v245\n"
      "v_fma_f32 v170, v170, v21, v246\n"
      "v_mfma_f32_16x16x32_bf16 v[180:183], a[96:99], v[68:71], v[180:183]\n"
      "ds_read_b128 v[248:251], v40 offset:16384\n"
      "ds_read_b64_tr_b16 v[98:99], v6 offset:59392\n"
      "v_fma_f32 v171, v171, v21, v247\n"
      "v_mul_f32 v168, v168, v170\n"
      "v_mul_f32 v169, v169, v171\n"
      "v_cvt_pk_bf16_f32 v168, v168, v169\n"
      "v_mfma_f32_16x16x32_bf16 v[188:191], a[100:103], v[68:71], v[188:191]\n"
      "global_load_dwordx4 a[76:79], v44, s[6:7] offset:3072 sc1\n"
      "ds_read_b128 v[252:255], v40 offset:17408\n"
      "ds_write_b32 v46, v168 offset:10240\n"
      "ds_read_b64_tr_b16 v[100:101], v34 offset:57344\n"
      "v_fmac_f32 v140, v22, v224\n"
      "v_mfma_f32_16x16x32_bf16 v[196:199], a[104:107], v[68:71], v[196:199]\n"
      "global_load_lds_dwordx4 v43, s[6:7] offset:1024 sc1\n"
      "ds_read_b128 v[36:39], v40 offset:18432\n"
      "ds_read_b64_tr_b16 v[102:103], v34 offset:59392\n"
      "v_fmac_f32 v141, v22, v225\n"
      "v_mfma_f32_16x16x32_bf16 v[200:203], a[96:99], v[72:75], v[200:203]\n"
      "ds_read_b128 v[60:63], v40 offset:19456\n"
      "ds_read_b64_tr_b16 v[104:105], v33 offset:57344\n"
      "v_fmac_f32 v142, v22, v226\n"
      "v_fmac_f32 v143, v22, v227\n"
      "v_fma_f32 v140, v140, v23, v236\n"
      "v_mfma_f32_16x16x32_bf16 v[208:211], a[100:103], v[72:75], v[208:211]\n"
      "ds_read_b64_tr_b16 v[106:107], v33 offset:59392\n"
      "v_fma_f32 v141, v141, v23, v237\n"
      "v_fma_f32 v142, v142, v23, v238\n"
      "v_add_u32 v44, 28672, v7\n"
      "v_mfma_f32_16x16x32_bf16 v[216:219], a[104:107], v[72:75], v[216:219]\n"
      "global_load_dwordx4 a[80:83], v44, s[6:7] offset:-4096 sc1\n"
      "ds_read_b64_tr_b16 v[108:109], v35 offset:57344\n"
      "v_fma_f32 v143, v143, v23, v239\n"
      "v_mul_f32 v140, v140, v142\n"
      "v_mul_f32 v141, v141, v143\n"
      "v_cvt_pk_bf16_f32 v140, v140, v141\n"
      "v_mfma_f32_16x16x32_bf16 v[204:207], a[96:99], v[76:79], v[204:207]\n"
      "global_load_lds_dwordx4 v43, s[6:7] offset:2048 sc1\n"
      "ds_write_b32 v46, v140 offset:3072\n"
      "ds_read_b64_tr_b16 v[110:111], v35 offset:59392\n"
      "v_fmac_f32 v156, v22, v228\n"
      "v_mfma_f32_16x16x32_bf16 v[212:215], a[100:103], v[76:79], v[212:215]\n"
      "ds_read_b64_tr_b16 v[112:113], v6 offset:61440\n"
      "s_waitcnt lgkmcnt(6)\n"
      "v_add3_u32 v3, v3, v248, v249\n"
      "v_add3_u32 v3, v3, v250, v251\n"
      "v_fmac_f32 v157, v22, v229\n"
      "v_mfma_f32_16x16x32_bf16 v[220:223], a[104:107], v[76:79], v[220:223]\n"
      "ds_read_b64_tr_b16 v[114:115], v6 offset:63488\n"
      "v_add3_u32 v3, v3, v252, v253\n"
      "v_add3_u32 v3, v3, v254, v255\n"
      "v_fmac_f32 v158, v22, v230\n"
      "v_fmac_f32 v159, v22, v231\n"
      "v_fma_f32 v156, v156, v23, v240\n"
      "v_mfma_f32_16x16x32_bf16 v[176:179], a[108:111], v[80:83], v[176:179]\n"
      "global_load_dwordx4 a[84:87], v44, s[6:7] offset:-3072 sc1\n"
      "v_add3_u32 v3, v3, v36, v37\n"
      "v_add3_u32 v3, v3, v38, v39\n"
      "v_fma_f32 v157, v157, v23, v241\n"
      "v_fma_f32 v158, v158, v23, v242\n"
      "v_mfma_f32_16x16x32_bf16 v[184:187], a[112:115], v[80:83], v[184:187]\n"
      "global_load_lds_dwordx4 v43, s[6:7] offset:3072 sc1\n"
      "ds_read_b64_tr_b16 v[116:117], v34 offset:61440\n"
      "v_add3_u32 v3, v3, v60, v61\n"
      "v_fma_f32 v159, v159, v23, v243\n"
      "v_mul_f32 v156, v156, v158\n"
      "v_mul_f32 v157, v157, v159\n"
      "v_cvt_pk_bf16_f32 v156, v156, v157\n"
      "v_mfma_f32_16x16x32_bf16 v[192:195], a[116:119], v[80:83], v[192:195]\n"
      "ds_write_b32 v46, v156 offset:7168\n"
      "ds_read_b64_tr_b16 v[118:119], v34 offset:63488\n"
      "v_add3_u32 v3, v3, v62, v63\n"
      "v_mfma_f32_16x16x32_bf16 v[180:183], a[108:111], v[84:87], v[180:183]\n"
      "ds_read_b64_tr_b16 v[120:121], v33 offset:61440\n"
      "v_fmac_f32 v172, v22, v232\n"
      "v_fmac_f32 v173, v22, v233\n"
      "v_mfma_f32_16x16x32_bf16 v[188:191], a[112:115], v[84:87], v[188:191]\n"
      "global_load_dwordx4 a[88:91], v44, s[6:7] offset:-2048 sc1\n"
      "ds_read_b64_tr_b16 v[122:123], v33 offset:63488\n"
      "v_add_u32_dpp v3, v3, v3 quad_perm:[1,0,3,2] row_mask:0xf bank_mask:0xf\n"
      "v_fmac_f32 v174, v22, v234\n"
      "v_fmac_f32 v175, v22, v235\n"
      "s_add_u32 m0, s40, 8192\n"
      "v_add_u32 v43, 49152, v7\n"
      "v_mfma_f32_16x16x32_bf16 v[196:199], a[116:119], v[84:87], v[196:199]\n"
      "global_load_lds_dwordx4 v43, s[6:7] offset:0 sc1\n"
      "ds_read_b64_tr_b16 v[124:125], v35 offset:61440\n"
      "v_fma_f32 v172, v172, v23, v244\n"
      "v_fma_f32 v173, v173, v23, v245\n"
      "v_fma_f32 v174, v174, v23, v246\n"
      "v_mfma_f32_16x16x32_bf16 v[200:203], a[108:111], v[88:91], v[200:203]\n"
      "ds_read_b64_tr_b16 v[126:127], v35 offset:63488\n"
      "v_add_u32_dpp v3, v3, v3 quad_perm:[2,3,0,1] row_mask:0xf bank_mask:0xf\n"
      "v_fma_f32 v175, v175, v23, v247\n"
      "v_mul_f32 v172, v172, v174\n"
      "v_mfma_f32_16x16x32_bf16 v[208:211], a[112:115], v[88:91], v[208:211]\n"
      "v_mul_f32 v173, v173, v175\n"
      "v_cvt_pk_bf16_f32 v172, v172, v173\n"
      "v_mfma_f32_16x16x32_bf16 v[216:219], a[116:119], v[88:91], v[216:219]\n"
      "global_load_dwordx4 a[92:95], v44, s[6:7] offset:-1024 sc1\n"
      "ds_write_b32 v46, v172 offset:11264\n"
      "v_add_u32_dpp v3, v3, v3 row_half_mirror row_mask:0xf bank_mask:0xf\n"
      "v_cmp_ne_u32 vcc, s71, v3\n"
      "v_mfma_f32_16x16x32_bf16 v[204:207], a[108:111], v[92:95], v[204:207]\n"
      "v_mfma_f32_16x16x32_bf16 v[212:215], a[112:115], v[92:95], v[212:215]\n"
      "s_cbranch_vccnz L_sx9_%=\n"
      "L_bx9_%=:\n"
      "v_mfma_f32_16x16x32_bf16 v[220:223], a[116:119], v[92:95], v[220:223]\n"
      "s_waitcnt lgkmcnt(0)\n"
      "s_barrier\n"
      "v_mfma_f32_16x16x32_bf16 v[176:179], a[120:123], v[96:99], v[176:179]\n"
      "global_load_dwordx4 a[96:99], v44, s[6:7] offset:0 sc1\n"
      "ds_read_b128 v[160:163], v47 offset:0\n"
      "v_add_u32 v57, s40, v7\n"
      "v_mfma_f32_16x16x32_bf16 v[184:187], a[124:127], v[96:99], v[184:187]\n"
      "ds_read_b128 v[164:167], v47 offset:4096\n"
      "v_mfma_f32_16x16x32_bf16 v[192:195], a[128:131], v[96:99], v[192:195]\n"
      "ds_read_b128 v[168:171], v47 offset:8192\n"
      "ds_read_b64_tr_b16 v[64:65], v6 offset:0\n"
      "v_mfma_f32_16x16x32_bf16 v[180:183], a[120:123], v[100:103], v[180:183]\n"
      "ds_read_b64_tr_b16 v[66:67], v6 offset:2048\n"
      "v_mfma_f32_16x16x32_bf16 v[188:191], a[124:127], v[100:103], v[188:191]\n"
      "global_load_dwordx4 a[100:103], v44, s[6:7] offset:1024 sc1\n"
      "ds_read_b64_tr_b16 v[68:69], v34 offset:0\n"
      "v_mfma_f32_16x16x32_bf16 v[196:199], a[128:131], v[100:103], v[196:199]\n"
      "ds_read_b64_tr_b16 v[70:71], v34 offset:2048\n"
      "s_waitcnt lgkmcnt(4)\n"
      "v_mfma_f32_16x16x32_bf16 v[200:203], a[120:123], v[104:107], v[200:203]\n"
      "ds_read_b64_tr_b16 v[72:73], v6 offset:4096\n"
      "v_mfma_f32_16x16x32_bf16 v[208:211], a[124:127], v[104:107], v[208:211]\n"
      "buffer_store_dwordx4 v[160:163], v7, s[32:35], s61 offen offset:0 sc1\n"
      "ds_read_b64_tr_b16 v[74:75], v6 offset:6144\n"
      "v_add3_u32 v58, v160, v161, s30\n"
      "v_mfma_f32_16x16x32_bf16 v[216:219], a[128:131], v[104:107], v[216:219]\n"
      "global_load_dwordx4 a[104:107], v44, s[6:7] offset:2048 sc1\n"
      "ds_read_b64_tr_b16 v[76:77], v34 offset:4096\n"
      "v_add3_u32 v58, v58, v162, v163\n"
      "v_add3_u32 v58, v58, v164, v165\n"
      "s_add_u32 m0, s41, 0\n"
      "v_add_u32 v43, 50176, v7\n"
      "v_mfma_f32_16x16x32_bf16 v[204:207], a[120:123], v[108:111], v[204:207]\n"
      "buffer_store_dwordx4 v[164:167], v7, s[32:35], s61 offen offset:1024 sc1\n"
      "global_load_lds_dwordx4 v43, s[6:7] offset:0 sc1\n"
      "ds_read_b64_tr_b16 v[78:79], v34 offset:6144\n"
      "v_add3_u32 v58, v58, v166, v167\n"
      "v_mfma_f32_16x16x32_bf16 v[212:215], a[124:127], v[108:111], v[212:215]\n"
      "ds_read_b64_tr_b16 v[80:81], v6 offset:8192\n"
      "v_add3_u32 v58, v58, v168, v169\n"
      "v_add3_u32 v58, v58, v170, v171\n"
      "v_mfma_f32_16x16x32_bf16 v[220:223], a[128:131], v[108:111], v[220:223]\n"
      "buffer_store_dwordx4 v[168:171], v7, s[32:35], s61 offen offset:2048 sc1\n"
      "v_mfma_f32_16x16x32_bf16 v[176:179], a[132:135], v[112:115], v[176:179]\n"
      "global_load_dwordx4 a[108:111], v44, s[6:7] offset:3072 sc1\n"
      "ds_read_b64_tr_b16 v[82:83], v6 offset:10240\n"
      "v_mfma_f32_16x16x32_bf16 v[184:187], a[136:139], v[112:115], v[184:187]\n"
      "buffer_store_dword v58, v42, s[32:35], s62 offen sc1\n"
      "global_load_lds_dwordx4 v43, s[6:7] offset:1024 sc1\n"
      "ds_read_b64_tr_b16 v[84:85], v34 offset:8192\n"
      "v_mfma_f32_16x16x32_bf16 v[192:195], a[140:143], v[112:115], v[192:195]\n"
      "ds_read_b64_tr_b16 v[86:87], v34 offset:10240\n"
      "v_mfma_f32_16x16x32_bf16 v[180:183], a[132:135], v[116:119], v[180:183]\n"
      "ds_read_b64_tr_b16 v[88:89], v6 offset:12288\n"
      "v_add_u32 v44, 36864, v7\n"
      "v_mfma_f32_16x16x32_bf16 v[188:191], a[136:139], v[116:119], v[188:191]\n"
      "global_load_dwordx4 a[112:115], v44, s[6:7] offset:-4096 sc1\n"
      "ds_read_b64_tr_b16 v[90:91], v6 offset:14336\n"
      "v_mfma_f32_16x16x32_bf16 v[196:199], a[140:143], v[116:119], v[196:199]\n"
      "global_load_lds_dwordx4 v43, s[6:7] offset:2048 sc1\n"
      "ds_read_b64_tr_b16 v[92:93], v34 offset:12288\n"
      "v_mfma_f32_16x16x32_bf16 v[200:203], a[132:135], v[120:123], v[200:203]\n"
      "ds_read_b64_tr_b16 v[94:95], v34 offset:14336\n"
      "v_mfma_f32_16x16x32_bf16 v[208:211], a[136:139], v[120:123], v[208:211]\n"
      "ds_read_b64 v[24:25], v41 offset:512\n"
      "v_mfma_f32_16x16x32_bf16 v[216:219], a[140:143], v[120:123], v[216:219]\n"
      "global_load_dwordx4 a[116:119], v44, s[6:7] offset:-3072 sc1\n"
      "v_mfma_f32_16x16x32_bf16 v[204:207], a[132:135], v[124:127], v[204:207]\n"
      "ds_read_b64 v[26:27], v41 offset:768\n"
      "v_mfma_f32_16x16x32_bf16 v[212:215], a[136:139], v[124:127], v[212:215]\n"
      "ds_read_b64 v[28:29], v41 offset:640\n"
      "v_mfma_f32_16x16x32_bf16 v[220:223], a[140:143], v[124:127], v[220:223]\n"
      "ds_read_b64 v[30:31], v41 offset:896\n"
      "s_waitcnt lgkmcnt(4)\n"
      "v_mfma_f32_16x16x32_bf16 v[176:179], a[144:147], v[64:67], v[176:179]\n"
      "global_load_dwordx4 a[120:123], v44, s[6:7] offset:-2048 sc1\n"
      "v_mfma_f32_16x16x32_bf16 v[184:187], a[148:151], v[64:67], v[184:187]\n"
      "ds_read_b64_tr_b16 v[96:97], v6 offset:16384\n"
      "v_mfma_f32_16x16x32_bf16 v[192:195], a[152:155], v[64:67], v[192:195]\n"
      "ds_read_b64_tr_b16 v[98:99], v6 offset:18432\n"
      "v_mfma_f32_16x16x32_bf16 v[180:183], a[144:147], v[68:71], v[180:183]\n"
      "ds_read_b64_tr_b16 v[100:101], v34 offset:16384\n"
      "v_mfma_f32_16x16x32_bf16 v[188:191], a[148:151], v[68:71], v[188:191]\n"
      "global_load_dwordx4 a[124:127], v44, s[6:7] offset:-1024 sc1\n"
      "v_mfma_f32_16x16x32_bf16 v[196:199], a[152:155], v[68:71], v[196:199]\n"
      "ds_read_b64_tr_b16 v[102:103], v34 offset:18432\n"
      "v_mfma_f32_16x16x32_bf16 v[176:179], a[156:159], v[72:75], v[176:179]\n"
      "ds_read_b64_tr_b16 v[104:105], v6 offset:20480\n"
      "v_mfma_f32_16x16x32_bf16 v[184:187], a[160:163], v[72:75], v[184:187]\n"
      "ds_read_b64_tr_b16 v[106:107], v6 offset:22528\n"
      "v_mfma_f32_16x16x32_bf16 v[192:195], a[164:167], v[72:75], v[192:195]\n"
      "global_load_dwordx4 a[128:131], v44, s[6:7] offset:0 sc1\n"
      "ds_read_b64_tr_b16 v[108:109], v34 offset:20480\n"
      "v_mfma_f32_16x16x32_bf16 v[180:183], a[156:159], v[76:79], v[180:183]\n"
      "ds_read_b64_tr_b16 v[110:111], v34 offset:22528\n"
      "v_mfma_f32_16x16x32_bf16 v[188:191], a[160:163], v[76:79], v[188:191]\n"
      "ds_read_b64_tr_b16 v[112:113], v6 offset:24576\n"
      "v_mfma_f32_16x16x32_bf16 v[196:199], a[164:167], v[76:79], v[196:199]\n"
      "ds_read_b64_tr_b16 v[114:115], v6 offset:26624\n"
      "v_mfma_f32_16x16x32_bf16 v[176:179], a[168:171], v[80:83], v[176:179]\n"
      "global_load_dwordx4 a[132:135], v44, s[6:7] offset:1024 sc1\n"
      "v_mfma_f32_16x16x32_bf16 v[184:187], a[172:175], v[80:83], v[184:187]\n"
      "ds_read_b64_tr_b16 v[116:117], v34 offset:24576\n"
      "v_mfma_f32_16x16x32_bf16 v[192:195], a[176:179], v[80:83], v[192:195]\n"
      "ds_read_b64_tr_b16 v[118:119], v34 offset:26624\n"
      "v_mfma_f32_16x16x32_bf16 v[180:183], a[168:171], v[84:87], v[180:183]\n"
      "ds_read_b64_tr_b16 v[120:121], v6 offset:28672\n"
      "v_mfma_f32_16x16x32_bf16 v[188:191], a[172:175], v[84:87], v[188:191]\n"
      "global_load_dwordx4 a[136:139], v44, s[6:7] offset:2048 sc1\n"
      "ds_read_b64_tr_b16 v[122:123], v6 offset:30720\n"
      "v_mfma_f32_16x16x32_bf16 v[196:199], a[176:179], v[84:87], v[196:199]\n"
      "ds_read_b64_tr_b16 v[124:125], v34 offset:28672\n"
      "v_mfma_f32_16x16x32_bf16 v[176:179], a[180:183], v[88:91], v[176:179]\n"
      "ds_read_b64_tr_b16 v[126:127], v34 offset:30720\n"
      "v_mfma_f32_16x16x32_bf16 v[184:187], a[184:187], v[88:91], v[184:187]\n"
      "v_mfma_f32_16x16x32_bf16 v[192:195], a[188:191], v[88:91], v[192:195]\n"
      "global_load_dwordx4 a[140:143], v44, s[6:7] offset:3072 sc1\n"
      "v_mfma_f32_16x16x32_bf16 v[180:183], a[180:183], v[92:95], v[180:183]\n"
      "v_mfma_f32_16x16x32_bf16 v[188:191], a[184:187], v[92:95], v[188:191]\n"
      "v_mfma_f32_16x16x32_bf16 v[196:199], a[188:191], v[92:95], v[196:199]\n"
      "s_waitcnt lgkmcnt(0)\n"
      "v_mfma_f32_16x16x32_bf16 v[176:179], a[192:195], v[96:99], v[176:179]\n"
      "ds_read_b64_tr_b16 v[64:65], v33 offset:0\n"
      "v_mfma_f32_16x16x32_bf16 v[184:187], a[196:199], v[96:99], v[184:187]\n"
      "ds_read_b64_tr_b16 v[66:67], v33 offset:2048\n"
      "v_mfma_f32_16x16x32_bf16 v[192:195], a[200:203], v[96:99], v[192:195]\n"
      "ds_read_b64_tr_b16 v[68:69], v35 offset:0\n"
      "v_mfma_f32_16x16x32_bf16 v[180:183], a[192:195], v[100:103], v[180:183]\n"
      "ds_read_b64_tr_b16 v[70:71], v35 offset:2048\n"
      "v_mfma_f32_16x16x32_bf16 v[188:191], a[196:199], v[100:103], v[188:191]\n"
      "ds_read_b64_tr_b16 v[72:73], v33 offset:4096\n"
      "v_mfma_f32_16x16x32_bf16 v[196:199], a[200:203], v[100:103], v[196:199]\n"
      "ds_read_b64_tr_b16 v[74:75], v33 offset:6144\n"
      "v_mfma_f32_16x16x32_bf16 v[176:179], a[204:207], v[104:107], v[176:179]\n"
      "ds_read_b64_tr_b16 v[76:77], v35 offset:4096\n"
      "v_add_u32 v45, 4096, v7\n"
      "v_mfma_f32_16x16x32_bf16 v[184:187], a[208:211], v[104:107], v[184:187]\n"
      "ds_read_b64_tr_b16 v[78:79], v35 offset:6144\n"
      "v_mfma_f32_16x16x32_bf16 v[192:195], a[212:215], v[104:107], v[192:195]\n"
      "global_load_dwordx4 v[8:11], v45, s[10:11] offset:-4096 sc1\n"
      "v_mfma_f32_16x16x32_bf16 v[180:183], a[204:207], v[108:111], v[180:183]\n"
      "ds_read_b64_tr_b16 v[80:81], v33 offset:8192\n"
      "v_mfma_f32_16x16x32_bf16 v[188:191], a[208:211], v[108:111], v[188:191]\n"
      "global_load_dwordx4 v[12:15], v45, s[12:13] offset:-4096 sc1\n"
      "v_mfma_f32_16x16x32_bf16 v[196:199], a[212:215], v[108:111], v[196:199]\n"
      "ds_read_b64_tr_b16 v[82:83], v33 offset:10240\n"
      "v_mfma_f32_16x16x32_bf16 v[176:179], a[216:219], v[112:115], v[176:179]\n"
      "ds_read_b64_tr_b16 v[84:85], v35 offset:8192\n"
      "v_mfma_f32_16x16x32_bf16 v[184:187], a[220:223], v[112:115], v[184:187]\n"
      "global_load_dwordx4 v[16:19], v45, s[10:11] offset:-3072 sc1\n"
      "v_mfma_f32_16x16x32_bf16 v[192:195], a[224:227], v[112:115], v[192:195]\n"
      "ds_read_b64_tr_b16 v[86:87], v35 offset:10240\n"
      "v_mfma_f32_16x16x32_bf16 v[180:183], a[216:219], v[116:119], v[180:183]\n"
      "ds_read_b64_tr_b16 v[88:89], v33 offset:12288\n"
      "v_mfma_f32_16x16x32_bf16 v[188:191], a[220:223], v[116:119], v[188:191]\n"
      "global_load_dwordx4 v[20:23], v45, s[12:13] offset:-3072 sc1\n"
      "ds_read_b64_tr_b16 v[90:91], v33 offset:14336\n"
      "v_mfma_f32_16x16x32_bf16 v[196:199], a[224:227], v[116:119], v[196:199]\n"
      "ds_read_b64_tr_b16 v[92:93], v35 offset:12288\n"
      "v_mfma_f32_16x16x32_bf16 v[176:179], a[228:231], v[120:123], v[176:179]\n"
      "ds_read_b64_tr_b16 v[94:95], v35 offset:14336\n"
      "v_mfma_f32_16x16x32_bf16 v[184:187], a[232:235], v[120:123], v[184:187]\n"
      "global_load_dwordx4 v[128:131], v45, s[10:11] offset:-2048 sc1\n"
      "v_mfma_f32_16x16x32_bf16 v[192:195], a[236:239], v[120:123], v[192:195]\n"
      "v_mfma_f32_16x16x32_bf16 v[180:183], a[228:231], v[124:127], v[180:183]\n"
      "v_mfma_f32_16x16x32_bf16 v[188:191], a[232:235], v[124:127], v[188:191]\n"
      "global_load_dwordx4 v[132:135], v45, s[12:13] offset:-2048 sc1\n"
      "v_mfma_f32_16x16x32_bf16 v[196:199], a[236:239], v[124:127], v[196:199]\n"
      "buffer_load_dword v36, v42, s[32:35], s63 offen offset:0 sc1\n"
      "buffer_load_dword v37, v42, s[32:35], s63 offen offset:256 sc1\n"
      "s_waitcnt lgkmcnt(0)\n"
      "s_barrier\n"
      "v_mfma_f32_16x16x32_bf16 v[200:203], a[144:147], v[64:67], v[200:203]\n"
      "ds_read_b64_tr_b16 v[96:97], v33 offset:16384\n"
      "v_mfma_f32_16x16x32_bf16 v[208:211], a[148:151], v[64:67], v[208:211]\n"
      "global_load_dwordx4 v[136:139], v45, s[10:11] offset:-1024 sc1\n"
      "v_mfma_f32_16x16x32_bf16 v[216:219], a[152:155], v[64:67], v[216:219]\n"
      "ds_read_b64_tr_b16 v[98:99], v33 offset:18432\n"
      "v_mfma_f32_16x16x32_bf16 v[204:207], a[144:147], v[68:71], v[204:207]\n"
      "ds_read_b64_tr_b16 v[100:101], v35 offset:16384\n"
      "v_fmac_f32 v176, v24, v224\n"
      "v_mfma_f32_16x16x32_bf16 v[212:215], a[148:151], v[68:71], v[212:215]\n"
      "global_load_dwordx4 v[140:143], v45, s[12:13] offset:-1024 sc1\n"
      "v_fmac_f32 v177, v24, v225\n"
      "v_fmac_f32 v178, v24, v226\n"
      "v_mfma_f32_16x16x32_bf16 v[220:223], a[152:155], v[68:71], v[220:223]\n"
      "ds_read_b64_tr_b16 v[102:103], v35 offset:18432\n"
      "v_fmac_f32 v179, v24, v227\n"
      "v_fma_f32 v176, v176, v25, v236\n"
      "v_mfma_f32_16x16x32_bf16 v[200:203], a[156:159], v[72:75], v[200:203]\n"
      "ds_read_b64_tr_b16 v[104:105], v33 offset:20480\n"
      "v_fma_f32 v177, v177, v25, v237\n"
      "v_mfma_f32_16x16x32_bf16 v[208:211], a[160:163], v[72:75], v[208:211]\n"
      "global_load_dwordx4 v[144:147], v45, s[10:11] offset:0 sc1\n"
      "ds_read_b64_tr_b16 v[106:107], v33 offset:22528\n"
      "v_fma_f32 v178, v178, v25, v238\n"
      "v_fma_f32 v179, v179, v25, v239\n"
      "v_mfma_f32_16x16x32_bf16 v[216:219], a[164:167], v[72:75], v[216:219]\n"
      "ds_read_b64_tr_b16 v[108:109], v35 offset:20480\n"
      "v_mul_f32 v176, v176, v178\n"
      "v_mul_f32 v177, v177, v179\n"
      "v_mfma_f32_16x16x32_bf16 v[204:207], a[156:159], v[76:79], v[204:207]\n"
      "ds_read_b64_tr_b16 v[110:111], v35 offset:22528\n"
      "v_cvt_pk_bf16_f32 v176, v176, v177\n"
      "v_mfma_f32_16x16x32_bf16 v[212:215], a[160:163], v[76:79], v[212:215]\n"
      "global_load_dwordx4 v[148:151], v45, s[12:13] offset:0 sc1\n"
      "ds_write_b32 v46, v176 offset:0\n"
      "ds_read_b64_tr_b16 v[112:113], v33 offset:24576\n"
      "v_fmac_f32 v184, v24, v228\n"
      "v_mfma_f32_16x16x32_bf16 v[220:223], a[164:167], v[76:79], v[220:223]\n"
      "ds_read_b64_tr_b16 v[114:115], v33 offset:26624\n"
      "v_fmac_f32 v185, v24, v229\n"
      "v_fmac_f32 v186, v24, v230\n"
      "v_mfma_f32_16x16x32_bf16 v[200:203], a[168:171], v[80:83], v[200:203]\n"
      "ds_read_b64_tr_b16 v[116:117], v35 offset:24576\n"
      "v_fmac_f32 v187, v24, v231\n"
      "v_fma_f32 v184, v184, v25, v240\n"
      "v_mfma_f32_16x16x32_bf16 v[208:211], a[172:175], v[80:83], v[208:211]\n"
      "global_load_dwordx4 v[152:155], v45, s[10:11] offset:1024 sc1\n"
      "v_fma_f32 v185, v185, v25, v241\n"
      "v_mfma_f32_16x16x32_bf16 v[216:219], a[176:179], v[80:83], v[216:219]\n"
      "ds_read_b64_tr_b16 v[118:119], v35 offset:26624\n"
      "v_fma_f32 v186, v186, v25, v242\n"
      "v_fma_f32 v187, v187, v25, v243\n"
      "v_mfma_f32_16x16x32_bf16 v[204:207], a[168:171], v[84:87], v[204:207]\n"
      "ds_read_b64_tr_b16 v[120:121], v33 offset:28672\n"
      "v_mul_f32 v184, v184, v186\n"
      "v_mul_f32 v185, v185, v187\n"
      "v_mfma_f32_16x16x32_bf16 v[212:215], a[172:175], v[84:87], v[212:215]\n"
      "global_load_dwordx4 v[156:159], v45, s[12:13] offset:1024 sc1\n"
      "ds_read_b64_tr_b16 v[122:123], v33 offset:30720\n"
      "v_cvt_pk_bf16_f32 v184, v184, v185\n"
      "v_mfma_f32_16x16x32_bf16 v[220:223], a[176:179], v[84:87], v[220:223]\n"
      "buffer_load_dword v38, v42, s[32:35], s63 offen offset:512 sc1\n"
      "buffer_load_dword v39, v42, s[32:35], s63 offen offset:768 sc1\n"
      "ds_write_b32 v46, v184 offset:4096\n"
      "ds_read_b64_tr_b16 v[124:125], v35 offset:28672\n"
      "v_fmac_f32 v192, v24, v232\n"
      "v_mfma_f32_16x16x32_bf16 v[200:203], a[180:183], v[88:91], v[200:203]\n"
      "ds_read_b64_tr_b16 v[126:127], v35 offset:30720\n"
      "v_fmac_f32 v193, v24, v233\n"
      "v_fmac_f32 v194, v24, v234\n"
      "v_add_u32 v45, 10240, v7\n"
      "v_mfma_f32_16x16x32_bf16 v[208:211], a[184:187], v[88:91], v[208:211]\n"
      "global_load_dwordx4 v[172:175], v45, s[10:11] offset:-4096 sc1\n"
      "v_fmac_f32 v195, v24, v235\n"
      "v_fma_f32 v192, v192, v25, v244\n"
      "v_mfma_f32_16x16x32_bf16 v[216:219], a[188:191], v[88:91], v[216:219]\n"
      "v_fma_f32 v193, v193, v25, v245\n"
      "v_fma_f32 v194, v194, v25, v246\n"
      "v_mfma_f32_16x16x32_bf16 v[204:207], a[180:183], v[92:95], v[204:207]\n"
      "v_fma_f32 v195, v195, v25, v247\n"
      "v_mul_f32 v192, v192, v194\n"
      "v_mfma_f32_16x16x32_bf16 v[212:215], a[184:187], v[92:95], v[212:215]\n"
      "global_load_dwordx4 v[160:163], v45, s[12:13] offset:-4096 sc1\n"
      "v_mul_f32 v193, v193, v195\n"
      "v_mfma_f32_16x16x32_bf16 v[220:223], a[188:191], v[92:95], v[220:223]\n"
      "v_cvt_pk_bf16_f32 v192, v192, v193\n"
      "s_waitcnt lgkmcnt(0)\n"
      "v_mfma_f32_16x16x32_bf16 v[200:203], a[192:195], v[96:99], v[200:203]\n"
      "ds_write_b32 v46, v192 offset:8192\n"
      "v_fmac_f32 v180, v26, v224\n"
      "v_fmac_f32 v181, v26, v225\n"
      "v_mfma_f32_16x16x32_bf16 v[208:211], a[196:199], v[96:99], v[208:211]\n"
      "global_load_dwordx4 v[164:167], v45, s[10:11] offset:-3072 sc1\n"
      "v_fmac_f32 v182, v26, v226\n"
      "v_mfma_f32_16x16x32_bf16 v[216:219], a[200:203], v[96:99], v[216:219]\n"
      "v_fmac_f32 v183, v26, v227\n"
      "v_fma_f32 v180, v180, v27, v236\n"
      "v_mfma_f32_16x16x32_bf16 v[204:207], a[192:195], v[100:103], v[204:207]\n"
      "v_fma_f32 v181, v181, v27, v237\n"
      "v_fma_f32 v182, v182, v27, v238\n"
      "v_mfma_f32_16x16x32_bf16 v[212:215], a[196:199], v[100:103], v[212:215]\n"
      "global_load_dwordx4 v[168:171], v45, s[12:13] offset:-3072 sc1\n"
      "v_fma_f32 v183, v183, v27, v239\n"
      "v_mfma_f32_16x16x32_bf16 v[220:223], a[200:203], v[100:103], v[220:223]\n"
      "v_mul_f32 v180, v180, v182\n"
      "v_mul_f32 v181, v181, v183\n"
      "v_mfma_f32_16x16x32_bf16 v[200:203], a[204:207], v[104:107], v[200:203]\n"
      "v_cvt_pk_bf16_f32 v180, v180, v181\n"
      "s_waitcnt vmcnt(40)\n"
      "v_mfma_f32_16x16x32_bf16 v[208:211], a[208:211], v[104:107], v[208:211]\n"
      "global_load_dwordx4 v[64:67], v45, s[10:11] offset:-2048 sc1\n"
      "ds_write_b32 v46, v180 offset:2048\n"
      "ds_read_b128 a[144:147], v57 offset:0\n"
      "v_fmac_f32 v188, v26, v228\n"
      "v_mfma_f32_16x16x32_bf16 v[216:219], a[212:215], v[104:107], v[216:219]\n"
      "ds_read_b128 a[148:151], v57 offset:1024\n"
      "v_fmac_f32 v189, v26, v229\n"
      "v_fmac_f32 v190, v26, v230\n"
      "v_mfma_f32_16x16x32_bf16 v[204:207], a[204:207], v[108:111], v[204:207]\n"
      "ds_read_b128 a[152:155], v57 offset:2048\n"
      "v_fmac_f32 v191, v26, v231\n"
      "v_fma_f32 v188, v188, v27, v240\n"
      "v_mfma_f32_16x16x32_bf16 v[212:215], a[208:211], v[108:111], v[212:215]\n"
      "global_load_dwordx4 v[68:71], v45, s[12:13] offset:-2048 sc1\n"
      "v_fma_f32 v189, v189, v27, v241\n"
      "v_mfma_f32_16x16x32_bf16 v[220:223], a[212:215], v[108:111], v[220:223]\n"
      "buffer_load_dword v60, v42, s[32:35], s63 offen offset:1024 sc1\n"
      "buffer_load_dword v61, v42, s[32:35], s63 offen offset:1280 sc1\n"
      "v_fma_f32 v190, v190, v27, v242\n"
      "v_fma_f32 v191, v191, v27, v243\n"
      "v_mfma_f32_16x16x32_bf16 v[200:203], a[216:219], v[112:115], v[200:203]\n"
      "ds_read_b128 a[156:159], v57 offset:3072\n"
      "v_mul_f32 v188, v188, v190\n"
      "v_mul_f32 v189, v189, v191\n"
      "v_mfma_f32_16x16x32_bf16 v[208:211], a[220:223], v[112:115], v[208:211]\n"
      "global_load_dwordx4 v[72:75], v45, s[10:11] offset:-1024 sc1\n"
      "v_cvt_pk_bf16_f32 v188, v188, v189\n"
      "v_mfma_f32_16x16x32_bf16 v[216:219], a[224:227], v[112:115], v[216:219]\n"
      "ds_write_b32 v46, v188 offset:6144\n"
      "ds_read_b128 a[160:163], v57 offset:4096\n"
      "v_fmac_f32 v196, v26, v232\n"
      "v_mfma_f32_16x16x32_bf16 v[204:207], a[216:219], v[116:119], v[204:207]\n"
      "ds_read_b128 a[164:167], v57 offset:5120\n"
      "s_waitcnt vmcnt(17)\n"
      "v_fmac_f32 v197, v26, v233\n"
      "v_fmac_f32 v198, v26, v234\n"
      "v_mfma_f32_16x16x32_bf16 v[212:215], a[220:223], v[116:119], v[212:215]\n"
      "global_load_dwordx4 v[76:79], v45, s[12:13] offset:-1024 sc1\n"
      "v_add3_u32 v2, v8, v9, s30\n"
      "v_add3_u32 v2, v2, v10, v11\n"
      "v_add3_u32 v2, v2, v16, v17\n"
      "v_fmac_f32 v199, v26, v235\n"
      "v_fma_f32 v196, v196, v27, v244\n"
      "v_mfma_f32_16x16x32_bf16 v[220:223], a[224:227], v[116:119], v[220:223]\n"
      "ds_read_b128 a[168:171], v57 offset:6144\n"
      "v_add3_u32 v2, v2, v18, v19\n"
      "v_add3_u32 v2, v2, v128, v129\n"
      "v_add3_u32 v2, v2, v130, v131\n"
      "v_fma_f32 v197, v197, v27, v245\n"
      "v_mfma_f32_16x16x32_bf16 v[200:203], a[228:231], v[120:123], v[200:203]\n"
      "ds_read_b128 a[172:175], v57 offset:7168\n"
      "v_xor_b32 v2, v2, v36\n"
      "v_add3_u32 v3, v12, v13, s30\n"
      "v_add3_u32 v3, v3, v14, v15\n"
      "v_fma_f32 v198, v198, v27, v246\n"
      "v_fma_f32 v199, v199, v27, v247\n"
      "v_mfma_f32_16x16x32_bf16 v[208:211], a[232:235], v[120:123], v[208:211]\n"
      "global_load_dwordx4 v[80:83], v45, s[10:11] offset:0 sc1\n"
      "v_add3_u32 v3, v3, v20, v21\n"
      "v_add3_u32 v3, v3, v22, v23\n"
      "v_add3_u32 v3, v3, v132, v133\n"
      "v_mul_f32 v196, v196, v198\n"
      "v_mul_f32 v197, v197, v199\n"
      "v_mfma_f32_16x16x32_bf16 v[216:219], a[236:239], v[120:123], v[216:219]\n"
      "ds_read_b128 a[176:179], v57 offset:8192\n"
      "v_add3_u32 v3, v3, v134, v135\n"
      "v_xor_b32 v3, v3, v37\n"
      "v_or_b32 v2, v2, v3\n"
      "v_cvt_pk_bf16_f32 v196, v196, v197\n"
      "v_mfma_f32_16x16x32_bf16 v[204:207], a[228:231], v[124:127], v[204:207]\n"
      "ds_write_b32 v46, v196 offset:10240\n"
      "v_cmp_ne_u32 vcc, 0, v2\n"
      "v_mfma_f32_16x16x32_bf16 v[212:215], a[232:235], v[124:127], v[212:215]\n"
      "global_load_dwordx4 v[84:87], v45, s[12:13] offset:0 sc1\n"
      "v_mfma_f32_16x16x32_bf16 v[220:223], a[236:239], v[124:127], v[220:223]\n"
      "s_cbranch_vccnz L_shA0_%=\n"
      "L_bhA0_%=:\n"
      "s_waitcnt lgkmcnt(0)\n"
      "s_barrier\n"
      "v_mfma_f32_16x16x32_bf16 v[96:99], a[240:243], v[8:11], 0\n"
      "ds_read_b128 v[176:179], v47 offset:0\n"
      "v_mfma_f32_16x16x32_bf16 v[104:107], a[244:247], v[8:11], 0\n"
      "global_load_dwordx4 v[88:91], v45, s[10:11] offset:1024 sc1\n"
      "ds_read_b128 v[180:183], v47 offset:4096\n"
      "v_fmac_f32 v200, v28, v224\n"
      "v_mfma_f32_16x16x32_bf16 v[112:115], a[248:251], v[8:11], 0\n"
      "ds_read_b128 v[184:187], v47 offset:8192\n"
      "v_fmac_f32 v201, v28, v225\n"
      "v_fmac_f32 v202, v28, v226\n"
      "v_fmac_f32 v203, v28, v227\n"
      "v_fma_f32 v200, v200, v29, v236\n"
      "v_fma_f32 v201, v201, v29, v237\n"
      "v_fma_f32 v202, v202, v29, v238\n"
      "v_fma_f32 v203, v203, v29, v239\n"
      "v_mul_f32 v200, v200, v202\n"
      "v_mul_f32 v201, v201, v203\n"
      "v_cvt_pk_bf16_f32 v200, v200, v201\n"
      "v_mfma_f32_16x16x32_bf16 v[120:123], a[252:255], v[8:11], 0\n"
      "ds_write_b32 v46, v200 offset:1024\n"
      "v_add_u32 v43, s59, v7\n"
      "v_add_u32 v59, s59, v42\n"
      "L_pg5_%=:\n"
      "s_cbranch_execz L_pgr_%=\n"
      "v_fmac_f32 v208, v28, v228\n"
      "v_fmac_f32 v209, v28, v229\n"
      "v_fmac_f32 v210, v28, v230\n"
      "v_fmac_f32 v211, v28, v231\n"
      "v_fma_f32 v208, v208, v29, v240\n"
      "v_fma_f32 v209, v209, v29, v241\n"
      "v_fma_f32 v210, v210, v29, v242\n"
      "v_fma_f32 v211, v211, v29, v243\n"
      "v_mul_f32 v208, v208, v210\n"
      "v_mul_f32 v209, v209, v211\n"
      "v_cvt_pk_bf16_f32 v208, v208, v209\n"
      "v_mfma_f32_16x16x32_bf16 v[248:251], a[0:3], v[8:11], 0\n"
      "global_load_dwordx4 v[92:95], v45, s[12:13] offset:1024 sc1\n"
      "ds_write_b32 v46, v208 offset:5120\n"
      "v_fmac_f32 v216, v28, v232\n"
      "v_fmac_f32 v217, v28, v233\n"
      "v_mfma_f32_16x16x32_bf16 v[100:103], a[240:243], v[12:15], 0\n"
      "buffer_load_dword v62, v42, s[32:35], s63 offen offset:1536 sc1\n"
      "buffer_load_dword v63, v42, s[32:35], s63 offen offset:1792 sc1\n"
      "v_fmac_f32 v218, v28, v234\n"
      "v_fmac_f32 v219, v28, v235\n"
      "v_fma_f32 v216, v216, v29, v244\n"
      "v_fma_f32 v217, v217, v29, v245\n"
      "v_fma_f32 v218, v218, v29, v246\n"
      "v_fma_f32 v219, v219, v29, v247\n"
      "v_mul_f32 v216, v216, v218\n"
      "v_mul_f32 v217, v217, v219\n"
      "v_cvt_pk_bf16_f32 v216, v216, v217\n"
      "v_mfma_f32_16x16x32_bf16 v[108:111], a[244:247], v[12:15], 0\n"
      "ds_write_b32 v46, v216 offset:9216\n"
      "s_waitcnt lgkmcnt(3)\n"
      "v_fmac_f32 v204, v30, v224\n"
      "v_fmac_f32 v205, v30, v225\n"
      "v_fmac_f32 v206, v30, v226\n"
      "v_fmac_f32 v207, v30, v227\n"
      "v_fma_f32 v204, v204, v31, v236\n"
      "v_fma_f32 v205, v205, v31, v237\n"
      "v_fma_f32 v206, v206, v31, v238\n"
      "v_fma_f32 v207, v207, v31, v239\n"
      "v_mul_f32 v204, v204, v206\n"
      "v_mul_f32 v205, v205, v207\n"
      "v_cvt_pk_bf16_f32 v204, v204, v205\n"
      "v_mfma_f32_16x16x32_bf16 v[116:119], a[248:251], v[12:15], 0\n"
      "buffer_store_dwordx4 v[176:179], v43, s[32:35], s69 offen offset:0 sc1\n"
      "ds_write_b32 v46, v204 offset:3072\n"
      "v_add3_u32 v58, v176, v177, s30\n"
      "v_add3_u32 v58, v58, v178, v179\n"
      "v_mfma_f32_16x16x32_bf16 v[124:127], a[252:255], v[12:15], 0\n"
      "v_add3_u32 v58, v58, v180, v181\n"
      "v_fmac_f32 v212, v30, v228\n"
      "v_fmac_f32 v213, v30, v229\n"
      "v_fmac_f32 v214, v30, v230\n"
      "v_fmac_f32 v215, v30, v231\n"
      "v_fma_f32 v212, v212, v31, v240\n"
      "v_fma_f32 v213, v213, v31, v241\n"
      "v_fma_f32 v214, v214, v31, v242\n"
      "v_fma_f32 v215, v215, v31, v243\n"
      "v_mul_f32 v212, v212, v214\n"
      "v_mul_f32 v213, v213, v215\n"
      "v_cvt_pk_bf16_f32 v212, v212, v213\n"
      "v_mfma_f32_16x16x32_bf16 v[252:255], a[0:3], v[12:15], 0\n"
      "buffer_store_dwordx4 v[180:183], v43, s[32:35], s69 offen offset:1024 sc1\n"
      "ds_write_b32 v46, v212 offset:7168\n"
      "v_add3_u32 v58, v58, v182, v183\n"
      "v_fmac_f32 v220, v30, v232\n"
      "v_fmac_f32 v221, v30, v233\n"
      "v_fmac_f32 v222, v30, v234\n"
      "v_fmac_f32 v223, v30, v235\n"
      "v_mfma_f32_16x16x32_bf16 v[96:99], a[4:7], v[16:19], v[96:99]\n"
      "v_add3_u32 v58, v58, v184, v185\n"
      "v_add3_u32 v58, v58, v186, v187\n"
      "v_fma_f32 v220, v220, v31, v244\n"
      "v_fma_f32 v221, v221, v31, v245\n"
      "v_fma_f32 v222, v222, v31, v246\n"
      "v_fma_f32 v223, v223, v31, v247\n"
      "v_mul_f32 v220, v220, v222\n"
      "v_mul_f32 v221, v221, v223\n"
      "v_cvt_pk_bf16_f32 v220, v220, v221\n"
      "v_mfma_f32_16x16x32_bf16 v[104:107], a[8:11], v[16:19], v[104:107]\n"
      "buffer_store_dwordx4 v[184:187], v43, s[32:35], s69 offen offset:2048 sc1\n"
      "ds_write_b32 v46, v220 offset:11264\n"
      "v_add_u32 v57, s41, v7\n"
      "v_mfma_f32_16x16x32_bf16 v[112:115], a[12:15], v[16:19], v[112:115]\n"
      "ds_read_b128 a[180:183], v57 offset:0\n"
      "v_mfma_f32_16x16x32_bf16 v[120:123], a[16:19], v[16:19], v[120:123]\n"
      "buffer_store_dword v58, v59, s[32:35], s70 offen sc1\n"
      "v_mfma_f32_16x16x32_bf16 v[248:251], a[20:23], v[16:19], v[248:251]\n"
      "ds_read_b128 a[184:187], v57 offset:1024\n"
      "s_waitcnt lgkmcnt(2)\n"
      "s_barrier\n"
      "v_mfma_f32_16x16x32_bf16 v[100:103], a[4:7], v[20:23], v[100:103]\n"
      "ds_read_b128 v[200:203], v47 offset:0\n"
      "v_mfma_f32_16x16x32_bf16 v[108:111], a[8:11], v[20:23], v[108:111]\n"
      "ds_read_b128 v[204:207], v47 offset:4096\n"
      "v_mfma_f32_16x16x32_bf16 v[116:119], a[12:15], v[20:23], v[116:119]\n"
      "ds_read_b128 v[208:211], v47 offset:8192\n"
      "v_xor_b32 v43, 0x80000000, v43\n"
      "v_mfma_f32_16x16x32_bf16 v[124:127], a[16:19], v[20:23], v[124:127]\n"
      "ds_read_b128 a[188:191], v57 offset:2048\n"
      "v_xor_b32 v59, 0x80000000, v59\n"
      "v_mfma_f32_16x16x32_bf16 v[252:255], a[20:23], v[20:23], v[252:255]\n"
      "v_mfma_f32_16x16x32_bf16 v[96:99], a[24:27], v[128:131], v[96:99]\n"
      "v_mfma_f32_16x16x32_bf16 v[104:107], a[28:31], v[128:131], v[104:107]\n"
      "s_waitcnt vmcnt(20)\n"
      "s_waitcnt lgkmcnt(1)\n"
      "v_mfma_f32_16x16x32_bf16 v[112:115], a[32:35], v[128:131], v[112:115]\n"
      "buffer_store_dwordx4 v[200:203], v43, s[32:35], s69 offen offset:0 sc1\n"
      "v_add3_u32 v2, v136, v137, s30\n"
      "v_add3_u32 v2, v2, v138, v139\n"
      "v_add3_u32 v2, v2, v144, v145\n"
      "v_add3_u32 v58, v200, v201, s30\n"
      "v_mfma_f32_16x16x32_bf16 v[120:123], a[36:39], v[128:131], v[120:123]\n"
      "v_add3_u32 v2, v2, v146, v147\n"
      "v_add3_u32 v2, v2, v152, v153\n"
      "v_add3_u32 v2, v2, v154, v155\n"
      "v_add3_u32 v58, v58, v202, v203\n"
      "v_add3_u32 v58, v58, v204, v205\n"
      "v_mfma_f32_16x16x32_bf16 v[248:251], a[40:43], v[128:131], v[248:251]\n"
      "buffer_store_dwordx4 v[204:207], v43, s[32:35], s69 offen offset:1024 sc1\n"
      "v_xor_b32 v2, v2, v38\n"
      "v_add3_u32 v3, v140, v141, s30\n"
      "v_add3_u32 v3, v3, v142, v143\n"
      "v_add3_u32 v58, v58, v206, v207\n"
      "v_mfma_f32_16x16x32_bf16 v[100:103], a[24:27], v[132:135], v[100:103]\n"
      "v_add3_u32 v3, v3, v148, v149\n"
      "v_add3_u32 v3, v3, v150, v151\n"
      "v_add3_u32 v3, v3, v156, v157\n"
      "v_add3_u32 v58, v58, v208, v209\n"
      "v_add3_u32 v58, v58, v210, v211\n"
      "v_mfma_f32_16x16x32_bf16 v[108:111], a[28:31], v[132:135], v[108:111]\n"
      "buffer_store_dwordx4 v[208:211], v43, s[32:35], s69 offen offset:2048 sc1\n"
      "v_add3_u32 v3, v3, v158, v159\n"
      "v_xor_b32 v3, v3, v39\n"
      "v_or_b32 v2, v2, v3\n"
      "v_mfma_f32_16x16x32_bf16 v[116:119], a[32:35], v[132:135], v[116:119]\n"
      "v_cmp_ne_u32 vcc, 0, v2\n"
      "v_mfma_f32_16x16x32_bf16 v[124:127], a[36:39], v[132:135], v[124:127]\n"
      "buffer_store_dword v58, v59, s[32:35], s70 offen sc1\n"
      "v_mfma_f32_16x16x32_bf16 v[252:255], a[40:43], v[132:135], v[252:255]\n"
      "s_cbranch_vccnz L_shA1_%=\n"
      "L_bhA1_%=:\n"
      "v_mfma_f32_16x16x32_bf16 v[96:99], a[44:47], v[136:139], v[96:99]\n"
      "s_add_u32 s43, s73, s97\n"
      "v_add_u32 v56, s43, v7\n"
      "v_mfma_f32_16x16x32_bf16 v[104:107], a[48:51], v[136:139], v[104:107]\n"
      "s_and_b32 s52, s60, 1\n"
      "s_lshr_b32 s53, s60, 1\n"
      "v_mfma_f32_16x16x32_bf16 v[112:115], a[52:55], v[136:139], v[112:115]\n"
      "v_add_u32 v44, 53248, v7\n"
      "s_mul_i32 s54, s52, 20480\n"
      "v_mfma_f32_16x16x32_bf16 v[120:123], a[56:59], v[136:139], v[120:123]\n"
      "s_lshl_b32 s53, s53, 11\n"
      "s_add_u32 s54, s54, s53\n"
      "v_mfma_f32_16x16x32_bf16 v[248:251], a[60:63], v[136:139], v[248:251]\n"
      "global_load_dwordx4 a[192:195], v44, s[6:7] offset:0 sc1\n"
      "v_add_u32 v49, s54, v7\n"
      "v_mfma_f32_16x16x32_bf16 v[100:103], a[44:47], v[140:143], v[100:103]\n"
      "s_lshl_b32 s52, s52, 5\n"
      "v_xor_b32 v53, s52, v6\n"
      "v_mfma_f32_16x16x32_bf16 v[108:111], a[48:51], v[140:143], v[108:111]\n"
      "s_lshl_b32 s52, s53, 1\n"
      "s_add_u32 s52, s52, 110592\n"
      "v_mfma_f32_16x16x32_bf16 v[116:119], a[52:55], v[140:143], v[116:119]\n"
      "global_load_dwordx4 a[196:199], v44, s[6:7] offset:1024 sc1\n"
      "v_add_u32 v53, s52, v53\n"
      "v_mfma_f32_16x16x32_bf16 v[124:127], a[56:59], v[140:143], v[124:127]\n"
      "s_lshr_b32 s53, s53, 4\n"
      "s_add_u32 s53, s53, 163328\n"
      "v_mfma_f32_16x16x32_bf16 v[252:255], a[60:63], v[140:143], v[252:255]\n"
      "v_lshrrev_b32 v48, 4, v7\n"
      "v_and_b32 v50, 0x30, v48\n"
      "v_mfma_f32_16x16x32_bf16 v[96:99], a[64:67], v[144:147], v[96:99]\n"
      "global_load_dwordx4 a[200:203], v44, s[6:7] offset:2048 sc1\n"
      "v_add_u32 v54, s53, v50\n"
      "v_mfma_f32_16x16x32_bf16 v[104:107], a[68:71], v[144:147], v[104:107]\n"
      "v_and_b32 v48, 15, v48\n"
      "v_lshrrev_b32 v50, 1, v50\n"
      "v_mfma_f32_16x16x32_bf16 v[112:115], a[72:75], v[144:147], v[112:115]\n"
      "v_mul_u32_u24 v48, 1280, v48\n"
      "v_add3_u32 v55, v48, v50, s74\n"
      "v_mfma_f32_16x16x32_bf16 v[120:123], a[76:79], v[144:147], v[120:123]\n"
      "global_load_dwordx4 a[204:207], v44, s[6:7] offset:3072 sc1\n"
      "v_mfma_f32_16x16x32_bf16 v[248:251], a[80:83], v[144:147], v[248:251]\n"
      "v_mfma_f32_16x16x32_bf16 v[100:103], a[64:67], v[148:151], v[100:103]\n"
      "v_add_u32 v44, 61440, v7\n"
      "v_mfma_f32_16x16x32_bf16 v[108:111], a[68:71], v[148:151], v[108:111]\n"
      "global_load_dwordx4 a[208:211], v44, s[6:7] offset:-4096 sc1\n"
      "v_mfma_f32_16x16x32_bf16 v[116:119], a[72:75], v[148:151], v[116:119]\n"
      "v_mfma_f32_16x16x32_bf16 v[124:127], a[76:79], v[148:151], v[124:127]\n"
      "v_mfma_f32_16x16x32_bf16 v[252:255], a[80:83], v[148:151], v[252:255]\n"
      "global_load_dwordx4 a[212:215], v44, s[6:7] offset:-3072 sc1\n"
      "v_mfma_f32_16x16x32_bf16 v[96:99], a[84:87], v[152:155], v[96:99]\n"
      "v_mfma_f32_16x16x32_bf16 v[104:107], a[88:91], v[152:155], v[104:107]\n"
      "s_waitcnt vmcnt(22)\n"
      "s_waitcnt lgkmcnt(0)\n"
      "v_mfma_f32_16x16x32_bf16 v[112:115], a[92:95], v[152:155], v[112:115]\n"
      "global_load_dwordx4 a[216:219], v44, s[6:7] offset:-2048 sc1\n"
      "v_add3_u32 v2, v172, v173, s30\n"
      "v_add3_u32 v2, v2, v174, v175\n"
      "v_add3_u32 v2, v2, v164, v165\n"
      "v_mfma_f32_16x16x32_bf16 v[120:123], a[96:99], v[152:155], v[120:123]\n"
      "v_add3_u32 v2, v2, v166, v167\n"
      "v_add3_u32 v2, v2, v64, v65\n"
      "v_add3_u32 v2, v2, v66, v67\n"
      "v_mfma_f32_16x16x32_bf16 v[248:251], a[100:103], v[152:155], v[248:251]\n"
      "v_xor_b32 v2, v2, v60\n"
      "v_add3_u32 v3, v160, v161, s30\n"
      "v_add3_u32 v3, v3, v162, v163\n"
      "v_mfma_f32_16x16x32_bf16 v[100:103], a[84:87], v[156:159], v[100:103]\n"
      "global_load_dwordx4 a[220:223], v44, s[6:7] offset:-1024 sc1\n"
      "v_add3_u32 v3, v3, v168, v169\n"
      "v_add3_u32 v3, v3, v170, v171\n"
      "v_add3_u32 v3, v3, v68, v69\n"
      "v_mfma_f32_16x16x32_bf16 v[108:111], a[88:91], v[156:159], v[108:111]\n"
      "v_add3_u32 v3, v3, v70, v71\n"
      "v_xor_b32 v3, v3, v61\n"
      "v_or_b32 v2, v2, v3\n"
      "v_mfma_f32_16x16x32_bf16 v[116:119], a[92:95], v[156:159], v[116:119]\n"
      "v_cmp_ne_u32 vcc, 0, v2\n"
      "v_mfma_f32_16x16x32_bf16 v[124:127], a[96:99], v[156:159], v[124:127]\n"
      "v_mfma_f32_16x16x32_bf16 v[252:255], a[100:103], v[156:159], v[252:255]\n"
      "s_cbranch_vccnz L_shA2_%=\n"
      "L_bhA2_%=:\n"
      "v_mfma_f32_16x16x32_bf16 v[96:99], a[104:107], v[172:175], v[96:99]\n"
      "v_mfma_f32_16x16x32_bf16 v[104:107], a[108:111], v[172:175], v[104:107]\n"
      "v_add_u32 v45, 102400, v7\n"
      "v_mfma_f32_16x16x32_bf16 v[112:115], a[112:115], v[172:175], v[112:115]\n"
      "global_load_dwordx4 v[224:227], v45, s[10:11] offset:-4096 sc1\n"
      "v_mfma_f32_16x16x32_bf16 v[120:123], a[116:119], v[172:175], v[120:123]\n"
      "v_mfma_f32_16x16x32_bf16 v[248:251], a[120:123], v[172:175], v[248:251]\n"
      "v_mfma_f32_16x16x32_bf16 v[100:103], a[104:107], v[160:163], v[100:103]\n"
      "global_load_dwordx4 v[228:231], v45, s[10:11] offset:-3072 sc1\n"
      "v_mfma_f32_16x16x32_bf16 v[108:111], a[108:111], v[160:163], v[108:111]\n"
      "v_mfma_f32_16x16x32_bf16 v[116:119], a[112:115], v[160:163], v[116:119]\n"
      "v_mfma_f32_16x16x32_bf16 v[124:127], a[116:119], v[160:163], v[124:127]\n"
      "global_load_dwordx4 v[232:235], v45, s[10:11] offset:-2048 sc1\n"
      "v_mfma_f32_16x16x32_bf16 v[252:255], a[120:123], v[160:163], v[252:255]\n"
      "v_mfma_f32_16x16x32_bf16 v[96:99], a[124:127], v[164:167], v[96:99]\n"
      "v_mfma_f32_16x16x32_bf16 v[104:107], a[128:131], v[164:167], v[104:107]\n"
      "global_load_dwordx4 v[236:239], v45, s[10:11] offset:-1024 sc1\n"
      "v_mfma_f32_16x16x32_bf16 v[112:115], a[132:135], v[164:167], v[112:115]\n"
      "v_mfma_f32_16x16x32_bf16 v[120:123], a[136:139], v[164:167], v[120:123]\n"
      "v_mfma_f32_16x16x32_bf16 v[248:251], a[140:143], v[164:167], v[248:251]\n"
      "global_load_dwordx4 v[240:243], v45, s[10:11] offset:0 sc1\n"
      "v_mfma_f32_16x16x32_bf16 v[100:103], a[124:127], v[168:171], v[100:103]\n"
      "v_mfma_f32_16x16x32_bf16 v[108:111], a[128:131], v[168:171], v[108:111]\n"
      "v_mfma_f32_16x16x32_bf16 v[116:119], a[132:135], v[168:171], v[116:119]\n"
      "global_load_dwordx4 v[244:247], v45, s[10:11] offset:1024 sc1\n"
      "v_mfma_f32_16x16x32_bf16 v[124:127], a[136:139], v[168:171], v[124:127]\n"
      "buffer_load_dword v32, v42, s[32:35], s63 offen offset:2048 sc1\n"
      "buffer_load_dword v33, v42, s[32:35], s63 offen offset:2304 sc1\n"
      "v_mfma_f32_16x16x32_bf16 v[252:255], a[140:143], v[168:171], v[252:255]\n"
      "v_mfma_f32_16x16x32_bf16 v[96:99], a[144:147], v[64:67], v[96:99]\n"
      "v_mfma_f32_16x16x32_bf16 v[104:107], a[148:151], v[64:67], v[104:107]\n"
      "v_add_u32 v45, 108544, v7\n"
      "s_waitcnt vmcnt(8)\n"
      "v_mfma_f32_16x16x32_bf16 v[112:115], a[152:155], v[64:67], v[112:115]\n"
      "v_add3_u32 v2, v72, v73, s30\n"
      "v_add3_u32 v2, v2, v74, v75\n"
      "v_add3_u32 v2, v2, v80, v81\n"
      "v_mfma_f32_16x16x32_bf16 v[120:123], a[156:159], v[64:67], v[120:123]\n"
      "global_load_dwordx4 v[8:11], v45, s[10:11] offset:-4096 sc1\n"
      "v_add3_u32 v2, v2, v82, v83\n"
      "v_add3_u32 v2, v2, v88, v89\n"
      "v_add3_u32 v2, v2, v90, v91\n"
      "v_mfma_f32_16x16x32_bf16 v[248:251], a[160:163], v[64:67], v[248:251]\n"
      "v_xor_b32 v2, v2, v62\n"
      "v_add3_u32 v3, v76, v77, s30\n"
      "v_add3_u32 v3, v3, v78, v79\n"
      "v_mfma_f32_16x16x32_bf16 v[100:103], a[144:147], v[68:71], v[100:103]\n"
      "v_add3_u32 v3, v3, v84, v85\n"
      "v_add3_u32 v3, v3, v86, v87\n"
      "v_add3_u32 v3, v3, v92, v93\n"
      "v_mfma_f32_16x16x32_bf16 v[108:111], a[148:151], v[68:71], v[108:111]\n"
      "v_add3_u32 v3, v3, v94, v95\n"
      "v_xor_b32 v3, v3, v63\n"
      "v_or_b32 v2, v2, v3\n"
      "v_mfma_f32_16x16x32_bf16 v[116:119], a[152:155], v[68:71], v[116:119]\n"
      "global_load_dwordx4 v[12:15], v45, s[10:11] offset:-3072 sc1\n"
      "v_cmp_ne_u32 vcc, 0, v2\n"
      "v_mfma_f32_16x16x32_bf16 v[124:127], a[156:159], v[68:71], v[124:127]\n"
      "v_mfma_f32_16x16x32_bf16 v[252:255], a[160:163], v[68:71], v[252:255]\n"
      "s_cbranch_vccnz L_shA3_%=\n"
      "L_bhA3_%=:\n"
      "v_mfma_f32_16x16x32_bf16 v[96:99], a[164:167], v[72:75], v[96:99]\n"
      "v_mfma_f32_16x16x32_bf16 v[104:107], a[168:171], v[72:75], v[104:107]\n"
      "global_load_dwordx4 v[16:19], v45, s[10:11] offset:-2048 sc1\n"
      "v_mfma_f32_16x16x32_bf16 v[112:115], a[172:175], v[72:75], v[112:115]\n"
      "v_mfma_f32_16x16x32_bf16 v[120:123], a[176:179], v[72:75], v[120:123]\n"
      "v_mfma_f32_16x16x32_bf16 v[248:251], a[180:183], v[72:75], v[248:251]\n"
      "v_mfma_f32_16x16x32_bf16 v[100:103], a[164:167], v[76:79], v[100:103]\n"
      "global_load_dwordx4 v[20:23], v45, s[10:11] offset:-1024 sc1\n"
      "v_mfma_f32_16x16x32_bf16 v[108:111], a[168:171], v[76:79], v[108:111]\n"
      "v_mfma_f32_16x16x32_bf16 v[116:119], a[172:175], v[76:79], v[116:119]\n"
      "v_mfma_f32_16x16x32_bf16 v[124:127], a[176:179], v[76:79], v[124:127]\n"
      "global_load_dwordx4 v[128:131], v45, s[10:11] offset:0 sc1\n"
      "v_mfma_f32_16x16x32_bf16 v[252:255], a[180:183], v[76:79], v[252:255]\n"
      "v_mfma_f32_16x16x32_bf16 v[96:99], a[184:187], v[80:83], v[96:99]\n"
      "v_mfma_f32_16x16x32_bf16 v[104:107], a[188:191], v[80:83], v[104:107]\n"
      "global_load_dwordx4 v[132:135], v45, s[10:11] offset:1024 sc1\n"
      "v_mfma_f32_16x16x32_bf16 v[112:115], a[192:195], v[80:83], v[112:115]\n"
      "buffer_load_dword v34, v42, s[32:35], s63 offen offset:2560 sc1\n"
      "buffer_load_dword v35, v42, s[32:35], s63 offen offset:2816 sc1\n"
      "v_mfma_f32_16x16x32_bf16 v[120:123], a[196:199], v[80:83], v[120:123]\n"
      "v_mfma_f32_16x16x32_bf16 v[248:251], a[200:203], v[80:83], v[248:251]\n"
      "v_mfma_f32_16x16x32_bf16 v[100:103], a[184:187], v[84:87], v[100:103]\n"
      "v_add_u32 v45, 102400, v7\n"
      "v_mfma_f32_16x16x32_bf16 v[108:111], a[188:191], v[84:87], v[108:111]\n"
      "v_mfma_f32_16x16x32_bf16 v[116:119], a[192:195], v[84:87], v[116:119]\n"
      "global_load_dwordx4 v[136:139], v45, s[12:13] offset:-4096 sc1\n"
      "v_mfma_f32_16x16x32_bf16 v[124:127], a[196:199], v[84:87], v[124:127]\n"
      "v_mfma_f32_16x16x32_bf16 v[252:255], a[200:203], v[84:87], v[252:255]\n"
      "v_mfma_f32_16x16x32_bf16 v[96:99], a[204:207], v[88:91], v[96:99]\n"
      "v_mfma_f32_16x16x32_bf16 v[104:107], a[208:211], v[88:91], v[104:107]\n"
      "global_load_dwordx4 v[140:143], v45, s[12:13] offset:-3072 sc1\n"
      "s_waitcnt vmcnt(10)\n"
      "v_mfma_f32_16x16x32_bf16 v[112:115], a[212:215], v[88:91], v[112:115]\n"
      "v_add3_u32 v2, v224, v225, s30\n"
      "v_add3_u32 v2, v2, v226, v227\n"
      "v_add3_u32 v2, v2, v228, v229\n"
      "v_mfma_f32_16x16x32_bf16 v[120:123], a[216:219], v[88:91], v[120:123]\n"
      "v_add3_u32 v2, v2, v230, v231\n"
      "v_add3_u32 v2, v2, v232, v233\n"
      "v_add3_u32 v2, v2, v234, v235\n"
      "v_mfma_f32_16x16x32_bf16 v[248:251], a[220:223], v[88:91], v[248:251]\n"
      "v_xor_b32 v2, v2, v32\n"
      "v_add3_u32 v3, v236, v237, s30\n"
      "v_add3_u32 v3, v3, v238, v239\n"
      "v_mfma_f32_16x16x32_bf16 v[100:103], a[204:207], v[92:95], v[100:103]\n"
      "global_load_dwordx4 v[144:147], v45, s[12:13] offset:-2048 sc1\n"
      "v_add3_u32 v3, v3, v240, v241\n"
      "v_add3_u32 v3, v3, v242, v243\n"
      "v_add3_u32 v3, v3, v244, v245\n"
      "v_mfma_f32_16x16x32_bf16 v[108:111], a[208:211], v[92:95], v[108:111]\n"
      "v_add3_u32 v3, v3, v246, v247\n"
      "v_xor_b32 v3, v3, v33\n"
      "v_or_b32 v2, v2, v3\n"
      "v_mfma_f32_16x16x32_bf16 v[116:119], a[212:215], v[92:95], v[116:119]\n"
      "v_cmp_ne_u32 vcc, 0, v2\n"
      "v_mfma_f32_16x16x32_bf16 v[124:127], a[216:219], v[92:95], v[124:127]\n"
      "v_mfma_f32_16x16x32_bf16 v[252:255], a[220:223], v[92:95], v[252:255]\n"
      "global_load_dwordx4 v[148:151], v45, s[12:13] offset:-1024 sc1\n"
      "s_cbranch_vccnz L_shB0_%=\n"
      "L_bhB0_%=:\n"
      "v_mfma_f32_16x16x32_bf16 v[176:179], a[240:243], v[224:227], 0\n"
      "ds_read_b128 v[80:83], v54 offset:0\n"
      "v_mfma_f32_16x16x32_bf16 v[180:183], a[244:247], v[224:227], 0\n"
      "ds_read_b128 v[84:87], v54 offset:64\n"
      "v_mfma_f32_16x16x32_bf16 v[184:187], a[248:251], v[224:227], 0\n"
      "ds_read_b128 v[88:91], v54 offset:128\n"
      "v_mfma_f32_16x16x32_bf16 v[188:191], a[252:255], v[224:227], 0\n"
      "global_load_dwordx4 v[152:155], v45, s[12:13] offset:0 sc1\n"
      "v_mfma_f32_16x16x32_bf16 v[192:195], a[0:3], v[224:227], 0\n"
      "ds_write_b128 v56, v[96:99] offset:0\n"
      "v_mfma_f32_16x16x32_bf16 v[176:179], a[4:7], v[228:231], v[176:179]\n"
      "v_mfma_f32_16x16x32_bf16 v[180:183], a[8:11], v[228:231], v[180:183]\n"
      "v_mfma_f32_16x16x32_bf16 v[184:187], a[12:15], v[228:231], v[184:187]\n"
      "global_load_dwordx4 v[156:159], v45, s[12:13] offset:1024 sc1\n"
      "v_mfma_f32_16x16x32_bf16 v[188:191], a[16:19], v[228:231], v[188:191]\n"
      "ds_write_b128 v56, v[100:103] offset:20480\n"
      "v_mfma_f32_16x16x32_bf16 v[192:195], a[20:23], v[228:231], v[192:195]\n"
      "v_mfma_f32_16x16x32_bf16 v[176:179], a[24:27], v[232:235], v[176:179]\n"
      "v_mfma_f32_16x16x32_bf16 v[180:183], a[28:31], v[232:235], v[180:183]\n"
      "buffer_load_dword v24, v42, s[32:35], s63 offen offset:3072 sc1\n"
      "v_mfma_f32_16x16x32_bf16 v[184:187], a[32:35], v[232:235], v[184:187]\n"
      "buffer_load_dword v25, v42, s[32:35], s63 offen offset:3328 sc1\n"
      "v_add_u32 v45, 108544, v7\n"
      "v_mfma_f32_16x16x32_bf16 v[188:191], a[36:39], v[232:235], v[188:191]\n"
      "global_load_dwordx4 v[172:175], v45, s[12:13] offset:-4096 sc1\n"
      "ds_write_b128 v56, v[104:107] offset:1024\n"
      "v_mfma_f32_16x16x32_bf16 v[192:195], a[40:43], v[232:235], v[192:195]\n"
      "v_mfma_f32_16x16x32_bf16 v[176:179], a[44:47], v[236:239], v[176:179]\n"
      "v_mfma_f32_16x16x32_bf16 v[180:183], a[48:51], v[236:239], v[180:183]\n"
      "ds_write_b128 v56, v[108:111] offset:21504\n"
      "v_mfma_f32_16x16x32_bf16 v[184:187], a[52:55], v[236:239], v[184:187]\n"
      "global_load_dwordx4 v[160:163], v45, s[12:13] offset:-3072 sc1\n"
      "v_mfma_f32_16x16x32_bf16 v[188:191], a[56:59], v[236:239], v[188:191]\n"
      "v_mfma_f32_16x16x32_bf16 v[192:195], a[60:63], v[236:239], v[192:195]\n"
      "ds_write_b128 v56, v[112:115] offset:2048\n"
      "v_mfma_f32_16x16x32_bf16 v[176:179], a[64:67], v[240:243], v[176:179]\n"
      "v_mfma_f32_16x16x32_bf16 v[180:183], a[68:71], v[240:243], v[180:183]\n"
      "global_load_dwordx4 v[164:167], v45, s[12:13] offset:-2048 sc1\n"
      "s_waitcnt vmcnt(11)\n"
      "v_mfma_f32_16x16x32_bf16 v[184:187], a[72:75], v[240:243], v[184:187]\n"
      "ds_write_b128 v56, v[116:119] offset:22528\n"
      "v_add3_u32 v2, v8, v9, s30\n"
      "v_add3_u32 v2, v2, v10, v11\n"
      "v_add3_u32 v2, v2, v12, v13\n"
      "v_add3_u32 v2, v2, v14, v15\n"
      "v_mfma_f32_16x16x32_bf16 v[188:191], a[76:79], v[240:243], v[188:191]\n"
      "v_add3_u32 v2, v2, v16, v17\n"
      "v_add3_u32 v2, v2, v18, v19\n"
      "v_mfma_f32_16x16x32_bf16 v[192:195], a[80:83], v[240:243], v[192:195]\n"
      "v_xor_b32 v2, v2, v34\n"
      "v_add3_u32 v3, v20, v21, s30\n"
      "v_add3_u32 v3, v3, v22, v23\n"
      "v_mfma_f32_16x16x32_bf16 v[176:179], a[84:87], v[244:247], v[176:179]\n"
      "global_load_dwordx4 v[168:171], v45, s[12:13] offset:-1024 sc1\n"
      "ds_write_b128 v56, v[120:123] offset:3072\n"
      "v_add3_u32 v3, v3, v128, v129\n"
      "v_add3_u32 v3, v3, v130, v131\n"
      "v_add3_u32 v3, v3, v132, v133\n"
      "v_add3_u32 v3, v3, v134, v135\n"
      "v_mfma_f32_16x16x32_bf16 v[180:183], a[88:91], v[244:247], v[180:183]\n"
      "v_xor_b32 v3, v3, v35\n"
      "v_or_b32 v2, v2, v3\n"
      "v_mfma_f32_16x16x32_bf16 v[184:187], a[92:95], v[244:247], v[184:187]\n"
      "v_cmp_ne_u32 vcc, 0, v2\n"
      "v_mfma_f32_16x16x32_bf16 v[188:191], a[96:99], v[244:247], v[188:191]\n"
      "ds_write_b128 v56, v[124:127] offset:23552\n"
      "v_mfma_f32_16x16x32_bf16 v[192:195], a[100:103], v[244:247], v[192:195]\n"
      "global_load_dwordx4 v[64:67], v45, s[12:13] offset:0 sc1\n"
      "s_cbranch_vccnz L_shB1_%=\n"
      "L_bhB1_%=:\n"
      "v_mfma_f32_16x16x32_bf16 v[176:179], a[104:107], v[8:11], v[176:179]\n"
      "v_mfma_f32_16x16x32_bf16 v[180:183], a[108:111], v[8:11], v[180:183]\n"
      "ds_write_b128 v56, v[248:251] offset:4096\n"
      "v_mfma_f32_16x16x32_bf16 v[184:187], a[112:115], v[8:11], v[184:187]\n"
      "v_mfma_f32_16x16x32_bf16 v[188:191], a[116:119], v[8:11], v[188:191]\n"
      "global_load_dwordx4 v[68:71], v45, s[12:13] offset:1024 sc1\n"
      "v_mfma_f32_16x16x32_bf16 v[192:195], a[120:123], v[8:11], v[192:195]\n"
      "ds_write_b128 v56, v[252:255] offset:24576\n"
      "v_mfma_f32_16x16x32_bf16 v[176:179], a[124:127], v[12:15], v[176:179]\n"
      "v_mfma_f32_16x16x32_bf16 v[180:183], a[128:131], v[12:15], v[180:183]\n"
      "v_mfma_f32_16x16x32_bf16 v[184:187], a[132:135], v[12:15], v[184:187]\n"
      "buffer_load_dword v26, v42, s[32:35], s63 offen offset:3584 sc1\n"
      "v_mfma_f32_16x16x32_bf16 v[188:191], a[136:139], v[12:15], v[188:191]\n"
      "buffer_load_dword v27, v42, s[32:35], s63 offen offset:3840 sc1\n"
      "v_mfma_f32_16x16x32_bf16 v[192:195], a[140:143], v[12:15], v[192:195]\n"
      "v_mfma_f32_16x16x32_bf16 v[176:179], a[144:147], v[16:19], v[176:179]\n"
      "s_waitcnt lgkmcnt(0)\n"
      "s_barrier\n"
      "v_mfma_f32_16x16x32_bf16 v[180:183], a[148:151], v[16:19], v[180:183]\n"
      "ds_read_b128 v[96:99], v49 offset:0\n"
      "v_add_u32 v56, 0xa000, v56\n"
      "v_mfma_f32_16x16x32_bf16 v[184:187], a[152:155], v[16:19], v[184:187]\n"
      "ds_read_b128 v[100:103], v49 offset:5120\n"
      "v_mfma_f32_16x16x32_bf16 v[188:191], a[156:159], v[16:19], v[188:191]\n"
      "ds_read_b128 v[104:107], v49 offset:10240\n"
      "v_mfma_f32_16x16x32_bf16 v[192:195], a[160:163], v[16:19], v[192:195]\n"
      "ds_read_b128 v[108:111], v49 offset:15360\n"
      "v_mfma_f32_16x16x32_bf16 v[176:179], a[164:167], v[20:23], v[176:179]\n"
      "ds_read_b64_tr_b16 v[72:73], v53 offset:0\n"
      "v_mfma_f32_16x16x32_bf16 v[180:183], a[168:171], v[20:23], v[180:183]\n"
      "ds_read_b64_tr_b16 v[74:75], v53 offset:2048\n"
      "v_mfma_f32_16x16x32_bf16 v[184:187], a[172:175], v[20:23], v[184:187]\n"
      "ds_read_b64_tr_b16 v[76:77], v53 offset:4096\n"
      "v_mfma_f32_16x16x32_bf16 v[188:191], a[176:179], v[20:23], v[188:191]\n"
      "ds_read_b128 v[112:115], v49 offset:1024\n"
      "v_mfma_f32_16x16x32_bf16 v[192:195], a[180:183], v[20:23], v[192:195]\n"
      "ds_read_b128 v[116:119], v49 offset:6144\n"
      "v_mfma_f32_16x16x32_bf16 v[176:179], a[184:187], v[128:131], v[176:179]\n"
      "ds_read_b128 v[120:123], v49 offset:11264\n"
      "v_mfma_f32_16x16x32_bf16 v[180:183], a[188:191], v[128:131], v[180:183]\n"
      "ds_read_b128 v[124:127], v49 offset:16384\n"
      "s_waitcnt vmcnt(8)\n"
      "v_mfma_f32_16x16x32_bf16 v[184:187], a[192:195], v[128:131], v[184:187]\n"
      "v_add3_u32 v2, v136, v137, s30\n"
      "v_add3_u32 v2, v2, v138, v139\n"
      "v_add3_u32 v2, v2, v140, v141\n"
      "s_waitcnt lgkmcnt(4)\n"
      "v_mfma_f32_16x16x32_bf16 v[188:191], a[196:199], v[128:131], v[188:191]\n"
      "v_add3_u32 v2, v2, v142, v143\n"
      "v_add3_u32 v2, v2, v144, v145\n"
      "v_add3_u32 v2, v2, v146, v147\n"
      "v_add_f32 v96, v96, v100\n"
      "v_add_f32 v97, v97, v101\n"
      "v_mfma_f32_16x16x32_bf16 v[192:195], a[200:203], v[128:131], v[192:195]\n"
      "v_xor_b32 v2, v2, v24\n"
      "v_add3_u32 v3, v148, v149, s30\n"
      "v_add3_u32 v3, v3, v150, v151\n"
      "v_add_f32 v98, v98, v102\n"
      "v_add_f32 v99, v99, v103\n"
      "v_mfma_f32_16x16x32_bf16 v[176:179], a[204:207], v[132:135], v[176:179]\n"
      "v_add3_u32 v3, v3, v152, v153\n"
      "v_add3_u32 v3, v3, v154, v155\n"
      "v_add3_u32 v3, v3, v156, v157\n"
      "v_add_f32 v104, v104, v108\n"
      "v_add_f32 v105, v105, v109\n"
      "v_mfma_f32_16x16x32_bf16 v[180:183], a[208:211], v[132:135], v[180:183]\n"
      "v_add3_u32 v3, v3, v158, v159\n"
      "v_xor_b32 v3, v3, v25\n"
      "v_or_b32 v2, v2, v3\n"
      "v_add_f32 v106, v106, v110\n"
      "v_add_f32 v107, v107, v111\n"
      "v_mfma_f32_16x16x32_bf16 v[184:187], a[212:215], v[132:135], v[184:187]\n"
      "v_cmp_ne_u32 vcc, 0, v2\n"
      "v_add_f32 v96, v96, v104\n"
      "v_add_f32 v97, v97, v105\n"
      "v_mfma_f32_16x16x32_bf16 v[188:191], a[216:219], v[132:135], v[188:191]\n"
      "v_add_f32 v98, v98, v106\n"
      "v_add_f32 v99, v99, v107\n"
      "v_mfma_f32_16x16x32_bf16 v[192:195], a[220:223], v[132:135], v[192:195]\n"
      "s_cbranch_vccnz L_shB2_%=\n"
      "L_bhB2_%=:\n"
      "v_cvt_pk_bf16_f32 v96, 0, v96\n"
      "v_mfma_f32_16x16x32_bf16 v[200:203], a[240:243], v[136:139], 0\n"
      "v_cvt_pk_bf16_f32 v97, 0, v97\n"
      "v_cvt_pk_bf16_f32 v98, 0, v98\n"
      "v_mfma_f32_16x16x32_bf16 v[204:207], a[244:247], v[136:139], 0\n"
      "v_cvt_pk_bf16_f32 v99, 0, v99\n"
      "v_mul_f32 v96, v96, v80\n"
      "v_mfma_f32_16x16x32_bf16 v[208:211], a[248:251], v[136:139], 0\n"
      "v_mul_f32 v97, v97, v81\n"
      "v_mul_f32 v98, v98, v82\n"
      "v_mfma_f32_16x16x32_bf16 v[212:215], a[252:255], v[136:139], 0\n"
      "v_mul_f32 v99, v99, v83\n"
      "v_cvt_pk_bf16_f32 v96, 0, v96\n"
      "v_mfma_f32_16x16x32_bf16 v[216:219], a[0:3], v[136:139], 0\n"
      "v_cvt_pk_bf16_f32 v97, 0, v97\n"
      "v_cvt_pk_bf16_f32 v98, 0, v98\n"
      "v_mfma_f32_16x16x32_bf16 v[200:203], a[4:7], v[140:143], v[200:203]\n"
      "v_cvt_pk_bf16_f32 v99, 0, v99\n"
      "v_lshlrev_b32 v100, 16, v72\n"
      "v_mfma_f32_16x16x32_bf16 v[204:207], a[8:11], v[140:143], v[204:207]\n"
      "v_and_b32 v101, 0xffff0000, v72\n"
      "v_lshlrev_b32 v102, 16, v73\n"
      "v_mfma_f32_16x16x32_bf16 v[208:211], a[12:15], v[140:143], v[208:211]\n"
      "v_and_b32 v103, 0xffff0000, v73\n"
      "v_add_f32 v96, v96, v100\n"
      "v_mfma_f32_16x16x32_bf16 v[212:215], a[16:19], v[140:143], v[212:215]\n"
      "v_add_f32 v97, v97, v101\n"
      "v_add_f32 v98, v98, v102\n"
      "v_mfma_f32_16x16x32_bf16 v[216:219], a[20:23], v[140:143], v[216:219]\n"
      "v_add_f32 v99, v99, v103\n"
      "v_cvt_pk_bf16_f32 v72, v96, v97\n"
      "v_mfma_f32_16x16x32_bf16 v[200:203], a[24:27], v[144:147], v[200:203]\n"
      "ds_write_b128 v56, v[176:179] offset:0\n"
      "v_cvt_pk_bf16_f32 v73, v98, v99\n"
      "v_mfma_f32_16x16x32_bf16 v[204:207], a[28:31], v[144:147], v[204:207]\n"
      "global_store_dwordx2 v55, v[72:73], s[28:29] offset:0\n"
      "s_waitcnt lgkmcnt(1)\n"
      "v_mfma_f32_16x16x32_bf16 v[208:211], a[32:35], v[144:147], v[208:211]\n"
      "ds_read_b128 v[96:99], v49 offset:2048\n"
      "v_add_f32 v112, v112, v116\n"
      "v_mfma_f32_16x16x32_bf16 v[212:215], a[36:39], v[144:147], v[212:215]\n"
      "ds_read_b128 v[100:103], v49 offset:7168\n"
      "v_add_f32 v113, v113, v117\n"
      "v_mfma_f32_16x16x32_bf16 v[216:219], a[40:43], v[144:147], v[216:219]\n"
      "ds_read_b128 v[104:107], v49 offset:12288\n"
      "v_add_f32 v114, v114, v118\n"
      "v_mfma_f32_16x16x32_bf16 v[200:203], a[44:47], v[148:151], v[200:203]\n"
      "ds_read_b128 v[108:111], v49 offset:17408\n"
      "v_add_f32 v115, v115, v119\n"
      "v_mfma_f32_16x16x32_bf16 v[204:207], a[48:51], v[148:151], v[204:207]\n"
      "v_add_f32 v120, v120, v124\n"
      "v_add_f32 v121, v121, v125\n"
      "v_mfma_f32_16x16x32_bf16 v[208:211], a[52:55], v[148:151], v[208:211]\n"
      "v_add_f32 v122, v122, v126\n"
      "v_add_f32 v123, v123, v127\n"
      "v_mfma_f32_16x16x32_bf16 v[212:215], a[56:59], v[148:151], v[212:215]\n"
      "v_add_f32 v112, v112, v120\n"
      "v_add_f32 v113, v113, v121\n"
      "v_mfma_f32_16x16x32_bf16 v[216:219], a[60:63], v[148:151], v[216:219]\n"
      "v_add_f32 v114, v114, v122\n"
      "v_add_f32 v115, v115, v123\n"
      "v_mfma_f32_16x16x32_bf16 v[200:203], a[64:67], v[152:155], v[200:203]\n"
      "v_cvt_pk_bf16_f32 v112, 0, v112\n"
      "v_cvt_pk_bf16_f32 v113, 0, v113\n"
      "v_mfma_f32_16x16x32_bf16 v[204:207], a[68:71], v[152:155], v[204:207]\n"
      "s_waitcnt vmcnt(1)\n"
      "v_cvt_pk_bf16_f32 v114, 0, v114\n"
      "v_mfma_f32_16x16x32_bf16 v[208:211], a[72:75], v[152:155], v[208:211]\n"
      "v_add3_u32 v2, v172, v173, s30\n"
      "v_add3_u32 v2, v2, v174, v175\n"
      "v_add3_u32 v2, v2, v160, v161\n"
      "v_cvt_pk_bf16_f32 v115, 0, v115\n"
      "v_mul_f32 v112, v112, v84\n"
      "v_mfma_f32_16x16x32_bf16 v[212:215], a[76:79], v[152:155], v[212:215]\n"
      "v_add3_u32 v2, v2, v162, v163\n"
      "v_add3_u32 v2, v2, v164, v165\n"
      "v_add3_u32 v2, v2, v166, v167\n"
      "v_mul_f32 v113, v113, v85\n"
      "v_mul_f32 v114, v114, v86\n"
      "v_mfma_f32_16x16x32_bf16 v[216:219], a[80:83], v[152:155], v[216:219]\n"
      "v_xor_b32 v2, v2, v26\n"
      "v_add3_u32 v3, v168, v169, s30\n"
      "v_add3_u32 v3, v3, v170, v171\n"
      "v_mul_f32 v115, v115, v87\n"
      "v_cvt_pk_bf16_f32 v112, 0, v112\n"
      "v_mfma_f32_16x16x32_bf16 v[200:203], a[84:87], v[156:159], v[200:203]\n"
      "ds_write_b128 v56, v[180:183] offset:1024\n"
      "v_add3_u32 v3, v3, v64, v65\n"
      "v_add3_u32 v3, v3, v66, v67\n"
      "L_pg6_%=:\n"
      "s_cbranch_execz L_pgr_%=\n"
      "v_add3_u32 v3, v3, v68, v69\n"
      "v_cvt_pk_bf16_f32 v113, 0, v113\n"
      "v_mfma_f32_16x16x32_bf16 v[204:207], a[88:91], v[156:159], v[204:207]\n"
      "v_add3_u32 v3, v3, v70, v71\n"
      "v_xor_b32 v3, v3, v27\n"
      "v_or_b32 v2, v2, v3\n"
      "v_cvt_pk_bf16_f32 v114, 0, v114\n"
      "v_cvt_pk_bf16_f32 v115, 0, v115\n"
      "v_mfma_f32_16x16x32_bf16 v[208:211], a[92:95], v[156:159], v[208:211]\n"
      "v_cmp_ne_u32 vcc, 0, v2\n"
      "v_lshlrev_b32 v116, 16, v74\n"
      "v_and_b32 v117, 0xffff0000, v74\n"
      "v_mfma_f32_16x16x32_bf16 v[212:215], a[96:99], v[156:159], v[212:215]\n"
      "v_lshlrev_b32 v118, 16, v75\n"
      "v_and_b32 v119, 0xffff0000, v75\n"
      "v_mfma_f32_16x16x32_bf16 v[216:219], a[100:103], v[156:159], v[216:219]\n"
      "ds_write_b128 v56, v[184:187] offset:2048\n"
      "s_cbranch_vccnz L_shB3_%=\n"
      "L_bhB3_%=:\n"
      "v_add_f32 v112, v112, v116\n"
      "v_add_f32 v113, v113, v117\n"
      "v_add_f32 v114, v114, v118\n"
      "v_mfma_f32_16x16x32_bf16 v[200:203], a[104:107], v[172:175], v[200:203]\n"
      "v_add_f32 v115, v115, v119\n"
      "v_cvt_pk_bf16_f32 v74, v112, v113\n"
      "v_mfma_f32_16x16x32_bf16 v[204:207], a[108:111], v[172:175], v[204:207]\n"
      "v_cvt_pk_bf16_f32 v75, v114, v115\n"
      "v_mfma_f32_16x16x32_bf16 v[208:211], a[112:115], v[172:175], v[208:211]\n"
      "global_store_dwordx2 v55, v[74:75], s[28:29] offset:32\n"
      "s_waitcnt lgkmcnt(2)\n"
      "v_mfma_f32_16x16x32_bf16 v[212:215], a[116:119], v[172:175], v[212:215]\n"
      "ds_write_b128 v56, v[188:191] offset:3072\n"
      "v_add_f32 v96, v96, v100\n"
      "v_add_f32 v97, v97, v101\n"
      "v_add_f32 v98, v98, v102\n"
      "v_add_f32 v99, v99, v103\n"
      "v_mfma_f32_16x16x32_bf16 v[216:219], a[120:123], v[172:175], v[216:219]\n"
      "v_add_f32 v104, v104, v108\n"
      "v_add_f32 v105, v105, v109\n"
      "v_mfma_f32_16x16x32_bf16 v[200:203], a[124:127], v[160:163], v[200:203]\n"
      "v_add_f32 v106, v106, v110\n"
      "v_add_f32 v107, v107, v111\n"
      "v_mfma_f32_16x16x32_bf16 v[204:207], a[128:131], v[160:163], v[204:207]\n"
      "v_add_f32 v96, v96, v104\n"
      "v_add_f32 v97, v97, v105\n"
      "v_mfma_f32_16x16x32_bf16 v[208:211], a[132:135], v[160:163], v[208:211]\n"
      "ds_write_b128 v56, v[192:195] offset:4096\n"
      "v_add_f32 v98, v98, v106\n"
      "v_add_f32 v99, v99, v107\n"
      "v_cvt_pk_bf16_f32 v96, 0, v96\n"
      "v_cvt_pk_bf16_f32 v97, 0, v97\n"
      "v_mfma_f32_16x16x32_bf16 v[212:215], a[136:139], v[160:163], v[212:215]\n"
      "v_cvt_pk_bf16_f32 v98, 0, v98\n"
      "v_cvt_pk_bf16_f32 v99, 0, v99\n"
      "v_mfma_f32_16x16x32_bf16 v[216:219], a[140:143], v[160:163], v[216:219]\n"
      "v_mul_f32 v96, v96, v88\n"
      "v_mul_f32 v97, v97, v89\n"
      "v_mfma_f32_16x16x32_bf16 v[200:203], a[144:147], v[164:167], v[200:203]\n"
      "v_mul_f32 v98, v98, v90\n"
      "v_mul_f32 v99, v99, v91\n"
      "v_mfma_f32_16x16x32_bf16 v[204:207], a[148:151], v[164:167], v[204:207]\n"
      "v_cvt_pk_bf16_f32 v96, 0, v96\n"
      "v_cvt_pk_bf16_f32 v97, 0, v97\n"
      "v_mfma_f32_16x16x32_bf16 v[208:211], a[152:155], v[164:167], v[208:211]\n"
      "v_cvt_pk_bf16_f32 v98, 0, v98\n"
      "v_cvt_pk_bf16_f32 v99, 0, v99\n"
      "v_mfma_f32_16x16x32_bf16 v[212:215], a[156:159], v[164:167], v[212:215]\n"
      "v_lshlrev_b32 v100, 16, v76\n"
      "v_and_b32 v101, 0xffff0000, v76\n"
      "v_mfma_f32_16x16x32_bf16 v[216:219], a[160:163], v[164:167], v[216:219]\n"
      "v_lshlrev_b32 v102, 16, v77\n"
      "v_and_b32 v103, 0xffff0000, v77\n"
      "v_mfma_f32_16x16x32_bf16 v[200:203], a[164:167], v[168:171], v[200:203]\n"
      "v_add_f32 v96, v96, v100\n"
      "v_add_f32 v97, v97, v101\n"
      "v_mfma_f32_16x16x32_bf16 v[200:203], a[184:187], v[64:67], v[200:203]\n"
      "v_add_f32 v98, v98, v102\n"
      "v_add_f32 v99, v99, v103\n"
      "v_mfma_f32_16x16x32_bf16 v[200:203], a[204:207], v[68:71], v[200:203]\n"
      "v_cvt_pk_bf16_f32 v76, v96, v97\n"
      "v_cvt_pk_bf16_f32 v77, v98, v99\n"
      "v_mfma_f32_16x16x32_bf16 v[204:207], a[168:171], v[168:171], v[204:207]\n"
      "global_store_dwordx2 v55, v[76:77], s[28:29] offset:64\n"
      "v_mfma_f32_16x16x32_bf16 v[204:207], a[188:191], v[64:67], v[204:207]\n"
      "v_mfma_f32_16x16x32_bf16 v[204:207], a[208:211], v[68:71], v[204:207]\n"
      "ds_write_b128 v56, v[200:203] offset:20480\n"
      "v_mfma_f32_16x16x32_bf16 v[208:211], a[172:175], v[168:171], v[208:211]\n"
      "v_mfma_f32_16x16x32_bf16 v[208:211], a[192:195], v[64:67], v[208:211]\n"
      "v_mfma_f32_16x16x32_bf16 v[208:211], a[212:215], v[68:71], v[208:211]\n"
      "ds_write_b128 v56, v[204:207] offset:21504\n"
      "v_mfma_f32_16x16x32_bf16 v[212:215], a[176:179], v[168:171], v[212:215]\n"
      "v_mfma_f32_16x16x32_bf16 v[212:215], a[196:199], v[64:67], v[212:215]\n"
      "v_mfma_f32_16x16x32_bf16 v[212:215], a[216:219], v[68:71], v[212:215]\n"
      "ds_write_b128 v56, v[208:211] offset:22528\n"
      "v_mfma_f32_16x16x32_bf16 v[216:219], a[180:183], v[168:171], v[216:219]\n"
      "v_mfma_f32_16x16x32_bf16 v[216:219], a[200:203], v[64:67], v[216:219]\n"
      "v_mfma_f32_16x16x32_bf16 v[216:219], a[220:223], v[68:71], v[216:219]\n"
      "ds_write_b128 v56, v[212:215] offset:23552\n"
      "v_xor_b32 v53, 64, v53\n"
      "ds_read_b64_tr_b16 v[64:65], v53 offset:0\n"
      "ds_read_b64_tr_b16 v[66:67], v53 offset:2048\n"
      "ds_read_b64_tr_b16 v[68:69], v53 offset:4096\n"
      "v_add_u32 v49, 0xa000, v49\n"
      "v_add_u32 v55, 81920, v55\n"
      "s_nop 0\n"
      "ds_write_b128 v56, v[216:219] offset:24576\n"
      "s_waitcnt lgkmcnt(0)\n"
      "s_barrier\n"
      "ds_read_b128 v[96:99], v49 offset:0\n"
      "ds_read_b128 v[100:103], v49 offset:5120\n"
      "ds_read_b128 v[104:107], v49 offset:10240\n"
      "ds_read_b128 v[108:111], v49 offset:15360\n"
      "ds_read_b128 v[112:115], v49 offset:1024\n"
      "ds_read_b128 v[116:119], v49 offset:6144\n"
      "ds_read_b128 v[120:123], v49 offset:11264\n"
      "ds_read_b128 v[124:127], v49 offset:16384\n"
      "ds_read_b128 v[128:131], v49 offset:2048\n"
      "ds_read_b128 v[132:135], v49 offset:7168\n"
      "ds_read_b128 v[136:139], v49 offset:12288\n"
      "ds_read_b128 v[140:143], v49 offset:17408\n"
      "v_lshlrev_b32 v144, 16, v64\n"
      "v_and_b32 v145, 0xffff0000, v64\n"
      "v_lshlrev_b32 v146, 16, v65\n"
      "v_and_b32 v147, 0xffff0000, v65\n"
      "v_lshlrev_b32 v148, 16, v66\n"
      "v_and_b32 v149, 0xffff0000, v66\n"
      "v_lshlrev_b32 v150, 16, v67\n"
      "v_and_b32 v151, 0xffff0000, v67\n"
      "v_lshlrev_b32 v152, 16, v68\n"
      "v_and_b32 v153, 0xffff0000, v68\n"
      "v_lshlrev_b32 v154, 16, v69\n"
      "v_and_b32 v155, 0xffff0000, v69\n"
      "s_waitcnt lgkmcnt(8)\n"
      "v_pk_add_f32 v[96:97], v[96:97], v[100:101]\n"
      "v_pk_add_f32 v[98:99], v[98:99], v[102:103]\n"
      "v_pk_add_f32 v[104:105], v[104:105], v[108:109]\n"
      "v_pk_add_f32 v[106:107], v[106:107], v[110:111]\n"
      "v_pk_add_f32 v[96:97], v[96:97], v[104:105]\n"
      "v_pk_add_f32 v[98:99], v[98:99], v[106:107]\n"
      "v_cvt_pk_bf16_f32 v96, 0, v96\n"
      "v_cvt_pk_bf16_f32 v97, 0, v97\n"
      "v_cvt_pk_bf16_f32 v98, 0, v98\n"
      "v_cvt_pk_bf16_f32 v99, 0, v99\n"
      "v_pk_mul_f32 v[96:97], v[96:97], v[80:81]\n"
      "v_pk_mul_f32 v[98:99], v[98:99], v[82:83]\n"
      "v_cvt_pk_bf16_f32 v96, 0, v96\n"
      "v_cvt_pk_bf16_f32 v97, 0, v97\n"
      "v_cvt_pk_bf16_f32 v98, 0, v98\n"
      "v_cvt_pk_bf16_f32 v99, 0, v99\n"
      "v_pk_add_f32 v[96:97], v[96:97], v[144:145]\n"
      "v_pk_add_f32 v[98:99], v[98:99], v[146:147]\n"
      "v_cvt_pk_bf16_f32 v96, v96, v97\n"
      "v_cvt_pk_bf16_f32 v97, v98, v99\n"
      "global_store_dwordx2 v55, v[96:97], s[28:29] offset:0\n"
      "s_waitcnt lgkmcnt(4)\n"
      "v_pk_add_f32 v[112:113], v[112:113], v[116:117]\n"
      "v_pk_add_f32 v[114:115], v[114:115], v[118:119]\n"
      "v_pk_add_f32 v[120:121], v[120:121], v[124:125]\n"
      "v_pk_add_f32 v[122:123], v[122:123], v[126:127]\n"
      "v_pk_add_f32 v[112:113], v[112:113], v[120:121]\n"
      "v_pk_add_f32 v[114:115], v[114:115], v[122:123]\n"
      "v_cvt_pk_bf16_f32 v112, 0, v112\n"
      "v_cvt_pk_bf16_f32 v113, 0, v113\n"
      "v_cvt_pk_bf16_f32 v114, 0, v114\n"
      "v_cvt_pk_bf16_f32 v115, 0, v115\n"
      "v_pk_mul_f32 v[112:113], v[112:113], v[84:85]\n"
      "v_pk_mul_f32 v[114:115], v[114:115], v[86:87]\n"
      "v_cvt_pk_bf16_f32 v112, 0, v112\n"
      "v_cvt_pk_bf16_f32 v113, 0, v113\n"
      "v_cvt_pk_bf16_f32 v114, 0, v114\n"
      "v_cvt_pk_bf16_f32 v115, 0, v115\n"
      "v_pk_add_f32 v[112:113], v[112:113], v[148:149]\n"
      "v_pk_add_f32 v[114:115], v[114:115], v[150:151]\n"
      "v_cvt_pk_bf16_f32 v112, v112, v113\n"
      "v_cvt_pk_bf16_f32 v113, v114, v115\n"
      "global_store_dwordx2 v55, v[112:113], s[28:29] offset:32\n"
      "s_waitcnt lgkmcnt(0)\n"
      "v_pk_add_f32 v[128:129], v[128:129], v[132:133]\n"
      "v_pk_add_f32 v[130:131], v[130:131], v[134:135]\n"
      "v_pk_add_f32 v[136:137], v[136:137], v[140:141]\n"
      "v_pk_add_f32 v[138:139], v[138:139], v[142:143]\n"
      "v_pk_add_f32 v[128:129], v[128:129], v[136:137]\n"
      "v_pk_add_f32 v[130:131], v[130:131], v[138:139]\n"
      "v_cvt_pk_bf16_f32 v128, 0, v128\n"
      "v_cvt_pk_bf16_f32 v129, 0, v129\n"
      "v_cvt_pk_bf16_f32 v130, 0, v130\n"
      "v_cvt_pk_bf16_f32 v131, 0, v131\n"
      "v_pk_mul_f32 v[128:129], v[128:129], v[88:89]\n"
      "v_pk_mul_f32 v[130:131], v[130:131], v[90:91]\n"
      "v_cvt_pk_bf16_f32 v128, 0, v128\n"
      "v_cvt_pk_bf16_f32 v129, 0, v129\n"
      "v_cvt_pk_bf16_f32 v130, 0, v130\n"
      "v_cvt_pk_bf16_f32 v131, 0, v131\n"
      "v_pk_add_f32 v[128:129], v[128:129], v[152:153]\n"
      "v_pk_add_f32 v[130:131], v[130:131], v[154:155]\n"
      "v_cvt_pk_bf16_f32 v128, v128, v129\n"
      "v_cvt_pk_bf16_f32 v129, v130, v131\n"
      "global_store_dwordx2 v55, v[128:129], s[28:29] offset:64\n"
      "s_or_b32 s40, s2, s60\n"
      "s_cmp_lg_u32 s40, 0\n"
      "s_cbranch_scc1 L_fin%=\n"
      "v_mov_b32 v8, s30\n"
      "v_mov_b32 v9, s31\n"
      "v_mov_b32 v10, 0\n"
      "s_mov_b64 exec, 1\n"
      "global_store_dwordx2 v10, v[8:9], s[26:27] sc1\n"
      "s_mov_b64 exec, -1\n"
      "L_fin%=:\n"
      "s_endpgm\n"
      "L_psx_%=:\n"
      "s_lshr_b32 s42, s92, 12\n"
      "s_lshr_b32 s43, s93, 12\n"
      "s_mulk_i32 s42, 4224\n"
      "s_mulk_i32 s43, 4224\n"
      "s_add_u32 s42, s42, 0x100000\n"
      "s_add_u32 s43, s43, 0x100000\n"
      "buffer_load_dwordx4 v[128:131], v54, s[32:35], s42 offen sc1\n"
      "s_add_u32 s42, s42, 1081344\n"
      "buffer_load_dwordx4 v[136:139], v54, s[32:35], s43 offen sc1\n"
      "buffer_load_dwordx4 v[132:135], v54, s[32:35], s42 offen sc1\n"
      "buffer_load_dword v140, v237, s[32:35], s70 offen sc1\n"
      "buffer_load_dword v141, v237, s[32:35], s98 offen sc1\n"
      "buffer_load_dword v142, v237, s[32:35], s71 offen sc1\n"
      "s_waitcnt vmcnt(0)\n"
      "v_add3_u32 v143, v128, v129, v130\n"
      "v_add3_u32 v143, v143, v131, v132\n"
      "v_add3_u32 v143, v143, v133, v134\n"
      "v_add3_u32 v143, v143, v135, v136\n"
      "v_add3_u32 v143, v143, v137, v138\n"
      "v_add_u32 v143, v143, v139\n"
      "v_add3_u32 v140, v140, v141, v142\n"
      "s_mul_i32 s47, s30, 3\n"
      "v_add_u32_dpp v143, v143, v143 quad_perm:[1,0,3,2] row_mask:0xf bank_mask:0xf\n"
      "s_nop 1\n"
      "v_add_u32_dpp v143, v143, v143 quad_perm:[2,3,0,1] row_mask:0xf bank_mask:0xf\n"
      "s_nop 1\n"
      "v_add_u32_dpp v143, v143, v143 row_half_mirror row_mask:0xf bank_mask:0xf\n"
      "v_add_u32 v143, v143, v140\n"
      "v_cmp_ne_u32 vcc, s47, v143\n"
      "s_sub_u32 s44, s44, 1\n"
      "s_cbranch_vccz L_pbx_%=\n"
      "s_cmp_lg_u32 s44, 0\n"
      "s_cbranch_scc1 L_psx_%=\n"
      "s_branch L_pbx_%=\n"
      "L_cv_s%=:\n"
      "buffer_load_dwordx2 v[200:201], v4, s[32:35], s80 offen offset:0 sc1\n"
      "buffer_load_dword v250, v61, s[32:35], s85 offen offset:0 sc1\n"
      "buffer_load_dwordx4 v[224:227], v57, s[32:35], s82 offen offset:0 sc1\n"
      "buffer_load_dwordx4 v[228:231], v57, s[32:35], s82 offen offset:1024 sc1\n"
      "buffer_load_dwordx4 v[192:195], v54, s[32:35], s53 offen offset:0 sc1\n"
      "buffer_load_dwordx4 v[196:199], v54, s[32:35], s54 offen offset:0 sc1\n"
      "buffer_load_dword v248, v60, s[32:35], s84 offen offset:0 sc1\n"
      "buffer_load_dword v249, v60, s[32:35], s84 offen offset:2048 sc1\n"
      "buffer_load_dwordx4 v[208:211], v55, s[32:35], 0 offen offset:0 sc1\n"
      "buffer_load_dwordx4 v[212:215], v55, s[32:35], 0 offen offset:1024 sc1\n"
      "buffer_load_dwordx4 v[216:219], v55, s[32:35], 0 offen offset:2048 sc1\n"
      "buffer_load_dwordx4 v[220:223], v55, s[32:35], 0 offen offset:3072 sc1\n"
      "buffer_load_dwordx2 v[204:205], v58, s[32:35], s83 offen offset:0 sc1\n"
      "s_waitcnt vmcnt(0)\n"
      "v_add_u32 v251, v200, v201\n"
      "v_add3_u32 v34, v192, v193, v194\n"
      "v_add3_u32 v34, v34, v195, v196\n"
      "v_add_u32_dpp v251, v251, v251 row_half_mirror row_mask:0xf bank_mask:0xf\n"
      "v_add3_u32 v34, v34, v197, v198\n"
      "v_add3_u32 v251, v251, v34, v199\n"
      "v_add3_u32 v34, v248, v249, v250\n"
      "v_add3_u32 v34, v34, v211, v215\n"
      "v_add_u32_dpp v251, v251, v251 quad_perm:[1,0,3,2] row_mask:0xf bank_mask:0xf\n"
      "v_add3_u32 v34, v34, v219, v223\n"
      "v_add3_u32 v34, v34, v227, v231\n"
      "v_add_u32_dpp v251, v251, v251 quad_perm:[2,3,0,1] row_mask:0xf bank_mask:0xf\n"
      "v_add3_u32 v251, v251, v34, v205\n"
      "v_cmp_ne_u32 vcc, s24, v251\n"
      "s_sub_u32 s44, s44, 1\n"
      "s_cbranch_vccz L_cv_r%=\n"
      "s_cmp_lg_u32 s44, 0\n"
      "s_cbranch_scc1 L_cv_s%=\n"
      "s_branch L_cv_r%=\n"
      "L_cw_s%=:\n"
      "buffer_load_dwordx2 v[252:253], v51, s[32:35], s76 offen offset:0 sc1\n"
      "s_waitcnt vmcnt(0)\n"
      "v_cmp_ne_u64 vcc, s[30:31], v[252:253]\n"
      "s_sub_u32 s44, s44, 1\n"
      "s_cbranch_vccz L_cw_r%=\n"
      "s_cmp_lg_u32 s44, 0\n"
      "s_cbranch_scc1 L_cw_s%=\n"
      "s_branch L_cw_r%=\n"
      "L_cm_s%=:\n"
      "buffer_load_dwordx2 v[254:255], v2, s[32:35], s45 offen sc1\n"
      "s_waitcnt vmcnt(0)\n"
      "v_cmp_ne_u32 vcc, s30, v255\n"
      "s_sub_u32 s44, s44, 1\n"
      "s_cbranch_vccz L_cm_r%=\n"
      "s_cmp_lg_u32 s44, 0\n"
      "s_cbranch_scc1 L_cm_s%=\n"
      "s_branch L_cm_r%=\n"
      "L_sx1_%=:\n"
      "s_mov_b32 s45, m0\n"
      "s_mov_b32 s58, s47\n"
      "s_add_u32 m0, s73, 16384\n"
      "v_add_u32 v248, 81920, v40\n"
      "global_load_lds_dwordx4 v248, s[4:5] offset:0 sc1\n"
      "global_load_lds_dwordx4 v248, s[4:5] offset:1024 sc1\n"
      "global_load_lds_dwordx4 v248, s[4:5] offset:2048 sc1\n"
      "global_load_lds_dwordx4 v248, s[4:5] offset:3072 sc1\n"
      "s_mov_b32 s47, 4\n"
      "buffer_load_dword v3, v5, s[32:35], s47 offen sc1\n"
      "s_waitcnt vmcnt(0)\n"
      "s_mov_b32 m0, s45\n"
      "s_mov_b32 s47, s58\n"
      "ds_read_b128 v[248:251], v40 offset:16384\n"
      "ds_read_b128 v[252:255], v40 offset:17408\n"
      "ds_read_b128 v[36:39], v40 offset:18432\n"
      "ds_read_b128 v[60:63], v40 offset:19456\n"
      "s_waitcnt lgkmcnt(0)\n"
      "v_add3_u32 v3, v3, v248, v249\n"
      "v_add3_u32 v3, v3, v250, v251\n"
      "v_add3_u32 v3, v3, v252, v253\n"
      "v_add3_u32 v3, v3, v254, v255\n"
      "v_add3_u32 v3, v3, v36, v37\n"
      "v_add3_u32 v3, v3, v38, v39\n"
      "v_add3_u32 v3, v3, v60, v61\n"
      "v_add3_u32 v3, v3, v62, v63\n"
      "s_nop 1\n"
      "v_add_u32_dpp v3, v3, v3 quad_perm:[1,0,3,2] row_mask:0xf bank_mask:0xf\n"
      "s_nop 1\n"
      "v_add_u32_dpp v3, v3, v3 quad_perm:[2,3,0,1] row_mask:0xf bank_mask:0xf\n"
      "s_nop 1\n"
      "v_add_u32_dpp v3, v3, v3 row_half_mirror row_mask:0xf bank_mask:0xf\n"
      "v_cmp_ne_u32 vcc, s71, v3\n"
      "s_sub_u32 s44, s44, 1\n"
      "s_cbranch_vccz L_bx1_%=\n"
      "s_cmp_lg_u32 s44, 0\n"
      "s_cbranch_scc1 L_sx1_%=\n"
      "s_branch L_bx1_%=\n"
      "L_sx2_%=:\n"
      "s_mov_b32 s45, m0\n"
      "s_mov_b32 s58, s47\n"
      "s_add_u32 m0, s73, 32768\n"
      "v_add_u32 v248, 16384, v40\n"
      "global_load_lds_dwordx4 v248, s[4:5] offset:0 sc1\n"
      "global_load_lds_dwordx4 v248, s[4:5] offset:1024 sc1\n"
      "global_load_lds_dwordx4 v248, s[4:5] offset:2048 sc1\n"
      "global_load_lds_dwordx4 v248, s[4:5] offset:3072 sc1\n"
      "s_mov_b32 s47, 32768\n"
      "buffer_load_dword v48, v5, s[32:35], s47 offen sc1\n"
      "s_waitcnt vmcnt(0)\n"
      "s_mov_b32 m0, s45\n"
      "s_mov_b32 s47, s58\n"
      "ds_read_b128 v[248:251], v40 offset:32768\n"
      "ds_read_b128 v[252:255], v40 offset:33792\n"
      "ds_read_b128 v[36:39], v40 offset:34816\n"
      "ds_read_b128 v[60:63], v40 offset:35840\n"
      "s_waitcnt lgkmcnt(0)\n"
      "v_add3_u32 v48, v48, v248, v249\n"
      "v_add3_u32 v48, v48, v250, v251\n"
      "v_add3_u32 v48, v48, v252, v253\n"
      "v_add3_u32 v48, v48, v254, v255\n"
      "v_add3_u32 v48, v48, v36, v37\n"
      "v_add3_u32 v48, v48, v38, v39\n"
      "v_add3_u32 v48, v48, v60, v61\n"
      "v_add3_u32 v48, v48, v62, v63\n"
      "s_nop 1\n"
      "v_add_u32_dpp v48, v48, v48 quad_perm:[1,0,3,2] row_mask:0xf bank_mask:0xf\n"
      "s_nop 1\n"
      "v_add_u32_dpp v48, v48, v48 quad_perm:[2,3,0,1] row_mask:0xf bank_mask:0xf\n"
      "s_nop 1\n"
      "v_add_u32_dpp v48, v48, v48 row_half_mirror row_mask:0xf bank_mask:0xf\n"
      "v_cmp_ne_u32 vcc, s71, v48\n"
      "s_sub_u32 s44, s44, 1\n"
      "s_cbranch_vccz L_bx2_%=\n"
      "s_cmp_lg_u32 s44, 0\n"
      "s_cbranch_scc1 L_sx2_%=\n"
      "s_branch L_bx2_%=\n"
      "L_sx3_%=:\n"
      "s_mov_b32 s45, m0\n"
      "s_mov_b32 s58, s47\n"
      "s_add_u32 m0, s73, 49152\n"
      "v_add_u32 v248, 32768, v40\n"
      "global_load_lds_dwordx4 v248, s[4:5] offset:0 sc1\n"
      "global_load_lds_dwordx4 v248, s[4:5] offset:1024 sc1\n"
      "global_load_lds_dwordx4 v248, s[4:5] offset:2048 sc1\n"
      "global_load_lds_dwordx4 v248, s[4:5] offset:3072 sc1\n"
      "s_mov_b32 s47, 65536\n"
      "buffer_load_dword v54, v5, s[32:35], s47 offen sc1\n"
      "s_waitcnt vmcnt(0)\n"
      "s_mov_b32 m0, s45\n"
      "s_mov_b32 s47, s58\n"
      "ds_read_b128 v[248:251], v40 offset:49152\n"
      "ds_read_b128 v[252:255], v40 offset:50176\n"
      "ds_read_b128 v[36:39], v40 offset:51200\n"
      "ds_read_b128 v[60:63], v40 offset:52224\n"
      "s_waitcnt lgkmcnt(0)\n"
      "v_add3_u32 v54, v54, v248, v249\n"
      "v_add3_u32 v54, v54, v250, v251\n"
      "v_add3_u32 v54, v54, v252, v253\n"
      "v_add3_u32 v54, v54, v254, v255\n"
      "v_add3_u32 v54, v54, v36, v37\n"
      "v_add3_u32 v54, v54, v38, v39\n"
      "v_add3_u32 v54, v54, v60, v61\n"
      "v_add3_u32 v54, v54, v62, v63\n"
      "s_nop 1\n"
      "v_add_u32_dpp v54, v54, v54 quad_perm:[1,0,3,2] row_mask:0xf bank_mask:0xf\n"
      "s_nop 1\n"
      "v_add_u32_dpp v54, v54, v54 quad_perm:[2,3,0,1] row_mask:0xf bank_mask:0xf\n"
      "s_nop 1\n"
      "v_add_u32_dpp v54, v54, v54 row_half_mirror row_mask:0xf bank_mask:0xf\n"
      "v_cmp_ne_u32 vcc, s71, v54\n"
      "s_sub_u32 s44, s44, 1\n"
      "s_cbranch_vccz L_bx3_%=\n"
      "s_cmp_lg_u32 s44, 0\n"
      "s_cbranch_scc1 L_sx3_%=\n"
      "s_branch L_bx3_%=\n"
      "L_sx4_%=:\n"
      "s_mov_b32 s45, m0\n"
      "s_mov_b32 s58, s47\n"
      "s_add_u32 m0, s73, 0\n"
      "v_add_u32 v248, 49152, v40\n"
      "global_load_lds_dwordx4 v248, s[4:5] offset:0 sc1\n"
      "global_load_lds_dwordx4 v248, s[4:5] offset:1024 sc1\n"
      "global_load_lds_dwordx4 v248, s[4:5] offset:2048 sc1\n"
      "global_load_lds_dwordx4 v248, s[4:5] offset:3072 sc1\n"
      "s_mov_b32 s47, 98304\n"
      "buffer_load_dword v56, v5, s[32:35], s47 offen sc1\n"
      "s_waitcnt vmcnt(0)\n"
      "s_mov_b32 m0, s45\n"
      "s_mov_b32 s47, s58\n"
      "ds_read_b128 v[248:251], v40 offset:0\n"
      "ds_read_b128 v[252:255], v40 offset:1024\n"
      "ds_read_b128 v[36:39], v40 offset:2048\n"
      "ds_read_b128 v[60:63], v40 offset:3072\n"
      "s_waitcnt lgkmcnt(0)\n"
      "v_add3_u32 v56, v56, v248, v249\n"
      "v_add3_u32 v56, v56, v250, v251\n"
      "v_add3_u32 v56, v56, v252, v253\n"
      "v_add3_u32 v56, v56, v254, v255\n"
      "v_add3_u32 v56, v56, v36, v37\n"
      "v_add3_u32 v56, v56, v38, v39\n"
      "v_add3_u32 v56, v56, v60, v61\n"
      "v_add3_u32 v56, v56, v62, v63\n"
      "s_nop 1\n"
      "v_add_u32_dpp v56, v56, v56 quad_perm:[1,0,3,2] row_mask:0xf bank_mask:0xf\n"
      "s_nop 1\n"
      "v_add_u32_dpp v56, v56, v56 quad_perm:[2,3,0,1] row_mask:0xf bank_mask:0xf\n"
      "s_nop 1\n"
      "v_add_u32_dpp v56, v56, v56 row_half_mirror row_mask:0xf bank_mask:0xf\n"
      "v_cmp_ne_u32 vcc, s71, v56\n"
      "s_sub_u32 s44, s44, 1\n"
      "s_cbranch_vccz L_bx4_%=\n"
      "s_cmp_lg_u32 s44, 0\n"
      "s_cbranch_scc1 L_sx4_%=\n"
      "s_branch L_bx4_%=\n"
      "L_sx5_%=:\n"
      "s_mov_b32 s45, m0\n"
      "s_mov_b32 s58, s47\n"
      "s_add_u32 m0, s73, 16384\n"
      "v_add_u32 v248, 65536, v40\n"
      "global_load_lds_dwordx4 v248, s[4:5] offset:0 sc1\n"
      "global_load_lds_dwordx4 v248, s[4:5] offset:1024 sc1\n"
      "global_load_lds_dwordx4 v248, s[4:5] offset:2048 sc1\n"
      "global_load_lds_dwordx4 v248, s[4:5] offset:3072 sc1\n"
      "s_mov_b32 s47, 131072\n"
      "buffer_load_dword v2, v5, s[32:35], s47 offen sc1\n"
      "s_waitcnt vmcnt(0)\n"
      "s_mov_b32 m0, s45\n"
      "s_mov_b32 s47, s58\n"
      "ds_read_b128 v[248:251], v40 offset:16384\n"
      "ds_read_b128 v[252:255], v40 offset:17408\n"
      "ds_read_b128 v[36:39], v40 offset:18432\n"
      "ds_read_b128 v[60:63], v40 offset:19456\n"
      "s_waitcnt lgkmcnt(0)\n"
      "v_add3_u32 v2, v2, v248, v249\n"
      "v_add3_u32 v2, v2, v250, v251\n"
      "v_add3_u32 v2, v2, v252, v253\n"
      "v_add3_u32 v2, v2, v254, v255\n"
      "v_add3_u32 v2, v2, v36, v37\n"
      "v_add3_u32 v2, v2, v38, v39\n"
      "v_add3_u32 v2, v2, v60, v61\n"
      "v_add3_u32 v2, v2, v62, v63\n"
      "s_nop 1\n"
      "v_add_u32_dpp v2, v2, v2 quad_perm:[1,0,3,2] row_mask:0xf bank_mask:0xf\n"
      "s_nop 1\n"
      "v_add_u32_dpp v2, v2, v2 quad_perm:[2,3,0,1] row_mask:0xf bank_mask:0xf\n"
      "s_nop 1\n"
      "v_add_u32_dpp v2, v2, v2 row_half_mirror row_mask:0xf bank_mask:0xf\n"
      "v_cmp_ne_u32 vcc, s71, v2\n"
      "s_sub_u32 s44, s44, 1\n"
      "s_cbranch_vccz L_bx5_%=\n"
      "s_cmp_lg_u32 s44, 0\n"
      "s_cbranch_scc1 L_sx5_%=\n"
      "s_branch L_bx5_%=\n"
      "L_sx6_%=:\n"
      "s_mov_b32 s45, m0\n"
      "s_mov_b32 s58, s47\n"
      "s_add_u32 m0, s73, 32768\n"
      "v_add_u32 v248, 98304, v40\n"
      "global_load_lds_dwordx4 v248, s[4:5] offset:0 sc1\n"
      "global_load_lds_dwordx4 v248, s[4:5] offset:1024 sc1\n"
      "global_load_lds_dwordx4 v248, s[4:5] offset:2048 sc1\n"
      "global_load_lds_dwordx4 v248, s[4:5] offset:3072 sc1\n"
      "s_mov_b32 s47, 32772\n"
      "buffer_load_dword v49, v5, s[32:35], s47 offen sc1\n"
      "s_waitcnt vmcnt(0)\n"
      "s_mov_b32 m0, s45\n"
      "s_mov_b32 s47, s58\n"
      "ds_read_b128 v[248:251], v40 offset:32768\n"
      "ds_read_b128 v[252:255], v40 offset:33792\n"
      "ds_read_b128 v[36:39], v40 offset:34816\n"
      "ds_read_b128 v[60:63], v40 offset:35840\n"
      "s_waitcnt lgkmcnt(0)\n"
      "v_add3_u32 v49, v49, v248, v249\n"
      "v_add3_u32 v49, v49, v250, v251\n"
      "v_add3_u32 v49, v49, v252, v253\n"
      "v_add3_u32 v49, v49, v254, v255\n"
      "v_add3_u32 v49, v49, v36, v37\n"
      "v_add3_u32 v49, v49, v38, v39\n"
      "v_add3_u32 v49, v49, v60, v61\n"
      "v_add3_u32 v49, v49, v62, v63\n"
      "s_nop 1\n"
      "v_add_u32_dpp v49, v49, v49 quad_perm:[1,0,3,2] row_mask:0xf bank_mask:0xf\n"
      "s_nop 1\n"
      "v_add_u32_dpp v49, v49, v49 quad_perm:[2,3,0,1] row_mask:0xf bank_mask:0xf\n"
      "s_nop 1\n"
      "v_add_u32_dpp v49, v49, v49 row_half_mirror row_mask:0xf bank_mask:0xf\n"
      "v_cmp_ne_u32 vcc, s71, v49\n"
      "s_sub_u32 s44, s44, 1\n"
      "s_cbranch_vccz L_bx6_%=\n"
      "s_cmp_lg_u32 s44, 0\n"
      "s_cbranch_scc1 L_sx6_%=\n"
      "s_branch L_bx6_%=\n"
      "L_sx7_%=:\n"
      "s_mov_b32 s45, m0\n"
      "s_mov_b32 s58, s47\n"
      "s_add_u32 m0, s73, 49152\n"
      "v_add_u32 v248, 114688, v40\n"
      "global_load_lds_dwordx4 v248, s[4:5] offset:0 sc1\n"
      "global_load_lds_dwordx4 v248, s[4:5] offset:1024 sc1\n"
      "global_load_lds_dwordx4 v248, s[4:5] offset:2048 sc1\n"
      "global_load_lds_dwordx4 v248, s[4:5] offset:3072 sc1\n"
      "s_mov_b32 s47, 65540\n"
      "buffer_load_dword v55, v5, s[32:35], s47 offen sc1\n"
      "s_waitcnt vmcnt(0)\n"
      "s_mov_b32 m0, s45\n"
      "s_mov_b32 s47, s58\n"
      "ds_read_b128 v[248:251], v40 offset:49152\n"
      "ds_read_b128 v[252:255], v40 offset:50176\n"
      "ds_read_b128 v[36:39], v40 offset:51200\n"
      "ds_read_b128 v[60:63], v40 offset:52224\n"
      "s_waitcnt lgkmcnt(0)\n"
      "v_add3_u32 v55, v55, v248, v249\n"
      "v_add3_u32 v55, v55, v250, v251\n"
      "v_add3_u32 v55, v55, v252, v253\n"
      "v_add3_u32 v55, v55, v254, v255\n"
      "v_add3_u32 v55, v55, v36, v37\n"
      "v_add3_u32 v55, v55, v38, v39\n"
      "v_add3_u32 v55, v55, v60, v61\n"
      "v_add3_u32 v55, v55, v62, v63\n"
      "s_nop 1\n"
      "v_add_u32_dpp v55, v55, v55 quad_perm:[1,0,3,2] row_mask:0xf bank_mask:0xf\n"
      "s_nop 1\n"
      "v_add_u32_dpp v55, v55, v55 quad_perm:[2,3,0,1] row_mask:0xf bank_mask:0xf\n"
      "s_nop 1\n"
      "v_add_u32_dpp v55, v55, v55 row_half_mirror row_mask:0xf bank_mask:0xf\n"
      "v_cmp_ne_u32 vcc, s71, v55\n"
      "s_sub_u32 s44, s44, 1\n"
      "s_cbranch_vccz L_bx7_%=\n"
      "s_cmp_lg_u32 s44, 0\n"
      "s_cbranch_scc1 L_sx7_%=\n"
      "s_branch L_bx7_%=\n"
      "L_sx8_%=:\n"
      "s_mov_b32 s45, m0\n"
      "s_mov_b32 s58, s47\n"
      "s_add_u32 m0, s73, 0\n"
      "v_add_u32 v248, 131072, v40\n"
      "global_load_lds_dwordx4 v248, s[4:5] offset:0 sc1\n"
      "global_load_lds_dwordx4 v248, s[4:5] offset:1024 sc1\n"
      "global_load_lds_dwordx4 v248, s[4:5] offset:2048 sc1\n"
      "global_load_lds_dwordx4 v248, s[4:5] offset:3072 sc1\n"
      "s_mov_b32 s47, 98308\n"
      "buffer_load_dword v57, v5, s[32:35], s47 offen sc1\n"
      "s_waitcnt vmcnt(0)\n"
      "s_mov_b32 m0, s45\n"
      "s_mov_b32 s47, s58\n"
      "ds_read_b128 v[248:251], v40 offset:0\n"
      "ds_read_b128 v[252:255], v40 offset:1024\n"
      "ds_read_b128 v[36:39], v40 offset:2048\n"
      "ds_read_b128 v[60:63], v40 offset:3072\n"
      "s_waitcnt lgkmcnt(0)\n"
      "v_add3_u32 v57, v57, v248, v249\n"
      "v_add3_u32 v57, v57, v250, v251\n"
      "v_add3_u32 v57, v57, v252, v253\n"
      "v_add3_u32 v57, v57, v254, v255\n"
      "v_add3_u32 v57, v57, v36, v37\n"
      "v_add3_u32 v57, v57, v38, v39\n"
      "v_add3_u32 v57, v57, v60, v61\n"
      "v_add3_u32 v57, v57, v62, v63\n"
      "s_nop 1\n"
      "v_add_u32_dpp v57, v57, v57 quad_perm:[1,0,3,2] row_mask:0xf bank_mask:0xf\n"
      "s_nop 1\n"
      "v_add_u32_dpp v57, v57, v57 quad_perm:[2,3,0,1] row_mask:0xf bank_mask:0xf\n"
      "s_nop 1\n"
      "v_add_u32_dpp v57, v57, v57 row_half_mirror row_mask:0xf bank_mask:0xf\n"
      "v_cmp_ne_u32 vcc, s71, v57\n"
      "s_sub_u32 s44, s44, 1\n"
      "s_cbranch_vccz L_bx8_%=\n"
      "s_cmp_lg_u32 s44, 0\n"
      "s_cbranch_scc1 L_sx8_%=\n"
      "s_branch L_bx8_%=\n"
      "L_sx9_%=:\n"
      "s_mov_b32 s45, m0\n"
      "s_mov_b32 s58, s47\n"
      "s_add_u32 m0, s73, 16384\n"
      "v_add_u32 v248, 147456, v40\n"
      "global_load_lds_dwordx4 v248, s[4:5] offset:0 sc1\n"
      "global_load_lds_dwordx4 v248, s[4:5] offset:1024 sc1\n"
      "global_load_lds_dwordx4 v248, s[4:5] offset:2048 sc1\n"
      "global_load_lds_dwordx4 v248, s[4:5] offset:3072 sc1\n"
      "s_mov_b32 s47, 131076\n"
      "buffer_load_dword v3, v5, s[32:35], s47 offen sc1\n"
      "s_waitcnt vmcnt(0)\n"
      "s_mov_b32 m0, s45\n"
      "s_mov_b32 s47, s58\n"
      "ds_read_b128 v[248:251], v40 offset:16384\n"
      "ds_read_b128 v[252:255], v40 offset:17408\n"
      "ds_read_b128 v[36:39], v40 offset:18432\n"
      "ds_read_b128 v[60:63], v40 offset:19456\n"
      "s_waitcnt lgkmcnt(0)\n"
      "v_add3_u32 v3, v3, v248, v249\n"
      "v_add3_u32 v3, v3, v250, v251\n"
      "v_add3_u32 v3, v3, v252, v253\n"
      "v_add3_u32 v3, v3, v254, v255\n"
      "v_add3_u32 v3, v3, v36, v37\n"
      "v_add3_u32 v3, v3, v38, v39\n"
      "v_add3_u32 v3, v3, v60, v61\n"
      "v_add3_u32 v3, v3, v62, v63\n"
      "s_nop 1\n"
      "v_add_u32_dpp v3, v3, v3 quad_perm:[1,0,3,2] row_mask:0xf bank_mask:0xf\n"
      "s_nop 1\n"
      "v_add_u32_dpp v3, v3, v3 quad_perm:[2,3,0,1] row_mask:0xf bank_mask:0xf\n"
      "s_nop 1\n"
      "v_add_u32_dpp v3, v3, v3 row_half_mirror row_mask:0xf bank_mask:0xf\n"
      "v_cmp_ne_u32 vcc, s71, v3\n"
      "s_sub_u32 s44, s44, 1\n"
      "s_cbranch_vccz L_bx9_%=\n"
      "s_cmp_lg_u32 s44, 0\n"
      "s_cbranch_scc1 L_sx9_%=\n"
      "s_branch L_bx9_%=\n"
      "L_sx0_%=:\n"
      "s_mov_b32 s45, m0\n"
      "s_mov_b32 s58, s47\n"
      "s_add_u32 m0, s73, 0\n"
      "v_add_u32 v248, 0, v40\n"
      "global_load_lds_dwordx4 v248, s[4:5] offset:0 sc1\n"
      "global_load_lds_dwordx4 v248, s[4:5] offset:1024 sc1\n"
      "global_load_lds_dwordx4 v248, s[4:5] offset:2048 sc1\n"
      "global_load_lds_dwordx4 v248, s[4:5] offset:3072 sc1\n"
      "s_mov_b32 s47, 0\n"
      "buffer_load_dword v2, v5, s[32:35], s47 offen sc1\n"
      "s_waitcnt vmcnt(0)\n"
      "s_mov_b32 m0, s45\n"
      "s_mov_b32 s47, s58\n"
      "ds_read_b128 v[248:251], v40 offset:0\n"
      "ds_read_b128 v[252:255], v40 offset:1024\n"
      "ds_read_b128 v[36:39], v40 offset:2048\n"
      "ds_read_b128 v[60:63], v40 offset:3072\n"
      "s_waitcnt lgkmcnt(0)\n"
      "v_add3_u32 v2, v2, v248, v249\n"
      "v_add3_u32 v2, v2, v250, v251\n"
      "v_add3_u32 v2, v2, v252, v253\n"
      "v_add3_u32 v2, v2, v254, v255\n"
      "v_add3_u32 v2, v2, v36, v37\n"
      "v_add3_u32 v2, v2, v38, v39\n"
      "v_add3_u32 v2, v2, v60, v61\n"
      "v_add3_u32 v2, v2, v62, v63\n"
      "s_nop 1\n"
      "v_add_u32_dpp v2, v2, v2 quad_perm:[1,0,3,2] row_mask:0xf bank_mask:0xf\n"
      "s_nop 1\n"
      "v_add_u32_dpp v2, v2, v2 quad_perm:[2,3,0,1] row_mask:0xf bank_mask:0xf\n"
      "s_nop 1\n"
      "v_add_u32_dpp v2, v2, v2 row_half_mirror row_mask:0xf bank_mask:0xf\n"
      "v_cmp_ne_u32 vcc, s71, v2\n"
      "s_sub_u32 s44, s44, 1\n"
      "s_cbranch_vccz L_bx0_%=\n"
      "s_cmp_lg_u32 s44, 0\n"
      "s_cbranch_scc1 L_sx0_%=\n"
      "s_branch L_bx0_%=\n"
      "L_sfd_%=:\n"
      "global_load_dwordx2 v[58:59], v42, s[26:27] sc1\n"
      "buffer_load_dwordx2 v[46:47], v51, s[32:35], s75 offen sc1\n"
      "s_waitcnt vmcnt(0)\n"
      "v_cmp_ne_u64 s[52:53], s[30:31], v[46:47]\n"
      "v_cmp_ne_u64 vcc, s[30:31], v[58:59]\n"
      "s_or_b64 vcc, vcc, s[52:53]\n"
      "s_sub_u32 s44, s44, 1\n"
      "s_cbranch_vccz L_bfd_%=\n"
      "s_cmp_lg_u32 s44, 0\n"
      "s_cbranch_scc1 L_sfd_%=\n"
      "s_branch L_bfd_%=\n"
      "L_sxp_%=:\n"
      "s_mov_b32 s45, m0\n"
      "s_mov_b32 s58, s47\n"
      "s_mov_b32 m0, s94\n"
      "s_nop 0\n"
      "buffer_load_dwordx4 v7, s[32:35], s67 offen offset:0 sc1 lds\n"
      "buffer_load_dwordx4 v7, s[32:35], s67 offen offset:1024 sc1 lds\n"
      "buffer_load_dwordx4 v7, s[32:35], s67 offen offset:2048 sc1 lds\n"
      "buffer_load_dword v32, v31, s[32:35], s68 offen sc1\n"
      "s_waitcnt vmcnt(0)\n"
      "s_mov_b32 m0, s45\n"
      "s_mov_b32 s47, s58\n"
      "ds_read_b128 v[248:251], v50 offset:0\n"
      "ds_read_b128 v[252:255], v50 offset:1024\n"
      "ds_read_b128 v[36:39], v50 offset:2048\n"
      "s_waitcnt lgkmcnt(0)\n"
      "v_add3_u32 v32, v32, v248, v249\n"
      "v_add3_u32 v32, v32, v250, v251\n"
      "v_add3_u32 v32, v32, v252, v253\n"
      "v_add3_u32 v32, v32, v254, v255\n"
      "v_add3_u32 v32, v32, v36, v37\n"
      "v_add3_u32 v32, v32, v38, v39\n"
      "s_nop 1\n"
      "v_add_u32_dpp v32, v32, v32 quad_perm:[1,0,3,2] row_mask:0xf bank_mask:0xf\n"
      "s_nop 1\n"
      "v_add_u32_dpp v32, v32, v32 quad_perm:[2,3,0,1] row_mask:0xf bank_mask:0xf\n"
      "s_nop 1\n"
      "v_add_u32_dpp v32, v32, v32 row_half_mirror row_mask:0xf bank_mask:0xf\n"
      "v_cmp_ne_u32 vcc, s72, v32\n"
      "s_sub_u32 s44, s44, 1\n"
      "s_cbranch_vccz L_bxp_%=\n"
      "s_cmp_lg_u32 s44, 0\n"
      "s_cbranch_scc1 L_sxp_%=\n"
      "s_branch L_bxp_%=\n"
      "L_sg_%=:\n"
      "buffer_load_dwordx4 v[8:11], v50, s[32:35], s93 offen offset:0 sc1\n"
      "buffer_load_dwordx4 v[12:15], v50, s[32:35], s93 offen offset:1024 sc1\n"
      "buffer_load_dwordx4 v[16:19], v50, s[32:35], s93 offen offset:2048 sc1\n"
      "buffer_load_dwordx4 v[20:23], v50, s[32:35], s93 offen offset:3072 sc1\n"
      "s_waitcnt vmcnt(0)\n"
      "v_add3_u32 v10, v10, v14, v18\n"
      "v_add_u32 v10, v10, v22\n"
      "v_cmp_ne_u32 vcc, s71, v10\n"
      "s_sub_u32 s44, s44, 1\n"
      "s_cbranch_vccz L_bg_%=\n"
      "s_cmp_lg_u32 s44, 0\n"
      "s_cbranch_scc1 L_sg_%=\n"
      "s_branch L_bg_%=\n"
      "L_shA0_%=:\n"
      "v_add_u32 v2, 4096, v7\n"
      "global_load_dwordx4 v[8:11], v2, s[10:11] offset:-4096 sc1\n"
      "global_load_dwordx4 v[16:19], v2, s[10:11] offset:-3072 sc1\n"
      "global_load_dwordx4 v[128:131], v2, s[10:11] offset:-2048 sc1\n"
      "v_add_u32 v2, 4096, v7\n"
      "global_load_dwordx4 v[12:15], v2, s[12:13] offset:-4096 sc1\n"
      "global_load_dwordx4 v[20:23], v2, s[12:13] offset:-3072 sc1\n"
      "global_load_dwordx4 v[132:135], v2, s[12:13] offset:-2048 sc1\n"
      "buffer_load_dword v36, v42, s[32:35], s63 offen offset:0 sc1\n"
      "buffer_load_dword v37, v42, s[32:35], s63 offen offset:256 sc1\n"
      "s_waitcnt vmcnt(0)\n"
      "v_add3_u32 v2, v8, v9, s30\n"
      "v_add3_u32 v2, v2, v10, v11\n"
      "v_add3_u32 v2, v2, v16, v17\n"
      "v_add3_u32 v2, v2, v18, v19\n"
      "v_add3_u32 v2, v2, v128, v129\n"
      "v_add3_u32 v2, v2, v130, v131\n"
      "v_xor_b32 v2, v2, v36\n"
      "v_add3_u32 v3, v12, v13, s30\n"
      "v_add3_u32 v3, v3, v14, v15\n"
      "v_add3_u32 v3, v3, v20, v21\n"
      "v_add3_u32 v3, v3, v22, v23\n"
      "v_add3_u32 v3, v3, v132, v133\n"
      "v_add3_u32 v3, v3, v134, v135\n"
      "v_xor_b32 v3, v3, v37\n"
      "v_or_b32 v2, v2, v3\n"
      "v_cmp_ne_u32 vcc, 0, v2\n"
      "s_sub_u32 s44, s44, 1\n"
      "s_cbranch_vccz L_bhA0_%=\n"
      "s_cmp_lg_u32 s44, 0\n"
      "s_cbranch_scc1 L_shA0_%=\n"
      "s_branch L_bhA0_%=\n"
      "L_shA1_%=:\n"
      "v_add_u32 v2, 4096, v7\n"
      "global_load_dwordx4 v[136:139], v2, s[10:11] offset:-1024 sc1\n"
      "global_load_dwordx4 v[144:147], v2, s[10:11] offset:0 sc1\n"
      "global_load_dwordx4 v[152:155], v2, s[10:11] offset:1024 sc1\n"
      "v_add_u32 v2, 4096, v7\n"
      "global_load_dwordx4 v[140:143], v2, s[12:13] offset:-1024 sc1\n"
      "global_load_dwordx4 v[148:151], v2, s[12:13] offset:0 sc1\n"
      "global_load_dwordx4 v[156:159], v2, s[12:13] offset:1024 sc1\n"
      "buffer_load_dword v38, v42, s[32:35], s63 offen offset:512 sc1\n"
      "buffer_load_dword v39, v42, s[32:35], s63 offen offset:768 sc1\n"
      "s_waitcnt vmcnt(0)\n"
      "v_add3_u32 v2, v136, v137, s30\n"
      "v_add3_u32 v2, v2, v138, v139\n"
      "v_add3_u32 v2, v2, v144, v145\n"
      "v_add3_u32 v2, v2, v146, v147\n"
      "v_add3_u32 v2, v2, v152, v153\n"
      "v_add3_u32 v2, v2, v154, v155\n"
      "v_xor_b32 v2, v2, v38\n"
      "v_add3_u32 v3, v140, v141, s30\n"
      "v_add3_u32 v3, v3, v142, v143\n"
      "v_add3_u32 v3, v3, v148, v149\n"
      "v_add3_u32 v3, v3, v150, v151\n"
      "v_add3_u32 v3, v3, v156, v157\n"
      "v_add3_u32 v3, v3, v158, v159\n"
      "v_xor_b32 v3, v3, v39\n"
      "v_or_b32 v2, v2, v3\n"
      "v_cmp_ne_u32 vcc, 0, v2\n"
      "s_sub_u32 s44, s44, 1\n"
      "s_cbranch_vccz L_bhA1_%=\n"
      "s_cmp_lg_u32 s44, 0\n"
      "s_cbranch_scc1 L_shA1_%=\n"
      "s_branch L_bhA1_%=\n"
      "L_shA2_%=:\n"
      "v_add_u32 v2, 10240, v7\n"
      "global_load_dwordx4 v[172:175], v2, s[10:11] offset:-4096 sc1\n"
      "global_load_dwordx4 v[164:167], v2, s[10:11] offset:-3072 sc1\n"
      "global_load_dwordx4 v[64:67], v2, s[10:11] offset:-2048 sc1\n"
      "v_add_u32 v2, 10240, v7\n"
      "global_load_dwordx4 v[160:163], v2, s[12:13] offset:-4096 sc1\n"
      "global_load_dwordx4 v[168:171], v2, s[12:13] offset:-3072 sc1\n"
      "global_load_dwordx4 v[68:71], v2, s[12:13] offset:-2048 sc1\n"
      "buffer_load_dword v60, v42, s[32:35], s63 offen offset:1024 sc1\n"
      "buffer_load_dword v61, v42, s[32:35], s63 offen offset:1280 sc1\n"
      "s_waitcnt vmcnt(0)\n"
      "v_add3_u32 v2, v172, v173, s30\n"
      "v_add3_u32 v2, v2, v174, v175\n"
      "v_add3_u32 v2, v2, v164, v165\n"
      "v_add3_u32 v2, v2, v166, v167\n"
      "v_add3_u32 v2, v2, v64, v65\n"
      "v_add3_u32 v2, v2, v66, v67\n"
      "v_xor_b32 v2, v2, v60\n"
      "v_add3_u32 v3, v160, v161, s30\n"
      "v_add3_u32 v3, v3, v162, v163\n"
      "v_add3_u32 v3, v3, v168, v169\n"
      "v_add3_u32 v3, v3, v170, v171\n"
      "v_add3_u32 v3, v3, v68, v69\n"
      "v_add3_u32 v3, v3, v70, v71\n"
      "v_xor_b32 v3, v3, v61\n"
      "v_or_b32 v2, v2, v3\n"
      "v_cmp_ne_u32 vcc, 0, v2\n"
      "s_sub_u32 s44, s44, 1\n"
      "s_cbranch_vccz L_bhA2_%=\n"
      "s_cmp_lg_u32 s44, 0\n"
      "s_cbranch_scc1 L_shA2_%=\n"
      "s_branch L_bhA2_%=\n"
      "L_shA3_%=:\n"
      "v_add_u32 v2, 10240, v7\n"
      "global_load_dwordx4 v[72:75], v2, s[10:11] offset:-1024 sc1\n"
      "global_load_dwordx4 v[80:83], v2, s[10:11] offset:0 sc1\n"
      "global_load_dwordx4 v[88:91], v2, s[10:11] offset:1024 sc1\n"
      "v_add_u32 v2, 10240, v7\n"
      "global_load_dwordx4 v[76:79], v2, s[12:13] offset:-1024 sc1\n"
      "global_load_dwordx4 v[84:87], v2, s[12:13] offset:0 sc1\n"
      "global_load_dwordx4 v[92:95], v2, s[12:13] offset:1024 sc1\n"
      "buffer_load_dword v62, v42, s[32:35], s63 offen offset:1536 sc1\n"
      "buffer_load_dword v63, v42, s[32:35], s63 offen offset:1792 sc1\n"
      "s_waitcnt vmcnt(0)\n"
      "v_add3_u32 v2, v72, v73, s30\n"
      "v_add3_u32 v2, v2, v74, v75\n"
      "v_add3_u32 v2, v2, v80, v81\n"
      "v_add3_u32 v2, v2, v82, v83\n"
      "v_add3_u32 v2, v2, v88, v89\n"
      "v_add3_u32 v2, v2, v90, v91\n"
      "v_xor_b32 v2, v2, v62\n"
      "v_add3_u32 v3, v76, v77, s30\n"
      "v_add3_u32 v3, v3, v78, v79\n"
      "v_add3_u32 v3, v3, v84, v85\n"
      "v_add3_u32 v3, v3, v86, v87\n"
      "v_add3_u32 v3, v3, v92, v93\n"
      "v_add3_u32 v3, v3, v94, v95\n"
      "v_xor_b32 v3, v3, v63\n"
      "v_or_b32 v2, v2, v3\n"
      "v_cmp_ne_u32 vcc, 0, v2\n"
      "s_sub_u32 s44, s44, 1\n"
      "s_cbranch_vccz L_bhA3_%=\n"
      "s_cmp_lg_u32 s44, 0\n"
      "s_cbranch_scc1 L_shA3_%=\n"
      "s_branch L_bhA3_%=\n"
      "L_shB0_%=:\n"
      "v_add_u32 v2, 102400, v7\n"
      "global_load_dwordx4 v[224:227], v2, s[10:11] offset:-4096 sc1\n"
      "global_load_dwordx4 v[228:231], v2, s[10:11] offset:-3072 sc1\n"
      "global_load_dwordx4 v[232:235], v2, s[10:11] offset:-2048 sc1\n"
      "v_add_u32 v2, 102400, v7\n"
      "global_load_dwordx4 v[236:239], v2, s[10:11] offset:-1024 sc1\n"
      "global_load_dwordx4 v[240:243], v2, s[10:11] offset:0 sc1\n"
      "global_load_dwordx4 v[244:247], v2, s[10:11] offset:1024 sc1\n"
      "buffer_load_dword v32, v42, s[32:35], s63 offen offset:2048 sc1\n"
      "buffer_load_dword v33, v42, s[32:35], s63 offen offset:2304 sc1\n"
      "s_waitcnt vmcnt(0)\n"
      "v_add3_u32 v2, v224, v225, s30\n"
      "v_add3_u32 v2, v2, v226, v227\n"
      "v_add3_u32 v2, v2, v228, v229\n"
      "v_add3_u32 v2, v2, v230, v231\n"
      "v_add3_u32 v2, v2, v232, v233\n"
      "v_add3_u32 v2, v2, v234, v235\n"
      "v_xor_b32 v2, v2, v32\n"
      "v_add3_u32 v3, v236, v237, s30\n"
      "v_add3_u32 v3, v3, v238, v239\n"
      "v_add3_u32 v3, v3, v240, v241\n"
      "v_add3_u32 v3, v3, v242, v243\n"
      "v_add3_u32 v3, v3, v244, v245\n"
      "v_add3_u32 v3, v3, v246, v247\n"
      "v_xor_b32 v3, v3, v33\n"
      "v_or_b32 v2, v2, v3\n"
      "v_cmp_ne_u32 vcc, 0, v2\n"
      "s_sub_u32 s44, s44, 1\n"
      "s_cbranch_vccz L_bhB0_%=\n"
      "s_cmp_lg_u32 s44, 0\n"
      "s_cbranch_scc1 L_shB0_%=\n"
      "s_branch L_bhB0_%=\n"
      "L_shB1_%=:\n"
      "v_add_u32 v2, 108544, v7\n"
      "global_load_dwordx4 v[8:11], v2, s[10:11] offset:-4096 sc1\n"
      "global_load_dwordx4 v[12:15], v2, s[10:11] offset:-3072 sc1\n"
      "global_load_dwordx4 v[16:19], v2, s[10:11] offset:-2048 sc1\n"
      "v_add_u32 v2, 108544, v7\n"
      "global_load_dwordx4 v[20:23], v2, s[10:11] offset:-1024 sc1\n"
      "global_load_dwordx4 v[128:131], v2, s[10:11] offset:0 sc1\n"
      "global_load_dwordx4 v[132:135], v2, s[10:11] offset:1024 sc1\n"
      "buffer_load_dword v34, v42, s[32:35], s63 offen offset:2560 sc1\n"
      "buffer_load_dword v35, v42, s[32:35], s63 offen offset:2816 sc1\n"
      "s_waitcnt vmcnt(0)\n"
      "v_add3_u32 v2, v8, v9, s30\n"
      "v_add3_u32 v2, v2, v10, v11\n"
      "v_add3_u32 v2, v2, v12, v13\n"
      "v_add3_u32 v2, v2, v14, v15\n"
      "v_add3_u32 v2, v2, v16, v17\n"
      "v_add3_u32 v2, v2, v18, v19\n"
      "v_xor_b32 v2, v2, v34\n"
      "v_add3_u32 v3, v20, v21, s30\n"
      "v_add3_u32 v3, v3, v22, v23\n"
      "v_add3_u32 v3, v3, v128, v129\n"
      "v_add3_u32 v3, v3, v130, v131\n"
      "v_add3_u32 v3, v3, v132, v133\n"
      "v_add3_u32 v3, v3, v134, v135\n"
      "v_xor_b32 v3, v3, v35\n"
      "v_or_b32 v2, v2, v3\n"
      "v_cmp_ne_u32 vcc, 0, v2\n"
      "s_sub_u32 s44, s44, 1\n"
      "s_cbranch_vccz L_bhB1_%=\n"
      "s_cmp_lg_u32 s44, 0\n"
      "s_cbranch_scc1 L_shB1_%=\n"
      "s_branch L_bhB1_%=\n"
      "L_shB2_%=:\n"
      "v_add_u32 v2, 102400, v7\n"
      "global_load_dwordx4 v[136:139], v2, s[12:13] offset:-4096 sc1\n"
      "global_load_dwordx4 v[140:143], v2, s[12:13] offset:-3072 sc1\n"
      "global_load_dwordx4 v[144:147], v2, s[12:13] offset:-2048 sc1\n"
      "v_add_u32 v2, 102400, v7\n"
      "global_load_dwordx4 v[148:151], v2, s[12:13] offset:-1024 sc1\n"
      "global_load_dwordx4 v[152:155], v2, s[12:13] offset:0 sc1\n"
      "global_load_dwordx4 v[156:159], v2, s[12:13] offset:1024 sc1\n"
      "buffer_load_dword v24, v42, s[32:35], s63 offen offset:3072 sc1\n"
      "buffer_load_dword v25, v42, s[32:35], s63 offen offset:3328 sc1\n"
      "s_waitcnt vmcnt(0)\n"
      "v_add3_u32 v2, v136, v137, s30\n"
      "v_add3_u32 v2, v2, v138, v139\n"
      "v_add3_u32 v2, v2, v140, v141\n"
      "v_add3_u32 v2, v2, v142, v143\n"
      "v_add3_u32 v2, v2, v144, v145\n"
      "v_add3_u32 v2, v2, v146, v147\n"
      "v_xor_b32 v2, v2, v24\n"
      "v_add3_u32 v3, v148, v149, s30\n"
      "v_add3_u32 v3, v3, v150, v151\n"
      "v_add3_u32 v3, v3, v152, v153\n"
      "v_add3_u32 v3, v3, v154, v155\n"
      "v_add3_u32 v3, v3, v156, v157\n"
      "v_add3_u32 v3, v3, v158, v159\n"
      "v_xor_b32 v3, v3, v25\n"
      "v_or_b32 v2, v2, v3\n"
      "v_cmp_ne_u32 vcc, 0, v2\n"
      "s_sub_u32 s44, s44, 1\n"
      "s_cbranch_vccz L_bhB2_%=\n"
      "s_cmp_lg_u32 s44, 0\n"
      "s_cbranch_scc1 L_shB2_%=\n"
      "s_branch L_bhB2_%=\n"
      "L_shB3_%=:\n"
      "v_add_u32 v2, 108544, v7\n"
      "global_load_dwordx4 v[172:175], v2, s[12:13] offset:-4096 sc1\n"
      "global_load_dwordx4 v[160:163], v2, s[12:13] offset:-3072 sc1\n"
      "global_load_dwordx4 v[164:167], v2, s[12:13] offset:-2048 sc1\n"
      "v_add_u32 v2, 108544, v7\n"
      "global_load_dwordx4 v[168:171], v2, s[12:13] offset:-1024 sc1\n"
      "global_load_dwordx4 v[64:67], v2, s[12:13] offset:0 sc1\n"
      "global_load_dwordx4 v[68:71], v2, s[12:13] offset:1024 sc1\n"
      "buffer_load_dword v26, v42, s[32:35], s63 offen offset:3584 sc1\n"
      "buffer_load_dword v27, v42, s[32:35], s63 offen offset:3840 sc1\n"
      "s_waitcnt vmcnt(0)\n"
      "v_add3_u32 v2, v172, v173, s30\n"
      "v_add3_u32 v2, v2, v174, v175\n"
      "v_add3_u32 v2, v2, v160, v161\n"
      "v_add3_u32 v2, v2, v162, v163\n"
      "v_add3_u32 v2, v2, v164, v165\n"
      "v_add3_u32 v2, v2, v166, v167\n"
      "v_xor_b32 v2, v2, v26\n"
      "v_add3_u32 v3, v168, v169, s30\n"
      "v_add3_u32 v3, v3, v170, v171\n"
      "v_add3_u32 v3, v3, v64, v65\n"
      "v_add3_u32 v3, v3, v66, v67\n"
      "v_add3_u32 v3, v3, v68, v69\n"
      "v_add3_u32 v3, v3, v70, v71\n"
      "v_xor_b32 v3, v3, v27\n"
      "v_or_b32 v2, v2, v3\n"
      "v_cmp_ne_u32 vcc, 0, v2\n"
      "s_sub_u32 s44, s44, 1\n"
      "s_cbranch_vccz L_bhB3_%=\n"
      "s_cmp_lg_u32 s44, 0\n"
      "s_cbranch_scc1 L_shB3_%=\n"
      "s_branch L_bhB3_%=\n"
      "L_sgb_%=:\n"
      "buffer_load_dwordx4 v[244:247], v53, s[32:35], s66 offen sc1\n"
      "s_waitcnt vmcnt(0)\n"
      "v_add_u32 v245, v245, v247\n"
      "v_cmp_ne_u32 vcc, s54, v245\n"
      "s_sub_u32 s44, s44, 1\n"
      "s_cbranch_vccz L_bgb_%=\n"
      "s_cmp_lg_u32 s44, 0\n"
      "s_cbranch_scc1 L_sgb_%=\n"
      "s_branch L_bgb_%=\n"
      : STATE, "+{s[32:35]}"(s32), "+{v[8:23]}"(v8), "+{v[24:27]}"(v24), "+{v[52:55]}"(v52), "+{v[112:127]}"(v112), "+{v[192:195]}"(t[0]), "+{v[240:243]}"(t[1]), "+{v[196:199]}"(t[2]), "+{v[200:203]}"(t[3]), "+{v[204:207]}"(t[4]), "+{v[208:211]}"(t[5]), "+{v[212:215]}"(t[6]), "+{v[216:219]}"(t[7]), "+{v[220:223]}"(t[8]), "+{v[224:227]}"(t[9]), "+{v[236:239]}"(t[10]), "+{v[40:43]}"(t[11]), "+{v[44:47]}"(t[12]), "+{v[48:51]}"(t[13]), "+{v[56:59]}"(t[14]), "+{v[60:63]}"(t[15]), "+{v[28:31]}"(t[16]), "+{v[4:7]}"(t[17])
      : "{s2}"(blockIdx.x)
      : "memory", "scc", "vcc", "s40", "s41", "s42", "s43", "s44", "s45", "s46", "s47", "s48", "s49", "s50", "s51", "v2", "v3", "v32", "v33", "v34", "v35", "v36", "v37", "v38", "v39", "v64", "v65", "v66", "v67", "v68", "v69", "v70", "v71", "v72", "v73", "v74", "v75", "v76", "v77", "v78", "v79", "v80", "v81", "v82", "v83", "v84", "v85", "v86", "v87", "v88", "v89", "v90", "v91", "v92", "v93", "v94", "v95", "v96", "v97", "v98", "v99", "v100", "v101", "v102", "v103", "v104", "v105", "v106", "v107", "v108", "v109", "v110", "v111", "v128", "v129", "v130", "v131", "v132", "v133", "v134", "v135", "v136", "v137", "v138", "v139", "v140", "v141", "v142", "v143", "v144", "v145", "v146", "v147", "v148", "v149", "v150", "v151", "v152", "v153", "v154", "v155", "v156", "v157", "v158", "v159", "v160", "v161", "v162", "v163", "v164", "v165", "v166", "v167", "v168", "v169", "v170", "v171", "v172", "v173", "v174", "v175", "v176", "v177", "v178", "v179", "v180", "v181", "v182", "v183", "v184", "v185", "v186", "v187", "v188", "v189", "v190", "v191", "v228", "v229", "v230", "v231", "v244", "v245", "v246", "v247", "v248", "v249", "v250", "v251", "v252", "v253", "v254", "v255", "a0", "a1", "a2", "a3", "a4", "a5", "a6", "a7", "a8", "a9", "a10", "a11", "a12", "a13", "a14", "a15", "a16", "a17", "a18", "a19", "a20", "a21", "a22", "a23", "a24", "a25", "a26", "a27", "a28", "a29", "a30", "a31", "a32", "a33", "a34", "a35", "a36", "a37", "a38", "a39", "a40", "a41", "a42", "a43", "a44", "a45", "a46", "a47", "a48", "a49", "a50", "a51", "a52", "a53", "a54", "a55", "a56", "a57", "a58", "a59", "a60", "a61", "a62", "a63", "a64", "a65", "a66", "a67", "a68", "a69", "a70", "a71", "a72", "a73", "a74", "a75", "a76", "a77", "a78", "a79", "a80", "a81", "a82", "a83", "a84", "a85", "a86", "a87", "a88", "a89", "a90", "a91", "a92", "a93", "a94", "a95", "a96", "a97", "a98", "a99", "a100", "a101", "a102", "a103", "a104", "a105", "a106", "a107", "a108", "a109", "a110", "a111", "a112", "a113", "a114", "a115", "a116", "a117", "a118", "a119", "a120", "a121", "a122", "a123", "a124", "a125", "a126", "a127", "a128", "a129", "a130", "a131", "a132", "a133", "a134", "a135", "a136", "a137", "a138", "a139", "a140", "a141", "a142", "a143", "a144", "a145", "a146", "a147", "a148", "a149", "a150", "a151", "a152", "a153", "a154", "a155", "a156", "a157", "a158", "a159", "a160", "a161", "a162", "a163", "a164", "a165", "a166", "a167", "a168", "a169", "a170", "a171", "a172", "a173", "a174", "a175", "a176", "a177", "a178", "a179", "a180", "a181", "a182", "a183", "a184", "a185", "a186", "a187", "a188", "a189", "a190", "a191", "a192", "a193", "a194", "a195", "a196", "a197", "a198", "a199", "a200", "a201", "a202", "a203", "a204", "a205", "a206", "a207", "a208", "a209", "a210", "a211", "a212", "a213", "a214", "a215", "a216", "a217", "a218", "a219", "a220", "a221", "a222", "a223", "a224", "a225", "a226", "a227", "a228", "a229", "a230", "a231", "a232", "a233", "a234", "a235", "a236", "a237", "a238", "a239", "a240", "a241", "a242", "a243", "a244", "a245", "a246", "a247", "a248", "a249", "a250", "a251", "a252", "a253", "a254", "a255");
}

extern "C" void kernel_launch(void* const* d_in, const int* in_sizes, int n_in, void* d_out, int out_size,
                              void* d_ws, size_t ws_size, hipStream_t stream) {
  mega<<<256, 256, 0, stream>>>((const u16*)d_in[0], (const u16*)d_in[1], (const u16*)d_in[2], (const u16*)d_in[3],
                                (const u16*)d_in[4], (const u16*)d_in[5], (const u16*)d_in[7], (const u16*)d_in[8],
                                (const u16*)d_in[9], (const u16*)d_in[10], (const u16*)d_in[11], (char*)d_ws, (u16*)d_out);
}
```

```cpp
#include <hip/hip_runtime.h>
#include <stdint.h>

typedef unsigned short u16;
typedef __attribute__((ext_vector_type(4))) unsigned int u32x4;
typedef __attribute__((ext_vector_type(8))) unsigned int u32x8;
typedef __attribute__((ext_vector_type(16))) unsigned int u32x16;
static constexpr int D_ = 640, F_ = 1536, RS_ = 4224;

static constexpr unsigned WS_FW = 20480;
static constexpr unsigned WS_FD = WS_FW + 61440;
static constexpr unsigned WS_STAT = 147456;
static constexpr unsigned WS_PART = 262144;
static constexpr unsigned WS_MOD = 786432;
static constexpr unsigned WS_KVT = 1u << 20;
static constexpr unsigned WS_QT = 4u << 20;
static constexpr unsigned WS_X1T = 7u << 20;
static constexpr unsigned WS_CK = 2752512;
static constexpr unsigned WS_WP = 10u << 20;
static constexpr unsigned WS_WD = 14u << 20;
static constexpr unsigned WS_HH = 16u << 20;
static constexpr unsigned WS_HC = 22u << 20;
static constexpr unsigned WS_ST2 = WS_HC + (512u << 10);
static constexpr unsigned WS_X1R = 23u << 20;
static constexpr unsigned WS_DUMMY = WS_HC + (768u << 10);

static constexpr int KCP = 4160, KCH = 4 * KCP;
static constexpr int XR_CH = 7680;
static constexpr int L_XI = 3 * KCH;
static constexpr int L_EX = 72960;
static constexpr int L_KV = L_EX, KVW = 6400;
static constexpr int L_KT = L_KV + 4 * KVW;
static constexpr int L_QT = L_KT + 84 * 144;
static constexpr int L_WR = L_QT + 10240;
static constexpr int L_GW = L_WR + 16384;
static constexpr int L_GS = L_GW + 20480;
static constexpr int L_SC = L_GS + 320;
static constexpr int L_XR = 110592, L_RS = 162304;
static constexpr int L_WD1 = 122880;
static_assert(L_KT == 98560 && L_QT == 110656 && L_WR == 120896 && L_GW == 137280, "");

struct __attribute__((aligned(256))) Tab { unsigned s[1024][64], l[4][6][64][4]; };
static constexpr Tab make_tab() {
  Tab t = {};
  for (int tid = 0; tid < 256; ++tid) {
    const int lane = tid & 63, ip = lane & 15, fq = lane >> 4, q4 = ip >> 2, p4 = lane & 3;
    const int fo = (ip & 3) * KCP + 2 * ((ip & 12) + 8 * (fq & 1) + 256 * (fq >> 1)), go = L_XI + (15 - ip + 8 * (fq >> 1)) * 96 + 16 * (fq & 1);
    const int w = tid >> 6, exw = L_EX + ip * 128 + (fq ^ (ip & 7)) * 16, rowA = 8 * tid;
    t.l[w][0][lane][0] = fo + (w >> 1) * KCH + (w & 1) * 256;
    t.l[w][0][lane][1] = go + (w >> 1) * XR_CH + (w & 1) * 384;
    t.l[w][0][lane][2] = fo + 2 * KCH + w * 128;
    t.l[w][0][lane][3] = go + 2 * XR_CH + w * 192;
    t.l[w][1][lane][0] = exw + w * 8192;
    t.l[w][1][lane][1] = L_EX + 32768 + w * 1024 + (lane >> 3) * 128 + ((lane & 7) ^ (lane >> 3)) * 16;
    t.l[w][1][lane][2] = L_XI + ((2040 - rowA) >> 5) * 96 + ((2040 - rowA) & 31) * 2;
    t.l[w][1][lane][3] = L_XI + 2 * XR_CH + ((252 - 4 * lane) >> 5) * 96 + ((252 - 4 * lane) & 31) * 2;
    t.l[w][2][lane][0] = lane < 8 ? lane * 8 : 0x80000000u;
    t.l[w][2][lane][1] = (ip >> 1) == 4 * (fq & 1) ? fq * 8 + (ip & 1) * 4 : 0x80000000u;
    t.l[w][2][lane][2] = fq * D_ * 128 + q4 * 32 + p4 * 8;
    t.l[w][2][lane][3] = (lane < 40 ? lane : 39) * 64;
    t.l[w][3][lane][0] = exw + 32768 + w * 4096;
    t.l[w][3][lane][1] = (lane & 1) * 2048 + (lane >> 1) * 16;
    t.l[w][3][lane][2] = p4 * 64 + (fq == 1 ? 256 * 32 : 0) + (q4 < 3 ? q4 : 4) * 20480;
    t.l[w][3][lane][3] = (exw + w * 8192) ^ 64;
    t.l[w][4][lane][0] = (tid >> 3) * 5120 + ((tid >> 2) & 1) * 64;
    t.l[w][4][lane][1] = fq * 5120 + ((lane >> 3) & 1) * 64;
    t.l[w][4][lane][2] = (exw + 32768 + w * 4096) ^ 64;
    t.l[w][4][lane][3] = (2 * (lane >> 5) + (q4 >> 1)) * (D_ * 128) + (2 * (fq & 1) + (q4 & 1)) * 32 + p4 * 8;
    t.l[w][5][lane][0] = L_GW + w * 2048 + (4 * fq + q4) * 64 + p4 * 8;
    t.l[w][5][lane][1] = (rowA >> 6) * D_ * 128 + ((lane >> 1) & 3) * 32 + (lane & 1) * 16;
    t.l[w][5][lane][2] = ((lane >> 1) & 3) == 2 * ((lane >> 3) & 1) ? w * 128 + (lane >> 3) * 8 + (lane & 1) * 4 : 0x80000000u;
    t.l[w][5][lane][3] = L_EX + w * 2048 + (lane >> 2) * 128 + ((2 * p4) ^ ((lane >> 2) & 7)) * 16;
  }
  for (int i = 0; i < 1024; ++i) {
    const int b = i >> 2, w = i & 3, xcd = b & 7, idx = b >> 3, L = xcd * 32 + idx, side = L & 1;
    const int ct = L & 7, rt = L >> 3, srow = xcd * 256 + idx * 8;
    const int chA = 128 + L, chH = L >> 1, rH = 512 * (side + (3 - 2 * side) * (w >> 1)) + 256 * (w & 1);
    const int id = idx < 29 ? idx : 29, gj = xcd * 30 + id, j_ks = 5 * (xcd >> 1) + (id * 4 + w) / 24, j_b = 24 * (xcd & 1) + (id * 4 + w) % 24;
    const int d_ks = gj / 5, d_cb = 4 * (gj % 5) + w;
    const unsigned dummy = WS_DUMMY + (i & 63) * 1024;
    const int bm = (xcd >> 2) * 8 + (idx >> 2), bn = (xcd & 3) * 4 + (idx & 3), bm2 = (xcd >> 1) * 8 + (idx >> 2), bn2 = (xcd & 1) * 4 + (idx & 3);
    t.s[i][0] = WS_KVT + (1984 - 64 * rt) * 2 + ct * 80 * RS_;
    t.s[i][1] = WS_KVT + WS_CK + (31 - rt) * RS_ + ct * 512;
    t.s[i][2] = idx < 30 ? WS_MOD + (gj >> 2) * 2048 + (gj & 3) * 64 : dummy;
    t.s[i][3] = 32 * j_ks * (F_ * 2) + (j_b - w) * 64;
    t.s[i][4] = WS_QT + rt * 128 + ct * 80 * RS_;
    t.s[i][5] = WS_QT + WS_CK + (rt * 40 + ct * 5) * 128;
    t.s[i][6] = srow * 1280;
    t.s[i][7] = WS_STAT + (srow >> 9) * 4096 + ((srow >> 3) & 63) * 16;
    t.s[i][8] = WS_MOD + (1920 + 640 * (w >> 1) + 32 * j_ks + 16 * (w & 1)) * 32;
    t.s[i][9] = idx < 30 ? WS_PART + j_ks * 24576 + j_b * 512 : dummy;
    t.s[i][10] = WS_KVT + WS_CK + chA / 20 * 128 + chA % 20 * 4;
    t.s[i][11] = WS_KVT + WS_CK + chH / 20 * 128 + chH % 20 * 4;
    t.s[i][12] = idx < 30 ? WS_WP + (4 * j_b + 0) / 3 * 61440 + (j_ks * 3 + (4 * j_b + 0) % 3) * 1024 : dummy;
    t.s[i][13] = idx < 30 ? WS_WP + (4 * j_b + 1) / 3 * 61440 + (j_ks * 3 + (4 * j_b + 1) % 3) * 1024 : dummy;
    t.s[i][14] = idx < 30 ? WS_WP + (4 * j_b + 2) / 3 * 61440 + (j_ks * 3 + (4 * j_b + 2) % 3) * 1024 : dummy;
    t.s[i][15] = idx < 30 ? WS_WP + (4 * j_b + 3) / 3 * 61440 + (j_ks * 3 + (4 * j_b + 3) % 3) * 1024 : dummy;
    t.s[i][16] = WS_FW + (3 * (bn * 4 + w) / 4 < 46 ? 3 * (bn * 4 + w) / 4 : 46) * 1280;
    t.s[i][17] = WS_X1T + WS_CK + chH / 2 * 512 + chH % 2 * 64 + (rH >> 9) * 128 + ((rH >> 6) & 7) * 8;
    t.s[i][18] = WS_X1T + ((rH >> 6) * D_ + chH) * 128;
    t.s[i][19] = idx < 30 ? WS_FW + (j_b * 20 + j_ks) * 64 : dummy;
    t.s[i][20] = WS_QT + chH * RS_ + rH * 2;
    t.s[i][21] = WS_X1T + WS_CK + chA / 2 * 512 + chA % 2 * 64;
    t.s[i][22] = WS_STAT + (rH >> 9) * 4096 + ((rH >> 3) & 63) * 16;
    t.s[i][23] = WS_MOD + chH / 8 * 256 + chH % 8 * 8;
    t.s[i][24] = WS_QT + WS_CK + chA / 16 * 128 + chA % 16 * 4;
    t.s[i][25] = WS_QT + WS_CK + (rH >> 6) * 5120 + chH / 16 * 128 + chH % 16 * 4;
    t.s[i][26] = idx < 30 ? WS_WD + ((2 * d_cb / 5 * 48 + d_ks) * 5 + 2 * d_cb % 5) * 1024 : dummy;
    t.s[i][27] = idx < 30 ? WS_WD + (((2 * d_cb + 1) / 5 * 48 + d_ks) * 5 + (2 * d_cb + 1) % 5) * 1024 : dummy;
    t.s[i][28] = idx < 30 ? WS_FD + (d_cb * 48 + d_ks) * 64 : dummy;
    t.s[i][29] = WS_X1T + chA * 128;
    t.s[i][30] = WS_X1T + WS_CK + bm / 4 * 128 + bm % 4 * 16 + 4;
    t.s[i][31] = WS_WP + (bn * 4 + w) * 61440;
    t.s[i][32] = chA * 4096;
    t.s[i][33] = chH * 4096;
    t.s[i][34] = ((1792 - rH) >> 5) * 96;
    t.s[i][35] = (96 * (d_ks / 3) + 16 * (d_ks % 3 >> 1) + 2 * (d_ks % 3 & 1)) * (D_ * 2) + d_cb * 64;
    t.s[i][36] = (d_ks % 3 == 1 ? 30 : 18) * (D_ * 2) + ((chA / 8 * 256 + chA % 8 * 8 - chH / 8 * 256 - chH % 8 * 8) << 16);
    t.s[i][37] = ((chA >> 1) & 3) * 32;
    t.s[i][38] = WS_KVT + WS_CK + (chA + 256) / 20 * 128 + (chA + 256) % 20 * 4;
    t.s[i][39] = side;
    t.s[i][40] = WS_ST2 + (bm2 * 8 + bn2) * 1024 + w * 256;
    t.s[i][41] = WS_ST2 + (2 * bm + (w >> 1)) * 8192 + (w & 1) * 512;
    t.s[i][42] = L_XR + (w < 3 ? 20 * w : 56) * 128;
    t.s[i][43] = L_RS + (w & 1) * 512 + (w >> 1) * 256;
    t.s[i][48] = w;
    t.s[i][49] = WS_HH + (((2 * bm + (w >> 1)) * 4 + (w & 1)) * 48 + 3 * bn) * 1024;
    t.s[i][50] = WS_HC + (((2 * bm + (w >> 1)) * 4 + (bn >> 2)) * 8 + (bn & 3)) * 512 + (w & 1) * 256;
    t.s[i][51] = WS_HC + (bm2 * 4 + w) * 4096;
    t.s[i][52] = WS_FD + (((5 * bn2) >> 1) * 48 + 12 * w) * 64;
    t.s[i][53] = WS_PART + (bn * 4 + w) * 384;
    t.s[i][54] = WS_MOD + (3200 + bn2 * 80 + (w < 3 ? 16 * w : 48)) * 32;
    t.s[i][55] = WS_X1R + (bm2 * D_ + bn2 * 80 + (w < 3 ? 20 * w : 56)) * 128;
    t.s[i][56] = WS_X1T + WS_CK + (bn2 * 80 + (w < 3 ? 20 * w : 56)) * 256 + bm2 / 8 * 128 + bm2 % 8 * 8 + bm2 % 2 * 4;
    t.s[i][57] = WS_HH + (((2 * bm + (w >> 1)) * 4 + 2 + (w & 1)) * 48 + 3 * bn) * 1024;
    t.s[i][58] = WS_HC + (((2 * bm + (w >> 1)) * 4 + (bn >> 2)) * 8 + 4) * 512 + (4 * (w & 1) + (bn & 3)) * 256;
    t.s[i][59] = WS_X1T + bm * 163840;
    t.s[i][60] = WS_WD + (bn2 * 48 + 12 * w) * 5120;
    t.s[i][61] = w * 4096;
    t.s[i][62] = ((bm2 >> 1) * 128 + (bm2 & 1) * 32 + (w & 1) * 16) * (D_ * 2) + bn2 * 160 + (w >> 1) * 64;
    t.s[i][63] = WS_HH + (bm2 * 192 + 12 * w) * 1024;
  }
  return t;
}
static __device__ const Tab TAB = make_tab();

__global__ __launch_bounds__(256, 1) __attribute__((aligned(4096))) void mega(const u16* q, const u16* k, const u16* v, const u16* shift_k, const u16* ssm_k, const u16* ssm_D, const u16* w_gate,
                                               const u16* w_up, const u16* w_down, const u16* c, const u16* w_mod, char* ws, u16* out) {
  __shared__ __attribute__((aligned(16))) char lds[163840];
  int w = (int)(size_t)lds, tid = threadIdx.x, v1;
  const Tab* tab = &TAB;
  u32x16 s4, s60, s76;
  u32x8 s20, s52, s92;
  u32x4 s28, s36, v232;
  asm volatile(
      "s_load_dwordx16 s[4:19], s[0:1], 0x0\n"
      "s_load_dwordx8 s[20:27], s[0:1], 0x40\n"
      "s_load_dwordx2 s[28:29], s[0:1], 0x60\n"
      "v_readfirstlane_b32 s3, v0\n"
      "v_and_b32 v2, 63, v0\n"
      "s_lshl_b32 s42, s2, 4\n"
      "s_and_b32 s43, s2, 7\n"
      "v_mul_u32_u24 v3, 205, v2\n"
      "s_and_b32 s52, s2, 0xc0\n"
      "s_lshr_b32 s3, s3, 6\n"
      "v_lshlrev_b32 v8, 4, v2\n"
      "s_cmp_lt_u32 s3, 2\n"
      "s_cbranch_scc1 L_pf%=\n"
      "s_getpc_b64 s[54:55]\n"
      "s_and_b32 s47, s2, 0xf8\n"
      "s_and_b32 s54, s54, 0xffffff00\n"
      "s_add_u32 s56, s40, 0x40000\n"
      "s_addc_u32 s57, s41, 0\n"
      "s_lshl_b32 s47, s47, 10\n"
      "s_mov_b32 s53, 38592\n"
      "s_cmp_eq_u32 s3, 2\n"
      "s_cselect_b64 s[54:55], s[56:57], s[54:55]\n"
      "s_cmov_b32 s53, 24512\n"
      "s_cmp_gt_u32 s47, s53\n"
      "s_cbranch_scc1 L_pf%=\n"
      "v_lshl_add_u32 v9, v8, 3, s47\n"
      "s_mov_b32 m0, 160512\n"
      "v_min_u32 v9, s53, v9\n"
      "global_load_lds_dword v9, s[54:55]\n"
      "L_pf%=:\n"
      "s_lshl_b32 s47, s43, 8\n"
      "v_add_u32 v9, 13120, v3\n"
      "v_add_u32 v10, 26240, v3\n"
      "s_add_u32 s52, s52, s47\n"
      "s_lshl_b32 s47, s3, 4\n"
      "v_lshrrev_b32 v3, 11, v3\n"
      "v_lshrrev_b32 v9, 11, v9\n"
      "s_add_u32 s52, s52, s47\n"
      "s_movk_i32 s47, 1120\n"
      "v_lshrrev_b32 v10, 11, v10\n"
      "s_mulk_i32 s52, 1280\n"
      "s_bfe_u32 s54, s2, 0x30003\n"
      "v_mad_u32_u24 v232, v3, s47, v8\n"
      "v_mad_u32_u24 v233, v9, s47, v8\n"
      "v_mad_u32_u24 v234, v10, s47, v8\n"
      "s_mul_i32 s47, s54, 160\n"
      "s_mul_i32 s53, s3, 6400\n"
      "s_add_u32 s52, s52, s47\n"
      "v_subrev_u32 v233, 6656, v233\n"
      "v_subrev_u32 v234, 5632, v234\n"
      "s_sub_u32 s47, s52, 3840\n"
      "s_add_u32 s53, s53, 72960\n"
      "v_add_u32 v3, s47, v232\n"
      "s_add_u32 s47, s52, 3840\n"
      "s_add_u32 s55, s52, 7680\n"
      "s_mov_b32 s38, 2621440\n"
      "s_mov_b32 s39, 0x20000\n"
      "s_mulk_i32 s54, 640\n"
      "s_waitcnt lgkmcnt(0)\n"
      "s_mov_b32 m0, s53\n"
      "s_mov_b32 s36, s6\n"
      "s_and_b32 s37, s7, 0xffff\n"
      "buffer_load_dwordx4 v3, s[36:39], 0 offen lds\n"
      "s_add_u32 m0, s53, 1024\n"
      "s_nop 0\n"
      "buffer_load_dwordx4 v233, s[36:39], s47 offen lds\n"
      "s_add_u32 m0, s53, 2048\n"
      "v_add_u32 v9, s54, v8\n"
      "buffer_load_dwordx4 v234, s[36:39], s47 offen lds\n"
      "s_add_u32 m0, s53, 3072\n"
      "s_mov_b32 s36, s8\n"
      "s_and_b32 s37, s9, 0xffff\n"
      "buffer_load_dwordx4 v232, s[36:39], s52 offen lds\n"
      "s_add_u32 m0, s53, 4096\n"
      "s_nop 0\n"
      "buffer_load_dwordx4 v233, s[36:39], s55 offen lds\n"
      "s_add_u32 m0, s53, 5120\n"
      "s_mov_b32 exec_hi, 0\n"
      "buffer_load_dwordx4 v234, s[36:39], s55 offen lds\n"
      "s_add_u32 m0, s53, 5632\n"
      "s_mov_b32 exec_hi, 0xff\n"
      "global_load_lds_dwordx4 v9, s[10:11]\n"
      "s_mov_b32 exec_hi, -1\n"
      "s_load_dwordx2 s[30:31], s[26:27], 0x0\n"
      "s_lshl_b32 s51, s3, 2\n"
      "s_add_u32 s42, s42, s51\n"
      "s_lshl_b32 s42, s42, 6\n"
      "s_add_u32 s56, s40, s42\n"
      "s_addc_u32 s57, s41, 0\n"
      "s_load_dwordx16 s[60:75], s[56:57], 0x0\n"
      "s_load_dwordx16 s[76:91], s[56:57], 0x40\n"
      "s_load_dwordx8 s[92:99], s[56:57], 0x80\n"
      : "+{s3}"(w), "+{v0}"(tid), "=&{s[4:19]}"(s4), "=&{s[20:27]}"(s20), "=&{s[28:31]}"(s28), "=&{s[36:39]}"(s36), "=&{s[52:59]}"(s52), "=&{s[60:75]}"(s60), "=&{s[76:91]}"(s76),
        "=&{s[92:99]}"(s92), "=&{v[232:235]}"(v232), "=&{v1}"(v1)
      : "{s[0:1]}"(__builtin_amdgcn_kernarg_segment_ptr()), "{s2}"(blockIdx.x), "{s[40:41]}"(tab)
      : "memory", "scc", "s42", "s43", "s47", "s48", "s49", "s50", "s51", "v2", "v3", "v8", "v9", "v10", "v11", "v12", "v13");
  u32x4 t[12];
  const int lane = tid & 63, ip = lane & 15, fq = lane >> 4, q4 = ip >> 2, p4 = lane & 3;
  const int ck = 40 * w + (lane < 40 ? lane : 39), z7 = ((tid < 84 ? tid : 83) * 37) >> 8;
  const bool dg = fq == q4;
  const unsigned msk = dg ? ((ip & 1) ? 0xFFFF0000u : 0xFFFFu) : 0u, mk0 = (ip & 2) ? 0u : msk, mk1 = (ip & 2) ? msk : 0u;
  t[0][0] = dg && p4 == 0 ? 0x3F803F80u : 0u;
  t[0][1] = dg && p4 == 1 ? 0x3F803F80u : 0u;
  t[0][2] = dg && p4 == 2 ? 0x3F803F80u : 0u;
  t[0][3] = dg && p4 == 3 ? 0x3F803F80u : 0u;
  t[1][0] = (mk0 & 0x0D0C0D0Cu) ^ 0x0C0C0C0Cu;
  t[1][1] = (mk1 & 0x0D0C0D0Cu) ^ 0x0C0C0C0Cu;
  t[1][2] = (mk0 & 0x0F0E0F0Eu) ^ 0x0C0C0C0Cu;
  t[1][3] = (mk1 & 0x0F0E0F0Eu) ^ 0x0C0C0C0Cu;
  t[2][0] = L_KV + w * KVW + ip * 160 + fq * 8;
  t[2][1] = L_KV + w * KVW + 5632 + ip * 8;
  t[2][2] = mk0;
  t[2][3] = mk1;
  t[3][0] = L_KT + ip * 144 + (60 - 16 * w - 4 * fq) * 2;
  t[3][1] = L_KT + (20 * w + (lane >> 3)) * 144 + (lane & 7) * 16;
  t[3][2] = (20 * w + (lane >> 3)) * RS_ + (lane & 7) * 16;
  t[3][3] = lane & 4 ? 0x80000000u : w * 128 + (8 * (lane & 7) + (lane >> 3)) * 4;
  t[4][0] = L_GS + ((ck & ~31) + ((ck >> 2) & 3) * 8 + ((ck >> 4) & 1) * 4 + (ck & 3)) * 2;
  t[4][1] = L_GW + (4 * fq + q4) * 128 + p4 * 8 + ((w ^ (2 * fq + (q4 >> 1))) & 3) * 32;
  t[4][2] = L_GS + fq * 16;
  t[4][3] = lane < 16 ? (2 * w + (lane >> 3)) * 256 + (lane & 7) * 8 : 0x80000000u;
  t[5][0] = (lane & 12) == 4 ? 1 << 20 : 0;
  t[5][1] = lane & 8 ? 0u : ~0u;
  t[5][2] = (8 * w + fq) * (F_ * 2) + (ip ^ (2 * fq)) * 16;
  t[5][3] = (2 * w + (lane >> 5)) * 1280 + (lane & 31) * 16;
  t[6][0] = p4 * 64 + (lane >> 5) * 256 + ((lane >> 2) & 7) * 8;
  t[6][1] = L_WR + (fq >> 1) * 4096 + (fq & 1) * 1024 + q4 * 256 + (((2 * w + 1) ^ q4) & 7) * 32 + p4 * 8;
  t[6][2] = lane == 0 ? w * 1024 : 0x80000000u;
  t[6][3] = ((fq & 1 ? w * 64 + 512 : fq * 32 + 128 * w) + ip * 4) | ((unsigned)(fq & (fq >> 1 | w >> 1)) << 31);
  t[7][0] = (ip < 8 ? ip : 8 + p4) * KCP + 56;
  t[7][1] = z7 * (KCP - 56) + (tid < 84 ? tid : 83) * 8;
  t[7][2] = L_XI + 64 * 96 + (tid < 90 ? tid : 89) * 16;
  t[7][3] = L_WR + (fq >> 1) * 4096 + (fq & 1) * 1024 + q4 * 256 + (((2 * w) ^ q4) & 7) * 32 + p4 * 8;
  t[8][0] = L_SC + w * 32 + (lane >> 2) * 2;
  t[8][1] = (2 * (tid >> 4) + ((tid >> 2) & 1)) * (D_ * 128) + (2 * ((tid >> 3) & 1) + ((tid >> 1) & 1)) * 32 + (tid & 1) * 16;
  t[8][2] = (4 * fq + ((lane >> 2) & 1)) * (D_ * 2) + p4 * 16;
  t[8][3] = ((lane >> 3) ^ (lane >> 4)) & 1;
  t[9][0] = L_QT + (8 * fq + q4) * 160 + p4 * 8 + w * 32;
  t[9][1] = L_QT + (8 * fq + q4) * 160 + p4 * 8 + 128 + (w & 1) * 5120;
  t[9][2] = (16 * w + ip) * RS_ + fq * 16;
  t[9][3] = w < 2 ? (64 + ip) * RS_ + fq * 16 + w * 64 : 0x80000000u;
  t[10][0] = L_SC + (ip & 4 ? 0 : 64) + fq * 8;
  t[10][1] = (tid >> 3) * RS_;
  t[10][2] = L_XI + (tid >> 2) * 96 + (tid & 3) * 16;
  t[10][3] = (fq >> 1) * 256 + (fq & 1) * 64 + ip * 4;
  t[11][0] = lane * 8;
  t[11][1] = lane & 4 ? 0x80000000u : (16 * w + 4 * (lane & 7) + fq) * 512 + ((lane >> 3) & 1) * 64;
  t[11][2] = (4 * fq + q4) * 128 + ((2 * fq + (q4 >> 1)) & 3) * 32 + p4 * 8;
  t[11][3] = lane * 16;
  asm volatile(
      "v_and_b32 v2, 63, v0\n"
      "s_and_b32 s43, s2, 7\n"
      "v_mul_u32_u24 v3, 205, v2\n"
      "v_lshlrev_b32 v8, 4, v2\n"
      "s_lshr_b32 s48, s2, 3\n"
      "s_min_u32 s48, s48, 29\n"
      "s_mul_i32 s49, s43, 30\n"
      "v_lshrrev_b32 v12, 3, v2\n"
      "s_add_u32 s48, s48, s49\n"
      "v_xor_b32 v13, v12, v2\n"
      "s_and_b32 s49, s48, 3\n"
      "v_and_b32 v11, 1, v2\n"
      "s_lshl_b32 s49, s49, 2\n"
      "v_and_or_b32 v13, v13, 6, v11\n"
      "s_add_u32 s49, s49, s3\n"
      "v_mul_u32_u24 v12, 7680, v12\n"
      "s_lshr_b32 s48, s48, 2\n"
      "s_mul_i32 s50, s49, 307200\n"
      "s_lshl_b32 s48, s48, 7\n"
      "v_lshl_add_u32 v12, v13, 4, v12\n"
      "s_add_u32 s48, s48, s50\n"
      "s_mul_i32 s50, s3, 5120\n"
      "s_mulk_i32 s49, 80\n"
      "v_min_u32 v13, 39, v2\n"
      "v_add_u32 v12, s48, v12\n"
      "v_lshl_add_u32 v13, v13, 1, s49\n"
      "s_add_u32 m0, s50, 137280\n"
      "global_load_ushort v235, v13, s[22:23]\n"
      "global_load_lds_dwordx4 v12, s[24:25]\n"
      "s_add_u32 m0, m0, 1024\n"
      "v_add_u32 v12, 61440, v12\n"
      "global_load_lds_dwordx4 v12, s[24:25]\n"
      "s_add_u32 m0, m0, 1024\n"
      "v_add_u32 v12, 61440, v12\n"
      "global_load_lds_dwordx4 v12, s[24:25]\n"
      "s_add_u32 m0, m0, 1024\n"
      "v_add_u32 v12, 61440, v12\n"
      "global_load_lds_dwordx4 v12, s[24:25]\n"
      "s_add_u32 m0, m0, 1024\n"
      "v_add_u32 v12, 61440, v12\n"
      "global_load_lds_dwordx4 v12, s[24:25]\n"
      "s_mov_b32 s32, s26\n"
      "s_and_b32 s33, s27, 0xffff\n"
      "s_mov_b32 s34, 27262976\n"
      "s_mov_b32 s35, 0x20000\n"
      "v_lshlrev_b32 v54, 4, v0\n"
      "s_sub_u32 s18, s18, 2048\n"
      "s_subb_u32 s19, s19, 0\n"
      "s_add_u32 s22, s20, 60416\n"
      "s_addc_u32 s23, s21, 0\n"
      "v_mov_b32 v66, 0\n"
      "v_mov_b32 v67, 0\n"
      "v_mov_b32 v72, 0\n"
      "v_mov_b32 v73, 0\n"
      "v_mov_b32 v76, 0\n"
      "v_mov_b32 v77, 0\n"
      "v_mov_b32 v82, 0\n"
      "v_mov_b32 v83, 0\n"
      "v_mov_b32 v88, 0\n"
      "v_mov_b32 v89, 0\n"
      "v_mov_b32 v92, 0\n"
      "v_mov_b32 v93, 0\n"
      "v_mov_b32 v98, 0\n"
      "v_mov_b32 v99, 0\n"
      "v_mov_b32 v104, 0\n"
      "v_mov_b32 v105, 0\n"
      "v_mov_b32 v108, 0\n"
      "v_mov_b32 v109, 0\n"
      "v_mov_b32 v114, 0\n"
      "v_mov_b32 v115, 0\n"
      "v_mov_b32 v120, 0\n"
      "v_mov_b32 v121, 0\n"
      "v_mov_b32 v124, 0\n"
      "v_mov_b32 v125, 0\n"
      "v_mov_b32 v34, 0\n"
      "v_mov_b32 v35, 0\n"
      "v_and_b32 v32, 0x3f803f80, v198\n"
      "v_and_b32 v33, 0x3f803f80, v199\n"
      "v_and_b32 v36, 0x3f803f80, v198\n"
      "v_and_b32 v37, 0x3f803f80, v199\n"
      "s_mov_b32 s48, 0xaaaaaaaa\n"
      "s_mov_b32 s49, s48\n"
      "s_mov_b32 s50, 0xcccccccc\n"
      "s_mov_b32 s51, s50\n"
      "v_mov_b32 v228, 0\n"
      "v_mov_b32 v229, 0\n"
      "v_mov_b32 v230, 0\n"
      "v_mov_b32 v231, 0\n"
      "s_mov_b32 s44, 0x40000\n"
      "s_mov_b32 s46, 0\n"
      "s_mul_i32 s42, s3, 6144\n"
      "s_add_u32 s42, s42, 0x41000\n"
      "s_add_u32 s0, s40, s42\n"
      "s_addc_u32 s1, s41, 0\n"
      "ds_write_b64 v217, v[228:229]\n"
      "ds_write_b128 v218, v[228:231] offset:0\n"
      "ds_write_b128 v218, v[228:231] offset:7680\n"
      "ds_write_b128 v218, v[228:231] offset:15360\n"
      "s_waitcnt lgkmcnt(0)\n"
      "s_add_u32 s30, s30, 1\n"
      "s_addc_u32 s31, s31, 0\n"
      "s_mul_i32 s24, s30, 10\n"
      "v_mov_b32 v52, s30\n"
      "v_mov_b32 v53, s31\n"
      "s_and_b32 s42, s96, 0xffff\n"
      "s_lshl_b32 s43, s3, 11\n"
      "v_mad_u32_u24 v222, v223, s42, v222\n"
      "s_cmp_eq_u32 s3, 3\n"
      "s_cmov_b32 s43, 7680\n"
      "v_add_u32 v222, s95, v222\n"
      "s_add_u32 s43, s43, 49920\n"
      "s_mov_b32 s36, s12\n"
      "s_and_b32 s37, s13, 0xffff\n"
      "s_add_u32 s40, s60, 33792\n"
      "s_add_u32 s41, s60, 67584\n"
      "s_add_u32 s47, s92, 0x100000\n"
      "s_sub_u32 s54, s92, s93\n"
      "s_lshl_b32 s45, s3, 12\n"
      "v_and_b32 v39, s54, v209\n"
      "v_add_u32 v210, s63, v210\n"
      "v_add_u32 v208, v39, v208\n"
      "v_add_u32 v39, 11264, v210\n"
      "s_waitcnt vmcnt(6)\n"
      "ds_read_b64 v[244:245], v197 offset:0\n"
      "ds_read_b64 v[246:247], v197 offset:128\n"
      "ds_read_b64 v[248:249], v197 offset:256\n"
      "ds_read_b64 v[250:251], v197 offset:384\n"
      "ds_read_b64 v[252:253], v197 offset:512\n"
      "ds_read_b64 a[0:1], v196 offset:0\n"
      "ds_read_b64 a[2:3], v196 offset:32\n"
      "ds_read_b64 a[4:5], v196 offset:64\n"
      "ds_read_b64 a[6:7], v196 offset:96\n"
      "ds_read_b64 a[8:9], v196 offset:128\n"
      "ds_read_b64 a[10:11], v196 offset:160\n"
      "ds_read_b64 a[12:13], v196 offset:192\n"
      "ds_read_b64 a[14:15], v196 offset:224\n"
      "ds_read_b64 a[16:17], v196 offset:256\n"
      "ds_read_b64 a[18:19], v196 offset:288\n"
      "s_waitcnt lgkmcnt(10)\n"
      "ds_read_b64 a[20:21], v196 offset:320\n"
      "ds_read_b64 a[22:23], v196 offset:352\n"
      "ds_read_b64 a[24:25], v196 offset:384\n"
      "v_perm_b32 v64, v245, v245, v242\n"
      "v_perm_b32 v65, v245, v245, v243\n"
      "v_perm_b32 v80, v245, v245, v240\n"
      "v_perm_b32 v81, v245, v245, v241\n"
      "ds_read_b64 a[26:27], v196 offset:416\n"
      "ds_read_b64 a[28:29], v196 offset:448\n"
      "ds_read_b64 a[30:31], v196 offset:480\n"
      "v_perm_b32 v68, v247, v247, v242\n"
      "v_perm_b32 v69, v247, v247, v243\n"
      "v_perm_b32 v84, v247, v247, v240\n"
      "v_perm_b32 v85, v247, v247, v241\n"
      "ds_read_b64 a[32:33], v196 offset:512\n"
      "ds_read_b64 a[34:35], v196 offset:544\n"
      "ds_read_b64 a[36:37], v196 offset:576\n"
      "v_perm_b32 v70, v249, v249, v242\n"
      "v_perm_b32 v71, v249, v249, v243\n"
      "v_perm_b32 v86, v249, v249, v240\n"
      "v_perm_b32 v87, v249, v249, v241\n"
      "ds_read_b64 a[38:39], v196 offset:608\n"
      "ds_read_b64 a[40:41], v196 offset:3072\n"
      "ds_read_b64 a[42:43], v196 offset:3104\n"
      "v_perm_b32 v74, v251, v251, v242\n"
      "v_perm_b32 v75, v251, v251, v243\n"
      "v_perm_b32 v90, v251, v251, v240\n"
      "v_perm_b32 v91, v251, v251, v241\n"
      "ds_read_b64 a[44:45], v196 offset:3136\n"
      "ds_read_b64 a[46:47], v196 offset:3168\n"
      "ds_read_b64 a[48:49], v196 offset:3200\n"
      "v_perm_b32 v78, v253, v253, v242\n"
      "v_perm_b32 v79, v253, v253, v243\n"
      "v_perm_b32 v94, v253, v253, v240\n"
      "v_perm_b32 v95, v253, v253, v241\n"
      "s_waitcnt lgkmcnt(14)\n"
      "v_mfma_f32_16x16x32_bf16 v[128:131], a[0:3], v[64:67], 0\n"
      "v_perm_b32 v96, v244, v244, v242\n"
      "v_perm_b32 v97, v244, v244, v243\n"
      "v_mfma_f32_16x16x32_bf16 v[132:135], a[10:13], v[80:83], 0\n"
      "buffer_load_dwordx4 v[8:11], v54, s[36:39], s92 offen\n"
      "v_perm_b32 v112, v244, v244, v240\n"
      "v_perm_b32 v113, v244, v244, v241\n"
      "v_mfma_f32_16x16x32_bf16 v[140:143], a[0:3], v[66:69], 0\n"
      "v_perm_b32 v100, v246, v246, v242\n"
      "v_perm_b32 v101, v246, v246, v243\n"
      "v_mfma_f32_16x16x32_bf16 v[144:147], a[10:13], v[82:85], 0\n"
      "buffer_load_dwordx4 v[14:17], v54, s[36:39], s47 offen\n"
      "v_perm_b32 v116, v246, v246, v240\n"
      "v_perm_b32 v117, v246, v246, v241\n"
      "v_mfma_f32_16x16x32_bf16 v[152:155], a[4:7], v[70:73], 0\n"
      "v_perm_b32 v102, v248, v248, v242\n"
      "v_perm_b32 v103, v248, v248, v243\n"
      "v_mfma_f32_16x16x32_bf16 v[156:159], a[14:17], v[86:89], 0\n"
      "buffer_load_dwordx4 v[20:23], v54, s[36:39], s93 offen\n"
      "v_perm_b32 v118, v248, v248, v240\n"
      "v_perm_b32 v119, v248, v248, v241\n"
      "v_mfma_f32_16x16x32_bf16 v[164:167], a[4:7], v[72:75], 0\n"
      "v_perm_b32 v106, v250, v250, v242\n"
      "v_perm_b32 v107, v250, v250, v243\n"
      "v_mfma_f32_16x16x32_bf16 v[168:171], a[14:17], v[88:91], 0\n"
      "buffer_load_dwordx2 v[26:27], v208, s[36:39], s93 offen\n"
      "v_perm_b32 v122, v250, v250, v240\n"
      "v_perm_b32 v123, v250, v250, v241\n"
      "v_mfma_f32_16x16x32_bf16 v[176:179], a[6:9], v[76:79], 0\n"
      "v_perm_b32 v110, v252, v252, v242\n"
      "v_perm_b32 v111, v252, v252, v243\n"
      "v_mfma_f32_16x16x32_bf16 v[180:183], a[16:19], v[92:95], 0\n"
      "v_perm_b32 v126, v252, v252, v240\n"
      "v_perm_b32 v127, v252, v252, v241\n"
      "s_waitcnt lgkmcnt(0)\n"
      "v_cvt_pk_bf16_f32 v136, v128, v132\n"
      "v_cvt_pk_bf16_f32 v137, v129, v133\n"
      "v_cvt_pk_bf16_f32 v138, v130, v134\n"
      "v_cvt_pk_bf16_f32 v139, v131, v135\n"
      "v_cvt_pk_bf16_f32 v148, v140, v144\n"
      "v_cvt_pk_bf16_f32 v149, v141, v145\n"
      "v_mfma_f32_16x16x32_bf16 v[128:131], v[192:195], v[136:139], 0\n"
      "v_cvt_pk_bf16_f32 v150, v142, v146\n"
      "v_cvt_pk_bf16_f32 v151, v143, v147\n"
      "v_mfma_f32_16x16x32_bf16 v[132:135], a[20:23], v[96:99], 0\n"
      "s_add_u32 m0, s45, 120896\n"
      "s_nop 0\n"
      "global_load_lds_dwordx4 v210, s[16:17]\n"
      "v_cvt_pk_bf16_f32 v160, v152, v156\n"
      "v_cvt_pk_bf16_f32 v161, v153, v157\n"
      "v_mfma_f32_16x16x32_bf16 v[140:143], v[192:195], v[148:151], 0\n"
      "v_cvt_pk_bf16_f32 v162, v154, v158\n"
      "v_cvt_pk_bf16_f32 v163, v155, v159\n"
      "v_mfma_f32_16x16x32_bf16 v[144:147], a[20:23], v[98:101], 0\n"
      "global_load_lds_dwordx4 v39, s[16:17] offset:1024\n"
      "v_cvt_pk_bf16_f32 v172, v164, v168\n"
      "v_cvt_pk_bf16_f32 v173, v165, v169\n"
      "v_mfma_f32_16x16x32_bf16 v[152:155], v[192:195], v[160:163], 0\n"
      "v_cvt_pk_bf16_f32 v174, v166, v170\n"
      "v_cvt_pk_bf16_f32 v175, v167, v171\n"
      "v_mfma_f32_16x16x32_bf16 v[156:159], a[24:27], v[102:105], 0\n"
      "global_load_lds_dwordx4 v210, s[18:19] offset:2048\n"
      "v_cvt_pk_bf16_f32 v184, v176, v180\n"
      "v_cvt_pk_bf16_f32 v185, v177, v181\n"
      "v_mfma_f32_16x16x32_bf16 v[164:167], v[192:195], v[172:175], 0\n"
      "v_cvt_pk_bf16_f32 v186, v178, v182\n"
      "v_cvt_pk_bf16_f32 v187, v179, v183\n"
      "v_mfma_f32_16x16x32_bf16 v[168:171], a[24:27], v[104:107], 0\n"
      "global_load_lds_dwordx4 v39, s[18:19] offset:3072\n"
      "v_cvt_pk_bf16_f32 v136, v128, v132\n"
      "v_cvt_pk_bf16_f32 v137, v129, v133\n"
      "v_mfma_f32_16x16x32_bf16 v[176:179], v[192:195], v[184:187], 0\n"
      "v_cvt_pk_bf16_f32 v138, v130, v134\n"
      "v_cvt_pk_bf16_f32 v139, v131, v135\n"
      "v_mfma_f32_16x16x32_bf16 v[180:183], a[26:29], v[108:111], 0\n"
      "v_cvt_pk_bf16_f32 v148, v140, v144\n"
      "v_cvt_pk_bf16_f32 v149, v141, v145\n"
      "v_mfma_f32_16x16x32_bf16 v[128:131], v[192:195], v[136:139], 0\n"
      "v_cvt_pk_bf16_f32 v150, v142, v146\n"
      "v_cvt_pk_bf16_f32 v151, v143, v147\n"
      "v_mfma_f32_16x16x32_bf16 v[132:135], a[30:33], v[112:115], 0\n"
      "v_cvt_pk_bf16_f32 v160, v152, v156\n"
      "v_cvt_pk_bf16_f32 v161, v153, v157\n"
      "v_mfma_f32_16x16x32_bf16 v[140:143], v[192:195], v[148:151], 0\n"
      "v_cvt_pk_bf16_f32 v162, v154, v158\n"
      "v_cvt_pk_bf16_f32 v163, v155, v159\n"
      "v_mfma_f32_16x16x32_bf16 v[144:147], a[30:33], v[114:117], 0\n"
      "v_cvt_pk_bf16_f32 v172, v164, v168\n"
      "v_cvt_pk_bf16_f32 v173, v165, v169\n"
      "v_mfma_f32_16x16x32_bf16 v[152:155], v[192:195], v[160:163], 0\n"
      "v_cvt_pk_bf16_f32 v174, v166, v170\n"
      "v_cvt_pk_bf16_f32 v175, v167, v171\n"
      "v_mfma_f32_16x16x32_bf16 v[156:159], a[34:37], v[118:121], 0\n"
      "v_cvt_pk_bf16_f32 v184, v176, v180\n"
      "v_cvt_pk_bf16_f32 v185, v177, v181\n"
      "v_mfma_f32_16x16x32_bf16 v[164:167], v[192:195], v[172:175], 0\n"
      "v_cvt_pk_bf16_f32 v186, v178, v182\n"
      "v_cvt_pk_bf16_f32 v187, v179, v183\n"
      "v_mfma_f32_16x16x32_bf16 v[168:171], a[34:37], v[120:123], 0\n"
      "v_cvt_pk_bf16_f32 v136, v128, v132\n"
      "v_cvt_pk_bf16_f32 v137, v129, v133\n"
      "v_mfma_f32_16x16x32_bf16 v[176:179], v[192:195], v[184:187], 0\n"
      "v_cvt_pk_bf16_f32 v138, v130, v134\n"
      "v_cvt_pk_bf16_f32 v139, v131, v135\n"
      "v_mfma_f32_16x16x32_bf16 v[180:183], a[36:39], v[124:127], 0\n"
      "v_cvt_pk_bf16_f32 v148, v140, v144\n"
      "v_cvt_pk_bf16_f32 v149, v141, v145\n"
      "v_mfma_f32_16x16x32_bf16 v[128:131], v[192:195], v[136:139], 0\n"
      "v_cvt_pk_bf16_f32 v150, v142, v146\n"
      "v_cvt_pk_bf16_f32 v151, v143, v147\n"
      "v_mfma_f32_16x16x32_bf16 v[132:135], a[40:43], v[32:35], 0\n"
      "v_cvt_pk_bf16_f32 v160, v152, v156\n"
      "v_cvt_pk_bf16_f32 v161, v153, v157\n"
      "v_mfma_f32_16x16x32_bf16 v[140:143], v[192:195], v[148:151], 0\n"
      "v_cvt_pk_bf16_f32 v162, v154, v158\n"
      "v_cvt_pk_bf16_f32 v163, v155, v159\n"
      "v_mfma_f32_16x16x32_bf16 v[144:147], a[40:43], v[34:37], 0\n"
      "v_cvt_pk_bf16_f32 v172, v164, v168\n"
      "v_cvt_pk_bf16_f32 v173, v165, v169\n"
      "v_mfma_f32_16x16x32_bf16 v[152:155], v[192:195], v[160:163], 0\n"
      "v_cvt_pk_bf16_f32 v174, v166, v170\n"
      "v_cvt_pk_bf16_f32 v175, v167, v171\n"
      "v_mfma_f32_16x16x32_bf16 v[156:159], a[44:47], v[32:35], 0\n"
      "v_cvt_pk_bf16_f32 v184, v176, v180\n"
      "v_cvt_pk_bf16_f32 v185, v177, v181\n"
      "v_mfma_f32_16x16x32_bf16 v[164:167], v[192:195], v[172:175], 0\n"
      "v_cvt_pk_bf16_f32 v186, v178, v182\n"
      "v_cvt_pk_bf16_f32 v187, v179, v183\n"
      "v_mfma_f32_16x16x32_bf16 v[168:171], a[44:47], v[34:37], 0\n"
      "L_pg0_%=:\n"
      "s_cbranch_execz L_pg4_%=\n"
      "v_cvt_pk_bf16_f32 v128, 0, v128\n"
      "v_cvt_pk_bf16_f32 v129, 0, v129\n"
      "v_mfma_f32_16x16x32_bf16 v[176:179], v[192:195], v[184:187], 0\n"
      "v_cvt_pk_bf16_f32 v130, 0, v130\n"
      "v_cvt_pk_bf16_f32 v131, 0, v131\n"
      "v_mfma_f32_16x16x32_bf16 v[180:183], a[46:49], v[34:37], 0\n"
      "v_mul_f32 v128, v128, v132\n"
      "v_mul_f32 v129, v129, v133\n"
      "v_mul_f32 v130, v130, v134\n"
      "v_mul_f32 v131, v131, v135\n"
      "v_cvt_pk_bf16_f32 v136, v131, v130\n"
      "v_cvt_pk_bf16_f32 v137, v129, v128\n"
      "v_cvt_pk_bf16_f32 v140, 0, v140\n"
      "v_cvt_pk_bf16_f32 v141, 0, v141\n"
      "v_cvt_pk_bf16_f32 v142, 0, v142\n"
      "v_cvt_pk_bf16_f32 v143, 0, v143\n"
      "ds_write_b64 v200, v[136:137] offset:0\n"
      "v_mul_f32 v140, v140, v144\n"
      "v_mul_f32 v141, v141, v145\n"
      "v_mul_f32 v142, v142, v146\n"
      "v_mul_f32 v143, v143, v147\n"
      "v_cvt_pk_bf16_f32 v148, v143, v142\n"
      "v_cvt_pk_bf16_f32 v149, v141, v140\n"
      "v_cvt_pk_bf16_f32 v152, 0, v152\n"
      "v_cvt_pk_bf16_f32 v153, 0, v153\n"
      "v_cvt_pk_bf16_f32 v154, 0, v154\n"
      "v_cvt_pk_bf16_f32 v155, 0, v155\n"
      "ds_write_b64 v200, v[148:149] offset:2304\n"
      "v_mul_f32 v152, v152, v156\n"
      "v_mul_f32 v153, v153, v157\n"
      "v_mul_f32 v154, v154, v158\n"
      "v_mul_f32 v155, v155, v159\n"
      "v_cvt_pk_bf16_f32 v160, v155, v154\n"
      "v_cvt_pk_bf16_f32 v161, v153, v152\n"
      "v_cvt_pk_bf16_f32 v164, 0, v164\n"
      "v_cvt_pk_bf16_f32 v165, 0, v165\n"
      "v_cvt_pk_bf16_f32 v166, 0, v166\n"
      "v_cvt_pk_bf16_f32 v167, 0, v167\n"
      "ds_write_b64 v200, v[160:161] offset:4608\n"
      "v_mul_f32 v164, v164, v168\n"
      "v_mul_f32 v165, v165, v169\n"
      "v_mul_f32 v166, v166, v170\n"
      "v_mul_f32 v167, v167, v171\n"
      "v_cvt_pk_bf16_f32 v172, v167, v166\n"
      "v_cvt_pk_bf16_f32 v173, v165, v164\n"
      "v_cvt_pk_bf16_f32 v176, 0, v176\n"
      "v_cvt_pk_bf16_f32 v177, 0, v177\n"
      "v_cvt_pk_bf16_f32 v178, 0, v178\n"
      "v_cvt_pk_bf16_f32 v179, 0, v179\n"
      "ds_write_b64 v200, v[172:173] offset:6912\n"
      "v_mul_f32 v176, v176, v180\n"
      "v_mul_f32 v177, v177, v181\n"
      "v_mul_f32 v178, v178, v182\n"
      "v_mul_f32 v179, v179, v183\n"
      "v_cvt_pk_bf16_f32 v184, v179, v178\n"
      "v_cvt_pk_bf16_f32 v185, v177, v176\n"
      "ds_write_b64 v200, v[184:185] offset:9216\n"
      "s_waitcnt lgkmcnt(0)\n"
      "s_barrier\n"
      "ds_read_b128 v[64:67], v201 offset:0\n"
      "ds_read_b128 v[68:71], v201 offset:1152\n"
      "ds_read_b128 v[72:75], v201 offset:2304\n"
      "s_waitcnt lgkmcnt(2)\n"
      "buffer_store_dwordx4 v[64:67], v202, s[32:35], s60 offen offset:0 sc1\n"
      "v_add3_u32 v76, v64, v65, v66\n"
      "v_add_u32 v76, v76, v67\n"
      "s_waitcnt lgkmcnt(1)\n"
      "buffer_store_dwordx4 v[68:71], v202, s[32:35], s40 offen offset:0 sc1\n"
      "v_add3_u32 v77, v68, v69, v70\n"
      "v_add_u32 v77, v77, v71\n"
      "s_waitcnt lgkmcnt(0)\n"
      "s_mov_b32 exec_hi, 0\n"
      "buffer_store_dwordx4 v[72:75], v202, s[32:35], s41 offen offset:0 sc1\n"
      "s_mov_b32 exec_hi, -1\n"
      "v_add3_u32 v78, v72, v73, v74\n"
      "v_add_u32 v78, v78, v75\n"
      "v_add_u32_dpp v76, v76, v76 quad_perm:[1,0,3,2] row_mask:0xf bank_mask:0xf\n"
      "v_add_u32_dpp v77, v77, v77 quad_perm:[1,0,3,2] row_mask:0xf bank_mask:0xf\n"
      "v_add_u32_dpp v78, v78, v78 quad_perm:[1,0,3,2] row_mask:0xf bank_mask:0xf\n"
      "v_add_u32_dpp v76, v76, v76 quad_perm:[2,3,0,1] row_mask:0xf bank_mask:0xf\n"
      "v_add_u32_dpp v77, v77, v77 quad_perm:[2,3,0,1] row_mask:0xf bank_mask:0xf\n"
      "v_add_u32_dpp v78, v78, v78 quad_perm:[2,3,0,1] row_mask:0xf bank_mask:0xf\n"
      "v_add_u32_dpp v76, v76, v76 row_half_mirror row_mask:0xf bank_mask:0xf\n"
      "v_add_u32_dpp v77, v77, v77 row_half_mirror row_mask:0xf bank_mask:0xf\n"
      "v_add_u32_dpp v78, v78, v78 row_half_mirror row_mask:0xf bank_mask:0xf\n"
      "v_cndmask_b32 v76, v76, v77, s[48:49]\n"
      "v_cndmask_b32 v76, v76, v78, s[50:51]\n"
      "v_sub_u32 v76, s30, v76\n"
      "buffer_store_dword v76, v203, s[32:35], s61 offen offset:0 sc1\n"
      "s_waitcnt vmcnt(12)\n"
      "v_lshlrev_b32 v38, 16, v235\n"
      "v_mul_f32 v172, 0xbfb8aa3b, v38\n"
      "v_exp_f32 v172, v172\n"
      "s_nop 0\n"
      "v_add_f32 v172, 1.0, v172\n"
      "v_rcp_f32 v172, v172\n"
      "s_nop 0\n"
      "v_mul_f32 v38, v38, v172\n"
      "v_cvt_pk_bf16_f32 v38, v38, v38\n"
      "ds_write_b16 v204, v38\n"
      "s_waitcnt lgkmcnt(0)\n"
      "s_barrier\n"
      "ds_read_b128 v[144:147], v206 offset:0\n"
      "ds_read_b64_tr_b16 v[148:149], v205 offset:0\n"
      "ds_read_b64_tr_b16 v[150:151], v205 offset:2048\n"
      "ds_read_b128 v[152:155], v206 offset:64\n"
      "ds_read_b64_tr_b16 v[156:157], v205 offset:4096\n"
      "ds_read_b64_tr_b16 v[158:159], v205 offset:6144\n"
      "ds_read_b128 v[160:163], v206 offset:128\n"
      "ds_read_b64_tr_b16 v[164:165], v205 offset:8192\n"
      "ds_read_b64_tr_b16 v[166:167], v205 offset:10240\n"
      "ds_read_b128 v[168:171], v206 offset:192\n"
      "ds_read_b64_tr_b16 v[172:173], v205 offset:12288\n"
      "ds_read_b64_tr_b16 v[174:175], v205 offset:14336\n"
      "ds_read_b128 v[176:179], v206 offset:256\n"
      "ds_read_b64_tr_b16 v[180:181], v205 offset:16384\n"
      "ds_read_b64_tr_b16 v[182:183], v205 offset:18432\n"
      "s_waitcnt lgkmcnt(12)\n"
      "v_mfma_f32_16x16x32_bf16 v[184:187], v[144:147], v[148:151], 0\n"
      "s_waitcnt lgkmcnt(9)\n"
      "v_mfma_f32_16x16x32_bf16 v[184:187], v[152:155], v[156:159], v[184:187]\n"
      "s_waitcnt lgkmcnt(6)\n"
      "v_mfma_f32_16x16x32_bf16 v[184:187], v[160:163], v[164:167], v[184:187]\n"
      "s_waitcnt lgkmcnt(3)\n"
      "v_mfma_f32_16x16x32_bf16 v[184:187], v[168:171], v[172:175], v[184:187]\n"
      "s_waitcnt lgkmcnt(0)\n"
      "v_mfma_f32_16x16x32_bf16 v[184:187], v[176:179], v[180:183], v[184:187]\n"
      "s_nop 7\n"
      "v_mov_b32 v185, s30\n"
      "buffer_store_dwordx2 v[184:185], v207, s[32:35], s62 offen offset:0 sc1\n"
      "s_mul_i32 s42, s3, 2560\n"
      "s_add_u32 m0, s42, 110656\n"
      "s_mov_b32 s36, s4\n"
      "s_and_b32 s37, s5, 0xffff\n"
      "buffer_load_dwordx4 v232, s[36:39], s52 offen lds\n"
      "s_add_u32 m0, m0, 1024\n"
      "s_nop 0\n"
      "buffer_load_dwordx4 v233, s[36:39], s55 offen lds\n"
      "s_add_u32 m0, m0, 1024\n"
      "s_mov_b32 exec_hi, 0\n"
      "buffer_load_dwordx4 v234, s[36:39], s55 offen lds\n"
      "s_mov_b32 exec_hi, -1\n"
      "buffer_load_dwordx4 v[112:115], v211, s[36:39], s66 offen\n"
      "buffer_load_dwordx4 v[116:119], v211, s[36:39], s66 offen offset:512\n"
      "v_bfe_u32 v2, v54, 1, 8\n"
      "v_sub_u32 v2, v211, v2\n"
      "buffer_load_dwordx2 v[120:121], v2, s[36:39], s66 offen offset:1024\n"
      "s_and_b32 s43, s2, 7\n"
      "s_lshr_b32 s42, s2, 3\n"
      "s_lshl_b32 s47, s43, 5\n"
      "s_add_u32 s42, s47, s42\n"
      "s_and_b32 s54, s42, -4\n"
      "s_lshl_b32 s47, s42, 1\n"
      "s_and_b32 s47, s47, -4\n"
      "s_load_dword s53, s[14:15], s54\n"
      "s_load_dword s58, s[14:15], s47 offset:0x100\n"
      "s_load_dword s59, s[14:15], s47 offset:0x300\n"
      "s_min_u32 s54, s3, 2\n"
      "s_lshl_b32 s54, s54, 10\n"
      "s_lshr_b32 s10, s42, 1\n"
      "s_lshl_b32 s47, s42, 12\n"
      "s_lshl_b32 s10, s10, 12\n"
      "s_add_u32 s47, s47, s54\n"
      "s_add_u32 s54, s54, s10\n"
      "s_load_dwordx2 s[6:7], s[12:13], s47 offset:0x80400\n"
      "s_add_u32 s47, s47, 0x100000\n"
      "s_load_dwordx2 s[10:11], s[12:13], s54 offset:0x400\n"
      "s_load_dwordx2 s[8:9], s[12:13], s47 offset:0x80400\n"
      "global_load_dwordx4 v[40:43], v7, s[0:1] offset:-4096\n"
      "global_load_dwordx4 v[44:47], v7, s[0:1] offset:-3072\n"
      "global_load_dwordx4 v[48:51], v7, s[0:1] offset:-2048\n"
      "global_load_dwordx4 v[56:59], v7, s[0:1] offset:-1024\n"
      "global_load_dwordx4 v[60:63], v7, s[0:1] offset:0\n"
      "global_load_dwordx4 v[28:31], v7, s[0:1] offset:1024\n"
      "s_mov_b64 exec, 0\n"
      "s_cmp_eq_u32 s3, 0\n"
      "s_cbranch_scc1 L_pg0_%=\n"
      "s_cmp_eq_u32 s3, 1\n"
      "s_cbranch_scc1 L_pg1_%=\n"
      "s_cmp_eq_u32 s3, 2\n"
      "s_cbranch_scc1 L_pg2_%=\n"
      "s_branch L_pg3_%=\n"
      "L_pgr_%=:\n"
      "s_mov_b64 exec, -1\n"
      "s_waitcnt vmcnt(6)\n"
      "s_waitcnt lgkmcnt(0)\n"
      "s_barrier\n"
      "ds_read_b64_tr_b16 v[64:65], v224 offset:0\n"
      "ds_read_b64_tr_b16 v[66:67], v224 offset:640\n"
      "ds_read_b64_tr_b16 v[68:69], v224 offset:5120\n"
      "ds_read_b64_tr_b16 v[70:71], v224 offset:5760\n"
      "ds_read_b64_tr_b16 v[72:73], v225 offset:0\n"
      "ds_read_b64_tr_b16 v[74:75], v225 offset:640\n"
      "ds_read_b64_tr_b16 v[144:145], v219 offset:0\n"
      "ds_read_b64_tr_b16 v[146:147], v219 offset:8192\n"
      "ds_read_b64_tr_b16 v[148:149], v219 offset:2048\n"
      "ds_read_b64_tr_b16 v[150:151], v219 offset:10240\n"
      "ds_read_b64_tr_b16 v[152:153], v213 offset:0\n"
      "ds_read_b64_tr_b16 v[154:155], v213 offset:8192\n"
      "ds_read_b64_tr_b16 v[156:157], v213 offset:2048\n"
      "ds_read_b64_tr_b16 v[158:159], v213 offset:10240\n"
      "v_mov_b32 v12, s6\n"
      "v_mov_b32 v13, s7\n"
      "v_mov_b32 v18, s8\n"
      "v_mov_b32 v19, s9\n"
      "v_mov_b32 v24, s10\n"
      "v_mov_b32 v25, s11\n"
      "v_and_b32 v188, 48, v7\n"
      "v_xor_b32 v188, 48, v188\n"
      "v_lshlrev_b64 v[26:27], v188, v[26:27]\n"
      "v_mov_b32_dpp v12, v8 wave_shl:1 row_mask:0xf bank_mask:0xf\n"
      "v_mov_b32_dpp v13, v9 wave_shl:1 row_mask:0xf bank_mask:0xf\n"
      "v_mov_b32_dpp v18, v14 wave_shl:1 row_mask:0xf bank_mask:0xf\n"
      "v_mov_b32_dpp v19, v15 wave_shl:1 row_mask:0xf bank_mask:0xf\n"
      "v_mov_b32_dpp v24, v20 wave_shl:1 row_mask:0xf bank_mask:0xf\n"
      "v_mov_b32_dpp v25, v21 wave_shl:1 row_mask:0xf bank_mask:0xf\n"
      "v_mov_b32 v122, 0\n"
      "v_mov_b32 v123, 0\n"
      "v_mov_b32 v96, 0x3f803f80\n"
      "v_dot2c_f32_bf16 v122, v112, v96\n"
      "v_dot2c_f32_bf16 v123, v112, v112\n"
      "v_dot2c_f32_bf16 v122, v113, v96\n"
      "v_dot2c_f32_bf16 v123, v113, v113\n"
      "v_dot2c_f32_bf16 v122, v114, v96\n"
      "v_dot2c_f32_bf16 v123, v114, v114\n"
      "v_dot2c_f32_bf16 v122, v115, v96\n"
      "v_dot2c_f32_bf16 v123, v115, v115\n"
      "v_dot2c_f32_bf16 v122, v116, v96\n"
      "v_dot2c_f32_bf16 v123, v116, v116\n"
      "v_dot2c_f32_bf16 v122, v117, v96\n"
      "v_dot2c_f32_bf16 v123, v117, v117\n"
      "v_dot2c_f32_bf16 v122, v118, v96\n"
      "v_dot2c_f32_bf16 v123, v118, v118\n"
      "v_dot2c_f32_bf16 v122, v119, v96\n"
      "v_dot2c_f32_bf16 v123, v119, v119\n"
      "v_dot2c_f32_bf16 v122, v120, v96\n"
      "v_dot2c_f32_bf16 v123, v120, v120\n"
      "v_dot2c_f32_bf16 v122, v121, v96\n"
      "v_dot2c_f32_bf16 v123, v121, v121\n"
      "s_lshr_b32 s40, s92, 12\n"
      "s_lshr_b32 s41, s93, 12\n"
      "s_mulk_i32 s40, 4224\n"
      "s_mulk_i32 s41, 4224\n"
      "s_add_u32 s40, s40, 0x100000\n"
      "s_add_u32 s41, s41, 0x100000\n"
      "buffer_load_dwordx4 v[128:131], v54, s[32:35], s40 offen sc1\n"
      "s_add_u32 s40, s40, 1081344\n"
      "buffer_load_dwordx4 v[136:139], v54, s[32:35], s41 offen sc1\n"
      "buffer_load_dwordx4 v[132:135], v54, s[32:35], s40 offen sc1\n"
      "buffer_load_dword v140, v237, s[32:35], s70 offen sc1\n"
      "buffer_load_dword v141, v237, s[32:35], s98 offen sc1\n"
      "buffer_load_dword v142, v237, s[32:35], s71 offen sc1\n"
      "s_waitcnt lgkmcnt(0)\n"
      "ds_write_b128 v54, v[10:13] offset:12544\n"
      "v_alignbit_b32 v160, v9, v8, 16\n"
      "v_alignbit_b32 v161, v10, v9, 16\n"
      "v_alignbit_b32 v162, v11, v10, 16\n"
      "v_alignbit_b32 v163, v12, v11, 16\n"
      "s_mov_b32 vcc_lo, 0x33333333\n"
      "s_mov_b32 vcc_hi, 0x33333333\n"
      "v_cndmask_b32_dpp v80, v148, v144, vcc quad_perm:[0,1,0,1] row_mask:0xf bank_mask:0xf\n"
      "v_cndmask_b32_dpp v81, v149, v145, vcc quad_perm:[0,1,0,1] row_mask:0xf bank_mask:0xf\n"
      "v_cndmask_b32_dpp v82, v150, v146, vcc quad_perm:[0,1,0,1] row_mask:0xf bank_mask:0xf\n"
      "v_cndmask_b32_dpp v83, v151, v147, vcc quad_perm:[0,1,0,1] row_mask:0xf bank_mask:0xf\n"
      "ds_write_b128 v54, v[160:163] offset:64\n"
      "v_alignbit_b32 v164, v10, v9, 16\n"
      "v_alignbit_b32 v165, v11, v10, 16\n"
      "v_alignbit_b32 v166, v12, v11, 16\n"
      "v_alignbit_b32 v167, v13, v12, 16\n"
      "v_cndmask_b32_dpp v88, v156, v152, vcc quad_perm:[0,1,0,1] row_mask:0xf bank_mask:0xf\n"
      "v_cndmask_b32_dpp v89, v157, v153, vcc quad_perm:[0,1,0,1] row_mask:0xf bank_mask:0xf\n"
      "v_cndmask_b32_dpp v90, v158, v154, vcc quad_perm:[0,1,0,1] row_mask:0xf bank_mask:0xf\n"
      "v_cndmask_b32_dpp v91, v159, v155, vcc quad_perm:[0,1,0,1] row_mask:0xf bank_mask:0xf\n"
      "s_not_b64 vcc, vcc\n"
      "v_cndmask_b32_dpp v84, v144, v148, vcc quad_perm:[2,3,2,3] row_mask:0xf bank_mask:0xf\n"
      "ds_write_b128 v54, v[164:167] offset:8384\n"
      "v_mov_b32 v168, v9\n"
      "v_mov_b32 v169, v10\n"
      "v_mov_b32 v170, v11\n"
      "v_mov_b32 v171, v12\n"
      "v_cndmask_b32_dpp v85, v145, v149, vcc quad_perm:[2,3,2,3] row_mask:0xf bank_mask:0xf\n"
      "v_cndmask_b32_dpp v86, v146, v150, vcc quad_perm:[2,3,2,3] row_mask:0xf bank_mask:0xf\n"
      "v_cndmask_b32_dpp v87, v147, v151, vcc quad_perm:[2,3,2,3] row_mask:0xf bank_mask:0xf\n"
      "v_cndmask_b32_dpp v92, v152, v156, vcc quad_perm:[2,3,2,3] row_mask:0xf bank_mask:0xf\n"
      "v_cndmask_b32_dpp v93, v153, v157, vcc quad_perm:[2,3,2,3] row_mask:0xf bank_mask:0xf\n"
      "v_cndmask_b32_dpp v94, v154, v158, vcc quad_perm:[2,3,2,3] row_mask:0xf bank_mask:0xf\n"
      "ds_write_b128 v54, v[168:171] offset:4224\n"
      "v_cndmask_b32_dpp v95, v155, v159, vcc quad_perm:[2,3,2,3] row_mask:0xf bank_mask:0xf\n"
      "buffer_store_dwordx4 v[80:83], v7, s[32:35], s72 offen offset:0 sc1\n"
      "buffer_store_dwordx4 v[84:87], v7, s[32:35], s73 offen offset:0 sc1\n"
      "buffer_store_dwordx4 v[88:91], v7, s[32:35], s74 offen offset:0 sc1\n"
      "buffer_store_dwordx4 v[92:95], v7, s[32:35], s75 offen offset:0 sc1\n"
      "v_add3_u32 v76, v64, v65, v66\n"
      "ds_write_b128 v54, v[16:19] offset:29184\n"
      "v_alignbit_b32 v172, v15, v14, 16\n"
      "v_alignbit_b32 v173, v16, v15, 16\n"
      "v_alignbit_b32 v174, v17, v16, 16\n"
      "v_alignbit_b32 v175, v18, v17, 16\n"
      "v_add_u32 v76, v76, v67\n"
      "v_add3_u32 v77, v68, v69, v70\n"
      "v_add_f32_dpp v122, v122, v122 quad_perm:[1,0,3,2] row_mask:0xf bank_mask:0xf\n"
      "v_add_u32 v77, v77, v71\n"
      "v_add_f32_dpp v123, v123, v123 quad_perm:[1,0,3,2] row_mask:0xf bank_mask:0xf\n"
      "v_add3_u32 v78, v72, v73, v74\n"
      "ds_write_b128 v54, v[172:175] offset:16704\n"
      "v_alignbit_b32 v176, v16, v15, 16\n"
      "v_alignbit_b32 v177, v17, v16, 16\n"
      "v_alignbit_b32 v178, v18, v17, 16\n"
      "v_alignbit_b32 v179, v19, v18, 16\n"
      "v_add_f32_dpp v122, v122, v122 quad_perm:[2,3,0,1] row_mask:0xf bank_mask:0xf\n"
      "v_add_u32 v78, v78, v75\n"
      "v_add_f32_dpp v123, v123, v123 quad_perm:[2,3,0,1] row_mask:0xf bank_mask:0xf\n"
      "v_mov_b32 v79, v78\n"
      "v_add_f32_dpp v122, v122, v122 row_half_mirror row_mask:0xf bank_mask:0xf\n"
      "v_permlane32_swap_b32 v76, v77\n"
      "ds_write_b128 v54, v[176:179] offset:25024\n"
      "v_mov_b32 v180, v15\n"
      "v_mov_b32 v181, v16\n"
      "v_mov_b32 v182, v17\n"
      "v_mov_b32 v183, v18\n"
      "v_add_f32_dpp v123, v123, v123 row_half_mirror row_mask:0xf bank_mask:0xf\n"
      "v_permlane32_swap_b32 v78, v79\n"
      "v_add_f32_dpp v122, v122, v122 row_mirror row_mask:0xf bank_mask:0xf\n"
      "v_add_u32 v76, v76, v77\n"
      "v_add_f32_dpp v123, v123, v123 row_mirror row_mask:0xf bank_mask:0xf\n"
      "v_add_u32 v78, v78, v79\n"
      "ds_write_b128 v54, v[180:183] offset:20864\n"
      "v_mov_b32 v96, v122\n"
      "v_permlane16_swap_b32 v76, v78\n"
      "v_mov_b32 v97, v123\n"
      "v_add_u32 v76, v76, v78\n"
      "v_permlane16_swap_b32 v122, v96\n"
      "v_sub_u32 v76, s30, v76\n"
      "ds_write_b128 v54, v[22:25] offset:45824\n"
      "v_alignbit_b32 v160, v21, v20, 16\n"
      "v_alignbit_b32 v161, v22, v21, 16\n"
      "v_alignbit_b32 v162, v23, v22, 16\n"
      "v_alignbit_b32 v163, v24, v23, 16\n"
      "v_permlane16_swap_b32 v123, v97\n"
      "v_add_f32 v122, v122, v96\n"
      "v_add_f32 v123, v123, v97\n"
      "v_mul_f32 v122, 0x3acccccd, v122\n"
      "v_mul_f32 v123, 0x3acccccd, v123\n"
      "v_fma_f32 v123, -v122, v122, v123\n"
      "ds_write_b128 v54, v[160:163] offset:33344\n"
      "v_alignbit_b32 v164, v22, v21, 16\n"
      "v_alignbit_b32 v165, v23, v22, 16\n"
      "v_alignbit_b32 v166, v24, v23, 16\n"
      "v_alignbit_b32 v167, v25, v24, 16\n"
      "v_max_f32 v123, 0, v123\n"
      "v_cvt_pk_bf16_f32 v123, 0, v123\n"
      "v_add_f32 v123, 0x3727c5ac, v123\n"
      "v_cvt_pk_bf16_f32 v123, 0, v123\n"
      "v_sqrt_f32 v123, v123\n"
      "v_readlane_b32 s42, v122, 32\n"
      "ds_write_b128 v54, v[164:167] offset:41664\n"
      "v_mov_b32 v168, v21\n"
      "v_mov_b32 v169, v22\n"
      "v_mov_b32 v170, v23\n"
      "v_mov_b32 v171, v24\n"
      "v_cvt_pk_bf16_f32 v123, 0, v123\n"
      "v_rcp_f32 v96, v123\n"
      "v_mov_b32 v99, s30\n"
      "v_cvt_pk_bf16_f32 v98, v122, s42\n"
      "v_readlane_b32 s42, v96, 32\n"
      "s_nop 1\n"
      "v_mov_b32 v97, s42\n"
      "ds_write_b128 v54, v[168:171] offset:37504\n"
      "buffer_store_dwordx4 v[96:99], v214, s[32:35], s67 offen offset:0 sc1\n"
      "buffer_store_dwordx4 v[64:67], v226, s[32:35], s64 offen offset:0 sc1\n"
      "buffer_store_dwordx4 v[68:71], v226, s[32:35], s64 offen offset:64 sc1\n"
      "buffer_store_dwordx4 v[72:75], v227, s[32:35], s64 offen offset:0 sc1\n"
      "buffer_store_dword v76, v215, s[32:35], s65 offen offset:0 sc1\n"
      "ds_write_b64 v216, v[26:27]\n"
      "s_waitcnt vmcnt(9)\n"
      "v_add3_u32 v143, v128, v129, v130\n"
      "v_add3_u32 v143, v143, v131, v132\n"
      "v_add3_u32 v143, v143, v133, v134\n"
      "v_add3_u32 v143, v143, v135, v136\n"
      "v_add3_u32 v143, v143, v137, v138\n"
      "v_add_u32 v143, v143, v139\n"
      "v_add3_u32 v140, v140, v141, v142\n"
      "s_mul_i32 s47, s30, 3\n"
      "v_add_u32_dpp v143, v143, v143 quad_perm:[1,0,3,2] row_mask:0xf bank_mask:0xf\n"
      "s_nop 1\n"
      "v_add_u32_dpp v143, v143, v143 quad_perm:[2,3,0,1] row_mask:0xf bank_mask:0xf\n"
      "s_nop 1\n"
      "v_add_u32_dpp v143, v143, v143 row_half_mirror row_mask:0xf bank_mask:0xf\n"
      "v_add_u32 v143, v143, v140\n"
      "v_cmp_ne_u32 vcc, s47, v143\n"
      "s_cbranch_vccnz L_psx_%=\n"
      "L_pbx_%=:\n"
      "ds_write_b128 v238, v[128:131] offset:0\n"
      "ds_write_b128 v238, v[132:135] offset:7680\n"
      "ds_write_b128 v238, v[136:139] offset:15360\n"
      "s_mul_i32 s42, s3, 2048\n"
      "s_add_u32 m0, s42, 137280\n"
      "s_waitcnt lgkmcnt(0)\n"
      "s_barrier\n"
      "s_cmp_lg_u32 s99, 0\n"
      "s_cbranch_scc1 L_cv_b%=\n"
      "ds_read_b128 v[160:163], v43 offset:4608\n"
      "ds_read_b64 v[112:113], v42 offset:0\n"
      "ds_read_b64 v[114:115], v42 offset:8\n"
      "ds_read_b64 v[148:149], v42 offset:3072\n"
      "ds_read_b64 v[150:151], v42 offset:3080\n"
      "ds_read_b128 v[172:175], v43 offset:0\n"
      "ds_read_b128 v[168:171], v43 offset:1536\n"
      "ds_read_b64 v[124:125], v42 offset:1024\n"
      "ds_read_b64 v[126:127], v42 offset:1032\n"
      "ds_read_b128 v[164:167], v43 offset:3072\n"
      "ds_read_b64 v[136:137], v42 offset:2048\n"
      "ds_read_b64 v[138:139], v42 offset:2056\n"
      "ds_read_b64 v[116:117], v42 offset:32\n"
      "ds_read_b64 v[118:119], v42 offset:40\n"
      "ds_read_b64 v[128:129], v42 offset:1056\n"
      "ds_read_b64 v[130:131], v42 offset:1064\n"
      "ds_read_b64 v[140:141], v42 offset:2080\n"
      "ds_read_b64 v[142:143], v42 offset:2088\n"
      "ds_read_b64 v[152:153], v42 offset:3104\n"
      "ds_read_b64 v[154:155], v42 offset:3112\n"
      "s_waitcnt lgkmcnt(8)\n"
      "v_mfma_f32_16x16x32_bf16 v[96:99], v[112:115], v[160:163], 0\n"
      "ds_read_b128 v[176:179], v43 offset:4640\n"
      "ds_read_b128 v[180:183], v43 offset:3104\n"
      "v_mov_b64 v[8:9], v[80:81]\n"
      "v_mfma_f32_16x16x32_bf16 v[104:107], v[148:151], v[160:163], 0\n"
      "ds_read_b128 v[184:187], v43 offset:1568\n"
      "v_mov_b64 v[10:11], v[82:83]\n"
      "v_mfma_f32_16x16x32_bf16 v[104:107], v[112:115], v[172:175], v[104:107]\n"
      "ds_read_b128 v[188:191], v43 offset:32\n"
      "v_mov_b64 v[12:13], v[84:85]\n"
      "v_mfma_f32_16x16x32_bf16 v[104:107], v[124:127], v[168:171], v[104:107]\n"
      "ds_read_b64 v[120:121], v42 offset:64\n"
      "ds_read_b64 v[122:123], v42 offset:72\n"
      "v_mov_b64 v[14:15], v[86:87]\n"
      "v_mfma_f32_16x16x32_bf16 v[104:107], v[136:139], v[164:167], v[104:107]\n"
      "ds_read_b64 v[132:133], v42 offset:1088\n"
      "s_waitcnt lgkmcnt(7)\n"
      "v_mfma_f32_16x16x32_bf16 v[100:103], v[116:119], v[160:163], 0\n"
      "ds_read_b64 v[134:135], v42 offset:1096\n"
      "v_mov_b64 v[16:17], v[88:89]\n"
      "v_mfma_f32_16x16x32_bf16 v[108:111], v[152:155], v[160:163], 0\n"
      "ds_read_b64 v[144:145], v42 offset:2112\n"
      "v_mov_b64 v[18:19], v[90:91]\n"
      "v_mfma_f32_16x16x32_bf16 v[108:111], v[116:119], v[172:175], v[108:111]\n"
      "ds_read_b64 v[146:147], v42 offset:2120\n"
      "v_mov_b64 v[20:21], v[92:93]\n"
      "v_mfma_f32_16x16x32_bf16 v[108:111], v[128:131], v[168:171], v[108:111]\n"
      "ds_read_b64 v[156:157], v42 offset:3136\n"
      "v_mov_b64 v[22:23], v[94:95]\n"
      "v_mfma_f32_16x16x32_bf16 v[108:111], v[140:143], v[164:167], v[108:111]\n"
      "ds_read_b64 v[158:159], v42 offset:3144\n"
      "s_waitcnt lgkmcnt(8)\n"
      "v_mfma_f32_16x16x32_bf16 v[96:99], v[116:119], v[176:179], v[96:99]\n"
      "ds_read_b128 v[160:163], v43 offset:4704\n"
      "ds_read_b128 v[164:167], v43 offset:3168\n"
      "s_waitcnt vmcnt(5)\n"
      "buffer_store_dwordx2 v[52:53], v48, s[32:35], s79 offen sc1\n"
      "v_mov_b32 v2, v212\n"
      "v_mfma_f32_16x16x32_bf16 v[104:107], v[152:155], v[176:179], v[104:107]\n"
      "ds_read_b128 v[168:171], v43 offset:1632\n"
      "v_mov_b32 v3, v220\n"
      "v_mfma_f32_16x16x32_bf16 v[104:107], v[116:119], v[188:191], v[104:107]\n"
      "ds_read_b128 v[172:175], v43 offset:96\n"
      "s_mov_b32 s45, s68\n"
      "v_mfma_f32_16x16x32_bf16 v[104:107], v[128:131], v[184:187], v[104:107]\n"
      "ds_read_b64 v[112:113], v42 offset:96\n"
      "ds_read_b64 v[114:115], v42 offset:104\n"
      "s_lshl_b32 s42, s99, 4\n"
      "v_mfma_f32_16x16x32_bf16 v[104:107], v[140:143], v[180:183], v[104:107]\n"
      "ds_read_b64 v[124:125], v42 offset:1120\n"
      "s_waitcnt lgkmcnt(7)\n"
      "v_mfma_f32_16x16x32_bf16 v[100:103], v[120:123], v[176:179], v[100:103]\n"
      "ds_read_b64 v[126:127], v42 offset:1128\n"
      "s_lshr_b32 s58, s58, s42\n"
      "s_lshr_b32 s59, s59, s42\n"
      "v_mfma_f32_16x16x32_bf16 v[108:111], v[156:159], v[176:179], v[108:111]\n"
      "ds_read_b64 v[136:137], v42 offset:2144\n"
      "s_lshr_b32 s42, s93, 8\n"
      "v_mfma_f32_16x16x32_bf16 v[108:111], v[120:123], v[188:191], v[108:111]\n"
      "ds_read_b64 v[138:139], v42 offset:2152\n"
      "s_and_b32 s42, s42, 16\n"
      "v_mfma_f32_16x16x32_bf16 v[108:111], v[132:135], v[184:187], v[108:111]\n"
      "ds_read_b64 v[148:149], v42 offset:3168\n"
      "s_lshr_b32 s25, s53, s42\n"
      "v_mfma_f32_16x16x32_bf16 v[108:111], v[144:147], v[180:183], v[108:111]\n"
      "ds_read_b64 v[150:151], v42 offset:3176\n"
      "s_lshl_b32 s14, s58, 16\n"
      "s_waitcnt lgkmcnt(8)\n"
      "v_mfma_f32_16x16x32_bf16 v[96:99], v[120:123], v[160:163], v[96:99]\n"
      "ds_read_b128 v[176:179], v43 offset:4736\n"
      "ds_read_b128 v[180:183], v43 offset:3200\n"
      "s_lshl_b32 s15, s59, 16\n"
      "v_mfma_f32_16x16x32_bf16 v[104:107], v[156:159], v[160:163], v[104:107]\n"
      "ds_read_b128 v[184:187], v43 offset:1664\n"
      "s_lshl_b32 s25, s25, 16\n"
      "v_mfma_f32_16x16x32_bf16 v[104:107], v[120:123], v[172:175], v[104:107]\n"
      "ds_read_b128 v[188:191], v43 offset:128\n"
      "s_lshr_b32 s42, s93, 8\n"
      "v_mfma_f32_16x16x32_bf16 v[104:107], v[132:135], v[168:171], v[104:107]\n"
      "ds_read_b64 v[116:117], v42 offset:128\n"
      "ds_read_b64 v[118:119], v42 offset:136\n"
      "s_and_b32 s42, s42, 0x60\n"
      "v_mfma_f32_16x16x32_bf16 v[104:107], v[144:147], v[164:167], v[104:107]\n"
      "ds_read_b64 v[128:129], v42 offset:1152\n"
      "v_xor_b32 v36, s97, v221\n"
      "s_waitcnt lgkmcnt(7)\n"
      "v_mfma_f32_16x16x32_bf16 v[100:103], v[112:115], v[160:163], v[100:103]\n"
      "ds_read_b64 v[130:131], v42 offset:1160\n"
      "v_xor_b32 v37, s42, v63\n"
      "v_mfma_f32_16x16x32_bf16 v[108:111], v[148:151], v[160:163], v[108:111]\n"
      "ds_read_b64 v[140:141], v42 offset:2176\n"
      "v_xor_b32 v50, s42, v50\n"
      "v_mfma_f32_16x16x32_bf16 v[108:111], v[112:115], v[172:175], v[108:111]\n"
      "ds_read_b64 v[142:143], v42 offset:2184\n"
      "v_xor_b32 v29, s97, v29\n"
      "v_add_u32 v47, s94, v47\n"
      "v_mfma_f32_16x16x32_bf16 v[108:111], v[124:127], v[168:171], v[108:111]\n"
      "ds_read_b64 v[152:153], v42 offset:3200\n"
      "v_add_u32 v5, s90, v5\n"
      "v_mfma_f32_16x16x32_bf16 v[108:111], v[136:139], v[164:167], v[108:111]\n"
      "ds_read_b64 v[154:155], v42 offset:3208\n"
      "s_lshl_b32 s42, s3, 12\n"
      "s_waitcnt lgkmcnt(8)\n"
      "v_mfma_f32_16x16x32_bf16 v[96:99], v[112:115], v[176:179], v[96:99]\n"
      "ds_read_b128 v[160:163], v41 offset:4608\n"
      "ds_read_b64 v[120:121], v40 offset:0\n"
      "s_add_u32 s42, s42, 147456\n"
      "v_mfma_f32_16x16x32_bf16 v[104:107], v[148:151], v[176:179], v[104:107]\n"
      "ds_read_b64 v[122:123], v40 offset:8\n"
      "ds_read_b128 v[164:167], v41 offset:3072\n"
      "v_add_u32 v55, s42, v7\n"
      "v_mfma_f32_16x16x32_bf16 v[104:107], v[112:115], v[188:191], v[104:107]\n"
      "ds_read_b64 v[132:133], v40 offset:1024\n"
      "ds_read_b64 v[134:135], v40 offset:1032\n"
      "s_lshr_b32 s43, s96, 16\n"
      "v_mfma_f32_16x16x32_bf16 v[104:107], v[124:127], v[184:187], v[104:107]\n"
      "ds_read_b128 v[168:171], v41 offset:1536\n"
      "ds_read_b64 v[144:145], v40 offset:2048\n"
      "s_add_u32 s55, s78, 0x1000000\n"
      "v_mfma_f32_16x16x32_bf16 v[104:107], v[136:139], v[180:183], v[104:107]\n"
      "ds_read_b64 v[146:147], v40 offset:2056\n"
      "ds_read_b128 v[172:175], v41 offset:0\n"
      "s_mov_b32 s59, s69\n"
      "s_waitcnt lgkmcnt(10)\n"
      "v_mfma_f32_16x16x32_bf16 v[100:103], v[116:119], v[176:179], v[100:103]\n"
      "ds_read_b64 v[156:157], v40 offset:3072\n"
      "s_lshr_b32 s53, s92, 12\n"
      "v_mfma_f32_16x16x32_bf16 v[108:111], v[152:155], v[176:179], v[108:111]\n"
      "ds_read_b64 v[158:159], v40 offset:3080\n"
      "s_mulk_i32 s53, 4224\n"
      "v_mfma_f32_16x16x32_bf16 v[108:111], v[116:119], v[188:191], v[108:111]\n"
      "s_add_u32 s53, s53, 0x400000\n"
      "s_add_u32 s54, s53, 1081344\n"
      "v_mfma_f32_16x16x32_bf16 v[108:111], v[128:131], v[184:187], v[108:111]\n"
      "s_mov_b32 exec_hi, 0\n"
      "v_add_u32 v58, s43, v58\n"
      "s_mov_b32 exec_hi, -1\n"
      "v_mfma_f32_16x16x32_bf16 v[108:111], v[140:143], v[180:183], v[108:111]\n"
      "s_waitcnt lgkmcnt(0)\n"
      "s_branch L_cv_e%=\n"
      "L_cv_b%=:\n"
      "ds_read_b128 v[164:167], v43 offset:3072\n"
      "ds_read_b64 v[112:113], v42 offset:0\n"
      "ds_read_b64 v[114:115], v42 offset:8\n"
      "ds_read_b128 v[160:163], v43 offset:4608\n"
      "ds_read_b64 v[124:125], v42 offset:1024\n"
      "ds_read_b64 v[126:127], v42 offset:1032\n"
      "ds_read_b64 v[136:137], v42 offset:2048\n"
      "ds_read_b64 v[138:139], v42 offset:2056\n"
      "ds_read_b128 v[168:171], v43 offset:1536\n"
      "ds_read_b64 v[116:117], v42 offset:32\n"
      "ds_read_b64 v[118:119], v42 offset:40\n"
      "ds_read_b64 v[128:129], v42 offset:1056\n"
      "ds_read_b64 v[130:131], v42 offset:1064\n"
      "ds_read_b64 v[140:141], v42 offset:2080\n"
      "ds_read_b64 v[142:143], v42 offset:2088\n"
      "s_waitcnt lgkmcnt(6)\n"
      "v_mfma_f32_16x16x32_bf16 v[96:99], v[112:115], v[164:167], 0\n"
      "ds_read_b128 v[176:179], v43 offset:4640\n"
      "v_mov_b64 v[8:9], v[80:81]\n"
      "v_mfma_f32_16x16x32_bf16 v[96:99], v[124:127], v[160:163], v[96:99]\n"
      "ds_read_b128 v[180:183], v43 offset:3104\n"
      "v_mov_b64 v[10:11], v[82:83]\n"
      "v_mfma_f32_16x16x32_bf16 v[104:107], v[124:127], v[164:167], 0\n"
      "ds_read_b128 v[184:187], v43 offset:1568\n"
      "v_mov_b64 v[12:13], v[84:85]\n"
      "v_mfma_f32_16x16x32_bf16 v[104:107], v[136:139], v[160:163], v[104:107]\n"
      "ds_read_b64 v[120:121], v42 offset:64\n"
      "v_mov_b64 v[14:15], v[86:87]\n"
      "v_mfma_f32_16x16x32_bf16 v[104:107], v[112:115], v[168:171], v[104:107]\n"
      "ds_read_b64 v[122:123], v42 offset:72\n"
      "s_waitcnt lgkmcnt(5)\n"
      "v_mfma_f32_16x16x32_bf16 v[100:103], v[116:119], v[164:167], 0\n"
      "ds_read_b64 v[132:133], v42 offset:1088\n"
      "v_mov_b64 v[16:17], v[88:89]\n"
      "v_mfma_f32_16x16x32_bf16 v[100:103], v[128:131], v[160:163], v[100:103]\n"
      "ds_read_b64 v[134:135], v42 offset:1096\n"
      "v_mov_b64 v[18:19], v[90:91]\n"
      "v_mfma_f32_16x16x32_bf16 v[108:111], v[128:131], v[164:167], 0\n"
      "ds_read_b64 v[144:145], v42 offset:2112\n"
      "v_mov_b64 v[20:21], v[92:93]\n"
      "v_mfma_f32_16x16x32_bf16 v[108:111], v[140:143], v[160:163], v[108:111]\n"
      "ds_read_b64 v[146:147], v42 offset:2120\n"
      "v_mov_b64 v[22:23], v[94:95]\n"
      "v_mfma_f32_16x16x32_bf16 v[108:111], v[116:119], v[168:171], v[108:111]\n"
      "v_mov_b32 v2, v212\n"
      "s_waitcnt lgkmcnt(6)\n"
      "v_mfma_f32_16x16x32_bf16 v[96:99], v[116:119], v[180:183], v[96:99]\n"
      "ds_read_b128 v[160:163], v43 offset:4704\n"
      "s_waitcnt vmcnt(5)\n"
      "buffer_store_dwordx2 v[52:53], v48, s[32:35], s79 offen sc1\n"
      "v_mov_b32 v3, v220\n"
      "v_mfma_f32_16x16x32_bf16 v[96:99], v[128:131], v[176:179], v[96:99]\n"
      "ds_read_b128 v[164:167], v43 offset:3168\n"
      "s_mov_b32 s45, s68\n"
      "v_mfma_f32_16x16x32_bf16 v[104:107], v[128:131], v[180:183], v[104:107]\n"
      "ds_read_b128 v[168:171], v43 offset:1632\n"
      "s_lshl_b32 s42, s99, 4\n"
      "v_mfma_f32_16x16x32_bf16 v[104:107], v[140:143], v[176:179], v[104:107]\n"
      "ds_read_b64 v[112:113], v42 offset:96\n"
      "s_lshr_b32 s58, s58, s42\n"
      "v_mfma_f32_16x16x32_bf16 v[104:107], v[116:119], v[184:187], v[104:107]\n"
      "ds_read_b64 v[114:115], v42 offset:104\n"
      "s_waitcnt lgkmcnt(5)\n"
      "v_mfma_f32_16x16x32_bf16 v[100:103], v[120:123], v[180:183], v[100:103]\n"
      "ds_read_b64 v[124:125], v42 offset:1120\n"
      "L_pg1_%=:\n"
      "s_cbranch_execz L_pg5_%=\n"
      "s_lshr_b32 s59, s59, s42\n"
      "v_mfma_f32_16x16x32_bf16 v[100:103], v[132:135], v[176:179], v[100:103]\n"
      "ds_read_b64 v[126:127], v42 offset:1128\n"
      "s_lshr_b32 s42, s93, 8\n"
      "v_mfma_f32_16x16x32_bf16 v[108:111], v[132:135], v[180:183], v[108:111]\n"
      "ds_read_b64 v[136:137], v42 offset:2144\n"
      "s_and_b32 s42, s42, 16\n"
      "v_mfma_f32_16x16x32_bf16 v[108:111], v[144:147], v[176:179], v[108:111]\n"
      "ds_read_b64 v[138:139], v42 offset:2152\n"
      "s_lshr_b32 s25, s53, s42\n"
      "v_mfma_f32_16x16x32_bf16 v[108:111], v[120:123], v[184:187], v[108:111]\n"
      "s_lshl_b32 s14, s58, 16\n"
      "s_waitcnt lgkmcnt(6)\n"
      "v_mfma_f32_16x16x32_bf16 v[96:99], v[120:123], v[164:167], v[96:99]\n"
      "ds_read_b128 v[176:179], v43 offset:4736\n"
      "s_lshl_b32 s15, s59, 16\n"
      "v_mfma_f32_16x16x32_bf16 v[96:99], v[132:135], v[160:163], v[96:99]\n"
      "ds_read_b128 v[180:183], v43 offset:3200\n"
      "s_lshl_b32 s25, s25, 16\n"
      "v_mfma_f32_16x16x32_bf16 v[104:107], v[132:135], v[164:167], v[104:107]\n"
      "ds_read_b128 v[184:187], v43 offset:1664\n"
      "s_lshr_b32 s42, s93, 8\n"
      "v_mfma_f32_16x16x32_bf16 v[104:107], v[144:147], v[160:163], v[104:107]\n"
      "ds_read_b64 v[116:117], v42 offset:128\n"
      "s_and_b32 s42, s42, 0x60\n"
      "v_mfma_f32_16x16x32_bf16 v[104:107], v[120:123], v[168:171], v[104:107]\n"
      "ds_read_b64 v[118:119], v42 offset:136\n"
      "v_xor_b32 v36, s97, v221\n"
      "s_waitcnt lgkmcnt(5)\n"
      "v_mfma_f32_16x16x32_bf16 v[100:103], v[112:115], v[164:167], v[100:103]\n"
      "ds_read_b64 v[128:129], v42 offset:1152\n"
      "v_xor_b32 v37, s42, v63\n"
      "v_mfma_f32_16x16x32_bf16 v[100:103], v[124:127], v[160:163], v[100:103]\n"
      "ds_read_b64 v[130:131], v42 offset:1160\n"
      "v_xor_b32 v50, s42, v50\n"
      "v_mfma_f32_16x16x32_bf16 v[108:111], v[124:127], v[164:167], v[108:111]\n"
      "ds_read_b64 v[140:141], v42 offset:2176\n"
      "v_xor_b32 v29, s97, v29\n"
      "v_add_u32 v47, s94, v47\n"
      "v_mfma_f32_16x16x32_bf16 v[108:111], v[136:139], v[160:163], v[108:111]\n"
      "ds_read_b64 v[142:143], v42 offset:2184\n"
      "v_add_u32 v5, s90, v5\n"
      "v_mfma_f32_16x16x32_bf16 v[108:111], v[112:115], v[168:171], v[108:111]\n"
      "s_lshl_b32 s42, s3, 12\n"
      "s_waitcnt lgkmcnt(6)\n"
      "v_mfma_f32_16x16x32_bf16 v[96:99], v[112:115], v[180:183], v[96:99]\n"
      "ds_read_b128 v[160:163], v41 offset:4608\n"
      "ds_read_b64 v[120:121], v40 offset:0\n"
      "s_add_u32 s42, s42, 147456\n"
      "v_mfma_f32_16x16x32_bf16 v[96:99], v[124:127], v[176:179], v[96:99]\n"
      "ds_read_b64 v[122:123], v40 offset:8\n"
      "ds_read_b128 v[164:167], v41 offset:3072\n"
      "v_add_u32 v55, s42, v7\n"
      "v_mfma_f32_16x16x32_bf16 v[104:107], v[124:127], v[180:183], v[104:107]\n"
      "ds_read_b64 v[132:133], v40 offset:1024\n"
      "ds_read_b64 v[134:135], v40 offset:1032\n"
      "s_lshr_b32 s43, s96, 16\n"
      "v_mfma_f32_16x16x32_bf16 v[104:107], v[136:139], v[176:179], v[104:107]\n"
      "ds_read_b128 v[168:171], v41 offset:1536\n"
      "ds_read_b64 v[144:145], v40 offset:2048\n"
      "s_add_u32 s55, s78, 0x1000000\n"
      "v_mfma_f32_16x16x32_bf16 v[104:107], v[112:115], v[184:187], v[104:107]\n"
      "ds_read_b64 v[146:147], v40 offset:2056\n"
      "ds_read_b128 v[172:175], v41 offset:0\n"
      "s_mov_b32 s59, s69\n"
      "s_waitcnt lgkmcnt(10)\n"
      "v_mfma_f32_16x16x32_bf16 v[100:103], v[116:119], v[180:183], v[100:103]\n"
      "ds_read_b64 v[156:157], v40 offset:3072\n"
      "s_lshr_b32 s53, s92, 12\n"
      "v_mfma_f32_16x16x32_bf16 v[100:103], v[128:131], v[176:179], v[100:103]\n"
      "ds_read_b64 v[158:159], v40 offset:3080\n"
      "s_mulk_i32 s53, 4224\n"
      "v_mfma_f32_16x16x32_bf16 v[108:111], v[128:131], v[180:183], v[108:111]\n"
      "s_add_u32 s53, s53, 0x400000\n"
      "s_add_u32 s54, s53, 1081344\n"
      "v_mfma_f32_16x16x32_bf16 v[108:111], v[140:143], v[176:179], v[108:111]\n"
      "s_mov_b32 exec_hi, 0\n"
      "v_add_u32 v58, s43, v58\n"
      "s_mov_b32 exec_hi, -1\n"
      "v_mfma_f32_16x16x32_bf16 v[108:111], v[116:119], v[184:187], v[108:111]\n"
      "s_waitcnt lgkmcnt(0)\n"
      "L_cv_e%=:\n"
      "ds_read_b64 v[112:113], v40 offset:32\n"
      "ds_read_b64 v[114:115], v40 offset:40\n"
      "s_add_u32 s8, s26, s91\n"
      "s_addc_u32 s9, s27, 0\n"
      "v_mfma_f32_16x16x32_bf16 v[64:67], v[120:123], v[160:163], 0\n"
      "global_load_lds_dwordx4 v222, s[20:21]\n"
      "ds_read_b64 v[124:125], v40 offset:1056\n"
      "v_mfma_f32_16x16x32_bf16 v[72:75], v[120:123], v[164:167], 0\n"
      "ds_read_b64 v[126:127], v40 offset:1064\n"
      "ds_read_b64 v[136:137], v40 offset:2080\n"
      "v_mfma_f32_16x16x32_bf16 v[72:75], v[132:135], v[160:163], v[72:75]\n"
      "ds_read_b64 v[138:139], v40 offset:2088\n"
      "v_mfma_f32_16x16x32_bf16 v[80:83], v[120:123], v[168:171], 0\n"
      "ds_read_b64 v[148:149], v40 offset:3104\n"
      "v_mfma_f32_16x16x32_bf16 v[80:83], v[132:135], v[164:167], v[80:83]\n"
      "ds_read_b64 v[150:151], v40 offset:3112\n"
      "v_mfma_f32_16x16x32_bf16 v[80:83], v[144:147], v[160:163], v[80:83]\n"
      "ds_write_b128 v56, v[96:99]\n"
      "ds_read_b128 v[176:179], v41 offset:4640\n"
      "ds_read_b128 v[180:183], v41 offset:3104\n"
      "ds_read_b128 v[184:187], v41 offset:1568\n"
      "v_mfma_f32_16x16x32_bf16 v[88:91], v[120:123], v[172:175], 0\n"
      "ds_read_b128 v[188:191], v41 offset:32\n"
      "v_mfma_f32_16x16x32_bf16 v[88:91], v[132:135], v[168:171], v[88:91]\n"
      "ds_read_b64 v[116:117], v40 offset:64\n"
      "v_mfma_f32_16x16x32_bf16 v[88:91], v[144:147], v[164:167], v[88:91]\n"
      "global_load_lds_dwordx4 v222, s[22:23] offset:1024\n"
      "v_mfma_f32_16x16x32_bf16 v[88:91], v[156:159], v[160:163], v[88:91]\n"
      "ds_write_b128 v56, v[104:107] offset:2048\n"
      "ds_read_b64 v[118:119], v40 offset:72\n"
      "ds_read_b64 v[128:129], v40 offset:1088\n"
      "ds_read_b64 v[130:131], v40 offset:1096\n"
      "s_waitcnt lgkmcnt(10)\n"
      "v_mfma_f32_16x16x32_bf16 v[68:71], v[112:115], v[160:163], 0\n"
      "ds_read_b64 v[140:141], v40 offset:2112\n"
      "v_mfma_f32_16x16x32_bf16 v[76:79], v[112:115], v[164:167], 0\n"
      "ds_read_b64 v[142:143], v40 offset:2120\n"
      "v_mfma_f32_16x16x32_bf16 v[76:79], v[124:127], v[160:163], v[76:79]\n"
      "ds_read_b64 v[152:153], v40 offset:3136\n"
      "s_load_dwordx16 s[60:75], s[56:57], 0xc0\n"
      "v_mfma_f32_16x16x32_bf16 v[84:87], v[112:115], v[168:171], 0\n"
      "ds_write_b128 v62, v[100:103]\n"
      "ds_read_b64 v[154:155], v40 offset:3144\n"
      "s_load_dwordx4 s[92:95], s[56:57], 0xa0\n"
      "v_mfma_f32_16x16x32_bf16 v[84:87], v[124:127], v[164:167], v[84:87]\n"
      "v_mfma_f32_16x16x32_bf16 v[84:87], v[136:139], v[160:163], v[84:87]\n"
      "v_mfma_f32_16x16x32_bf16 v[92:95], v[112:115], v[172:175], 0\n"
      "v_mfma_f32_16x16x32_bf16 v[92:95], v[124:127], v[168:171], v[92:95]\n"
      "ds_write_b128 v62, v[108:111] offset:2048\n"
      "v_mfma_f32_16x16x32_bf16 v[92:95], v[136:139], v[164:167], v[92:95]\n"
      "v_mfma_f32_16x16x32_bf16 v[92:95], v[148:151], v[160:163], v[92:95]\n"
      "s_waitcnt lgkmcnt(11)\n"
      "v_mfma_f32_16x16x32_bf16 v[64:67], v[112:115], v[176:179], v[64:67]\n"
      "buffer_load_dwordx2 v[254:255], v2, s[32:35], s45 offen sc1\n"
      "v_mfma_f32_16x16x32_bf16 v[72:75], v[112:115], v[180:183], v[72:75]\n"
      "ds_read_b128 v[160:163], v41 offset:4704\n"
      "v_mfma_f32_16x16x32_bf16 v[72:75], v[124:127], v[176:179], v[72:75]\n"
      "ds_read_b128 v[164:167], v41 offset:3168\n"
      "v_mfma_f32_16x16x32_bf16 v[80:83], v[112:115], v[184:187], v[80:83]\n"
      "ds_read_b128 v[168:171], v41 offset:1632\n"
      "v_mfma_f32_16x16x32_bf16 v[80:83], v[124:127], v[180:183], v[80:83]\n"
      "ds_read_b128 v[172:175], v41 offset:96\n"
      "v_mfma_f32_16x16x32_bf16 v[80:83], v[136:139], v[176:179], v[80:83]\n"
      "ds_read_b64 v[120:121], v40 offset:96\n"
      "v_mfma_f32_16x16x32_bf16 v[88:91], v[112:115], v[188:191], v[88:91]\n"
      "s_waitcnt lgkmcnt(5)\n"
      "s_barrier\n"
      "v_mfma_f32_16x16x32_bf16 v[88:91], v[124:127], v[184:187], v[88:91]\n"
      "ds_read_b128 v[96:99], v45 offset:0\n"
      "v_mfma_f32_16x16x32_bf16 v[88:91], v[136:139], v[180:183], v[88:91]\n"
      "ds_read_b128 v[100:103], v45 offset:4096\n"
      "v_mfma_f32_16x16x32_bf16 v[88:91], v[148:151], v[176:179], v[88:91]\n"
      "ds_read_b128 v[104:107], v45 offset:8192\n"
      "v_mfma_f32_16x16x32_bf16 v[68:71], v[116:119], v[176:179], v[68:71]\n"
      "ds_read_b128 v[108:111], v45 offset:12288\n"
      "v_mfma_f32_16x16x32_bf16 v[76:79], v[116:119], v[180:183], v[76:79]\n"
      "ds_read_b64 v[202:203], v47\n"
      "v_mfma_f32_16x16x32_bf16 v[76:79], v[128:131], v[176:179], v[76:79]\n"
      "ds_read_b64 v[122:123], v40 offset:104\n"
      "v_mfma_f32_16x16x32_bf16 v[84:87], v[116:119], v[184:187], v[84:87]\n"
      "ds_read_b64 v[132:133], v40 offset:1120\n"
      "v_mfma_f32_16x16x32_bf16 v[84:87], v[128:131], v[180:183], v[84:87]\n"
      "ds_read_b64 v[134:135], v40 offset:1128\n"
      "v_mfma_f32_16x16x32_bf16 v[84:87], v[140:143], v[176:179], v[84:87]\n"
      "ds_read_b64 v[144:145], v40 offset:2144\n"
      "v_mfma_f32_16x16x32_bf16 v[92:95], v[116:119], v[188:191], v[92:95]\n"
      "ds_read_b64 v[146:147], v40 offset:2152\n"
      "s_waitcnt lgkmcnt(5)\n"
      "v_mfma_f32_16x16x32_bf16 v[92:95], v[128:131], v[184:187], v[92:95]\n"
      "ds_read_b64 v[156:157], v40 offset:3168\n"
      "v_add_f32 v96, v96, v100\n"
      "v_mfma_f32_16x16x32_bf16 v[92:95], v[140:143], v[180:183], v[92:95]\n"
      "ds_read_b64 v[158:159], v40 offset:3176\n"
      "v_add_f32 v97, v97, v101\n"
      "v_mfma_f32_16x16x32_bf16 v[92:95], v[152:155], v[176:179], v[92:95]\n"
      "v_add_f32 v98, v98, v102\n"
      "v_mfma_f32_16x16x32_bf16 v[64:67], v[116:119], v[160:163], v[64:67]\n"
      "ds_read_b128 v[176:179], v41 offset:4736\n"
      "v_add_f32 v99, v99, v103\n"
      "v_mfma_f32_16x16x32_bf16 v[72:75], v[116:119], v[164:167], v[72:75]\n"
      "ds_read_b128 v[180:183], v41 offset:3200\n"
      "v_add_f32 v104, v104, v108\n"
      "v_mfma_f32_16x16x32_bf16 v[72:75], v[128:131], v[160:163], v[72:75]\n"
      "ds_read_b128 v[184:187], v41 offset:1664\n"
      "v_add_f32 v105, v105, v109\n"
      "v_mfma_f32_16x16x32_bf16 v[80:83], v[116:119], v[168:171], v[80:83]\n"
      "ds_read_b128 v[188:191], v41 offset:128\n"
      "v_add_f32 v106, v106, v110\n"
      "v_mfma_f32_16x16x32_bf16 v[80:83], v[128:131], v[164:167], v[80:83]\n"
      "ds_read_b64 v[112:113], v40 offset:128\n"
      "v_add_f32 v107, v107, v111\n"
      "v_mfma_f32_16x16x32_bf16 v[80:83], v[140:143], v[160:163], v[80:83]\n"
      "ds_read_b64 v[114:115], v40 offset:136\n"
      "v_add_f32 v96, v96, v104\n"
      "v_mfma_f32_16x16x32_bf16 v[88:91], v[116:119], v[172:175], v[88:91]\n"
      "buffer_load_dwordx2 v[200:201], v4, s[32:35], s80 offen offset:0 sc1\n"
      "v_add_f32 v97, v97, v105\n"
      "v_mfma_f32_16x16x32_bf16 v[88:91], v[128:131], v[168:171], v[88:91]\n"
      "ds_read_b64 v[124:125], v40 offset:1152\n"
      "v_add_f32 v98, v98, v106\n"
      "v_mfma_f32_16x16x32_bf16 v[88:91], v[140:143], v[164:167], v[88:91]\n"
      "buffer_load_dword v250, v61, s[32:35], s85 offen offset:0 sc1\n"
      "v_add_f32 v99, v99, v107\n"
      "v_mfma_f32_16x16x32_bf16 v[88:91], v[152:155], v[160:163], v[88:91]\n"
      "ds_read_b64 v[126:127], v40 offset:1160\n"
      "s_waitcnt lgkmcnt(8)\n"
      "v_mfma_f32_16x16x32_bf16 v[68:71], v[120:123], v[160:163], v[68:71]\n"
      "buffer_load_dwordx4 v[224:227], v57, s[32:35], s82 offen offset:0 sc1\n"
      "v_cvt_pk_bf16_f32 v96, 0, v96\n"
      "v_mfma_f32_16x16x32_bf16 v[76:79], v[120:123], v[164:167], v[76:79]\n"
      "ds_read_b64 v[136:137], v40 offset:2176\n"
      "v_cvt_pk_bf16_f32 v97, 0, v97\n"
      "v_mfma_f32_16x16x32_bf16 v[76:79], v[132:135], v[160:163], v[76:79]\n"
      "ds_read_b64 v[138:139], v40 offset:2184\n"
      "v_cvt_pk_bf16_f32 v98, 0, v98\n"
      "v_mfma_f32_16x16x32_bf16 v[84:87], v[120:123], v[168:171], v[84:87]\n"
      "ds_read_b64 v[148:149], v40 offset:3200\n"
      "v_cvt_pk_bf16_f32 v99, 0, v99\n"
      "v_mfma_f32_16x16x32_bf16 v[84:87], v[132:135], v[164:167], v[84:87]\n"
      "buffer_load_dwordx4 v[228:231], v57, s[32:35], s82 offen offset:1024 sc1\n"
      "v_and_b32 v100, 0xffff0000, v203\n"
      "v_mfma_f32_16x16x32_bf16 v[84:87], v[144:147], v[160:163], v[84:87]\n"
      "ds_read_b64 v[150:151], v40 offset:3208\n"
      "v_lshlrev_b32 v101, 16, v203\n"
      "v_mfma_f32_16x16x32_bf16 v[92:95], v[120:123], v[172:175], v[92:95]\n"
      "s_lshr_b32 s58, s3, 1\n"
      "s_mul_i32 s58, s58, 0x3f800000\n"
      "v_mfma_f32_16x16x32_bf16 v[92:95], v[132:135], v[168:171], v[92:95]\n"
      "v_and_b32 v102, 0xffff0000, v202\n"
      "v_lshlrev_b32 v103, 16, v202\n"
      "v_mfma_f32_16x16x32_bf16 v[92:95], v[144:147], v[164:167], v[92:95]\n"
      "buffer_load_dwordx4 v[192:195], v54, s[32:35], s53 offen offset:0 sc1\n"
      "v_mul_f32 v100, s25, v100\n"
      "v_mfma_f32_16x16x32_bf16 v[92:95], v[156:159], v[160:163], v[92:95]\n"
      "v_mul_f32 v101, s25, v101\n"
      "s_waitcnt lgkmcnt(8)\n"
      "v_mfma_f32_16x16x32_bf16 v[64:67], v[120:123], v[176:179], v[64:67]\n"
      "s_waitcnt vmcnt(5)\n"
      "v_cmp_ne_u32 vcc, s30, v255\n"
      "v_mfma_f32_16x16x32_bf16 v[72:75], v[120:123], v[180:183], v[72:75]\n"
      "ds_read_b128 v[160:163], v41 offset:4800\n"
      "v_mul_f32 v102, s25, v102\n"
      "v_mfma_f32_16x16x32_bf16 v[72:75], v[132:135], v[176:179], v[72:75]\n"
      "buffer_load_dwordx4 v[196:199], v54, s[32:35], s54 offen offset:0 sc1\n"
      "s_cbranch_vccnz L_cm_s%=\n"
      "L_cm_r%=:\n"
      "v_add_f32_dpp v254, v254, v254 quad_perm:[1,0,3,2] row_mask:0xf bank_mask:0xf\n"
      "v_mfma_f32_16x16x32_bf16 v[80:83], v[120:123], v[184:187], v[80:83]\n"
      "ds_read_b128 v[164:167], v41 offset:3264\n"
      "v_mul_f32 v103, s25, v103\n"
      "v_mfma_f32_16x16x32_bf16 v[80:83], v[132:135], v[180:183], v[80:83]\n"
      "ds_read_b128 v[168:171], v41 offset:1728\n"
      "v_add_f32_dpp v254, v254, v254 quad_perm:[2,3,0,1] row_mask:0xf bank_mask:0xf\n"
      "v_mfma_f32_16x16x32_bf16 v[80:83], v[144:147], v[176:179], v[80:83]\n"
      "ds_read_b128 v[172:175], v41 offset:192\n"
      "v_cvt_pk_bf16_f32 v254, 0, v254\n"
      "v_mfma_f32_16x16x32_bf16 v[88:91], v[120:123], v[188:191], v[88:91]\n"
      "buffer_load_dword v248, v60, s[32:35], s84 offen offset:0 sc1\n"
      "v_add_f32 v254, s58, v254\n"
      "v_mfma_f32_16x16x32_bf16 v[88:91], v[132:135], v[184:187], v[88:91]\n"
      "ds_read_b64 v[116:117], v40 offset:160\n"
      "v_cvt_pk_bf16_f32 v254, v254, v254\n"
      "v_mfma_f32_16x16x32_bf16 v[88:91], v[144:147], v[180:183], v[88:91]\n"
      "ds_write_b16 v3, v254\n"
      "ds_read_b64 v[118:119], v40 offset:168\n"
      "v_cvt_pk_bf16_f32 v100, 0, v100\n"
      "v_mfma_f32_16x16x32_bf16 v[88:91], v[156:159], v[176:179], v[88:91]\n"
      "ds_read_b64 v[128:129], v40 offset:1184\n"
      "s_waitcnt lgkmcnt(8)\n"
      "v_mfma_f32_16x16x32_bf16 v[68:71], v[112:115], v[176:179], v[68:71]\n"
      "buffer_load_dword v249, v60, s[32:35], s84 offen offset:2048 sc1\n"
      "v_cvt_pk_bf16_f32 v101, 0, v101\n"
      "v_mfma_f32_16x16x32_bf16 v[76:79], v[112:115], v[180:183], v[76:79]\n"
      "ds_read_b64 v[130:131], v40 offset:1192\n"
      "v_cvt_pk_bf16_f32 v102, 0, v102\n"
      "v_mfma_f32_16x16x32_bf16 v[76:79], v[124:127], v[176:179], v[76:79]\n"
      "ds_read_b64 v[140:141], v40 offset:2208\n"
      "v_cvt_pk_bf16_f32 v103, 0, v103\n"
      "v_mfma_f32_16x16x32_bf16 v[84:87], v[112:115], v[184:187], v[84:87]\n"
      "ds_read_b64 v[142:143], v40 offset:2216\n"
      "s_waitcnt lgkmcnt(5)\n"
      "s_barrier\n"
      "v_mfma_f32_16x16x32_bf16 v[84:87], v[124:127], v[180:183], v[84:87]\n"
      "buffer_load_dwordx2 v[252:253], v51, s[32:35], s76 offen offset:0 sc1\n"
      "ds_read2_b64 v[24:27], v236 offset0:0 offset1:4\n"
      "v_add_f32 v96, v96, v100\n"
      "v_mfma_f32_16x16x32_bf16 v[84:87], v[136:139], v[176:179], v[84:87]\n"
      "ds_read_b64 v[152:153], v40 offset:3232\n"
      "v_add_f32 v97, v97, v101\n"
      "v_mfma_f32_16x16x32_bf16 v[92:95], v[112:115], v[188:191], v[92:95]\n"
      "ds_read_b64 v[154:155], v40 offset:3240\n"
      "v_add_f32 v98, v98, v102\n"
      "v_mfma_f32_16x16x32_bf16 v[92:95], v[124:127], v[184:187], v[92:95]\n"
      "s_mov_b32 s48, 0\n"
      "s_mov_b32 s49, -1\n"
      "v_mfma_f32_16x16x32_bf16 v[92:95], v[136:139], v[180:183], v[92:95]\n"
      "v_add_f32 v99, v99, v103\n"
      "v_cvt_pk_bf16_f32 v96, 0, v96\n"
      "v_mfma_f32_16x16x32_bf16 v[92:95], v[148:151], v[176:179], v[92:95]\n"
      "s_waitcnt lgkmcnt(2)\n"
      "v_mfma_f32_16x16x32_bf16 v[64:67], v[112:115], v[160:163], v[64:67]\n"
      "buffer_load_dwordx4 v[208:211], v55, s[32:35], 0 offen offset:0 sc1\n"
      "v_mfma_f32_16x16x32_bf16 v[232:235], v[24:27], v[8:11], 0\n"
      "v_mfma_f32_16x16x32_bf16 v[72:75], v[112:115], v[164:167], v[72:75]\n"
      "ds_read_b128 v[176:179], v41 offset:4832\n"
      "v_mfma_f32_16x16x32_bf16 v[8:11], v[24:27], v[12:15], 0\n"
      "v_mfma_f32_16x16x32_bf16 v[72:75], v[124:127], v[160:163], v[72:75]\n"
      "ds_read_b128 v[180:183], v41 offset:3296\n"
      "v_mfma_f32_16x16x32_bf16 v[12:15], v[24:27], v[16:19], 0\n"
      "v_mfma_f32_16x16x32_bf16 v[80:83], v[112:115], v[168:171], v[80:83]\n"
      "ds_read_b128 v[184:187], v41 offset:1760\n"
      "v_mfma_f32_16x16x32_bf16 v[16:19], v[24:27], v[20:23], 0\n"
      "v_mfma_f32_16x16x32_bf16 v[80:83], v[124:127], v[164:167], v[80:83]\n"
      "buffer_load_dwordx4 v[212:215], v55, s[32:35], 0 offen offset:1024 sc1\n"
      "v_cvt_pk_bf16_f32 v97, 0, v97\n"
      "v_mfma_f32_16x16x32_bf16 v[80:83], v[136:139], v[160:163], v[80:83]\n"
      "ds_read_b128 v[188:191], v41 offset:224\n"
      "v_cvt_pk_bf16_f32 v98, 0, v98\n"
      "v_mfma_f32_16x16x32_bf16 v[88:91], v[112:115], v[172:175], v[88:91]\n"
      "v_cndmask_b32 v206, v232, v12, s[48:49]\n"
      "v_cndmask_b32 v207, v8, v16, s[48:49]\n"
      "v_mfma_f32_16x16x32_bf16 v[88:91], v[124:127], v[168:171], v[88:91]\n"
      "ds_read_b64 v[120:121], v40 offset:192\n"
      "v_cvt_pk_bf16_f32 v99, 0, v99\n"
      "v_mfma_f32_16x16x32_bf16 v[88:91], v[136:139], v[164:167], v[88:91]\n"
      "buffer_load_dwordx4 v[216:219], v55, s[32:35], 0 offen offset:2048 sc1\n"
      "v_mfma_f32_16x16x32_bf16 v[88:91], v[148:151], v[160:163], v[88:91]\n"
      "ds_read_b64 v[122:123], v40 offset:200\n"
      "s_waitcnt lgkmcnt(6)\n"
      "v_mfma_f32_16x16x32_bf16 v[68:71], v[116:119], v[160:163], v[68:71]\n"
      "ds_read_b64 v[132:133], v40 offset:1216\n"
      "v_mfma_f32_16x16x32_bf16 v[76:79], v[116:119], v[164:167], v[76:79]\n"
      "ds_read_b64 v[134:135], v40 offset:1224\n"
      "v_mfma_f32_16x16x32_bf16 v[76:79], v[128:131], v[160:163], v[76:79]\n"
      "buffer_load_dwordx4 v[220:223], v55, s[32:35], 0 offen offset:3072 sc1\n"
      "v_mfma_f32_16x16x32_bf16 v[84:87], v[116:119], v[168:171], v[84:87]\n"
      "ds_read_b64 v[144:145], v40 offset:2240\n"
      "v_mfma_f32_16x16x32_bf16 v[84:87], v[128:131], v[164:167], v[84:87]\n"
      "ds_read_b64 v[146:147], v40 offset:2248\n"
      "v_mfma_f32_16x16x32_bf16 v[84:87], v[140:143], v[160:163], v[84:87]\n"
      "ds_read_b64 v[156:157], v40 offset:3264\n"
      "v_mfma_f32_16x16x32_bf16 v[92:95], v[116:119], v[172:175], v[92:95]\n"
      "buffer_load_dwordx2 v[204:205], v58, s[32:35], s83 offen offset:0 sc1\n"
      "v_mfma_f32_16x16x32_bf16 v[92:95], v[128:131], v[168:171], v[92:95]\n"
      "ds_read_b64 v[158:159], v40 offset:3272\n"
      "v_mfma_f32_16x16x32_bf16 v[92:95], v[140:143], v[164:167], v[92:95]\n"
      "v_mfma_f32_16x16x32_bf16 v[92:95], v[152:155], v[160:163], v[92:95]\n"
      "s_waitcnt lgkmcnt(8)\n"
      "v_mfma_f32_16x16x32_bf16 v[64:67], v[116:119], v[176:179], v[64:67]\n"
      "ds_read_b128 v[160:163], v41 offset:4896\n"
      "v_mfma_f32_16x16x32_bf16 v[72:75], v[116:119], v[180:183], v[72:75]\n"
      "ds_read_b128 v[164:167], v41 offset:3360\n"
      "v_mfma_f32_16x16x32_bf16 v[72:75], v[128:131], v[176:179], v[72:75]\n"
      "ds_read_b128 v[168:171], v41 offset:1824\n"
      "v_mfma_f32_16x16x32_bf16 v[80:83], v[116:119], v[184:187], v[80:83]\n"
      "ds_read_b128 v[172:175], v41 offset:288\n"
      "v_mfma_f32_16x16x32_bf16 v[80:83], v[128:131], v[180:183], v[80:83]\n"
      "ds_read_b64 v[112:113], v40 offset:224\n"
      "v_mfma_f32_16x16x32_bf16 v[80:83], v[140:143], v[176:179], v[80:83]\n"
      "ds_read_b64 v[114:115], v40 offset:232\n"
      "v_mfma_f32_16x16x32_bf16 v[88:91], v[116:119], v[188:191], v[88:91]\n"
      "ds_read_b64 v[124:125], v40 offset:1248\n"
      "s_waitcnt vmcnt(5)\n"
      "v_mfma_f32_16x16x32_bf16 v[88:91], v[128:131], v[184:187], v[88:91]\n"
      "ds_read_b64 v[126:127], v40 offset:1256\n"
      "v_cmp_ne_u64 vcc, s[30:31], v[252:253]\n"
      "v_mfma_f32_16x16x32_bf16 v[88:91], v[140:143], v[180:183], v[88:91]\n"
      "ds_read_b64 v[136:137], v40 offset:2272\n"
      "v_mfma_f32_16x16x32_bf16 v[88:91], v[152:155], v[176:179], v[88:91]\n"
      "s_cbranch_vccnz L_cw_s%=\n"
      "L_cw_r%=:\n"
      "s_waitcnt lgkmcnt(9)\n"
      "v_mfma_f32_16x16x32_bf16 v[68:71], v[120:123], v[176:179], v[68:71]\n"
      "ds_read_b64 v[138:139], v40 offset:2280\n"
      "v_add_u32 v35, 4096, v7\n"
      "v_mfma_f32_16x16x32_bf16 v[76:79], v[120:123], v[180:183], v[76:79]\n"
      "ds_read_b64 v[148:149], v40 offset:3296\n"
      "v_mfma_f32_16x16x32_bf16 v[76:79], v[132:135], v[176:179], v[76:79]\n"
      "global_load_dwordx4 a[0:3], v35, s[8:9] offset:-4096 sc1\n"
      "v_mfma_f32_16x16x32_bf16 v[84:87], v[120:123], v[184:187], v[84:87]\n"
      "ds_read_b64 v[150:151], v40 offset:3304\n"
      "v_mfma_f32_16x16x32_bf16 v[84:87], v[132:135], v[180:183], v[84:87]\n"
      "v_mfma_f32_16x16x32_bf16 v[84:87], v[144:147], v[176:179], v[84:87]\n"
      "v_mfma_f32_16x16x32_bf16 v[92:95], v[120:123], v[188:191], v[92:95]\n"
      "global_load_dwordx4 a[4:7], v35, s[8:9] offset:-3072 sc1\n"
      "v_mfma_f32_16x16x32_bf16 v[92:95], v[132:135], v[184:187], v[92:95]\n"
      "v_mfma_f32_16x16x32_bf16 v[92:95], v[144:147], v[180:183], v[92:95]\n"
      "s_waitcnt vmcnt(2)\n"
      "v_add_u32 v251, v200, v201\n"
      "v_mfma_f32_16x16x32_bf16 v[92:95], v[156:159], v[176:179], v[92:95]\n"
      "v_add3_u32 v34, v192, v193, v194\n"
      "v_add3_u32 v34, v34, v195, v196\n"
      "v_add_u32_dpp v251, v251, v251 row_half_mirror row_mask:0xf bank_mask:0xf\n"
      "v_add3_u32 v34, v34, v197, v198\n"
      "s_waitcnt lgkmcnt(8)\n"
      "v_mfma_f32_16x16x32_bf16 v[64:67], v[120:123], v[160:163], v[64:67]\n"
      "global_load_dwordx4 a[8:11], v35, s[8:9] offset:-2048 sc1\n"
      "v_add3_u32 v251, v251, v34, v199\n"
      "v_add3_u32 v34, v248, v249, v250\n"
      "v_add3_u32 v34, v34, v211, v215\n"
      "v_add_u32_dpp v251, v251, v251 quad_perm:[1,0,3,2] row_mask:0xf bank_mask:0xf\n"
      "v_mfma_f32_16x16x32_bf16 v[72:75], v[120:123], v[164:167], v[72:75]\n"
      "ds_read_b128 v[176:179], v41 offset:4928\n"
      "v_add3_u32 v34, v34, v219, v223\n"
      "v_add3_u32 v34, v34, v227, v231\n"
      "v_add_u32_dpp v251, v251, v251 quad_perm:[2,3,0,1] row_mask:0xf bank_mask:0xf\n"
      "v_add3_u32 v251, v251, v34, v205\n"
      "v_mfma_f32_16x16x32_bf16 v[72:75], v[132:135], v[160:163], v[72:75]\n"
      "ds_read_b128 v[180:183], v41 offset:3392\n"
      "v_cmp_ne_u32 vcc, s24, v251\n"
      "v_mfma_f32_16x16x32_bf16 v[80:83], v[120:123], v[168:171], v[80:83]\n"
      "ds_read_b128 v[184:187], v41 offset:1856\n"
      "v_mfma_f32_16x16x32_bf16 v[80:83], v[132:135], v[164:167], v[80:83]\n"
      "global_load_dwordx4 a[12:15], v35, s[8:9] offset:-1024 sc1\n"
      "s_cbranch_vccnz L_cv_s%=\n"
      "L_cv_r%=:\n"
      "v_add_f32_dpp v204, v204, v204 quad_perm:[1,0,3,2] row_mask:0xf bank_mask:0xf\n"
      "v_lshlrev_b32 v100, 16, v200\n"
      "v_and_b32 v101, 0xffff0000, v200\n"
      "v_add_f32_dpp v204, v204, v204 quad_perm:[2,3,0,1] row_mask:0xf bank_mask:0xf\n"
      "v_mfma_f32_16x16x32_bf16 v[80:83], v[144:147], v[160:163], v[80:83]\n"
      "ds_read_b128 v[188:191], v41 offset:320\n"
      "v_cvt_pk_bf16_f32 v204, 0, v204\n"
      "v_lshlrev_b32 v102, 16, v201\n"
      "v_add_f32 v205, 1.0, v204\n"
      "v_mfma_f32_16x16x32_bf16 v[88:91], v[120:123], v[172:175], v[88:91]\n"
      "ds_read_b64 v[116:117], v40 offset:256\n"
      "v_and_b32 v103, 0xffff0000, v201\n"
      "v_cvt_pk_bf16_f32 v205, 0, v205\n"
      "v_lshlrev_b32 v104, 16, v226\n"
      "v_mfma_f32_16x16x32_bf16 v[88:91], v[132:135], v[168:171], v[88:91]\n"
      "ds_read_b64 v[118:119], v40 offset:264\n"
      "v_readlane_b32 s40, v204, 32\n"
      "v_and_b32 v105, 0xffff0000, v226\n"
      "v_readlane_b32 s41, v205, 36\n"
      "v_lshlrev_b32 v106, 16, v230\n"
      "v_mfma_f32_16x16x32_bf16 v[88:91], v[144:147], v[164:167], v[88:91]\n"
      "global_load_dwordx4 a[16:19], v35, s[8:9] offset:0 sc1\n"
      "ds_read_b64_tr_b16 v[240:241], v28 offset:0\n"
      "ds_read_b64_tr_b16 v[242:243], v28 offset:1024\n"
      "ds_read_b64_tr_b16 v[244:245], v28 offset:32\n"
      "ds_read_b64_tr_b16 v[246:247], v28 offset:1056\n"
      "v_readlane_b32 s42, v204, 40\n"
      "v_and_b32 v107, 0xffff0000, v230\n"
      "v_readlane_b32 s43, v205, 44\n"
      "v_mfma_f32_16x16x32_bf16 v[88:91], v[156:159], v[160:163], v[88:91]\n"
      "ds_read_b64 v[128:129], v40 offset:1280\n"
      "v_sub_f32 v104, v100, v104\n"
      "v_sub_f32 v105, v101, v105\n"
      "v_sub_f32 v106, v102, v106\n"
      "s_waitcnt lgkmcnt(11)\n"
      "v_mfma_f32_16x16x32_bf16 v[68:71], v[112:115], v[160:163], v[68:71]\n"
      "ds_read_b64 v[130:131], v40 offset:1288\n"
      "v_sub_f32 v107, v103, v107\n"
      "v_cvt_pk_bf16_f32 v104, 0, v104\n"
      "v_cvt_pk_bf16_f32 v105, 0, v105\n"
      "v_mfma_f32_16x16x32_bf16 v[76:79], v[112:115], v[164:167], v[76:79]\n"
      "ds_read_b64 v[140:141], v40 offset:2304\n"
      "v_cvt_pk_bf16_f32 v106, 0, v106\n"
      "v_cvt_pk_bf16_f32 v107, 0, v107\n"
      "v_mul_f32 v104, v104, v224\n"
      "v_mul_f32 v105, v105, v225\n"
      "v_mfma_f32_16x16x32_bf16 v[76:79], v[124:127], v[160:163], v[76:79]\n"
      "global_load_dwordx4 a[20:23], v35, s[8:9] offset:1024 sc1\n"
      "ds_read_b64 v[142:143], v40 offset:2312\n"
      "v_mul_f32 v106, v106, v228\n"
      "v_mul_f32 v107, v107, v229\n"
      "v_cvt_pk_bf16_f32 v104, 0, v104\n"
      "v_mfma_f32_16x16x32_bf16 v[84:87], v[112:115], v[168:171], v[84:87]\n"
      "ds_read_b64 v[152:153], v40 offset:3328\n"
      "v_cvt_pk_bf16_f32 v105, 0, v105\n"
      "v_cvt_pk_bf16_f32 v106, 0, v106\n"
      "v_cvt_pk_bf16_f32 v107, 0, v107\n"
      "v_mfma_f32_16x16x32_bf16 v[84:87], v[124:127], v[164:167], v[84:87]\n"
      "ds_read_b64 v[154:155], v40 offset:3336\n"
      "v_mul_f32 v104, s41, v104\n"
      "v_mul_f32 v105, s41, v105\n"
      "v_mul_f32 v106, s41, v106\n"
      "v_mul_f32 v107, s41, v107\n"
      "v_mfma_f32_16x16x32_bf16 v[84:87], v[136:139], v[160:163], v[84:87]\n"
      "v_cvt_pk_bf16_f32 v104, 0, v104\n"
      "v_cvt_pk_bf16_f32 v105, 0, v105\n"
      "v_cvt_pk_bf16_f32 v106, 0, v106\n"
      "v_mfma_f32_16x16x32_bf16 v[92:95], v[112:115], v[172:175], v[92:95]\n"
      "global_load_dwordx4 a[24:27], v35, s[8:9] offset:2048 sc1\n"
      "v_cvt_pk_bf16_f32 v107, 0, v107\n"
      "v_add_f32 v104, s40, v104\n"
      "v_add_f32 v105, s40, v105\n"
      "s_waitcnt lgkmcnt(6)\n"
      "v_mfma_f32_16x16x32_bf16 v[92:95], v[124:127], v[168:171], v[92:95]\n"
      "v_add_f32 v106, s40, v106\n"
      "v_add_f32 v107, s40, v107\n"
      "v_cvt_pk_bf16_f32 v104, 0, v104\n"
      "v_mfma_f32_16x16x32_bf16 v[92:95], v[136:139], v[164:167], v[92:95]\n"
      "v_cvt_pk_bf16_f32 v105, 0, v105\n"
      "v_cvt_pk_bf16_f32 v106, 0, v106\n"
      "v_cvt_pk_bf16_f32 v107, 0, v107\n"
      "v_mul_f32 v104, v104, v96\n"
      "v_mfma_f32_16x16x32_bf16 v[92:95], v[148:151], v[160:163], v[92:95]\n"
      "v_mul_f32 v105, v105, v97\n"
      "v_mul_f32 v106, v106, v98\n"
      "v_mul_f32 v107, v107, v99\n"
      "s_waitcnt lgkmcnt(0)\n"
      "v_mfma_f32_16x16x32_bf16 v[64:67], v[112:115], v[176:179], v[64:67]\n"
      "buffer_store_dwordx4 v[240:243], v7, s[32:35], s86 offen sc1\n"
      "v_cvt_pk_bf16_f32 v104, 0, v104\n"
      "v_cvt_pk_bf16_f32 v105, 0, v105\n"
      "v_cvt_pk_bf16_f32 v106, 0, v106\n"
      "v_mfma_f32_16x16x32_bf16 v[68:71], v[116:119], v[176:179], v[68:71]\n"
      "v_cvt_pk_bf16_f32 v107, 0, v107\n"
      "v_mul_f32 v104, s42, v104\n"
      "v_mul_f32 v105, s42, v105\n"
      "v_mul_f32 v106, s42, v106\n"
      "v_mfma_f32_16x16x32_bf16 v[72:75], v[112:115], v[180:183], v[72:75]\n"
      "v_mul_f32 v107, s42, v107\n"
      "v_cvt_pk_bf16_f32 v104, 0, v104\n"
      "v_cvt_pk_bf16_f32 v105, 0, v105\n"
      "v_mfma_f32_16x16x32_bf16 v[72:75], v[124:127], v[176:179], v[72:75]\n"
      "ds_write_b128 v44, v[64:67] offset:0\n"
      "v_cvt_pk_bf16_f32 v106, 0, v106\n"
      "v_cvt_pk_bf16_f32 v107, 0, v107\n"
      "v_add_f32 v104, v104, v100\n"
      "v_add_f32 v105, v105, v101\n"
      "v_mfma_f32_16x16x32_bf16 v[76:79], v[116:119], v[180:183], v[76:79]\n"
      "buffer_store_dwordx4 v[244:247], v7, s[32:35], s87 offen sc1\n"
      "global_load_dwordx4 a[28:31], v35, s[8:9] offset:3072 sc1\n"
      "ds_write_b128 v59, v[68:71] offset:0\n"
      "v_add_f32 v106, v106, v102\n"
      "v_add_f32 v107, v107, v103\n"
      "v_cvt_pk_bf16_f32 v104, 0, v104\n"
      "v_cvt_pk_bf16_f32 v105, 0, v105\n"
      "v_mfma_f32_16x16x32_bf16 v[76:79], v[128:131], v[176:179], v[76:79]\n"
      "v_cvt_pk_bf16_f32 v106, 0, v106\n"
      "v_cvt_pk_bf16_f32 v107, 0, v107\n"
      "v_mfma_f32_16x16x32_bf16 v[80:83], v[112:115], v[184:187], v[80:83]\n"
      "buffer_store_dword v206, v239, s[32:35], s59 offen offset:0 sc1\n"
      "buffer_store_dword v207, v239, s[32:35], s59 offen offset:128 sc1\n"
      "ds_write_b128 v44, v[72:75] offset:2048\n"
      "v_cvt_pk_bf16_f32 v108, v104, v105\n"
      "v_cvt_pk_bf16_f32 v109, v106, v107\n"
      "v_mul_f32 v104, s43, v104\n"
      "v_add_u32 v35, 12288, v7\n"
      "v_mfma_f32_16x16x32_bf16 v[80:83], v[124:127], v[180:183], v[80:83]\n"
      "global_load_dwordx4 a[32:35], v35, s[8:9] offset:-4096 sc1\n"
      "v_mul_f32 v105, s43, v105\n"
      "v_mul_f32 v106, s43, v106\n"
      "v_mul_f32 v107, s43, v107\n"
      "v_mfma_f32_16x16x32_bf16 v[80:83], v[136:139], v[176:179], v[80:83]\n"
      "ds_write_b128 v59, v[76:79] offset:2048\n"
      "ds_read_b128 v[232:235], v46\n"
      "v_cvt_pk_bf16_f32 v110, v104, v105\n"
      "v_cvt_pk_bf16_f32 v111, v106, v107\n"
      "v_add_u32 v96, v108, v109\n"
      "v_mfma_f32_16x16x32_bf16 v[84:87], v[116:119], v[184:187], v[84:87]\n"
      "ds_read_b128 v[236:239], v46 offset:7680\n"
      "v_add_u32 v97, v110, v111\n"
      "v_mfma_f32_16x16x32_bf16 v[84:87], v[128:131], v[180:183], v[84:87]\n"
      "buffer_store_dwordx2 v[110:111], v50, s[32:35], s78 offen sc1\n"
      "global_load_dwordx4 a[36:39], v35, s[8:9] offset:-3072 sc1\n"
      "v_add_u32_dpp v96, v96, v96 quad_perm:[1,0,3,2] row_mask:0xf bank_mask:0xf\n"
      "v_add_u32_dpp v97, v97, v97 quad_perm:[1,0,3,2] row_mask:0xf bank_mask:0xf\n"
      "s_add_u32 s4, s26, s71\n"
      "s_addc_u32 s5, s27, 0\n"
      "v_mfma_f32_16x16x32_bf16 v[84:87], v[140:143], v[176:179], v[84:87]\n"
      "ds_write_b128 v44, v[80:83] offset:4096\n"
      "v_add_u32_dpp v96, v96, v96 quad_perm:[2,3,0,1] row_mask:0xf bank_mask:0xf\n"
      "v_add_u32_dpp v97, v97, v97 quad_perm:[2,3,0,1] row_mask:0xf bank_mask:0xf\n"
      "s_add_u32 s6, s26, s72\n"
      "s_addc_u32 s7, s27, 0\n"
      "v_mfma_f32_16x16x32_bf16 v[88:91], v[112:115], v[188:191], v[88:91]\n"
      "v_add_u32_dpp v96, v96, v96 row_half_mirror row_mask:0xf bank_mask:0xf\n"
      "v_add_u32_dpp v97, v97, v97 row_half_mirror row_mask:0xf bank_mask:0xf\n"
      "v_mfma_f32_16x16x32_bf16 v[88:91], v[124:127], v[184:187], v[88:91]\n"
      "buffer_store_dwordx2 v[108:109], v37, s[32:35], s55 offen sc1\n"
      "global_load_dwordx4 a[40:43], v35, s[8:9] offset:-2048 sc1\n"
      "v_mov_b32 v98, v96\n"
      "v_add_u32_dpp v97, v97, v97 row_mirror row_mask:0xf bank_mask:0xf\n"
      "v_mfma_f32_16x16x32_bf16 v[88:91], v[136:139], v[180:183], v[88:91]\n"
      "L_pg2_%=:\n"
      "s_cbranch_execz L_pg6_%=\n"
      "ds_write_b128 v59, v[84:87] offset:4096\n"
      "s_mov_b32 s42, 0xaaaa5555\n"
      "s_mov_b32 s43, s42\n"
      "s_add_u32 s10, s26, s75\n"
      "s_addc_u32 s11, s27, 0\n"
      "v_mfma_f32_16x16x32_bf16 v[88:91], v[148:151], v[176:179], v[88:91]\n"
      "v_permlane16_swap_b32 v96, v98\n"
      "v_add_u32 v96, v96, v98\n"
      "v_mfma_f32_16x16x32_bf16 v[92:95], v[116:119], v[188:191], v[92:95]\n"
      "global_load_dwordx4 a[44:47], v35, s[8:9] offset:-1024 sc1\n"
      "v_cndmask_b32 v96, v97, v96, s[42:43]\n"
      "v_sub_u32 v96, v52, v96\n"
      "s_add_u32 s12, s10, 49152\n"
      "s_addc_u32 s13, s11, 0\n"
      "v_mfma_f32_16x16x32_bf16 v[92:95], v[128:131], v[184:187], v[92:95]\n"
      "buffer_store_dword v96, v49, s[32:35], s77 offen sc1\n"
      "s_lshl_b32 s71, s30, 2\n"
      "v_mfma_f32_16x16x32_bf16 v[92:95], v[140:143], v[180:183], v[92:95]\n"
      "ds_write_b128 v44, v[88:91] offset:6144\n"
      "v_mfma_f32_16x16x32_bf16 v[92:95], v[152:155], v[176:179], v[92:95]\n"
      "global_load_dwordx4 a[48:51], v35, s[8:9] offset:0 sc1\n"
      "s_nop 7\n"
      "ds_write_b128 v59, v[92:95] offset:6144\n"
      "v_readlane_b32 s40, v204, 0\n"
      "v_readlane_b32 s42, v205, 4\n"
      "v_xor_b32 v33, 16, v31\n"
      "v_readlane_b32 s58, v204, 8\n"
      "v_readlane_b32 s48, v204, 16\n"
      "v_readlane_b32 s50, v205, 20\n"
      "v_readlane_b32 s52, v204, 24\n"
      "v_readlane_b32 s41, v205, 12\n"
      "v_readlane_b32 s49, v205, 28\n"
      "s_mov_b32 vcc_lo, 0xaa55aa55\n"
      "s_mov_b32 vcc_hi, 0xaa55aa55\n"
      "v_add_u32 v40, s73, v7\n"
      "v_lshlrev_b32 v96, 16, v210\n"
      "v_and_b32 v97, 0xffff0000, v210\n"
      "v_lshlrev_b32 v98, 16, v214\n"
      "v_and_b32 v99, 0xffff0000, v214\n"
      "v_lshlrev_b32 v100, 16, v218\n"
      "v_and_b32 v101, 0xffff0000, v218\n"
      "v_lshlrev_b32 v102, 16, v222\n"
      "v_and_b32 v103, 0xffff0000, v222\n"
      "s_waitcnt lgkmcnt(0)\n"
      "s_barrier\n"
      "ds_read_b128 v[64:67], v31 offset:0\n"
      "ds_read_b128 v[68:71], v33 offset:0\n"
      "ds_read_b128 v[72:75], v31 offset:8192\n"
      "ds_read_b128 v[76:79], v33 offset:8192\n"
      "ds_read_b128 v[80:83], v31 offset:16384\n"
      "ds_read_b128 v[84:87], v33 offset:16384\n"
      "ds_read_b128 v[88:91], v31 offset:24576\n"
      "ds_read_b128 v[92:95], v33 offset:24576\n"
      "v_add_u32 v34, 12288, v7\n"
      "global_load_dwordx4 a[52:55], v34, s[8:9] offset:1024 sc1\n"
      "v_lshlrev_b32 v112, 16, v192\n"
      "v_and_b32 v113, 0xffff0000, v192\n"
      "v_lshlrev_b32 v114, 16, v193\n"
      "v_and_b32 v115, 0xffff0000, v193\n"
      "v_lshlrev_b32 v116, 16, v194\n"
      "v_and_b32 v117, 0xffff0000, v194\n"
      "v_lshlrev_b32 v118, 16, v195\n"
      "v_and_b32 v119, 0xffff0000, v195\n"
      "v_pk_add_f32 v[104:105], v[112:113], v[96:97] neg_lo:[0,1] neg_hi:[0,1]\n"
      "v_pk_add_f32 v[106:107], v[114:115], v[98:99] neg_lo:[0,1] neg_hi:[0,1]\n"
      "v_pk_add_f32 v[108:109], v[116:117], v[100:101] neg_lo:[0,1] neg_hi:[0,1]\n"
      "v_pk_add_f32 v[110:111], v[118:119], v[102:103] neg_lo:[0,1] neg_hi:[0,1]\n"
      "global_load_dwordx4 a[56:59], v34, s[8:9] offset:2048 sc1\n"
      "v_cvt_pk_bf16_f32 v104, 0, v104\n"
      "v_cvt_pk_bf16_f32 v105, 0, v105\n"
      "v_cvt_pk_bf16_f32 v106, 0, v106\n"
      "v_cvt_pk_bf16_f32 v107, 0, v107\n"
      "v_cvt_pk_bf16_f32 v108, 0, v108\n"
      "v_cvt_pk_bf16_f32 v109, 0, v109\n"
      "v_cvt_pk_bf16_f32 v110, 0, v110\n"
      "v_cvt_pk_bf16_f32 v111, 0, v111\n"
      "v_pk_mul_f32 v[104:105], v[104:105], v[208:209]\n"
      "v_pk_mul_f32 v[106:107], v[106:107], v[212:213]\n"
      "v_pk_mul_f32 v[108:109], v[108:109], v[216:217]\n"
      "v_pk_mul_f32 v[110:111], v[110:111], v[220:221]\n"
      "global_load_dwordx4 a[60:63], v34, s[8:9] offset:3072 sc1\n"
      "v_cvt_pk_bf16_f32 v104, 0, v104\n"
      "v_cvt_pk_bf16_f32 v105, 0, v105\n"
      "v_cvt_pk_bf16_f32 v106, 0, v106\n"
      "v_cvt_pk_bf16_f32 v107, 0, v107\n"
      "v_cvt_pk_bf16_f32 v108, 0, v108\n"
      "v_cvt_pk_bf16_f32 v109, 0, v109\n"
      "v_cvt_pk_bf16_f32 v110, 0, v110\n"
      "v_cvt_pk_bf16_f32 v111, 0, v111\n"
      "v_pk_mul_f32 v[104:105], v[104:105], s[42:43] op_sel_hi:[1,0]\n"
      "v_pk_mul_f32 v[106:107], v[106:107], s[42:43] op_sel_hi:[1,0]\n"
      "v_pk_mul_f32 v[108:109], v[108:109], s[42:43] op_sel_hi:[1,0]\n"
      "v_pk_mul_f32 v[110:111], v[110:111], s[42:43] op_sel_hi:[1,0]\n"
      "v_add_u32 v34, 20480, v7\n"
      "global_load_dwordx4 a[64:67], v34, s[8:9] offset:-4096 sc1\n"
      "v_cvt_pk_bf16_f32 v104, 0, v104\n"
      "v_cvt_pk_bf16_f32 v105, 0, v105\n"
      "v_cvt_pk_bf16_f32 v106, 0, v106\n"
      "v_cvt_pk_bf16_f32 v107, 0, v107\n"
      "v_cvt_pk_bf16_f32 v108, 0, v108\n"
      "v_cvt_pk_bf16_f32 v109, 0, v109\n"
      "v_cvt_pk_bf16_f32 v110, 0, v110\n"
      "v_cvt_pk_bf16_f32 v111, 0, v111\n"
      "v_pk_add_f32 v[104:105], v[104:105], s[40:41] op_sel_hi:[1,0]\n"
      "v_pk_add_f32 v[106:107], v[106:107], s[40:41] op_sel_hi:[1,0]\n"
      "v_pk_add_f32 v[108:109], v[108:109], s[40:41] op_sel_hi:[1,0]\n"
      "v_pk_add_f32 v[110:111], v[110:111], s[40:41] op_sel_hi:[1,0]\n"
      "global_load_dwordx4 a[68:71], v34, s[8:9] offset:-3072 sc1\n"
      "v_cvt_pk_bf16_f32 v104, 0, v104\n"
      "v_cvt_pk_bf16_f32 v105, 0, v105\n"
      "v_cvt_pk_bf16_f32 v106, 0, v106\n"
      "v_cvt_pk_bf16_f32 v107, 0, v107\n"
      "v_cvt_pk_bf16_f32 v108, 0, v108\n"
      "v_cvt_pk_bf16_f32 v109, 0, v109\n"
      "v_cvt_pk_bf16_f32 v110, 0, v110\n"
      "v_cvt_pk_bf16_f32 v111, 0, v111\n"
      "v_and_b32 v120, 0xffff0000, v235\n"
      "v_lshlrev_b32 v121, 16, v235\n"
      "v_and_b32 v122, 0xffff0000, v234\n"
      "v_lshlrev_b32 v123, 16, v234\n"
      "global_load_dwordx4 a[72:75], v34, s[8:9] offset:-2048 sc1\n"
      "v_and_b32 v124, 0xffff0000, v233\n"
      "v_lshlrev_b32 v125, 16, v233\n"
      "v_and_b32 v126, 0xffff0000, v232\n"
      "v_lshlrev_b32 v127, 16, v232\n"
      "v_pk_mul_f32 v[120:121], v[120:121], s[14:15] op_sel_hi:[1,0]\n"
      "v_pk_mul_f32 v[122:123], v[122:123], s[14:15] op_sel_hi:[1,0]\n"
      "v_pk_mul_f32 v[124:125], v[124:125], s[14:15] op_sel_hi:[1,0]\n"
      "v_pk_mul_f32 v[126:127], v[126:127], s[14:15] op_sel_hi:[1,0]\n"
      "v_cvt_pk_bf16_f32 v120, 0, v120\n"
      "v_cvt_pk_bf16_f32 v121, 0, v121\n"
      "v_cvt_pk_bf16_f32 v122, 0, v122\n"
      "v_cvt_pk_bf16_f32 v123, 0, v123\n"
      "global_load_dwordx4 a[76:79], v34, s[8:9] offset:-1024 sc1\n"
      "v_cvt_pk_bf16_f32 v124, 0, v124\n"
      "v_cvt_pk_bf16_f32 v125, 0, v125\n"
      "v_cvt_pk_bf16_f32 v126, 0, v126\n"
      "v_cvt_pk_bf16_f32 v127, 0, v127\n"
      "s_add_u32 s43, s89, 32768\n"
      "s_add_u32 s53, s89, 0x1000000\n"
      "s_add_u32 s54, s43, 0x1000000\n"
      "s_add_u32 s55, s79, 176128\n"
      "s_add_u32 s45, s81, 65536\n"
      "s_waitcnt lgkmcnt(0)\n"
      "v_pk_add_f32 v[64:65], v[64:65], v[72:73]\n"
      "v_pk_add_f32 v[66:67], v[66:67], v[74:75]\n"
      "global_load_dwordx4 a[80:83], v34, s[8:9] offset:0 sc1\n"
      "v_pk_add_f32 v[68:69], v[68:69], v[76:77]\n"
      "v_pk_add_f32 v[70:71], v[70:71], v[78:79]\n"
      "v_cvt_pk_bf16_f32 v64, 0, v64\n"
      "v_cvt_pk_bf16_f32 v65, 0, v65\n"
      "v_cvt_pk_bf16_f32 v66, 0, v66\n"
      "v_cvt_pk_bf16_f32 v67, 0, v67\n"
      "v_cvt_pk_bf16_f32 v68, 0, v68\n"
      "v_cvt_pk_bf16_f32 v69, 0, v69\n"
      "v_cvt_pk_bf16_f32 v70, 0, v70\n"
      "v_cvt_pk_bf16_f32 v71, 0, v71\n"
      "v_pk_add_f32 v[120:121], v[120:121], v[64:65]\n"
      "v_pk_add_f32 v[122:123], v[122:123], v[66:67]\n"
      "global_load_dwordx4 a[84:87], v34, s[8:9] offset:1024 sc1\n"
      "v_pk_add_f32 v[124:125], v[124:125], v[68:69]\n"
      "v_pk_add_f32 v[126:127], v[126:127], v[70:71]\n"
      "v_cvt_pk_bf16_f32 v120, 0, v120\n"
      "v_cvt_pk_bf16_f32 v121, 0, v121\n"
      "v_cvt_pk_bf16_f32 v122, 0, v122\n"
      "v_cvt_pk_bf16_f32 v123, 0, v123\n"
      "v_cvt_pk_bf16_f32 v124, 0, v124\n"
      "v_cvt_pk_bf16_f32 v125, 0, v125\n"
      "v_cvt_pk_bf16_f32 v126, 0, v126\n"
      "v_cvt_pk_bf16_f32 v127, 0, v127\n"
      "v_pk_mul_f32 v[104:105], v[104:105], v[120:121]\n"
      "v_pk_mul_f32 v[106:107], v[106:107], v[122:123]\n"
      "global_load_dwordx4 a[88:91], v34, s[8:9] offset:2048 sc1\n"
      "v_pk_mul_f32 v[108:109], v[108:109], v[124:125]\n"
      "v_pk_mul_f32 v[110:111], v[110:111], v[126:127]\n"
      "v_cvt_pk_bf16_f32 v104, 0, v104\n"
      "v_cvt_pk_bf16_f32 v105, 0, v105\n"
      "v_cvt_pk_bf16_f32 v106, 0, v106\n"
      "v_cvt_pk_bf16_f32 v107, 0, v107\n"
      "v_cvt_pk_bf16_f32 v108, 0, v108\n"
      "v_cvt_pk_bf16_f32 v109, 0, v109\n"
      "v_cvt_pk_bf16_f32 v110, 0, v110\n"
      "v_cvt_pk_bf16_f32 v111, 0, v111\n"
      "v_pk_mul_f32 v[104:105], v[104:105], s[58:59] op_sel_hi:[1,0]\n"
      "v_pk_mul_f32 v[106:107], v[106:107], s[58:59] op_sel_hi:[1,0]\n"
      "global_load_dwordx4 a[92:95], v34, s[8:9] offset:3072 sc1\n"
      "v_pk_mul_f32 v[108:109], v[108:109], s[58:59] op_sel_hi:[1,0]\n"
      "v_pk_mul_f32 v[110:111], v[110:111], s[58:59] op_sel_hi:[1,0]\n"
      "v_cvt_pk_bf16_f32 v104, 0, v104\n"
      "v_cvt_pk_bf16_f32 v105, 0, v105\n"
      "v_cvt_pk_bf16_f32 v106, 0, v106\n"
      "v_cvt_pk_bf16_f32 v107, 0, v107\n"
      "v_cvt_pk_bf16_f32 v108, 0, v108\n"
      "v_cvt_pk_bf16_f32 v109, 0, v109\n"
      "v_cvt_pk_bf16_f32 v110, 0, v110\n"
      "v_cvt_pk_bf16_f32 v111, 0, v111\n"
      "v_pk_add_f32 v[104:105], v[104:105], v[112:113]\n"
      "v_pk_add_f32 v[106:107], v[106:107], v[114:115]\n"
      "v_add_u32 v34, 28672, v7\n"
      "global_load_dwordx4 a[96:99], v34, s[8:9] offset:-4096 sc1\n"
      "v_pk_add_f32 v[108:109], v[108:109], v[116:117]\n"
      "v_pk_add_f32 v[110:111], v[110:111], v[118:119]\n"
      "v_cvt_pk_bf16_f32 v104, 0, v104\n"
      "v_cvt_pk_bf16_f32 v105, 0, v105\n"
      "v_cvt_pk_bf16_f32 v106, 0, v106\n"
      "v_cvt_pk_bf16_f32 v107, 0, v107\n"
      "v_cvt_pk_bf16_f32 v108, 0, v108\n"
      "v_cvt_pk_bf16_f32 v109, 0, v109\n"
      "v_cvt_pk_bf16_f32 v110, 0, v110\n"
      "v_cvt_pk_bf16_f32 v111, 0, v111\n"
      "v_pk_mul_f32 v[120:121], v[104:105], s[40:41] op_sel:[0,1] op_sel_hi:[1,1]\n"
      "v_pk_mul_f32 v[122:123], v[106:107], s[40:41] op_sel:[0,1] op_sel_hi:[1,1]\n"
      "v_pk_mul_f32 v[124:125], v[108:109], s[40:41] op_sel:[0,1] op_sel_hi:[1,1]\n"
      "v_pk_mul_f32 v[126:127], v[110:111], s[40:41] op_sel:[0,1] op_sel_hi:[1,1]\n"
      "v_cvt_pk_bf16_f32 v128, v120, v121\n"
      "v_cvt_pk_bf16_f32 v129, v122, v123\n"
      "v_cvt_pk_bf16_f32 v130, v124, v125\n"
      "v_cvt_pk_bf16_f32 v131, v126, v127\n"
      "buffer_store_dwordx4 v[128:131], v29, s[32:35], s89 offen sc1\n"
      "v_cvt_pk_bf16_f32 v132, v104, v105\n"
      "v_cvt_pk_bf16_f32 v133, v106, v107\n"
      "v_cvt_pk_bf16_f32 v134, v108, v109\n"
      "v_cvt_pk_bf16_f32 v135, v110, v111\n"
      "v_add3_u32 v35, v128, v129, v130\n"
      "v_add_u32 v35, v35, v131\n"
      "buffer_store_dwordx4 v[132:135], v36, s[32:35], s53 offen sc1\n"
      "v_add3_u32 v38, v132, v133, v134\n"
      "v_add_u32 v38, v38, v135\n"
      "v_lshlrev_b32 v112, 16, v196\n"
      "v_add_u32_dpp v35, v35, v35 quad_perm:[1,0,3,2] row_mask:0xf bank_mask:0xf\n"
      "v_and_b32 v113, 0xffff0000, v196\n"
      "v_add_u32_dpp v38, v38, v38 quad_perm:[1,0,3,2] row_mask:0xf bank_mask:0xf\n"
      "v_lshlrev_b32 v114, 16, v197\n"
      "v_add_u32_dpp v35, v35, v35 quad_perm:[2,3,0,1] row_mask:0xf bank_mask:0xf\n"
      "v_and_b32 v115, 0xffff0000, v197\n"
      "v_add_u32_dpp v38, v38, v38 quad_perm:[2,3,0,1] row_mask:0xf bank_mask:0xf\n"
      "v_lshlrev_b32 v116, 16, v198\n"
      "v_add_u32_dpp v35, v35, v35 row_half_mirror row_mask:0xf bank_mask:0xf\n"
      "v_and_b32 v117, 0xffff0000, v198\n"
      "v_add_u32_dpp v38, v38, v38 row_ror:8 row_mask:0xf bank_mask:0xf\n"
      "v_lshlrev_b32 v118, 16, v199\n"
      "v_cndmask_b32 v35, v35, v38, vcc\n"
      "v_and_b32 v119, 0xffff0000, v199\n"
      "v_sub_u32 v35, s30, v35\n"
      "v_pk_add_f32 v[104:105], v[112:113], v[96:97] neg_lo:[0,1] neg_hi:[0,1]\n"
      "buffer_store_dword v35, v30, s[32:35], s81 offen sc1\n"
      "s_waitcnt vmcnt(23)\n"
      "buffer_store_dwordx2 v[52:53], v48, s[32:35], s88 offen sc1\n"
      "v_pk_add_f32 v[106:107], v[114:115], v[98:99] neg_lo:[0,1] neg_hi:[0,1]\n"
      "v_pk_add_f32 v[108:109], v[116:117], v[100:101] neg_lo:[0,1] neg_hi:[0,1]\n"
      "v_pk_add_f32 v[110:111], v[118:119], v[102:103] neg_lo:[0,1] neg_hi:[0,1]\n"
      "v_cvt_pk_bf16_f32 v104, 0, v104\n"
      "v_cvt_pk_bf16_f32 v105, 0, v105\n"
      "v_cvt_pk_bf16_f32 v106, 0, v106\n"
      "v_cvt_pk_bf16_f32 v107, 0, v107\n"
      "v_cvt_pk_bf16_f32 v108, 0, v108\n"
      "v_cvt_pk_bf16_f32 v109, 0, v109\n"
      "v_cvt_pk_bf16_f32 v110, 0, v110\n"
      "v_cvt_pk_bf16_f32 v111, 0, v111\n"
      "buffer_store_dwordx2 v[52:53], v48, s[32:35], s55 offen sc1\n"
      "v_pk_mul_f32 v[104:105], v[104:105], v[208:209]\n"
      "v_pk_mul_f32 v[106:107], v[106:107], v[212:213]\n"
      "v_pk_mul_f32 v[108:109], v[108:109], v[216:217]\n"
      "v_pk_mul_f32 v[110:111], v[110:111], v[220:221]\n"
      "v_cvt_pk_bf16_f32 v104, 0, v104\n"
      "v_cvt_pk_bf16_f32 v105, 0, v105\n"
      "v_cvt_pk_bf16_f32 v106, 0, v106\n"
      "v_cvt_pk_bf16_f32 v107, 0, v107\n"
      "v_cvt_pk_bf16_f32 v108, 0, v108\n"
      "v_cvt_pk_bf16_f32 v109, 0, v109\n"
      "v_cvt_pk_bf16_f32 v110, 0, v110\n"
      "global_load_dwordx4 a[100:103], v34, s[8:9] offset:-3072 sc1\n"
      "v_cvt_pk_bf16_f32 v111, 0, v111\n"
      "v_pk_mul_f32 v[104:105], v[104:105], s[50:51] op_sel_hi:[1,0]\n"
      "v_pk_mul_f32 v[106:107], v[106:107], s[50:51] op_sel_hi:[1,0]\n"
      "v_pk_mul_f32 v[108:109], v[108:109], s[50:51] op_sel_hi:[1,0]\n"
      "v_pk_mul_f32 v[110:111], v[110:111], s[50:51] op_sel_hi:[1,0]\n"
      "v_cvt_pk_bf16_f32 v104, 0, v104\n"
      "v_cvt_pk_bf16_f32 v105, 0, v105\n"
      "v_cvt_pk_bf16_f32 v106, 0, v106\n"
      "v_cvt_pk_bf16_f32 v107, 0, v107\n"
      "v_cvt_pk_bf16_f32 v108, 0, v108\n"
      "v_cvt_pk_bf16_f32 v109, 0, v109\n"
      "s_add_u32 m0, s73, 0\n"
      "v_add_u32 v43, 0, v40\n"
      "global_load_lds_dwordx4 v43, s[4:5] offset:0 sc1\n"
      "v_cvt_pk_bf16_f32 v110, 0, v110\n"
      "v_cvt_pk_bf16_f32 v111, 0, v111\n"
      "v_pk_add_f32 v[104:105], v[104:105], s[48:49] op_sel_hi:[1,0]\n"
      "v_pk_add_f32 v[106:107], v[106:107], s[48:49] op_sel_hi:[1,0]\n"
      "v_pk_add_f32 v[108:109], v[108:109], s[48:49] op_sel_hi:[1,0]\n"
      "v_pk_add_f32 v[110:111], v[110:111], s[48:49] op_sel_hi:[1,0]\n"
      "v_cvt_pk_bf16_f32 v104, 0, v104\n"
      "v_cvt_pk_bf16_f32 v105, 0, v105\n"
      "v_cvt_pk_bf16_f32 v106, 0, v106\n"
      "v_cvt_pk_bf16_f32 v107, 0, v107\n"
      "v_cvt_pk_bf16_f32 v108, 0, v108\n"
      "global_load_lds_dwordx4 v43, s[4:5] offset:1024 sc1\n"
      "v_cvt_pk_bf16_f32 v109, 0, v109\n"
      "v_cvt_pk_bf16_f32 v110, 0, v110\n"
      "v_cvt_pk_bf16_f32 v111, 0, v111\n"
      "v_and_b32 v120, 0xffff0000, v239\n"
      "v_lshlrev_b32 v121, 16, v239\n"
      "v_and_b32 v122, 0xffff0000, v238\n"
      "v_lshlrev_b32 v123, 16, v238\n"
      "v_and_b32 v124, 0xffff0000, v237\n"
      "v_lshlrev_b32 v125, 16, v237\n"
      "v_and_b32 v126, 0xffff0000, v236\n"
      "v_lshlrev_b32 v127, 16, v236\n"
      "global_load_lds_dwordx4 v43, s[4:5] offset:2048 sc1\n"
      "v_pk_mul_f32 v[120:121], v[120:121], s[14:15] op_sel:[0,1] op_sel_hi:[1,1]\n"
      "v_pk_mul_f32 v[122:123], v[122:123], s[14:15] op_sel:[0,1] op_sel_hi:[1,1]\n"
      "v_pk_mul_f32 v[124:125], v[124:125], s[14:15] op_sel:[0,1] op_sel_hi:[1,1]\n"
      "v_pk_mul_f32 v[126:127], v[126:127], s[14:15] op_sel:[0,1] op_sel_hi:[1,1]\n"
      "v_cvt_pk_bf16_f32 v120, 0, v120\n"
      "v_cvt_pk_bf16_f32 v121, 0, v121\n"
      "v_cvt_pk_bf16_f32 v122, 0, v122\n"
      "v_cvt_pk_bf16_f32 v123, 0, v123\n"
      "v_cvt_pk_bf16_f32 v124, 0, v124\n"
      "v_cvt_pk_bf16_f32 v125, 0, v125\n"
      "v_cvt_pk_bf16_f32 v126, 0, v126\n"
      "global_load_lds_dwordx4 v43, s[4:5] offset:3072 sc1\n"
      "v_cvt_pk_bf16_f32 v127, 0, v127\n"
      "v_pk_add_f32 v[80:81], v[80:81], v[88:89]\n"
      "v_pk_add_f32 v[82:83], v[82:83], v[90:91]\n"
      "v_pk_add_f32 v[84:85], v[84:85], v[92:93]\n"
      "v_pk_add_f32 v[86:87], v[86:87], v[94:95]\n"
      "v_cvt_pk_bf16_f32 v80, 0, v80\n"
      "v_cvt_pk_bf16_f32 v81, 0, v81\n"
      "v_cvt_pk_bf16_f32 v82, 0, v82\n"
      "v_cvt_pk_bf16_f32 v83, 0, v83\n"
      "v_cvt_pk_bf16_f32 v84, 0, v84\n"
      "v_cvt_pk_bf16_f32 v85, 0, v85\n"
      "s_mov_b32 s47, 0\n"
      "buffer_load_dwordx2 v[2:3], v5, s[32:35], s47 offen sc1\n"
      "v_cvt_pk_bf16_f32 v86, 0, v86\n"
      "v_cvt_pk_bf16_f32 v87, 0, v87\n"
      "v_pk_add_f32 v[120:121], v[120:121], v[80:81]\n"
      "v_pk_add_f32 v[122:123], v[122:123], v[82:83]\n"
      "v_pk_add_f32 v[124:125], v[124:125], v[84:85]\n"
      "v_pk_add_f32 v[126:127], v[126:127], v[86:87]\n"
      "v_cvt_pk_bf16_f32 v120, 0, v120\n"
      "v_cvt_pk_bf16_f32 v121, 0, v121\n"
      "v_cvt_pk_bf16_f32 v122, 0, v122\n"
      "v_cvt_pk_bf16_f32 v123, 0, v123\n"
      "v_cvt_pk_bf16_f32 v124, 0, v124\n"
      "s_add_u32 m0, s73, 16384\n"
      "v_add_u32 v43, 81920, v40\n"
      "global_load_lds_dwordx4 v43, s[4:5] offset:0 sc1\n"
      "v_cvt_pk_bf16_f32 v125, 0, v125\n"
      "v_cvt_pk_bf16_f32 v126, 0, v126\n"
      "v_cvt_pk_bf16_f32 v127, 0, v127\n"
      "v_pk_mul_f32 v[104:105], v[104:105], v[120:121]\n"
      "v_pk_mul_f32 v[106:107], v[106:107], v[122:123]\n"
      "v_pk_mul_f32 v[108:109], v[108:109], v[124:125]\n"
      "v_pk_mul_f32 v[110:111], v[110:111], v[126:127]\n"
      "v_cvt_pk_bf16_f32 v104, 0, v104\n"
      "v_cvt_pk_bf16_f32 v105, 0, v105\n"
      "v_cvt_pk_bf16_f32 v106, 0, v106\n"
      "v_cvt_pk_bf16_f32 v107, 0, v107\n"
      "global_load_lds_dwordx4 v43, s[4:5] offset:1024 sc1\n"
      "v_cvt_pk_bf16_f32 v108, 0, v108\n"
      "v_cvt_pk_bf16_f32 v109, 0, v109\n"
      "v_cvt_pk_bf16_f32 v110, 0, v110\n"
      "v_cvt_pk_bf16_f32 v111, 0, v111\n"
      "v_pk_mul_f32 v[104:105], v[104:105], s[52:53] op_sel_hi:[1,0]\n"
      "v_pk_mul_f32 v[106:107], v[106:107], s[52:53] op_sel_hi:[1,0]\n"
      "v_pk_mul_f32 v[108:109], v[108:109], s[52:53] op_sel_hi:[1,0]\n"
      "v_pk_mul_f32 v[110:111], v[110:111], s[52:53] op_sel_hi:[1,0]\n"
      "v_cvt_pk_bf16_f32 v104, 0, v104\n"
      "v_cvt_pk_bf16_f32 v105, 0, v105\n"
      "v_cvt_pk_bf16_f32 v106, 0, v106\n"
      "global_load_lds_dwordx4 v43, s[4:5] offset:2048 sc1\n"
      "v_cvt_pk_bf16_f32 v107, 0, v107\n"
      "v_cvt_pk_bf16_f32 v108, 0, v108\n"
      "v_cvt_pk_bf16_f32 v109, 0, v109\n"
      "v_cvt_pk_bf16_f32 v110, 0, v110\n"
      "v_cvt_pk_bf16_f32 v111, 0, v111\n"
      "v_pk_add_f32 v[104:105], v[104:105], v[112:113]\n"
      "v_pk_add_f32 v[106:107], v[106:107], v[114:115]\n"
      "v_pk_add_f32 v[108:109], v[108:109], v[116:117]\n"
      "v_pk_add_f32 v[110:111], v[110:111], v[118:119]\n"
      "v_cvt_pk_bf16_f32 v104, 0, v104\n"
      "v_cvt_pk_bf16_f32 v105, 0, v105\n"
      "global_load_lds_dwordx4 v43, s[4:5] offset:3072 sc1\n"
      "v_cvt_pk_bf16_f32 v106, 0, v106\n"
      "v_cvt_pk_bf16_f32 v107, 0, v107\n"
      "v_cvt_pk_bf16_f32 v108, 0, v108\n"
      "v_cvt_pk_bf16_f32 v109, 0, v109\n"
      "v_cvt_pk_bf16_f32 v110, 0, v110\n"
      "v_cvt_pk_bf16_f32 v111, 0, v111\n"
      "v_pk_mul_f32 v[120:121], v[104:105], s[48:49] op_sel:[0,1] op_sel_hi:[1,1]\n"
      "v_pk_mul_f32 v[122:123], v[106:107], s[48:49] op_sel:[0,1] op_sel_hi:[1,1]\n"
      "v_pk_mul_f32 v[124:125], v[108:109], s[48:49] op_sel:[0,1] op_sel_hi:[1,1]\n"
      "v_pk_mul_f32 v[126:127], v[110:111], s[48:49] op_sel:[0,1] op_sel_hi:[1,1]\n"
      "v_cvt_pk_bf16_f32 v136, v120, v121\n"
      "v_cvt_pk_bf16_f32 v137, v122, v123\n"
      "v_cvt_pk_bf16_f32 v138, v124, v125\n"
      "v_cvt_pk_bf16_f32 v139, v126, v127\n"
      "buffer_store_dwordx4 v[136:139], v29, s[32:35], s43 offen sc1\n"
      "v_cvt_pk_bf16_f32 v140, v104, v105\n"
      "v_cvt_pk_bf16_f32 v141, v106, v107\n"
      "v_cvt_pk_bf16_f32 v142, v108, v109\n"
      "v_cvt_pk_bf16_f32 v143, v110, v111\n"
      "v_add3_u32 v35, v136, v137, v138\n"
      "v_add_u32 v35, v35, v139\n"
      "buffer_store_dwordx4 v[140:143], v36, s[32:35], s54 offen sc1\n"
      "v_add3_u32 v38, v140, v141, v142\n"
      "v_add_u32 v38, v38, v143\n"
      "v_add_u32_dpp v35, v35, v35 quad_perm:[1,0,3,2] row_mask:0xf bank_mask:0xf\n"
      "s_nop 0\n"
      "v_add_u32_dpp v38, v38, v38 quad_perm:[1,0,3,2] row_mask:0xf bank_mask:0xf\n"
      "v_add_u32_dpp v35, v35, v35 quad_perm:[2,3,0,1] row_mask:0xf bank_mask:0xf\n"
      "s_nop 0\n"
      "v_add_u32_dpp v38, v38, v38 quad_perm:[2,3,0,1] row_mask:0xf bank_mask:0xf\n"
      "v_add_u32_dpp v35, v35, v35 row_half_mirror row_mask:0xf bank_mask:0xf\n"
      "s_nop 0\n"
      "v_add_u32_dpp v38, v38, v38 row_ror:8 row_mask:0xf bank_mask:0xf\n"
      "v_cndmask_b32 v35, v35, v38, vcc\n"
      "v_sub_u32 v35, s30, v35\n"
      "buffer_store_dword v35, v30, s[32:35], s45 offen sc1\n"
      "global_load_dwordx4 a[104:107], v34, s[8:9] offset:-2048 sc1\n"
      "global_load_dwordx4 a[108:111], v34, s[8:9] offset:-1024 sc1\n"
      "global_load_dwordx4 a[112:115], v34, s[8:9] offset:0 sc1\n"
      "global_load_dwordx4 a[116:119], v34, s[8:9] offset:1024 sc1\n"
      "global_load_dwordx4 a[120:123], v34, s[8:9] offset:2048 sc1\n"
      "global_load_dwordx4 a[124:127], v34, s[8:9] offset:3072 sc1\n"
      "v_add_u32 v34, 36864, v7\n"
      "global_load_dwordx4 a[128:131], v34, s[8:9] offset:-4096 sc1\n"
      "global_load_dwordx4 a[132:135], v34, s[8:9] offset:-3072 sc1\n"
      "global_load_dwordx4 a[136:139], v34, s[8:9] offset:-2048 sc1\n"
      "global_load_dwordx4 a[140:143], v34, s[8:9] offset:-1024 sc1\n"
      "global_load_dwordx4 a[144:147], v34, s[8:9] offset:0 sc1\n"
      "global_load_dwordx4 a[148:151], v34, s[8:9] offset:1024 sc1\n"
      "global_load_dwordx4 a[152:155], v34, s[8:9] offset:2048 sc1\n"
      "global_load_dwordx4 a[156:159], v34, s[8:9] offset:3072 sc1\n"
      "v_xor_b32 v33, 32, v6\n"
      "v_xor_b32 v34, 64, v6\n"
      "v_xor_b32 v35, 96, v6\n"
      "v_add_u32 v45, 45056, v7\n"
      "s_waitcnt vmcnt(21)\n"
      "ds_read_b128 v[248:251], v40 offset:0\n"
      "ds_read_b128 v[252:255], v40 offset:1024\n"
      "ds_read_b128 v[36:39], v40 offset:2048\n"
      "ds_read_b128 v[60:63], v40 offset:3072\n"
      "s_waitcnt lgkmcnt(3)\n"
      "v_add3_u32 v2, v2, v248, v249\n"
      "v_add3_u32 v2, v2, v250, v251\n"
      "s_waitcnt lgkmcnt(2)\n"
      "v_add3_u32 v2, v2, v252, v253\n"
      "v_add3_u32 v2, v2, v254, v255\n"
      "s_waitcnt lgkmcnt(1)\n"
      "v_add3_u32 v2, v2, v36, v37\n"
      "v_add3_u32 v2, v2, v38, v39\n"
      "s_waitcnt lgkmcnt(0)\n"
      "v_add3_u32 v2, v2, v60, v61\n"
      "v_add3_u32 v2, v2, v62, v63\n"
      "s_nop 1\n"
      "v_add_u32_dpp v2, v2, v2 quad_perm:[1,0,3,2] row_mask:0xf bank_mask:0xf\n"
      "s_nop 1\n"
      "v_add_u32_dpp v2, v2, v2 quad_perm:[2,3,0,1] row_mask:0xf bank_mask:0xf\n"
      "s_nop 1\n"
      "v_add_u32_dpp v2, v2, v2 row_half_mirror row_mask:0xf bank_mask:0xf\n"
      "v_cmp_ne_u32 vcc, s71, v2\n"
      "s_cbranch_vccnz L_sx0_%=\n"
      "L_bx0_%=:\n"
      "s_barrier\n"
      "ds_read_b64_tr_b16 v[64:65], v6 offset:0\n"
      "ds_read_b64_tr_b16 v[66:67], v6 offset:2048\n"
      "ds_read_b64_tr_b16 v[80:81], v6 offset:4096\n"
      "ds_read_b64_tr_b16 v[82:83], v6 offset:6144\n"
      "ds_read_b64_tr_b16 v[68:69], v33 offset:0\n"
      "ds_read_b64_tr_b16 v[70:71], v33 offset:2048\n"
      "ds_read_b64_tr_b16 v[84:85], v33 offset:4096\n"
      "ds_read_b64_tr_b16 v[86:87], v33 offset:6144\n"
      "ds_read_b64_tr_b16 v[72:73], v34 offset:0\n"
      "ds_read_b64_tr_b16 v[74:75], v34 offset:2048\n"
      "ds_read_b64_tr_b16 v[88:89], v34 offset:4096\n"
      "ds_read_b64_tr_b16 v[90:91], v34 offset:6144\n"
      "ds_read_b64_tr_b16 v[76:77], v35 offset:0\n"
      "ds_read_b64_tr_b16 v[78:79], v35 offset:2048\n"
      "ds_read_b64_tr_b16 v[92:93], v35 offset:4096\n"
      "ds_read_b64_tr_b16 v[94:95], v35 offset:6144\n"
      "s_waitcnt lgkmcnt(0)\n"
      "v_mfma_f32_16x16x32_bf16 v[128:131], a[0:3], v[64:67], 0\n"
      "global_load_dwordx4 a[160:163], v45, s[8:9] offset:-4096 sc1\n"
      "v_lshrrev_b32 v42, 8, v7\n"
      "v_mfma_f32_16x16x32_bf16 v[128:131], a[12:15], v[80:83], v[128:131]\n"
      "ds_read_b64_tr_b16 v[96:97], v6 offset:8192\n"
      "v_bfe_u32 v41, v7, 4, 4\n"
      "v_mfma_f32_16x16x32_bf16 v[144:147], a[4:7], v[64:67], 0\n"
      "ds_read_b64_tr_b16 v[98:99], v6 offset:10240\n"
      "s_waitcnt vmcnt(18)\n"
      "v_mfma_f32_16x16x32_bf16 v[144:147], a[16:19], v[80:83], v[144:147]\n"
      "ds_read_b128 v[248:251], v40 offset:16384\n"
      "v_min_u32 v42, 2, v42\n"
      "v_mfma_f32_16x16x32_bf16 v[160:163], a[8:11], v[64:67], 0\n"
      "global_load_dwordx4 a[164:167], v45, s[8:9] offset:-3072 sc1\n"
      "ds_read_b128 v[252:255], v40 offset:17408\n"
      "ds_read_b64_tr_b16 v[100:101], v33 offset:8192\n"
      "v_min_u32 v41, 11, v41\n"
      "v_mfma_f32_16x16x32_bf16 v[160:163], a[20:23], v[80:83], v[160:163]\n"
      "ds_read_b128 v[36:39], v40 offset:18432\n"
      "ds_read_b64_tr_b16 v[102:103], v33 offset:10240\n"
      "v_mad_u32_u24 v42, v42, 48, v41\n"
      "v_mfma_f32_16x16x32_bf16 v[132:135], a[0:3], v[68:71], 0\n"
      "ds_read_b128 v[60:63], v40 offset:19456\n"
      "ds_read_b64_tr_b16 v[104:105], v34 offset:8192\n"
      "v_lshl_add_u32 v42, v42, 6, s64\n"
      "v_mfma_f32_16x16x32_bf16 v[132:135], a[12:15], v[84:87], v[132:135]\n"
      "ds_read_b64_tr_b16 v[106:107], v34 offset:10240\n"
      "s_add_u32 s75, s76, 176128\n"
      "v_mfma_f32_16x16x32_bf16 v[148:151], a[4:7], v[68:71], 0\n"
      "global_load_dwordx4 a[168:171], v45, s[8:9] offset:-2048 sc1\n"
      "ds_read_b64_tr_b16 v[108:109], v35 offset:8192\n"
      "v_mfma_f32_16x16x32_bf16 v[148:151], a[16:19], v[84:87], v[148:151]\n"
      "ds_read_b64_tr_b16 v[110:111], v35 offset:10240\n"
      "v_mfma_f32_16x16x32_bf16 v[164:167], a[8:11], v[68:71], 0\n"
      "ds_read_b64_tr_b16 v[112:113], v6 offset:12288\n"
      "s_waitcnt lgkmcnt(5)\n"
      "v_add3_u32 v3, v3, v248, v249\n"
      "v_add3_u32 v3, v3, v250, v251\n"
      "v_mfma_f32_16x16x32_bf16 v[164:167], a[20:23], v[84:87], v[164:167]\n"
      "ds_read_b64_tr_b16 v[114:115], v6 offset:14336\n"
      "v_add3_u32 v3, v3, v252, v253\n"
      "v_add3_u32 v3, v3, v254, v255\n"
      "v_mfma_f32_16x16x32_bf16 v[136:139], a[0:3], v[72:75], 0\n"
      "global_load_dwordx4 a[172:175], v45, s[8:9] offset:-1024 sc1\n"
      "v_add3_u32 v3, v3, v36, v37\n"
      "v_add3_u32 v3, v3, v38, v39\n"
      "v_mfma_f32_16x16x32_bf16 v[136:139], a[12:15], v[88:91], v[136:139]\n"
      "ds_read_b64_tr_b16 v[116:117], v33 offset:12288\n"
      "v_add3_u32 v3, v3, v60, v61\n"
      "v_mfma_f32_16x16x32_bf16 v[152:155], a[4:7], v[72:75], 0\n"
      "ds_read_b64_tr_b16 v[118:119], v33 offset:14336\n"
      "v_add3_u32 v3, v3, v62, v63\n"
      "v_mfma_f32_16x16x32_bf16 v[152:155], a[16:19], v[88:91], v[152:155]\n"
      "ds_read_b64_tr_b16 v[120:121], v34 offset:12288\n"
      "v_mfma_f32_16x16x32_bf16 v[168:171], a[8:11], v[72:75], 0\n"
      "global_load_dwordx4 a[176:179], v45, s[8:9] offset:0 sc1\n"
      "ds_read_b64_tr_b16 v[122:123], v34 offset:14336\n"
      "v_add_u32_dpp v3, v3, v3 quad_perm:[1,0,3,2] row_mask:0xf bank_mask:0xf\n"
      "v_mfma_f32_16x16x32_bf16 v[168:171], a[20:23], v[88:91], v[168:171]\n"
      "ds_read_b64_tr_b16 v[124:125], v35 offset:12288\n"
      "v_mfma_f32_16x16x32_bf16 v[140:143], a[0:3], v[76:79], 0\n"
      "ds_read_b64_tr_b16 v[126:127], v35 offset:14336\n"
      "v_add_u32_dpp v3, v3, v3 quad_perm:[2,3,0,1] row_mask:0xf bank_mask:0xf\n"
      "L_pg3_%=:\n"
      "s_cbranch_execz L_pg7_%=\n"
      "v_mfma_f32_16x16x32_bf16 v[140:143], a[12:15], v[92:95], v[140:143]\n"
      "v_mfma_f32_16x16x32_bf16 v[156:159], a[4:7], v[76:79], 0\n"
      "global_load_dwordx4 a[180:183], v45, s[8:9] offset:1024 sc1\n"
      "s_add_u32 m0, s73, 32768\n"
      "v_add_u32 v43, 16384, v40\n"
      "v_add_u32_dpp v3, v3, v3 row_half_mirror row_mask:0xf bank_mask:0xf\n"
      "v_cmp_ne_u32 vcc, s71, v3\n"
      "v_mfma_f32_16x16x32_bf16 v[156:159], a[16:19], v[92:95], v[156:159]\n"
      "v_mfma_f32_16x16x32_bf16 v[172:175], a[8:11], v[76:79], 0\n"
      "global_load_lds_dwordx4 v43, s[4:5] offset:0 sc1\n"
      "s_cbranch_vccnz L_sx1_%=\n"
      "L_bx1_%=:\n"
      "v_mfma_f32_16x16x32_bf16 v[172:175], a[20:23], v[92:95], v[172:175]\n"
      "s_waitcnt lgkmcnt(0)\n"
      "s_barrier\n"
      "v_mfma_f32_16x16x32_bf16 v[128:131], a[24:27], v[96:99], v[128:131]\n"
      "ds_read_b64_tr_b16 v[64:65], v6 offset:16384\n"
      "v_and_b32 v41, 0x1f0, v7\n"
      "v_mfma_f32_16x16x32_bf16 v[128:131], a[36:39], v[112:115], v[128:131]\n"
      "ds_read_b64_tr_b16 v[66:67], v6 offset:18432\n"
      "v_min_u32 v41, 0x170, v41\n"
      "v_mfma_f32_16x16x32_bf16 v[144:147], a[28:31], v[96:99], v[144:147]\n"
      "global_load_lds_dwordx4 v43, s[4:5] offset:1024 sc1\n"
      "v_lshrrev_b32 v52, 9, v7\n"
      "v_mfma_f32_16x16x32_bf16 v[144:147], a[40:43], v[112:115], v[144:147]\n"
      "ds_read_b64_tr_b16 v[68:69], v34 offset:16384\n"
      "v_mul_u32_u24 v52, 0x3c000, v52\n"
      "v_mfma_f32_16x16x32_bf16 v[160:163], a[32:35], v[96:99], v[160:163]\n"
      "ds_read_b64_tr_b16 v[70:71], v34 offset:18432\n"
      "v_add3_u32 v53, v52, v41, s65\n"
      "v_mfma_f32_16x16x32_bf16 v[160:163], a[44:47], v[112:115], v[160:163]\n"
      "ds_read_b64_tr_b16 v[72:73], v33 offset:16384\n"
      "s_mul_i32 s96, s60, 384\n"
      "v_mfma_f32_16x16x32_bf16 v[132:135], a[24:27], v[100:103], v[132:135]\n"
      "global_load_lds_dwordx4 v43, s[4:5] offset:2048 sc1\n"
      "s_add_u32 s96, s96, 160768\n"
      "v_mfma_f32_16x16x32_bf16 v[132:135], a[36:39], v[116:119], v[132:135]\n"
      "ds_read_b64_tr_b16 v[74:75], v33 offset:18432\n"
      "v_lshrrev_b32 v52, 4, v7\n"
      "v_mfma_f32_16x16x32_bf16 v[148:151], a[28:31], v[100:103], v[148:151]\n"
      "global_load_dwordx2 v[58:59], v42, s[26:27] sc1\n"
      "ds_read_b64_tr_b16 v[76:77], v35 offset:16384\n"
      "v_and_b32 v52, 0x30, v52\n"
      "v_mfma_f32_16x16x32_bf16 v[148:151], a[40:43], v[116:119], v[148:151]\n"
      "ds_read_b64_tr_b16 v[78:79], v35 offset:18432\n"
      "v_add_u32 v52, s96, v52\n"
      "v_mfma_f32_16x16x32_bf16 v[164:167], a[32:35], v[100:103], v[164:167]\n"
      "global_load_lds_dwordx4 v43, s[4:5] offset:3072 sc1\n"
      "s_mov_b32 s47, 32768\n"
      "v_mfma_f32_16x16x32_bf16 v[164:167], a[44:47], v[116:119], v[164:167]\n"
      "ds_read_b64_tr_b16 v[80:81], v6 offset:20480\n"
      "s_mul_i32 s40, s60, 9216\n"
      "v_mfma_f32_16x16x32_bf16 v[136:139], a[24:27], v[104:107], v[136:139]\n"
      "buffer_load_dwordx2 v[48:49], v5, s[32:35], s47 offen sc1\n"
      "ds_read_b64_tr_b16 v[82:83], v6 offset:22528\n"
      "s_add_u32 s40, s40, 122880\n"
      "v_mfma_f32_16x16x32_bf16 v[136:139], a[36:39], v[120:123], v[136:139]\n"
      "ds_read_b64_tr_b16 v[84:85], v34 offset:20480\n"
      "v_bfe_u32 v30, v7, 4, 3\n"
      "v_mfma_f32_16x16x32_bf16 v[152:155], a[28:31], v[104:107], v[152:155]\n"
      "ds_read_b64_tr_b16 v[86:87], v34 offset:22528\n"
      "v_lshrrev_b32 v31, 8, v7\n"
      "v_mfma_f32_16x16x32_bf16 v[152:155], a[40:43], v[120:123], v[152:155]\n"
      "ds_read_b64_tr_b16 v[88:89], v33 offset:20480\n"
      "v_lshl_add_u32 v31, v30, 2, v31\n"
      "v_mfma_f32_16x16x32_bf16 v[168:171], a[32:35], v[104:107], v[168:171]\n"
      "ds_read_b64_tr_b16 v[90:91], v33 offset:22528\n"
      "v_sub_u32 v30, 2, v30\n"
      "v_mfma_f32_16x16x32_bf16 v[168:171], a[44:47], v[120:123], v[168:171]\n"
      "ds_read_b64_tr_b16 v[92:93], v35 offset:20480\n"
      "s_add_u32 m0, s73, 49152\n"
      "v_add_u32 v43, 32768, v40\n"
      "v_mfma_f32_16x16x32_bf16 v[140:143], a[24:27], v[108:111], v[140:143]\n"
      "buffer_load_dwordx2 v[46:47], v51, s[32:35], s75 offen sc1\n"
      "global_load_lds_dwordx4 v43, s[4:5] offset:0 sc1\n"
      "v_and_b32 v29, 64, v4\n"
      "v_mfma_f32_16x16x32_bf16 v[140:143], a[36:39], v[124:127], v[140:143]\n"
      "ds_read_b64_tr_b16 v[94:95], v35 offset:22528\n"
      "v_lshl_add_u32 v31, v31, 9, v29\n"
      "v_mfma_f32_16x16x32_bf16 v[156:159], a[28:31], v[108:111], v[156:159]\n"
      "v_and_b32 v30, 0x80000000, v30\n"
      "v_or_b32 v31, v31, v30\n"
      "v_mfma_f32_16x16x32_bf16 v[156:159], a[40:43], v[124:127], v[156:159]\n"
      "v_add_u32 v50, s94, v7\n"
      "s_mul_i32 s72, s30, 3\n"
      "v_mfma_f32_16x16x32_bf16 v[172:175], a[32:35], v[108:111], v[172:175]\n"
      "global_load_lds_dwordx4 v43, s[4:5] offset:1024 sc1\n"
      "s_lshl_b32 s42, s60, 5\n"
      "v_mfma_f32_16x16x32_bf16 v[172:175], a[44:47], v[124:127], v[172:175]\n"
      "v_xor_b32 v27, s42, v6\n"
      "s_waitcnt lgkmcnt(0)\n"
      "v_mfma_f32_16x16x32_bf16 v[176:179], a[0:3], v[64:67], 0\n"
      "ds_read_b64_tr_b16 v[96:97], v6 offset:24576\n"
      "v_add_u32 v27, 110592, v27\n"
      "v_mfma_f32_16x16x32_bf16 v[176:179], a[12:15], v[80:83], v[176:179]\n"
      "ds_read_b64_tr_b16 v[98:99], v6 offset:26624\n"
      "v_mov_b32 v18, 0\n"
      "v_mfma_f32_16x16x32_bf16 v[184:187], a[4:7], v[64:67], 0\n"
      "global_load_lds_dwordx4 v43, s[4:5] offset:2048 sc1\n"
      "s_waitcnt vmcnt(4)\n"
      "v_mfma_f32_16x16x32_bf16 v[184:187], a[16:19], v[80:83], v[184:187]\n"
      "ds_read_b128 v[248:251], v40 offset:32768\n"
      "v_mov_b32 v19, 0\n"
      "v_mfma_f32_16x16x32_bf16 v[192:195], a[8:11], v[64:67], 0\n"
      "ds_read_b128 v[252:255], v40 offset:33792\n"
      "ds_read_b64_tr_b16 v[100:101], v34 offset:24576\n"
      "v_mov_b32 v28, 0x3f803f80\n"
      "v_mov_b32 v29, 0x3f803f80\n"
      "v_mov_b32 v30, 0x3f803f80\n"
      "v_mfma_f32_16x16x32_bf16 v[192:195], a[20:23], v[80:83], v[192:195]\n"
      "global_load_lds_dwordx4 v43, s[4:5] offset:3072 sc1\n"
      "ds_read_b128 v[36:39], v40 offset:34816\n"
      "ds_read_b64_tr_b16 v[102:103], v34 offset:26624\n"
      "s_mov_b32 s47, 65536\n"
      "s_lshl_b32 s41, s60, 13\n"
      "v_mfma_f32_16x16x32_bf16 v[180:183], a[0:3], v[68:71], 0\n"
      "buffer_load_dwordx2 v[54:55], v5, s[32:35], s47 offen sc1\n"
      "ds_read_b128 v[60:63], v40 offset:35840\n"
      "ds_read_b64_tr_b16 v[104:105], v33 offset:24576\n"
      "s_mov_b32 m0, s94\n"
      "v_mfma_f32_16x16x32_bf16 v[180:183], a[12:15], v[84:87], v[180:183]\n"
      "buffer_load_dwordx4 v7, s[32:35], s67 offen offset:0 sc1 lds\n"
      "ds_read_b64_tr_b16 v[106:107], v33 offset:26624\n"
      "s_add_u32 s41, s41, 65536\n"
      "v_mfma_f32_16x16x32_bf16 v[188:191], a[4:7], v[68:71], 0\n"
      "ds_read_b64_tr_b16 v[108:109], v35 offset:24576\n"
      "s_mov_b32 s48, 0xaaaaaaaa\n"
      "v_mfma_f32_16x16x32_bf16 v[188:191], a[16:19], v[84:87], v[188:191]\n"
      "ds_read_b64_tr_b16 v[110:111], v35 offset:26624\n"
      "s_mov_b32 s49, s48\n"
      "v_mfma_f32_16x16x32_bf16 v[196:199], a[8:11], v[68:71], 0\n"
      "ds_read_b64_tr_b16 v[112:113], v6 offset:28672\n"
      "s_waitcnt lgkmcnt(5)\n"
      "v_add3_u32 v48, v48, v248, v249\n"
      "v_add3_u32 v48, v48, v250, v251\n"
      "v_mfma_f32_16x16x32_bf16 v[196:199], a[20:23], v[84:87], v[196:199]\n"
      "buffer_load_dwordx4 v7, s[32:35], s67 offen offset:1024 sc1 lds\n"
      "v_add3_u32 v48, v48, v252, v253\n"
      "v_add3_u32 v48, v48, v254, v255\n"
      "v_mfma_f32_16x16x32_bf16 v[200:203], a[0:3], v[72:75], 0\n"
      "ds_read_b64_tr_b16 v[114:115], v6 offset:30720\n"
      "v_add3_u32 v48, v48, v36, v37\n"
      "v_add3_u32 v48, v48, v38, v39\n"
      "v_mfma_f32_16x16x32_bf16 v[200:203], a[12:15], v[88:91], v[200:203]\n"
      "ds_read_b64_tr_b16 v[116:117], v34 offset:28672\n"
      "v_add3_u32 v48, v48, v60, v61\n"
      "v_mfma_f32_16x16x32_bf16 v[208:211], a[4:7], v[72:75], 0\n"
      "ds_read_b64_tr_b16 v[118:119], v34 offset:30720\n"
      "v_add3_u32 v48, v48, v62, v63\n"
      "s_mov_b32 s50, 0xcccccccc\n"
      "v_mfma_f32_16x16x32_bf16 v[208:211], a[16:19], v[88:91], v[208:211]\n"
      "buffer_load_dwordx4 v7, s[32:35], s67 offen offset:2048 sc1 lds\n"
      "ds_read_b64_tr_b16 v[120:121], v33 offset:28672\n"
      "s_mov_b32 s51, s50\n"
      "v_mfma_f32_16x16x32_bf16 v[216:219], a[8:11], v[72:75], 0\n"
      "ds_read_b64_tr_b16 v[122:123], v33 offset:30720\n"
      "v_add_u32_dpp v48, v48, v48 quad_perm:[1,0,3,2] row_mask:0xf bank_mask:0xf\n"
      "v_mfma_f32_16x16x32_bf16 v[216:219], a[20:23], v[88:91], v[216:219]\n"
      "ds_read_b64_tr_b16 v[124:125], v35 offset:28672\n"
      "s_lshl_b32 s59, s60, 31\n"
      "v_mfma_f32_16x16x32_bf16 v[204:207], a[0:3], v[76:79], 0\n"
      "ds_read_b64_tr_b16 v[126:127], v35 offset:30720\n"
      "v_add_u32_dpp v48, v48, v48 quad_perm:[2,3,0,1] row_mask:0xf bank_mask:0xf\n"
      "v_mfma_f32_16x16x32_bf16 v[204:207], a[12:15], v[92:95], v[204:207]\n"
      "buffer_load_dword v32, v31, s[32:35], s68 offen sc1\n"
      "s_lshl_b32 s54, s30, 1\n"
      "v_mfma_f32_16x16x32_bf16 v[212:215], a[4:7], v[76:79], 0\n"
      "v_add_u32_dpp v48, v48, v48 row_half_mirror row_mask:0xf bank_mask:0xf\n"
      "v_cmp_ne_u32 vcc, s71, v48\n"
      "v_mfma_f32_16x16x32_bf16 v[212:215], a[16:19], v[92:95], v[212:215]\n"
      "s_lshl_b32 s55, s60, 6\n"
      "s_add_u32 s55, s55, 163328\n"
      "v_mfma_f32_16x16x32_bf16 v[220:223], a[8:11], v[76:79], 0\n"
      "s_cbranch_vccnz L_sx2_%=\n"
      "L_bx2_%=:\n"
      "v_mfma_f32_16x16x32_bf16 v[220:223], a[20:23], v[92:95], v[220:223]\n"
      "s_waitcnt lgkmcnt(0)\n"
      "s_barrier\n"
      "v_mfma_f32_16x16x32_bf16 v[176:179], a[24:27], v[96:99], v[176:179]\n"
      "ds_read_b64_tr_b16 v[64:65], v6 offset:32768\n"
      "v_mfma_f32_16x16x32_bf16 v[176:179], a[36:39], v[112:115], v[176:179]\n"
      "ds_read_b64_tr_b16 v[66:67], v6 offset:34816\n"
      "v_mfma_f32_16x16x32_bf16 v[184:187], a[28:31], v[96:99], v[184:187]\n"
      "ds_read_b64_tr_b16 v[68:69], v33 offset:32768\n"
      "s_waitcnt vmcnt(9)\n"
      "v_cmp_ne_u64 s[52:53], s[30:31], v[46:47]\n"
      "v_mfma_f32_16x16x32_bf16 v[184:187], a[40:43], v[112:115], v[184:187]\n"
      "ds_read_b64_tr_b16 v[70:71], v33 offset:34816\n"
      "s_add_u32 m0, s73, 0\n"
      "v_add_u32 v43, 49152, v40\n"
      "v_add_u32 v44, 4096, v7\n"
      "v_cmp_ne_u64 vcc, s[30:31], v[58:59]\n"
      "v_mfma_f32_16x16x32_bf16 v[192:195], a[32:35], v[96:99], v[192:195]\n"
      "ds_read_b64_tr_b16 v[72:73], v34 offset:32768\n"
      "s_waitcnt vmcnt(4)\n"
      "v_mfma_f32_16x16x32_bf16 v[192:195], a[44:47], v[112:115], v[192:195]\n"
      "global_load_lds_dwordx4 v43, s[4:5] offset:0 sc1\n"
      "ds_read_b128 v[248:251], v40 offset:49152\n"
      "s_or_b64 vcc, vcc, s[52:53]\n"
      "v_mfma_f32_16x16x32_bf16 v[180:183], a[24:27], v[100:103], v[180:183]\n"
      "ds_read_b128 v[252:255], v40 offset:50176\n"
      "s_cbranch_vccnz L_sfd_%=\n"
      "L_bfd_%=:\n"
      "v_mfma_f32_16x16x32_bf16 v[180:183], a[36:39], v[116:119], v[180:183]\n"
      "ds_read_b128 v[36:39], v40 offset:51200\n"
      "ds_read_b64_tr_b16 v[74:75], v34 offset:34816\n"
      "v_add_u32 v51, s96, v41\n"
      "v_mfma_f32_16x16x32_bf16 v[188:191], a[28:31], v[100:103], v[188:191]\n"
      "ds_read_b128 v[60:63], v40 offset:52224\n"
      "ds_read_b64_tr_b16 v[76:77], v35 offset:32768\n"
      "v_lshrrev_b32 v42, 2, v7\n"
      "v_mfma_f32_16x16x32_bf16 v[188:191], a[40:43], v[116:119], v[188:191]\n"
      "global_load_lds_dwordx4 v43, s[4:5] offset:1024 sc1\n"
      "ds_read_b64_tr_b16 v[78:79], v35 offset:34816\n"
      "s_lshl_b32 s97, s60, 10\n"
      "v_mfma_f32_16x16x32_bf16 v[196:199], a[32:35], v[100:103], v[196:199]\n"
      "ds_read_b64_tr_b16 v[80:81], v6 offset:36864\n"
      "s_add_u32 s96, s97, 98304\n"
      "v_mfma_f32_16x16x32_bf16 v[196:199], a[44:47], v[116:119], v[196:199]\n"
      "ds_read_b64_tr_b16 v[82:83], v6 offset:38912\n"
      "v_add_u32 v47, s96, v7\n"
      "v_mfma_f32_16x16x32_bf16 v[200:203], a[24:27], v[104:107], v[200:203]\n"
      "ds_read_b64_tr_b16 v[84:85], v33 offset:36864\n"
      "s_waitcnt lgkmcnt(5)\n"
      "v_add3_u32 v54, v54, v248, v249\n"
      "v_add3_u32 v54, v54, v250, v251\n"
      "v_mfma_f32_16x16x32_bf16 v[200:203], a[36:39], v[120:123], v[200:203]\n"
      "global_load_lds_dwordx4 v43, s[4:5] offset:2048 sc1\n"
      "v_add3_u32 v54, v54, v252, v253\n"
      "v_add3_u32 v54, v54, v254, v255\n"
      "v_mfma_f32_16x16x32_bf16 v[208:211], a[28:31], v[104:107], v[208:211]\n"
      "ds_read_b64_tr_b16 v[86:87], v33 offset:38912\n"
      "v_add3_u32 v54, v54, v36, v37\n"
      "v_add3_u32 v54, v54, v38, v39\n"
      "v_mfma_f32_16x16x32_bf16 v[208:211], a[40:43], v[120:123], v[208:211]\n"
      "ds_read_b64_tr_b16 v[88:89], v34 offset:36864\n"
      "v_add3_u32 v54, v54, v60, v61\n"
      "v_mfma_f32_16x16x32_bf16 v[216:219], a[32:35], v[104:107], v[216:219]\n"
      "ds_read_b64_tr_b16 v[90:91], v34 offset:38912\n"
      "v_add3_u32 v54, v54, v62, v63\n"
      "v_mfma_f32_16x16x32_bf16 v[216:219], a[44:47], v[120:123], v[216:219]\n"
      "global_load_lds_dwordx4 v43, s[4:5] offset:3072 sc1\n"
      "ds_read_b64_tr_b16 v[92:93], v35 offset:36864\n"
      "s_mov_b32 s47, 98304\n"
      "v_mfma_f32_16x16x32_bf16 v[204:207], a[24:27], v[108:111], v[204:207]\n"
      "ds_read_b64_tr_b16 v[94:95], v35 offset:38912\n"
      "v_add_u32_dpp v54, v54, v54 quad_perm:[1,0,3,2] row_mask:0xf bank_mask:0xf\n"
      "v_mfma_f32_16x16x32_bf16 v[204:207], a[36:39], v[124:127], v[204:207]\n"
      "v_bfe_u32 v46, v7, 8, 1\n"
      "v_xor_b32 v46, s60, v46\n"
      "v_mfma_f32_16x16x32_bf16 v[212:215], a[28:31], v[108:111], v[212:215]\n"
      "v_add_u32_dpp v54, v54, v54 quad_perm:[2,3,0,1] row_mask:0xf bank_mask:0xf\n"
      "v_lshl_add_u32 v46, v46, 2, v7\n"
      "v_mfma_f32_16x16x32_bf16 v[212:215], a[40:43], v[124:127], v[212:215]\n"
      "buffer_load_dwordx2 v[56:57], v5, s[32:35], s47 offen sc1\n"
      "v_add_u32 v46, 98304, v46\n"
      "v_mfma_f32_16x16x32_bf16 v[220:223], a[32:35], v[108:111], v[220:223]\n"
      "v_add_u32_dpp v54, v54, v54 row_half_mirror row_mask:0xf bank_mask:0xf\n"
      "v_cmp_ne_u32 vcc, s71, v54\n"
      "v_mfma_f32_16x16x32_bf16 v[220:223], a[44:47], v[124:127], v[220:223]\n"
      "s_waitcnt lgkmcnt(0)\n"
      "v_mfma_f32_16x16x32_bf16 v[128:131], a[48:51], v[64:67], v[128:131]\n"
      "ds_read_b64_tr_b16 v[96:97], v6 offset:40960\n"
      "s_cbranch_vccnz L_sx3_%=\n"
      "L_bx3_%=:\n"
      "v_and_b32 v41, 0x78, v4\n"
      "v_mfma_f32_16x16x32_bf16 v[128:131], a[60:63], v[80:83], v[128:131]\n"
      "ds_read_b64_tr_b16 v[98:99], v6 offset:43008\n"
      "v_add_u32 v41, 162304, v41\n"
      "v_mfma_f32_16x16x32_bf16 v[144:147], a[52:55], v[64:67], v[144:147]\n"
      "ds_read_b64_tr_b16 v[100:101], v33 offset:40960\n"
      "s_waitcnt vmcnt(5)\n"
      "v_mfma_f32_16x16x32_bf16 v[144:147], a[64:67], v[80:83], v[144:147]\n"
      "ds_read_b128 v[248:251], v50 offset:0\n"
      "s_add_u32 m0, s73, 16384\n"
      "v_add_u32 v43, 65536, v40\n"
      "v_mfma_f32_16x16x32_bf16 v[160:163], a[56:59], v[64:67], v[160:163]\n"
      "ds_read_b128 v[252:255], v50 offset:1024\n"
      "v_mfma_f32_16x16x32_bf16 v[160:163], a[68:71], v[80:83], v[160:163]\n"
      "global_load_dwordx4 v[224:227], v53, s[26:27] sc1\n"
      "ds_read_b64_tr_b16 v[102:103], v33 offset:43008\n"
      "v_mfma_f32_16x16x32_bf16 v[132:135], a[48:51], v[68:71], v[132:135]\n"
      "ds_read_b128 v[36:39], v50 offset:2048\n"
      "ds_read_b64_tr_b16 v[104:105], v34 offset:40960\n"
      "v_add_u32 v53, 0x6000, v53\n"
      "v_mfma_f32_16x16x32_bf16 v[132:135], a[60:63], v[84:87], v[132:135]\n"
      "ds_read_b64_tr_b16 v[106:107], v34 offset:43008\n"
      "v_mfma_f32_16x16x32_bf16 v[148:151], a[52:55], v[68:71], v[148:151]\n"
      "global_load_dwordx4 v[228:231], v53, s[26:27] sc1\n"
      "ds_read_b64_tr_b16 v[108:109], v35 offset:40960\n"
      "v_mfma_f32_16x16x32_bf16 v[148:151], a[64:67], v[84:87], v[148:151]\n"
      "ds_read_b64_tr_b16 v[110:111], v35 offset:43008\n"
      "v_add_u32 v53, 0x6000, v53\n"
      "v_mfma_f32_16x16x32_bf16 v[164:167], a[56:59], v[68:71], v[164:167]\n"
      "ds_read_b64_tr_b16 v[112:113], v6 offset:45056\n"
      "s_waitcnt lgkmcnt(5)\n"
      "v_add3_u32 v32, v32, v248, v249\n"
      "v_add3_u32 v32, v32, v250, v251\n"
      "v_mfma_f32_16x16x32_bf16 v[164:167], a[68:71], v[84:87], v[164:167]\n"
      "global_load_dwordx4 v[232:235], v53, s[26:27] sc1\n"
      "v_add3_u32 v32, v32, v252, v253\n"
      "v_mfma_f32_16x16x32_bf16 v[136:139], a[48:51], v[72:75], v[136:139]\n"
      "ds_read_b64_tr_b16 v[114:115], v6 offset:47104\n"
      "v_add_u32 v53, 0x6000, v53\n"
      "v_add3_u32 v32, v32, v254, v255\n"
      "v_mfma_f32_16x16x32_bf16 v[136:139], a[60:63], v[88:91], v[136:139]\n"
      "ds_read_b64_tr_b16 v[116:117], v33 offset:45056\n"
      "v_add3_u32 v32, v32, v36, v37\n"
      "v_mfma_f32_16x16x32_bf16 v[152:155], a[52:55], v[72:75], v[152:155]\n"
      "global_load_dwordx4 v[236:239], v53, s[26:27] sc1\n"
      "ds_read_b64_tr_b16 v[118:119], v33 offset:47104\n"
      "v_add3_u32 v32, v32, v38, v39\n"
      "v_mfma_f32_16x16x32_bf16 v[152:155], a[64:67], v[88:91], v[152:155]\n"
      "ds_read_b64_tr_b16 v[120:121], v34 offset:45056\n"
      "v_add_u32 v53, 0x6000, v53\n"
      "v_mfma_f32_16x16x32_bf16 v[168:171], a[56:59], v[72:75], v[168:171]\n"
      "ds_read_b64_tr_b16 v[122:123], v34 offset:47104\n"
      "v_add_u32_dpp v32, v32, v32 quad_perm:[1,0,3,2] row_mask:0xf bank_mask:0xf\n"
      "v_mfma_f32_16x16x32_bf16 v[168:171], a[68:71], v[88:91], v[168:171]\n"
      "global_load_dwordx4 v[240:243], v53, s[26:27] sc1\n"
      "ds_read_b64_tr_b16 v[124:125], v35 offset:45056\n"
      "v_mfma_f32_16x16x32_bf16 v[140:143], a[48:51], v[76:79], v[140:143]\n"
      "ds_read_b64_tr_b16 v[126:127], v35 offset:47104\n"
      "v_add_u32 v53, 0x6000, v53\n"
      "v_add_u32_dpp v32, v32, v32 quad_perm:[2,3,0,1] row_mask:0xf bank_mask:0xf\n"
      "v_mfma_f32_16x16x32_bf16 v[140:143], a[60:63], v[92:95], v[140:143]\n"
      "v_mfma_f32_16x16x32_bf16 v[156:159], a[52:55], v[76:79], v[156:159]\n"
      "global_load_dwordx4 v[244:247], v53, s[26:27] sc1\n"
      "v_add_u32_dpp v32, v32, v32 row_half_mirror row_mask:0xf bank_mask:0xf\n"
      "v_cmp_ne_u32 vcc, s72, v32\n"
      "v_mfma_f32_16x16x32_bf16 v[156:159], a[64:67], v[92:95], v[156:159]\n"
      "v_mfma_f32_16x16x32_bf16 v[172:175], a[56:59], v[76:79], v[172:175]\n"
      "s_cbranch_vccnz L_sxp_%=\n"
      "L_bxp_%=:\n"
      "v_mfma_f32_16x16x32_bf16 v[172:175], a[68:71], v[92:95], v[172:175]\n"
      "global_load_lds_dwordx4 v43, s[4:5] offset:0 sc1\n"
      "s_waitcnt lgkmcnt(0)\n"
      "s_barrier\n"
      "v_mfma_f32_16x16x32_bf16 v[128:131], a[72:75], v[96:99], v[128:131]\n"
      "ds_read_b64_tr_b16 v[8:9], v27 offset:0\n"
      "v_mfma_f32_16x16x32_bf16 v[128:131], a[84:87], v[112:115], v[128:131]\n"
      "ds_read_b64_tr_b16 v[10:11], v27 offset:2048\n"
      "v_mfma_f32_16x16x32_bf16 v[144:147], a[76:79], v[96:99], v[144:147]\n"
      "ds_read_b64_tr_b16 v[12:13], v27 offset:4096\n"
      "ds_read_b64_tr_b16 v[64:65], v6 offset:49152\n"
      "v_mfma_f32_16x16x32_bf16 v[144:147], a[88:91], v[112:115], v[144:147]\n"
      "global_load_lds_dwordx4 v43, s[4:5] offset:1024 sc1\n"
      "ds_read_b64_tr_b16 v[14:15], v27 offset:6144\n"
      "ds_read_b64_tr_b16 v[66:67], v6 offset:51200\n"
      "v_mfma_f32_16x16x32_bf16 v[160:163], a[80:83], v[96:99], v[160:163]\n"
      "ds_read_b64_tr_b16 v[16:17], v27 offset:8192\n"
      "v_mov_b32 v31, 0x3f803f80\n"
      "v_mfma_f32_16x16x32_bf16 v[160:163], a[92:95], v[112:115], v[160:163]\n"
      "ds_read_b64_tr_b16 v[68:69], v33 offset:49152\n"
      "v_and_b32 v50, 0xf0, v7\n"
      "v_mfma_f32_16x16x32_bf16 v[132:135], a[72:75], v[100:103], v[132:135]\n"
      "ds_read_b64_tr_b16 v[70:71], v33 offset:51200\n"
      "s_mov_b32 s42, 0x00f0000f\n"
      "v_mfma_f32_16x16x32_bf16 v[132:135], a[84:87], v[116:119], v[132:135]\n"
      "global_load_lds_dwordx4 v43, s[4:5] offset:2048 sc1\n"
      "s_mov_b32 s43, 0xf0000f00\n"
      "v_mfma_f32_16x16x32_bf16 v[148:151], a[76:79], v[100:103], v[148:151]\n"
      "ds_read_b64_tr_b16 v[72:73], v34 offset:49152\n"
      "v_bfrev_b32 v32, 1\n"
      "v_mfma_f32_16x16x32_bf16 v[148:151], a[88:91], v[116:119], v[148:151]\n"
      "ds_read_b64_tr_b16 v[74:75], v34 offset:51200\n"
      "v_cndmask_b32 v50, v32, v50, s[42:43]\n"
      "v_mfma_f32_16x16x32_bf16 v[164:167], a[80:83], v[100:103], v[164:167]\n"
      "ds_read_b64_tr_b16 v[76:77], v35 offset:49152\n"
      "v_mfma_f32_16x16x32_bf16 v[164:167], a[92:95], v[116:119], v[164:167]\n"
      "global_load_lds_dwordx4 v43, s[4:5] offset:3072 sc1\n"
      "s_waitcnt lgkmcnt(5)\n"
      "v_mfma_f32_16x16x32_bf16 v[20:23], v[28:31], v[8:11], 0\n"
      "ds_read_b64_tr_b16 v[78:79], v35 offset:51200\n"
      "s_mov_b32 s47, 131072\n"
      "v_mfma_f32_16x16x32_bf16 v[24:27], v[8:11], v[8:11], 0\n"
      "ds_read_b64_tr_b16 v[80:81], v6 offset:53248\n"
      "v_mfma_f32_16x16x32_bf16 v[20:23], v[28:31], v[12:15], v[20:23]\n"
      "ds_read_b64_tr_b16 v[82:83], v6 offset:55296\n"
      "v_mfma_f32_16x16x32_bf16 v[24:27], v[12:15], v[12:15], v[24:27]\n"
      "buffer_load_dwordx2 v[2:3], v5, s[32:35], s47 offen sc1\n"
      "v_mfma_f32_16x16x32_bf16 v[20:23], v[28:31], v[16:19], v[20:23]\n"
      "ds_read_b64_tr_b16 v[84:85], v33 offset:53248\n"
      "v_mfma_f32_16x16x32_bf16 v[24:27], v[16:19], v[16:19], v[24:27]\n"
      "ds_read_b64_tr_b16 v[86:87], v33 offset:55296\n"
      "v_mfma_f32_16x16x32_bf16 v[136:139], a[72:75], v[104:107], v[136:139]\n"
      "ds_read_b64_tr_b16 v[88:89], v34 offset:53248\n"
      "v_mfma_f32_16x16x32_bf16 v[136:139], a[84:87], v[120:123], v[136:139]\n"
      "ds_read_b64_tr_b16 v[90:91], v34 offset:55296\n"
      "v_mfma_f32_16x16x32_bf16 v[152:155], a[76:79], v[104:107], v[152:155]\n"
      "ds_read_b64_tr_b16 v[92:93], v35 offset:53248\n"
      "v_cndmask_b32 v24, v24, v25, s[48:49]\n"
      "v_cndmask_b32 v26, v26, v27, s[48:49]\n"
      "v_mfma_f32_16x16x32_bf16 v[152:155], a[88:91], v[120:123], v[152:155]\n"
      "ds_read_b64_tr_b16 v[94:95], v35 offset:55296\n"
      "s_add_u32 m0, s73, 32768\n"
      "v_add_u32 v43, 98304, v40\n"
      "v_mov_b32 v22, s30\n"
      "v_cndmask_b32 v21, v24, v26, s[50:51]\n"
      "v_mfma_f32_16x16x32_bf16 v[168:171], a[80:83], v[104:107], v[168:171]\n"
      "v_mov_b32 v23, s31\n"
      "v_mfma_f32_16x16x32_bf16 v[168:171], a[92:95], v[120:123], v[168:171]\n"
      "buffer_store_dwordx4 v[20:23], v50, s[32:35], s92 offen sc1\n"
      "v_and_b32 v32, 0x200, v7\n"
      "v_mfma_f32_16x16x32_bf16 v[140:143], a[72:75], v[108:111], v[140:143]\n"
      "v_and_b32 v50, 0x1f0, v7\n"
      "v_lshl_add_u32 v50, v32, 3, v50\n"
      "v_mfma_f32_16x16x32_bf16 v[140:143], a[84:87], v[124:127], v[140:143]\n"
      "global_load_dwordx4 a[184:187], v45, s[8:9] offset:2048 sc1\n"
      "v_and_b32 v32, 0xf8, v4\n"
      "v_mfma_f32_16x16x32_bf16 v[156:159], a[76:79], v[108:111], v[156:159]\n"
      "v_add_u32 v32, s95, v32\n"
      "v_mfma_f32_16x16x32_bf16 v[156:159], a[88:91], v[124:127], v[156:159]\n"
      "v_mfma_f32_16x16x32_bf16 v[172:175], a[80:83], v[108:111], v[172:175]\n"
      "v_mfma_f32_16x16x32_bf16 v[172:175], a[92:95], v[124:127], v[172:175]\n"
      "global_load_dwordx4 a[188:191], v45, s[8:9] offset:3072 sc1\n"
      "s_waitcnt lgkmcnt(0)\n"
      "v_mfma_f32_16x16x32_bf16 v[128:131], a[96:99], v[64:67], v[128:131]\n"
      "ds_read_b64_tr_b16 v[96:97], v6 offset:57344\n"
      "v_mfma_f32_16x16x32_bf16 v[128:131], a[108:111], v[80:83], v[128:131]\n"
      "ds_read_b64_tr_b16 v[98:99], v6 offset:59392\n"
      "v_add_u32 v45, 53248, v7\n"
      "v_mfma_f32_16x16x32_bf16 v[144:147], a[100:103], v[64:67], v[144:147]\n"
      "ds_read_b64_tr_b16 v[100:101], v33 offset:57344\n"
      "s_waitcnt vmcnt(14)\n"
      "v_mfma_f32_16x16x32_bf16 v[144:147], a[112:115], v[80:83], v[144:147]\n"
      "global_load_dwordx4 a[192:195], v45, s[8:9] offset:-4096 sc1\n"
      "ds_read_b128 v[248:251], v40 offset:0\n"
      "v_mfma_f32_16x16x32_bf16 v[160:163], a[104:107], v[64:67], v[160:163]\n"
      "ds_read_b128 v[252:255], v40 offset:1024\n"
      "v_mfma_f32_16x16x32_bf16 v[160:163], a[116:119], v[80:83], v[160:163]\n"
      "ds_read_b128 v[36:39], v40 offset:2048\n"
      "ds_read_b64_tr_b16 v[102:103], v33 offset:59392\n"
      "v_mfma_f32_16x16x32_bf16 v[132:135], a[96:99], v[68:71], v[132:135]\n"
      "ds_read_b128 v[60:63], v40 offset:3072\n"
      "ds_read_b64_tr_b16 v[104:105], v34 offset:57344\n"
      "v_mfma_f32_16x16x32_bf16 v[132:135], a[108:111], v[84:87], v[132:135]\n"
      "global_load_dwordx4 a[196:199], v45, s[8:9] offset:-3072 sc1\n"
      "ds_read_b64_tr_b16 v[106:107], v34 offset:59392\n"
      "v_mfma_f32_16x16x32_bf16 v[148:151], a[100:103], v[68:71], v[148:151]\n"
      "ds_read_b64_tr_b16 v[108:109], v35 offset:57344\n"
      "v_mfma_f32_16x16x32_bf16 v[148:151], a[112:115], v[84:87], v[148:151]\n"
      "ds_read_b64_tr_b16 v[110:111], v35 offset:59392\n"
      "v_mfma_f32_16x16x32_bf16 v[164:167], a[104:107], v[68:71], v[164:167]\n"
      "ds_read_b64_tr_b16 v[112:113], v6 offset:61440\n"
      "s_waitcnt lgkmcnt(5)\n"
      "v_add3_u32 v56, v56, v248, v249\n"
      "v_add3_u32 v56, v56, v250, v251\n"
      "v_mfma_f32_16x16x32_bf16 v[164:167], a[116:119], v[84:87], v[164:167]\n"
      "global_load_dwordx4 a[200:203], v45, s[8:9] offset:-2048 sc1\n"
      "v_add3_u32 v56, v56, v252, v253\n"
      "v_add3_u32 v56, v56, v254, v255\n"
      "v_mfma_f32_16x16x32_bf16 v[136:139], a[96:99], v[72:75], v[136:139]\n"
      "ds_read_b64_tr_b16 v[114:115], v6 offset:63488\n"
      "v_add3_u32 v56, v56, v36, v37\n"
      "v_add3_u32 v56, v56, v38, v39\n"
      "v_mfma_f32_16x16x32_bf16 v[136:139], a[108:111], v[88:91], v[136:139]\n"
      "ds_read_b64_tr_b16 v[116:117], v33 offset:61440\n"
      "v_add3_u32 v56, v56, v60, v61\n"
      "v_mfma_f32_16x16x32_bf16 v[152:155], a[100:103], v[72:75], v[152:155]\n"
      "ds_read_b64_tr_b16 v[118:119], v33 offset:63488\n"
      "v_add3_u32 v56, v56, v62, v63\n"
      "v_mfma_f32_16x16x32_bf16 v[152:155], a[112:115], v[88:91], v[152:155]\n"
      "global_load_dwordx4 a[204:207], v45, s[8:9] offset:-1024 sc1\n"
      "ds_read_b64_tr_b16 v[120:121], v34 offset:61440\n"
      "v_mfma_f32_16x16x32_bf16 v[168:171], a[104:107], v[72:75], v[168:171]\n"
      "ds_read_b64_tr_b16 v[122:123], v34 offset:63488\n"
      "v_add_u32_dpp v56, v56, v56 quad_perm:[1,0,3,2] row_mask:0xf bank_mask:0xf\n"
      "v_mfma_f32_16x16x32_bf16 v[168:171], a[116:119], v[88:91], v[168:171]\n"
      "ds_read_b64_tr_b16 v[124:125], v35 offset:61440\n"
      "s_waitcnt vmcnt(12)\n"
      "v_mfma_f32_16x16x32_bf16 v[140:143], a[96:99], v[76:79], v[140:143]\n"
      "ds_read_b64_tr_b16 v[126:127], v35 offset:63488\n"
      "v_add_u32_dpp v56, v56, v56 quad_perm:[2,3,0,1] row_mask:0xf bank_mask:0xf\n"
      "v_mfma_f32_16x16x32_bf16 v[140:143], a[108:111], v[92:95], v[140:143]\n"
      "global_load_dwordx4 a[208:211], v45, s[8:9] offset:0 sc1\n"
      "v_add_f32 v224, v224, v228\n"
      "v_mfma_f32_16x16x32_bf16 v[156:159], a[100:103], v[76:79], v[156:159]\n"
      "v_add_u32_dpp v56, v56, v56 row_half_mirror row_mask:0xf bank_mask:0xf\n"
      "v_cmp_ne_u32 vcc, s71, v56\n"
      "v_add_f32 v225, v225, v229\n"
      "v_mfma_f32_16x16x32_bf16 v[156:159], a[112:115], v[92:95], v[156:159]\n"
      "v_add_f32 v226, v226, v230\n"
      "v_add_f32 v227, v227, v231\n"
      "v_mfma_f32_16x16x32_bf16 v[172:175], a[104:107], v[76:79], v[172:175]\n"
      "s_cbranch_vccnz L_sx4_%=\n"
      "L_bx4_%=:\n"
      "v_add_f32 v232, v232, v236\n"
      "v_mfma_f32_16x16x32_bf16 v[172:175], a[116:119], v[92:95], v[172:175]\n"
      "global_load_dwordx4 a[212:215], v45, s[8:9] offset:1024 sc1\n"
      "s_waitcnt lgkmcnt(0)\n"
      "s_barrier\n"
      "v_mfma_f32_16x16x32_bf16 v[128:131], a[120:123], v[96:99], v[128:131]\n"
      "ds_read_b64_tr_b16 v[64:65], v6 offset:0\n"
      "v_add_f32 v233, v233, v237\n"
      "v_mfma_f32_16x16x32_bf16 v[128:131], a[132:135], v[112:115], v[128:131]\n"
      "ds_read_b64_tr_b16 v[66:67], v6 offset:2048\n"
      "v_add_f32 v234, v234, v238\n"
      "v_mfma_f32_16x16x32_bf16 v[144:147], a[124:127], v[96:99], v[144:147]\n"
      "ds_read_b64_tr_b16 v[68:69], v33 offset:0\n"
      "v_add_f32 v235, v235, v239\n"
      "v_mfma_f32_16x16x32_bf16 v[144:147], a[136:139], v[112:115], v[144:147]\n"
      "global_load_lds_dwordx4 v43, s[4:5] offset:0 sc1\n"
      "v_add_f32 v240, v240, v244\n"
      "v_mfma_f32_16x16x32_bf16 v[160:163], a[128:131], v[96:99], v[160:163]\n"
      "ds_read_b64_tr_b16 v[70:71], v33 offset:2048\n"
      "v_add_f32 v241, v241, v245\n"
      "v_mfma_f32_16x16x32_bf16 v[160:163], a[140:143], v[112:115], v[160:163]\n"
      "ds_read_b64_tr_b16 v[72:73], v34 offset:0\n"
      "v_add_f32 v242, v242, v246\n"
      "v_mfma_f32_16x16x32_bf16 v[132:135], a[120:123], v[100:103], v[132:135]\n"
      "ds_read_b64_tr_b16 v[74:75], v34 offset:2048\n"
      "v_add_f32 v243, v243, v247\n"
      "v_mfma_f32_16x16x32_bf16 v[132:135], a[132:135], v[116:119], v[132:135]\n"
      "global_load_dwordx4 a[216:219], v45, s[8:9] offset:2048 sc1\n"
      "v_add_f32 v224, v224, v232\n"
      "v_mfma_f32_16x16x32_bf16 v[148:151], a[124:127], v[100:103], v[148:151]\n"
      "ds_read_b64_tr_b16 v[76:77], v35 offset:0\n"
      "v_add_f32 v225, v225, v233\n"
      "v_mfma_f32_16x16x32_bf16 v[148:151], a[136:139], v[116:119], v[148:151]\n"
      "ds_read_b64_tr_b16 v[78:79], v35 offset:2048\n"
      "v_add_f32 v226, v226, v234\n"
      "v_mfma_f32_16x16x32_bf16 v[164:167], a[128:131], v[100:103], v[164:167]\n"
      "ds_read_b64_tr_b16 v[80:81], v6 offset:4096\n"
      "v_add_f32 v227, v227, v235\n"
      "v_mfma_f32_16x16x32_bf16 v[164:167], a[140:143], v[116:119], v[164:167]\n"
      "global_load_lds_dwordx4 v43, s[4:5] offset:1024 sc1\n"
      "v_add_f32 v224, v224, v240\n"
      "v_mfma_f32_16x16x32_bf16 v[136:139], a[120:123], v[104:107], v[136:139]\n"
      "ds_read_b64_tr_b16 v[82:83], v6 offset:6144\n"
      "v_add_f32 v225, v225, v241\n"
      "v_mfma_f32_16x16x32_bf16 v[136:139], a[132:135], v[120:123], v[136:139]\n"
      "ds_read_b64_tr_b16 v[84:85], v33 offset:4096\n"
      "v_add_f32 v226, v226, v242\n"
      "v_mfma_f32_16x16x32_bf16 v[152:155], a[124:127], v[104:107], v[152:155]\n"
      "ds_read_b64_tr_b16 v[86:87], v33 offset:6144\n"
      "v_add_f32 v227, v227, v243\n"
      "v_add_u32 v53, 0x6000, v53\n"
      "v_mfma_f32_16x16x32_bf16 v[152:155], a[136:139], v[120:123], v[152:155]\n"
      "global_load_dwordx4 v[228:231], v53, s[26:27] sc1\n"
      "ds_read_b64_tr_b16 v[88:89], v34 offset:4096\n"
      "v_mfma_f32_16x16x32_bf16 v[168:171], a[128:131], v[104:107], v[168:171]\n"
      "ds_read_b64_tr_b16 v[90:91], v34 offset:6144\n"
      "v_add_u32 v53, 0x6000, v53\n"
      "v_mfma_f32_16x16x32_bf16 v[168:171], a[140:143], v[120:123], v[168:171]\n"
      "ds_read_b64_tr_b16 v[92:93], v35 offset:4096\n"
      "v_mfma_f32_16x16x32_bf16 v[140:143], a[120:123], v[108:111], v[140:143]\n"
      "global_load_dwordx4 v[232:235], v53, s[26:27] sc1\n"
      "v_add_u32 v53, 0x6000, v53\n"
      "v_mfma_f32_16x16x32_bf16 v[140:143], a[132:135], v[124:127], v[140:143]\n"
      "global_load_dwordx4 v[236:239], v53, s[26:27] sc1\n"
      "ds_read_b64_tr_b16 v[94:95], v35 offset:6144\n"
      "v_add_u32 v53, 0x6000, v53\n"
      "v_mfma_f32_16x16x32_bf16 v[156:159], a[124:127], v[108:111], v[156:159]\n"
      "global_load_dwordx4 a[220:223], v45, s[8:9] offset:3072 sc1\n"
      "global_load_dwordx4 v[240:243], v53, s[26:27] sc1\n"
      "v_and_b32 v53, 0x300, v7\n"
      "v_mfma_f32_16x16x32_bf16 v[156:159], a[136:139], v[124:127], v[156:159]\n"
      "v_and_b32 v59, 0x30, v7\n"
      "v_lshl_add_u32 v53, v59, 2, v53\n"
      "v_mfma_f32_16x16x32_bf16 v[172:175], a[128:131], v[108:111], v[172:175]\n"
      "L_pg4_%=:\n"
      "s_cbranch_execz L_pgr_%=\n"
      "v_bfe_u32 v59, v7, 6, 2\n"
      "v_lshl_add_u32 v53, v59, 4, v53\n"
      "v_lshrrev_b32 v59, 6, v7\n"
      "v_mfma_f32_16x16x32_bf16 v[172:175], a[140:143], v[124:127], v[172:175]\n"
      "global_load_lds_dwordx4 v43, s[4:5] offset:2048 sc1\n"
      "v_add_u32 v45, 61440, v7\n"
      "s_waitcnt lgkmcnt(0)\n"
      "v_mfma_f32_16x16x32_bf16 v[128:131], a[144:147], v[64:67], v[128:131]\n"
      "global_load_dwordx4 a[224:227], v45, s[8:9] offset:-4096 sc1\n"
      "buffer_load_dwordx4 v[244:247], v53, s[32:35], s66 offen sc1\n"
      "v_mfma_f32_16x16x32_bf16 v[128:131], a[156:159], v[80:83], v[128:131]\n"
      "ds_read_b64_tr_b16 v[96:97], v6 offset:8192\n"
      "v_mfma_f32_16x16x32_bf16 v[144:147], a[148:151], v[64:67], v[144:147]\n"
      "ds_read_b64_tr_b16 v[98:99], v6 offset:10240\n"
      "s_waitcnt vmcnt(20)\n"
      "v_mfma_f32_16x16x32_bf16 v[144:147], a[160:163], v[80:83], v[144:147]\n"
      "global_load_lds_dwordx4 v43, s[4:5] offset:3072 sc1\n"
      "ds_read_b128 v[248:251], v40 offset:16384\n"
      "v_mfma_f32_16x16x32_bf16 v[160:163], a[152:155], v[64:67], v[160:163]\n"
      "global_load_dwordx4 a[228:231], v45, s[8:9] offset:-3072 sc1\n"
      "ds_read_b128 v[252:255], v40 offset:17408\n"
      "ds_read_b64_tr_b16 v[100:101], v33 offset:8192\n"
      "v_mfma_f32_16x16x32_bf16 v[160:163], a[164:167], v[80:83], v[160:163]\n"
      "ds_read_b128 v[36:39], v40 offset:18432\n"
      "ds_read_b64_tr_b16 v[102:103], v33 offset:10240\n"
      "v_mfma_f32_16x16x32_bf16 v[132:135], a[144:147], v[68:71], v[132:135]\n"
      "ds_read_b128 v[60:63], v40 offset:19456\n"
      "ds_read_b64_tr_b16 v[104:105], v34 offset:8192\n"
      "v_mfma_f32_16x16x32_bf16 v[132:135], a[156:159], v[84:87], v[132:135]\n"
      "ds_read_b64_tr_b16 v[106:107], v34 offset:10240\n"
      "v_mfma_f32_16x16x32_bf16 v[148:151], a[148:151], v[68:71], v[148:151]\n"
      "global_load_dwordx4 a[232:235], v45, s[8:9] offset:-2048 sc1\n"
      "ds_read_b64_tr_b16 v[108:109], v35 offset:8192\n"
      "v_mfma_f32_16x16x32_bf16 v[148:151], a[160:163], v[84:87], v[148:151]\n"
      "ds_read_b64_tr_b16 v[110:111], v35 offset:10240\n"
      "v_mfma_f32_16x16x32_bf16 v[164:167], a[152:155], v[68:71], v[164:167]\n"
      "buffer_load_dwordx4 v[8:11], v50, s[32:35], s93 offen offset:0 sc1\n"
      "s_waitcnt lgkmcnt(4)\n"
      "v_add3_u32 v2, v2, v248, v249\n"
      "v_add3_u32 v2, v2, v250, v251\n"
      "v_mfma_f32_16x16x32_bf16 v[164:167], a[164:167], v[84:87], v[164:167]\n"
      "ds_read_b64_tr_b16 v[112:113], v6 offset:12288\n"
      "v_add3_u32 v2, v2, v252, v253\n"
      "v_add3_u32 v2, v2, v254, v255\n"
      "v_mfma_f32_16x16x32_bf16 v[136:139], a[144:147], v[72:75], v[136:139]\n"
      "global_load_dwordx4 a[236:239], v45, s[8:9] offset:-1024 sc1\n"
      "ds_read_b64_tr_b16 v[114:115], v6 offset:14336\n"
      "v_add3_u32 v2, v2, v36, v37\n"
      "v_add3_u32 v2, v2, v38, v39\n"
      "v_mfma_f32_16x16x32_bf16 v[136:139], a[156:159], v[88:91], v[136:139]\n"
      "ds_read_b64_tr_b16 v[116:117], v33 offset:12288\n"
      "v_add3_u32 v2, v2, v60, v61\n"
      "v_mfma_f32_16x16x32_bf16 v[152:155], a[148:151], v[72:75], v[152:155]\n"
      "buffer_load_dwordx4 v[12:15], v50, s[32:35], s93 offen offset:1024 sc1\n"
      "ds_read_b64_tr_b16 v[118:119], v33 offset:14336\n"
      "v_add3_u32 v2, v2, v62, v63\n"
      "v_mfma_f32_16x16x32_bf16 v[152:155], a[160:163], v[88:91], v[152:155]\n"
      "ds_read_b64_tr_b16 v[120:121], v34 offset:12288\n"
      "v_mfma_f32_16x16x32_bf16 v[168:171], a[152:155], v[72:75], v[168:171]\n"
      "ds_read_b64_tr_b16 v[122:123], v34 offset:14336\n"
      "v_add_u32_dpp v2, v2, v2 quad_perm:[1,0,3,2] row_mask:0xf bank_mask:0xf\n"
      "v_mfma_f32_16x16x32_bf16 v[168:171], a[164:167], v[88:91], v[168:171]\n"
      "ds_read_b64_tr_b16 v[124:125], v35 offset:12288\n"
      "v_mfma_f32_16x16x32_bf16 v[140:143], a[144:147], v[76:79], v[140:143]\n"
      "buffer_load_dwordx4 v[16:19], v50, s[32:35], s93 offen offset:2048 sc1\n"
      "v_add_u32_dpp v2, v2, v2 quad_perm:[2,3,0,1] row_mask:0xf bank_mask:0xf\n"
      "v_mfma_f32_16x16x32_bf16 v[140:143], a[156:159], v[92:95], v[140:143]\n"
      "ds_read_b64_tr_b16 v[126:127], v35 offset:14336\n"
      "v_mfma_f32_16x16x32_bf16 v[156:159], a[148:151], v[76:79], v[156:159]\n"
      "v_add_u32_dpp v2, v2, v2 row_half_mirror row_mask:0xf bank_mask:0xf\n"
      "v_cmp_ne_u32 vcc, s71, v2\n"
      "v_mfma_f32_16x16x32_bf16 v[156:159], a[160:163], v[92:95], v[156:159]\n"
      "v_mfma_f32_16x16x32_bf16 v[172:175], a[152:155], v[76:79], v[172:175]\n"
      "buffer_load_dwordx4 v[20:23], v50, s[32:35], s93 offen offset:3072 sc1\n"
      "s_cbranch_vccnz L_sx5_%=\n"
      "L_bx5_%=:\n"
      "v_mfma_f32_16x16x32_bf16 v[172:175], a[164:167], v[92:95], v[172:175]\n"
      "s_waitcnt vmcnt(25)\n"
      "s_waitcnt lgkmcnt(0)\n"
      "s_barrier\n"
      "v_mfma_f32_16x16x32_bf16 v[128:131], a[168:171], v[96:99], v[128:131]\n"
      "ds_read_b64_tr_b16 v[64:65], v6 offset:16384\n"
      "v_mfma_f32_16x16x32_bf16 v[128:131], a[180:183], v[112:115], v[128:131]\n"
      "ds_read_b64_tr_b16 v[66:67], v6 offset:18432\n"
      "v_mfma_f32_16x16x32_bf16 v[144:147], a[172:175], v[96:99], v[144:147]\n"
      "ds_read_b64_tr_b16 v[68:69], v33 offset:16384\n"
      "v_mfma_f32_16x16x32_bf16 v[144:147], a[184:187], v[112:115], v[144:147]\n"
      "ds_read_b64_tr_b16 v[70:71], v33 offset:18432\n"
      "v_mfma_f32_16x16x32_bf16 v[160:163], a[176:179], v[96:99], v[160:163]\n"
      "ds_read_b64_tr_b16 v[72:73], v34 offset:16384\n"
      "s_add_u32 m0, s73, 49152\n"
      "v_add_u32 v43, 114688, v40\n"
      "v_mfma_f32_16x16x32_bf16 v[160:163], a[188:191], v[112:115], v[160:163]\n"
      "ds_read_b64_tr_b16 v[74:75], v34 offset:18432\n"
      "v_mfma_f32_16x16x32_bf16 v[132:135], a[168:171], v[100:103], v[132:135]\n"
      "global_load_lds_dwordx4 v43, s[4:5] offset:0 sc1\n"
      "v_mfma_f32_16x16x32_bf16 v[132:135], a[180:183], v[116:119], v[132:135]\n"
      "ds_read_b64_tr_b16 v[76:77], v35 offset:16384\n"
      "v_mfma_f32_16x16x32_bf16 v[148:151], a[172:175], v[100:103], v[148:151]\n"
      "ds_read_b64_tr_b16 v[78:79], v35 offset:18432\n"
      "v_mfma_f32_16x16x32_bf16 v[148:151], a[184:187], v[116:119], v[148:151]\n"
      "ds_read_b64_tr_b16 v[80:81], v6 offset:20480\n"
      "v_mfma_f32_16x16x32_bf16 v[164:167], a[176:179], v[100:103], v[164:167]\n"
      "global_load_lds_dwordx4 v43, s[4:5] offset:1024 sc1\n"
      "v_mfma_f32_16x16x32_bf16 v[164:167], a[188:191], v[116:119], v[164:167]\n"
      "ds_read_b64_tr_b16 v[82:83], v6 offset:22528\n"
      "v_mfma_f32_16x16x32_bf16 v[136:139], a[168:171], v[104:107], v[136:139]\n"
      "ds_read_b64_tr_b16 v[84:85], v33 offset:20480\n"
      "s_waitcnt vmcnt(10)\n"
      "v_mfma_f32_16x16x32_bf16 v[136:139], a[180:183], v[120:123], v[136:139]\n"
      "ds_read_b64_tr_b16 v[86:87], v33 offset:22528\n"
      "v_add_f32 v228, v228, v232\n"
      "v_mfma_f32_16x16x32_bf16 v[152:155], a[172:175], v[104:107], v[152:155]\n"
      "global_load_lds_dwordx4 v43, s[4:5] offset:2048 sc1\n"
      "v_add_f32 v229, v229, v233\n"
      "v_mfma_f32_16x16x32_bf16 v[152:155], a[184:187], v[120:123], v[152:155]\n"
      "ds_read_b64_tr_b16 v[88:89], v34 offset:20480\n"
      "v_add_f32 v230, v230, v234\n"
      "v_mfma_f32_16x16x32_bf16 v[168:171], a[176:179], v[104:107], v[168:171]\n"
      "ds_read_b64_tr_b16 v[90:91], v34 offset:22528\n"
      "v_add_f32 v231, v231, v235\n"
      "v_mfma_f32_16x16x32_bf16 v[168:171], a[188:191], v[120:123], v[168:171]\n"
      "ds_read_b64_tr_b16 v[92:93], v35 offset:20480\n"
      "v_add_f32 v236, v236, v240\n"
      "v_mfma_f32_16x16x32_bf16 v[140:143], a[168:171], v[108:111], v[140:143]\n"
      "global_load_lds_dwordx4 v43, s[4:5] offset:3072 sc1\n"
      "v_add_f32 v237, v237, v241\n"
      "v_mfma_f32_16x16x32_bf16 v[140:143], a[180:183], v[124:127], v[140:143]\n"
      "ds_read_b64_tr_b16 v[94:95], v35 offset:22528\n"
      "v_add_f32 v238, v238, v242\n"
      "v_mfma_f32_16x16x32_bf16 v[156:159], a[172:175], v[108:111], v[156:159]\n"
      "s_add_u32 m0, s73, 0\n"
      "v_add_u32 v43, 131072, v40\n"
      "v_mfma_f32_16x16x32_bf16 v[156:159], a[184:187], v[124:127], v[156:159]\n"
      "v_add_f32 v239, v239, v243\n"
      "v_add_f32 v224, v224, v228\n"
      "v_mfma_f32_16x16x32_bf16 v[172:175], a[176:179], v[108:111], v[172:175]\n"
      "global_load_dwordx4 a[240:243], v44, s[6:7] offset:-4096 sc1\n"
      "v_add_f32 v225, v225, v229\n"
      "v_mfma_f32_16x16x32_bf16 v[172:175], a[188:191], v[124:127], v[172:175]\n"
      "s_waitcnt lgkmcnt(0)\n"
      "v_mfma_f32_16x16x32_bf16 v[128:131], a[192:195], v[64:67], v[128:131]\n"
      "ds_read_b64_tr_b16 v[96:97], v6 offset:24576\n"
      "v_add_f32 v226, v226, v230\n"
      "v_mfma_f32_16x16x32_bf16 v[128:131], a[204:207], v[80:83], v[128:131]\n"
      "ds_read_b64_tr_b16 v[98:99], v6 offset:26624\n"
      "v_add_f32 v227, v227, v231\n"
      "v_mfma_f32_16x16x32_bf16 v[144:147], a[196:199], v[64:67], v[144:147]\n"
      "global_load_dwordx4 a[244:247], v44, s[6:7] offset:-3072 sc1\n"
      "s_waitcnt vmcnt(13)\n"
      "v_mfma_f32_16x16x32_bf16 v[144:147], a[208:211], v[80:83], v[144:147]\n"
      "ds_read_b128 v[248:251], v40 offset:32768\n"
      "v_add_f32 v224, v224, v236\n"
      "v_mfma_f32_16x16x32_bf16 v[160:163], a[200:203], v[64:67], v[160:163]\n"
      "ds_read_b128 v[252:255], v40 offset:33792\n"
      "ds_read_b64_tr_b16 v[100:101], v33 offset:24576\n"
      "v_add_f32 v225, v225, v237\n"
      "v_mfma_f32_16x16x32_bf16 v[160:163], a[212:215], v[80:83], v[160:163]\n"
      "ds_read_b128 v[36:39], v40 offset:34816\n"
      "ds_read_b64_tr_b16 v[102:103], v33 offset:26624\n"
      "v_add_f32 v226, v226, v238\n"
      "v_mfma_f32_16x16x32_bf16 v[132:135], a[192:195], v[68:71], v[132:135]\n"
      "global_load_dwordx4 a[248:251], v44, s[6:7] offset:-2048 sc1\n"
      "ds_read_b128 v[60:63], v40 offset:35840\n"
      "ds_read_b64_tr_b16 v[104:105], v34 offset:24576\n"
      "v_add_f32 v227, v227, v239\n"
      "v_mfma_f32_16x16x32_bf16 v[132:135], a[204:207], v[84:87], v[132:135]\n"
      "ds_read_b64_tr_b16 v[106:107], v34 offset:26624\n"
      "v_mov_b32 v228, v224\n"
      "v_mfma_f32_16x16x32_bf16 v[148:151], a[196:199], v[68:71], v[148:151]\n"
      "ds_read_b64_tr_b16 v[108:109], v35 offset:24576\n"
      "v_mov_b32 v229, v225\n"
      "v_mfma_f32_16x16x32_bf16 v[148:151], a[208:211], v[84:87], v[148:151]\n"
      "ds_read_b64_tr_b16 v[110:111], v35 offset:26624\n"
      "v_mov_b32 v230, v226\n"
      "v_mfma_f32_16x16x32_bf16 v[164:167], a[200:203], v[68:71], v[164:167]\n"
      "global_load_lds_dwordx4 v43, s[4:5] offset:0 sc1\n"
      "s_waitcnt lgkmcnt(4)\n"
      "v_add3_u32 v49, v49, v248, v249\n"
      "v_add3_u32 v49, v49, v250, v251\n"
      "v_mfma_f32_16x16x32_bf16 v[164:167], a[212:215], v[84:87], v[164:167]\n"
      "ds_read_b64_tr_b16 v[112:113], v6 offset:28672\n"
      "v_add3_u32 v49, v49, v252, v253\n"
      "v_add3_u32 v49, v49, v254, v255\n"
      "v_mov_b32 v231, v227\n"
      "v_mfma_f32_16x16x32_bf16 v[136:139], a[192:195], v[72:75], v[136:139]\n"
      "ds_read_b64_tr_b16 v[114:115], v6 offset:30720\n"
      "v_add3_u32 v49, v49, v36, v37\n"
      "v_add3_u32 v49, v49, v38, v39\n"
      "v_mfma_f32_16x16x32_bf16 v[136:139], a[204:207], v[88:91], v[136:139]\n"
      "ds_read_b64_tr_b16 v[116:117], v33 offset:28672\n"
      "v_add3_u32 v49, v49, v60, v61\n"
      "v_lshl_add_u32 v59, v59, 3, s55\n"
      "v_mfma_f32_16x16x32_bf16 v[152:155], a[196:199], v[72:75], v[152:155]\n"
      "global_load_dwordx4 a[252:255], v44, s[6:7] offset:-1024 sc1\n"
      "ds_read_b64_tr_b16 v[118:119], v33 offset:30720\n"
      "v_add3_u32 v49, v49, v62, v63\n"
      "v_permlane32_swap_b32 v224, v228\n"
      "v_mfma_f32_16x16x32_bf16 v[152:155], a[208:211], v[88:91], v[152:155]\n"
      "ds_read_b64_tr_b16 v[120:121], v34 offset:28672\n"
      "v_permlane32_swap_b32 v225, v229\n"
      "v_mfma_f32_16x16x32_bf16 v[168:171], a[200:203], v[72:75], v[168:171]\n"
      "ds_read_b64_tr_b16 v[122:123], v34 offset:30720\n"
      "v_add_u32_dpp v49, v49, v49 quad_perm:[1,0,3,2] row_mask:0xf bank_mask:0xf\n"
      "v_permlane32_swap_b32 v226, v230\n"
      "v_mfma_f32_16x16x32_bf16 v[168:171], a[212:215], v[88:91], v[168:171]\n"
      "ds_read_b64_tr_b16 v[124:125], v35 offset:28672\n"
      "v_permlane32_swap_b32 v227, v231\n"
      "v_mfma_f32_16x16x32_bf16 v[140:143], a[192:195], v[76:79], v[140:143]\n"
      "global_load_lds_dwordx4 v43, s[4:5] offset:1024 sc1\n"
      "v_add_u32_dpp v49, v49, v49 quad_perm:[2,3,0,1] row_mask:0xf bank_mask:0xf\n"
      "v_add_f32 v224, v224, v228\n"
      "v_mfma_f32_16x16x32_bf16 v[140:143], a[204:207], v[92:95], v[140:143]\n"
      "ds_read_b64_tr_b16 v[126:127], v35 offset:30720\n"
      "v_add_f32 v225, v225, v229\n"
      "v_mfma_f32_16x16x32_bf16 v[156:159], a[196:199], v[76:79], v[156:159]\n"
      "v_add_u32_dpp v49, v49, v49 row_half_mirror row_mask:0xf bank_mask:0xf\n"
      "v_cmp_ne_u32 vcc, s71, v49\n"
      "v_add_f32 v226, v226, v230\n"
      "v_mfma_f32_16x16x32_bf16 v[156:159], a[208:211], v[92:95], v[156:159]\n"
      "v_add_f32 v227, v227, v231\n"
      "v_mfma_f32_16x16x32_bf16 v[172:175], a[200:203], v[76:79], v[172:175]\n"
      "global_load_dwordx4 a[0:3], v44, s[6:7] offset:0 sc1\n"
      "s_cbranch_vccnz L_sx6_%=\n"
      "L_bx6_%=:\n"
      "v_mfma_f32_16x16x32_bf16 v[172:175], a[212:215], v[92:95], v[172:175]\n"
      "ds_write_b128 v51, v[224:227]\n"
      "s_waitcnt vmcnt(14)\n"
      "s_waitcnt lgkmcnt(1)\n"
      "s_barrier\n"
      "v_mfma_f32_16x16x32_bf16 v[128:131], a[216:219], v[96:99], v[128:131]\n"
      "ds_read_b64_tr_b16 v[64:65], v6 offset:32768\n"
      "v_mfma_f32_16x16x32_bf16 v[128:131], a[228:231], v[112:115], v[128:131]\n"
      "global_load_lds_dwordx4 v43, s[4:5] offset:2048 sc1\n"
      "v_mfma_f32_16x16x32_bf16 v[144:147], a[220:223], v[96:99], v[144:147]\n"
      "ds_read_b64_tr_b16 v[66:67], v6 offset:34816\n"
      "s_waitcnt vmcnt(12)\n"
      "v_add3_u32 v10, v10, v14, v18\n"
      "v_mfma_f32_16x16x32_bf16 v[144:147], a[232:235], v[112:115], v[144:147]\n"
      "ds_read_b64_tr_b16 v[68:69], v34 offset:32768\n"
      "v_add_u32 v10, v10, v22\n"
      "v_cmp_ne_u32 vcc, s71, v10\n"
      "v_mfma_f32_16x16x32_bf16 v[160:163], a[224:227], v[96:99], v[160:163]\n"
      "global_load_dwordx4 a[4:7], v44, s[6:7] offset:1024 sc1\n"
      "v_mfma_f32_16x16x32_bf16 v[160:163], a[236:239], v[112:115], v[160:163]\n"
      "global_load_lds_dwordx4 v43, s[4:5] offset:3072 sc1\n"
      "ds_read_b64_tr_b16 v[70:71], v34 offset:34816\n"
      "s_cbranch_vccnz L_sg_%=\n"
      "L_bg_%=:\n"
      "v_add_f32 v8, v8, v12\n"
      "v_add_f32 v9, v9, v13\n"
      "v_mfma_f32_16x16x32_bf16 v[132:135], a[216:219], v[100:103], v[132:135]\n"
      "ds_read_b64_tr_b16 v[72:73], v33 offset:32768\n"
      "v_add_f32 v16, v16, v20\n"
      "v_add_f32 v17, v17, v21\n"
      "v_mfma_f32_16x16x32_bf16 v[132:135], a[228:231], v[116:119], v[132:135]\n"
      "ds_read_b64_tr_b16 v[74:75], v33 offset:34816\n"
      "v_add_f32 v8, v8, v16\n"
      "v_add_u32 v245, v245, v247\n"
      "v_mfma_f32_16x16x32_bf16 v[148:151], a[220:223], v[100:103], v[148:151]\n"
      "global_load_dwordx4 a[8:11], v44, s[6:7] offset:2048 sc1\n"
      "ds_read_b64_tr_b16 v[76:77], v35 offset:32768\n"
      "v_add_f32 v9, v9, v17\n"
      "v_mov_b32 v10, v8\n"
      "v_cmp_ne_u32 vcc, s54, v245\n"
      "v_mfma_f32_16x16x32_bf16 v[148:151], a[232:235], v[116:119], v[148:151]\n"
      "ds_read_b64_tr_b16 v[78:79], v35 offset:34816\n"
      "v_mov_b32 v11, v9\n"
      "s_nop 0\n"
      "v_mfma_f32_16x16x32_bf16 v[164:167], a[224:227], v[100:103], v[164:167]\n"
      "ds_read_b64_tr_b16 v[80:81], v6 offset:36864\n"
      "v_permlane32_swap_b32 v8, v10\n"
      "v_permlane32_swap_b32 v9, v11\n"
      "s_cbranch_vccnz L_sgb_%=\n"
      "L_bgb_%=:\n"
      "v_add_f32 v8, v8, v10\n"
      "v_mfma_f32_16x16x32_bf16 v[164:167], a[236:239], v[116:119], v[164:167]\n"
      "ds_read_b64_tr_b16 v[82:83], v6 offset:38912\n"
      "v_add_f32_dpp v244, v244, v244 quad_perm:[1,0,3,2] row_mask:0xf bank_mask:0xf\n"
      "v_add_f32_dpp v246, v246, v246 quad_perm:[1,0,3,2] row_mask:0xf bank_mask:0xf\n"
      "v_mfma_f32_16x16x32_bf16 v[136:139], a[216:219], v[104:107], v[136:139]\n"
      "global_load_dwordx4 a[12:15], v44, s[6:7] offset:3072 sc1\n"
      "v_add_f32 v9, v9, v11\n"
      "v_mul_f32 v8, 0x3acccccd, v8\n"
      "v_add_f32_dpp v244, v244, v244 quad_perm:[2,3,0,1] row_mask:0xf bank_mask:0xf\n"
      "v_add_f32_dpp v246, v246, v246 quad_perm:[2,3,0,1] row_mask:0xf bank_mask:0xf\n"
      "v_mfma_f32_16x16x32_bf16 v[136:139], a[228:231], v[120:123], v[136:139]\n"
      "ds_read_b64_tr_b16 v[84:85], v34 offset:36864\n"
      "v_mul_f32 v9, 0x3acccccd, v9\n"
      "v_fma_f32 v9, -v8, v8, v9\n"
      "s_add_u32 m0, s40, 0\n"
      "v_add_u32 v43, 40960, v7\n"
      "v_mfma_f32_16x16x32_bf16 v[152:155], a[220:223], v[104:107], v[152:155]\n"
      "global_load_lds_dwordx4 v43, s[6:7] offset:0 sc1\n"
      "global_load_lds_dwordx4 v43, s[6:7] offset:1024 sc1\n"
      "global_load_lds_dwordx4 v43, s[6:7] offset:2048 sc1\n"
      "global_load_lds_dwordx4 v43, s[6:7] offset:3072 sc1\n"
      "ds_read_b64_tr_b16 v[86:87], v34 offset:38912\n"
      "v_max_f32 v9, 0, v9\n"
      "s_add_u32 m0, s73, 16384\n"
      "v_add_u32 v43, 147456, v40\n"
      "v_cvt_pk_bf16_f32 v244, 0, v244\n"
      "v_cvt_pk_bf16_f32 v245, 0, v246\n"
      "v_mfma_f32_16x16x32_bf16 v[152:155], a[232:235], v[120:123], v[152:155]\n"
      "global_load_lds_dwordx4 v43, s[4:5] offset:0 sc1\n"
      "ds_read_b64_tr_b16 v[88:89], v33 offset:36864\n"
      "ds_write_b64 v59, v[244:245]\n"
      "v_cvt_pk_bf16_f32 v9, 0, v9\n"
      "v_add_f32 v9, 0x3727c5ac, v9\n"
      "v_add_u32 v44, 12288, v7\n"
      "v_mfma_f32_16x16x32_bf16 v[168:171], a[224:227], v[104:107], v[168:171]\n"
      "global_load_dwordx4 a[16:19], v44, s[6:7] offset:-4096 sc1\n"
      "ds_read_b64_tr_b16 v[90:91], v33 offset:38912\n"
      "ds_read_b128 v[224:227], v52 offset:0\n"
      "v_cvt_pk_bf16_f32 v9, 0, v9\n"
      "v_mfma_f32_16x16x32_bf16 v[168:171], a[236:239], v[120:123], v[168:171]\n"
      "ds_read_b64_tr_b16 v[92:93], v35 offset:36864\n"
      "ds_read_b128 v[228:231], v52 offset:128\n"
      "v_sqrt_f32 v9, v9\n"
      "v_sub_f32 v8, 0, v8\n"
      "v_cvt_pk_bf16_f32 v9, 0, v9\n"
      "v_mfma_f32_16x16x32_bf16 v[140:143], a[216:219], v[108:111], v[140:143]\n"
      "ds_read_b128 v[232:235], v52 offset:256\n"
      "v_rcp_f32 v9, v9\n"
      "v_cvt_pk_bf16_f32 v8, 0, v8\n"
      "v_mfma_f32_16x16x32_bf16 v[140:143], a[228:231], v[124:127], v[140:143]\n"
      "global_load_lds_dwordx4 v43, s[4:5] offset:1024 sc1\n"
      "ds_write_b64 v32, v[8:9]\n"
      "ds_read_b64_tr_b16 v[94:95], v35 offset:38912\n"
      "v_mfma_f32_16x16x32_bf16 v[156:159], a[220:223], v[108:111], v[156:159]\n"
      "global_load_dwordx4 a[20:23], v44, s[6:7] offset:-3072 sc1\n"
      "ds_read_b128 v[236:239], v52 offset:64\n"
      "v_mfma_f32_16x16x32_bf16 v[156:159], a[232:235], v[124:127], v[156:159]\n"
      "ds_read_b128 v[240:243], v52 offset:192\n"
      "v_mfma_f32_16x16x32_bf16 v[172:175], a[224:227], v[108:111], v[172:175]\n"
      "ds_read_b128 v[244:247], v52 offset:320\n"
      "v_mfma_f32_16x16x32_bf16 v[172:175], a[236:239], v[124:127], v[172:175]\n"
      "global_load_lds_dwordx4 v43, s[4:5] offset:2048 sc1\n"
      "s_waitcnt lgkmcnt(3)\n"
      "s_barrier\n"
      "v_mfma_f32_16x16x32_bf16 v[176:179], a[48:51], v[64:67], v[176:179]\n"
      "global_load_dwordx4 a[24:27], v44, s[6:7] offset:-2048 sc1\n"
      "ds_read_b64 v[16:17], v41 offset:0\n"
      "v_mfma_f32_16x16x32_bf16 v[176:179], a[60:63], v[80:83], v[176:179]\n"
      "ds_read_b64 v[18:19], v41 offset:128\n"
      "v_mfma_f32_16x16x32_bf16 v[184:187], a[52:55], v[64:67], v[184:187]\n"
      "ds_read_b64 v[20:21], v41 offset:256\n"
      "ds_read_b64_tr_b16 v[96:97], v6 offset:40960\n"
      "s_waitcnt vmcnt(22)\n"
      "v_mfma_f32_16x16x32_bf16 v[184:187], a[64:67], v[80:83], v[184:187]\n"
      "global_load_lds_dwordx4 v43, s[4:5] offset:3072 sc1\n"
      "ds_read_b128 v[248:251], v40 offset:49152\n"
      "ds_read_b64 v[22:23], v41 offset:384\n"
      "ds_read_b64_tr_b16 v[98:99], v6 offset:43008\n"
      "v_mfma_f32_16x16x32_bf16 v[192:195], a[56:59], v[64:67], v[192:195]\n"
      "global_load_dwordx4 a[28:31], v44, s[6:7] offset:-1024 sc1\n"
      "ds_read_b128 v[252:255], v40 offset:50176\n"
      "ds_read_b64_tr_b16 v[100:101], v34 offset:40960\n"
      "v_mfma_f32_16x16x32_bf16 v[192:195], a[68:71], v[80:83], v[192:195]\n"
      "ds_read_b128 v[36:39], v40 offset:51200\n"
      "ds_read_b64_tr_b16 v[102:103], v34 offset:43008\n"
      "v_mfma_f32_16x16x32_bf16 v[180:183], a[48:51], v[68:71], v[180:183]\n"
      "ds_read_b128 v[60:63], v40 offset:52224\n"
      "ds_read_b64_tr_b16 v[104:105], v33 offset:40960\n"
      "s_waitcnt lgkmcnt(7)\n"
      "v_mfma_f32_16x16x32_bf16 v[180:183], a[60:63], v[84:87], v[180:183]\n"
      "ds_read_b64_tr_b16 v[106:107], v33 offset:43008\n"
      "v_fmac_f32 v128, v16, v224\n"
      "v_fmac_f32 v129, v16, v225\n"
      "v_mfma_f32_16x16x32_bf16 v[188:191], a[52:55], v[68:71], v[188:191]\n"
      "global_load_dwordx4 a[32:35], v44, s[6:7] offset:0 sc1\n"
      "ds_read_b64_tr_b16 v[108:109], v35 offset:40960\n"
      "v_fmac_f32 v130, v16, v226\n"
      "v_fmac_f32 v131, v16, v227\n"
      "v_mfma_f32_16x16x32_bf16 v[188:191], a[64:67], v[84:87], v[188:191]\n"
      "ds_read_b64_tr_b16 v[110:111], v35 offset:43008\n"
      "v_fma_f32 v128, v128, v17, v236\n"
      "v_fma_f32 v129, v129, v17, v237\n"
      "v_fma_f32 v130, v130, v17, v238\n"
      "v_mfma_f32_16x16x32_bf16 v[196:199], a[56:59], v[68:71], v[196:199]\n"
      "ds_read_b64_tr_b16 v[112:113], v6 offset:45056\n"
      "s_waitcnt lgkmcnt(5)\n"
      "v_add3_u32 v55, v55, v248, v249\n"
      "v_add3_u32 v55, v55, v250, v251\n"
      "v_fma_f32 v131, v131, v17, v239\n"
      "v_mul_f32 v128, v128, v130\n"
      "v_mfma_f32_16x16x32_bf16 v[196:199], a[68:71], v[84:87], v[196:199]\n"
      "ds_read_b64_tr_b16 v[114:115], v6 offset:47104\n"
      "v_add3_u32 v55, v55, v252, v253\n"
      "v_add3_u32 v55, v55, v254, v255\n"
      "v_mul_f32 v129, v129, v131\n"
      "v_cvt_pk_bf16_f32 v128, v128, v129\n"
      "v_mfma_f32_16x16x32_bf16 v[200:203], a[48:51], v[72:75], v[200:203]\n"
      "global_load_dwordx4 a[36:39], v44, s[6:7] offset:1024 sc1\n"
      "ds_write_b32 v46, v128 offset:0\n"
      "v_add3_u32 v55, v55, v36, v37\n"
      "v_add3_u32 v55, v55, v38, v39\n"
      "v_fmac_f32 v144, v16, v228\n"
      "v_fmac_f32 v145, v16, v229\n"
      "v_mfma_f32_16x16x32_bf16 v[200:203], a[60:63], v[88:91], v[200:203]\n"
      "ds_read_b64_tr_b16 v[116:117], v34 offset:45056\n"
      "v_add3_u32 v55, v55, v60, v61\n"
      "v_fmac_f32 v146, v16, v230\n"
      "v_fmac_f32 v147, v16, v231\n"
      "v_mfma_f32_16x16x32_bf16 v[208:211], a[52:55], v[72:75], v[208:211]\n"
      "ds_read_b64_tr_b16 v[118:119], v34 offset:47104\n"
      "v_add3_u32 v55, v55, v62, v63\n"
      "v_fma_f32 v144, v144, v17, v240\n"
      "v_fma_f32 v145, v145, v17, v241\n"
      "v_mfma_f32_16x16x32_bf16 v[208:211], a[64:67], v[88:91], v[208:211]\n"
      "ds_read_b64_tr_b16 v[120:121], v33 offset:45056\n"
      "v_fma_f32 v146, v146, v17, v242\n"
      "v_fma_f32 v147, v147, v17, v243\n"
      "v_mul_f32 v144, v144, v146\n"
      "v_mfma_f32_16x16x32_bf16 v[216:219], a[56:59], v[72:75], v[216:219]\n"
      "global_load_dwordx4 a[40:43], v44, s[6:7] offset:2048 sc1\n"
      "ds_read_b64_tr_b16 v[122:123], v33 offset:47104\n"
      "v_add_u32_dpp v55, v55, v55 quad_perm:[1,0,3,2] row_mask:0xf bank_mask:0xf\n"
      "v_mul_f32 v145, v145, v147\n"
      "v_cvt_pk_bf16_f32 v144, v144, v145\n"
      "v_mfma_f32_16x16x32_bf16 v[216:219], a[68:71], v[88:91], v[216:219]\n"
      "ds_write_b32 v46, v144 offset:4096\n"
      "ds_read_b64_tr_b16 v[124:125], v35 offset:45056\n"
      "v_fmac_f32 v160, v16, v232\n"
      "v_mfma_f32_16x16x32_bf16 v[204:207], a[48:51], v[76:79], v[204:207]\n"
      "ds_read_b64_tr_b16 v[126:127], v35 offset:47104\n"
      "v_add_u32_dpp v55, v55, v55 quad_perm:[2,3,0,1] row_mask:0xf bank_mask:0xf\n"
      "v_fmac_f32 v161, v16, v233\n"
      "v_fmac_f32 v162, v16, v234\n"
      "v_fmac_f32 v163, v16, v235\n"
      "v_mfma_f32_16x16x32_bf16 v[204:207], a[60:63], v[92:95], v[204:207]\n"
      "v_fma_f32 v160, v160, v17, v244\n"
      "v_fma_f32 v161, v161, v17, v245\n"
      "v_mfma_f32_16x16x32_bf16 v[212:215], a[52:55], v[76:79], v[212:215]\n"
      "global_load_dwordx4 a[44:47], v44, s[6:7] offset:3072 sc1\n"
      "v_add_u32_dpp v55, v55, v55 row_half_mirror row_mask:0xf bank_mask:0xf\n"
      "v_cmp_ne_u32 vcc, s71, v55\n"
      "v_fma_f32 v162, v162, v17, v246\n"
      "v_fma_f32 v163, v163, v17, v247\n"
      "v_mul_f32 v160, v160, v162\n"
      "v_mfma_f32_16x16x32_bf16 v[212:215], a[64:67], v[92:95], v[212:215]\n"
      "v_mul_f32 v161, v161, v163\n"
      "v_cvt_pk_bf16_f32 v160, v160, v161\n"
      "v_mfma_f32_16x16x32_bf16 v[220:223], a[56:59], v[76:79], v[220:223]\n"
      "ds_write_b32 v46, v160 offset:8192\n"
      "s_cbranch_vccnz L_sx7_%=\n"
      "L_bx7_%=:\n"
      "v_fmac_f32 v132, v18, v224\n"
      "v_mfma_f32_16x16x32_bf16 v[220:223], a[68:71], v[92:95], v[220:223]\n"
      "v_fmac_f32 v133, v18, v225\n"
      "v_fmac_f32 v134, v18, v226\n"
      "v_fmac_f32 v135, v18, v227\n"
      "s_waitcnt lgkmcnt(1)\n"
      "s_barrier\n"
      "v_mfma_f32_16x16x32_bf16 v[176:179], a[72:75], v[96:99], v[176:179]\n"
      "ds_read_b64_tr_b16 v[64:65], v6 offset:49152\n"
      "v_fma_f32 v132, v132, v19, v236\n"
      "v_fma_f32 v133, v133, v19, v237\n"
      "v_mfma_f32_16x16x32_bf16 v[176:179], a[84:87], v[112:115], v[176:179]\n"
      "ds_read_b64_tr_b16 v[66:67], v6 offset:51200\n"
      "v_fma_f32 v134, v134, v19, v238\n"
      "v_fma_f32 v135, v135, v19, v239\n"
      "v_add_u32 v44, 20480, v7\n"
      "v_mfma_f32_16x16x32_bf16 v[184:187], a[76:79], v[96:99], v[184:187]\n"
      "global_load_dwordx4 a[48:51], v44, s[6:7] offset:-4096 sc1\n"
      "v_mul_f32 v132, v132, v134\n"
      "v_mul_f32 v133, v133, v135\n"
      "v_cvt_pk_bf16_f32 v132, v132, v133\n"
      "v_mfma_f32_16x16x32_bf16 v[184:187], a[88:91], v[112:115], v[184:187]\n"
      "ds_write_b32 v46, v132 offset:1024\n"
      "ds_read_b64_tr_b16 v[68:69], v34 offset:49152\n"
      "v_fmac_f32 v148, v18, v228\n"
      "v_mfma_f32_16x16x32_bf16 v[192:195], a[80:83], v[96:99], v[192:195]\n"
      "global_load_dwordx4 a[52:55], v44, s[6:7] offset:-3072 sc1\n"
      "s_waitcnt vmcnt(20)\n"
      "v_fmac_f32 v149, v18, v229\n"
      "v_fmac_f32 v150, v18, v230\n"
      "v_mfma_f32_16x16x32_bf16 v[192:195], a[92:95], v[112:115], v[192:195]\n"
      "ds_read_b128 v[248:251], v40 offset:0\n"
      "ds_read_b64_tr_b16 v[70:71], v34 offset:51200\n"
      "v_fmac_f32 v151, v18, v231\n"
      "v_fma_f32 v148, v148, v19, v240\n"
      "v_fma_f32 v149, v149, v19, v241\n"
      "v_mfma_f32_16x16x32_bf16 v[180:183], a[72:75], v[100:103], v[180:183]\n"
      "ds_read_b128 v[252:255], v40 offset:1024\n"
      "ds_read_b64_tr_b16 v[72:73], v33 offset:49152\n"
      "v_fma_f32 v150, v150, v19, v242\n"
      "v_fma_f32 v151, v151, v19, v243\n"
      "v_mfma_f32_16x16x32_bf16 v[180:183], a[84:87], v[116:119], v[180:183]\n"
      "ds_read_b128 v[36:39], v40 offset:2048\n"
      "ds_read_b64_tr_b16 v[74:75], v33 offset:51200\n"
      "v_mul_f32 v148, v148, v150\n"
      "v_mul_f32 v149, v149, v151\n"
      "v_cvt_pk_bf16_f32 v148, v148, v149\n"
      "v_mfma_f32_16x16x32_bf16 v[188:191], a[76:79], v[100:103], v[188:191]\n"
      "global_load_dwordx4 a[56:59], v44, s[6:7] offset:-2048 sc1\n"
      "ds_read_b128 v[60:63], v40 offset:3072\n"
      "ds_write_b32 v46, v148 offset:5120\n"
      "ds_read_b64_tr_b16 v[76:77], v35 offset:49152\n"
      "v_fmac_f32 v164, v18, v232\n"
      "v_mfma_f32_16x16x32_bf16 v[188:191], a[88:91], v[116:119], v[188:191]\n"
      "ds_read_b64_tr_b16 v[78:79], v35 offset:51200\n"
      "v_fmac_f32 v165, v18, v233\n"
      "v_fmac_f32 v166, v18, v234\n"
      "v_mfma_f32_16x16x32_bf16 v[196:199], a[80:83], v[100:103], v[196:199]\n"
      "ds_read_b64_tr_b16 v[80:81], v6 offset:53248\n"
      "v_fmac_f32 v167, v18, v235\n"
      "v_fma_f32 v164, v164, v19, v244\n"
      "v_fma_f32 v165, v165, v19, v245\n"
      "v_mfma_f32_16x16x32_bf16 v[196:199], a[92:95], v[116:119], v[196:199]\n"
      "ds_read_b64_tr_b16 v[82:83], v6 offset:55296\n"
      "v_fma_f32 v166, v166, v19, v246\n"
      "v_fma_f32 v167, v167, v19, v247\n"
      "v_mfma_f32_16x16x32_bf16 v[200:203], a[72:75], v[104:107], v[200:203]\n"
      "global_load_dwordx4 a[60:63], v44, s[6:7] offset:-1024 sc1\n"
      "s_waitcnt lgkmcnt(5)\n"
      "v_add3_u32 v57, v57, v248, v249\n"
      "v_add3_u32 v57, v57, v250, v251\n"
      "v_mul_f32 v164, v164, v166\n"
      "v_mul_f32 v165, v165, v167\n"
      "v_cvt_pk_bf16_f32 v164, v164, v165\n"
      "v_mfma_f32_16x16x32_bf16 v[200:203], a[84:87], v[120:123], v[200:203]\n"
      "ds_write_b32 v46, v164 offset:9216\n"
      "ds_read_b64_tr_b16 v[84:85], v34 offset:53248\n"
      "v_add3_u32 v57, v57, v252, v253\n"
      "v_add3_u32 v57, v57, v254, v255\n"
      "v_fmac_f32 v136, v20, v224\n"
      "v_mfma_f32_16x16x32_bf16 v[208:211], a[76:79], v[104:107], v[208:211]\n"
      "ds_read_b64_tr_b16 v[86:87], v34 offset:55296\n"
      "v_add3_u32 v57, v57, v36, v37\n"
      "v_add3_u32 v57, v57, v38, v39\n"
      "v_fmac_f32 v137, v20, v225\n"
      "v_fmac_f32 v138, v20, v226\n"
      "v_mfma_f32_16x16x32_bf16 v[208:211], a[88:91], v[120:123], v[208:211]\n"
      "ds_read_b64_tr_b16 v[88:89], v33 offset:53248\n"
      "v_add3_u32 v57, v57, v60, v61\n"
      "v_fmac_f32 v139, v20, v227\n"
      "v_fma_f32 v136, v136, v21, v236\n"
      "v_mfma_f32_16x16x32_bf16 v[216:219], a[80:83], v[104:107], v[216:219]\n"
      "global_load_dwordx4 a[64:67], v44, s[6:7] offset:0 sc1\n"
      "ds_read_b64_tr_b16 v[90:91], v33 offset:55296\n"
      "v_add3_u32 v57, v57, v62, v63\n"
      "v_fma_f32 v137, v137, v21, v237\n"
      "v_fma_f32 v138, v138, v21, v238\n"
      "v_fma_f32 v139, v139, v21, v239\n"
      "v_mfma_f32_16x16x32_bf16 v[216:219], a[92:95], v[120:123], v[216:219]\n"
      "ds_read_b64_tr_b16 v[92:93], v35 offset:53248\n"
      "v_mul_f32 v136, v136, v138\n"
      "v_mul_f32 v137, v137, v139\n"
      "v_cvt_pk_bf16_f32 v136, v136, v137\n"
      "v_mfma_f32_16x16x32_bf16 v[204:207], a[72:75], v[108:111], v[204:207]\n"
      "ds_write_b32 v46, v136 offset:2048\n"
      "ds_read_b64_tr_b16 v[94:95], v35 offset:55296\n"
      "v_add_u32_dpp v57, v57, v57 quad_perm:[1,0,3,2] row_mask:0xf bank_mask:0xf\n"
      "v_mfma_f32_16x16x32_bf16 v[204:207], a[84:87], v[124:127], v[204:207]\n"
      "v_fmac_f32 v152, v20, v228\n"
      "v_fmac_f32 v153, v20, v229\n"
      "v_fmac_f32 v154, v20, v230\n"
      "v_mfma_f32_16x16x32_bf16 v[212:215], a[76:79], v[108:111], v[212:215]\n"
      "global_load_dwordx4 a[68:71], v44, s[6:7] offset:1024 sc1\n"
      "v_add_u32_dpp v57, v57, v57 quad_perm:[2,3,0,1] row_mask:0xf bank_mask:0xf\n"
      "v_fmac_f32 v155, v20, v231\n"
      "v_fma_f32 v152, v152, v21, v240\n"
      "v_mfma_f32_16x16x32_bf16 v[212:215], a[88:91], v[124:127], v[212:215]\n"
      "v_fma_f32 v153, v153, v21, v241\n"
      "v_fma_f32 v154, v154, v21, v242\n"
      "v_fma_f32 v155, v155, v21, v243\n"
      "v_mfma_f32_16x16x32_bf16 v[220:223], a[80:83], v[108:111], v[220:223]\n"
      "v_add_u32_dpp v57, v57, v57 row_half_mirror row_mask:0xf bank_mask:0xf\n"
      "v_cmp_ne_u32 vcc, s71, v57\n"
      "v_mul_f32 v152, v152, v154\n"
      "v_mul_f32 v153, v153, v155\n"
      "v_cvt_pk_bf16_f32 v152, v152, v153\n"
      "v_mfma_f32_16x16x32_bf16 v[220:223], a[92:95], v[124:127], v[220:223]\n"
      "ds_write_b32 v46, v152 offset:6144\n"
      "v_fmac_f32 v168, v20, v232\n"
      "s_waitcnt lgkmcnt(1)\n"
      "v_mfma_f32_16x16x32_bf16 v[176:179], a[96:99], v[64:67], v[176:179]\n"
      "ds_read_b64_tr_b16 v[96:97], v6 offset:57344\n"
      "s_cbranch_vccnz L_sx8_%=\n"
      "L_bx8_%=:\n"
      "v_fmac_f32 v169, v20, v233\n"
      "v_fmac_f32 v170, v20, v234\n"
      "s_add_u32 m0, s40, 4096\n"
      "v_add_u32 v43, 45056, v7\n"
      "v_mfma_f32_16x16x32_bf16 v[176:179], a[108:111], v[80:83], v[176:179]\n"
      "L_pg5_%=:\n"
      "s_cbranch_execz L_pgr_%=\n"
      "global_load_lds_dwordx4 v43, s[6:7] offset:0 sc1\n"
      "v_fmac_f32 v171, v20, v235\n"
      "v_fma_f32 v168, v168, v21, v244\n"
      "v_mfma_f32_16x16x32_bf16 v[184:187], a[100:103], v[64:67], v[184:187]\n"
      "global_load_dwordx4 a[72:75], v44, s[6:7] offset:2048 sc1\n"
      "s_waitcnt vmcnt(13)\n"
      "v_fma_f32 v169, v169, v21, v245\n"
      "v_fma_f32 v170, v170, v21, v246\n"
      "v_mfma_f32_16x16x32_bf16 v[184:187], a[112:115], v[80:83], v[184:187]\n"
      "ds_read_b128 v[248:251], v40 offset:16384\n"
      "ds_read_b64_tr_b16 v[98:99], v6 offset:59392\n"
      "v_fma_f32 v171, v171, v21, v247\n"
      "v_mul_f32 v168, v168, v170\n"
      "v_mul_f32 v169, v169, v171\n"
      "v_cvt_pk_bf16_f32 v168, v168, v169\n"
      "v_mfma_f32_16x16x32_bf16 v[192:195], a[104:107], v[64:67], v[192:195]\n"
      "global_load_dwordx4 a[76:79], v44, s[6:7] offset:3072 sc1\n"
      "ds_read_b128 v[252:255], v40 offset:17408\n"
      "ds_write_b32 v46, v168 offset:10240\n"
      "ds_read_b64_tr_b16 v[100:101], v34 offset:57344\n"
      "v_fmac_f32 v140, v22, v224\n"
      "v_mfma_f32_16x16x32_bf16 v[192:195], a[116:119], v[80:83], v[192:195]\n"
      "global_load_lds_dwordx4 v43, s[6:7] offset:1024 sc1\n"
      "ds_read_b128 v[36:39], v40 offset:18432\n"
      "ds_read_b64_tr_b16 v[102:103], v34 offset:59392\n"
      "v_fmac_f32 v141, v22, v225\n"
      "v_mfma_f32_16x16x32_bf16 v[180:183], a[96:99], v[68:71], v[180:183]\n"
      "ds_read_b128 v[60:63], v40 offset:19456\n"
      "ds_read_b64_tr_b16 v[104:105], v33 offset:57344\n"
      "v_fmac_f32 v142, v22, v226\n"
      "v_fmac_f32 v143, v22, v227\n"
      "v_fma_f32 v140, v140, v23, v236\n"
      "v_mfma_f32_16x16x32_bf16 v[180:183], a[108:111], v[84:87], v[180:183]\n"
      "ds_read_b64_tr_b16 v[106:107], v33 offset:59392\n"
      "v_fma_f32 v141, v141, v23, v237\n"
      "v_fma_f32 v142, v142, v23, v238\n"
      "v_add_u32 v44, 28672, v7\n"
      "v_mfma_f32_16x16x32_bf16 v[188:191], a[100:103], v[68:71], v[188:191]\n"
      "global_load_dwordx4 a[80:83], v44, s[6:7] offset:-4096 sc1\n"
      "ds_read_b64_tr_b16 v[108:109], v35 offset:57344\n"
      "v_fma_f32 v143, v143, v23, v239\n"
      "v_mul_f32 v140, v140, v142\n"
      "v_mul_f32 v141, v141, v143\n"
      "v_cvt_pk_bf16_f32 v140, v140, v141\n"
      "v_mfma_f32_16x16x32_bf16 v[188:191], a[112:115], v[84:87], v[188:191]\n"
      "global_load_lds_dwordx4 v43, s[6:7] offset:2048 sc1\n"
      "ds_write_b32 v46, v140 offset:3072\n"
      "ds_read_b64_tr_b16 v[110:111], v35 offset:59392\n"
      "v_fmac_f32 v156, v22, v228\n"
      "v_mfma_f32_16x16x32_bf16 v[196:199], a[104:107], v[68:71], v[196:199]\n"
      "ds_read_b64_tr_b16 v[112:113], v6 offset:61440\n"
      "s_waitcnt lgkmcnt(6)\n"
      "v_add3_u32 v3, v3, v248, v249\n"
      "v_add3_u32 v3, v3, v250, v251\n"
      "v_fmac_f32 v157, v22, v229\n"
      "v_mfma_f32_16x16x32_bf16 v[196:199], a[116:119], v[84:87], v[196:199]\n"
      "ds_read_b64_tr_b16 v[114:115], v6 offset:63488\n"
      "v_add3_u32 v3, v3, v252, v253\n"
      "v_add3_u32 v3, v3, v254, v255\n"
      "v_fmac_f32 v158, v22, v230\n"
      "v_fmac_f32 v159, v22, v231\n"
      "v_fma_f32 v156, v156, v23, v240\n"
      "v_mfma_f32_16x16x32_bf16 v[200:203], a[96:99], v[72:75], v[200:203]\n"
      "global_load_dwordx4 a[84:87], v44, s[6:7] offset:-3072 sc1\n"
      "v_add3_u32 v3, v3, v36, v37\n"
      "v_add3_u32 v3, v3, v38, v39\n"
      "v_fma_f32 v157, v157, v23, v241\n"
      "v_fma_f32 v158, v158, v23, v242\n"
      "v_mfma_f32_16x16x32_bf16 v[200:203], a[108:111], v[88:91], v[200:203]\n"
      "global_load_lds_dwordx4 v43, s[6:7] offset:3072 sc1\n"
      "ds_read_b64_tr_b16 v[116:117], v34 offset:61440\n"
      "v_add3_u32 v3, v3, v60, v61\n"
      "v_fma_f32 v159, v159, v23, v243\n"
      "v_mul_f32 v156, v156, v158\n"
      "v_mul_f32 v157, v157, v159\n"
      "v_cvt_pk_bf16_f32 v156, v156, v157\n"
      "v_mfma_f32_16x16x32_bf16 v[208:211], a[100:103], v[72:75], v[208:211]\n"
      "ds_write_b32 v46, v156 offset:7168\n"
      "ds_read_b64_tr_b16 v[118:119], v34 offset:63488\n"
      "v_add3_u32 v3, v3, v62, v63\n"
      "v_mfma_f32_16x16x32_bf16 v[208:211], a[112:115], v[88:91], v[208:211]\n"
      "ds_read_b64_tr_b16 v[120:121], v33 offset:61440\n"
      "v_fmac_f32 v172, v22, v232\n"
      "v_fmac_f32 v173, v22, v233\n"
      "v_mfma_f32_16x16x32_bf16 v[216:219], a[104:107], v[72:75], v[216:219]\n"
      "global_load_dwordx4 a[88:91], v44, s[6:7] offset:-2048 sc1\n"
      "ds_read_b64_tr_b16 v[122:123], v33 offset:63488\n"
      "v_add_u32_dpp v3, v3, v3 quad_perm:[1,0,3,2] row_mask:0xf bank_mask:0xf\n"
      "v_fmac_f32 v174, v22, v234\n"
      "v_fmac_f32 v175, v22, v235\n"
      "s_add_u32 m0, s40, 8192\n"
      "v_add_u32 v43, 49152, v7\n"
      "v_mfma_f32_16x16x32_bf16 v[216:219], a[116:119], v[88:91], v[216:219]\n"
      "global_load_lds_dwordx4 v43, s[6:7] offset:0 sc1\n"
      "ds_read_b64_tr_b16 v[124:125], v35 offset:61440\n"
      "v_fma_f32 v172, v172, v23, v244\n"
      "v_fma_f32 v173, v173, v23, v245\n"
      "v_fma_f32 v174, v174, v23, v246\n"
      "v_mfma_f32_16x16x32_bf16 v[204:207], a[96:99], v[76:79], v[204:207]\n"
      "ds_read_b64_tr_b16 v[126:127], v35 offset:63488\n"
      "v_add_u32_dpp v3, v3, v3 quad_perm:[2,3,0,1] row_mask:0xf bank_mask:0xf\n"
      "v_fma_f32 v175, v175, v23, v247\n"
      "v_mul_f32 v172, v172, v174\n"
      "v_mfma_f32_16x16x32_bf16 v[204:207], a[108:111], v[92:95], v[204:207]\n"
      "v_mul_f32 v173, v173, v175\n"
      "v_cvt_pk_bf16_f32 v172, v172, v173\n"
      "v_mfma_f32_16x16x32_bf16 v[212:215], a[100:103], v[76:79], v[212:215]\n"
      "global_load_dwordx4 a[92:95], v44, s[6:7] offset:-1024 sc1\n"
      "ds_write_b32 v46, v172 offset:11264\n"
      "v_add_u32_dpp v3, v3, v3 row_half_mirror row_mask:0xf bank_mask:0xf\n"
      "v_cmp_ne_u32 vcc, s71, v3\n"
      "v_mfma_f32_16x16x32_bf16 v[212:215], a[112:115], v[92:95], v[212:215]\n"
      "v_mfma_f32_16x16x32_bf16 v[220:223], a[104:107], v[76:79], v[220:223]\n"
      "s_cbranch_vccnz L_sx9_%=\n"
      "L_bx9_%=:\n"
      "v_mfma_f32_16x16x32_bf16 v[220:223], a[116:119], v[92:95], v[220:223]\n"
      "s_waitcnt lgkmcnt(0)\n"
      "s_barrier\n"
      "v_mfma_f32_16x16x32_bf16 v[176:179], a[120:123], v[96:99], v[176:179]\n"
      "ds_read_b128 v[160:163], v47 offset:0\n"
      "v_add_u32 v57, s40, v7\n"
      "v_mfma_f32_16x16x32_bf16 v[176:179], a[132:135], v[112:115], v[176:179]\n"
      "ds_read_b128 v[164:167], v47 offset:4096\n"
      "v_mfma_f32_16x16x32_bf16 v[184:187], a[124:127], v[96:99], v[184:187]\n"
      "global_load_dwordx4 a[96:99], v44, s[6:7] offset:0 sc1\n"
      "ds_read_b128 v[168:171], v47 offset:8192\n"
      "ds_read_b64_tr_b16 v[64:65], v6 offset:0\n"
      "v_mfma_f32_16x16x32_bf16 v[184:187], a[136:139], v[112:115], v[184:187]\n"
      "ds_read_b64_tr_b16 v[66:67], v6 offset:2048\n"
      "v_mfma_f32_16x16x32_bf16 v[192:195], a[128:131], v[96:99], v[192:195]\n"
      "global_load_dwordx4 a[100:103], v44, s[6:7] offset:1024 sc1\n"
      "ds_read_b64_tr_b16 v[68:69], v34 offset:0\n"
      "v_mfma_f32_16x16x32_bf16 v[192:195], a[140:143], v[112:115], v[192:195]\n"
      "ds_read_b64_tr_b16 v[70:71], v34 offset:2048\n"
      "s_waitcnt lgkmcnt(4)\n"
      "v_mfma_f32_16x16x32_bf16 v[180:183], a[120:123], v[100:103], v[180:183]\n"
      "ds_read_b64_tr_b16 v[72:73], v6 offset:4096\n"
      "v_mfma_f32_16x16x32_bf16 v[180:183], a[132:135], v[116:119], v[180:183]\n"
      "buffer_store_dwordx4 v[160:163], v7, s[32:35], s61 offen offset:0 sc1\n"
      "ds_read_b64_tr_b16 v[74:75], v6 offset:6144\n"
      "v_add3_u32 v58, v160, v161, s30\n"
      "v_mfma_f32_16x16x32_bf16 v[188:191], a[124:127], v[100:103], v[188:191]\n"
      "global_load_dwordx4 a[104:107], v44, s[6:7] offset:2048 sc1\n"
      "ds_read_b64_tr_b16 v[76:77], v34 offset:4096\n"
      "v_add3_u32 v58, v58, v162, v163\n"
      "v_add3_u32 v58, v58, v164, v165\n"
      "s_add_u32 m0, s41, 0\n"
      "v_add_u32 v43, 50176, v7\n"
      "v_mfma_f32_16x16x32_bf16 v[188:191], a[136:139], v[116:119], v[188:191]\n"
      "buffer_store_dwordx4 v[164:167], v7, s[32:35], s61 offen offset:1024 sc1\n"
      "global_load_lds_dwordx4 v43, s[6:7] offset:0 sc1\n"
      "ds_read_b64_tr_b16 v[78:79], v34 offset:6144\n"
      "v_add3_u32 v58, v58, v166, v167\n"
      "v_mfma_f32_16x16x32_bf16 v[196:199], a[128:131], v[100:103], v[196:199]\n"
      "ds_read_b64_tr_b16 v[80:81], v6 offset:8192\n"
      "v_add3_u32 v58, v58, v168, v169\n"
      "v_add3_u32 v58, v58, v170, v171\n"
      "v_mfma_f32_16x16x32_bf16 v[196:199], a[140:143], v[116:119], v[196:199]\n"
      "buffer_store_dwordx4 v[168:171], v7, s[32:35], s61 offen offset:2048 sc1\n"
      "v_mfma_f32_16x16x32_bf16 v[200:203], a[120:123], v[104:107], v[200:203]\n"
      "global_load_dwordx4 a[108:111], v44, s[6:7] offset:3072 sc1\n"
      "ds_read_b64_tr_b16 v[82:83], v6 offset:10240\n"
      "v_mfma_f32_16x16x32_bf16 v[200:203], a[132:135], v[120:123], v[200:203]\n"
      "buffer_store_dword v58, v42, s[32:35], s62 offen sc1\n"
      "global_load_lds_dwordx4 v43, s[6:7] offset:1024 sc1\n"
      "ds_read_b64_tr_b16 v[84:85], v34 offset:8192\n"
      "v_mfma_f32_16x16x32_bf16 v[208:211], a[124:127], v[104:107], v[208:211]\n"
      "ds_read_b64_tr_b16 v[86:87], v34 offset:10240\n"
      "v_mfma_f32_16x16x32_bf16 v[208:211], a[136:139], v[120:123], v[208:211]\n"
      "ds_read_b64_tr_b16 v[88:89], v6 offset:12288\n"
      "v_add_u32 v44, 36864, v7\n"
      "v_mfma_f32_16x16x32_bf16 v[216:219], a[128:131], v[104:107], v[216:219]\n"
      "global_load_dwordx4 a[112:115], v44, s[6:7] offset:-4096 sc1\n"
      "ds_read_b64_tr_b16 v[90:91], v6 offset:14336\n"
      "v_mfma_f32_16x16x32_bf16 v[216:219], a[140:143], v[120:123], v[216:219]\n"
      "global_load_lds_dwordx4 v43, s[6:7] offset:2048 sc1\n"
      "ds_read_b64_tr_b16 v[92:93], v34 offset:12288\n"
      "v_mfma_f32_16x16x32_bf16 v[204:207], a[120:123], v[108:111], v[204:207]\n"
      "ds_read_b64_tr_b16 v[94:95], v34 offset:14336\n"
      "v_mfma_f32_16x16x32_bf16 v[204:207], a[132:135], v[124:127], v[204:207]\n"
      "ds_read_b64 v[24:25], v41 offset:512\n"
      "v_mfma_f32_16x16x32_bf16 v[212:215], a[124:127], v[108:111], v[212:215]\n"
      "global_load_dwordx4 a[116:119], v44, s[6:7] offset:-3072 sc1\n"
      "v_mfma_f32_16x16x32_bf16 v[212:215], a[136:139], v[124:127], v[212:215]\n"
      "ds_read_b64 v[26:27], v41 offset:768\n"
      "v_mfma_f32_16x16x32_bf16 v[220:223], a[128:131], v[108:111], v[220:223]\n"
      "ds_read_b64 v[28:29], v41 offset:640\n"
      "v_mfma_f32_16x16x32_bf16 v[220:223], a[140:143], v[124:127], v[220:223]\n"
      "ds_read_b64 v[30:31], v41 offset:896\n"
      "s_waitcnt lgkmcnt(4)\n"
      "v_mfma_f32_16x16x32_bf16 v[176:179], a[144:147], v[64:67], v[176:179]\n"
      "ds_read_b64_tr_b16 v[96:97], v6 offset:16384\n"
      "v_mfma_f32_16x16x32_bf16 v[176:179], a[156:159], v[72:75], v[176:179]\n"
      "ds_read_b64_tr_b16 v[98:99], v6 offset:18432\n"
      "v_mfma_f32_16x16x32_bf16 v[184:187], a[148:151], v[64:67], v[184:187]\n"
      "global_load_dwordx4 a[120:123], v44, s[6:7] offset:-2048 sc1\n"
      "v_mfma_f32_16x16x32_bf16 v[184:187], a[160:163], v[72:75], v[184:187]\n"
      "ds_read_b64_tr_b16 v[100:101], v34 offset:16384\n"
      "v_mfma_f32_16x16x32_bf16 v[192:195], a[152:155], v[64:67], v[192:195]\n"
      "global_load_dwordx4 a[124:127], v44, s[6:7] offset:-1024 sc1\n"
      "v_mfma_f32_16x16x32_bf16 v[192:195], a[164:167], v[72:75], v[192:195]\n"
      "ds_read_b64_tr_b16 v[102:103], v34 offset:18432\n"
      "v_mfma_f32_16x16x32_bf16 v[180:183], a[144:147], v[68:71], v[180:183]\n"
      "ds_read_b64_tr_b16 v[104:105], v6 offset:20480\n"
      "v_mfma_f32_16x16x32_bf16 v[180:183], a[156:159], v[76:79], v[180:183]\n"
      "ds_read_b64_tr_b16 v[106:107], v6 offset:22528\n"
      "v_mfma_f32_16x16x32_bf16 v[188:191], a[148:151], v[68:71], v[188:191]\n"
      "global_load_dwordx4 a[128:131], v44, s[6:7] offset:0 sc1\n"
      "ds_read_b64_tr_b16 v[108:109], v34 offset:20480\n"
      "v_mfma_f32_16x16x32_bf16 v[188:191], a[160:163], v[76:79], v[188:191]\n"
      "ds_read_b64_tr_b16 v[110:111], v34 offset:22528\n"
      "v_mfma_f32_16x16x32_bf16 v[196:199], a[152:155], v[68:71], v[196:199]\n"
      "ds_read_b64_tr_b16 v[112:113], v6 offset:24576\n"
      "v_mfma_f32_16x16x32_bf16 v[196:199], a[164:167], v[76:79], v[196:199]\n"
      "ds_read_b64_tr_b16 v[114:115], v6 offset:26624\n"
      "v_mfma_f32_16x16x32_bf16 v[176:179], a[168:171], v[80:83], v[176:179]\n"
      "global_load_dwordx4 a[132:135], v44, s[6:7] offset:1024 sc1\n"
      "v_mfma_f32_16x16x32_bf16 v[176:179], a[180:183], v[88:91], v[176:179]\n"
      "ds_read_b64_tr_b16 v[116:117], v34 offset:24576\n"
      "v_mfma_f32_16x16x32_bf16 v[184:187], a[172:175], v[80:83], v[184:187]\n"
      "ds_read_b64_tr_b16 v[118:119], v34 offset:26624\n"
      "v_mfma_f32_16x16x32_bf16 v[184:187], a[184:187], v[88:91], v[184:187]\n"
      "ds_read_b64_tr_b16 v[120:121], v6 offset:28672\n"
      "v_mfma_f32_16x16x32_bf16 v[192:195], a[176:179], v[80:83], v[192:195]\n"
      "global_load_dwordx4 a[136:139], v44, s[6:7] offset:2048 sc1\n"
      "ds_read_b64_tr_b16 v[122:123], v6 offset:30720\n"
      "v_mfma_f32_16x16x32_bf16 v[192:195], a[188:191], v[88:91], v[192:195]\n"
      "ds_read_b64_tr_b16 v[124:125], v34 offset:28672\n"
      "v_mfma_f32_16x16x32_bf16 v[180:183], a[168:171], v[84:87], v[180:183]\n"
      "ds_read_b64_tr_b16 v[126:127], v34 offset:30720\n"
      "v_mfma_f32_16x16x32_bf16 v[180:183], a[180:183], v[92:95], v[180:183]\n"
      "v_mfma_f32_16x16x32_bf16 v[188:191], a[172:175], v[84:87], v[188:191]\n"
      "global_load_dwordx4 a[140:143], v44, s[6:7] offset:3072 sc1\n"
      "v_mfma_f32_16x16x32_bf16 v[188:191], a[184:187], v[92:95], v[188:191]\n"
      "v_mfma_f32_16x16x32_bf16 v[196:199], a[176:179], v[84:87], v[196:199]\n"
      "v_mfma_f32_16x16x32_bf16 v[196:199], a[188:191], v[92:95], v[196:199]\n"
      "s_waitcnt lgkmcnt(0)\n"
      "v_mfma_f32_16x16x32_bf16 v[176:179], a[192:195], v[96:99], v[176:179]\n"
      "ds_read_b64_tr_b16 v[64:65], v33 offset:0\n"
      "v_mfma_f32_16x16x32_bf16 v[176:179], a[204:207], v[104:107], v[176:179]\n"
      "ds_read_b64_tr_b16 v[66:67], v33 offset:2048\n"
      "v_mfma_f32_16x16x32_bf16 v[184:187], a[196:199], v[96:99], v[184:187]\n"
      "ds_read_b64_tr_b16 v[68:69], v35 offset:0\n"
      "v_mfma_f32_16x16x32_bf16 v[184:187], a[208:211], v[104:107], v[184:187]\n"
      "ds_read_b64_tr_b16 v[70:71], v35 offset:2048\n"
      "v_mfma_f32_16x16x32_bf16 v[192:195], a[200:203], v[96:99], v[192:195]\n"
      "ds_read_b64_tr_b16 v[72:73], v33 offset:4096\n"
      "v_mfma_f32_16x16x32_bf16 v[192:195], a[212:215], v[104:107], v[192:195]\n"
      "ds_read_b64_tr_b16 v[74:75], v33 offset:6144\n"
      "v_mfma_f32_16x16x32_bf16 v[180:183], a[192:195], v[100:103], v[180:183]\n"
      "ds_read_b64_tr_b16 v[76:77], v35 offset:4096\n"
      "v_mfma_f32_16x16x32_bf16 v[180:183], a[204:207], v[108:111], v[180:183]\n"
      "ds_read_b64_tr_b16 v[78:79], v35 offset:6144\n"
      "v_mfma_f32_16x16x32_bf16 v[188:191], a[196:199], v[100:103], v[188:191]\n"
      "ds_read_b64_tr_b16 v[80:81], v33 offset:8192\n"
      "v_mfma_f32_16x16x32_bf16 v[188:191], a[208:211], v[108:111], v[188:191]\n"
      "ds_read_b64_tr_b16 v[82:83], v33 offset:10240\n"
      "v_mfma_f32_16x16x32_bf16 v[196:199], a[200:203], v[100:103], v[196:199]\n"
      "ds_read_b64_tr_b16 v[84:85], v35 offset:8192\n"
      "v_mfma_f32_16x16x32_bf16 v[196:199], a[212:215], v[108:111], v[196:199]\n"
      "ds_read_b64_tr_b16 v[86:87], v35 offset:10240\n"
      "v_mfma_f32_16x16x32_bf16 v[176:179], a[216:219], v[112:115], v[176:179]\n"
      "ds_read_b64_tr_b16 v[88:89], v33 offset:12288\n"
      "v_add_u32 v45, 4096, v7\n"
      "v_mfma_f32_16x16x32_bf16 v[176:179], a[228:231], v[120:123], v[176:179]\n"
      "ds_read_b64_tr_b16 v[90:91], v33 offset:14336\n"
      "v_mfma_f32_16x16x32_bf16 v[184:187], a[220:223], v[112:115], v[184:187]\n"
      "global_load_dwordx4 v[8:11], v45, s[10:11] offset:-4096 sc1\n"
      "v_mfma_f32_16x16x32_bf16 v[184:187], a[232:235], v[120:123], v[184:187]\n"
      "global_load_dwordx4 v[12:15], v45, s[12:13] offset:-4096 sc1\n"
      "global_load_dwordx4 v[16:19], v45, s[10:11] offset:-3072 sc1\n"
      "v_mfma_f32_16x16x32_bf16 v[192:195], a[224:227], v[112:115], v[192:195]\n"
      "global_load_dwordx4 v[20:23], v45, s[12:13] offset:-3072 sc1\n"
      "v_mfma_f32_16x16x32_bf16 v[192:195], a[236:239], v[120:123], v[192:195]\n"
      "ds_read_b64_tr_b16 v[92:93], v35 offset:12288\n"
      "v_mfma_f32_16x16x32_bf16 v[180:183], a[216:219], v[116:119], v[180:183]\n"
      "ds_read_b64_tr_b16 v[94:95], v35 offset:14336\n"
      "v_mfma_f32_16x16x32_bf16 v[180:183], a[228:231], v[124:127], v[180:183]\n"
      "global_load_dwordx4 v[128:131], v45, s[10:11] offset:-2048 sc1\n"
      "v_mfma_f32_16x16x32_bf16 v[188:191], a[220:223], v[116:119], v[188:191]\n"
      "v_mfma_f32_16x16x32_bf16 v[188:191], a[232:235], v[124:127], v[188:191]\n"
      "v_mfma_f32_16x16x32_bf16 v[196:199], a[224:227], v[116:119], v[196:199]\n"
      "global_load_dwordx4 v[132:135], v45, s[12:13] offset:-2048 sc1\n"
      "v_mfma_f32_16x16x32_bf16 v[196:199], a[236:239], v[124:127], v[196:199]\n"
      "buffer_load_dword v36, v42, s[32:35], s63 offen offset:0 sc1\n"
      "buffer_load_dword v37, v42, s[32:35], s63 offen offset:256 sc1\n"
      "s_waitcnt lgkmcnt(0)\n"
      "s_barrier\n"
      "v_mfma_f32_16x16x32_bf16 v[200:203], a[144:147], v[64:67], v[200:203]\n"
      "ds_read_b64_tr_b16 v[96:97], v33 offset:16384\n"
      "v_mfma_f32_16x16x32_bf16 v[200:203], a[156:159], v[72:75], v[200:203]\n"
      "global_load_dwordx4 v[136:139], v45, s[10:11] offset:-1024 sc1\n"
      "v_mfma_f32_16x16x32_bf16 v[208:211], a[148:151], v[64:67], v[208:211]\n"
      "ds_read_b64_tr_b16 v[98:99], v33 offset:18432\n"
      "v_mfma_f32_16x16x32_bf16 v[208:211], a[160:163], v[72:75], v[208:211]\n"
      "ds_read_b64_tr_b16 v[100:101], v35 offset:16384\n"
      "v_fmac_f32 v176, v24, v224\n"
      "v_mfma_f32_16x16x32_bf16 v[216:219], a[152:155], v[64:67], v[216:219]\n"
      "global_load_dwordx4 v[140:143], v45, s[12:13] offset:-1024 sc1\n"
      "v_fmac_f32 v177, v24, v225\n"
      "v_fmac_f32 v178, v24, v226\n"
      "v_mfma_f32_16x16x32_bf16 v[216:219], a[164:167], v[72:75], v[216:219]\n"
      "ds_read_b64_tr_b16 v[102:103], v35 offset:18432\n"
      "v_fmac_f32 v179, v24, v227\n"
      "v_fma_f32 v176, v176, v25, v236\n"
      "v_mfma_f32_16x16x32_bf16 v[204:207], a[144:147], v[68:71], v[204:207]\n"
      "ds_read_b64_tr_b16 v[104:105], v33 offset:20480\n"
      "v_fma_f32 v177, v177, v25, v237\n"
      "v_mfma_f32_16x16x32_bf16 v[204:207], a[156:159], v[76:79], v[204:207]\n"
      "global_load_dwordx4 v[144:147], v45, s[10:11] offset:0 sc1\n"
      "ds_read_b64_tr_b16 v[106:107], v33 offset:22528\n"
      "v_fma_f32 v178, v178, v25, v238\n"
      "v_fma_f32 v179, v179, v25, v239\n"
      "v_mfma_f32_16x16x32_bf16 v[212:215], a[148:151], v[68:71], v[212:215]\n"
      "ds_read_b64_tr_b16 v[108:109], v35 offset:20480\n"
      "v_mul_f32 v176, v176, v178\n"
      "v_mul_f32 v177, v177, v179\n"
      "v_mfma_f32_16x16x32_bf16 v[212:215], a[160:163], v[76:79], v[212:215]\n"
      "ds_read_b64_tr_b16 v[110:111], v35 offset:22528\n"
      "v_cvt_pk_bf16_f32 v176, v176, v177\n"
      "v_mfma_f32_16x16x32_bf16 v[220:223], a[152:155], v[68:71], v[220:223]\n"
      "global_load_dwordx4 v[148:151], v45, s[12:13] offset:0 sc1\n"
      "ds_write_b32 v46, v176 offset:0\n"
      "ds_read_b64_tr_b16 v[112:113], v33 offset:24576\n"
      "v_fmac_f32 v184, v24, v228\n"
      "v_mfma_f32_16x16x32_bf16 v[220:223], a[164:167], v[76:79], v[220:223]\n"
      "ds_read_b64_tr_b16 v[114:115], v33 offset:26624\n"
      "v_fmac_f32 v185, v24, v229\n"
      "v_fmac_f32 v186, v24, v230\n"
      "v_mfma_f32_16x16x32_bf16 v[200:203], a[168:171], v[80:83], v[200:203]\n"
      "ds_read_b64_tr_b16 v[116:117], v35 offset:24576\n"
      "v_fmac_f32 v187, v24, v231\n"
      "v_fma_f32 v184, v184, v25, v240\n"
      "v_mfma_f32_16x16x32_bf16 v[200:203], a[180:183], v[88:91], v[200:203]\n"
      "global_load_dwordx4 v[152:155], v45, s[10:11] offset:1024 sc1\n"
      "v_fma_f32 v185, v185, v25, v241\n"
      "v_mfma_f32_16x16x32_bf16 v[208:211], a[172:175], v[80:83], v[208:211]\n"
      "ds_read_b64_tr_b16 v[118:119], v35 offset:26624\n"
      "v_fma_f32 v186, v186, v25, v242\n"
      "v_fma_f32 v187, v187, v25, v243\n"
      "v_mfma_f32_16x16x32_bf16 v[208:211], a[184:187], v[88:91], v[208:211]\n"
      "ds_read_b64_tr_b16 v[120:121], v33 offset:28672\n"
      "v_mul_f32 v184, v184, v186\n"
      "v_mul_f32 v185, v185, v187\n"
      "v_mfma_f32_16x16x32_bf16 v[216:219], a[176:179], v[80:83], v[216:219]\n"
      "global_load_dwordx4 v[156:159], v45, s[12:13] offset:1024 sc1\n"
      "ds_read_b64_tr_b16 v[122:123], v33 offset:30720\n"
      "v_cvt_pk_bf16_f32 v184, v184, v185\n"
      "v_mfma_f32_16x16x32_bf16 v[216:219], a[188:191], v[88:91], v[216:219]\n"
      "buffer_load_dword v38, v42, s[32:35], s63 offen offset:512 sc1\n"
      "buffer_load_dword v39, v42, s[32:35], s63 offen offset:768 sc1\n"
      "ds_write_b32 v46, v184 offset:4096\n"
      "ds_read_b64_tr_b16 v[124:125], v35 offset:28672\n"
      "v_fmac_f32 v192, v24, v232\n"
      "v_mfma_f32_16x16x32_bf16 v[204:207], a[168:171], v[84:87], v[204:207]\n"
      "ds_read_b64_tr_b16 v[126:127], v35 offset:30720\n"
      "v_fmac_f32 v193, v24, v233\n"
      "v_fmac_f32 v194, v24, v234\n"
      "v_add_u32 v45, 10240, v7\n"
      "v_mfma_f32_16x16x32_bf16 v[204:207], a[180:183], v[92:95], v[204:207]\n"
      "global_load_dwordx4 v[172:175], v45, s[10:11] offset:-4096 sc1\n"
      "v_fmac_f32 v195, v24, v235\n"
      "v_fma_f32 v192, v192, v25, v244\n"
      "v_mfma_f32_16x16x32_bf16 v[212:215], a[172:175], v[84:87], v[212:215]\n"
      "v_fma_f32 v193, v193, v25, v245\n"
      "v_fma_f32 v194, v194, v25, v246\n"
      "v_mfma_f32_16x16x32_bf16 v[212:215], a[184:187], v[92:95], v[212:215]\n"
      "v_fma_f32 v195, v195, v25, v247\n"
      "v_mul_f32 v192, v192, v194\n"
      "v_mfma_f32_16x16x32_bf16 v[220:223], a[176:179], v[84:87], v[220:223]\n"
      "global_load_dwordx4 v[160:163], v45, s[12:13] offset:-4096 sc1\n"
      "v_mul_f32 v193, v193, v195\n"
      "v_mfma_f32_16x16x32_bf16 v[220:223], a[188:191], v[92:95], v[220:223]\n"
      "v_cvt_pk_bf16_f32 v192, v192, v193\n"
      "s_waitcnt lgkmcnt(0)\n"
      "v_mfma_f32_16x16x32_bf16 v[200:203], a[192:195], v[96:99], v[200:203]\n"
      "ds_write_b32 v46, v192 offset:8192\n"
      "v_fmac_f32 v180, v26, v224\n"
      "v_fmac_f32 v181, v26, v225\n"
      "v_mfma_f32_16x16x32_bf16 v[200:203], a[204:207], v[104:107], v[200:203]\n"
      "global_load_dwordx4 v[164:167], v45, s[10:11] offset:-3072 sc1\n"
      "v_fmac_f32 v182, v26, v226\n"
      "v_mfma_f32_16x16x32_bf16 v[208:211], a[196:199], v[96:99], v[208:211]\n"
      "v_fmac_f32 v183, v26, v227\n"
      "v_fma_f32 v180, v180, v27, v236\n"
      "v_mfma_f32_16x16x32_bf16 v[208:211], a[208:211], v[104:107], v[208:211]\n"
      "v_fma_f32 v181, v181, v27, v237\n"
      "v_fma_f32 v182, v182, v27, v238\n"
      "v_mfma_f32_16x16x32_bf16 v[216:219], a[200:203], v[96:99], v[216:219]\n"
      "global_load_dwordx4 v[168:171], v45, s[12:13] offset:-3072 sc1\n"
      "v_fma_f32 v183, v183, v27, v239\n"
      "v_mfma_f32_16x16x32_bf16 v[216:219], a[212:215], v[104:107], v[216:219]\n"
      "v_mul_f32 v180, v180, v182\n"
      "v_mul_f32 v181, v181, v183\n"
      "v_mfma_f32_16x16x32_bf16 v[204:207], a[192:195], v[100:103], v[204:207]\n"
      "v_cvt_pk_bf16_f32 v180, v180, v181\n"
      "s_waitcnt vmcnt(40)\n"
      "v_mfma_f32_16x16x32_bf16 v[204:207], a[204:207], v[108:111], v[204:207]\n"
      "global_load_dwordx4 v[64:67], v45, s[10:11] offset:-2048 sc1\n"
      "ds_write_b32 v46, v180 offset:2048\n"
      "ds_read_b128 a[144:147], v57 offset:0\n"
      "v_fmac_f32 v188, v26, v228\n"
      "v_mfma_f32_16x16x32_bf16 v[212:215], a[196:199], v[100:103], v[212:215]\n"
      "ds_read_b128 a[148:151], v57 offset:1024\n"
      "v_fmac_f32 v189, v26, v229\n"
      "v_fmac_f32 v190, v26, v230\n"
      "v_mfma_f32_16x16x32_bf16 v[212:215], a[208:211], v[108:111], v[212:215]\n"
      "ds_read_b128 a[152:155], v57 offset:2048\n"
      "v_fmac_f32 v191, v26, v231\n"
      "v_fma_f32 v188, v188, v27, v240\n"
      "v_mfma_f32_16x16x32_bf16 v[220:223], a[200:203], v[100:103], v[220:223]\n"
      "global_load_dwordx4 v[68:71], v45, s[12:13] offset:-2048 sc1\n"
      "v_fma_f32 v189, v189, v27, v241\n"
      "v_mfma_f32_16x16x32_bf16 v[220:223], a[212:215], v[108:111], v[220:223]\n"
      "buffer_load_dword v60, v42, s[32:35], s63 offen offset:1024 sc1\n"
      "buffer_load_dword v61, v42, s[32:35], s63 offen offset:1280 sc1\n"
      "v_fma_f32 v190, v190, v27, v242\n"
      "v_fma_f32 v191, v191, v27, v243\n"
      "v_mfma_f32_16x16x32_bf16 v[200:203], a[216:219], v[112:115], v[200:203]\n"
      "ds_read_b128 a[156:159], v57 offset:3072\n"
      "v_mul_f32 v188, v188, v190\n"
      "v_mul_f32 v189, v189, v191\n"
      "v_mfma_f32_16x16x32_bf16 v[200:203], a[228:231], v[120:123], v[200:203]\n"
      "global_load_dwordx4 v[72:75], v45, s[10:11] offset:-1024 sc1\n"
      "v_cvt_pk_bf16_f32 v188, v188, v189\n"
      "v_mfma_f32_16x16x32_bf16 v[208:211], a[220:223], v[112:115], v[208:211]\n"
      "ds_write_b32 v46, v188 offset:6144\n"
      "ds_read_b128 a[160:163], v57 offset:4096\n"
      "v_fmac_f32 v196, v26, v232\n"
      "v_mfma_f32_16x16x32_bf16 v[208:211], a[232:235], v[120:123], v[208:211]\n"
      "ds_read_b128 a[164:167], v57 offset:5120\n"
      "s_waitcnt vmcnt(17)\n"
      "v_fmac_f32 v197, v26, v233\n"
      "v_fmac_f32 v198, v26, v234\n"
      "v_mfma_f32_16x16x32_bf16 v[216:219], a[224:227], v[112:115], v[216:219]\n"
      "global_load_dwordx4 v[76:79], v45, s[12:13] offset:-1024 sc1\n"
      "v_add3_u32 v2, v8, v9, s30\n"
      "v_add3_u32 v2, v2, v10, v11\n"
      "v_add3_u32 v2, v2, v16, v17\n"
      "v_fmac_f32 v199, v26, v235\n"
      "v_fma_f32 v196, v196, v27, v244\n"
      "v_mfma_f32_16x16x32_bf16 v[216:219], a[236:239], v[120:123], v[216:219]\n"
      "ds_read_b128 a[168:171], v57 offset:6144\n"
      "v_add3_u32 v2, v2, v18, v19\n"
      "v_add3_u32 v2, v2, v128, v129\n"
      "v_add3_u32 v2, v2, v130, v131\n"
      "v_fma_f32 v197, v197, v27, v245\n"
      "v_mfma_f32_16x16x32_bf16 v[204:207], a[216:219], v[116:119], v[204:207]\n"
      "ds_read_b128 a[172:175], v57 offset:7168\n"
      "v_xor_b32 v2, v2, v36\n"
      "v_add3_u32 v3, v12, v13, s30\n"
      "v_add3_u32 v3, v3, v14, v15\n"
      "v_fma_f32 v198, v198, v27, v246\n"
      "v_fma_f32 v199, v199, v27, v247\n"
      "v_mfma_f32_16x16x32_bf16 v[204:207], a[228:231], v[124:127], v[204:207]\n"
      "global_load_dwordx4 v[80:83], v45, s[10:11] offset:0 sc1\n"
      "v_add3_u32 v3, v3, v20, v21\n"
      "v_add3_u32 v3, v3, v22, v23\n"
      "v_add3_u32 v3, v3, v132, v133\n"
      "v_mul_f32 v196, v196, v198\n"
      "v_mul_f32 v197, v197, v199\n"
      "v_mfma_f32_16x16x32_bf16 v[212:215], a[220:223], v[116:119], v[212:215]\n"
      "ds_read_b128 a[176:179], v57 offset:8192\n"
      "v_add3_u32 v3, v3, v134, v135\n"
      "v_xor_b32 v3, v3, v37\n"
      "v_or_b32 v2, v2, v3\n"
      "v_cvt_pk_bf16_f32 v196, v196, v197\n"
      "v_mfma_f32_16x16x32_bf16 v[212:215], a[232:235], v[124:127], v[212:215]\n"
      "ds_write_b32 v46, v196 offset:10240\n"
      "v_cmp_ne_u32 vcc, 0, v2\n"
      "v_mfma_f32_16x16x32_bf16 v[220:223], a[224:227], v[116:119], v[220:223]\n"
      "global_load_dwordx4 v[84:87], v45, s[12:13] offset:0 sc1\n"
      "v_mfma_f32_16x16x32_bf16 v[220:223], a[236:239], v[124:127], v[220:223]\n"
      "s_cbranch_vccnz L_shA0_%=\n"
      "L_bhA0_%=:\n"
      "s_waitcnt lgkmcnt(0)\n"
      "s_barrier\n"
      "v_mfma_f32_16x16x32_bf16 v[96:99], a[240:243], v[8:11], 0\n"
      "ds_read_b128 v[176:179], v47 offset:0\n"
      "v_mfma_f32_16x16x32_bf16 v[96:99], a[4:7], v[16:19], v[96:99]\n"
      "global_load_dwordx4 v[88:91], v45, s[10:11] offset:1024 sc1\n"
      "ds_read_b128 v[180:183], v47 offset:4096\n"
      "v_fmac_f32 v200, v28, v224\n"
      "v_mfma_f32_16x16x32_bf16 v[96:99], a[24:27], v[128:131], v[96:99]\n"
      "ds_read_b128 v[184:187], v47 offset:8192\n"
      "v_fmac_f32 v201, v28, v225\n"
      "v_fmac_f32 v202, v28, v226\n"
      "v_fmac_f32 v203, v28, v227\n"
      "v_fma_f32 v200, v200, v29, v236\n"
      "v_fma_f32 v201, v201, v29, v237\n"
      "v_fma_f32 v202, v202, v29, v238\n"
      "v_fma_f32 v203, v203, v29, v239\n"
      "v_mul_f32 v200, v200, v202\n"
      "v_mul_f32 v201, v201, v203\n"
      "v_cvt_pk_bf16_f32 v200, v200, v201\n"
      "v_mfma_f32_16x16x32_bf16 v[104:107], a[244:247], v[8:11], 0\n"
      "ds_write_b32 v46, v200 offset:1024\n"
      "v_add_u32 v43, s59, v7\n"
      "v_add_u32 v59, s59, v42\n"
      "v_fmac_f32 v208, v28, v228\n"
      "v_fmac_f32 v209, v28, v229\n"
      "v_fmac_f32 v210, v28, v230\n"
      "v_fmac_f32 v211, v28, v231\n"
      "v_fma_f32 v208, v208, v29, v240\n"
      "v_fma_f32 v209, v209, v29, v241\n"
      "v_fma_f32 v210, v210, v29, v242\n"
      "v_fma_f32 v211, v211, v29, v243\n"
      "v_mul_f32 v208, v208, v210\n"
      "v_mul_f32 v209, v209, v211\n"
      "v_cvt_pk_bf16_f32 v208, v208, v209\n"
      "v_mfma_f32_16x16x32_bf16 v[104:107], a[8:11], v[16:19], v[104:107]\n"
      "global_load_dwordx4 v[92:95], v45, s[12:13] offset:1024 sc1\n"
      "ds_write_b32 v46, v208 offset:5120\n"
      "v_fmac_f32 v216, v28, v232\n"
      "v_fmac_f32 v217, v28, v233\n"
      "v_mfma_f32_16x16x32_bf16 v[104:107], a[28:31], v[128:131], v[104:107]\n"
      "buffer_load_dword v62, v42, s[32:35], s63 offen offset:1536 sc1\n"
      "buffer_load_dword v63, v42, s[32:35], s63 offen offset:1792 sc1\n"
      "v_fmac_f32 v218, v28, v234\n"
      "v_fmac_f32 v219, v28, v235\n"
      "v_fma_f32 v216, v216, v29, v244\n"
      "v_fma_f32 v217, v217, v29, v245\n"
      "v_fma_f32 v218, v218, v29, v246\n"
      "v_fma_f32 v219, v219, v29, v247\n"
      "v_mul_f32 v216, v216, v218\n"
      "v_mul_f32 v217, v217, v219\n"
      "v_cvt_pk_bf16_f32 v216, v216, v217\n"
      "v_mfma_f32_16x16x32_bf16 v[112:115], a[248:251], v[8:11], 0\n"
      "ds_write_b32 v46, v216 offset:9216\n"
      "s_waitcnt lgkmcnt(3)\n"
      "v_fmac_f32 v204, v30, v224\n"
      "v_fmac_f32 v205, v30, v225\n"
      "v_fmac_f32 v206, v30, v226\n"
      "v_fmac_f32 v207, v30, v227\n"
      "v_fma_f32 v204, v204, v31, v236\n"
      "v_fma_f32 v205, v205, v31, v237\n"
      "v_fma_f32 v206, v206, v31, v238\n"
      "v_fma_f32 v207, v207, v31, v239\n"
      "v_mul_f32 v204, v204, v206\n"
      "v_mul_f32 v205, v205, v207\n"
      "v_cvt_pk_bf16_f32 v204, v204, v205\n"
      "v_mfma_f32_16x16x32_bf16 v[112:115], a[12:15], v[16:19], v[112:115]\n"
      "buffer_store_dwordx4 v[176:179], v43, s[32:35], s69 offen offset:0 sc1\n"
      "ds_write_b32 v46, v204 offset:3072\n"
      "v_add3_u32 v58, v176, v177, s30\n"
      "v_add3_u32 v58, v58, v178, v179\n"
      "v_mfma_f32_16x16x32_bf16 v[112:115], a[32:35], v[128:131], v[112:115]\n"
      "v_add3_u32 v58, v58, v180, v181\n"
      "v_fmac_f32 v212, v30, v228\n"
      "v_fmac_f32 v213, v30, v229\n"
      "v_fmac_f32 v214, v30, v230\n"
      "v_fmac_f32 v215, v30, v231\n"
      "v_fma_f32 v212, v212, v31, v240\n"
      "v_fma_f32 v213, v213, v31, v241\n"
      "v_fma_f32 v214, v214, v31, v242\n"
      "v_fma_f32 v215, v215, v31, v243\n"
      "v_mul_f32 v212, v212, v214\n"
      "v_mul_f32 v213, v213, v215\n"
      "v_cvt_pk_bf16_f32 v212, v212, v213\n"
      "v_mfma_f32_16x16x32_bf16 v[120:123], a[252:255], v[8:11], 0\n"
      "buffer_store_dwordx4 v[180:183], v43, s[32:35], s69 offen offset:1024 sc1\n"
      "ds_write_b32 v46, v212 offset:7168\n"
      "v_add3_u32 v58, v58, v182, v183\n"
      "v_fmac_f32 v220, v30, v232\n"
      "v_fmac_f32 v221, v30, v233\n"
      "v_fmac_f32 v222, v30, v234\n"
      "v_fmac_f32 v223, v30, v235\n"
      "v_mfma_f32_16x16x32_bf16 v[120:123], a[16:19], v[16:19], v[120:123]\n"
      "v_add3_u32 v58, v58, v184, v185\n"
      "v_add3_u32 v58, v58, v186, v187\n"
      "v_fma_f32 v220, v220, v31, v244\n"
      "v_fma_f32 v221, v221, v31, v245\n"
      "L_pg6_%=:\n"
      "s_cbranch_execz L_pgr_%=\n"
      "v_fma_f32 v222, v222, v31, v246\n"
      "v_fma_f32 v223, v223, v31, v247\n"
      "v_mul_f32 v220, v220, v222\n"
      "v_mul_f32 v221, v221, v223\n"
      "v_cvt_pk_bf16_f32 v220, v220, v221\n"
      "v_mfma_f32_16x16x32_bf16 v[120:123], a[36:39], v[128:131], v[120:123]\n"
      "buffer_store_dwordx4 v[184:187], v43, s[32:35], s69 offen offset:2048 sc1\n"
      "ds_write_b32 v46, v220 offset:11264\n"
      "v_add_u32 v57, s41, v7\n"
      "v_mfma_f32_16x16x32_bf16 v[248:251], a[0:3], v[8:11], 0\n"
      "ds_read_b128 a[180:183], v57 offset:0\n"
      "v_mfma_f32_16x16x32_bf16 v[248:251], a[20:23], v[16:19], v[248:251]\n"
      "buffer_store_dword v58, v59, s[32:35], s70 offen sc1\n"
      "v_mfma_f32_16x16x32_bf16 v[248:251], a[40:43], v[128:131], v[248:251]\n"
      "ds_read_b128 a[184:187], v57 offset:1024\n"
      "s_waitcnt lgkmcnt(2)\n"
      "s_barrier\n"
      "v_mfma_f32_16x16x32_bf16 v[100:103], a[240:243], v[12:15], 0\n"
      "ds_read_b128 v[200:203], v47 offset:0\n"
      "v_mfma_f32_16x16x32_bf16 v[100:103], a[4:7], v[20:23], v[100:103]\n"
      "ds_read_b128 v[204:207], v47 offset:4096\n"
      "v_mfma_f32_16x16x32_bf16 v[100:103], a[24:27], v[132:135], v[100:103]\n"
      "ds_read_b128 v[208:211], v47 offset:8192\n"
      "v_xor_b32 v43, 0x80000000, v43\n"
      "v_mfma_f32_16x16x32_bf16 v[108:111], a[244:247], v[12:15], 0\n"
      "ds_read_b128 a[188:191], v57 offset:2048\n"
      "v_xor_b32 v59, 0x80000000, v59\n"
      "v_mfma_f32_16x16x32_bf16 v[108:111], a[8:11], v[20:23], v[108:111]\n"
      "v_mfma_f32_16x16x32_bf16 v[108:111], a[28:31], v[132:135], v[108:111]\n"
      "v_mfma_f32_16x16x32_bf16 v[116:119], a[248:251], v[12:15], 0\n"
      "s_waitcnt vmcnt(20)\n"
      "s_waitcnt lgkmcnt(1)\n"
      "v_mfma_f32_16x16x32_bf16 v[116:119], a[12:15], v[20:23], v[116:119]\n"
      "buffer_store_dwordx4 v[200:203], v43, s[32:35], s69 offen offset:0 sc1\n"
      "v_add3_u32 v2, v136, v137, s30\n"
      "v_add3_u32 v2, v2, v138, v139\n"
      "v_add3_u32 v2, v2, v144, v145\n"
      "v_add3_u32 v58, v200, v201, s30\n"
      "v_mfma_f32_16x16x32_bf16 v[116:119], a[32:35], v[132:135], v[116:119]\n"
      "v_add3_u32 v2, v2, v146, v147\n"
      "v_add3_u32 v2, v2, v152, v153\n"
      "v_add3_u32 v2, v2, v154, v155\n"
      "v_add3_u32 v58, v58, v202, v203\n"
      "v_add3_u32 v58, v58, v204, v205\n"
      "v_mfma_f32_16x16x32_bf16 v[124:127], a[252:255], v[12:15], 0\n"
      "buffer_store_dwordx4 v[204:207], v43, s[32:35], s69 offen offset:1024 sc1\n"
      "v_xor_b32 v2, v2, v38\n"
      "v_add3_u32 v3, v140, v141, s30\n"
      "v_add3_u32 v3, v3, v142, v143\n"
      "v_add3_u32 v58, v58, v206, v207\n"
      "v_mfma_f32_16x16x32_bf16 v[124:127], a[16:19], v[20:23], v[124:127]\n"
      "v_add3_u32 v3, v3, v148, v149\n"
      "v_add3_u32 v3, v3, v150, v151\n"
      "v_add3_u32 v3, v3, v156, v157\n"
      "v_add3_u32 v58, v58, v208, v209\n"
      "v_add3_u32 v58, v58, v210, v211\n"
      "v_mfma_f32_16x16x32_bf16 v[124:127], a[36:39], v[132:135], v[124:127]\n"
      "buffer_store_dwordx4 v[208:211], v43, s[32:35], s69 offen offset:2048 sc1\n"
      "v_add3_u32 v3, v3, v158, v159\n"
      "v_xor_b32 v3, v3, v39\n"
      "v_or_b32 v2, v2, v3\n"
      "v_mfma_f32_16x16x32_bf16 v[252:255], a[0:3], v[12:15], 0\n"
      "v_cmp_ne_u32 vcc, 0, v2\n"
      "v_mfma_f32_16x16x32_bf16 v[252:255], a[20:23], v[20:23], v[252:255]\n"
      "buffer_store_dword v58, v59, s[32:35], s70 offen sc1\n"
      "v_mfma_f32_16x16x32_bf16 v[252:255], a[40:43], v[132:135], v[252:255]\n"
      "s_cbranch_vccnz L_shA1_%=\n"
      "L_bhA1_%=:\n"
      "v_mfma_f32_16x16x32_bf16 v[96:99], a[44:47], v[136:139], v[96:99]\n"
      "s_add_u32 s43, s73, s97\n"
      "v_add_u32 v56, s43, v7\n"
      "v_mfma_f32_16x16x32_bf16 v[96:99], a[64:67], v[144:147], v[96:99]\n"
      "s_and_b32 s52, s60, 1\n"
      "s_lshr_b32 s53, s60, 1\n"
      "v_mfma_f32_16x16x32_bf16 v[96:99], a[84:87], v[152:155], v[96:99]\n"
      "v_add_u32 v44, 53248, v7\n"
      "s_mul_i32 s54, s52, 20480\n"
      "v_mfma_f32_16x16x32_bf16 v[104:107], a[48:51], v[136:139], v[104:107]\n"
      "s_lshl_b32 s53, s53, 11\n"
      "s_add_u32 s54, s54, s53\n"
      "v_mfma_f32_16x16x32_bf16 v[104:107], a[68:71], v[144:147], v[104:107]\n"
      "global_load_dwordx4 a[192:195], v44, s[6:7] offset:0 sc1\n"
      "v_add_u32 v49, s54, v7\n"
      "v_mfma_f32_16x16x32_bf16 v[104:107], a[88:91], v[152:155], v[104:107]\n"
      "s_lshl_b32 s52, s52, 5\n"
      "v_xor_b32 v53, s52, v6\n"
      "v_mfma_f32_16x16x32_bf16 v[112:115], a[52:55], v[136:139], v[112:115]\n"
      "s_lshl_b32 s52, s53, 1\n"
      "s_add_u32 s52, s52, 110592\n"
      "v_mfma_f32_16x16x32_bf16 v[112:115], a[72:75], v[144:147], v[112:115]\n"
      "global_load_dwordx4 a[196:199], v44, s[6:7] offset:1024 sc1\n"
      "v_add_u32 v53, s52, v53\n"
      "v_mfma_f32_16x16x32_bf16 v[112:115], a[92:95], v[152:155], v[112:115]\n"
      "s_lshr_b32 s53, s53, 4\n"
      "s_add_u32 s53, s53, 163328\n"
      "v_mfma_f32_16x16x32_bf16 v[120:123], a[56:59], v[136:139], v[120:123]\n"
      "v_lshrrev_b32 v48, 4, v7\n"
      "v_and_b32 v50, 0x30, v48\n"
      "v_mfma_f32_16x16x32_bf16 v[120:123], a[76:79], v[144:147], v[120:123]\n"
      "global_load_dwordx4 a[200:203], v44, s[6:7] offset:2048 sc1\n"
      "v_add_u32 v54, s53, v50\n"
      "v_mfma_f32_16x16x32_bf16 v[120:123], a[96:99], v[152:155], v[120:123]\n"
      "v_and_b32 v48, 15, v48\n"
      "v_lshrrev_b32 v50, 1, v50\n"
      "v_mfma_f32_16x16x32_bf16 v[248:251], a[60:63], v[136:139], v[248:251]\n"
      "v_mul_u32_u24 v48, 1280, v48\n"
      "v_add3_u32 v55, v48, v50, s74\n"
      "v_mfma_f32_16x16x32_bf16 v[248:251], a[80:83], v[144:147], v[248:251]\n"
      "global_load_dwordx4 a[204:207], v44, s[6:7] offset:3072 sc1\n"
      "v_mfma_f32_16x16x32_bf16 v[248:251], a[100:103], v[152:155], v[248:251]\n"
      "v_mfma_f32_16x16x32_bf16 v[100:103], a[44:47], v[140:143], v[100:103]\n"
      "v_add_u32 v44, 61440, v7\n"
      "v_mfma_f32_16x16x32_bf16 v[100:103], a[64:67], v[148:151], v[100:103]\n"
      "global_load_dwordx4 a[208:211], v44, s[6:7] offset:-4096 sc1\n"
      "v_mfma_f32_16x16x32_bf16 v[100:103], a[84:87], v[156:159], v[100:103]\n"
      "v_mfma_f32_16x16x32_bf16 v[108:111], a[48:51], v[140:143], v[108:111]\n"
      "v_mfma_f32_16x16x32_bf16 v[108:111], a[68:71], v[148:151], v[108:111]\n"
      "global_load_dwordx4 a[212:215], v44, s[6:7] offset:-3072 sc1\n"
      "v_mfma_f32_16x16x32_bf16 v[108:111], a[88:91], v[156:159], v[108:111]\n"
      "v_mfma_f32_16x16x32_bf16 v[116:119], a[52:55], v[140:143], v[116:119]\n"
      "s_waitcnt vmcnt(22)\n"
      "s_waitcnt lgkmcnt(0)\n"
      "v_mfma_f32_16x16x32_bf16 v[116:119], a[72:75], v[148:151], v[116:119]\n"
      "global_load_dwordx4 a[216:219], v44, s[6:7] offset:-2048 sc1\n"
      "v_add3_u32 v2, v172, v173, s30\n"
      "v_add3_u32 v2, v2, v174, v175\n"
      "v_add3_u32 v2, v2, v164, v165\n"
      "v_mfma_f32_16x16x32_bf16 v[116:119], a[92:95], v[156:159], v[116:119]\n"
      "v_add3_u32 v2, v2, v166, v167\n"
      "v_add3_u32 v2, v2, v64, v65\n"
      "v_add3_u32 v2, v2, v66, v67\n"
      "v_mfma_f32_16x16x32_bf16 v[124:127], a[56:59], v[140:143], v[124:127]\n"
      "v_xor_b32 v2, v2, v60\n"
      "v_add3_u32 v3, v160, v161, s30\n"
      "v_add3_u32 v3, v3, v162, v163\n"
      "v_mfma_f32_16x16x32_bf16 v[124:127], a[76:79], v[148:151], v[124:127]\n"
      "global_load_dwordx4 a[220:223], v44, s[6:7] offset:-1024 sc1\n"
      "v_add3_u32 v3, v3, v168, v169\n"
      "v_add3_u32 v3, v3, v170, v171\n"
      "v_add3_u32 v3, v3, v68, v69\n"
      "v_mfma_f32_16x16x32_bf16 v[124:127], a[96:99], v[156:159], v[124:127]\n"
      "v_add3_u32 v3, v3, v70, v71\n"
      "v_xor_b32 v3, v3, v61\n"
      "v_or_b32 v2, v2, v3\n"
      "v_mfma_f32_16x16x32_bf16 v[252:255], a[60:63], v[140:143], v[252:255]\n"
      "v_cmp_ne_u32 vcc, 0, v2\n"
      "v_mfma_f32_16x16x32_bf16 v[252:255], a[80:83], v[148:151], v[252:255]\n"
      "v_mfma_f32_16x16x32_bf16 v[252:255], a[100:103], v[156:159], v[252:255]\n"
      "s_cbranch_vccnz L_shA2_%=\n"
      "L_bhA2_%=:\n"
      "v_mfma_f32_16x16x32_bf16 v[96:99], a[104:107], v[172:175], v[96:99]\n"
      "v_mfma_f32_16x16x32_bf16 v[96:99], a[124:127], v[164:167], v[96:99]\n"
      "v_add_u32 v45, 102400, v7\n"
      "v_mfma_f32_16x16x32_bf16 v[96:99], a[144:147], v[64:67], v[96:99]\n"
      "v_mfma_f32_16x16x32_bf16 v[104:107], a[108:111], v[172:175], v[104:107]\n"
      "global_load_dwordx4 v[224:227], v45, s[10:11] offset:-4096 sc1\n"
      "v_mfma_f32_16x16x32_bf16 v[104:107], a[128:131], v[164:167], v[104:107]\n"
      "v_mfma_f32_16x16x32_bf16 v[104:107], a[148:151], v[64:67], v[104:107]\n"
      "v_mfma_f32_16x16x32_bf16 v[112:115], a[112:115], v[172:175], v[112:115]\n"
      "v_mfma_f32_16x16x32_bf16 v[112:115], a[132:135], v[164:167], v[112:115]\n"
      "global_load_dwordx4 v[228:231], v45, s[10:11] offset:-3072 sc1\n"
      "v_mfma_f32_16x16x32_bf16 v[112:115], a[152:155], v[64:67], v[112:115]\n"
      "v_mfma_f32_16x16x32_bf16 v[120:123], a[116:119], v[172:175], v[120:123]\n"
      "v_mfma_f32_16x16x32_bf16 v[120:123], a[136:139], v[164:167], v[120:123]\n"
      "global_load_dwordx4 v[232:235], v45, s[10:11] offset:-2048 sc1\n"
      "v_mfma_f32_16x16x32_bf16 v[120:123], a[156:159], v[64:67], v[120:123]\n"
      "v_mfma_f32_16x16x32_bf16 v[248:251], a[120:123], v[172:175], v[248:251]\n"
      "v_mfma_f32_16x16x32_bf16 v[248:251], a[140:143], v[164:167], v[248:251]\n"
      "global_load_dwordx4 v[236:239], v45, s[10:11] offset:-1024 sc1\n"
      "v_mfma_f32_16x16x32_bf16 v[248:251], a[160:163], v[64:67], v[248:251]\n"
      "v_mfma_f32_16x16x32_bf16 v[100:103], a[104:107], v[160:163], v[100:103]\n"
      "v_mfma_f32_16x16x32_bf16 v[100:103], a[124:127], v[168:171], v[100:103]\n"
      "global_load_dwordx4 v[240:243], v45, s[10:11] offset:0 sc1\n"
      "v_mfma_f32_16x16x32_bf16 v[100:103], a[144:147], v[68:71], v[100:103]\n"
      "v_mfma_f32_16x16x32_bf16 v[108:111], a[108:111], v[160:163], v[108:111]\n"
      "v_mfma_f32_16x16x32_bf16 v[108:111], a[128:131], v[168:171], v[108:111]\n"
      "global_load_dwordx4 v[244:247], v45, s[10:11] offset:1024 sc1\n"
      "v_mfma_f32_16x16x32_bf16 v[108:111], a[148:151], v[68:71], v[108:111]\n"
      "buffer_load_dword v32, v42, s[32:35], s63 offen offset:2048 sc1\n"
      "buffer_load_dword v33, v42, s[32:35], s63 offen offset:2304 sc1\n"
      "v_mfma_f32_16x16x32_bf16 v[116:119], a[112:115], v[160:163], v[116:119]\n"
      "s_waitcnt vmcnt(8)\n"
      "v_add3_u32 v2, v72, v73, s30\n"
      "v_mfma_f32_16x16x32_bf16 v[116:119], a[132:135], v[168:171], v[116:119]\n"
      "v_add3_u32 v2, v2, v74, v75\n"
      "v_add3_u32 v2, v2, v80, v81\n"
      "v_mfma_f32_16x16x32_bf16 v[116:119], a[152:155], v[68:71], v[116:119]\n"
      "v_add_u32 v45, 108544, v7\n"
      "v_add3_u32 v2, v2, v82, v83\n"
      "v_add3_u32 v2, v2, v88, v89\n"
      "v_add3_u32 v2, v2, v90, v91\n"
      "v_mfma_f32_16x16x32_bf16 v[124:127], a[116:119], v[160:163], v[124:127]\n"
      "v_xor_b32 v2, v2, v62\n"
      "v_add3_u32 v3, v76, v77, s30\n"
      "v_add3_u32 v3, v3, v78, v79\n"
      "v_mfma_f32_16x16x32_bf16 v[124:127], a[136:139], v[168:171], v[124:127]\n"
      "global_load_dwordx4 v[8:11], v45, s[10:11] offset:-4096 sc1\n"
      "v_add3_u32 v3, v3, v84, v85\n"
      "v_add3_u32 v3, v3, v86, v87\n"
      "v_add3_u32 v3, v3, v92, v93\n"
      "v_mfma_f32_16x16x32_bf16 v[124:127], a[156:159], v[68:71], v[124:127]\n"
      "v_add3_u32 v3, v3, v94, v95\n"
      "v_xor_b32 v3, v3, v63\n"
      "v_or_b32 v2, v2, v3\n"
      "v_mfma_f32_16x16x32_bf16 v[252:255], a[120:123], v[160:163], v[252:255]\n"
      "v_cmp_ne_u32 vcc, 0, v2\n"
      "v_mfma_f32_16x16x32_bf16 v[252:255], a[140:143], v[168:171], v[252:255]\n"
      "v_mfma_f32_16x16x32_bf16 v[252:255], a[160:163], v[68:71], v[252:255]\n"
      "global_load_dwordx4 v[12:15], v45, s[10:11] offset:-3072 sc1\n"
      "s_cbranch_vccnz L_shA3_%=\n"
      "L_bhA3_%=:\n"
      "v_mfma_f32_16x16x32_bf16 v[96:99], a[164:167], v[72:75], v[96:99]\n"
      "v_mfma_f32_16x16x32_bf16 v[96:99], a[184:187], v[80:83], v[96:99]\n"
      "v_mfma_f32_16x16x32_bf16 v[96:99], a[204:207], v[88:91], v[96:99]\n"
      "global_load_dwordx4 v[16:19], v45, s[10:11] offset:-2048 sc1\n"
      "v_mfma_f32_16x16x32_bf16 v[104:107], a[168:171], v[72:75], v[104:107]\n"
      "v_mfma_f32_16x16x32_bf16 v[104:107], a[188:191], v[80:83], v[104:107]\n"
      "v_mfma_f32_16x16x32_bf16 v[104:107], a[208:211], v[88:91], v[104:107]\n"
      "global_load_dwordx4 v[20:23], v45, s[10:11] offset:-1024 sc1\n"
      "v_mfma_f32_16x16x32_bf16 v[112:115], a[172:175], v[72:75], v[112:115]\n"
      "v_mfma_f32_16x16x32_bf16 v[112:115], a[192:195], v[80:83], v[112:115]\n"
      "v_mfma_f32_16x16x32_bf16 v[112:115], a[212:215], v[88:91], v[112:115]\n"
      "global_load_dwordx4 v[128:131], v45, s[10:11] offset:0 sc1\n"
      "v_mfma_f32_16x16x32_bf16 v[120:123], a[176:179], v[72:75], v[120:123]\n"
      "v_mfma_f32_16x16x32_bf16 v[120:123], a[196:199], v[80:83], v[120:123]\n"
      "v_mfma_f32_16x16x32_bf16 v[120:123], a[216:219], v[88:91], v[120:123]\n"
      "global_load_dwordx4 v[132:135], v45, s[10:11] offset:1024 sc1\n"
      "v_mfma_f32_16x16x32_bf16 v[248:251], a[180:183], v[72:75], v[248:251]\n"
      "buffer_load_dword v34, v42, s[32:35], s63 offen offset:2560 sc1\n"
      "buffer_load_dword v35, v42, s[32:35], s63 offen offset:2816 sc1\n"
      "v_mfma_f32_16x16x32_bf16 v[248:251], a[200:203], v[80:83], v[248:251]\n"
      "v_mfma_f32_16x16x32_bf16 v[248:251], a[220:223], v[88:91], v[248:251]\n"
      "v_mfma_f32_16x16x32_bf16 v[100:103], a[164:167], v[76:79], v[100:103]\n"
      "v_add_u32 v45, 102400, v7\n"
      "v_mfma_f32_16x16x32_bf16 v[100:103], a[184:187], v[84:87], v[100:103]\n"
      "v_mfma_f32_16x16x32_bf16 v[100:103], a[204:207], v[92:95], v[100:103]\n"
      "global_load_dwordx4 v[136:139], v45, s[12:13] offset:-4096 sc1\n"
      "v_mfma_f32_16x16x32_bf16 v[108:111], a[168:171], v[76:79], v[108:111]\n"
      "v_mfma_f32_16x16x32_bf16 v[108:111], a[188:191], v[84:87], v[108:111]\n"
      "v_mfma_f32_16x16x32_bf16 v[108:111], a[208:211], v[92:95], v[108:111]\n"
      "v_mfma_f32_16x16x32_bf16 v[116:119], a[172:175], v[76:79], v[116:119]\n"
      "global_load_dwordx4 v[140:143], v45, s[12:13] offset:-3072 sc1\n"
      "s_waitcnt vmcnt(10)\n"
      "v_mfma_f32_16x16x32_bf16 v[116:119], a[192:195], v[84:87], v[116:119]\n"
      "v_add3_u32 v2, v224, v225, s30\n"
      "v_add3_u32 v2, v2, v226, v227\n"
      "v_add3_u32 v2, v2, v228, v229\n"
      "v_mfma_f32_16x16x32_bf16 v[116:119], a[212:215], v[92:95], v[116:119]\n"
      "v_add3_u32 v2, v2, v230, v231\n"
      "v_add3_u32 v2, v2, v232, v233\n"
      "v_add3_u32 v2, v2, v234, v235\n"
      "v_mfma_f32_16x16x32_bf16 v[124:127], a[176:179], v[76:79], v[124:127]\n"
      "v_xor_b32 v2, v2, v32\n"
      "v_add3_u32 v3, v236, v237, s30\n"
      "v_add3_u32 v3, v3, v238, v239\n"
      "v_mfma_f32_16x16x32_bf16 v[124:127], a[196:199], v[84:87], v[124:127]\n"
      "global_load_dwordx4 v[144:147], v45, s[12:13] offset:-2048 sc1\n"
      "v_add3_u32 v3, v3, v240, v241\n"
      "v_add3_u32 v3, v3, v242, v243\n"
      "v_add3_u32 v3, v3, v244, v245\n"
      "v_mfma_f32_16x16x32_bf16 v[124:127], a[216:219], v[92:95], v[124:127]\n"
      "v_add3_u32 v3, v3, v246, v247\n"
      "v_xor_b32 v3, v3, v33\n"
      "v_or_b32 v2, v2, v3\n"
      "v_mfma_f32_16x16x32_bf16 v[252:255], a[180:183], v[76:79], v[252:255]\n"
      "v_cmp_ne_u32 vcc, 0, v2\n"
      "v_mfma_f32_16x16x32_bf16 v[252:255], a[200:203], v[84:87], v[252:255]\n"
      "v_mfma_f32_16x16x32_bf16 v[252:255], a[220:223], v[92:95], v[252:255]\n"
      "global_load_dwordx4 v[148:151], v45, s[12:13] offset:-1024 sc1\n"
      "s_cbranch_vccnz L_shB0_%=\n"
      "L_bhB0_%=:\n"
      "v_mfma_f32_16x16x32_bf16 v[176:179], a[240:243], v[224:227], 0\n"
      "ds_read_b128 v[80:83], v54 offset:0\n"
      "v_mfma_f32_16x16x32_bf16 v[176:179], a[4:7], v[228:231], v[176:179]\n"
      "ds_read_b128 v[84:87], v54 offset:64\n"
      "v_mfma_f32_16x16x32_bf16 v[176:179], a[24:27], v[232:235], v[176:179]\n"
      "ds_read_b128 v[88:91], v54 offset:128\n"
      "v_mfma_f32_16x16x32_bf16 v[180:183], a[244:247], v[224:227], 0\n"
      "global_load_dwordx4 v[152:155], v45, s[12:13] offset:0 sc1\n"
      "v_mfma_f32_16x16x32_bf16 v[180:183], a[8:11], v[228:231], v[180:183]\n"
      "ds_write_b128 v56, v[96:99] offset:0\n"
      "v_mfma_f32_16x16x32_bf16 v[180:183], a[28:31], v[232:235], v[180:183]\n"
      "v_mfma_f32_16x16x32_bf16 v[184:187], a[248:251], v[224:227], 0\n"
      "v_mfma_f32_16x16x32_bf16 v[184:187], a[12:15], v[228:231], v[184:187]\n"
      "global_load_dwordx4 v[156:159], v45, s[12:13] offset:1024 sc1\n"
      "v_mfma_f32_16x16x32_bf16 v[184:187], a[32:35], v[232:235], v[184:187]\n"
      "ds_write_b128 v56, v[100:103] offset:20480\n"
      "v_mfma_f32_16x16x32_bf16 v[188:191], a[252:255], v[224:227], 0\n"
      "v_mfma_f32_16x16x32_bf16 v[188:191], a[16:19], v[228:231], v[188:191]\n"
      "v_mfma_f32_16x16x32_bf16 v[188:191], a[36:39], v[232:235], v[188:191]\n"
      "buffer_load_dword v24, v42, s[32:35], s63 offen offset:3072 sc1\n"
      "v_mfma_f32_16x16x32_bf16 v[192:195], a[0:3], v[224:227], 0\n"
      "buffer_load_dword v25, v42, s[32:35], s63 offen offset:3328 sc1\n"
      "v_add_u32 v45, 108544, v7\n"
      "v_mfma_f32_16x16x32_bf16 v[192:195], a[20:23], v[228:231], v[192:195]\n"
      "global_load_dwordx4 v[172:175], v45, s[12:13] offset:-4096 sc1\n"
      "ds_write_b128 v56, v[104:107] offset:1024\n"
      "v_mfma_f32_16x16x32_bf16 v[192:195], a[40:43], v[232:235], v[192:195]\n"
      "v_mfma_f32_16x16x32_bf16 v[176:179], a[44:47], v[236:239], v[176:179]\n"
      "v_mfma_f32_16x16x32_bf16 v[176:179], a[64:67], v[240:243], v[176:179]\n"
      "ds_write_b128 v56, v[108:111] offset:21504\n"
      "v_mfma_f32_16x16x32_bf16 v[176:179], a[84:87], v[244:247], v[176:179]\n"
      "global_load_dwordx4 v[160:163], v45, s[12:13] offset:-3072 sc1\n"
      "v_mfma_f32_16x16x32_bf16 v[180:183], a[48:51], v[236:239], v[180:183]\n"
      "v_mfma_f32_16x16x32_bf16 v[180:183], a[68:71], v[240:243], v[180:183]\n"
      "ds_write_b128 v56, v[112:115] offset:2048\n"
      "v_mfma_f32_16x16x32_bf16 v[180:183], a[88:91], v[244:247], v[180:183]\n"
      "v_mfma_f32_16x16x32_bf16 v[184:187], a[52:55], v[236:239], v[184:187]\n"
      "global_load_dwordx4 v[164:167], v45, s[12:13] offset:-2048 sc1\n"
      "s_waitcnt vmcnt(11)\n"
      "v_mfma_f32_16x16x32_bf16 v[184:187], a[72:75], v[240:243], v[184:187]\n"
      "ds_write_b128 v56, v[116:119] offset:22528\n"
      "v_add3_u32 v2, v8, v9, s30\n"
      "v_add3_u32 v2, v2, v10, v11\n"
      "v_add3_u32 v2, v2, v12, v13\n"
      "v_add3_u32 v2, v2, v14, v15\n"
      "v_mfma_f32_16x16x32_bf16 v[184:187], a[92:95], v[244:247], v[184:187]\n"
      "v_add3_u32 v2, v2, v16, v17\n"
      "v_add3_u32 v2, v2, v18, v19\n"
      "v_mfma_f32_16x16x32_bf16 v[188:191], a[56:59], v[236:239], v[188:191]\n"
      "v_xor_b32 v2, v2, v34\n"
      "v_add3_u32 v3, v20, v21, s30\n"
      "v_add3_u32 v3, v3, v22, v23\n"
      "v_mfma_f32_16x16x32_bf16 v[188:191], a[76:79], v[240:243], v[188:191]\n"
      "global_load_dwordx4 v[168:171], v45, s[12:13] offset:-1024 sc1\n"
      "ds_write_b128 v56, v[120:123] offset:3072\n"
      "v_add3_u32 v3, v3, v128, v129\n"
      "v_add3_u32 v3, v3, v130, v131\n"
      "v_add3_u32 v3, v3, v132, v133\n"
      "v_add3_u32 v3, v3, v134, v135\n"
      "v_mfma_f32_16x16x32_bf16 v[188:191], a[96:99], v[244:247], v[188:191]\n"
      "v_xor_b32 v3, v3, v35\n"
      "v_or_b32 v2, v2, v3\n"
      "v_mfma_f32_16x16x32_bf16 v[192:195], a[60:63], v[236:239], v[192:195]\n"
      "v_cmp_ne_u32 vcc, 0, v2\n"
      "v_mfma_f32_16x16x32_bf16 v[192:195], a[80:83], v[240:243], v[192:195]\n"
      "ds_write_b128 v56, v[124:127] offset:23552\n"
      "v_mfma_f32_16x16x32_bf16 v[192:195], a[100:103], v[244:247], v[192:195]\n"
      "global_load_dwordx4 v[64:67], v45, s[12:13] offset:0 sc1\n"
      "s_cbranch_vccnz L_shB1_%=\n"
      "L_bhB1_%=:\n"
      "v_mfma_f32_16x16x32_bf16 v[176:179], a[104:107], v[8:11], v[176:179]\n"
      "v_mfma_f32_16x16x32_bf16 v[176:179], a[124:127], v[12:15], v[176:179]\n"
      "ds_write_b128 v56, v[248:251] offset:4096\n"
      "v_mfma_f32_16x16x32_bf16 v[176:179], a[144:147], v[16:19], v[176:179]\n"
      "v_mfma_f32_16x16x32_bf16 v[180:183], a[108:111], v[8:11], v[180:183]\n"
      "global_load_dwordx4 v[68:71], v45, s[12:13] offset:1024 sc1\n"
      "v_mfma_f32_16x16x32_bf16 v[180:183], a[128:131], v[12:15], v[180:183]\n"
      "ds_write_b128 v56, v[252:255] offset:24576\n"
      "v_mfma_f32_16x16x32_bf16 v[180:183], a[148:151], v[16:19], v[180:183]\n"
      "v_mfma_f32_16x16x32_bf16 v[184:187], a[112:115], v[8:11], v[184:187]\n"
      "v_mfma_f32_16x16x32_bf16 v[184:187], a[132:135], v[12:15], v[184:187]\n"
      "buffer_load_dword v26, v42, s[32:35], s63 offen offset:3584 sc1\n"
      "v_mfma_f32_16x16x32_bf16 v[184:187], a[152:155], v[16:19], v[184:187]\n"
      "buffer_load_dword v27, v42, s[32:35], s63 offen offset:3840 sc1\n"
      "v_mfma_f32_16x16x32_bf16 v[188:191], a[116:119], v[8:11], v[188:191]\n"
      "v_mfma_f32_16x16x32_bf16 v[188:191], a[136:139], v[12:15], v[188:191]\n"
      "s_waitcnt lgkmcnt(0)\n"
      "s_barrier\n"
      "v_mfma_f32_16x16x32_bf16 v[188:191], a[156:159], v[16:19], v[188:191]\n"
      "ds_read_b128 v[96:99], v49 offset:0\n"
      "v_add_u32 v56, 0xa000, v56\n"
      "v_mfma_f32_16x16x32_bf16 v[192:195], a[120:123], v[8:11], v[192:195]\n"
      "ds_read_b128 v[100:103], v49 offset:5120\n"
      "v_mfma_f32_16x16x32_bf16 v[192:195], a[140:143], v[12:15], v[192:195]\n"
      "ds_read_b128 v[104:107], v49 offset:10240\n"
      "v_mfma_f32_16x16x32_bf16 v[192:195], a[160:163], v[16:19], v[192:195]\n"
      "ds_read_b128 v[108:111], v49 offset:15360\n"
      "v_mfma_f32_16x16x32_bf16 v[176:179], a[164:167], v[20:23], v[176:179]\n"
      "ds_read_b64_tr_b16 v[72:73], v53 offset:0\n"
      "v_mfma_f32_16x16x32_bf16 v[176:179], a[184:187], v[128:131], v[176:179]\n"
      "ds_read_b64_tr_b16 v[74:75], v53 offset:2048\n"
      "v_mfma_f32_16x16x32_bf16 v[176:179], a[204:207], v[132:135], v[176:179]\n"
      "ds_read_b64_tr_b16 v[76:77], v53 offset:4096\n"
      "v_mfma_f32_16x16x32_bf16 v[180:183], a[168:171], v[20:23], v[180:183]\n"
      "ds_read_b128 v[112:115], v49 offset:1024\n"
      "v_mfma_f32_16x16x32_bf16 v[180:183], a[188:191], v[128:131], v[180:183]\n"
      "ds_read_b128 v[116:119], v49 offset:6144\n"
      "v_mfma_f32_16x16x32_bf16 v[180:183], a[208:211], v[132:135], v[180:183]\n"
      "ds_read_b128 v[120:123], v49 offset:11264\n"
      "v_mfma_f32_16x16x32_bf16 v[184:187], a[172:175], v[20:23], v[184:187]\n"
      "ds_read_b128 v[124:127], v49 offset:16384\n"
      "s_waitcnt vmcnt(8)\n"
      "v_mfma_f32_16x16x32_bf16 v[184:187], a[192:195], v[128:131], v[184:187]\n"
      "v_add3_u32 v2, v136, v137, s30\n"
      "v_add3_u32 v2, v2, v138, v139\n"
      "v_add3_u32 v2, v2, v140, v141\n"
      "s_waitcnt lgkmcnt(4)\n"
      "v_mfma_f32_16x16x32_bf16 v[184:187], a[212:215], v[132:135], v[184:187]\n"
      "v_add3_u32 v2, v2, v142, v143\n"
      "v_add3_u32 v2, v2, v144, v145\n"
      "v_add3_u32 v2, v2, v146, v147\n"
      "v_add_f32 v96, v96, v100\n"
      "v_add_f32 v97, v97, v101\n"
      "v_mfma_f32_16x16x32_bf16 v[188:191], a[176:179], v[20:23], v[188:191]\n"
      "v_xor_b32 v2, v2, v24\n"
      "v_add3_u32 v3, v148, v149, s30\n"
      "v_add3_u32 v3, v3, v150, v151\n"
      "v_add_f32 v98, v98, v102\n"
      "v_add_f32 v99, v99, v103\n"
      "v_mfma_f32_16x16x32_bf16 v[188:191], a[196:199], v[128:131], v[188:191]\n"
      "v_add3_u32 v3, v3, v152, v153\n"
      "v_add3_u32 v3, v3, v154, v155\n"
      "v_add3_u32 v3, v3, v156, v157\n"
      "v_add_f32 v104, v104, v108\n"
      "v_add_f32 v105, v105, v109\n"
      "v_mfma_f32_16x16x32_bf16 v[188:191], a[216:219], v[132:135], v[188:191]\n"
      "v_add3_u32 v3, v3, v158, v159\n"
      "v_xor_b32 v3, v3, v25\n"
      "v_or_b32 v2, v2, v3\n"
      "v_add_f32 v106, v106, v110\n"
      "v_add_f32 v107, v107, v111\n"
      "v_mfma_f32_16x16x32_bf16 v[192:195], a[180:183], v[20:23], v[192:195]\n"
      "v_cmp_ne_u32 vcc, 0, v2\n"
      "v_add_f32 v96, v96, v104\n"
      "v_add_f32 v97, v97, v105\n"
      "v_mfma_f32_16x16x32_bf16 v[192:195], a[200:203], v[128:131], v[192:195]\n"
      "v_add_f32 v98, v98, v106\n"
      "v_add_f32 v99, v99, v107\n"
      "v_mfma_f32_16x16x32_bf16 v[192:195], a[220:223], v[132:135], v[192:195]\n"
      "s_cbranch_vccnz L_shB2_%=\n"
      "L_bhB2_%=:\n"
      "v_cvt_pk_bf16_f32 v96, 0, v96\n"
      "v_mfma_f32_16x16x32_bf16 v[200:203], a[240:243], v[136:139], 0\n"
      "v_cvt_pk_bf16_f32 v97, 0, v97\n"
      "v_cvt_pk_bf16_f32 v98, 0, v98\n"
      "v_mfma_f32_16x16x32_bf16 v[200:203], a[4:7], v[140:143], v[200:203]\n"
      "v_cvt_pk_bf16_f32 v99, 0, v99\n"
      "v_mul_f32 v96, v96, v80\n"
      "v_mfma_f32_16x16x32_bf16 v[200:203], a[24:27], v[144:147], v[200:203]\n"
      "v_mul_f32 v97, v97, v81\n"
      "v_mul_f32 v98, v98, v82\n"
      "v_mfma_f32_16x16x32_bf16 v[204:207], a[244:247], v[136:139], 0\n"
      "v_mul_f32 v99, v99, v83\n"
      "v_cvt_pk_bf16_f32 v96, 0, v96\n"
      "v_mfma_f32_16x16x32_bf16 v[204:207], a[8:11], v[140:143], v[204:207]\n"
      "v_cvt_pk_bf16_f32 v97, 0, v97\n"
      "v_cvt_pk_bf16_f32 v98, 0, v98\n"
      "v_mfma_f32_16x16x32_bf16 v[204:207], a[28:31], v[144:147], v[204:207]\n"
      "v_cvt_pk_bf16_f32 v99, 0, v99\n"
      "v_lshlrev_b32 v100, 16, v72\n"
      "v_mfma_f32_16x16x32_bf16 v[208:211], a[248:251], v[136:139], 0\n"
      "v_and_b32 v101, 0xffff0000, v72\n"
      "v_lshlrev_b32 v102, 16, v73\n"
      "v_mfma_f32_16x16x32_bf16 v[208:211], a[12:15], v[140:143], v[208:211]\n"
      "v_and_b32 v103, 0xffff0000, v73\n"
      "v_add_f32 v96, v96, v100\n"
      "v_mfma_f32_16x16x32_bf16 v[208:211], a[32:35], v[144:147], v[208:211]\n"
      "v_add_f32 v97, v97, v101\n"
      "v_add_f32 v98, v98, v102\n"
      "v_mfma_f32_16x16x32_bf16 v[212:215], a[252:255], v[136:139], 0\n"
      "v_add_f32 v99, v99, v103\n"
      "v_cvt_pk_bf16_f32 v72, v96, v97\n"
      "v_mfma_f32_16x16x32_bf16 v[212:215], a[16:19], v[140:143], v[212:215]\n"
      "ds_write_b128 v56, v[176:179] offset:0\n"
      "v_cvt_pk_bf16_f32 v73, v98, v99\n"
      "v_mfma_f32_16x16x32_bf16 v[212:215], a[36:39], v[144:147], v[212:215]\n"
      "global_store_dwordx2 v55, v[72:73], s[28:29] offset:0 sc1\n"
      "s_waitcnt lgkmcnt(1)\n"
      "v_mfma_f32_16x16x32_bf16 v[216:219], a[0:3], v[136:139], 0\n"
      "ds_read_b128 v[96:99], v49 offset:2048\n"
      "v_add_f32 v112, v112, v116\n"
      "v_mfma_f32_16x16x32_bf16 v[216:219], a[20:23], v[140:143], v[216:219]\n"
      "ds_read_b128 v[100:103], v49 offset:7168\n"
      "v_add_f32 v113, v113, v117\n"
      "v_mfma_f32_16x16x32_bf16 v[216:219], a[40:43], v[144:147], v[216:219]\n"
      "ds_read_b128 v[104:107], v49 offset:12288\n"
      "v_add_f32 v114, v114, v118\n"
      "v_mfma_f32_16x16x32_bf16 v[200:203], a[44:47], v[148:151], v[200:203]\n"
      "ds_read_b128 v[108:111], v49 offset:17408\n"
      "v_add_f32 v115, v115, v119\n"
      "v_mfma_f32_16x16x32_bf16 v[200:203], a[64:67], v[152:155], v[200:203]\n"
      "v_add_f32 v120, v120, v124\n"
      "v_add_f32 v121, v121, v125\n"
      "v_mfma_f32_16x16x32_bf16 v[200:203], a[84:87], v[156:159], v[200:203]\n"
      "v_add_f32 v122, v122, v126\n"
      "v_add_f32 v123, v123, v127\n"
      "v_mfma_f32_16x16x32_bf16 v[204:207], a[48:51], v[148:151], v[204:207]\n"
      "v_add_f32 v112, v112, v120\n"
      "v_add_f32 v113, v113, v121\n"
      "v_mfma_f32_16x16x32_bf16 v[204:207], a[68:71], v[152:155], v[204:207]\n"
      "v_add_f32 v114, v114, v122\n"
      "v_add_f32 v115, v115, v123\n"
      "v_mfma_f32_16x16x32_bf16 v[204:207], a[88:91], v[156:159], v[204:207]\n"
      "v_cvt_pk_bf16_f32 v112, 0, v112\n"
      "v_cvt_pk_bf16_f32 v113, 0, v113\n"
      "v_mfma_f32_16x16x32_bf16 v[208:211], a[52:55], v[148:151], v[208:211]\n"
      "s_waitcnt vmcnt(1)\n"
      "v_cvt_pk_bf16_f32 v114, 0, v114\n"
      "v_mfma_f32_16x16x32_bf16 v[208:211], a[72:75], v[152:155], v[208:211]\n"
      "v_add3_u32 v2, v172, v173, s30\n"
      "v_add3_u32 v2, v2, v174, v175\n"
      "v_add3_u32 v2, v2, v160, v161\n"
      "v_cvt_pk_bf16_f32 v115, 0, v115\n"
      "v_mul_f32 v112, v112, v84\n"
      "v_mfma_f32_16x16x32_bf16 v[208:211], a[92:95], v[156:159], v[208:211]\n"
      "v_add3_u32 v2, v2, v162, v163\n"
      "v_add3_u32 v2, v2, v164, v165\n"
      "v_add3_u32 v2, v2, v166, v167\n"
      "v_mul_f32 v113, v113, v85\n"
      "v_mul_f32 v114, v114, v86\n"
      "v_mfma_f32_16x16x32_bf16 v[212:215], a[56:59], v[148:151], v[212:215]\n"
      "v_xor_b32 v2, v2, v26\n"
      "v_add3_u32 v3, v168, v169, s30\n"
      "v_add3_u32 v3, v3, v170, v171\n"
      "v_mul_f32 v115, v115, v87\n"
      "v_cvt_pk_bf16_f32 v112, 0, v112\n"
      "v_mfma_f32_16x16x32_bf16 v[212:215], a[76:79], v[152:155], v[212:215]\n"
      "ds_write_b128 v56, v[180:183] offset:1024\n"
      "v_add3_u32 v3, v3, v64, v65\n"
      "v_add3_u32 v3, v3, v66, v67\n"
      "v_add3_u32 v3, v3, v68, v69\n"
      "v_cvt_pk_bf16_f32 v113, 0, v113\n"
      "v_mfma_f32_16x16x32_bf16 v[212:215], a[96:99], v[156:159], v[212:215]\n"
      "v_add3_u32 v3, v3, v70, v71\n"
      "v_xor_b32 v3, v3, v27\n"
      "v_or_b32 v2, v2, v3\n"
      "v_cvt_pk_bf16_f32 v114, 0, v114\n"
      "v_cvt_pk_bf16_f32 v115, 0, v115\n"
      "v_mfma_f32_16x16x32_bf16 v[216:219], a[60:63], v[148:151], v[216:219]\n"
      "v_cmp_ne_u32 vcc, 0, v2\n"
      "v_lshlrev_b32 v116, 16, v74\n"
      "v_and_b32 v117, 0xffff0000, v74\n"
      "v_mfma_f32_16x16x32_bf16 v[216:219], a[80:83], v[152:155], v[216:219]\n"
      "v_lshlrev_b32 v118, 16, v75\n"
      "v_and_b32 v119, 0xffff0000, v75\n"
      "v_mfma_f32_16x16x32_bf16 v[216:219], a[100:103], v[156:159], v[216:219]\n"
      "ds_write_b128 v56, v[184:187] offset:2048\n"
      "s_cbranch_vccnz L_shB3_%=\n"
      "L_bhB3_%=:\n"
      "v_add_f32 v112, v112, v116\n"
      "v_add_f32 v113, v113, v117\n"
      "v_add_f32 v114, v114, v118\n"
      "v_mfma_f32_16x16x32_bf16 v[200:203], a[104:107], v[172:175], v[200:203]\n"
      "v_add_f32 v115, v115, v119\n"
      "v_cvt_pk_bf16_f32 v74, v112, v113\n"
      "v_mfma_f32_16x16x32_bf16 v[200:203], a[124:127], v[160:163], v[200:203]\n"
      "v_cvt_pk_bf16_f32 v75, v114, v115\n"
      "v_mfma_f32_16x16x32_bf16 v[200:203], a[144:147], v[164:167], v[200:203]\n"
      "global_store_dwordx2 v55, v[74:75], s[28:29] offset:32 sc1\n"
      "s_waitcnt lgkmcnt(2)\n"
      "v_mfma_f32_16x16x32_bf16 v[204:207], a[108:111], v[172:175], v[204:207]\n"
      "ds_write_b128 v56, v[188:191] offset:3072\n"
      "v_add_f32 v96, v96, v100\n"
      "v_add_f32 v97, v97, v101\n"
      "v_add_f32 v98, v98, v102\n"
      "v_add_f32 v99, v99, v103\n"
      "v_mfma_f32_16x16x32_bf16 v[204:207], a[128:131], v[160:163], v[204:207]\n"
      "v_add_f32 v104, v104, v108\n"
      "v_add_f32 v105, v105, v109\n"
      "v_mfma_f32_16x16x32_bf16 v[204:207], a[148:151], v[164:167], v[204:207]\n"
      "v_add_f32 v106, v106, v110\n"
      "v_add_f32 v107, v107, v111\n"
      "v_mfma_f32_16x16x32_bf16 v[208:211], a[112:115], v[172:175], v[208:211]\n"
      "v_add_f32 v96, v96, v104\n"
      "v_add_f32 v97, v97, v105\n"
      "v_mfma_f32_16x16x32_bf16 v[208:211], a[132:135], v[160:163], v[208:211]\n"
      "ds_write_b128 v56, v[192:195] offset:4096\n"
      "v_add_f32 v98, v98, v106\n"
      "v_add_f32 v99, v99, v107\n"
      "v_cvt_pk_bf16_f32 v96, 0, v96\n"
      "v_cvt_pk_bf16_f32 v97, 0, v97\n"
      "v_mfma_f32_16x16x32_bf16 v[208:211], a[152:155], v[164:167], v[208:211]\n"
      "v_cvt_pk_bf16_f32 v98, 0, v98\n"
      "v_cvt_pk_bf16_f32 v99, 0, v99\n"
      "v_mfma_f32_16x16x32_bf16 v[212:215], a[116:119], v[172:175], v[212:215]\n"
      "v_mul_f32 v96, v96, v88\n"
      "v_mul_f32 v97, v97, v89\n"
      "v_mfma_f32_16x16x32_bf16 v[212:215], a[136:139], v[160:163], v[212:215]\n"
      "v_mul_f32 v98, v98, v90\n"
      "v_mul_f32 v99, v99, v91\n"
      "v_mfma_f32_16x16x32_bf16 v[212:215], a[156:159], v[164:167], v[212:215]\n"
      "v_cvt_pk_bf16_f32 v96, 0, v96\n"
      "v_cvt_pk_bf16_f32 v97, 0, v97\n"
      "v_mfma_f32_16x16x32_bf16 v[216:219], a[120:123], v[172:175], v[216:219]\n"
      "v_cvt_pk_bf16_f32 v98, 0, v98\n"
      "v_cvt_pk_bf16_f32 v99, 0, v99\n"
      "v_mfma_f32_16x16x32_bf16 v[216:219], a[140:143], v[160:163], v[216:219]\n"
      "v_lshlrev_b32 v100, 16, v76\n"
      "v_and_b32 v101, 0xffff0000, v76\n"
      "v_mfma_f32_16x16x32_bf16 v[216:219], a[160:163], v[164:167], v[216:219]\n"
      "v_lshlrev_b32 v102, 16, v77\n"
      "v_and_b32 v103, 0xffff0000, v77\n"
      "v_mfma_f32_16x16x32_bf16 v[200:203], a[164:167], v[168:171], v[200:203]\n"
      "v_add_f32 v96, v96, v100\n"
      "L_pg7_%=:\n"
      "s_cbranch_execz L_pgr_%=\n"
      "v_add_f32 v97, v97, v101\n"
      "v_mfma_f32_16x16x32_bf16 v[200:203], a[184:187], v[64:67], v[200:203]\n"
      "v_add_f32 v98, v98, v102\n"
      "v_add_f32 v99, v99, v103\n"
      "v_mfma_f32_16x16x32_bf16 v[200:203], a[204:207], v[68:71], v[200:203]\n"
      "v_cvt_pk_bf16_f32 v76, v96, v97\n"
      "v_cvt_pk_bf16_f32 v77, v98, v99\n"
      "v_mfma_f32_16x16x32_bf16 v[204:207], a[168:171], v[168:171], v[204:207]\n"
      "global_store_dwordx2 v55, v[76:77], s[28:29] offset:64 sc1\n"
      "v_mfma_f32_16x16x32_bf16 v[204:207], a[188:191], v[64:67], v[204:207]\n"
      "v_mfma_f32_16x16x32_bf16 v[204:207], a[208:211], v[68:71], v[204:207]\n"
      "ds_write_b128 v56, v[200:203] offset:20480\n"
      "v_mfma_f32_16x16x32_bf16 v[208:211], a[172:175], v[168:171], v[208:211]\n"
      "v_mfma_f32_16x16x32_bf16 v[208:211], a[192:195], v[64:67], v[208:211]\n"
      "v_mfma_f32_16x16x32_bf16 v[208:211], a[212:215], v[68:71], v[208:211]\n"
      "ds_write_b128 v56, v[204:207] offset:21504\n"
      "v_mfma_f32_16x16x32_bf16 v[212:215], a[176:179], v[168:171], v[212:215]\n"
      "v_mfma_f32_16x16x32_bf16 v[212:215], a[196:199], v[64:67], v[212:215]\n"
      "v_mfma_f32_16x16x32_bf16 v[212:215], a[216:219], v[68:71], v[212:215]\n"
      "ds_write_b128 v56, v[208:211] offset:22528\n"
      "v_mfma_f32_16x16x32_bf16 v[216:219], a[180:183], v[168:171], v[216:219]\n"
      "v_mfma_f32_16x16x32_bf16 v[216:219], a[200:203], v[64:67], v[216:219]\n"
      "v_mfma_f32_16x16x32_bf16 v[216:219], a[220:223], v[68:71], v[216:219]\n"
      "ds_write_b128 v56, v[212:215] offset:23552\n"
      "v_xor_b32 v53, 64, v53\n"
      "ds_read_b64_tr_b16 v[64:65], v53 offset:0\n"
      "ds_read_b64_tr_b16 v[66:67], v53 offset:2048\n"
      "ds_read_b64_tr_b16 v[68:69], v53 offset:4096\n"
      "v_add_u32 v49, 0xa000, v49\n"
      "v_add_u32 v55, 81920, v55\n"
      "s_nop 0\n"
      "ds_write_b128 v56, v[216:219] offset:24576\n"
      "s_waitcnt lgkmcnt(0)\n"
      "s_barrier\n"
      "ds_read_b128 v[96:99], v49 offset:0\n"
      "ds_read_b128 v[100:103], v49 offset:5120\n"
      "ds_read_b128 v[104:107], v49 offset:10240\n"
      "ds_read_b128 v[108:111], v49 offset:15360\n"
      "ds_read_b128 v[112:115], v49 offset:1024\n"
      "ds_read_b128 v[116:119], v49 offset:6144\n"
      "ds_read_b128 v[120:123], v49 offset:11264\n"
      "ds_read_b128 v[124:127], v49 offset:16384\n"
      "ds_read_b128 v[128:131], v49 offset:2048\n"
      "ds_read_b128 v[132:135], v49 offset:7168\n"
      "ds_read_b128 v[136:139], v49 offset:12288\n"
      "ds_read_b128 v[140:143], v49 offset:17408\n"
      "v_lshlrev_b32 v144, 16, v64\n"
      "v_and_b32 v145, 0xffff0000, v64\n"
      "v_lshlrev_b32 v146, 16, v65\n"
      "v_and_b32 v147, 0xffff0000, v65\n"
      "v_lshlrev_b32 v148, 16, v66\n"
      "v_and_b32 v149, 0xffff0000, v66\n"
      "v_lshlrev_b32 v150, 16, v67\n"
      "v_and_b32 v151, 0xffff0000, v67\n"
      "v_lshlrev_b32 v152, 16, v68\n"
      "v_and_b32 v153, 0xffff0000, v68\n"
      "v_lshlrev_b32 v154, 16, v69\n"
      "v_and_b32 v155, 0xffff0000, v69\n"
      "s_waitcnt lgkmcnt(8)\n"
      "v_pk_add_f32 v[96:97], v[96:97], v[100:101]\n"
      "v_pk_add_f32 v[98:99], v[98:99], v[102:103]\n"
      "v_pk_add_f32 v[104:105], v[104:105], v[108:109]\n"
      "v_pk_add_f32 v[106:107], v[106:107], v[110:111]\n"
      "v_pk_add_f32 v[96:97], v[96:97], v[104:105]\n"
      "v_pk_add_f32 v[98:99], v[98:99], v[106:107]\n"
      "v_cvt_pk_bf16_f32 v96, 0, v96\n"
      "v_cvt_pk_bf16_f32 v97, 0, v97\n"
      "v_cvt_pk_bf16_f32 v98, 0, v98\n"
      "v_cvt_pk_bf16_f32 v99, 0, v99\n"
      "v_pk_mul_f32 v[96:97], v[96:97], v[80:81]\n"
      "v_pk_mul_f32 v[98:99], v[98:99], v[82:83]\n"
      "v_cvt_pk_bf16_f32 v96, 0, v96\n"
      "v_cvt_pk_bf16_f32 v97, 0, v97\n"
      "v_cvt_pk_bf16_f32 v98, 0, v98\n"
      "v_cvt_pk_bf16_f32 v99, 0, v99\n"
      "v_pk_add_f32 v[96:97], v[96:97], v[144:145]\n"
      "v_pk_add_f32 v[98:99], v[98:99], v[146:147]\n"
      "v_cvt_pk_bf16_f32 v96, v96, v97\n"
      "v_cvt_pk_bf16_f32 v97, v98, v99\n"
      "global_store_dwordx2 v55, v[96:97], s[28:29] offset:0\n"
      "s_waitcnt lgkmcnt(4)\n"
      "v_pk_add_f32 v[112:113], v[112:113], v[116:117]\n"
      "v_pk_add_f32 v[114:115], v[114:115], v[118:119]\n"
      "v_pk_add_f32 v[120:121], v[120:121], v[124:125]\n"
      "v_pk_add_f32 v[122:123], v[122:123], v[126:127]\n"
      "v_pk_add_f32 v[112:113], v[112:113], v[120:121]\n"
      "v_pk_add_f32 v[114:115], v[114:115], v[122:123]\n"
      "v_cvt_pk_bf16_f32 v112, 0, v112\n"
      "v_cvt_pk_bf16_f32 v113, 0, v113\n"
      "v_cvt_pk_bf16_f32 v114, 0, v114\n"
      "v_cvt_pk_bf16_f32 v115, 0, v115\n"
      "v_pk_mul_f32 v[112:113], v[112:113], v[84:85]\n"
      "v_pk_mul_f32 v[114:115], v[114:115], v[86:87]\n"
      "v_cvt_pk_bf16_f32 v112, 0, v112\n"
      "v_cvt_pk_bf16_f32 v113, 0, v113\n"
      "v_cvt_pk_bf16_f32 v114, 0, v114\n"
      "v_cvt_pk_bf16_f32 v115, 0, v115\n"
      "v_pk_add_f32 v[112:113], v[112:113], v[148:149]\n"
      "v_pk_add_f32 v[114:115], v[114:115], v[150:151]\n"
      "v_cvt_pk_bf16_f32 v112, v112, v113\n"
      "v_cvt_pk_bf16_f32 v113, v114, v115\n"
      "global_store_dwordx2 v55, v[112:113], s[28:29] offset:32\n"
      "s_waitcnt lgkmcnt(0)\n"
      "v_pk_add_f32 v[128:129], v[128:129], v[132:133]\n"
      "v_pk_add_f32 v[130:131], v[130:131], v[134:135]\n"
      "v_pk_add_f32 v[136:137], v[136:137], v[140:141]\n"
      "v_pk_add_f32 v[138:139], v[138:139], v[142:143]\n"
      "v_pk_add_f32 v[128:129], v[128:129], v[136:137]\n"
      "v_pk_add_f32 v[130:131], v[130:131], v[138:139]\n"
      "v_cvt_pk_bf16_f32 v128, 0, v128\n"
      "v_cvt_pk_bf16_f32 v129, 0, v129\n"
      "v_cvt_pk_bf16_f32 v130, 0, v130\n"
      "v_cvt_pk_bf16_f32 v131, 0, v131\n"
      "v_pk_mul_f32 v[128:129], v[128:129], v[88:89]\n"
      "v_pk_mul_f32 v[130:131], v[130:131], v[90:91]\n"
      "v_cvt_pk_bf16_f32 v128, 0, v128\n"
      "v_cvt_pk_bf16_f32 v129, 0, v129\n"
      "v_cvt_pk_bf16_f32 v130, 0, v130\n"
      "v_cvt_pk_bf16_f32 v131, 0, v131\n"
      "v_pk_add_f32 v[128:129], v[128:129], v[152:153]\n"
      "v_pk_add_f32 v[130:131], v[130:131], v[154:155]\n"
      "v_cvt_pk_bf16_f32 v128, v128, v129\n"
      "v_cvt_pk_bf16_f32 v129, v130, v131\n"
      "global_store_dwordx2 v55, v[128:129], s[28:29] offset:64\n"
      "s_or_b32 s40, s2, s60\n"
      "s_cmp_lg_u32 s40, 0\n"
      "s_cbranch_scc1 L_fin%=\n"
      "v_mov_b32 v8, s30\n"
      "v_mov_b32 v9, s31\n"
      "v_mov_b32 v10, 0\n"
      "s_mov_b64 exec, 1\n"
      "global_store_dwordx2 v10, v[8:9], s[26:27] sc1\n"
      "s_mov_b64 exec, -1\n"
      "L_fin%=:\n"
      "s_endpgm\n"
      "L_psx_%=:\n"
      "s_lshr_b32 s40, s92, 12\n"
      "s_lshr_b32 s41, s93, 12\n"
      "s_mulk_i32 s40, 4224\n"
      "s_mulk_i32 s41, 4224\n"
      "s_add_u32 s40, s40, 0x100000\n"
      "s_add_u32 s41, s41, 0x100000\n"
      "buffer_load_dwordx4 v[128:131], v54, s[32:35], s40 offen sc1\n"
      "s_add_u32 s40, s40, 1081344\n"
      "buffer_load_dwordx4 v[136:139], v54, s[32:35], s41 offen sc1\n"
      "buffer_load_dwordx4 v[132:135], v54, s[32:35], s40 offen sc1\n"
      "buffer_load_dword v140, v237, s[32:35], s70 offen sc1\n"
      "buffer_load_dword v141, v237, s[32:35], s98 offen sc1\n"
      "buffer_load_dword v142, v237, s[32:35], s71 offen sc1\n"
      "s_waitcnt vmcnt(0)\n"
      "v_add3_u32 v143, v128, v129, v130\n"
      "v_add3_u32 v143, v143, v131, v132\n"
      "v_add3_u32 v143, v143, v133, v134\n"
      "v_add3_u32 v143, v143, v135, v136\n"
      "v_add3_u32 v143, v143, v137, v138\n"
      "v_add_u32 v143, v143, v139\n"
      "v_add3_u32 v140, v140, v141, v142\n"
      "s_mul_i32 s47, s30, 3\n"
      "v_add_u32_dpp v143, v143, v143 quad_perm:[1,0,3,2] row_mask:0xf bank_mask:0xf\n"
      "s_nop 1\n"
      "v_add_u32_dpp v143, v143, v143 quad_perm:[2,3,0,1] row_mask:0xf bank_mask:0xf\n"
      "s_nop 1\n"
      "v_add_u32_dpp v143, v143, v143 row_half_mirror row_mask:0xf bank_mask:0xf\n"
      "v_add_u32 v143, v143, v140\n"
      "v_cmp_ne_u32 vcc, s47, v143\n"
      "s_sub_u32 s44, s44, 1\n"
      "s_cbranch_vccz L_pbx_%=\n"
      "s_cmp_gt_i32 s44, 0\n"
      "s_cbranch_scc1 L_psx_%=\n"
      "s_branch L_pbx_%=\n"
      "L_cv_s%=:\n"
      "buffer_load_dwordx2 v[200:201], v4, s[32:35], s80 offen offset:0 sc1\n"
      "buffer_load_dword v250, v61, s[32:35], s85 offen offset:0 sc1\n"
      "buffer_load_dwordx4 v[224:227], v57, s[32:35], s82 offen offset:0 sc1\n"
      "buffer_load_dwordx4 v[228:231], v57, s[32:35], s82 offen offset:1024 sc1\n"
      "buffer_load_dwordx4 v[192:195], v54, s[32:35], s53 offen offset:0 sc1\n"
      "buffer_load_dwordx4 v[196:199], v54, s[32:35], s54 offen offset:0 sc1\n"
      "buffer_load_dword v248, v60, s[32:35], s84 offen offset:0 sc1\n"
      "buffer_load_dword v249, v60, s[32:35], s84 offen offset:2048 sc1\n"
      "buffer_load_dwordx4 v[208:211], v55, s[32:35], 0 offen offset:0 sc1\n"
      "buffer_load_dwordx4 v[212:215], v55, s[32:35], 0 offen offset:1024 sc1\n"
      "buffer_load_dwordx4 v[216:219], v55, s[32:35], 0 offen offset:2048 sc1\n"
      "buffer_load_dwordx4 v[220:223], v55, s[32:35], 0 offen offset:3072 sc1\n"
      "buffer_load_dwordx2 v[204:205], v58, s[32:35], s83 offen offset:0 sc1\n"
      "s_waitcnt vmcnt(0)\n"
      "v_add_u32 v251, v200, v201\n"
      "v_add3_u32 v34, v192, v193, v194\n"
      "v_add3_u32 v34, v34, v195, v196\n"
      "v_add_u32_dpp v251, v251, v251 row_half_mirror row_mask:0xf bank_mask:0xf\n"
      "v_add3_u32 v34, v34, v197, v198\n"
      "v_add3_u32 v251, v251, v34, v199\n"
      "v_add3_u32 v34, v248, v249, v250\n"
      "v_add3_u32 v34, v34, v211, v215\n"
      "v_add_u32_dpp v251, v251, v251 quad_perm:[1,0,3,2] row_mask:0xf bank_mask:0xf\n"
      "v_add3_u32 v34, v34, v219, v223\n"
      "v_add3_u32 v34, v34, v227, v231\n"
      "v_add_u32_dpp v251, v251, v251 quad_perm:[2,3,0,1] row_mask:0xf bank_mask:0xf\n"
      "v_add3_u32 v251, v251, v34, v205\n"
      "v_cmp_ne_u32 vcc, s24, v251\n"
      "s_sub_u32 s44, s44, 1\n"
      "s_cbranch_vccz L_cv_r%=\n"
      "s_cmp_gt_i32 s44, 0\n"
      "s_cbranch_scc1 L_cv_s%=\n"
      "s_branch L_cv_r%=\n"
      "L_cw_s%=:\n"
      "buffer_load_dwordx2 v[252:253], v51, s[32:35], s76 offen offset:0 sc1\n"
      "s_waitcnt vmcnt(0)\n"
      "v_cmp_ne_u64 vcc, s[30:31], v[252:253]\n"
      "s_sub_u32 s44, s44, 1\n"
      "s_cbranch_vccz L_cw_r%=\n"
      "s_cmp_gt_i32 s44, 0\n"
      "s_cbranch_scc1 L_cw_s%=\n"
      "s_branch L_cw_r%=\n"
      "L_cm_s%=:\n"
      "buffer_load_dwordx2 v[254:255], v2, s[32:35], s45 offen sc1\n"
      "s_waitcnt vmcnt(0)\n"
      "v_cmp_ne_u32 vcc, s30, v255\n"
      "s_sub_u32 s44, s44, 1\n"
      "s_cbranch_vccz L_cm_r%=\n"
      "s_cmp_gt_i32 s44, 0\n"
      "s_cbranch_scc1 L_cm_s%=\n"
      "s_branch L_cm_r%=\n"
      "L_sx1_%=:\n"
      "s_mov_b32 s45, m0\n"
      "s_mov_b32 s58, s47\n"
      "s_add_u32 m0, s73, 16384\n"
      "v_add_u32 v248, 81920, v40\n"
      "global_load_lds_dwordx4 v248, s[4:5] offset:0 sc1\n"
      "global_load_lds_dwordx4 v248, s[4:5] offset:1024 sc1\n"
      "global_load_lds_dwordx4 v248, s[4:5] offset:2048 sc1\n"
      "global_load_lds_dwordx4 v248, s[4:5] offset:3072 sc1\n"
      "s_mov_b32 s47, 4\n"
      "buffer_load_dword v3, v5, s[32:35], s47 offen sc1\n"
      "s_waitcnt vmcnt(0)\n"
      "s_mov_b32 m0, s45\n"
      "s_mov_b32 s47, s58\n"
      "ds_read_b128 v[248:251], v40 offset:16384\n"
      "ds_read_b128 v[252:255], v40 offset:17408\n"
      "ds_read_b128 v[36:39], v40 offset:18432\n"
      "ds_read_b128 v[60:63], v40 offset:19456\n"
      "s_waitcnt lgkmcnt(0)\n"
      "v_add3_u32 v3, v3, v248, v249\n"
      "v_add3_u32 v3, v3, v250, v251\n"
      "v_add3_u32 v3, v3, v252, v253\n"
      "v_add3_u32 v3, v3, v254, v255\n"
      "v_add3_u32 v3, v3, v36, v37\n"
      "v_add3_u32 v3, v3, v38, v39\n"
      "v_add3_u32 v3, v3, v60, v61\n"
      "v_add3_u32 v3, v3, v62, v63\n"
      "s_nop 1\n"
      "v_add_u32_dpp v3, v3, v3 quad_perm:[1,0,3,2] row_mask:0xf bank_mask:0xf\n"
      "s_nop 1\n"
      "v_add_u32_dpp v3, v3, v3 quad_perm:[2,3,0,1] row_mask:0xf bank_mask:0xf\n"
      "s_nop 1\n"
      "v_add_u32_dpp v3, v3, v3 row_half_mirror row_mask:0xf bank_mask:0xf\n"
      "v_cmp_ne_u32 vcc, s71, v3\n"
      "s_sub_u32 s44, s44, 1\n"
      "s_cbranch_vccz L_bx1_%=\n"
      "s_cmp_gt_i32 s44, 0\n"
      "s_cbranch_scc1 L_sx1_%=\n"
      "s_branch L_bx1_%=\n"
      "L_sx2_%=:\n"
      "s_mov_b32 s45, m0\n"
      "s_mov_b32 s58, s47\n"
      "s_add_u32 m0, s73, 32768\n"
      "v_add_u32 v248, 16384, v40\n"
      "global_load_lds_dwordx4 v248, s[4:5] offset:0 sc1\n"
      "global_load_lds_dwordx4 v248, s[4:5] offset:1024 sc1\n"
      "global_load_lds_dwordx4 v248, s[4:5] offset:2048 sc1\n"
      "global_load_lds_dwordx4 v248, s[4:5] offset:3072 sc1\n"
      "s_mov_b32 s47, 32768\n"
      "buffer_load_dword v48, v5, s[32:35], s47 offen sc1\n"
      "s_waitcnt vmcnt(0)\n"
      "s_mov_b32 m0, s45\n"
      "s_mov_b32 s47, s58\n"
      "ds_read_b128 v[248:251], v40 offset:32768\n"
      "ds_read_b128 v[252:255], v40 offset:33792\n"
      "ds_read_b128 v[36:39], v40 offset:34816\n"
      "ds_read_b128 v[60:63], v40 offset:35840\n"
      "s_waitcnt lgkmcnt(0)\n"
      "v_add3_u32 v48, v48, v248, v249\n"
      "v_add3_u32 v48, v48, v250, v251\n"
      "v_add3_u32 v48, v48, v252, v253\n"
      "v_add3_u32 v48, v48, v254, v255\n"
      "v_add3_u32 v48, v48, v36, v37\n"
      "v_add3_u32 v48, v48, v38, v39\n"
      "v_add3_u32 v48, v48, v60, v61\n"
      "v_add3_u32 v48, v48, v62, v63\n"
      "s_nop 1\n"
      "v_add_u32_dpp v48, v48, v48 quad_perm:[1,0,3,2] row_mask:0xf bank_mask:0xf\n"
      "s_nop 1\n"
      "v_add_u32_dpp v48, v48, v48 quad_perm:[2,3,0,1] row_mask:0xf bank_mask:0xf\n"
      "s_nop 1\n"
      "v_add_u32_dpp v48, v48, v48 row_half_mirror row_mask:0xf bank_mask:0xf\n"
      "v_cmp_ne_u32 vcc, s71, v48\n"
      "s_sub_u32 s44, s44, 1\n"
      "s_cbranch_vccz L_bx2_%=\n"
      "s_cmp_gt_i32 s44, 0\n"
      "s_cbranch_scc1 L_sx2_%=\n"
      "s_branch L_bx2_%=\n"
      "L_sx3_%=:\n"
      "s_mov_b32 s45, m0\n"
      "s_mov_b32 s58, s47\n"
      "s_add_u32 m0, s73, 49152\n"
      "v_add_u32 v248, 32768, v40\n"
      "global_load_lds_dwordx4 v248, s[4:5] offset:0 sc1\n"
      "global_load_lds_dwordx4 v248, s[4:5] offset:1024 sc1\n"
      "global_load_lds_dwordx4 v248, s[4:5] offset:2048 sc1\n"
      "global_load_lds_dwordx4 v248, s[4:5] offset:3072 sc1\n"
      "s_mov_b32 s47, 65536\n"
      "buffer_load_dword v54, v5, s[32:35], s47 offen sc1\n"
      "s_waitcnt vmcnt(0)\n"
      "s_mov_b32 m0, s45\n"
      "s_mov_b32 s47, s58\n"
      "ds_read_b128 v[248:251], v40 offset:49152\n"
      "ds_read_b128 v[252:255], v40 offset:50176\n"
      "ds_read_b128 v[36:39], v40 offset:51200\n"
      "ds_read_b128 v[60:63], v40 offset:52224\n"
      "s_waitcnt lgkmcnt(0)\n"
      "v_add3_u32 v54, v54, v248, v249\n"
      "v_add3_u32 v54, v54, v250, v251\n"
      "v_add3_u32 v54, v54, v252, v253\n"
      "v_add3_u32 v54, v54, v254, v255\n"
      "v_add3_u32 v54, v54, v36, v37\n"
      "v_add3_u32 v54, v54, v38, v39\n"
      "v_add3_u32 v54, v54, v60, v61\n"
      "v_add3_u32 v54, v54, v62, v63\n"
      "s_nop 1\n"
      "v_add_u32_dpp v54, v54, v54 quad_perm:[1,0,3,2] row_mask:0xf bank_mask:0xf\n"
      "s_nop 1\n"
      "v_add_u32_dpp v54, v54, v54 quad_perm:[2,3,0,1] row_mask:0xf bank_mask:0xf\n"
      "s_nop 1\n"
      "v_add_u32_dpp v54, v54, v54 row_half_mirror row_mask:0xf bank_mask:0xf\n"
      "v_cmp_ne_u32 vcc, s71, v54\n"
      "s_sub_u32 s44, s44, 1\n"
      "s_cbranch_vccz L_bx3_%=\n"
      "s_cmp_gt_i32 s44, 0\n"
      "s_cbranch_scc1 L_sx3_%=\n"
      "s_branch L_bx3_%=\n"
      "L_sx4_%=:\n"
      "s_mov_b32 s45, m0\n"
      "s_mov_b32 s58, s47\n"
      "s_add_u32 m0, s73, 0\n"
      "v_add_u32 v248, 49152, v40\n"
      "global_load_lds_dwordx4 v248, s[4:5] offset:0 sc1\n"
      "global_load_lds_dwordx4 v248, s[4:5] offset:1024 sc1\n"
      "global_load_lds_dwordx4 v248, s[4:5] offset:2048 sc1\n"
      "global_load_lds_dwordx4 v248, s[4:5] offset:3072 sc1\n"
      "s_mov_b32 s47, 98304\n"
      "buffer_load_dword v56, v5, s[32:35], s47 offen sc1\n"
      "s_waitcnt vmcnt(0)\n"
      "s_mov_b32 m0, s45\n"
      "s_mov_b32 s47, s58\n"
      "ds_read_b128 v[248:251], v40 offset:0\n"
      "ds_read_b128 v[252:255], v40 offset:1024\n"
      "ds_read_b128 v[36:39], v40 offset:2048\n"
      "ds_read_b128 v[60:63], v40 offset:3072\n"
      "s_waitcnt lgkmcnt(0)\n"
      "v_add3_u32 v56, v56, v248, v249\n"
      "v_add3_u32 v56, v56, v250, v251\n"
      "v_add3_u32 v56, v56, v252, v253\n"
      "v_add3_u32 v56, v56, v254, v255\n"
      "v_add3_u32 v56, v56, v36, v37\n"
      "v_add3_u32 v56, v56, v38, v39\n"
      "v_add3_u32 v56, v56, v60, v61\n"
      "v_add3_u32 v56, v56, v62, v63\n"
      "s_nop 1\n"
      "v_add_u32_dpp v56, v56, v56 quad_perm:[1,0,3,2] row_mask:0xf bank_mask:0xf\n"
      "s_nop 1\n"
      "v_add_u32_dpp v56, v56, v56 quad_perm:[2,3,0,1] row_mask:0xf bank_mask:0xf\n"
      "s_nop 1\n"
      "v_add_u32_dpp v56, v56, v56 row_half_mirror row_mask:0xf bank_mask:0xf\n"
      "v_cmp_ne_u32 vcc, s71, v56\n"
      "s_sub_u32 s44, s44, 1\n"
      "s_cbranch_vccz L_bx4_%=\n"
      "s_cmp_gt_i32 s44, 0\n"
      "s_cbranch_scc1 L_sx4_%=\n"
      "s_branch L_bx4_%=\n"
      "L_sx5_%=:\n"
      "s_mov_b32 s45, m0\n"
      "s_mov_b32 s58, s47\n"
      "s_add_u32 m0, s73, 16384\n"
      "v_add_u32 v248, 65536, v40\n"
      "global_load_lds_dwordx4 v248, s[4:5] offset:0 sc1\n"
      "global_load_lds_dwordx4 v248, s[4:5] offset:1024 sc1\n"
      "global_load_lds_dwordx4 v248, s[4:5] offset:2048 sc1\n"
      "global_load_lds_dwordx4 v248, s[4:5] offset:3072 sc1\n"
      "s_mov_b32 s47, 131072\n"
      "buffer_load_dword v2, v5, s[32:35], s47 offen sc1\n"
      "s_waitcnt vmcnt(0)\n"
      "s_mov_b32 m0, s45\n"
      "s_mov_b32 s47, s58\n"
      "ds_read_b128 v[248:251], v40 offset:16384\n"
      "ds_read_b128 v[252:255], v40 offset:17408\n"
      "ds_read_b128 v[36:39], v40 offset:18432\n"
      "ds_read_b128 v[60:63], v40 offset:19456\n"
      "s_waitcnt lgkmcnt(0)\n"
      "v_add3_u32 v2, v2, v248, v249\n"
      "v_add3_u32 v2, v2, v250, v251\n"
      "v_add3_u32 v2, v2, v252, v253\n"
      "v_add3_u32 v2, v2, v254, v255\n"
      "v_add3_u32 v2, v2, v36, v37\n"
      "v_add3_u32 v2, v2, v38, v39\n"
      "v_add3_u32 v2, v2, v60, v61\n"
      "v_add3_u32 v2, v2, v62, v63\n"
      "s_nop 1\n"
      "v_add_u32_dpp v2, v2, v2 quad_perm:[1,0,3,2] row_mask:0xf bank_mask:0xf\n"
      "s_nop 1\n"
      "v_add_u32_dpp v2, v2, v2 quad_perm:[2,3,0,1] row_mask:0xf bank_mask:0xf\n"
      "s_nop 1\n"
      "v_add_u32_dpp v2, v2, v2 row_half_mirror row_mask:0xf bank_mask:0xf\n"
      "v_cmp_ne_u32 vcc, s71, v2\n"
      "s_sub_u32 s44, s44, 1\n"
      "s_cbranch_vccz L_bx5_%=\n"
      "s_cmp_gt_i32 s44, 0\n"
      "s_cbranch_scc1 L_sx5_%=\n"
      "s_branch L_bx5_%=\n"
      "L_sx6_%=:\n"
      "s_mov_b32 s45, m0\n"
      "s_mov_b32 s58, s47\n"
      "s_add_u32 m0, s73, 32768\n"
      "v_add_u32 v248, 98304, v40\n"
      "global_load_lds_dwordx4 v248, s[4:5] offset:0 sc1\n"
      "global_load_lds_dwordx4 v248, s[4:5] offset:1024 sc1\n"
      "global_load_lds_dwordx4 v248, s[4:5] offset:2048 sc1\n"
      "global_load_lds_dwordx4 v248, s[4:5] offset:3072 sc1\n"
      "s_mov_b32 s47, 32772\n"
      "buffer_load_dword v49, v5, s[32:35], s47 offen sc1\n"
      "s_waitcnt vmcnt(0)\n"
      "s_mov_b32 m0, s45\n"
      "s_mov_b32 s47, s58\n"
      "ds_read_b128 v[248:251], v40 offset:32768\n"
      "ds_read_b128 v[252:255], v40 offset:33792\n"
      "ds_read_b128 v[36:39], v40 offset:34816\n"
      "ds_read_b128 v[60:63], v40 offset:35840\n"
      "s_waitcnt lgkmcnt(0)\n"
      "v_add3_u32 v49, v49, v248, v249\n"
      "v_add3_u32 v49, v49, v250, v251\n"
      "v_add3_u32 v49, v49, v252, v253\n"
      "v_add3_u32 v49, v49, v254, v255\n"
      "v_add3_u32 v49, v49, v36, v37\n"
      "v_add3_u32 v49, v49, v38, v39\n"
      "v_add3_u32 v49, v49, v60, v61\n"
      "v_add3_u32 v49, v49, v62, v63\n"
      "s_nop 1\n"
      "v_add_u32_dpp v49, v49, v49 quad_perm:[1,0,3,2] row_mask:0xf bank_mask:0xf\n"
      "s_nop 1\n"
      "v_add_u32_dpp v49, v49, v49 quad_perm:[2,3,0,1] row_mask:0xf bank_mask:0xf\n"
      "s_nop 1\n"
      "v_add_u32_dpp v49, v49, v49 row_half_mirror row_mask:0xf bank_mask:0xf\n"
      "v_cmp_ne_u32 vcc, s71, v49\n"
      "s_sub_u32 s44, s44, 1\n"
      "s_cbranch_vccz L_bx6_%=\n"
      "s_cmp_gt_i32 s44, 0\n"
      "s_cbranch_scc1 L_sx6_%=\n"
      "s_branch L_bx6_%=\n"
      "L_sx7_%=:\n"
      "s_mov_b32 s45, m0\n"
      "s_mov_b32 s58, s47\n"
      "s_add_u32 m0, s73, 49152\n"
      "v_add_u32 v248, 114688, v40\n"
      "global_load_lds_dwordx4 v248, s[4:5] offset:0 sc1\n"
      "global_load_lds_dwordx4 v248, s[4:5] offset:1024 sc1\n"
      "global_load_lds_dwordx4 v248, s[4:5] offset:2048 sc1\n"
      "global_load_lds_dwordx4 v248, s[4:5] offset:3072 sc1\n"
      "s_mov_b32 s47, 65540\n"
      "buffer_load_dword v55, v5, s[32:35], s47 offen sc1\n"
      "s_waitcnt vmcnt(0)\n"
      "s_mov_b32 m0, s45\n"
      "s_mov_b32 s47, s58\n"
      "ds_read_b128 v[248:251], v40 offset:49152\n"
      "ds_read_b128 v[252:255], v40 offset:50176\n"
      "ds_read_b128 v[36:39], v40 offset:51200\n"
      "ds_read_b128 v[60:63], v40 offset:52224\n"
      "s_waitcnt lgkmcnt(0)\n"
      "v_add3_u32 v55, v55, v248, v249\n"
      "v_add3_u32 v55, v55, v250, v251\n"
      "v_add3_u32 v55, v55, v252, v253\n"
      "v_add3_u32 v55, v55, v254, v255\n"
      "v_add3_u32 v55, v55, v36, v37\n"
      "v_add3_u32 v55, v55, v38, v39\n"
      "v_add3_u32 v55, v55, v60, v61\n"
      "v_add3_u32 v55, v55, v62, v63\n"
      "s_nop 1\n"
      "v_add_u32_dpp v55, v55, v55 quad_perm:[1,0,3,2] row_mask:0xf bank_mask:0xf\n"
      "s_nop 1\n"
      "v_add_u32_dpp v55, v55, v55 quad_perm:[2,3,0,1] row_mask:0xf bank_mask:0xf\n"
      "s_nop 1\n"
      "v_add_u32_dpp v55, v55, v55 row_half_mirror row_mask:0xf bank_mask:0xf\n"
      "v_cmp_ne_u32 vcc, s71, v55\n"
      "s_sub_u32 s44, s44, 1\n"
      "s_cbranch_vccz L_bx7_%=\n"
      "s_cmp_gt_i32 s44, 0\n"
      "s_cbranch_scc1 L_sx7_%=\n"
      "s_branch L_bx7_%=\n"
      "L_sx8_%=:\n"
      "s_mov_b32 s45, m0\n"
      "s_mov_b32 s58, s47\n"
      "s_add_u32 m0, s73, 0\n"
      "v_add_u32 v248, 131072, v40\n"
      "global_load_lds_dwordx4 v248, s[4:5] offset:0 sc1\n"
      "global_load_lds_dwordx4 v248, s[4:5] offset:1024 sc1\n"
      "global_load_lds_dwordx4 v248, s[4:5] offset:2048 sc1\n"
      "global_load_lds_dwordx4 v248, s[4:5] offset:3072 sc1\n"
      "s_mov_b32 s47, 98308\n"
      "buffer_load_dword v57, v5, s[32:35], s47 offen sc1\n"
      "s_waitcnt vmcnt(0)\n"
      "s_mov_b32 m0, s45\n"
      "s_mov_b32 s47, s58\n"
      "ds_read_b128 v[248:251], v40 offset:0\n"
      "ds_read_b128 v[252:255], v40 offset:1024\n"
      "ds_read_b128 v[36:39], v40 offset:2048\n"
      "ds_read_b128 v[60:63], v40 offset:3072\n"
      "s_waitcnt lgkmcnt(0)\n"
      "v_add3_u32 v57, v57, v248, v249\n"
      "v_add3_u32 v57, v57, v250, v251\n"
      "v_add3_u32 v57, v57, v252, v253\n"
      "v_add3_u32 v57, v57, v254, v255\n"
      "v_add3_u32 v57, v57, v36, v37\n"
      "v_add3_u32 v57, v57, v38, v39\n"
      "v_add3_u32 v57, v57, v60, v61\n"
      "v_add3_u32 v57, v57, v62, v63\n"
      "s_nop 1\n"
      "v_add_u32_dpp v57, v57, v57 quad_perm:[1,0,3,2] row_mask:0xf bank_mask:0xf\n"
      "s_nop 1\n"
      "v_add_u32_dpp v57, v57, v57 quad_perm:[2,3,0,1] row_mask:0xf bank_mask:0xf\n"
      "s_nop 1\n"
      "v_add_u32_dpp v57, v57, v57 row_half_mirror row_mask:0xf bank_mask:0xf\n"
      "v_cmp_ne_u32 vcc, s71, v57\n"
      "s_sub_u32 s44, s44, 1\n"
      "s_cbranch_vccz L_bx8_%=\n"
      "s_cmp_gt_i32 s44, 0\n"
      "s_cbranch_scc1 L_sx8_%=\n"
      "s_branch L_bx8_%=\n"
      "L_sx9_%=:\n"
      "s_mov_b32 s45, m0\n"
      "s_mov_b32 s58, s47\n"
      "s_add_u32 m0, s73, 16384\n"
      "v_add_u32 v248, 147456, v40\n"
      "global_load_lds_dwordx4 v248, s[4:5] offset:0 sc1\n"
      "global_load_lds_dwordx4 v248, s[4:5] offset:1024 sc1\n"
      "global_load_lds_dwordx4 v248, s[4:5] offset:2048 sc1\n"
      "global_load_lds_dwordx4 v248, s[4:5] offset:3072 sc1\n"
      "s_mov_b32 s47, 131076\n"
      "buffer_load_dword v3, v5, s[32:35], s47 offen sc1\n"
      "s_waitcnt vmcnt(0)\n"
      "s_mov_b32 m0, s45\n"
      "s_mov_b32 s47, s58\n"
      "ds_read_b128 v[248:251], v40 offset:16384\n"
      "ds_read_b128 v[252:255], v40 offset:17408\n"
      "ds_read_b128 v[36:39], v40 offset:18432\n"
      "ds_read_b128 v[60:63], v40 offset:19456\n"
      "s_waitcnt lgkmcnt(0)\n"
      "v_add3_u32 v3, v3, v248, v249\n"
      "v_add3_u32 v3, v3, v250, v251\n"
      "v_add3_u32 v3, v3, v252, v253\n"
      "v_add3_u32 v3, v3, v254, v255\n"
      "v_add3_u32 v3, v3, v36, v37\n"
      "v_add3_u32 v3, v3, v38, v39\n"
      "v_add3_u32 v3, v3, v60, v61\n"
      "v_add3_u32 v3, v3, v62, v63\n"
      "s_nop 1\n"
      "v_add_u32_dpp v3, v3, v3 quad_perm:[1,0,3,2] row_mask:0xf bank_mask:0xf\n"
      "s_nop 1\n"
      "v_add_u32_dpp v3, v3, v3 quad_perm:[2,3,0,1] row_mask:0xf bank_mask:0xf\n"
      "s_nop 1\n"
      "v_add_u32_dpp v3, v3, v3 row_half_mirror row_mask:0xf bank_mask:0xf\n"
      "v_cmp_ne_u32 vcc, s71, v3\n"
      "s_sub_u32 s44, s44, 1\n"
      "s_cbranch_vccz L_bx9_%=\n"
      "s_cmp_gt_i32 s44, 0\n"
      "s_cbranch_scc1 L_sx9_%=\n"
      "s_branch L_bx9_%=\n"
      "L_sx0_%=:\n"
      "s_mov_b32 s45, m0\n"
      "s_mov_b32 s58, s47\n"
      "s_add_u32 m0, s73, 0\n"
      "v_add_u32 v248, 0, v40\n"
      "global_load_lds_dwordx4 v248, s[4:5] offset:0 sc1\n"
      "global_load_lds_dwordx4 v248, s[4:5] offset:1024 sc1\n"
      "global_load_lds_dwordx4 v248, s[4:5] offset:2048 sc1\n"
      "global_load_lds_dwordx4 v248, s[4:5] offset:3072 sc1\n"
      "s_mov_b32 s47, 0\n"
      "buffer_load_dword v2, v5, s[32:35], s47 offen sc1\n"
      "s_waitcnt vmcnt(0)\n"
      "s_mov_b32 m0, s45\n"
      "s_mov_b32 s47, s58\n"
      "ds_read_b128 v[248:251], v40 offset:0\n"
      "ds_read_b128 v[252:255], v40 offset:1024\n"
      "ds_read_b128 v[36:39], v40 offset:2048\n"
      "ds_read_b128 v[60:63], v40 offset:3072\n"
      "s_waitcnt lgkmcnt(0)\n"
      "v_add3_u32 v2, v2, v248, v249\n"
      "v_add3_u32 v2, v2, v250, v251\n"
      "v_add3_u32 v2, v2, v252, v253\n"
      "v_add3_u32 v2, v2, v254, v255\n"
      "v_add3_u32 v2, v2, v36, v37\n"
      "v_add3_u32 v2, v2, v38, v39\n"
      "v_add3_u32 v2, v2, v60, v61\n"
      "v_add3_u32 v2, v2, v62, v63\n"
      "s_nop 1\n"
      "v_add_u32_dpp v2, v2, v2 quad_perm:[1,0,3,2] row_mask:0xf bank_mask:0xf\n"
      "s_nop 1\n"
      "v_add_u32_dpp v2, v2, v2 quad_perm:[2,3,0,1] row_mask:0xf bank_mask:0xf\n"
      "s_nop 1\n"
      "v_add_u32_dpp v2, v2, v2 row_half_mirror row_mask:0xf bank_mask:0xf\n"
      "v_cmp_ne_u32 vcc, s71, v2\n"
      "s_sub_u32 s44, s44, 1\n"
      "s_cbranch_vccz L_bx0_%=\n"
      "s_cmp_gt_i32 s44, 0\n"
      "s_cbranch_scc1 L_sx0_%=\n"
      "s_branch L_bx0_%=\n"
      "L_sfd_%=:\n"
      "global_load_dwordx2 v[58:59], v42, s[26:27] sc1\n"
      "buffer_load_dwordx2 v[46:47], v51, s[32:35], s75 offen sc1\n"
      "s_waitcnt vmcnt(0)\n"
      "v_cmp_ne_u64 s[52:53], s[30:31], v[46:47]\n"
      "v_cmp_ne_u64 vcc, s[30:31], v[58:59]\n"
      "s_or_b64 vcc, vcc, s[52:53]\n"
      "s_sub_u32 s44, s44, 1\n"
      "s_cbranch_vccz L_bfd_%=\n"
      "s_cmp_gt_i32 s44, 0\n"
      "s_cbranch_scc1 L_sfd_%=\n"
      "s_branch L_bfd_%=\n"
      "L_sxp_%=:\n"
      "s_mov_b32 s45, m0\n"
      "s_mov_b32 s58, s47\n"
      "s_mov_b32 m0, s94\n"
      "s_nop 0\n"
      "buffer_load_dwordx4 v7, s[32:35], s67 offen offset:0 sc1 lds\n"
      "buffer_load_dwordx4 v7, s[32:35], s67 offen offset:1024 sc1 lds\n"
      "buffer_load_dwordx4 v7, s[32:35], s67 offen offset:2048 sc1 lds\n"
      "buffer_load_dword v32, v31, s[32:35], s68 offen sc1\n"
      "s_waitcnt vmcnt(0)\n"
      "s_mov_b32 m0, s45\n"
      "s_mov_b32 s47, s58\n"
      "ds_read_b128 v[248:251], v50 offset:0\n"
      "ds_read_b128 v[252:255], v50 offset:1024\n"
      "ds_read_b128 v[36:39], v50 offset:2048\n"
      "s_waitcnt lgkmcnt(0)\n"
      "v_add3_u32 v32, v32, v248, v249\n"
      "v_add3_u32 v32, v32, v250, v251\n"
      "v_add3_u32 v32, v32, v252, v253\n"
      "v_add3_u32 v32, v32, v254, v255\n"
      "v_add3_u32 v32, v32, v36, v37\n"
      "v_add3_u32 v32, v32, v38, v39\n"
      "s_nop 1\n"
      "v_add_u32_dpp v32, v32, v32 quad_perm:[1,0,3,2] row_mask:0xf bank_mask:0xf\n"
      "s_nop 1\n"
      "v_add_u32_dpp v32, v32, v32 quad_perm:[2,3,0,1] row_mask:0xf bank_mask:0xf\n"
      "s_nop 1\n"
      "v_add_u32_dpp v32, v32, v32 row_half_mirror row_mask:0xf bank_mask:0xf\n"
      "v_cmp_ne_u32 vcc, s72, v32\n"
      "s_sub_u32 s44, s44, 1\n"
      "s_cbranch_vccz L_bxp_%=\n"
      "s_cmp_gt_i32 s44, 0\n"
      "s_cbranch_scc1 L_sxp_%=\n"
      "s_branch L_bxp_%=\n"
      "L_sg_%=:\n"
      "buffer_load_dwordx4 v[8:11], v50, s[32:35], s93 offen offset:0 sc1\n"
      "buffer_load_dwordx4 v[12:15], v50, s[32:35], s93 offen offset:1024 sc1\n"
      "buffer_load_dwordx4 v[16:19], v50, s[32:35], s93 offen offset:2048 sc1\n"
      "buffer_load_dwordx4 v[20:23], v50, s[32:35], s93 offen offset:3072 sc1\n"
      "s_waitcnt vmcnt(0)\n"
      "v_add3_u32 v10, v10, v14, v18\n"
      "v_add_u32 v10, v10, v22\n"
      "v_cmp_ne_u32 vcc, s71, v10\n"
      "s_sub_u32 s44, s44, 1\n"
      "s_cbranch_vccz L_bg_%=\n"
      "s_cmp_gt_i32 s44, 0\n"
      "s_cbranch_scc1 L_sg_%=\n"
      "s_branch L_bg_%=\n"
      "L_shA0_%=:\n"
      "v_add_u32 v2, 4096, v7\n"
      "global_load_dwordx4 v[8:11], v2, s[10:11] offset:-4096 sc1\n"
      "global_load_dwordx4 v[16:19], v2, s[10:11] offset:-3072 sc1\n"
      "global_load_dwordx4 v[128:131], v2, s[10:11] offset:-2048 sc1\n"
      "v_add_u32 v2, 4096, v7\n"
      "global_load_dwordx4 v[12:15], v2, s[12:13] offset:-4096 sc1\n"
      "global_load_dwordx4 v[20:23], v2, s[12:13] offset:-3072 sc1\n"
      "global_load_dwordx4 v[132:135], v2, s[12:13] offset:-2048 sc1\n"
      "buffer_load_dword v36, v42, s[32:35], s63 offen offset:0 sc1\n"
      "buffer_load_dword v37, v42, s[32:35], s63 offen offset:256 sc1\n"
      "s_waitcnt vmcnt(0)\n"
      "v_add3_u32 v2, v8, v9, s30\n"
      "v_add3_u32 v2, v2, v10, v11\n"
      "v_add3_u32 v2, v2, v16, v17\n"
      "v_add3_u32 v2, v2, v18, v19\n"
      "v_add3_u32 v2, v2, v128, v129\n"
      "v_add3_u32 v2, v2, v130, v131\n"
      "v_xor_b32 v2, v2, v36\n"
      "v_add3_u32 v3, v12, v13, s30\n"
      "v_add3_u32 v3, v3, v14, v15\n"
      "v_add3_u32 v3, v3, v20, v21\n"
      "v_add3_u32 v3, v3, v22, v23\n"
      "v_add3_u32 v3, v3, v132, v133\n"
      "v_add3_u32 v3, v3, v134, v135\n"
      "v_xor_b32 v3, v3, v37\n"
      "v_or_b32 v2, v2, v3\n"
      "v_cmp_ne_u32 vcc, 0, v2\n"
      "s_sub_u32 s44, s44, 1\n"
      "s_cbranch_vccz L_bhA0_%=\n"
      "s_cmp_gt_i32 s44, 0\n"
      "s_cbranch_scc1 L_shA0_%=\n"
      "s_branch L_bhA0_%=\n"
      "L_shA1_%=:\n"
      "v_add_u32 v2, 4096, v7\n"
      "global_load_dwordx4 v[136:139], v2, s[10:11] offset:-1024 sc1\n"
      "global_load_dwordx4 v[144:147], v2, s[10:11] offset:0 sc1\n"
      "global_load_dwordx4 v[152:155], v2, s[10:11] offset:1024 sc1\n"
      "v_add_u32 v2, 4096, v7\n"
      "global_load_dwordx4 v[140:143], v2, s[12:13] offset:-1024 sc1\n"
      "global_load_dwordx4 v[148:151], v2, s[12:13] offset:0 sc1\n"
      "global_load_dwordx4 v[156:159], v2, s[12:13] offset:1024 sc1\n"
      "buffer_load_dword v38, v42, s[32:35], s63 offen offset:512 sc1\n"
      "buffer_load_dword v39, v42, s[32:35], s63 offen offset:768 sc1\n"
      "s_waitcnt vmcnt(0)\n"
      "v_add3_u32 v2, v136, v137, s30\n"
      "v_add3_u32 v2, v2, v138, v139\n"
      "v_add3_u32 v2, v2, v144, v145\n"
      "v_add3_u32 v2, v2, v146, v147\n"
      "v_add3_u32 v2, v2, v152, v153\n"
      "v_add3_u32 v2, v2, v154, v155\n"
      "v_xor_b32 v2, v2, v38\n"
      "v_add3_u32 v3, v140, v141, s30\n"
      "v_add3_u32 v3, v3, v142, v143\n"
      "v_add3_u32 v3, v3, v148, v149\n"
      "v_add3_u32 v3, v3, v150, v151\n"
      "v_add3_u32 v3, v3, v156, v157\n"
      "v_add3_u32 v3, v3, v158, v159\n"
      "v_xor_b32 v3, v3, v39\n"
      "v_or_b32 v2, v2, v3\n"
      "v_cmp_ne_u32 vcc, 0, v2\n"
      "s_sub_u32 s44, s44, 1\n"
      "s_cbranch_vccz L_bhA1_%=\n"
      "s_cmp_gt_i32 s44, 0\n"
      "s_cbranch_scc1 L_shA1_%=\n"
      "s_branch L_bhA1_%=\n"
      "L_shA2_%=:\n"
      "v_add_u32 v2, 10240, v7\n"
      "global_load_dwordx4 v[172:175], v2, s[10:11] offset:-4096 sc1\n"
      "global_load_dwordx4 v[164:167], v2, s[10:11] offset:-3072 sc1\n"
      "global_load_dwordx4 v[64:67], v2, s[10:11] offset:-2048 sc1\n"
      "v_add_u32 v2, 10240, v7\n"
      "global_load_dwordx4 v[160:163], v2, s[12:13] offset:-4096 sc1\n"
      "global_load_dwordx4 v[168:171], v2, s[12:13] offset:-3072 sc1\n"
      "global_load_dwordx4 v[68:71], v2, s[12:13] offset:-2048 sc1\n"
      "buffer_load_dword v60, v42, s[32:35], s63 offen offset:1024 sc1\n"
      "buffer_load_dword v61, v42, s[32:35], s63 offen offset:1280 sc1\n"
      "s_waitcnt vmcnt(0)\n"
      "v_add3_u32 v2, v172, v173, s30\n"
      "v_add3_u32 v2, v2, v174, v175\n"
      "v_add3_u32 v2, v2, v164, v165\n"
      "v_add3_u32 v2, v2, v166, v167\n"
      "v_add3_u32 v2, v2, v64, v65\n"
      "v_add3_u32 v2, v2, v66, v67\n"
      "v_xor_b32 v2, v2, v60\n"
      "v_add3_u32 v3, v160, v161, s30\n"
      "v_add3_u32 v3, v3, v162, v163\n"
      "v_add3_u32 v3, v3, v168, v169\n"
      "v_add3_u32 v3, v3, v170, v171\n"
      "v_add3_u32 v3, v3, v68, v69\n"
      "v_add3_u32 v3, v3, v70, v71\n"
      "v_xor_b32 v3, v3, v61\n"
      "v_or_b32 v2, v2, v3\n"
      "v_cmp_ne_u32 vcc, 0, v2\n"
      "s_sub_u32 s44, s44, 1\n"
      "s_cbranch_vccz L_bhA2_%=\n"
      "s_cmp_gt_i32 s44, 0\n"
      "s_cbranch_scc1 L_shA2_%=\n"
      "s_branch L_bhA2_%=\n"
      "L_shA3_%=:\n"
      "v_add_u32 v2, 10240, v7\n"
      "global_load_dwordx4 v[72:75], v2, s[10:11] offset:-1024 sc1\n"
      "global_load_dwordx4 v[80:83], v2, s[10:11] offset:0 sc1\n"
      "global_load_dwordx4 v[88:91], v2, s[10:11] offset:1024 sc1\n"
      "v_add_u32 v2, 10240, v7\n"
      "global_load_dwordx4 v[76:79], v2, s[12:13] offset:-1024 sc1\n"
      "global_load_dwordx4 v[84:87], v2, s[12:13] offset:0 sc1\n"
      "global_load_dwordx4 v[92:95], v2, s[12:13] offset:1024 sc1\n"
      "buffer_load_dword v62, v42, s[32:35], s63 offen offset:1536 sc1\n"
      "buffer_load_dword v63, v42, s[32:35], s63 offen offset:1792 sc1\n"
      "s_waitcnt vmcnt(0)\n"
      "v_add3_u32 v2, v72, v73, s30\n"
      "v_add3_u32 v2, v2, v74, v75\n"
      "v_add3_u32 v2, v2, v80, v81\n"
      "v_add3_u32 v2, v2, v82, v83\n"
      "v_add3_u32 v2, v2, v88, v89\n"
      "v_add3_u32 v2, v2, v90, v91\n"
      "v_xor_b32 v2, v2, v62\n"
      "v_add3_u32 v3, v76, v77, s30\n"
      "v_add3_u32 v3, v3, v78, v79\n"
      "v_add3_u32 v3, v3, v84, v85\n"
      "v_add3_u32 v3, v3, v86, v87\n"
      "v_add3_u32 v3, v3, v92, v93\n"
      "v_add3_u32 v3, v3, v94, v95\n"
      "v_xor_b32 v3, v3, v63\n"
      "v_or_b32 v2, v2, v3\n"
      "v_cmp_ne_u32 vcc, 0, v2\n"
      "s_sub_u32 s44, s44, 1\n"
      "s_cbranch_vccz L_bhA3_%=\n"
      "s_cmp_gt_i32 s44, 0\n"
      "s_cbranch_scc1 L_shA3_%=\n"
      "s_branch L_bhA3_%=\n"
      "L_shB0_%=:\n"
      "v_add_u32 v2, 102400, v7\n"
      "global_load_dwordx4 v[224:227], v2, s[10:11] offset:-4096 sc1\n"
      "global_load_dwordx4 v[228:231], v2, s[10:11] offset:-3072 sc1\n"
      "global_load_dwordx4 v[232:235], v2, s[10:11] offset:-2048 sc1\n"
      "v_add_u32 v2, 102400, v7\n"
      "global_load_dwordx4 v[236:239], v2, s[10:11] offset:-1024 sc1\n"
      "global_load_dwordx4 v[240:243], v2, s[10:11] offset:0 sc1\n"
      "global_load_dwordx4 v[244:247], v2, s[10:11] offset:1024 sc1\n"
      "buffer_load_dword v32, v42, s[32:35], s63 offen offset:2048 sc1\n"
      "buffer_load_dword v33, v42, s[32:35], s63 offen offset:2304 sc1\n"
      "s_waitcnt vmcnt(0)\n"
      "v_add3_u32 v2, v224, v225, s30\n"
      "v_add3_u32 v2, v2, v226, v227\n"
      "v_add3_u32 v2, v2, v228, v229\n"
      "v_add3_u32 v2, v2, v230, v231\n"
      "v_add3_u32 v2, v2, v232, v233\n"
      "v_add3_u32 v2, v2, v234, v235\n"
      "v_xor_b32 v2, v2, v32\n"
      "v_add3_u32 v3, v236, v237, s30\n"
      "v_add3_u32 v3, v3, v238, v239\n"
      "v_add3_u32 v3, v3, v240, v241\n"
      "v_add3_u32 v3, v3, v242, v243\n"
      "v_add3_u32 v3, v3, v244, v245\n"
      "v_add3_u32 v3, v3, v246, v247\n"
      "v_xor_b32 v3, v3, v33\n"
      "v_or_b32 v2, v2, v3\n"
      "v_cmp_ne_u32 vcc, 0, v2\n"
      "s_sub_u32 s44, s44, 1\n"
      "s_cbranch_vccz L_bhB0_%=\n"
      "s_cmp_gt_i32 s44, 0\n"
      "s_cbranch_scc1 L_shB0_%=\n"
      "s_branch L_bhB0_%=\n"
      "L_shB1_%=:\n"
      "v_add_u32 v2, 108544, v7\n"
      "global_load_dwordx4 v[8:11], v2, s[10:11] offset:-4096 sc1\n"
      "global_load_dwordx4 v[12:15], v2, s[10:11] offset:-3072 sc1\n"
      "global_load_dwordx4 v[16:19], v2, s[10:11] offset:-2048 sc1\n"
      "v_add_u32 v2, 108544, v7\n"
      "global_load_dwordx4 v[20:23], v2, s[10:11] offset:-1024 sc1\n"
      "global_load_dwordx4 v[128:131], v2, s[10:11] offset:0 sc1\n"
      "global_load_dwordx4 v[132:135], v2, s[10:11] offset:1024 sc1\n"
      "buffer_load_dword v34, v42, s[32:35], s63 offen offset:2560 sc1\n"
      "buffer_load_dword v35, v42, s[32:35], s63 offen offset:2816 sc1\n"
      "s_waitcnt vmcnt(0)\n"
      "v_add3_u32 v2, v8, v9, s30\n"
      "v_add3_u32 v2, v2, v10, v11\n"
      "v_add3_u32 v2, v2, v12, v13\n"
      "v_add3_u32 v2, v2, v14, v15\n"
      "v_add3_u32 v2, v2, v16, v17\n"
      "v_add3_u32 v2, v2, v18, v19\n"
      "v_xor_b32 v2, v2, v34\n"
      "v_add3_u32 v3, v20, v21, s30\n"
      "v_add3_u32 v3, v3, v22, v23\n"
      "v_add3_u32 v3, v3, v128, v129\n"
      "v_add3_u32 v3, v3, v130, v131\n"
      "v_add3_u32 v3, v3, v132, v133\n"
      "v_add3_u32 v3, v3, v134, v135\n"
      "v_xor_b32 v3, v3, v35\n"
      "v_or_b32 v2, v2, v3\n"
      "v_cmp_ne_u32 vcc, 0, v2\n"
      "s_sub_u32 s44, s44, 1\n"
      "s_cbranch_vccz L_bhB1_%=\n"
      "s_cmp_gt_i32 s44, 0\n"
      "s_cbranch_scc1 L_shB1_%=\n"
      "s_branch L_bhB1_%=\n"
      "L_shB2_%=:\n"
      "v_add_u32 v2, 102400, v7\n"
      "global_load_dwordx4 v[136:139], v2, s[12:13] offset:-4096 sc1\n"
      "global_load_dwordx4 v[140:143], v2, s[12:13] offset:-3072 sc1\n"
      "global_load_dwordx4 v[144:147], v2, s[12:13] offset:-2048 sc1\n"
      "v_add_u32 v2, 102400, v7\n"
      "global_load_dwordx4 v[148:151], v2, s[12:13] offset:-1024 sc1\n"
      "global_load_dwordx4 v[152:155], v2, s[12:13] offset:0 sc1\n"
      "global_load_dwordx4 v[156:159], v2, s[12:13] offset:1024 sc1\n"
      "buffer_load_dword v24, v42, s[32:35], s63 offen offset:3072 sc1\n"
      "buffer_load_dword v25, v42, s[32:35], s63 offen offset:3328 sc1\n"
      "s_waitcnt vmcnt(0)\n"
      "v_add3_u32 v2, v136, v137, s30\n"
      "v_add3_u32 v2, v2, v138, v139\n"
      "v_add3_u32 v2, v2, v140, v141\n"
      "v_add3_u32 v2, v2, v142, v143\n"
      "v_add3_u32 v2, v2, v144, v145\n"
      "v_add3_u32 v2, v2, v146, v147\n"
      "v_xor_b32 v2, v2, v24\n"
      "v_add3_u32 v3, v148, v149, s30\n"
      "v_add3_u32 v3, v3, v150, v151\n"
      "v_add3_u32 v3, v3, v152, v153\n"
      "v_add3_u32 v3, v3, v154, v155\n"
      "v_add3_u32 v3, v3, v156, v157\n"
      "v_add3_u32 v3, v3, v158, v159\n"
      "v_xor_b32 v3, v3, v25\n"
      "v_or_b32 v2, v2, v3\n"
      "v_cmp_ne_u32 vcc, 0, v2\n"
      "s_sub_u32 s44, s44, 1\n"
      "s_cbranch_vccz L_bhB2_%=\n"
      "s_cmp_gt_i32 s44, 0\n"
      "s_cbranch_scc1 L_shB2_%=\n"
      "s_branch L_bhB2_%=\n"
      "L_shB3_%=:\n"
      "v_add_u32 v2, 108544, v7\n"
      "global_load_dwordx4 v[172:175], v2, s[12:13] offset:-4096 sc1\n"
      "global_load_dwordx4 v[160:163], v2, s[12:13] offset:-3072 sc1\n"
      "global_load_dwordx4 v[164:167], v2, s[12:13] offset:-2048 sc1\n"
      "v_add_u32 v2, 108544, v7\n"
      "global_load_dwordx4 v[168:171], v2, s[12:13] offset:-1024 sc1\n"
      "global_load_dwordx4 v[64:67], v2, s[12:13] offset:0 sc1\n"
      "global_load_dwordx4 v[68:71], v2, s[12:13] offset:1024 sc1\n"
      "buffer_load_dword v26, v42, s[32:35], s63 offen offset:3584 sc1\n"
      "buffer_load_dword v27, v42, s[32:35], s63 offen offset:3840 sc1\n"
      "s_waitcnt vmcnt(0)\n"
      "v_add3_u32 v2, v172, v173, s30\n"
      "v_add3_u32 v2, v2, v174, v175\n"
      "v_add3_u32 v2, v2, v160, v161\n"
      "v_add3_u32 v2, v2, v162, v163\n"
      "v_add3_u32 v2, v2, v164, v165\n"
      "v_add3_u32 v2, v2, v166, v167\n"
      "v_xor_b32 v2, v2, v26\n"
      "v_add3_u32 v3, v168, v169, s30\n"
      "v_add3_u32 v3, v3, v170, v171\n"
      "v_add3_u32 v3, v3, v64, v65\n"
      "v_add3_u32 v3, v3, v66, v67\n"
      "v_add3_u32 v3, v3, v68, v69\n"
      "v_add3_u32 v3, v3, v70, v71\n"
      "v_xor_b32 v3, v3, v27\n"
      "v_or_b32 v2, v2, v3\n"
      "v_cmp_ne_u32 vcc, 0, v2\n"
      "s_sub_u32 s44, s44, 1\n"
      "s_cbranch_vccz L_bhB3_%=\n"
      "s_cmp_gt_i32 s44, 0\n"
      "s_cbranch_scc1 L_shB3_%=\n"
      "s_branch L_bhB3_%=\n"
      "L_sgb_%=:\n"
      "buffer_load_dwordx4 v[244:247], v53, s[32:35], s66 offen sc1\n"
      "s_waitcnt vmcnt(0)\n"
      "v_add_u32 v245, v245, v247\n"
      "v_cmp_ne_u32 vcc, s54, v245\n"
      "s_sub_u32 s44, s44, 1\n"
      "s_cbranch_vccz L_bgb_%=\n"
      "s_cmp_gt_i32 s44, 0\n"
      "s_cbranch_scc1 L_sgb_%=\n"
      "s_branch L_bgb_%=\n"
      : "+{s3}"(w), "+{v0}"(tid), "+{s[4:19]}"(s4), "+{s[20:27]}"(s20), "+{s[28:31]}"(s28), "+{s[36:39]}"(s36), "+{s[40:41]}"(tab), "+{s[52:59]}"(s52), "+{s[60:75]}"(s60),
        "+{s[76:91]}"(s76), "+{s[92:99]}"(s92), "+{v[232:235]}"(v232), "+{v1}"(v1), "+{v[192:195]}"(t[0]), "+{v[240:243]}"(t[1]), "+{v[196:199]}"(t[2]), "+{v[200:203]}"(t[3]), "+{v[204:207]}"(t[4]), "+{v[208:211]}"(t[5]), "+{v[212:215]}"(t[6]), "+{v[216:219]}"(t[7]), "+{v[220:223]}"(t[8]), "+{v[224:227]}"(t[9]), "+{v[236:239]}"(t[10]), "+{v[4:7]}"(t[11])
      : "{s2}"(blockIdx.x)
      : "memory", "scc", "vcc", "s0", "s1", "s32", "s33", "s34", "s35", "s42", "s43", "s44", "s45", "s46", "s47", "s48", "s49", "s50", "s51", "v2", "v3", "v8", "v9", "v10", "v11", "v12", "v13", "v14", "v15", "v16", "v17", "v18", "v19", "v20", "v21", "v22", "v23", "v24", "v25", "v26", "v27", "v28", "v29", "v30", "v31", "v32", "v33", "v34", "v35", "v36", "v37", "v38", "v39", "v40", "v41", "v42", "v43", "v44", "v45", "v46", "v47", "v48", "v49", "v50", "v51", "v52", "v53", "v54", "v55", "v56", "v57", "v58", "v59", "v60", "v61", "v62", "v63", "v64", "v65", "v66", "v67", "v68", "v69", "v70", "v71", "v72", "v73", "v74", "v75", "v76", "v77", "v78", "v79", "v80", "v81", "v82", "v83", "v84", "v85", "v86", "v87", "v88", "v89", "v90", "v91", "v92", "v93", "v94", "v95", "v96", "v97", "v98", "v99", "v100", "v101", "v102", "v103", "v104", "v105", "v106", "v107", "v108", "v109", "v110", "v111", "v112", "v113", "v114", "v115", "v116", "v117", "v118", "v119", "v120", "v121", "v122", "v123", "v124", "v125", "v126", "v127", "v128", "v129", "v130", "v131", "v132", "v133", "v134", "v135", "v136", "v137", "v138", "v139", "v140", "v141", "v142", "v143", "v144", "v145", "v146", "v147", "v148", "v149", "v150", "v151", "v152", "v153", "v154", "v155", "v156", "v157", "v158", "v159", "v160", "v161", "v162", "v163", "v164", "v165", "v166", "v167", "v168", "v169", "v170", "v171", "v172", "v173", "v174", "v175", "v176", "v177", "v178", "v179", "v180", "v181", "v182", "v183", "v184", "v185", "v186", "v187", "v188", "v189", "v190", "v191", "v228", "v229", "v230", "v231", "v244", "v245", "v246", "v247", "v248", "v249", "v250", "v251", "v252", "v253", "v254", "v255", "a0", "a1", "a2", "a3", "a4", "a5", "a6", "a7", "a8", "a9", "a10", "a11", "a12", "a13", "a14", "a15", "a16", "a17", "a18", "a19", "a20", "a21", "a22", "a23", "a24", "a25", "a26", "a27", "a28", "a29", "a30", "a31", "a32", "a33", "a34", "a35", "a36", "a37", "a38", "a39", "a40", "a41", "a42", "a43", "a44", "a45", "a46", "a47", "a48", "a49", "a50", "a51", "a52", "a53", "a54", "a55", "a56", "a57", "a58", "a59", "a60", "a61", "a62", "a63", "a64", "a65", "a66", "a67", "a68", "a69", "a70", "a71", "a72", "a73", "a74", "a75", "a76", "a77", "a78", "a79", "a80", "a81", "a82", "a83", "a84", "a85", "a86", "a87", "a88", "a89", "a90", "a91", "a92", "a93", "a94", "a95", "a96", "a97", "a98", "a99", "a100", "a101", "a102", "a103", "a104", "a105", "a106", "a107", "a108", "a109", "a110", "a111", "a112", "a113", "a114", "a115", "a116", "a117", "a118", "a119", "a120", "a121", "a122", "a123", "a124", "a125", "a126", "a127", "a128", "a129", "a130", "a131", "a132", "a133", "a134", "a135", "a136", "a137", "a138", "a139", "a140", "a141", "a142", "a143", "a144", "a145", "a146", "a147", "a148", "a149", "a150", "a151", "a152", "a153", "a154", "a155", "a156", "a157", "a158", "a159", "a160", "a161", "a162", "a163", "a164", "a165", "a166", "a167", "a168", "a169", "a170", "a171", "a172", "a173", "a174", "a175", "a176", "a177", "a178", "a179", "a180", "a181", "a182", "a183", "a184", "a185", "a186", "a187", "a188", "a189", "a190", "a191", "a192", "a193", "a194", "a195", "a196", "a197", "a198", "a199", "a200", "a201", "a202", "a203", "a204", "a205", "a206", "a207", "a208", "a209", "a210", "a211", "a212", "a213", "a214", "a215", "a216", "a217", "a218", "a219", "a220", "a221", "a222", "a223", "a224", "a225", "a226", "a227", "a228", "a229", "a230", "a231", "a232", "a233", "a234", "a235", "a236", "a237", "a238", "a239", "a240", "a241", "a242", "a243", "a244", "a245", "a246", "a247", "a248", "a249", "a250", "a251", "a252", "a253", "a254", "a255");
}

extern "C" void kernel_launch(void* const* d_in, const int* in_sizes, int n_in, void* d_out, int out_size,
                              void* d_ws, size_t ws_size, hipStream_t stream) {
  mega<<<256, 256, 0, stream>>>((const u16*)d_in[0], (const u16*)d_in[1], (const u16*)d_in[2], (const u16*)d_in[3],
                                (const u16*)d_in[4], (const u16*)d_in[5], (const u16*)d_in[7], (const u16*)d_in[8],
                                (const u16*)d_in[9], (const u16*)d_in[10], (const u16*)d_in[11], (char*)d_ws, (u16*)d_out);
}
```

```cpp
#include <hip/hip_runtime.h>
#include <stdint.h>

typedef unsigned short u16;
typedef __attribute__((ext_vector_type(4))) unsigned int u32x4;
typedef __attribute__((ext_vector_type(8))) unsigned int u32x8;
typedef __attribute__((ext_vector_type(16))) unsigned int u32x16;
static constexpr int D_ = 640, F_ = 1536, RS_ = 4224;

static constexpr unsigned WS_FW = 20480;
static constexpr unsigned WS_FD = WS_FW + 61440;
static constexpr unsigned WS_STAT = 147456;
static constexpr unsigned WS_PART = 262144;
static constexpr unsigned WS_MOD = 786432;
static constexpr unsigned WS_KVT = 1u << 20;
static constexpr unsigned WS_QT = 4u << 20;
static constexpr unsigned WS_X1T = 7u << 20;
static constexpr unsigned WS_CK = 2752512;
static constexpr unsigned WS_WP = 10u << 20;
static constexpr unsigned WS_WD = 14u << 20;
static constexpr unsigned WS_HH = 16u << 20;
static constexpr unsigned WS_HC = 22u << 20;
static constexpr unsigned WS_ST2 = WS_HC + (512u << 10);
static constexpr unsigned WS_X1R = 23u << 20;
static constexpr unsigned WS_DUMMY = WS_HC + (768u << 10);

static constexpr int KCP = 4160, KCH = 4 * KCP;
static constexpr int XR_CH = 7680;
static constexpr int L_XI = 3 * KCH;
static constexpr int L_EX = 72960;
static constexpr int L_KV = L_EX, KVW = 6400;
static constexpr int L_KT = L_KV + 4 * KVW;
static constexpr int L_QT = L_KT + 84 * 144;
static constexpr int L_WR = L_QT + 10240;
static constexpr int L_GW = L_WR + 16384;
static constexpr int L_GS = L_GW + 20480;
static constexpr int L_SC = L_GS + 320;
static constexpr int L_XR = 110592, L_RS = 162304;
static constexpr int L_WD1 = 122880;
static_assert(L_KT == 98560 && L_QT == 110656 && L_WR == 120896 && L_GW == 137280, "");

struct __attribute__((aligned(256))) Tab { unsigned s[1024][64], l[4][6][64][4]; };
static constexpr Tab make_tab() {
  Tab t = {};
  for (int tid = 0; tid < 256; ++tid) {
    const int lane = tid & 63, ip = lane & 15, fq = lane >> 4, q4 = ip >> 2, p4 = lane & 3;
    const int fo = (ip & 3) * KCP + 2 * ((ip & 12) + 8 * (fq & 1) + 256 * (fq >> 1)), go = L_XI + (15 - ip + 8 * (fq >> 1)) * 96 + 16 * (fq & 1);
    const int w = tid >> 6, exw = L_EX + ip * 128 + (fq ^ (ip & 7)) * 16, rowA = 8 * tid;
    t.l[w][0][lane][0] = fo + (w >> 1) * KCH + (w & 1) * 256;
    t.l[w][0][lane][1] = go + (w >> 1) * XR_CH + (w & 1) * 384;
    t.l[w][0][lane][2] = fo + 2 * KCH + w * 128;
    t.l[w][0][lane][3] = go + 2 * XR_CH + w * 192;
    t.l[w][1][lane][0] = exw + w * 8192;
    t.l[w][1][lane][1] = L_EX + 32768 + w * 1024 + (lane >> 3) * 128 + ((lane & 7) ^ (lane >> 3)) * 16;
    t.l[w][1][lane][2] = L_XI + ((2040 - rowA) >> 5) * 96 + ((2040 - rowA) & 31) * 2;
    t.l[w][1][lane][3] = L_XI + 2 * XR_CH + ((252 - 4 * lane) >> 5) * 96 + ((252 - 4 * lane) & 31) * 2;
    t.l[w][2][lane][0] = lane < 8 ? lane * 8 : 0x80000000u;
    t.l[w][2][lane][1] = (ip >> 1) == 4 * (fq & 1) ? fq * 8 + (ip & 1) * 4 : 0x80000000u;
    t.l[w][2][lane][2] = fq * D_ * 128 + q4 * 32 + p4 * 8;
    t.l[w][2][lane][3] = (lane < 40 ? lane : 39) * 64;
    t.l[w][3][lane][0] = exw + 32768 + w * 4096;
    t.l[w][3][lane][1] = (lane & 1) * 2048 + (lane >> 1) * 16;
    t.l[w][3][lane][2] = p4 * 64 + (fq == 1 ? 256 * 32 : 0) + (q4 < 3 ? q4 : 4) * 20480;
    t.l[w][3][lane][3] = (exw + w * 8192) ^ 64;
    t.l[w][4][lane][0] = (tid >> 3) * 5120 + ((tid >> 2) & 1) * 64;
    t.l[w][4][lane][1] = fq * 5120 + ((lane >> 3) & 1) * 64;
    t.l[w][4][lane][2] = (exw + 32768 + w * 4096) ^ 64;
    t.l[w][4][lane][3] = (2 * (lane >> 5) + (q4 >> 1)) * (D_ * 128) + (2 * (fq & 1) + (q4 & 1)) * 32 + p4 * 8;
    t.l[w][5][lane][0] = L_GW + w * 2048 + (4 * fq + q4) * 64 + p4 * 8;
    t.l[w][5][lane][1] = (rowA >> 6) * D_ * 128 + ((lane >> 1) & 3) * 32 + (lane & 1) * 16;
    t.l[w][5][lane][2] = ((lane >> 1) & 3) == 2 * ((lane >> 3) & 1) ? w * 128 + (lane >> 3) * 8 + (lane & 1) * 4 : 0x80000000u;
    t.l[w][5][lane][3] = L_EX + w * 2048 + (lane >> 2) * 128 + ((2 * p4) ^ ((lane >> 2) & 7)) * 16;
  }
  for (int i = 0; i < 1024; ++i) {
    const int b = i >> 2, w = i & 3, xcd = b & 7, idx = b >> 3, L = xcd * 32 + idx, side = L & 1;
    const int ct = L & 7, rt = L >> 3, srow = xcd * 256 + idx * 8;
    const int chA = 128 + L, chH = L >> 1, rH = 512 * (side + (3 - 2 * side) * (w >> 1)) + 256 * (w & 1);
    const int id = idx < 29 ? idx : 29, gj = xcd * 30 + id, j_ks = 5 * (xcd >> 1) + (id * 4 + w) / 24, j_b = 24 * (xcd & 1) + (id * 4 + w) % 24;
    const int d_ks = gj / 5, d_cb = 4 * (gj % 5) + w;
    const unsigned dummy = WS_DUMMY + (i & 63) * 1024;
    const int bm = (xcd >> 2) * 8 + (idx >> 2), bn = (xcd & 3) * 4 + (idx & 3), bm2 = (xcd >> 1) * 8 + (idx >> 2), bn2 = (xcd & 1) * 4 + (idx & 3);
    t.s[i][0] = WS_KVT + (1984 - 64 * rt) * 2 + ct * 80 * RS_;
    t.s[i][1] = WS_KVT + WS_CK + (31 - rt) * RS_ + ct * 512;
    t.s[i][2] = idx < 30 ? WS_MOD + (gj >> 2) * 2048 + (gj & 3) * 64 : dummy;
    t.s[i][3] = 32 * j_ks * (F_ * 2) + (j_b - w) * 64;
    t.s[i][4] = WS_QT + rt * 128 + ct * 80 * RS_;
    t.s[i][5] = WS_QT + WS_CK + (rt * 40 + ct * 5) * 128;
    t.s[i][6] = srow * 1280;
    t.s[i][7] = WS_STAT + (srow >> 9) * 4096 + ((srow >> 3) & 63) * 16;
    t.s[i][8] = WS_MOD + (1920 + 640 * (w >> 1) + 32 * j_ks + 16 * (w & 1)) * 32;
    t.s[i][9] = idx < 30 ? WS_PART + j_ks * 24576 + j_b * 512 : dummy;
    t.s[i][10] = WS_KVT + WS_CK + chA / 20 * 128 + chA % 20 * 4;
    t.s[i][11] = WS_KVT + WS_CK + chH / 20 * 128 + chH % 20 * 4;
    t.s[i][12] = idx < 30 ? WS_WP + (4 * j_b + 0) / 3 * 61440 + (j_ks * 3 + (4 * j_b + 0) % 3) * 1024 : dummy;
    t.s[i][13] = idx < 30 ? WS_WP + (4 * j_b + 1) / 3 * 61440 + (j_ks * 3 + (4 * j_b + 1) % 3) * 1024 : dummy;
    t.s[i][14] = idx < 30 ? WS_WP + (4 * j_b + 2) / 3 * 61440 + (j_ks * 3 + (4 * j_b + 2) % 3) * 1024 : dummy;
    t.s[i][15] = idx < 30 ? WS_WP + (4 * j_b + 3) / 3 * 61440 + (j_ks * 3 + (4 * j_b + 3) % 3) * 1024 : dummy;
    t.s[i][16] = WS_FW + (3 * (bn * 4 + w) / 4 < 46 ? 3 * (bn * 4 + w) / 4 : 46) * 1280;
    t.s[i][17] = WS_X1T + WS_CK + chH / 2 * 512 + chH % 2 * 64 + (rH >> 9) * 128 + ((rH >> 6) & 7) * 8;
    t.s[i][18] = WS_X1T + ((rH >> 6) * D_ + chH) * 128;
    t.s[i][19] = idx < 30 ? WS_FW + (j_b * 20 + j_ks) * 64 : dummy;
    t.s[i][20] = WS_QT + chH * RS_ + rH * 2;
    t.s[i][21] = WS_X1T + WS_CK + chA / 2 * 512 + chA % 2 * 64;
    t.s[i][22] = WS_STAT + (rH >> 9) * 4096 + ((rH >> 3) & 63) * 16;
    t.s[i][23] = WS_MOD + chH / 8 * 256 + chH % 8 * 8;
    t.s[i][24] = WS_QT + WS_CK + chA / 16 * 128 + chA % 16 * 4;
    t.s[i][25] = WS_QT + WS_CK + (rH >> 6) * 5120 + chH / 16 * 128 + chH % 16 * 4;
    t.s[i][26] = idx < 30 ? WS_WD + ((2 * d_cb / 5 * 48 + d_ks) * 5 + 2 * d_cb % 5) * 1024 : dummy;
    t.s[i][27] = idx < 30 ? WS_WD + (((2 * d_cb + 1) / 5 * 48 + d_ks) * 5 + (2 * d_cb + 1) % 5) * 1024 : dummy;
    t.s[i][28] = idx < 30 ? WS_FD + (d_cb * 48 + d_ks) * 64 : dummy;
    t.s[i][29] = WS_X1T + chA * 128;
    t.s[i][30] = WS_X1T + WS_CK + bm / 4 * 128 + bm % 4 * 16 + 4;
    t.s[i][31] = WS_WP + (bn * 4 + w) * 61440;
    t.s[i][32] = chA * 4096;
    t.s[i][33] = chH * 4096;
    t.s[i][34] = ((1792 - rH) >> 5) * 96;
    t.s[i][35] = (96 * (d_ks / 3) + 16 * (d_ks % 3 >> 1) + 2 * (d_ks % 3 & 1)) * (D_ * 2) + d_cb * 64;
    t.s[i][36] = (d_ks % 3 == 1 ? 30 : 18) * (D_ * 2) + ((chA / 8 * 256 + chA % 8 * 8 - chH / 8 * 256 - chH % 8 * 8) << 16);
    t.s[i][37] = ((chA >> 1) & 3) * 32;
    t.s[i][38] = WS_KVT + WS_CK + (chA + 256) / 20 * 128 + (chA + 256) % 20 * 4;
    t.s[i][39] = side;
    t.s[i][40] = WS_ST2 + (bm2 * 8 + bn2) * 1024 + w * 256;
    t.s[i][41] = WS_ST2 + (2 * bm + (w >> 1)) * 8192 + (w & 1) * 512;
    t.s[i][42] = L_XR + (w < 3 ? 20 * w : 56) * 128;
    t.s[i][43] = L_RS + (w & 1) * 512 + (w >> 1) * 256;
    t.s[i][48] = w;
    t.s[i][49] = WS_HH + (((2 * bm + (w >> 1)) * 4 + (w & 1)) * 48 + 3 * bn) * 1024;
    t.s[i][50] = WS_HC + (((2 * bm + (w >> 1)) * 4 + (bn >> 2)) * 8 + (bn & 3)) * 512 + (w & 1) * 256;
    t.s[i][51] = WS_HC + (bm2 * 4 + w) * 4096;
    t.s[i][52] = WS_FD + (((5 * bn2) >> 1) * 48 + 12 * w) * 64;
    t.s[i][53] = WS_PART + (bn * 4 + w) * 384;
    t.s[i][54] = WS_MOD + (3200 + bn2 * 80 + (w < 3 ? 16 * w : 48)) * 32;
    t.s[i][55] = WS_X1R + (bm2 * D_ + bn2 * 80 + (w < 3 ? 20 * w : 56)) * 128;
    t.s[i][56] = WS_X1T + WS_CK + (bn2 * 80 + (w < 3 ? 20 * w : 56)) * 256 + bm2 / 8 * 128 + bm2 % 8 * 8 + bm2 % 2 * 4;
    t.s[i][57] = WS_HH + (((2 * bm + (w >> 1)) * 4 + 2 + (w & 1)) * 48 + 3 * bn) * 1024;
    t.s[i][58] = WS_HC + (((2 * bm + (w >> 1)) * 4 + (bn >> 2)) * 8 + 4) * 512 + (4 * (w & 1) + (bn & 3)) * 256;
    t.s[i][59] = WS_X1T + bm * 163840;
    t.s[i][60] = WS_WD + (bn2 * 48 + 12 * w) * 5120;
    t.s[i][61] = w * 4096;
    t.s[i][62] = ((bm2 >> 1) * 128 + (bm2 & 1) * 32 + (w & 1) * 16) * (D_ * 2) + bn2 * 160 + (w >> 1) * 64;
    t.s[i][63] = WS_HH + (bm2 * 192 + 12 * w) * 1024;
  }
  return t;
}
static __device__ const Tab TAB = make_tab();

__global__ __launch_bounds__(256, 1) __attribute__((aligned(4096))) void mega(const u16* q, const u16* k, const u16* v, const u16* shift_k, const u16* ssm_k, const u16* ssm_D, const u16* w_gate,
                                               const u16* w_up, const u16* w_down, const u16* c, const u16* w_mod, char* ws, u16* out) {
  __shared__ __attribute__((aligned(16))) char lds[163840];
  int w = (int)(size_t)lds, tid = threadIdx.x, v1;
  const Tab* tab = &TAB;
  u32x16 s4, s60, s76;
  u32x8 s20, s52, s92;
  u32x4 s28, s36, v232;
  asm volatile(
      "s_load_dwordx16 s[4:19], s[0:1], 0x0\n"
      "s_load_dwordx8 s[20:27], s[0:1], 0x40\n"
      "s_load_dwordx2 s[28:29], s[0:1], 0x60\n"
      "v_readfirstlane_b32 s3, v0\n"
      "v_and_b32 v2, 63, v0\n"
      "s_lshl_b32 s42, s2, 4\n"
      "s_and_b32 s43, s2, 7\n"
      "v_mul_u32_u24 v3, 205, v2\n"
      "s_and_b32 s52, s2, 0xc0\n"
      "s_lshr_b32 s3, s3, 6\n"
      "v_lshlrev_b32 v8, 4, v2\n"
      "s_cmp_lt_u32 s3, 2\n"
      "s_cbranch_scc1 L_pf%=\n"
      "s_getpc_b64 s[54:55]\n"
      "s_and_b32 s47, s2, 0xf8\n"
      "s_and_b32 s54, s54, 0xffffff00\n"
      "s_add_u32 s56, s40, 0x40000\n"
      "s_addc_u32 s57, s41, 0\n"
      "s_lshl_b32 s47, s47, 10\n"
      "s_mov_b32 s53, 38656\n"
      "s_cmp_eq_u32 s3, 2\n"
      "s_cselect_b64 s[54:55], s[56:57], s[54:55]\n"
      "s_cmov_b32 s53, 24512\n"
      "s_cmp_gt_u32 s47, s53\n"
      "s_cbranch_scc1 L_pf%=\n"
      "v_lshl_add_u32 v9, v8, 3, s47\n"
      "s_mov_b32 m0, 160512\n"
      "v_min_u32 v9, s53, v9\n"
      "global_load_lds_dword v9, s[54:55]\n"
      "L_pf%=:\n"
      "s_lshl_b32 s47, s43, 8\n"
      "v_add_u32 v9, 13120, v3\n"
      "v_add_u32 v10, 26240, v3\n"
      "s_add_u32 s52, s52, s47\n"
      "s_lshl_b32 s47, s3, 4\n"
      "v_lshrrev_b32 v3, 11, v3\n"
      "v_lshrrev_b32 v9, 11, v9\n"
      "s_add_u32 s52, s52, s47\n"
      "s_movk_i32 s47, 1120\n"
      "v_lshrrev_b32 v10, 11, v10\n"
      "s_mulk_i32 s52, 1280\n"
      "s_bfe_u32 s54, s2, 0x30003\n"
      "v_mad_u32_u24 v232, v3, s47, v8\n"
      "v_mad_u32_u24 v233, v9, s47, v8\n"
      "v_mad_u32_u24 v234, v10, s47, v8\n"
      "s_mul_i32 s47, s54, 160\n"
      "s_mul_i32 s53, s3, 6400\n"
      "s_add_u32 s52, s52, s47\n"
      "v_subrev_u32 v233, 6656, v233\n"
      "v_subrev_u32 v234, 5632, v234\n"
      "s_sub_u32 s47, s52, 3840\n"
      "s_add_u32 s53, s53, 72960\n"
      "v_add_u32 v3, s47, v232\n"
      "s_add_u32 s47, s52, 3840\n"
      "s_add_u32 s55, s52, 7680\n"
      "s_mov_b32 s38, 2621440\n"
      "s_mov_b32 s39, 0x20000\n"
      "s_mulk_i32 s54, 640\n"
      "s_waitcnt lgkmcnt(0)\n"
      "s_mov_b32 m0, s53\n"
      "s_mov_b32 s36, s6\n"
      "s_and_b32 s37, s7, 0xffff\n"
      "buffer_load_dwordx4 v3, s[36:39], 0 offen lds\n"
      "s_add_u32 m0, s53, 1024\n"
      "s_nop 0\n"
      "buffer_load_dwordx4 v233, s[36:39], s47 offen lds\n"
      "s_add_u32 m0, s53, 2048\n"
      "v_add_u32 v9, s54, v8\n"
      "buffer_load_dwordx4 v234, s[36:39], s47 offen lds\n"
      "s_add_u32 m0, s53, 3072\n"
      "s_mov_b32 s36, s8\n"
      "s_and_b32 s37, s9, 0xffff\n"
      "buffer_load_dwordx4 v232, s[36:39], s52 offen lds\n"
      "s_add_u32 m0, s53, 4096\n"
      "s_nop 0\n"
      "buffer_load_dwordx4 v233, s[36:39], s55 offen lds\n"
      "s_add_u32 m0, s53, 5120\n"
      "s_mov_b32 exec_hi, 0\n"
      "buffer_load_dwordx4 v234, s[36:39], s55 offen lds\n"
      "s_add_u32 m0, s53, 5632\n"
      "s_mov_b32 exec_hi, 0xff\n"
      "global_load_lds_dwordx4 v9, s[10:11]\n"
      "s_mov_b32 exec_hi, -1\n"
      "s_load_dwordx2 s[30:31], s[26:27], 0x0\n"
      "s_lshl_b32 s51, s3, 2\n"
      "s_add_u32 s42, s42, s51\n"
      "s_lshl_b32 s42, s42, 6\n"
      "s_add_u32 s56, s40, s42\n"
      "s_addc_u32 s57, s41, 0\n"
      "s_load_dwordx16 s[60:75], s[56:57], 0x0\n"
      "s_load_dwordx16 s[76:91], s[56:57], 0x40\n"
      "s_load_dwordx8 s[92:99], s[56:57], 0x80\n"
      : "+{s3}"(w), "+{v0}"(tid), "=&{s[4:19]}"(s4), "=&{s[20:27]}"(s20), "=&{s[28:31]}"(s28), "=&{s[36:39]}"(s36), "=&{s[52:59]}"(s52), "=&{s[60:75]}"(s60), "=&{s[76:91]}"(s76),
        "=&{s[92:99]}"(s92), "=&{v[232:235]}"(v232), "=&{v1}"(v1)
      : "{s[0:1]}"(__builtin_amdgcn_kernarg_segment_ptr()), "{s2}"(blockIdx.x), "{s[40:41]}"(tab)
      : "memory", "scc", "s42", "s43", "s47", "s48", "s49", "s50", "s51", "v2", "v3", "v8", "v9", "v10", "v11", "v12", "v13");
  u32x4 t[12];
  const int lane = tid & 63, ip = lane & 15, fq = lane >> 4, q4 = ip >> 2, p4 = lane & 3;
  const int ck = 40 * w + (lane < 40 ? lane : 39), z7 = ((tid < 84 ? tid : 83) * 37) >> 8;
  const bool dg = fq == q4;
  const unsigned msk = dg ? ((ip & 1) ? 0xFFFF0000u : 0xFFFFu) : 0u, mk0 = (ip & 2) ? 0u : msk, mk1 = (ip & 2) ? msk : 0u;
  t[0][0] = dg && p4 == 0 ? 0x3F803F80u : 0u;
  t[0][1] = dg && p4 == 1 ? 0x3F803F80u : 0u;
  t[0][2] = dg && p4 == 2 ? 0x3F803F80u : 0u;
  t[0][3] = dg && p4 == 3 ? 0x3F803F80u : 0u;
  t[1][0] = (mk0 & 0x0D0C0D0Cu) ^ 0x0C0C0C0Cu;
  t[1][1] = (mk1 & 0x0D0C0D0Cu) ^ 0x0C0C0C0Cu;
  t[1][2] = (mk0 & 0x0F0E0F0Eu) ^ 0x0C0C0C0Cu;
  t[1][3] = (mk1 & 0x0F0E0F0Eu) ^ 0x0C0C0C0Cu;
  t[2][0] = L_KV + w * KVW + ip * 160 + fq * 8;
  t[2][1] = L_KV + w * KVW + 5632 + ip * 8;
  t[2][2] = mk0;
  t[2][3] = mk1;
  t[3][0] = L_KT + ip * 144 + (60 - 16 * w - 4 * fq) * 2;
  t[3][1] = L_KT + (20 * w + (lane >> 3)) * 144 + (lane & 7) * 16;
  t[3][2] = (20 * w + (lane >> 3)) * RS_ + (lane & 7) * 16;
  t[3][3] = lane & 4 ? 0x80000000u : w * 128 + (8 * (lane & 7) + (lane >> 3)) * 4;
  t[4][0] = L_GS + ((ck & ~31) + ((ck >> 2) & 3) * 8 + ((ck >> 4) & 1) * 4 + (ck & 3)) * 2;
  t[4][1] = L_GW + (4 * fq + q4) * 128 + p4 * 8 + ((w ^ (2 * fq + (q4 >> 1))) & 3) * 32;
  t[4][2] = L_GS + fq * 16;
  t[4][3] = lane < 16 ? (2 * w + (lane >> 3)) * 256 + (lane & 7) * 8 : 0x80000000u;
  t[5][0] = (lane & 12) == 4 ? 1 << 20 : 0;
  t[5][1] = lane & 8 ? 0u : ~0u;
  t[5][2] = (8 * w + fq) * (F_ * 2) + (ip ^ (2 * fq)) * 16;
  t[5][3] = (2 * w + (lane >> 5)) * 1280 + (lane & 31) * 16;
  t[6][0] = p4 * 64 + (lane >> 5) * 256 + ((lane >> 2) & 7) * 8;
  t[6][1] = L_WR + (fq >> 1) * 4096 + (fq & 1) * 1024 + q4 * 256 + (((2 * w + 1) ^ q4) & 7) * 32 + p4 * 8;
  t[6][2] = lane == 0 ? w * 1024 : 0x80000000u;
  t[6][3] = ((fq & 1 ? w * 64 + 512 : fq * 32 + 128 * w) + ip * 4) | ((unsigned)(fq & (fq >> 1 | w >> 1)) << 31);
  t[7][0] = (ip < 8 ? ip : 8 + p4) * KCP + 56;
  t[7][1] = z7 * (KCP - 56) + (tid < 84 ? tid : 83) * 8;
  t[7][2] = L_XI + 64 * 96 + (tid < 90 ? tid : 89) * 16;
  t[7][3] = L_WR + (fq >> 1) * 4096 + (fq & 1) * 1024 + q4 * 256 + (((2 * w) ^ q4) & 7) * 32 + p4 * 8;
  t[8][0] = L_SC + w * 32 + (lane >> 2) * 2;
  t[8][1] = (2 * (tid >> 4) + ((tid >> 2) & 1)) * (D_ * 128) + (2 * ((tid >> 3) & 1) + ((tid >> 1) & 1)) * 32 + (tid & 1) * 16;
  t[8][2] = (4 * fq + ((lane >> 2) & 1)) * (D_ * 2) + p4 * 16;
  t[8][3] = ((lane >> 3) ^ (lane >> 4)) & 1;
  t[9][0] = L_QT + (8 * fq + q4) * 160 + p4 * 8 + w * 32;
  t[9][1] = L_QT + (8 * fq + q4) * 160 + p4 * 8 + 128 + (w & 1) * 5120;
  t[9][2] = (16 * w + ip) * RS_ + fq * 16;
  t[9][3] = w < 2 ? (64 + ip) * RS_ + fq * 16 + w * 64 : 0x80000000u;
  t[10][0] = L_SC + (ip & 4 ? 0 : 64) + fq * 8;
  t[10][1] = (tid >> 3) * RS_;
  t[10][2] = L_XI + (tid >> 2) * 96 + (tid & 3) * 16;
  t[10][3] = (fq >> 1) * 256 + (fq & 1) * 64 + ip * 4;
  t[11][0] = lane * 8;
  t[11][1] = lane & 4 ? 0x80000000u : (16 * w + 4 * (lane & 7) + fq) * 512 + ((lane >> 3) & 1) * 64;
  t[11][2] = (4 * fq + q4) * 128 + ((2 * fq + (q4 >> 1)) & 3) * 32 + p4 * 8;
  t[11][3] = lane * 16;
  asm volatile(
      "v_and_b32 v2, 63, v0\n"
      "s_and_b32 s43, s2, 7\n"
      "v_mul_u32_u24 v3, 205, v2\n"
      "v_lshlrev_b32 v8, 4, v2\n"
      "s_lshr_b32 s48, s2, 3\n"
      "s_min_u32 s48, s48, 29\n"
      "s_mul_i32 s49, s43, 30\n"
      "v_lshrrev_b32 v12, 3, v2\n"
      "s_add_u32 s48, s48, s49\n"
      "v_xor_b32 v13, v12, v2\n"
      "s_and_b32 s49, s48, 3\n"
      "v_and_b32 v11, 1, v2\n"
      "s_lshl_b32 s49, s49, 2\n"
      "v_and_or_b32 v13, v13, 6, v11\n"
      "s_add_u32 s49, s49, s3\n"
      "v_mul_u32_u24 v12, 7680, v12\n"
      "s_lshr_b32 s48, s48, 2\n"
      "s_mul_i32 s50, s49, 307200\n"
      "s_lshl_b32 s48, s48, 7\n"
      "v_lshl_add_u32 v12, v13, 4, v12\n"
      "s_add_u32 s48, s48, s50\n"
      "s_mul_i32 s50, s3, 5120\n"
      "s_mulk_i32 s49, 80\n"
      "v_min_u32 v13, 39, v2\n"
      "v_add_u32 v12, s48, v12\n"
      "v_lshl_add_u32 v13, v13, 1, s49\n"
      "s_add_u32 m0, s50, 137280\n"
      "global_load_ushort v235, v13, s[22:23]\n"
      "global_load_lds_dwordx4 v12, s[24:25]\n"
      "s_add_u32 m0, m0, 1024\n"
      "v_add_u32 v12, 61440, v12\n"
      "global_load_lds_dwordx4 v12, s[24:25]\n"
      "s_add_u32 m0, m0, 1024\n"
      "v_add_u32 v12, 61440, v12\n"
      "global_load_lds_dwordx4 v12, s[24:25]\n"
      "s_add_u32 m0, m0, 1024\n"
      "v_add_u32 v12, 61440, v12\n"
      "global_load_lds_dwordx4 v12, s[24:25]\n"
      "s_add_u32 m0, m0, 1024\n"
      "v_add_u32 v12, 61440, v12\n"
      "global_load_lds_dwordx4 v12, s[24:25]\n"
      "s_mov_b32 s32, s26\n"
      "s_and_b32 s33, s27, 0xffff\n"
      "s_mov_b32 s34, 27262976\n"
      "s_mov_b32 s35, 0x20000\n"
      "v_lshlrev_b32 v54, 4, v0\n"
      "s_sub_u32 s18, s18, 2048\n"
      "s_subb_u32 s19, s19, 0\n"
      "v_mov_b32 v66, 0\n"
      "v_mov_b32 v67, 0\n"
      "v_mov_b32 v72, 0\n"
      "v_mov_b32 v73, 0\n"
      "v_mov_b32 v76, 0\n"
      "v_mov_b32 v77, 0\n"
      "v_mov_b32 v82, 0\n"
      "v_mov_b32 v83, 0\n"
      "v_mov_b32 v88, 0\n"
      "v_mov_b32 v89, 0\n"
      "v_mov_b32 v92, 0\n"
      "v_mov_b32 v93, 0\n"
      "v_mov_b32 v98, 0\n"
      "v_mov_b32 v99, 0\n"
      "v_mov_b32 v104, 0\n"
      "v_mov_b32 v105, 0\n"
      "v_mov_b32 v108, 0\n"
      "v_mov_b32 v109, 0\n"
      "v_mov_b32 v114, 0\n"
      "v_mov_b32 v115, 0\n"
      "v_mov_b32 v120, 0\n"
      "v_mov_b32 v121, 0\n"
      "v_mov_b32 v124, 0\n"
      "v_mov_b32 v125, 0\n"
      "v_mov_b32 v34, 0\n"
      "v_mov_b32 v35, 0\n"
      "v_and_b32 v32, 0x3f803f80, v198\n"
      "v_and_b32 v33, 0x3f803f80, v199\n"
      "v_and_b32 v36, 0x3f803f80, v198\n"
      "v_and_b32 v37, 0x3f803f80, v199\n"
      "s_mov_b32 s48, 0xaaaaaaaa\n"
      "s_mov_b32 s49, s48\n"
      "s_mov_b32 s50, 0xcccccccc\n"
      "s_mov_b32 s51, s50\n"
      "v_mov_b32 v128, 0\n"
      "v_mov_b32 v129, 0\n"
      "v_mov_b32 v130, 0\n"
      "v_mov_b32 v131, 0\n"
      "s_mov_b32 s44, 0x40000\n"
      "s_mov_b32 s46, 0\n"
      "s_mul_i32 s42, s3, 6144\n"
      "s_add_u32 s42, s42, 0x41000\n"
      "s_add_u32 s0, s40, s42\n"
      "s_addc_u32 s1, s41, 0\n"
      "ds_write_b64 v217, v[128:129]\n"
      "ds_write_b128 v218, v[128:131] offset:0\n"
      "ds_write_b128 v218, v[128:131] offset:7680\n"
      "ds_write_b128 v218, v[128:131] offset:15360\n"
      "s_waitcnt lgkmcnt(0)\n"
      "s_add_u32 s30, s30, 1\n"
      "s_addc_u32 s31, s31, 0\n"
      "v_mov_b32 v52, s30\n"
      "v_mov_b32 v53, s31\n"
      "s_and_b32 s42, s96, 0xffff\n"
      "s_lshl_b32 s43, s3, 11\n"
      "v_mad_u32_u24 v222, v223, s42, v222\n"
      "s_cmp_eq_u32 s3, 3\n"
      "s_cmov_b32 s43, 7680\n"
      "v_add_u32 v222, s95, v222\n"
      "s_add_u32 s43, s43, 49920\n"
      "s_mov_b32 s36, s12\n"
      "s_and_b32 s37, s13, 0xffff\n"
      "s_add_u32 s40, s60, 33792\n"
      "s_add_u32 s41, s60, 67584\n"
      "s_add_u32 s47, s92, 0x100000\n"
      "s_sub_u32 s54, s92, s93\n"
      "s_lshl_b32 s45, s3, 12\n"
      "v_and_b32 v39, s54, v209\n"
      "v_add_u32 v210, s63, v210\n"
      "v_add_u32 v208, v39, v208\n"
      "v_add_u32 v39, 11264, v210\n"
      "s_waitcnt vmcnt(6)\n"
      "ds_read_b64 v[244:245], v197 offset:0\n"
      "ds_read_b64 v[246:247], v197 offset:128\n"
      "ds_read_b64 v[248:249], v197 offset:256\n"
      "ds_read_b64 v[250:251], v197 offset:384\n"
      "ds_read_b64 v[252:253], v197 offset:512\n"
      "ds_read_b64 a[0:1], v196 offset:0\n"
      "ds_read_b64 a[2:3], v196 offset:32\n"
      "ds_read_b64 a[4:5], v196 offset:64\n"
      "ds_read_b64 a[6:7], v196 offset:96\n"
      "ds_read_b64 a[8:9], v196 offset:128\n"
      "ds_read_b64 a[10:11], v196 offset:160\n"
      "ds_read_b64 a[12:13], v196 offset:192\n"
      "ds_read_b64 a[14:15], v196 offset:224\n"
      "ds_read_b64 a[16:17], v196 offset:256\n"
      "ds_read_b64 a[18:19], v196 offset:288\n"
      "s_waitcnt lgkmcnt(10)\n"
      "ds_read_b64 a[20:21], v196 offset:320\n"
      "ds_read_b64 a[22:23], v196 offset:352\n"
      "ds_read_b64 a[24:25], v196 offset:384\n"
      "v_perm_b32 v64, v245, v245, v242\n"
      "v_perm_b32 v65, v245, v245, v243\n"
      "v_perm_b32 v80, v245, v245, v240\n"
      "v_perm_b32 v81, v245, v245, v241\n"
      "ds_read_b64 a[26:27], v196 offset:416\n"
      "ds_read_b64 a[28:29], v196 offset:448\n"
      "ds_read_b64 a[30:31], v196 offset:480\n"
      "v_perm_b32 v68, v247, v247, v242\n"
      "v_perm_b32 v69, v247, v247, v243\n"
      "v_perm_b32 v84, v247, v247, v240\n"
      "v_perm_b32 v85, v247, v247, v241\n"
      "ds_read_b64 a[32:33], v196 offset:512\n"
      "ds_read_b64 a[34:35], v196 offset:544\n"
      "ds_read_b64 a[36:37], v196 offset:576\n"
      "v_perm_b32 v70, v249, v249, v242\n"
      "v_perm_b32 v71, v249, v249, v243\n"
      "v_perm_b32 v86, v249, v249, v240\n"
      "v_perm_b32 v87, v249, v249, v241\n"
      "ds_read_b64 a[38:39], v196 offset:608\n"
      "ds_read_b64 a[40:41], v196 offset:3072\n"
      "ds_read_b64 a[42:43], v196 offset:3104\n"
      "v_perm_b32 v74, v251, v251, v242\n"
      "v_perm_b32 v75, v251, v251, v243\n"
      "v_perm_b32 v90, v251, v251, v240\n"
      "v_perm_b32 v91, v251, v251, v241\n"
      "ds_read_b64 a[44:45], v196 offset:3136\n"
      "ds_read_b64 a[46:47], v196 offset:3168\n"
      "ds_read_b64 a[48:49], v196 offset:3200\n"
      "v_perm_b32 v78, v253, v253, v242\n"
      "v_perm_b32 v79, v253, v253, v243\n"
      "v_perm_b32 v94, v253, v253, v240\n"
      "v_perm_b32 v95, v253, v253, v241\n"
      "s_waitcnt lgkmcnt(14)\n"
      "v_mfma_f32_16x16x32_bf16 v[128:131], a[0:3], v[64:67], 0\n"
      "v_perm_b32 v96, v244, v244, v242\n"
      "v_perm_b32 v97, v244, v244, v243\n"
      "v_mfma_f32_16x16x32_bf16 v[132:135], a[10:13], v[80:83], 0\n"
      "buffer_load_dwordx4 v[8:11], v54, s[36:39], s92 offen\n"
      "v_perm_b32 v112, v244, v244, v240\n"
      "v_perm_b32 v113, v244, v244, v241\n"
      "v_mfma_f32_16x16x32_bf16 v[140:143], a[0:3], v[66:69], 0\n"
      "v_perm_b32 v100, v246, v246, v242\n"
      "v_perm_b32 v101, v246, v246, v243\n"
      "v_mfma_f32_16x16x32_bf16 v[144:147], a[10:13], v[82:85], 0\n"
      "buffer_load_dwordx4 v[14:17], v54, s[36:39], s47 offen\n"
      "v_perm_b32 v116, v246, v246, v240\n"
      "v_perm_b32 v117, v246, v246, v241\n"
      "v_mfma_f32_16x16x32_bf16 v[152:155], a[4:7], v[70:73], 0\n"
      "v_perm_b32 v102, v248, v248, v242\n"
      "v_perm_b32 v103, v248, v248, v243\n"
      "v_mfma_f32_16x16x32_bf16 v[156:159], a[14:17], v[86:89], 0\n"
      "buffer_load_dwordx4 v[20:23], v54, s[36:39], s93 offen\n"
      "v_perm_b32 v118, v248, v248, v240\n"
      "v_perm_b32 v119, v248, v248, v241\n"
      "v_mfma_f32_16x16x32_bf16 v[164:167], a[4:7], v[72:75], 0\n"
      "v_perm_b32 v106, v250, v250, v242\n"
      "v_perm_b32 v107, v250, v250, v243\n"
      "v_mfma_f32_16x16x32_bf16 v[168:171], a[14:17], v[88:91], 0\n"
      "buffer_load_dwordx2 v[26:27], v208, s[36:39], s93 offen\n"
      "v_perm_b32 v122, v250, v250, v240\n"
      "v_perm_b32 v123, v250, v250, v241\n"
      "v_mfma_f32_16x16x32_bf16 v[176:179], a[6:9], v[76:79], 0\n"
      "v_perm_b32 v110, v252, v252, v242\n"
      "v_perm_b32 v111, v252, v252, v243\n"
      "v_mfma_f32_16x16x32_bf16 v[180:183], a[16:19], v[92:95], 0\n"
      "v_perm_b32 v126, v252, v252, v240\n"
      "v_perm_b32 v127, v252, v252, v241\n"
      "s_waitcnt lgkmcnt(0)\n"
      "v_cvt_pk_bf16_f32 v136, v128, v132\n"
      "v_cvt_pk_bf16_f32 v137, v129, v133\n"
      "v_cvt_pk_bf16_f32 v138, v130, v134\n"
      "v_cvt_pk_bf16_f32 v139, v131, v135\n"
      "v_cvt_pk_bf16_f32 v148, v140, v144\n"
      "v_cvt_pk_bf16_f32 v149, v141, v145\n"
      "v_mfma_f32_16x16x32_bf16 v[128:131], v[192:195], v[136:139], 0\n"
      "v_cvt_pk_bf16_f32 v150, v142, v146\n"
      "v_cvt_pk_bf16_f32 v151, v143, v147\n"
      "v_mfma_f32_16x16x32_bf16 v[132:135], a[20:23], v[96:99], 0\n"
      "s_add_u32 m0, s45, 120896\n"
      "s_nop 0\n"
      "global_load_lds_dwordx4 v210, s[16:17]\n"
      "v_cvt_pk_bf16_f32 v160, v152, v156\n"
      "v_cvt_pk_bf16_f32 v161, v153, v157\n"
      "v_mfma_f32_16x16x32_bf16 v[140:143], v[192:195], v[148:151], 0\n"
      "v_cvt_pk_bf16_f32 v162, v154, v158\n"
      "v_cvt_pk_bf16_f32 v163, v155, v159\n"
      "v_mfma_f32_16x16x32_bf16 v[144:147], a[20:23], v[98:101], 0\n"
      "global_load_lds_dwordx4 v39, s[16:17] offset:1024\n"
      "v_cvt_pk_bf16_f32 v172, v164, v168\n"
      "v_cvt_pk_bf16_f32 v173, v165, v169\n"
      "v_mfma_f32_16x16x32_bf16 v[152:155], v[192:195], v[160:163], 0\n"
      "v_cvt_pk_bf16_f32 v174, v166, v170\n"
      "v_cvt_pk_bf16_f32 v175, v167, v171\n"
      "v_mfma_f32_16x16x32_bf16 v[156:159], a[24:27], v[102:105], 0\n"
      "global_load_lds_dwordx4 v210, s[18:19] offset:2048\n"
      "v_cvt_pk_bf16_f32 v184, v176, v180\n"
      "v_cvt_pk_bf16_f32 v185, v177, v181\n"
      "v_mfma_f32_16x16x32_bf16 v[164:167], v[192:195], v[172:175], 0\n"
      "v_cvt_pk_bf16_f32 v186, v178, v182\n"
      "v_cvt_pk_bf16_f32 v187, v179, v183\n"
      "v_mfma_f32_16x16x32_bf16 v[168:171], a[24:27], v[104:107], 0\n"
      "global_load_lds_dwordx4 v39, s[18:19] offset:3072\n"
      "v_cvt_pk_bf16_f32 v136, v128, v132\n"
      "v_cvt_pk_bf16_f32 v137, v129, v133\n"
      "v_mfma_f32_16x16x32_bf16 v[176:179], v[192:195], v[184:187], 0\n"
      "v_cvt_pk_bf16_f32 v138, v130, v134\n"
      "v_cvt_pk_bf16_f32 v139, v131, v135\n"
      "v_mfma_f32_16x16x32_bf16 v[180:183], a[26:29], v[108:111], 0\n"
      "v_cvt_pk_bf16_f32 v148, v140, v144\n"
      "v_cvt_pk_bf16_f32 v149, v141, v145\n"
      "v_mfma_f32_16x16x32_bf16 v[128:131], v[192:195], v[136:139], 0\n"
      "v_cvt_pk_bf16_f32 v150, v142, v146\n"
      "v_cvt_pk_bf16_f32 v151, v143, v147\n"
      "v_mfma_f32_16x16x32_bf16 v[132:135], a[30:33], v[112:115], 0\n"
      "v_cvt_pk_bf16_f32 v160, v152, v156\n"
      "v_cvt_pk_bf16_f32 v161, v153, v157\n"
      "v_mfma_f32_16x16x32_bf16 v[140:143], v[192:195], v[148:151], 0\n"
      "v_cvt_pk_bf16_f32 v162, v154, v158\n"
      "v_cvt_pk_bf16_f32 v163, v155, v159\n"
      "v_mfma_f32_16x16x32_bf16 v[144:147], a[30:33], v[114:117], 0\n"
      "v_cvt_pk_bf16_f32 v172, v164, v168\n"
      "v_cvt_pk_bf16_f32 v173, v165, v169\n"
      "v_mfma_f32_16x16x32_bf16 v[152:155], v[192:195], v[160:163], 0\n"
      "v_cvt_pk_bf16_f32 v174, v166, v170\n"
      "v_cvt_pk_bf16_f32 v175, v167, v171\n"
      "v_mfma_f32_16x16x32_bf16 v[156:159], a[34:37], v[118:121], 0\n"
      "v_cvt_pk_bf16_f32 v184, v176, v180\n"
      "v_cvt_pk_bf16_f32 v185, v177, v181\n"
      "v_mfma_f32_16x16x32_bf16 v[164:167], v[192:195], v[172:175], 0\n"
      "v_cvt_pk_bf16_f32 v186, v178, v182\n"
      "v_cvt_pk_bf16_f32 v187, v179, v183\n"
      "v_mfma_f32_16x16x32_bf16 v[168:171], a[34:37], v[120:123], 0\n"
      "v_cvt_pk_bf16_f32 v136, v128, v132\n"
      "v_cvt_pk_bf16_f32 v137, v129, v133\n"
      "v_mfma_f32_16x16x32_bf16 v[176:179], v[192:195], v[184:187], 0\n"
      "v_cvt_pk_bf16_f32 v138, v130, v134\n"
      "v_cvt_pk_bf16_f32 v139, v131, v135\n"
      "v_mfma_f32_16x16x32_bf16 v[180:183], a[36:39], v[124:127], 0\n"
      "v_cvt_pk_bf16_f32 v148, v140, v144\n"
      "v_cvt_pk_bf16_f32 v149, v141, v145\n"
      "v_mfma_f32_16x16x32_bf16 v[128:131], v[192:195], v[136:139], 0\n"
      "v_cvt_pk_bf16_f32 v150, v142, v146\n"
      "v_cvt_pk_bf16_f32 v151, v143, v147\n"
      "v_mfma_f32_16x16x32_bf16 v[132:135], a[40:43], v[32:35], 0\n"
      "v_cvt_pk_bf16_f32 v160, v152, v156\n"
      "v_cvt_pk_bf16_f32 v161, v153, v157\n"
      "v_mfma_f32_16x16x32_bf16 v[140:143], v[192:195], v[148:151], 0\n"
      "v_cvt_pk_bf16_f32 v162, v154, v158\n"
      "v_cvt_pk_bf16_f32 v163, v155, v159\n"
      "v_mfma_f32_16x16x32_bf16 v[144:147], a[40:43], v[34:37], 0\n"
      "v_cvt_pk_bf16_f32 v172, v164, v168\n"
      "v_cvt_pk_bf16_f32 v173, v165, v169\n"
      "v_mfma_f32_16x16x32_bf16 v[152:155], v[192:195], v[160:163], 0\n"
      "v_cvt_pk_bf16_f32 v174, v166, v170\n"
      "v_cvt_pk_bf16_f32 v175, v167, v171\n"
      "v_mfma_f32_16x16x32_bf16 v[156:159], a[44:47], v[32:35], 0\n"
      "v_cvt_pk_bf16_f32 v184, v176, v180\n"
      "v_cvt_pk_bf16_f32 v185, v177, v181\n"
      "v_mfma_f32_16x16x32_bf16 v[164:167], v[192:195], v[172:175], 0\n"
      "v_cvt_pk_bf16_f32 v186, v178, v182\n"
      "v_cvt_pk_bf16_f32 v187, v179, v183\n"
      "v_mfma_f32_16x16x32_bf16 v[168:171], a[44:47], v[34:37], 0\n"
      "v_cvt_pk_bf16_f32 v128, 0, v128\n"
      "v_cvt_pk_bf16_f32 v129, 0, v129\n"
      "L_pg0_%=:\n"
      "s_cbranch_execz L_pg4_%=\n"
      "v_mfma_f32_16x16x32_bf16 v[176:179], v[192:195], v[184:187], 0\n"
      "v_cvt_pk_bf16_f32 v130, 0, v130\n"
      "v_cvt_pk_bf16_f32 v131, 0, v131\n"
      "v_mfma_f32_16x16x32_bf16 v[180:183], a[46:49], v[34:37], 0\n"
      "v_mul_f32 v128, v128, v132\n"
      "v_mul_f32 v129, v129, v133\n"
      "v_mul_f32 v130, v130, v134\n"
      "v_mul_f32 v131, v131, v135\n"
      "v_cvt_pk_bf16_f32 v136, v131, v130\n"
      "v_cvt_pk_bf16_f32 v137, v129, v128\n"
      "v_cvt_pk_bf16_f32 v140, 0, v140\n"
      "v_cvt_pk_bf16_f32 v141, 0, v141\n"
      "v_cvt_pk_bf16_f32 v142, 0, v142\n"
      "v_cvt_pk_bf16_f32 v143, 0, v143\n"
      "ds_write_b64 v200, v[136:137] offset:0\n"
      "v_mul_f32 v140, v140, v144\n"
      "v_mul_f32 v141, v141, v145\n"
      "v_mul_f32 v142, v142, v146\n"
      "v_mul_f32 v143, v143, v147\n"
      "v_cvt_pk_bf16_f32 v148, v143, v142\n"
      "v_cvt_pk_bf16_f32 v149, v141, v140\n"
      "v_cvt_pk_bf16_f32 v152, 0, v152\n"
      "v_cvt_pk_bf16_f32 v153, 0, v153\n"
      "v_cvt_pk_bf16_f32 v154, 0, v154\n"
      "v_cvt_pk_bf16_f32 v155, 0, v155\n"
      "ds_write_b64 v200, v[148:149] offset:2304\n"
      "v_mul_f32 v152, v152, v156\n"
      "v_mul_f32 v153, v153, v157\n"
      "v_mul_f32 v154, v154, v158\n"
      "v_mul_f32 v155, v155, v159\n"
      "v_cvt_pk_bf16_f32 v160, v155, v154\n"
      "v_cvt_pk_bf16_f32 v161, v153, v152\n"
      "v_cvt_pk_bf16_f32 v164, 0, v164\n"
      "v_cvt_pk_bf16_f32 v165, 0, v165\n"
      "v_cvt_pk_bf16_f32 v166, 0, v166\n"
      "v_cvt_pk_bf16_f32 v167, 0, v167\n"
      "ds_write_b64 v200, v[160:161] offset:4608\n"
      "v_mul_f32 v164, v164, v168\n"
      "v_mul_f32 v165, v165, v169\n"
      "v_mul_f32 v166, v166, v170\n"
      "v_mul_f32 v167, v167, v171\n"
      "v_cvt_pk_bf16_f32 v172, v167, v166\n"
      "v_cvt_pk_bf16_f32 v173, v165, v164\n"
      "v_cvt_pk_bf16_f32 v176, 0, v176\n"
      "v_cvt_pk_bf16_f32 v177, 0, v177\n"
      "v_cvt_pk_bf16_f32 v178, 0, v178\n"
      "v_cvt_pk_bf16_f32 v179, 0, v179\n"
      "ds_write_b64 v200, v[172:173] offset:6912\n"
      "v_mul_f32 v176, v176, v180\n"
      "v_mul_f32 v177, v177, v181\n"
      "v_mul_f32 v178, v178, v182\n"
      "v_mul_f32 v179, v179, v183\n"
      "v_cvt_pk_bf16_f32 v184, v179, v178\n"
      "v_cvt_pk_bf16_f32 v185, v177, v176\n"
      "ds_write_b64 v200, v[184:185] offset:9216\n"
      "s_waitcnt lgkmcnt(0)\n"
      "s_barrier\n"
      "ds_read_b128 v[64:67], v201 offset:0\n"
      "ds_read_b128 v[68:71], v201 offset:1152\n"
      "ds_read_b128 v[72:75], v201 offset:2304\n"
      "s_waitcnt lgkmcnt(2)\n"
      "buffer_store_dwordx4 v[64:67], v202, s[32:35], s60 offen offset:0 sc1\n"
      "v_add3_u32 v76, v64, v65, v66\n"
      "v_add_u32 v76, v76, v67\n"
      "s_waitcnt lgkmcnt(1)\n"
      "buffer_store_dwordx4 v[68:71], v202, s[32:35], s40 offen offset:0 sc1\n"
      "v_add3_u32 v77, v68, v69, v70\n"
      "v_add_u32 v77, v77, v71\n"
      "s_waitcnt lgkmcnt(0)\n"
      "s_mov_b32 exec_hi, 0\n"
      "buffer_store_dwordx4 v[72:75], v202, s[32:35], s41 offen offset:0 sc1\n"
      "s_mov_b32 exec_hi, -1\n"
      "v_add3_u32 v78, v72, v73, v74\n"
      "v_add_u32 v78, v78, v75\n"
      "v_add_u32_dpp v76, v76, v76 quad_perm:[1,0,3,2] row_mask:0xf bank_mask:0xf\n"
      "v_add_u32_dpp v77, v77, v77 quad_perm:[1,0,3,2] row_mask:0xf bank_mask:0xf\n"
      "v_add_u32_dpp v78, v78, v78 quad_perm:[1,0,3,2] row_mask:0xf bank_mask:0xf\n"
      "v_add_u32_dpp v76, v76, v76 quad_perm:[2,3,0,1] row_mask:0xf bank_mask:0xf\n"
      "v_add_u32_dpp v77, v77, v77 quad_perm:[2,3,0,1] row_mask:0xf bank_mask:0xf\n"
      "v_add_u32_dpp v78, v78, v78 quad_perm:[2,3,0,1] row_mask:0xf bank_mask:0xf\n"
      "v_add_u32_dpp v76, v76, v76 row_half_mirror row_mask:0xf bank_mask:0xf\n"
      "v_add_u32_dpp v77, v77, v77 row_half_mirror row_mask:0xf bank_mask:0xf\n"
      "v_add_u32_dpp v78, v78, v78 row_half_mirror row_mask:0xf bank_mask:0xf\n"
      "v_cndmask_b32 v76, v76, v77, s[48:49]\n"
      "v_cndmask_b32 v76, v76, v78, s[50:51]\n"
      "v_sub_u32 v76, s30, v76\n"
      "buffer_store_dword v76, v203, s[32:35], s61 offen offset:0 sc1\n"
      "s_waitcnt vmcnt(12)\n"
      "v_lshlrev_b32 v38, 16, v235\n"
      "v_mul_f32 v172, 0xbfb8aa3b, v38\n"
      "v_exp_f32 v172, v172\n"
      "s_nop 0\n"
      "v_add_f32 v172, 1.0, v172\n"
      "v_rcp_f32 v172, v172\n"
      "s_nop 0\n"
      "v_mul_f32 v38, v38, v172\n"
      "v_cvt_pk_bf16_f32 v38, v38, v38\n"
      "ds_write_b16 v204, v38\n"
      "s_waitcnt lgkmcnt(0)\n"
      "s_barrier\n"
      "ds_read_b128 v[144:147], v206 offset:0\n"
      "ds_read_b64_tr_b16 v[148:149], v205 offset:0\n"
      "ds_read_b64_tr_b16 v[150:151], v205 offset:2048\n"
      "ds_read_b128 v[152:155], v206 offset:64\n"
      "ds_read_b64_tr_b16 v[156:157], v205 offset:4096\n"
      "ds_read_b64_tr_b16 v[158:159], v205 offset:6144\n"
      "ds_read_b128 v[160:163], v206 offset:128\n"
      "ds_read_b64_tr_b16 v[164:165], v205 offset:8192\n"
      "ds_read_b64_tr_b16 v[166:167], v205 offset:10240\n"
      "ds_read_b128 v[168:171], v206 offset:192\n"
      "ds_read_b64_tr_b16 v[172:173], v205 offset:12288\n"
      "ds_read_b64_tr_b16 v[174:175], v205 offset:14336\n"
      "ds_read_b128 v[176:179], v206 offset:256\n"
      "ds_read_b64_tr_b16 v[180:181], v205 offset:16384\n"
      "ds_read_b64_tr_b16 v[182:183], v205 offset:18432\n"
      "s_waitcnt lgkmcnt(12)\n"
      "v_mfma_f32_16x16x32_bf16 v[184:187], v[144:147], v[148:151], 0\n"
      "s_waitcnt lgkmcnt(9)\n"
      "v_mfma_f32_16x16x32_bf16 v[184:187], v[152:155], v[156:159], v[184:187]\n"
      "s_waitcnt lgkmcnt(6)\n"
      "v_mfma_f32_16x16x32_bf16 v[184:187], v[160:163], v[164:167], v[184:187]\n"
      "s_waitcnt lgkmcnt(3)\n"
      "v_mfma_f32_16x16x32_bf16 v[184:187], v[168:171], v[172:175], v[184:187]\n"
      "s_waitcnt lgkmcnt(0)\n"
      "v_mfma_f32_16x16x32_bf16 v[184:187], v[176:179], v[180:183], v[184:187]\n"
      "s_nop 7\n"
      "v_mov_b32 v185, s30\n"
      "buffer_store_dwordx2 v[184:185], v207, s[32:35], s62 offen offset:0 sc1\n"
      "s_mul_i32 s42, s3, 2560\n"
      "s_add_u32 m0, s42, 110656\n"
      "s_mov_b32 s36, s4\n"
      "s_and_b32 s37, s5, 0xffff\n"
      "buffer_load_dwordx4 v232, s[36:39], s52 offen lds\n"
      "s_add_u32 m0, m0, 1024\n"
      "s_nop 0\n"
      "buffer_load_dwordx4 v233, s[36:39], s55 offen lds\n"
      "s_add_u32 m0, m0, 1024\n"
      "s_mov_b32 exec_hi, 0\n"
      "buffer_load_dwordx4 v234, s[36:39], s55 offen lds\n"
      "s_mov_b32 exec_hi, -1\n"
      "buffer_load_dwordx4 v[112:115], v211, s[36:39], s66 offen\n"
      "buffer_load_dwordx4 v[116:119], v211, s[36:39], s66 offen offset:512\n"
      "v_bfe_u32 v2, v54, 1, 8\n"
      "v_sub_u32 v2, v211, v2\n"
      "buffer_load_dwordx2 v[120:121], v2, s[36:39], s66 offen offset:1024\n"
      "s_and_b32 s43, s2, 7\n"
      "s_lshr_b32 s42, s2, 3\n"
      "s_lshl_b32 s47, s43, 5\n"
      "s_add_u32 s42, s47, s42\n"
      "s_and_b32 s54, s42, -4\n"
      "s_lshl_b32 s47, s42, 1\n"
      "s_and_b32 s47, s47, -4\n"
      "s_load_dword s53, s[14:15], s54\n"
      "s_load_dword s58, s[14:15], s47 offset:0x100\n"
      "s_load_dword s59, s[14:15], s47 offset:0x300\n"
      "s_min_u32 s54, s3, 2\n"
      "s_lshl_b32 s54, s54, 10\n"
      "s_lshr_b32 s10, s42, 1\n"
      "s_lshl_b32 s47, s42, 12\n"
      "s_lshl_b32 s10, s10, 12\n"
      "s_add_u32 s47, s47, s54\n"
      "s_add_u32 s54, s54, s10\n"
      "s_load_dwordx2 s[6:7], s[12:13], s47 offset:0x80400\n"
      "s_add_u32 s47, s47, 0x100000\n"
      "s_load_dwordx2 s[10:11], s[12:13], s54 offset:0x400\n"
      "s_load_dwordx2 s[8:9], s[12:13], s47 offset:0x80400\n"
      "global_load_dwordx4 v[40:43], v7, s[0:1] offset:-4096\n"
      "global_load_dwordx4 v[44:47], v7, s[0:1] offset:-3072\n"
      "global_load_dwordx4 v[48:51], v7, s[0:1] offset:-2048\n"
      "global_load_dwordx4 v[56:59], v7, s[0:1] offset:-1024\n"
      "global_load_dwordx4 v[60:63], v7, s[0:1] offset:0\n"
      "global_load_dwordx4 v[28:31], v7, s[0:1] offset:1024\n"
      "s_mov_b64 exec, 0\n"
      "s_cmp_eq_u32 s3, 0\n"
      "s_cbranch_scc1 L_pg0_%=\n"
      "s_cmp_eq_u32 s3, 1\n"
      "s_cbranch_scc1 L_pg1_%=\n"
      "s_cmp_eq_u32 s3, 2\n"
      "s_cbranch_scc1 L_pg2_%=\n"
      "s_branch L_pg3_%=\n"
      "L_pgr_%=:\n"
      "s_mov_b64 exec, -1\n"
      "s_waitcnt vmcnt(6)\n"
      "s_waitcnt lgkmcnt(0)\n"
      "s_barrier\n"
      "ds_read_b64_tr_b16 v[64:65], v224 offset:0\n"
      "ds_read_b64_tr_b16 v[66:67], v224 offset:640\n"
      "ds_read_b64_tr_b16 v[68:69], v224 offset:5120\n"
      "ds_read_b64_tr_b16 v[70:71], v224 offset:5760\n"
      "ds_read_b64_tr_b16 v[72:73], v225 offset:0\n"
      "ds_read_b64_tr_b16 v[74:75], v225 offset:640\n"
      "ds_read_b64_tr_b16 v[144:145], v219 offset:0\n"
      "ds_read_b64_tr_b16 v[146:147], v219 offset:8192\n"
      "ds_read_b64_tr_b16 v[148:149], v219 offset:2048\n"
      "ds_read_b64_tr_b16 v[150:151], v219 offset:10240\n"
      "ds_read_b64_tr_b16 v[152:153], v213 offset:0\n"
      "ds_read_b64_tr_b16 v[154:155], v213 offset:8192\n"
      "ds_read_b64_tr_b16 v[156:157], v213 offset:2048\n"
      "ds_read_b64_tr_b16 v[158:159], v213 offset:10240\n"
      "v_mov_b32 v12, s6\n"
      "v_mov_b32 v13, s7\n"
      "v_mov_b32 v18, s8\n"
      "v_mov_b32 v19, s9\n"
      "v_mov_b32 v24, s10\n"
      "v_mov_b32 v25, s11\n"
      "v_and_b32 v104, 48, v7\n"
      "v_xor_b32 v104, 48, v104\n"
      "v_lshlrev_b64 v[26:27], v104, v[26:27]\n"
      "v_mov_b32_dpp v12, v8 wave_shl:1 row_mask:0xf bank_mask:0xf\n"
      "v_mov_b32_dpp v13, v9 wave_shl:1 row_mask:0xf bank_mask:0xf\n"
      "v_mov_b32_dpp v18, v14 wave_shl:1 row_mask:0xf bank_mask:0xf\n"
      "v_mov_b32_dpp v19, v15 wave_shl:1 row_mask:0xf bank_mask:0xf\n"
      "v_mov_b32_dpp v24, v20 wave_shl:1 row_mask:0xf bank_mask:0xf\n"
      "v_mov_b32_dpp v25, v21 wave_shl:1 row_mask:0xf bank_mask:0xf\n"
      "v_mov_b32 v122, 0\n"
      "v_mov_b32 v123, 0\n"
      "v_mov_b32 v96, 0x3f803f80\n"
      "v_dot2c_f32_bf16 v122, v112, v96\n"
      "v_dot2c_f32_bf16 v123, v112, v112\n"
      "v_dot2c_f32_bf16 v122, v113, v96\n"
      "v_dot2c_f32_bf16 v123, v113, v113\n"
      "v_dot2c_f32_bf16 v122, v114, v96\n"
      "v_dot2c_f32_bf16 v123, v114, v114\n"
      "v_dot2c_f32_bf16 v122, v115, v96\n"
      "v_dot2c_f32_bf16 v123, v115, v115\n"
      "v_dot2c_f32_bf16 v122, v116, v96\n"
      "v_dot2c_f32_bf16 v123, v116, v116\n"
      "v_dot2c_f32_bf16 v122, v117, v96\n"
      "v_dot2c_f32_bf16 v123, v117, v117\n"
      "v_dot2c_f32_bf16 v122, v118, v96\n"
      "v_dot2c_f32_bf16 v123, v118, v118\n"
      "v_dot2c_f32_bf16 v122, v119, v96\n"
      "v_dot2c_f32_bf16 v123, v119, v119\n"
      "v_dot2c_f32_bf16 v122, v120, v96\n"
      "v_dot2c_f32_bf16 v123, v120, v120\n"
      "v_dot2c_f32_bf16 v122, v121, v96\n"
      "v_dot2c_f32_bf16 v123, v121, v121\n"
      "s_lshr_b32 s40, s92, 12\n"
      "s_lshr_b32 s41, s93, 12\n"
      "s_mulk_i32 s40, 4224\n"
      "s_mulk_i32 s41, 4224\n"
      "s_add_u32 s40, s40, 0x100000\n"
      "s_add_u32 s41, s41, 0x100000\n"
      "buffer_load_dwordx4 v[128:131], v54, s[32:35], s40 offen sc1\n"
      "s_add_u32 s40, s40, 1081344\n"
      "buffer_load_dwordx4 v[136:139], v54, s[32:35], s41 offen sc1\n"
      "buffer_load_dwordx4 v[132:135], v54, s[32:35], s40 offen sc1\n"
      "buffer_load_dword v140, v237, s[32:35], s70 offen sc1\n"
      "buffer_load_dword v141, v237, s[32:35], s98 offen sc1\n"
      "buffer_load_dword v142, v237, s[32:35], s71 offen sc1\n"
      "s_waitcnt lgkmcnt(0)\n"
      "ds_write_b128 v54, v[10:13] offset:12544\n"
      "v_alignbit_b32 v160, v9, v8, 16\n"
      "v_alignbit_b32 v161, v10, v9, 16\n"
      "v_alignbit_b32 v162, v11, v10, 16\n"
      "v_alignbit_b32 v163, v12, v11, 16\n"
      "s_mov_b32 vcc_lo, 0x33333333\n"
      "s_mov_b32 vcc_hi, 0x33333333\n"
      "v_cndmask_b32_dpp v80, v148, v144, vcc quad_perm:[0,1,0,1] row_mask:0xf bank_mask:0xf\n"
      "v_cndmask_b32_dpp v81, v149, v145, vcc quad_perm:[0,1,0,1] row_mask:0xf bank_mask:0xf\n"
      "v_cndmask_b32_dpp v82, v150, v146, vcc quad_perm:[0,1,0,1] row_mask:0xf bank_mask:0xf\n"
      "v_cndmask_b32_dpp v83, v151, v147, vcc quad_perm:[0,1,0,1] row_mask:0xf bank_mask:0xf\n"
      "ds_write_b128 v54, v[160:163] offset:64\n"
      "v_alignbit_b32 v164, v10, v9, 16\n"
      "v_alignbit_b32 v165, v11, v10, 16\n"
      "v_alignbit_b32 v166, v12, v11, 16\n"
      "v_alignbit_b32 v167, v13, v12, 16\n"
      "v_cndmask_b32_dpp v88, v156, v152, vcc quad_perm:[0,1,0,1] row_mask:0xf bank_mask:0xf\n"
      "v_cndmask_b32_dpp v89, v157, v153, vcc quad_perm:[0,1,0,1] row_mask:0xf bank_mask:0xf\n"
      "v_cndmask_b32_dpp v90, v158, v154, vcc quad_perm:[0,1,0,1] row_mask:0xf bank_mask:0xf\n"
      "v_cndmask_b32_dpp v91, v159, v155, vcc quad_perm:[0,1,0,1] row_mask:0xf bank_mask:0xf\n"
      "s_not_b64 vcc, vcc\n"
      "v_cndmask_b32_dpp v84, v144, v148, vcc quad_perm:[2,3,2,3] row_mask:0xf bank_mask:0xf\n"
      "ds_write_b128 v54, v[164:167] offset:8384\n"
      "v_mov_b32 v168, v9\n"
      "v_mov_b32 v169, v10\n"
      "v_mov_b32 v170, v11\n"
      "v_mov_b32 v171, v12\n"
      "v_cndmask_b32_dpp v85, v145, v149, vcc quad_perm:[2,3,2,3] row_mask:0xf bank_mask:0xf\n"
      "v_cndmask_b32_dpp v86, v146, v150, vcc quad_perm:[2,3,2,3] row_mask:0xf bank_mask:0xf\n"
      "v_cndmask_b32_dpp v87, v147, v151, vcc quad_perm:[2,3,2,3] row_mask:0xf bank_mask:0xf\n"
      "v_cndmask_b32_dpp v92, v152, v156, vcc quad_perm:[2,3,2,3] row_mask:0xf bank_mask:0xf\n"
      "v_cndmask_b32_dpp v93, v153, v157, vcc quad_perm:[2,3,2,3] row_mask:0xf bank_mask:0xf\n"
      "v_cndmask_b32_dpp v94, v154, v158, vcc quad_perm:[2,3,2,3] row_mask:0xf bank_mask:0xf\n"
      "ds_write_b128 v54, v[168:171] offset:4224\n"
      "v_cndmask_b32_dpp v95, v155, v159, vcc quad_perm:[2,3,2,3] row_mask:0xf bank_mask:0xf\n"
      "buffer_store_dwordx4 v[80:83], v7, s[32:35], s72 offen offset:0 sc1\n"
      "buffer_store_dwordx4 v[84:87], v7, s[32:35], s73 offen offset:0 sc1\n"
      "buffer_store_dwordx4 v[88:91], v7, s[32:35], s74 offen offset:0 sc1\n"
      "buffer_store_dwordx4 v[92:95], v7, s[32:35], s75 offen offset:0 sc1\n"
      "v_add3_u32 v76, v64, v65, v66\n"
      "ds_write_b128 v54, v[16:19] offset:29184\n"
      "v_alignbit_b32 v172, v15, v14, 16\n"
      "v_alignbit_b32 v173, v16, v15, 16\n"
      "v_alignbit_b32 v174, v17, v16, 16\n"
      "v_alignbit_b32 v175, v18, v17, 16\n"
      "v_add_u32 v76, v76, v67\n"
      "v_add3_u32 v77, v68, v69, v70\n"
      "v_add_f32_dpp v122, v122, v122 quad_perm:[1,0,3,2] row_mask:0xf bank_mask:0xf\n"
      "v_add_u32 v77, v77, v71\n"
      "v_add_f32_dpp v123, v123, v123 quad_perm:[1,0,3,2] row_mask:0xf bank_mask:0xf\n"
      "v_add3_u32 v78, v72, v73, v74\n"
      "ds_write_b128 v54, v[172:175] offset:16704\n"
      "v_alignbit_b32 v176, v16, v15, 16\n"
      "v_alignbit_b32 v177, v17, v16, 16\n"
      "v_alignbit_b32 v178, v18, v17, 16\n"
      "v_alignbit_b32 v179, v19, v18, 16\n"
      "v_add_f32_dpp v122, v122, v122 quad_perm:[2,3,0,1] row_mask:0xf bank_mask:0xf\n"
      "v_add_u32 v78, v78, v75\n"
      "v_add_f32_dpp v123, v123, v123 quad_perm:[2,3,0,1] row_mask:0xf bank_mask:0xf\n"
      "v_mov_b32 v79, v78\n"
      "v_add_f32_dpp v122, v122, v122 row_half_mirror row_mask:0xf bank_mask:0xf\n"
      "v_permlane32_swap_b32 v76, v77\n"
      "ds_write_b128 v54, v[176:179] offset:25024\n"
      "v_mov_b32 v180, v15\n"
      "v_mov_b32 v181, v16\n"
      "v_mov_b32 v182, v17\n"
      "v_mov_b32 v183, v18\n"
      "v_add_f32_dpp v123, v123, v123 row_half_mirror row_mask:0xf bank_mask:0xf\n"
      "v_permlane32_swap_b32 v78, v79\n"
      "v_add_f32_dpp v122, v122, v122 row_mirror row_mask:0xf bank_mask:0xf\n"
      "v_add_u32 v76, v76, v77\n"
      "v_add_f32_dpp v123, v123, v123 row_mirror row_mask:0xf bank_mask:0xf\n"
      "v_add_u32 v78, v78, v79\n"
      "ds_write_b128 v54, v[180:183] offset:20864\n"
      "v_mov_b32 v96, v122\n"
      "v_permlane16_swap_b32 v76, v78\n"
      "v_mov_b32 v97, v123\n"
      "v_add_u32 v76, v76, v78\n"
      "v_permlane16_swap_b32 v122, v96\n"
      "v_sub_u32 v76, s30, v76\n"
      "ds_write_b128 v54, v[22:25] offset:45824\n"
      "v_alignbit_b32 v160, v21, v20, 16\n"
      "v_alignbit_b32 v161, v22, v21, 16\n"
      "v_alignbit_b32 v162, v23, v22, 16\n"
      "v_alignbit_b32 v163, v24, v23, 16\n"
      "v_permlane16_swap_b32 v123, v97\n"
      "v_add_f32 v122, v122, v96\n"
      "v_add_f32 v123, v123, v97\n"
      "v_mul_f32 v122, 0x3acccccd, v122\n"
      "v_mul_f32 v123, 0x3acccccd, v123\n"
      "v_fma_f32 v123, -v122, v122, v123\n"
      "ds_write_b128 v54, v[160:163] offset:33344\n"
      "v_alignbit_b32 v164, v22, v21, 16\n"
      "v_alignbit_b32 v165, v23, v22, 16\n"
      "v_alignbit_b32 v166, v24, v23, 16\n"
      "v_alignbit_b32 v167, v25, v24, 16\n"
      "v_max_f32 v123, 0, v123\n"
      "v_cvt_pk_bf16_f32 v123, 0, v123\n"
      "v_add_f32 v123, 0x3727c5ac, v123\n"
      "v_cvt_pk_bf16_f32 v123, 0, v123\n"
      "v_sqrt_f32 v123, v123\n"
      "v_readlane_b32 s42, v122, 32\n"
      "ds_write_b128 v54, v[164:167] offset:41664\n"
      "v_mov_b32 v168, v21\n"
      "v_mov_b32 v169, v22\n"
      "v_mov_b32 v170, v23\n"
      "v_mov_b32 v171, v24\n"
      "v_cvt_pk_bf16_f32 v123, 0, v123\n"
      "v_rcp_f32 v96, v123\n"
      "v_mov_b32 v99, s30\n"
      "v_cvt_pk_bf16_f32 v98, v122, s42\n"
      "v_readlane_b32 s42, v96, 32\n"
      "s_nop 1\n"
      "v_mov_b32 v97, s42\n"
      "ds_write_b128 v54, v[168:171] offset:37504\n"
      "buffer_store_dwordx4 v[96:99], v214, s[32:35], s67 offen offset:0 sc1\n"
      "buffer_store_dwordx4 v[64:67], v226, s[32:35], s64 offen offset:0 sc1\n"
      "buffer_store_dwordx4 v[68:71], v226, s[32:35], s64 offen offset:64 sc1\n"
      "buffer_store_dwordx4 v[72:75], v227, s[32:35], s64 offen offset:0 sc1\n"
      "buffer_store_dword v76, v215, s[32:35], s65 offen offset:0 sc1\n"
      "ds_write_b64 v216, v[26:27]\n"
      "s_waitcnt vmcnt(9)\n"
      "v_add3_u32 v143, v128, v129, v130\n"
      "v_add3_u32 v143, v143, v131, v132\n"
      "v_add3_u32 v143, v143, v133, v134\n"
      "v_add3_u32 v143, v143, v135, v136\n"
      "v_add3_u32 v143, v143, v137, v138\n"
      "v_add_u32 v143, v143, v139\n"
      "v_add3_u32 v140, v140, v141, v142\n"
      "s_mul_i32 s47, s30, 3\n"
      "v_add_u32_dpp v143, v143, v143 quad_perm:[1,0,3,2] row_mask:0xf bank_mask:0xf\n"
      "s_nop 1\n"
      "v_add_u32_dpp v143, v143, v143 quad_perm:[2,3,0,1] row_mask:0xf bank_mask:0xf\n"
      "s_nop 1\n"
      "v_add_u32_dpp v143, v143, v143 row_half_mirror row_mask:0xf bank_mask:0xf\n"
      "v_add_u32 v143, v143, v140\n"
      "v_cmp_ne_u32 vcc, s47, v143\n"
      "s_cbranch_vccnz L_psx_%=\n"
      "L_pbx_%=:\n"
      "ds_write_b128 v238, v[128:131] offset:0\n"
      "ds_write_b128 v238, v[132:135] offset:7680\n"
      "ds_write_b128 v238, v[136:139] offset:15360\n"
      "s_mul_i32 s42, s3, 2048\n"
      "s_add_u32 m0, s42, 137280\n"
      "s_waitcnt lgkmcnt(0)\n"
      "s_barrier\n"
      "s_cmp_lg_u32 s99, 0\n"
      "s_cbranch_scc1 L_cv_b%=\n"
      "ds_read_b128 v[160:163], v43 offset:4608\n"
      "ds_read_b64 v[112:113], v42 offset:0\n"
      "ds_read_b64 v[114:115], v42 offset:8\n"
      "ds_read_b64 v[148:149], v42 offset:3072\n"
      "ds_read_b64 v[150:151], v42 offset:3080\n"
      "ds_read_b128 v[172:175], v43 offset:0\n"
      "ds_read_b128 v[168:171], v43 offset:1536\n"
      "ds_read_b64 v[124:125], v42 offset:1024\n"
      "ds_read_b64 v[126:127], v42 offset:1032\n"
      "ds_read_b128 v[164:167], v43 offset:3072\n"
      "ds_read_b64 v[136:137], v42 offset:2048\n"
      "ds_read_b64 v[138:139], v42 offset:2056\n"
      "ds_read_b64 v[116:117], v42 offset:32\n"
      "ds_read_b64 v[118:119], v42 offset:40\n"
      "ds_read_b64 v[128:129], v42 offset:1056\n"
      "ds_read_b64 v[130:131], v42 offset:1064\n"
      "ds_read_b64 v[140:141], v42 offset:2080\n"
      "ds_read_b64 v[142:143], v42 offset:2088\n"
      "ds_read_b64 v[152:153], v42 offset:3104\n"
      "ds_read_b64 v[154:155], v42 offset:3112\n"
      "s_waitcnt lgkmcnt(8)\n"
      "v_mfma_f32_16x16x32_bf16 v[96:99], v[112:115], v[160:163], 0\n"
      "ds_read_b128 v[176:179], v43 offset:4640\n"
      "ds_read_b128 v[180:183], v43 offset:3104\n"
      "v_mfma_f32_16x16x32_bf16 v[104:107], v[148:151], v[160:163], 0\n"
      "ds_read_b128 v[184:187], v43 offset:1568\n"
      "v_mfma_f32_16x16x32_bf16 v[104:107], v[112:115], v[172:175], v[104:107]\n"
      "ds_read_b128 v[188:191], v43 offset:32\n"
      "v_mfma_f32_16x16x32_bf16 v[104:107], v[124:127], v[168:171], v[104:107]\n"
      "ds_read_b64 v[120:121], v42 offset:64\n"
      "ds_read_b64 v[122:123], v42 offset:72\n"
      "v_mfma_f32_16x16x32_bf16 v[104:107], v[136:139], v[164:167], v[104:107]\n"
      "ds_read_b64 v[132:133], v42 offset:1088\n"
      "s_add_u32 s22, s20, 60416\n"
      "s_addc_u32 s23, s21, 0\n"
      "v_mov_b64 v[8:9], v[80:81]\n"
      "s_waitcnt lgkmcnt(7)\n"
      "v_mfma_f32_16x16x32_bf16 v[100:103], v[116:119], v[160:163], 0\n"
      "ds_read_b64 v[134:135], v42 offset:1096\n"
      "v_mov_b64 v[10:11], v[82:83]\n"
      "v_mfma_f32_16x16x32_bf16 v[108:111], v[152:155], v[160:163], 0\n"
      "ds_read_b64 v[144:145], v42 offset:2112\n"
      "v_mov_b64 v[12:13], v[84:85]\n"
      "v_mfma_f32_16x16x32_bf16 v[108:111], v[116:119], v[172:175], v[108:111]\n"
      "ds_read_b64 v[146:147], v42 offset:2120\n"
      "v_mov_b64 v[14:15], v[86:87]\n"
      "v_mfma_f32_16x16x32_bf16 v[108:111], v[128:131], v[168:171], v[108:111]\n"
      "ds_read_b64 v[156:157], v42 offset:3136\n"
      "v_mov_b64 v[16:17], v[88:89]\n"
      "v_mfma_f32_16x16x32_bf16 v[108:111], v[140:143], v[164:167], v[108:111]\n"
      "ds_read_b64 v[158:159], v42 offset:3144\n"
      "v_mov_b64 v[18:19], v[90:91]\n"
      "s_waitcnt lgkmcnt(8)\n"
      "v_mfma_f32_16x16x32_bf16 v[96:99], v[116:119], v[176:179], v[96:99]\n"
      "ds_read_b128 v[160:163], v43 offset:4704\n"
      "ds_read_b128 v[164:167], v43 offset:3168\n"
      "s_waitcnt vmcnt(5)\n"
      "buffer_store_dwordx2 v[52:53], v48, s[32:35], s79 offen sc1\n"
      "v_mov_b64 v[20:21], v[92:93]\n"
      "v_mfma_f32_16x16x32_bf16 v[104:107], v[152:155], v[176:179], v[104:107]\n"
      "ds_read_b128 v[168:171], v43 offset:1632\n"
      "v_mov_b64 v[22:23], v[94:95]\n"
      "v_mov_b32 v2, v212\n"
      "v_mfma_f32_16x16x32_bf16 v[104:107], v[116:119], v[188:191], v[104:107]\n"
      "ds_read_b128 v[172:175], v43 offset:96\n"
      "v_mov_b32 v3, v220\n"
      "v_mfma_f32_16x16x32_bf16 v[104:107], v[128:131], v[184:187], v[104:107]\n"
      "ds_read_b64 v[112:113], v42 offset:96\n"
      "ds_read_b64 v[114:115], v42 offset:104\n"
      "s_mov_b32 s45, s68\n"
      "v_mfma_f32_16x16x32_bf16 v[104:107], v[140:143], v[180:183], v[104:107]\n"
      "ds_read_b64 v[124:125], v42 offset:1120\n"
      "s_lshl_b32 s42, s99, 4\n"
      "s_waitcnt lgkmcnt(7)\n"
      "v_mfma_f32_16x16x32_bf16 v[100:103], v[120:123], v[176:179], v[100:103]\n"
      "ds_read_b64 v[126:127], v42 offset:1128\n"
      "s_lshr_b32 s58, s58, s42\n"
      "v_mfma_f32_16x16x32_bf16 v[108:111], v[156:159], v[176:179], v[108:111]\n"
      "ds_read_b64 v[136:137], v42 offset:2144\n"
      "s_lshr_b32 s59, s59, s42\n"
      "v_mfma_f32_16x16x32_bf16 v[108:111], v[120:123], v[188:191], v[108:111]\n"
      "ds_read_b64 v[138:139], v42 offset:2152\n"
      "s_lshr_b32 s42, s93, 8\n"
      "v_mfma_f32_16x16x32_bf16 v[108:111], v[132:135], v[184:187], v[108:111]\n"
      "ds_read_b64 v[148:149], v42 offset:3168\n"
      "s_and_b32 s42, s42, 16\n"
      "s_lshr_b32 s25, s53, s42\n"
      "v_mfma_f32_16x16x32_bf16 v[108:111], v[144:147], v[180:183], v[108:111]\n"
      "ds_read_b64 v[150:151], v42 offset:3176\n"
      "s_lshl_b32 s14, s58, 16\n"
      "s_waitcnt lgkmcnt(8)\n"
      "v_mfma_f32_16x16x32_bf16 v[96:99], v[120:123], v[160:163], v[96:99]\n"
      "ds_read_b128 v[176:179], v43 offset:4736\n"
      "ds_read_b128 v[180:183], v43 offset:3200\n"
      "s_lshl_b32 s15, s59, 16\n"
      "v_mfma_f32_16x16x32_bf16 v[104:107], v[156:159], v[160:163], v[104:107]\n"
      "ds_read_b128 v[184:187], v43 offset:1664\n"
      "s_lshl_b32 s25, s25, 16\n"
      "v_mfma_f32_16x16x32_bf16 v[104:107], v[120:123], v[172:175], v[104:107]\n"
      "ds_read_b128 v[188:191], v43 offset:128\n"
      "s_mul_i32 s24, s30, 10\n"
      "v_mfma_f32_16x16x32_bf16 v[104:107], v[132:135], v[168:171], v[104:107]\n"
      "ds_read_b64 v[116:117], v42 offset:128\n"
      "ds_read_b64 v[118:119], v42 offset:136\n"
      "s_lshr_b32 s42, s93, 8\n"
      "v_mfma_f32_16x16x32_bf16 v[104:107], v[144:147], v[164:167], v[104:107]\n"
      "ds_read_b64 v[128:129], v42 offset:1152\n"
      "s_and_b32 s42, s42, 0x60\n"
      "s_waitcnt lgkmcnt(7)\n"
      "v_mfma_f32_16x16x32_bf16 v[100:103], v[112:115], v[160:163], v[100:103]\n"
      "ds_read_b64 v[130:131], v42 offset:1160\n"
      "v_xor_b32 v36, s97, v221\n"
      "v_xor_b32 v37, s42, v63\n"
      "v_mfma_f32_16x16x32_bf16 v[108:111], v[148:151], v[160:163], v[108:111]\n"
      "ds_read_b64 v[140:141], v42 offset:2176\n"
      "v_xor_b32 v50, s42, v50\n"
      "v_mfma_f32_16x16x32_bf16 v[108:111], v[112:115], v[172:175], v[108:111]\n"
      "ds_read_b64 v[142:143], v42 offset:2184\n"
      "v_xor_b32 v29, s97, v29\n"
      "v_mfma_f32_16x16x32_bf16 v[108:111], v[124:127], v[168:171], v[108:111]\n"
      "ds_read_b64 v[152:153], v42 offset:3200\n"
      "v_add_u32 v47, s94, v47\n"
      "v_mfma_f32_16x16x32_bf16 v[108:111], v[136:139], v[164:167], v[108:111]\n"
      "ds_read_b64 v[154:155], v42 offset:3208\n"
      "v_add_u32 v5, s90, v5\n"
      "s_waitcnt lgkmcnt(8)\n"
      "v_mfma_f32_16x16x32_bf16 v[96:99], v[112:115], v[176:179], v[96:99]\n"
      "ds_read_b128 v[160:163], v41 offset:4608\n"
      "ds_read_b64 v[120:121], v40 offset:0\n"
      "s_lshl_b32 s42, s3, 12\n"
      "v_mfma_f32_16x16x32_bf16 v[104:107], v[148:151], v[176:179], v[104:107]\n"
      "ds_read_b64 v[122:123], v40 offset:8\n"
      "ds_read_b128 v[164:167], v41 offset:3072\n"
      "s_add_u32 s42, s42, 147456\n"
      "v_mfma_f32_16x16x32_bf16 v[104:107], v[112:115], v[188:191], v[104:107]\n"
      "ds_read_b64 v[132:133], v40 offset:1024\n"
      "ds_read_b64 v[134:135], v40 offset:1032\n"
      "v_add_u32 v55, s42, v7\n"
      "s_lshr_b32 s43, s96, 16\n"
      "v_mfma_f32_16x16x32_bf16 v[104:107], v[124:127], v[184:187], v[104:107]\n"
      "ds_read_b128 v[168:171], v41 offset:1536\n"
      "ds_read_b64 v[144:145], v40 offset:2048\n"
      "s_add_u32 s55, s78, 0x1000000\n"
      "v_mfma_f32_16x16x32_bf16 v[104:107], v[136:139], v[180:183], v[104:107]\n"
      "ds_read_b64 v[146:147], v40 offset:2056\n"
      "ds_read_b128 v[172:175], v41 offset:0\n"
      "s_mov_b32 s59, s69\n"
      "s_waitcnt lgkmcnt(10)\n"
      "v_mfma_f32_16x16x32_bf16 v[100:103], v[116:119], v[176:179], v[100:103]\n"
      "ds_read_b64 v[156:157], v40 offset:3072\n"
      "s_lshr_b32 s53, s92, 12\n"
      "v_mfma_f32_16x16x32_bf16 v[108:111], v[152:155], v[176:179], v[108:111]\n"
      "ds_read_b64 v[158:159], v40 offset:3080\n"
      "s_mulk_i32 s53, 4224\n"
      "v_mfma_f32_16x16x32_bf16 v[108:111], v[116:119], v[188:191], v[108:111]\n"
      "s_add_u32 s53, s53, 0x400000\n"
      "s_add_u32 s54, s53, 1081344\n"
      "v_mfma_f32_16x16x32_bf16 v[108:111], v[128:131], v[184:187], v[108:111]\n"
      "s_mov_b32 exec_hi, 0\n"
      "v_add_u32 v58, s43, v58\n"
      "s_mov_b32 exec_hi, -1\n"
      "v_mfma_f32_16x16x32_bf16 v[108:111], v[140:143], v[180:183], v[108:111]\n"
      "s_waitcnt lgkmcnt(0)\n"
      "s_branch L_cv_e%=\n"
      "L_cv_b%=:\n"
      "ds_read_b128 v[164:167], v43 offset:3072\n"
      "ds_read_b64 v[112:113], v42 offset:0\n"
      "ds_read_b64 v[114:115], v42 offset:8\n"
      "ds_read_b128 v[160:163], v43 offset:4608\n"
      "ds_read_b64 v[124:125], v42 offset:1024\n"
      "ds_read_b64 v[126:127], v42 offset:1032\n"
      "ds_read_b64 v[136:137], v42 offset:2048\n"
      "ds_read_b64 v[138:139], v42 offset:2056\n"
      "ds_read_b128 v[168:171], v43 offset:1536\n"
      "ds_read_b64 v[116:117], v42 offset:32\n"
      "ds_read_b64 v[118:119], v42 offset:40\n"
      "ds_read_b64 v[128:129], v42 offset:1056\n"
      "ds_read_b64 v[130:131], v42 offset:1064\n"
      "ds_read_b64 v[140:141], v42 offset:2080\n"
      "ds_read_b64 v[142:143], v42 offset:2088\n"
      "s_waitcnt lgkmcnt(6)\n"
      "v_mfma_f32_16x16x32_bf16 v[96:99], v[112:115], v[164:167], 0\n"
      "ds_read_b128 v[176:179], v43 offset:4640\n"
      "v_mfma_f32_16x16x32_bf16 v[96:99], v[124:127], v[160:163], v[96:99]\n"
      "ds_read_b128 v[180:183], v43 offset:3104\n"
      "v_mfma_f32_16x16x32_bf16 v[104:107], v[124:127], v[164:167], 0\n"
      "ds_read_b128 v[184:187], v43 offset:1568\n"
      "v_mfma_f32_16x16x32_bf16 v[104:107], v[136:139], v[160:163], v[104:107]\n"
      "ds_read_b64 v[120:121], v42 offset:64\n"
      "v_mfma_f32_16x16x32_bf16 v[104:107], v[112:115], v[168:171], v[104:107]\n"
      "ds_read_b64 v[122:123], v42 offset:72\n"
      "s_add_u32 s22, s20, 60416\n"
      "s_addc_u32 s23, s21, 0\n"
      "v_mov_b64 v[8:9], v[80:81]\n"
      "s_waitcnt lgkmcnt(5)\n"
      "v_mfma_f32_16x16x32_bf16 v[100:103], v[116:119], v[164:167], 0\n"
      "ds_read_b64 v[132:133], v42 offset:1088\n"
      "v_mov_b64 v[10:11], v[82:83]\n"
      "v_mfma_f32_16x16x32_bf16 v[100:103], v[128:131], v[160:163], v[100:103]\n"
      "ds_read_b64 v[134:135], v42 offset:1096\n"
      "v_mov_b64 v[12:13], v[84:85]\n"
      "v_mfma_f32_16x16x32_bf16 v[108:111], v[128:131], v[164:167], 0\n"
      "ds_read_b64 v[144:145], v42 offset:2112\n"
      "v_mov_b64 v[14:15], v[86:87]\n"
      "v_mfma_f32_16x16x32_bf16 v[108:111], v[140:143], v[160:163], v[108:111]\n"
      "ds_read_b64 v[146:147], v42 offset:2120\n"
      "v_mov_b64 v[16:17], v[88:89]\n"
      "v_mfma_f32_16x16x32_bf16 v[108:111], v[116:119], v[168:171], v[108:111]\n"
      "v_mov_b64 v[18:19], v[90:91]\n"
      "s_waitcnt lgkmcnt(6)\n"
      "v_mfma_f32_16x16x32_bf16 v[96:99], v[116:119], v[180:183], v[96:99]\n"
      "ds_read_b128 v[160:163], v43 offset:4704\n"
      "s_waitcnt vmcnt(5)\n"
      "buffer_store_dwordx2 v[52:53], v48, s[32:35], s79 offen sc1\n"
      "v_mov_b64 v[20:21], v[92:93]\n"
      "v_mfma_f32_16x16x32_bf16 v[96:99], v[128:131], v[176:179], v[96:99]\n"
      "ds_read_b128 v[164:167], v43 offset:3168\n"
      "v_mov_b64 v[22:23], v[94:95]\n"
      "v_mov_b32 v2, v212\n"
      "v_mfma_f32_16x16x32_bf16 v[104:107], v[128:131], v[180:183], v[104:107]\n"
      "ds_read_b128 v[168:171], v43 offset:1632\n"
      "v_mov_b32 v3, v220\n"
      "v_mfma_f32_16x16x32_bf16 v[104:107], v[140:143], v[176:179], v[104:107]\n"
      "ds_read_b64 v[112:113], v42 offset:96\n"
      "s_mov_b32 s45, s68\n"
      "v_mfma_f32_16x16x32_bf16 v[104:107], v[116:119], v[184:187], v[104:107]\n"
      "ds_read_b64 v[114:115], v42 offset:104\n"
      "s_lshl_b32 s42, s99, 4\n"
      "s_waitcnt lgkmcnt(5)\n"
      "v_mfma_f32_16x16x32_bf16 v[100:103], v[120:123], v[180:183], v[100:103]\n"
      "L_pg1_%=:\n"
      "s_cbranch_execz L_pg5_%=\n"
      "ds_read_b64 v[124:125], v42 offset:1120\n"
      "s_lshr_b32 s58, s58, s42\n"
      "v_mfma_f32_16x16x32_bf16 v[100:103], v[132:135], v[176:179], v[100:103]\n"
      "ds_read_b64 v[126:127], v42 offset:1128\n"
      "s_lshr_b32 s59, s59, s42\n"
      "v_mfma_f32_16x16x32_bf16 v[108:111], v[132:135], v[180:183], v[108:111]\n"
      "ds_read_b64 v[136:137], v42 offset:2144\n"
      "s_lshr_b32 s42, s93, 8\n"
      "v_mfma_f32_16x16x32_bf16 v[108:111], v[144:147], v[176:179], v[108:111]\n"
      "ds_read_b64 v[138:139], v42 offset:2152\n"
      "s_and_b32 s42, s42, 16\n"
      "s_lshr_b32 s25, s53, s42\n"
      "v_mfma_f32_16x16x32_bf16 v[108:111], v[120:123], v[184:187], v[108:111]\n"
      "s_lshl_b32 s14, s58, 16\n"
      "s_waitcnt lgkmcnt(6)\n"
      "v_mfma_f32_16x16x32_bf16 v[96:99], v[120:123], v[164:167], v[96:99]\n"
      "ds_read_b128 v[176:179], v43 offset:4736\n"
      "s_lshl_b32 s15, s59, 16\n"
      "v_mfma_f32_16x16x32_bf16 v[96:99], v[132:135], v[160:163], v[96:99]\n"
      "ds_read_b128 v[180:183], v43 offset:3200\n"
      "s_lshl_b32 s25, s25, 16\n"
      "v_mfma_f32_16x16x32_bf16 v[104:107], v[132:135], v[164:167], v[104:107]\n"
      "ds_read_b128 v[184:187], v43 offset:1664\n"
      "s_mul_i32 s24, s30, 10\n"
      "v_mfma_f32_16x16x32_bf16 v[104:107], v[144:147], v[160:163], v[104:107]\n"
      "ds_read_b64 v[116:117], v42 offset:128\n"
      "s_lshr_b32 s42, s93, 8\n"
      "v_mfma_f32_16x16x32_bf16 v[104:107], v[120:123], v[168:171], v[104:107]\n"
      "ds_read_b64 v[118:119], v42 offset:136\n"
      "s_and_b32 s42, s42, 0x60\n"
      "s_waitcnt lgkmcnt(5)\n"
      "v_mfma_f32_16x16x32_bf16 v[100:103], v[112:115], v[164:167], v[100:103]\n"
      "ds_read_b64 v[128:129], v42 offset:1152\n"
      "v_xor_b32 v36, s97, v221\n"
      "v_xor_b32 v37, s42, v63\n"
      "v_mfma_f32_16x16x32_bf16 v[100:103], v[124:127], v[160:163], v[100:103]\n"
      "ds_read_b64 v[130:131], v42 offset:1160\n"
      "v_xor_b32 v50, s42, v50\n"
      "v_mfma_f32_16x16x32_bf16 v[108:111], v[124:127], v[164:167], v[108:111]\n"
      "ds_read_b64 v[140:141], v42 offset:2176\n"
      "v_xor_b32 v29, s97, v29\n"
      "v_mfma_f32_16x16x32_bf16 v[108:111], v[136:139], v[160:163], v[108:111]\n"
      "ds_read_b64 v[142:143], v42 offset:2184\n"
      "v_add_u32 v47, s94, v47\n"
      "v_mfma_f32_16x16x32_bf16 v[108:111], v[112:115], v[168:171], v[108:111]\n"
      "v_add_u32 v5, s90, v5\n"
      "s_waitcnt lgkmcnt(6)\n"
      "v_mfma_f32_16x16x32_bf16 v[96:99], v[112:115], v[180:183], v[96:99]\n"
      "ds_read_b128 v[160:163], v41 offset:4608\n"
      "ds_read_b64 v[120:121], v40 offset:0\n"
      "s_lshl_b32 s42, s3, 12\n"
      "v_mfma_f32_16x16x32_bf16 v[96:99], v[124:127], v[176:179], v[96:99]\n"
      "ds_read_b64 v[122:123], v40 offset:8\n"
      "ds_read_b128 v[164:167], v41 offset:3072\n"
      "s_add_u32 s42, s42, 147456\n"
      "v_mfma_f32_16x16x32_bf16 v[104:107], v[124:127], v[180:183], v[104:107]\n"
      "ds_read_b64 v[132:133], v40 offset:1024\n"
      "ds_read_b64 v[134:135], v40 offset:1032\n"
      "v_add_u32 v55, s42, v7\n"
      "s_lshr_b32 s43, s96, 16\n"
      "v_mfma_f32_16x16x32_bf16 v[104:107], v[136:139], v[176:179], v[104:107]\n"
      "ds_read_b128 v[168:171], v41 offset:1536\n"
      "ds_read_b64 v[144:145], v40 offset:2048\n"
      "s_add_u32 s55, s78, 0x1000000\n"
      "v_mfma_f32_16x16x32_bf16 v[104:107], v[112:115], v[184:187], v[104:107]\n"
      "ds_read_b64 v[146:147], v40 offset:2056\n"
      "ds_read_b128 v[172:175], v41 offset:0\n"
      "s_mov_b32 s59, s69\n"
      "s_waitcnt lgkmcnt(10)\n"
      "v_mfma_f32_16x16x32_bf16 v[100:103], v[116:119], v[180:183], v[100:103]\n"
      "ds_read_b64 v[156:157], v40 offset:3072\n"
      "s_lshr_b32 s53, s92, 12\n"
      "v_mfma_f32_16x16x32_bf16 v[100:103], v[128:131], v[176:179], v[100:103]\n"
      "ds_read_b64 v[158:159], v40 offset:3080\n"
      "s_mulk_i32 s53, 4224\n"
      "v_mfma_f32_16x16x32_bf16 v[108:111], v[128:131], v[180:183], v[108:111]\n"
      "s_add_u32 s53, s53, 0x400000\n"
      "s_add_u32 s54, s53, 1081344\n"
      "v_mfma_f32_16x16x32_bf16 v[108:111], v[140:143], v[176:179], v[108:111]\n"
      "s_mov_b32 exec_hi, 0\n"
      "v_add_u32 v58, s43, v58\n"
      "s_mov_b32 exec_hi, -1\n"
      "v_mfma_f32_16x16x32_bf16 v[108:111], v[116:119], v[184:187], v[108:111]\n"
      "s_waitcnt lgkmcnt(0)\n"
      "L_cv_e%=:\n"
      "ds_read_b64 v[112:113], v40 offset:32\n"
      "ds_read_b64 v[114:115], v40 offset:40\n"
      "s_add_u32 s8, s26, s91\n"
      "s_addc_u32 s9, s27, 0\n"
      "v_mfma_f32_16x16x32_bf16 v[64:67], v[120:123], v[160:163], 0\n"
      "global_load_lds_dwordx4 v222, s[20:21]\n"
      "ds_read_b64 v[124:125], v40 offset:1056\n"
      "v_mfma_f32_16x16x32_bf16 v[72:75], v[120:123], v[164:167], 0\n"
      "ds_read_b64 v[126:127], v40 offset:1064\n"
      "ds_read_b64 v[136:137], v40 offset:2080\n"
      "v_mfma_f32_16x16x32_bf16 v[72:75], v[132:135], v[160:163], v[72:75]\n"
      "ds_read_b64 v[138:139], v40 offset:2088\n"
      "v_mfma_f32_16x16x32_bf16 v[80:83], v[120:123], v[168:171], 0\n"
      "ds_read_b64 v[148:149], v40 offset:3104\n"
      "v_mfma_f32_16x16x32_bf16 v[80:83], v[132:135], v[164:167], v[80:83]\n"
      "ds_read_b64 v[150:151], v40 offset:3112\n"
      "v_mfma_f32_16x16x32_bf16 v[80:83], v[144:147], v[160:163], v[80:83]\n"
      "ds_write_b128 v56, v[96:99]\n"
      "ds_read_b128 v[176:179], v41 offset:4640\n"
      "ds_read_b128 v[180:183], v41 offset:3104\n"
      "ds_read_b128 v[184:187], v41 offset:1568\n"
      "v_mfma_f32_16x16x32_bf16 v[88:91], v[120:123], v[172:175], 0\n"
      "global_load_lds_dwordx4 v222, s[22:23] offset:1024\n"
      "v_mfma_f32_16x16x32_bf16 v[88:91], v[132:135], v[168:171], v[88:91]\n"
      "ds_read_b128 v[188:191], v41 offset:32\n"
      "v_mfma_f32_16x16x32_bf16 v[88:91], v[144:147], v[164:167], v[88:91]\n"
      "ds_read_b64 v[116:117], v40 offset:64\n"
      "v_mfma_f32_16x16x32_bf16 v[88:91], v[156:159], v[160:163], v[88:91]\n"
      "ds_write_b128 v56, v[104:107] offset:2048\n"
      "ds_read_b64 v[118:119], v40 offset:72\n"
      "ds_read_b64 v[128:129], v40 offset:1088\n"
      "ds_read_b64 v[130:131], v40 offset:1096\n"
      "s_waitcnt lgkmcnt(10)\n"
      "v_mfma_f32_16x16x32_bf16 v[68:71], v[112:115], v[160:163], 0\n"
      "ds_read_b64 v[140:141], v40 offset:2112\n"
      "v_mfma_f32_16x16x32_bf16 v[76:79], v[112:115], v[164:167], 0\n"
      "ds_read_b64 v[142:143], v40 offset:2120\n"
      "v_mfma_f32_16x16x32_bf16 v[76:79], v[124:127], v[160:163], v[76:79]\n"
      "ds_read_b64 v[152:153], v40 offset:3136\n"
      "s_load_dwordx16 s[60:75], s[56:57], 0xc0\n"
      "v_mfma_f32_16x16x32_bf16 v[84:87], v[112:115], v[168:171], 0\n"
      "ds_write_b128 v62, v[100:103]\n"
      "ds_read_b64 v[154:155], v40 offset:3144\n"
      "s_load_dwordx4 s[92:95], s[56:57], 0xa0\n"
      "v_mfma_f32_16x16x32_bf16 v[84:87], v[124:127], v[164:167], v[84:87]\n"
      "v_mfma_f32_16x16x32_bf16 v[84:87], v[136:139], v[160:163], v[84:87]\n"
      "v_mfma_f32_16x16x32_bf16 v[92:95], v[112:115], v[172:175], 0\n"
      "v_mfma_f32_16x16x32_bf16 v[92:95], v[124:127], v[168:171], v[92:95]\n"
      "ds_write_b128 v62, v[108:111] offset:2048\n"
      "v_mfma_f32_16x16x32_bf16 v[92:95], v[136:139], v[164:167], v[92:95]\n"
      "v_mfma_f32_16x16x32_bf16 v[92:95], v[148:151], v[160:163], v[92:95]\n"
      "s_waitcnt lgkmcnt(11)\n"
      "v_mfma_f32_16x16x32_bf16 v[64:67], v[112:115], v[176:179], v[64:67]\n"
      "buffer_load_dwordx2 v[254:255], v2, s[32:35], s45 offen sc1\n"
      "v_mfma_f32_16x16x32_bf16 v[72:75], v[112:115], v[180:183], v[72:75]\n"
      "ds_read_b128 v[160:163], v41 offset:4704\n"
      "v_mfma_f32_16x16x32_bf16 v[72:75], v[124:127], v[176:179], v[72:75]\n"
      "ds_read_b128 v[164:167], v41 offset:3168\n"
      "v_mfma_f32_16x16x32_bf16 v[80:83], v[112:115], v[184:187], v[80:83]\n"
      "ds_read_b128 v[168:171], v41 offset:1632\n"
      "v_mfma_f32_16x16x32_bf16 v[80:83], v[124:127], v[180:183], v[80:83]\n"
      "ds_read_b128 v[172:175], v41 offset:96\n"
      "v_mfma_f32_16x16x32_bf16 v[80:83], v[136:139], v[176:179], v[80:83]\n"
      "ds_read_b64 v[120:121], v40 offset:96\n"
      "v_mfma_f32_16x16x32_bf16 v[88:91], v[112:115], v[188:191], v[88:91]\n"
      "s_waitcnt lgkmcnt(5)\n"
      "s_barrier\n"
      "v_mfma_f32_16x16x32_bf16 v[88:91], v[124:127], v[184:187], v[88:91]\n"
      "ds_read_b128 v[96:99], v45 offset:0\n"
      "v_mfma_f32_16x16x32_bf16 v[88:91], v[136:139], v[180:183], v[88:91]\n"
      "ds_read_b128 v[100:103], v45 offset:4096\n"
      "v_mfma_f32_16x16x32_bf16 v[88:91], v[148:151], v[176:179], v[88:91]\n"
      "ds_read_b128 v[104:107], v45 offset:8192\n"
      "v_mfma_f32_16x16x32_bf16 v[68:71], v[116:119], v[176:179], v[68:71]\n"
      "ds_read_b128 v[108:111], v45 offset:12288\n"
      "v_mfma_f32_16x16x32_bf16 v[76:79], v[116:119], v[180:183], v[76:79]\n"
      "ds_read_b64 v[202:203], v47\n"
      "v_mfma_f32_16x16x32_bf16 v[76:79], v[128:131], v[176:179], v[76:79]\n"
      "ds_read_b64 v[122:123], v40 offset:104\n"
      "v_mfma_f32_16x16x32_bf16 v[84:87], v[116:119], v[184:187], v[84:87]\n"
      "ds_read_b64 v[132:133], v40 offset:1120\n"
      "v_mfma_f32_16x16x32_bf16 v[84:87], v[128:131], v[180:183], v[84:87]\n"
      "ds_read_b64 v[134:135], v40 offset:1128\n"
      "v_mfma_f32_16x16x32_bf16 v[84:87], v[140:143], v[176:179], v[84:87]\n"
      "ds_read_b64 v[144:145], v40 offset:2144\n"
      "v_mfma_f32_16x16x32_bf16 v[92:95], v[116:119], v[188:191], v[92:95]\n"
      "ds_read_b64 v[146:147], v40 offset:2152\n"
      "s_waitcnt lgkmcnt(5)\n"
      "v_mfma_f32_16x16x32_bf16 v[92:95], v[128:131], v[184:187], v[92:95]\n"
      "ds_read_b64 v[156:157], v40 offset:3168\n"
      "v_add_f32 v96, v96, v100\n"
      "v_mfma_f32_16x16x32_bf16 v[92:95], v[140:143], v[180:183], v[92:95]\n"
      "ds_read_b64 v[158:159], v40 offset:3176\n"
      "v_add_f32 v97, v97, v101\n"
      "v_mfma_f32_16x16x32_bf16 v[92:95], v[152:155], v[176:179], v[92:95]\n"
      "v_add_f32 v98, v98, v102\n"
      "v_mfma_f32_16x16x32_bf16 v[64:67], v[116:119], v[160:163], v[64:67]\n"
      "ds_read_b128 v[176:179], v41 offset:4736\n"
      "v_add_f32 v99, v99, v103\n"
      "v_mfma_f32_16x16x32_bf16 v[72:75], v[116:119], v[164:167], v[72:75]\n"
      "ds_read_b128 v[180:183], v41 offset:3200\n"
      "v_add_f32 v104, v104, v108\n"
      "v_mfma_f32_16x16x32_bf16 v[72:75], v[128:131], v[160:163], v[72:75]\n"
      "ds_read_b128 v[184:187], v41 offset:1664\n"
      "v_add_f32 v105, v105, v109\n"
      "v_mfma_f32_16x16x32_bf16 v[80:83], v[116:119], v[168:171], v[80:83]\n"
      "ds_read_b128 v[188:191], v41 offset:128\n"
      "v_add_f32 v106, v106, v110\n"
      "v_mfma_f32_16x16x32_bf16 v[80:83], v[128:131], v[164:167], v[80:83]\n"
      "ds_read_b64 v[112:113], v40 offset:128\n"
      "v_add_f32 v107, v107, v111\n"
      "v_mfma_f32_16x16x32_bf16 v[80:83], v[140:143], v[160:163], v[80:83]\n"
      "ds_read_b64 v[114:115], v40 offset:136\n"
      "v_add_f32 v96, v96, v104\n"
      "v_mfma_f32_16x16x32_bf16 v[88:91], v[116:119], v[172:175], v[88:91]\n"
      "ds_read_b64 v[124:125], v40 offset:1152\n"
      "v_add_f32 v97, v97, v105\n"
      "v_mfma_f32_16x16x32_bf16 v[88:91], v[128:131], v[168:171], v[88:91]\n"
      "ds_read_b64 v[126:127], v40 offset:1160\n"
      "v_add_f32 v98, v98, v106\n"
      "v_mfma_f32_16x16x32_bf16 v[88:91], v[140:143], v[164:167], v[88:91]\n"
      "ds_read_b64 v[136:137], v40 offset:2176\n"
      "v_add_f32 v99, v99, v107\n"
      "v_mfma_f32_16x16x32_bf16 v[88:91], v[152:155], v[160:163], v[88:91]\n"
      "ds_read_b64 v[138:139], v40 offset:2184\n"
      "s_waitcnt lgkmcnt(10)\n"
      "v_mfma_f32_16x16x32_bf16 v[68:71], v[120:123], v[160:163], v[68:71]\n"
      "buffer_load_dwordx2 v[200:201], v4, s[32:35], s80 offen offset:0 sc1\n"
      "v_cvt_pk_bf16_f32 v96, 0, v96\n"
      "v_mfma_f32_16x16x32_bf16 v[76:79], v[120:123], v[164:167], v[76:79]\n"
      "ds_read_b64 v[148:149], v40 offset:3200\n"
      "v_cvt_pk_bf16_f32 v97, 0, v97\n"
      "v_mfma_f32_16x16x32_bf16 v[76:79], v[132:135], v[160:163], v[76:79]\n"
      "buffer_load_dword v250, v61, s[32:35], s85 offen offset:0 sc1\n"
      "v_cvt_pk_bf16_f32 v98, 0, v98\n"
      "v_mfma_f32_16x16x32_bf16 v[84:87], v[120:123], v[168:171], v[84:87]\n"
      "ds_read_b64 v[150:151], v40 offset:3208\n"
      "v_cvt_pk_bf16_f32 v99, 0, v99\n"
      "v_mfma_f32_16x16x32_bf16 v[84:87], v[132:135], v[164:167], v[84:87]\n"
      "buffer_load_dwordx4 v[224:227], v57, s[32:35], s82 offen offset:0 sc1\n"
      "v_and_b32 v100, 0xffff0000, v203\n"
      "v_mfma_f32_16x16x32_bf16 v[84:87], v[144:147], v[160:163], v[84:87]\n"
      "s_lshr_b32 s58, s3, 1\n"
      "s_mul_i32 s58, s58, 0x3f800000\n"
      "v_mfma_f32_16x16x32_bf16 v[92:95], v[120:123], v[172:175], v[92:95]\n"
      "v_lshlrev_b32 v101, 16, v203\n"
      "v_and_b32 v102, 0xffff0000, v202\n"
      "v_mfma_f32_16x16x32_bf16 v[92:95], v[132:135], v[168:171], v[92:95]\n"
      "buffer_load_dwordx4 v[228:231], v57, s[32:35], s82 offen offset:1024 sc1\n"
      "v_lshlrev_b32 v103, 16, v202\n"
      "v_mfma_f32_16x16x32_bf16 v[92:95], v[144:147], v[164:167], v[92:95]\n"
      "v_mul_f32 v100, s25, v100\n"
      "v_mul_f32 v101, s25, v101\n"
      "v_mfma_f32_16x16x32_bf16 v[92:95], v[156:159], v[160:163], v[92:95]\n"
      "v_mul_f32 v102, s25, v102\n"
      "s_waitcnt lgkmcnt(8)\n"
      "v_mfma_f32_16x16x32_bf16 v[64:67], v[120:123], v[176:179], v[64:67]\n"
      "buffer_load_dwordx4 v[192:195], v54, s[32:35], s53 offen offset:0 sc1\n"
      "s_waitcnt vmcnt(5)\n"
      "v_cmp_ne_u32 vcc, s30, v255\n"
      "v_mfma_f32_16x16x32_bf16 v[72:75], v[120:123], v[180:183], v[72:75]\n"
      "ds_read_b128 v[160:163], v41 offset:4800\n"
      "v_mul_f32 v103, s25, v103\n"
      "v_mfma_f32_16x16x32_bf16 v[72:75], v[132:135], v[176:179], v[72:75]\n"
      "ds_read_b128 v[164:167], v41 offset:3264\n"
      "s_cbranch_vccnz L_cm_s%=\n"
      "L_cm_r%=:\n"
      "v_add_f32_dpp v254, v254, v254 quad_perm:[1,0,3,2] row_mask:0xf bank_mask:0xf\n"
      "v_mfma_f32_16x16x32_bf16 v[80:83], v[120:123], v[184:187], v[80:83]\n"
      "buffer_load_dwordx4 v[196:199], v54, s[32:35], s54 offen offset:0 sc1\n"
      "v_cvt_pk_bf16_f32 v100, 0, v100\n"
      "v_mfma_f32_16x16x32_bf16 v[80:83], v[132:135], v[180:183], v[80:83]\n"
      "ds_read_b128 v[168:171], v41 offset:1728\n"
      "v_add_f32_dpp v254, v254, v254 quad_perm:[2,3,0,1] row_mask:0xf bank_mask:0xf\n"
      "v_mfma_f32_16x16x32_bf16 v[80:83], v[144:147], v[176:179], v[80:83]\n"
      "ds_read_b128 v[172:175], v41 offset:192\n"
      "v_cvt_pk_bf16_f32 v254, 0, v254\n"
      "v_mfma_f32_16x16x32_bf16 v[88:91], v[120:123], v[188:191], v[88:91]\n"
      "buffer_load_dword v248, v60, s[32:35], s84 offen offset:0 sc1\n"
      "v_add_f32 v254, s58, v254\n"
      "v_mfma_f32_16x16x32_bf16 v[88:91], v[132:135], v[184:187], v[88:91]\n"
      "ds_read_b64 v[116:117], v40 offset:160\n"
      "v_cvt_pk_bf16_f32 v254, v254, v254\n"
      "v_mfma_f32_16x16x32_bf16 v[88:91], v[144:147], v[180:183], v[88:91]\n"
      "ds_write_b16 v3, v254\n"
      "ds_read_b64 v[118:119], v40 offset:168\n"
      "v_cvt_pk_bf16_f32 v101, 0, v101\n"
      "v_mfma_f32_16x16x32_bf16 v[88:91], v[156:159], v[176:179], v[88:91]\n"
      "buffer_load_dword v249, v60, s[32:35], s84 offen offset:2048 sc1\n"
      "s_waitcnt lgkmcnt(7)\n"
      "v_mfma_f32_16x16x32_bf16 v[68:71], v[112:115], v[176:179], v[68:71]\n"
      "ds_read_b64 v[128:129], v40 offset:1184\n"
      "v_cvt_pk_bf16_f32 v102, 0, v102\n"
      "v_mfma_f32_16x16x32_bf16 v[76:79], v[112:115], v[180:183], v[76:79]\n"
      "ds_read_b64 v[130:131], v40 offset:1192\n"
      "v_cvt_pk_bf16_f32 v103, 0, v103\n"
      "v_mfma_f32_16x16x32_bf16 v[76:79], v[124:127], v[176:179], v[76:79]\n"
      "buffer_load_dwordx4 v[208:211], v55, s[32:35], 0 offen offset:0 sc1\n"
      "v_add_f32 v96, v96, v100\n"
      "v_mfma_f32_16x16x32_bf16 v[84:87], v[112:115], v[184:187], v[84:87]\n"
      "ds_read_b64 v[140:141], v40 offset:2208\n"
      "s_waitcnt lgkmcnt(4)\n"
      "s_barrier\n"
      "v_mfma_f32_16x16x32_bf16 v[84:87], v[124:127], v[180:183], v[84:87]\n"
      "ds_read2_b64 v[24:27], v236 offset0:0 offset1:4\n"
      "v_add_f32 v97, v97, v101\n"
      "v_mfma_f32_16x16x32_bf16 v[84:87], v[136:139], v[176:179], v[84:87]\n"
      "buffer_load_dwordx2 v[252:253], v51, s[32:35], s76 offen offset:0 sc1\n"
      "v_add_f32 v98, v98, v102\n"
      "v_mfma_f32_16x16x32_bf16 v[92:95], v[112:115], v[188:191], v[92:95]\n"
      "ds_read_b64 v[142:143], v40 offset:2216\n"
      "v_add_f32 v99, v99, v103\n"
      "v_mfma_f32_16x16x32_bf16 v[92:95], v[124:127], v[184:187], v[92:95]\n"
      "ds_read_b64 v[152:153], v40 offset:3232\n"
      "v_cvt_pk_bf16_f32 v96, 0, v96\n"
      "v_mfma_f32_16x16x32_bf16 v[92:95], v[136:139], v[180:183], v[92:95]\n"
      "ds_read_b64 v[154:155], v40 offset:3240\n"
      "s_mov_b32 s48, 0\n"
      "s_mov_b32 s49, -1\n"
      "v_mfma_f32_16x16x32_bf16 v[92:95], v[148:151], v[176:179], v[92:95]\n"
      "s_waitcnt lgkmcnt(3)\n"
      "v_mfma_f32_16x16x32_bf16 v[64:67], v[112:115], v[160:163], v[64:67]\n"
      "ds_read_b128 v[176:179], v41 offset:4832\n"
      "v_mfma_f32_16x16x32_bf16 v[232:235], v[24:27], v[8:11], 0\n"
      "v_mfma_f32_16x16x32_bf16 v[72:75], v[112:115], v[164:167], v[72:75]\n"
      "ds_read_b128 v[180:183], v41 offset:3296\n"
      "v_mfma_f32_16x16x32_bf16 v[8:11], v[24:27], v[12:15], 0\n"
      "v_mfma_f32_16x16x32_bf16 v[72:75], v[124:127], v[160:163], v[72:75]\n"
      "ds_read_b128 v[184:187], v41 offset:1760\n"
      "v_mfma_f32_16x16x32_bf16 v[12:15], v[24:27], v[16:19], 0\n"
      "v_mfma_f32_16x16x32_bf16 v[80:83], v[112:115], v[168:171], v[80:83]\n"
      "buffer_load_dwordx4 v[212:215], v55, s[32:35], 0 offen offset:1024 sc1\n"
      "v_mfma_f32_16x16x32_bf16 v[16:19], v[24:27], v[20:23], 0\n"
      "v_mfma_f32_16x16x32_bf16 v[80:83], v[124:127], v[164:167], v[80:83]\n"
      "ds_read_b128 v[188:191], v41 offset:224\n"
      "v_cvt_pk_bf16_f32 v97, 0, v97\n"
      "v_mfma_f32_16x16x32_bf16 v[80:83], v[136:139], v[160:163], v[80:83]\n"
      "ds_read_b64 v[120:121], v40 offset:192\n"
      "v_cvt_pk_bf16_f32 v98, 0, v98\n"
      "v_mfma_f32_16x16x32_bf16 v[88:91], v[112:115], v[172:175], v[88:91]\n"
      "buffer_load_dwordx4 v[216:219], v55, s[32:35], 0 offen offset:2048 sc1\n"
      "v_cndmask_b32 v206, v232, v12, s[48:49]\n"
      "v_cndmask_b32 v207, v8, v16, s[48:49]\n"
      "v_mfma_f32_16x16x32_bf16 v[88:91], v[124:127], v[168:171], v[88:91]\n"
      "ds_read_b64 v[122:123], v40 offset:200\n"
      "v_cvt_pk_bf16_f32 v99, 0, v99\n"
      "v_mfma_f32_16x16x32_bf16 v[88:91], v[136:139], v[164:167], v[88:91]\n"
      "ds_read_b64 v[132:133], v40 offset:1216\n"
      "v_mfma_f32_16x16x32_bf16 v[88:91], v[148:151], v[160:163], v[88:91]\n"
      "buffer_load_dwordx4 v[220:223], v55, s[32:35], 0 offen offset:3072 sc1\n"
      "s_waitcnt lgkmcnt(7)\n"
      "v_mfma_f32_16x16x32_bf16 v[68:71], v[116:119], v[160:163], v[68:71]\n"
      "ds_read_b64 v[134:135], v40 offset:1224\n"
      "v_mfma_f32_16x16x32_bf16 v[76:79], v[116:119], v[164:167], v[76:79]\n"
      "ds_read_b64 v[144:145], v40 offset:2240\n"
      "v_mfma_f32_16x16x32_bf16 v[76:79], v[128:131], v[160:163], v[76:79]\n"
      "ds_read_b64 v[146:147], v40 offset:2248\n"
      "v_mfma_f32_16x16x32_bf16 v[84:87], v[116:119], v[168:171], v[84:87]\n"
      "ds_read_b64 v[156:157], v40 offset:3264\n"
      "v_mfma_f32_16x16x32_bf16 v[84:87], v[128:131], v[164:167], v[84:87]\n"
      "buffer_load_dwordx2 v[204:205], v58, s[32:35], s83 offen offset:0 sc1\n"
      "v_mfma_f32_16x16x32_bf16 v[84:87], v[140:143], v[160:163], v[84:87]\n"
      "ds_read_b64 v[158:159], v40 offset:3272\n"
      "v_mfma_f32_16x16x32_bf16 v[92:95], v[116:119], v[172:175], v[92:95]\n"
      "v_mfma_f32_16x16x32_bf16 v[92:95], v[128:131], v[168:171], v[92:95]\n"
      "v_mfma_f32_16x16x32_bf16 v[92:95], v[140:143], v[164:167], v[92:95]\n"
      "v_mfma_f32_16x16x32_bf16 v[92:95], v[152:155], v[160:163], v[92:95]\n"
      "s_waitcnt lgkmcnt(8)\n"
      "v_mfma_f32_16x16x32_bf16 v[64:67], v[116:119], v[176:179], v[64:67]\n"
      "ds_read_b128 v[160:163], v41 offset:4896\n"
      "v_mfma_f32_16x16x32_bf16 v[72:75], v[116:119], v[180:183], v[72:75]\n"
      "ds_read_b128 v[164:167], v41 offset:3360\n"
      "v_mfma_f32_16x16x32_bf16 v[72:75], v[128:131], v[176:179], v[72:75]\n"
      "ds_read_b128 v[168:171], v41 offset:1824\n"
      "v_mfma_f32_16x16x32_bf16 v[80:83], v[116:119], v[184:187], v[80:83]\n"
      "ds_read_b128 v[172:175], v41 offset:288\n"
      "v_mfma_f32_16x16x32_bf16 v[80:83], v[128:131], v[180:183], v[80:83]\n"
      "ds_read_b64 v[112:113], v40 offset:224\n"
      "v_mfma_f32_16x16x32_bf16 v[80:83], v[140:143], v[176:179], v[80:83]\n"
      "ds_read_b64 v[114:115], v40 offset:232\n"
      "v_mfma_f32_16x16x32_bf16 v[88:91], v[116:119], v[188:191], v[88:91]\n"
      "ds_read_b64 v[124:125], v40 offset:1248\n"
      "s_waitcnt vmcnt(4)\n"
      "v_mfma_f32_16x16x32_bf16 v[88:91], v[128:131], v[184:187], v[88:91]\n"
      "ds_read_b64 v[126:127], v40 offset:1256\n"
      "v_cmp_ne_u64 vcc, s[30:31], v[252:253]\n"
      "v_mfma_f32_16x16x32_bf16 v[88:91], v[140:143], v[180:183], v[88:91]\n"
      "ds_read_b64 v[136:137], v40 offset:2272\n"
      "v_mfma_f32_16x16x32_bf16 v[88:91], v[152:155], v[176:179], v[88:91]\n"
      "s_cbranch_vccnz L_cw_s%=\n"
      "L_cw_r%=:\n"
      "v_add_u32 v35, 4096, v7\n"
      "s_waitcnt lgkmcnt(9)\n"
      "v_mfma_f32_16x16x32_bf16 v[68:71], v[120:123], v[176:179], v[68:71]\n"
      "global_load_dwordx4 a[0:3], v35, s[8:9] offset:-4096 sc1\n"
      "v_mfma_f32_16x16x32_bf16 v[76:79], v[120:123], v[180:183], v[76:79]\n"
      "ds_read_b64 v[138:139], v40 offset:2280\n"
      "v_mfma_f32_16x16x32_bf16 v[76:79], v[132:135], v[176:179], v[76:79]\n"
      "ds_read_b64 v[148:149], v40 offset:3296\n"
      "v_mfma_f32_16x16x32_bf16 v[84:87], v[120:123], v[184:187], v[84:87]\n"
      "global_load_dwordx4 a[4:7], v35, s[8:9] offset:-3072 sc1\n"
      "v_mfma_f32_16x16x32_bf16 v[84:87], v[132:135], v[180:183], v[84:87]\n"
      "ds_read_b64 v[150:151], v40 offset:3304\n"
      "v_mfma_f32_16x16x32_bf16 v[84:87], v[144:147], v[176:179], v[84:87]\n"
      "v_mfma_f32_16x16x32_bf16 v[92:95], v[120:123], v[188:191], v[92:95]\n"
      "global_load_dwordx4 a[8:11], v35, s[8:9] offset:-2048 sc1\n"
      "v_mfma_f32_16x16x32_bf16 v[92:95], v[132:135], v[184:187], v[92:95]\n"
      "v_mfma_f32_16x16x32_bf16 v[92:95], v[144:147], v[180:183], v[92:95]\n"
      "v_mfma_f32_16x16x32_bf16 v[92:95], v[156:159], v[176:179], v[92:95]\n"
      "global_load_dwordx4 a[12:15], v35, s[8:9] offset:-1024 sc1\n"
      "s_waitcnt lgkmcnt(8)\n"
      "v_mfma_f32_16x16x32_bf16 v[64:67], v[120:123], v[160:163], v[64:67]\n"
      "ds_read_b128 v[176:179], v41 offset:4928\n"
      "v_mfma_f32_16x16x32_bf16 v[72:75], v[120:123], v[164:167], v[72:75]\n"
      "ds_read_b128 v[180:183], v41 offset:3392\n"
      "s_waitcnt vmcnt(4)\n"
      "v_mfma_f32_16x16x32_bf16 v[72:75], v[132:135], v[160:163], v[72:75]\n"
      "global_load_dwordx4 a[16:19], v35, s[8:9] offset:0 sc1\n"
      "v_add_u32 v251, v200, v201\n"
      "v_add3_u32 v34, v192, v193, v194\n"
      "v_add3_u32 v34, v34, v195, v196\n"
      "v_add_u32_dpp v251, v251, v251 row_half_mirror row_mask:0xf bank_mask:0xf\n"
      "v_add3_u32 v34, v34, v197, v198\n"
      "v_mfma_f32_16x16x32_bf16 v[80:83], v[120:123], v[168:171], v[80:83]\n"
      "ds_read_b128 v[184:187], v41 offset:1856\n"
      "v_add3_u32 v251, v251, v34, v199\n"
      "v_add3_u32 v34, v248, v249, v250\n"
      "v_add3_u32 v34, v34, v211, v215\n"
      "v_add_u32_dpp v251, v251, v251 quad_perm:[1,0,3,2] row_mask:0xf bank_mask:0xf\n"
      "v_mfma_f32_16x16x32_bf16 v[80:83], v[132:135], v[164:167], v[80:83]\n"
      "ds_read_b128 v[188:191], v41 offset:320\n"
      "v_add3_u32 v34, v34, v219, v223\n"
      "v_add3_u32 v34, v34, v227, v231\n"
      "v_add_u32_dpp v251, v251, v251 quad_perm:[2,3,0,1] row_mask:0xf bank_mask:0xf\n"
      "v_add3_u32 v251, v251, v34, v205\n"
      "v_mfma_f32_16x16x32_bf16 v[80:83], v[144:147], v[160:163], v[80:83]\n"
      "global_load_dwordx4 a[20:23], v35, s[8:9] offset:1024 sc1\n"
      "v_cmp_ne_u32 vcc, s24, v251\n"
      "v_mfma_f32_16x16x32_bf16 v[88:91], v[120:123], v[172:175], v[88:91]\n"
      "ds_read_b64 v[116:117], v40 offset:256\n"
      "v_mfma_f32_16x16x32_bf16 v[88:91], v[132:135], v[168:171], v[88:91]\n"
      "ds_read_b64 v[118:119], v40 offset:264\n"
      "s_cbranch_vccnz L_cv_s%=\n"
      "L_cv_r%=:\n"
      "v_add_f32_dpp v204, v204, v204 quad_perm:[1,0,3,2] row_mask:0xf bank_mask:0xf\n"
      "v_lshlrev_b32 v100, 16, v200\n"
      "v_and_b32 v101, 0xffff0000, v200\n"
      "v_add_f32_dpp v204, v204, v204 quad_perm:[2,3,0,1] row_mask:0xf bank_mask:0xf\n"
      "v_mfma_f32_16x16x32_bf16 v[88:91], v[144:147], v[164:167], v[88:91]\n"
      "global_load_dwordx4 a[24:27], v35, s[8:9] offset:2048 sc1\n"
      "ds_read_b64_tr_b16 v[240:241], v28 offset:0\n"
      "ds_read_b64_tr_b16 v[242:243], v28 offset:1024\n"
      "ds_read_b64_tr_b16 v[244:245], v28 offset:32\n"
      "ds_read_b64_tr_b16 v[246:247], v28 offset:1056\n"
      "v_cvt_pk_bf16_f32 v204, 0, v204\n"
      "v_lshlrev_b32 v102, 16, v201\n"
      "v_add_f32 v205, 1.0, v204\n"
      "v_and_b32 v103, 0xffff0000, v201\n"
      "v_mfma_f32_16x16x32_bf16 v[88:91], v[156:159], v[160:163], v[88:91]\n"
      "ds_read_b64 v[128:129], v40 offset:1280\n"
      "v_cvt_pk_bf16_f32 v205, 0, v205\n"
      "v_lshlrev_b32 v104, 16, v226\n"
      "v_readlane_b32 s40, v204, 32\n"
      "s_waitcnt lgkmcnt(11)\n"
      "v_mfma_f32_16x16x32_bf16 v[68:71], v[112:115], v[160:163], v[68:71]\n"
      "ds_read_b64 v[130:131], v40 offset:1288\n"
      "v_and_b32 v105, 0xffff0000, v226\n"
      "v_readlane_b32 s41, v205, 36\n"
      "v_lshlrev_b32 v106, 16, v230\n"
      "v_readlane_b32 s42, v204, 40\n"
      "v_mfma_f32_16x16x32_bf16 v[76:79], v[112:115], v[164:167], v[76:79]\n"
      "global_load_dwordx4 a[28:31], v35, s[8:9] offset:3072 sc1\n"
      "ds_read_b64 v[140:141], v40 offset:2304\n"
      "v_and_b32 v107, 0xffff0000, v230\n"
      "v_readlane_b32 s43, v205, 44\n"
      "v_sub_f32 v104, v100, v104\n"
      "v_mfma_f32_16x16x32_bf16 v[76:79], v[124:127], v[160:163], v[76:79]\n"
      "ds_read_b64 v[142:143], v40 offset:2312\n"
      "v_add_u32 v35, 12288, v7\n"
      "v_sub_f32 v105, v101, v105\n"
      "v_sub_f32 v106, v102, v106\n"
      "v_sub_f32 v107, v103, v107\n"
      "v_cvt_pk_bf16_f32 v104, 0, v104\n"
      "v_mfma_f32_16x16x32_bf16 v[84:87], v[112:115], v[168:171], v[84:87]\n"
      "ds_read_b64 v[152:153], v40 offset:3328\n"
      "v_cvt_pk_bf16_f32 v105, 0, v105\n"
      "v_cvt_pk_bf16_f32 v106, 0, v106\n"
      "v_cvt_pk_bf16_f32 v107, 0, v107\n"
      "v_mfma_f32_16x16x32_bf16 v[84:87], v[124:127], v[164:167], v[84:87]\n"
      "global_load_dwordx4 a[32:35], v35, s[8:9] offset:-4096 sc1\n"
      "ds_read_b64 v[154:155], v40 offset:3336\n"
      "v_mul_f32 v104, v104, v224\n"
      "v_mul_f32 v105, v105, v225\n"
      "v_mul_f32 v106, v106, v228\n"
      "v_mul_f32 v107, v107, v229\n"
      "v_mfma_f32_16x16x32_bf16 v[84:87], v[136:139], v[160:163], v[84:87]\n"
      "v_cvt_pk_bf16_f32 v104, 0, v104\n"
      "v_cvt_pk_bf16_f32 v105, 0, v105\n"
      "v_cvt_pk_bf16_f32 v106, 0, v106\n"
      "v_cvt_pk_bf16_f32 v107, 0, v107\n"
      "v_mfma_f32_16x16x32_bf16 v[92:95], v[112:115], v[172:175], v[92:95]\n"
      "v_mul_f32 v104, s41, v104\n"
      "v_mul_f32 v105, s41, v105\n"
      "v_mul_f32 v106, s41, v106\n"
      "s_waitcnt lgkmcnt(6)\n"
      "v_mfma_f32_16x16x32_bf16 v[92:95], v[124:127], v[168:171], v[92:95]\n"
      "buffer_store_dwordx4 v[240:243], v7, s[32:35], s86 offen sc1\n"
      "v_mul_f32 v107, s41, v107\n"
      "v_cvt_pk_bf16_f32 v104, 0, v104\n"
      "v_cvt_pk_bf16_f32 v105, 0, v105\n"
      "v_cvt_pk_bf16_f32 v106, 0, v106\n"
      "v_mfma_f32_16x16x32_bf16 v[92:95], v[136:139], v[164:167], v[92:95]\n"
      "v_cvt_pk_bf16_f32 v107, 0, v107\n"
      "v_add_f32 v104, s40, v104\n"
      "v_add_f32 v105, s40, v105\n"
      "v_mfma_f32_16x16x32_bf16 v[92:95], v[148:151], v[160:163], v[92:95]\n"
      "v_add_f32 v106, s40, v106\n"
      "v_add_f32 v107, s40, v107\n"
      "v_cvt_pk_bf16_f32 v104, 0, v104\n"
      "v_cvt_pk_bf16_f32 v105, 0, v105\n"
      "s_waitcnt lgkmcnt(0)\n"
      "v_mfma_f32_16x16x32_bf16 v[64:67], v[112:115], v[176:179], v[64:67]\n"
      "buffer_store_dwordx4 v[244:247], v7, s[32:35], s87 offen sc1\n"
      "v_cvt_pk_bf16_f32 v106, 0, v106\n"
      "v_cvt_pk_bf16_f32 v107, 0, v107\n"
      "v_mul_f32 v104, v104, v96\n"
      "v_mfma_f32_16x16x32_bf16 v[68:71], v[116:119], v[176:179], v[68:71]\n"
      "v_mul_f32 v105, v105, v97\n"
      "v_mul_f32 v106, v106, v98\n"
      "v_mul_f32 v107, v107, v99\n"
      "v_cvt_pk_bf16_f32 v104, 0, v104\n"
      "v_mfma_f32_16x16x32_bf16 v[72:75], v[112:115], v[180:183], v[72:75]\n"
      "v_cvt_pk_bf16_f32 v105, 0, v105\n"
      "v_cvt_pk_bf16_f32 v106, 0, v106\n"
      "v_cvt_pk_bf16_f32 v107, 0, v107\n"
      "v_mul_f32 v104, s42, v104\n"
      "v_mfma_f32_16x16x32_bf16 v[72:75], v[124:127], v[176:179], v[72:75]\n"
      "global_load_dwordx4 a[36:39], v35, s[8:9] offset:-3072 sc1\n"
      "ds_write_b128 v44, v[64:67] offset:0\n"
      "v_mul_f32 v105, s42, v105\n"
      "v_mul_f32 v106, s42, v106\n"
      "v_mul_f32 v107, s42, v107\n"
      "v_cvt_pk_bf16_f32 v104, 0, v104\n"
      "v_mfma_f32_16x16x32_bf16 v[76:79], v[116:119], v[180:183], v[76:79]\n"
      "ds_write_b128 v59, v[68:71] offset:0\n"
      "v_cvt_pk_bf16_f32 v105, 0, v105\n"
      "v_cvt_pk_bf16_f32 v106, 0, v106\n"
      "v_cvt_pk_bf16_f32 v107, 0, v107\n"
      "v_add_f32 v104, v104, v100\n"
      "v_mfma_f32_16x16x32_bf16 v[76:79], v[128:131], v[176:179], v[76:79]\n"
      "v_add_f32 v105, v105, v101\n"
      "v_add_f32 v106, v106, v102\n"
      "v_mfma_f32_16x16x32_bf16 v[80:83], v[112:115], v[184:187], v[80:83]\n"
      "buffer_store_dword v206, v239, s[32:35], s59 offen offset:0 sc1\n"
      "buffer_store_dword v207, v239, s[32:35], s59 offen offset:128 sc1\n"
      "ds_write_b128 v44, v[72:75] offset:2048\n"
      "v_add_f32 v107, v107, v103\n"
      "v_cvt_pk_bf16_f32 v104, 0, v104\n"
      "v_cvt_pk_bf16_f32 v105, 0, v105\n"
      "v_cvt_pk_bf16_f32 v106, 0, v106\n"
      "v_mfma_f32_16x16x32_bf16 v[80:83], v[124:127], v[180:183], v[80:83]\n"
      "global_load_dwordx4 a[40:43], v35, s[8:9] offset:-2048 sc1\n"
      "v_cvt_pk_bf16_f32 v107, 0, v107\n"
      "v_cvt_pk_bf16_f32 v108, v104, v105\n"
      "v_cvt_pk_bf16_f32 v109, v106, v107\n"
      "v_mfma_f32_16x16x32_bf16 v[80:83], v[136:139], v[176:179], v[80:83]\n"
      "ds_write_b128 v59, v[76:79] offset:2048\n"
      "v_mul_f32 v104, s43, v104\n"
      "v_mul_f32 v105, s43, v105\n"
      "v_mul_f32 v106, s43, v106\n"
      "v_mul_f32 v107, s43, v107\n"
      "v_mfma_f32_16x16x32_bf16 v[84:87], v[116:119], v[184:187], v[84:87]\n"
      "v_cvt_pk_bf16_f32 v110, v104, v105\n"
      "v_cvt_pk_bf16_f32 v111, v106, v107\n"
      "v_add_u32 v96, v108, v109\n"
      "v_mfma_f32_16x16x32_bf16 v[84:87], v[128:131], v[180:183], v[84:87]\n"
      "global_load_dwordx4 a[44:47], v35, s[8:9] offset:-1024 sc1\n"
      "v_add_u32 v97, v110, v111\n"
      "s_add_u32 s4, s26, s71\n"
      "s_addc_u32 s5, s27, 0\n"
      "v_mfma_f32_16x16x32_bf16 v[84:87], v[140:143], v[176:179], v[84:87]\n"
      "buffer_store_dwordx2 v[110:111], v50, s[32:35], s78 offen sc1\n"
      "ds_write_b128 v44, v[80:83] offset:4096\n"
      "ds_read_b128 v[232:235], v46\n"
      "ds_read_b128 v[236:239], v46 offset:7680\n"
      "v_add_u32_dpp v96, v96, v96 quad_perm:[1,0,3,2] row_mask:0xf bank_mask:0xf\n"
      "v_add_u32_dpp v97, v97, v97 quad_perm:[1,0,3,2] row_mask:0xf bank_mask:0xf\n"
      "v_mfma_f32_16x16x32_bf16 v[88:91], v[112:115], v[188:191], v[88:91]\n"
      "v_add_u32_dpp v96, v96, v96 quad_perm:[2,3,0,1] row_mask:0xf bank_mask:0xf\n"
      "v_add_u32_dpp v97, v97, v97 quad_perm:[2,3,0,1] row_mask:0xf bank_mask:0xf\n"
      "s_add_u32 s6, s26, s72\n"
      "s_addc_u32 s7, s27, 0\n"
      "v_mfma_f32_16x16x32_bf16 v[88:91], v[124:127], v[184:187], v[88:91]\n"
      "global_load_dwordx4 a[48:51], v35, s[8:9] offset:0 sc1\n"
      "v_add_u32_dpp v96, v96, v96 row_half_mirror row_mask:0xf bank_mask:0xf\n"
      "v_add_u32_dpp v97, v97, v97 row_half_mirror row_mask:0xf bank_mask:0xf\n"
      "L_pg2_%=:\n"
      "s_cbranch_execz L_pg6_%=\n"
      "v_mfma_f32_16x16x32_bf16 v[88:91], v[136:139], v[180:183], v[88:91]\n"
      "buffer_store_dwordx2 v[108:109], v37, s[32:35], s55 offen sc1\n"
      "ds_write_b128 v59, v[84:87] offset:4096\n"
      "v_mov_b32 v98, v96\n"
      "v_add_u32_dpp v97, v97, v97 row_mirror row_mask:0xf bank_mask:0xf\n"
      "s_add_u32 s10, s26, s75\n"
      "s_addc_u32 s11, s27, 0\n"
      "v_mfma_f32_16x16x32_bf16 v[88:91], v[148:151], v[176:179], v[88:91]\n"
      "s_mov_b32 s42, 0xaaaa5555\n"
      "s_mov_b32 s43, s42\n"
      "v_mfma_f32_16x16x32_bf16 v[92:95], v[116:119], v[188:191], v[92:95]\n"
      "global_load_dwordx4 a[52:55], v35, s[8:9] offset:1024 sc1\n"
      "v_permlane16_swap_b32 v96, v98\n"
      "v_add_u32 v96, v96, v98\n"
      "s_add_u32 s12, s10, 49152\n"
      "s_addc_u32 s13, s11, 0\n"
      "v_mfma_f32_16x16x32_bf16 v[92:95], v[128:131], v[184:187], v[92:95]\n"
      "v_cndmask_b32 v96, v97, v96, s[42:43]\n"
      "v_sub_u32 v96, v52, v96\n"
      "v_mfma_f32_16x16x32_bf16 v[92:95], v[140:143], v[180:183], v[92:95]\n"
      "buffer_store_dword v96, v49, s[32:35], s77 offen sc1\n"
      "ds_write_b128 v44, v[88:91] offset:6144\n"
      "s_lshl_b32 s71, s30, 2\n"
      "v_mfma_f32_16x16x32_bf16 v[92:95], v[152:155], v[176:179], v[92:95]\n"
      "global_load_dwordx4 a[56:59], v35, s[8:9] offset:2048 sc1\n"
      "s_nop 7\n"
      "ds_write_b128 v59, v[92:95] offset:6144\n"
      "v_readlane_b32 s40, v204, 0\n"
      "v_readlane_b32 s42, v205, 4\n"
      "v_xor_b32 v33, 16, v31\n"
      "v_readlane_b32 s58, v204, 8\n"
      "v_readlane_b32 s48, v204, 16\n"
      "v_readlane_b32 s50, v205, 20\n"
      "v_readlane_b32 s52, v204, 24\n"
      "v_readlane_b32 s41, v205, 12\n"
      "v_readlane_b32 s49, v205, 28\n"
      "s_mov_b32 vcc_lo, 0xaa55aa55\n"
      "s_mov_b32 vcc_hi, 0xaa55aa55\n"
      "v_add_u32 v40, s73, v7\n"
      "v_lshlrev_b32 v96, 16, v210\n"
      "v_and_b32 v97, 0xffff0000, v210\n"
      "v_lshlrev_b32 v98, 16, v214\n"
      "v_and_b32 v99, 0xffff0000, v214\n"
      "v_lshlrev_b32 v100, 16, v218\n"
      "v_and_b32 v101, 0xffff0000, v218\n"
      "v_lshlrev_b32 v102, 16, v222\n"
      "v_and_b32 v103, 0xffff0000, v222\n"
      "s_waitcnt lgkmcnt(0)\n"
      "s_barrier\n"
      "ds_read_b128 v[64:67], v31 offset:0\n"
      "ds_read_b128 v[68:71], v33 offset:0\n"
      "ds_read_b128 v[72:75], v31 offset:8192\n"
      "ds_read_b128 v[76:79], v33 offset:8192\n"
      "ds_read_b128 v[80:83], v31 offset:16384\n"
      "ds_read_b128 v[84:87], v33 offset:16384\n"
      "ds_read_b128 v[88:91], v31 offset:24576\n"
      "ds_read_b128 v[92:95], v33 offset:24576\n"
      "v_add_u32 v34, 12288, v7\n"
      "global_load_dwordx4 a[60:63], v34, s[8:9] offset:3072 sc1\n"
      "v_lshlrev_b32 v112, 16, v192\n"
      "v_and_b32 v113, 0xffff0000, v192\n"
      "v_lshlrev_b32 v114, 16, v193\n"
      "v_and_b32 v115, 0xffff0000, v193\n"
      "v_lshlrev_b32 v116, 16, v194\n"
      "v_and_b32 v117, 0xffff0000, v194\n"
      "v_lshlrev_b32 v118, 16, v195\n"
      "v_and_b32 v119, 0xffff0000, v195\n"
      "v_pk_add_f32 v[104:105], v[112:113], v[96:97] neg_lo:[0,1] neg_hi:[0,1]\n"
      "v_add_u32 v34, 20480, v7\n"
      "global_load_dwordx4 a[64:67], v34, s[8:9] offset:-4096 sc1\n"
      "v_pk_add_f32 v[106:107], v[114:115], v[98:99] neg_lo:[0,1] neg_hi:[0,1]\n"
      "v_pk_add_f32 v[108:109], v[116:117], v[100:101] neg_lo:[0,1] neg_hi:[0,1]\n"
      "v_pk_add_f32 v[110:111], v[118:119], v[102:103] neg_lo:[0,1] neg_hi:[0,1]\n"
      "v_cvt_pk_bf16_f32 v104, 0, v104\n"
      "v_cvt_pk_bf16_f32 v105, 0, v105\n"
      "v_cvt_pk_bf16_f32 v106, 0, v106\n"
      "v_cvt_pk_bf16_f32 v107, 0, v107\n"
      "v_cvt_pk_bf16_f32 v108, 0, v108\n"
      "v_cvt_pk_bf16_f32 v109, 0, v109\n"
      "global_load_dwordx4 a[68:71], v34, s[8:9] offset:-3072 sc1\n"
      "v_cvt_pk_bf16_f32 v110, 0, v110\n"
      "v_cvt_pk_bf16_f32 v111, 0, v111\n"
      "v_pk_mul_f32 v[104:105], v[104:105], v[208:209]\n"
      "v_pk_mul_f32 v[106:107], v[106:107], v[212:213]\n"
      "v_pk_mul_f32 v[108:109], v[108:109], v[216:217]\n"
      "v_pk_mul_f32 v[110:111], v[110:111], v[220:221]\n"
      "v_cvt_pk_bf16_f32 v104, 0, v104\n"
      "v_cvt_pk_bf16_f32 v105, 0, v105\n"
      "v_cvt_pk_bf16_f32 v106, 0, v106\n"
      "global_load_dwordx4 a[72:75], v34, s[8:9] offset:-2048 sc1\n"
      "v_cvt_pk_bf16_f32 v107, 0, v107\n"
      "v_cvt_pk_bf16_f32 v108, 0, v108\n"
      "v_cvt_pk_bf16_f32 v109, 0, v109\n"
      "v_cvt_pk_bf16_f32 v110, 0, v110\n"
      "v_cvt_pk_bf16_f32 v111, 0, v111\n"
      "v_pk_mul_f32 v[104:105], v[104:105], s[42:43] op_sel_hi:[1,0]\n"
      "v_pk_mul_f32 v[106:107], v[106:107], s[42:43] op_sel_hi:[1,0]\n"
      "v_pk_mul_f32 v[108:109], v[108:109], s[42:43] op_sel_hi:[1,0]\n"
      "v_pk_mul_f32 v[110:111], v[110:111], s[42:43] op_sel_hi:[1,0]\n"
      "global_load_dwordx4 a[76:79], v34, s[8:9] offset:-1024 sc1\n"
      "v_cvt_pk_bf16_f32 v104, 0, v104\n"
      "v_cvt_pk_bf16_f32 v105, 0, v105\n"
      "v_cvt_pk_bf16_f32 v106, 0, v106\n"
      "v_cvt_pk_bf16_f32 v107, 0, v107\n"
      "v_cvt_pk_bf16_f32 v108, 0, v108\n"
      "v_cvt_pk_bf16_f32 v109, 0, v109\n"
      "v_cvt_pk_bf16_f32 v110, 0, v110\n"
      "v_cvt_pk_bf16_f32 v111, 0, v111\n"
      "v_pk_add_f32 v[104:105], v[104:105], s[40:41] op_sel_hi:[1,0]\n"
      "global_load_dwordx4 a[80:83], v34, s[8:9] offset:0 sc1\n"
      "v_pk_add_f32 v[106:107], v[106:107], s[40:41] op_sel_hi:[1,0]\n"
      "v_pk_add_f32 v[108:109], v[108:109], s[40:41] op_sel_hi:[1,0]\n"
      "v_pk_add_f32 v[110:111], v[110:111], s[40:41] op_sel_hi:[1,0]\n"
      "v_cvt_pk_bf16_f32 v104, 0, v104\n"
      "v_cvt_pk_bf16_f32 v105, 0, v105\n"
      "v_cvt_pk_bf16_f32 v106, 0, v106\n"
      "v_cvt_pk_bf16_f32 v107, 0, v107\n"
      "v_cvt_pk_bf16_f32 v108, 0, v108\n"
      "v_cvt_pk_bf16_f32 v109, 0, v109\n"
      "global_load_dwordx4 a[84:87], v34, s[8:9] offset:1024 sc1\n"
      "v_cvt_pk_bf16_f32 v110, 0, v110\n"
      "v_cvt_pk_bf16_f32 v111, 0, v111\n"
      "v_and_b32 v120, 0xffff0000, v235\n"
      "v_lshlrev_b32 v121, 16, v235\n"
      "v_and_b32 v122, 0xffff0000, v234\n"
      "v_lshlrev_b32 v123, 16, v234\n"
      "v_and_b32 v124, 0xffff0000, v233\n"
      "v_lshlrev_b32 v125, 16, v233\n"
      "v_and_b32 v126, 0xffff0000, v232\n"
      "global_load_dwordx4 a[88:91], v34, s[8:9] offset:2048 sc1\n"
      "v_lshlrev_b32 v127, 16, v232\n"
      "v_pk_mul_f32 v[120:121], v[120:121], s[14:15] op_sel_hi:[1,0]\n"
      "v_pk_mul_f32 v[122:123], v[122:123], s[14:15] op_sel_hi:[1,0]\n"
      "v_pk_mul_f32 v[124:125], v[124:125], s[14:15] op_sel_hi:[1,0]\n"
      "v_pk_mul_f32 v[126:127], v[126:127], s[14:15] op_sel_hi:[1,0]\n"
      "v_cvt_pk_bf16_f32 v120, 0, v120\n"
      "v_cvt_pk_bf16_f32 v121, 0, v121\n"
      "v_cvt_pk_bf16_f32 v122, 0, v122\n"
      "v_cvt_pk_bf16_f32 v123, 0, v123\n"
      "global_load_dwordx4 a[92:95], v34, s[8:9] offset:3072 sc1\n"
      "v_cvt_pk_bf16_f32 v124, 0, v124\n"
      "v_cvt_pk_bf16_f32 v125, 0, v125\n"
      "v_cvt_pk_bf16_f32 v126, 0, v126\n"
      "v_cvt_pk_bf16_f32 v127, 0, v127\n"
      "s_add_u32 s43, s89, 32768\n"
      "s_add_u32 s53, s89, 0x1000000\n"
      "s_add_u32 s54, s43, 0x1000000\n"
      "s_add_u32 s55, s79, 176128\n"
      "s_add_u32 s45, s81, 65536\n"
      "v_add_u32 v34, 28672, v7\n"
      "global_load_dwordx4 a[96:99], v34, s[8:9] offset:-4096 sc1\n"
      "s_waitcnt lgkmcnt(0)\n"
      "v_pk_add_f32 v[64:65], v[64:65], v[72:73]\n"
      "v_pk_add_f32 v[66:67], v[66:67], v[74:75]\n"
      "v_pk_add_f32 v[68:69], v[68:69], v[76:77]\n"
      "v_pk_add_f32 v[70:71], v[70:71], v[78:79]\n"
      "v_cvt_pk_bf16_f32 v64, 0, v64\n"
      "v_cvt_pk_bf16_f32 v65, 0, v65\n"
      "v_cvt_pk_bf16_f32 v66, 0, v66\n"
      "v_cvt_pk_bf16_f32 v67, 0, v67\n"
      "global_load_dwordx4 a[100:103], v34, s[8:9] offset:-3072 sc1\n"
      "v_cvt_pk_bf16_f32 v68, 0, v68\n"
      "v_cvt_pk_bf16_f32 v69, 0, v69\n"
      "v_cvt_pk_bf16_f32 v70, 0, v70\n"
      "v_cvt_pk_bf16_f32 v71, 0, v71\n"
      "v_pk_add_f32 v[120:121], v[120:121], v[64:65]\n"
      "v_pk_add_f32 v[122:123], v[122:123], v[66:67]\n"
      "v_pk_add_f32 v[124:125], v[124:125], v[68:69]\n"
      "v_pk_add_f32 v[126:127], v[126:127], v[70:71]\n"
      "v_cvt_pk_bf16_f32 v120, 0, v120\n"
      "global_load_dwordx4 a[104:107], v34, s[8:9] offset:-2048 sc1\n"
      "v_cvt_pk_bf16_f32 v121, 0, v121\n"
      "v_cvt_pk_bf16_f32 v122, 0, v122\n"
      "v_cvt_pk_bf16_f32 v123, 0, v123\n"
      "v_cvt_pk_bf16_f32 v124, 0, v124\n"
      "v_cvt_pk_bf16_f32 v125, 0, v125\n"
      "v_cvt_pk_bf16_f32 v126, 0, v126\n"
      "v_cvt_pk_bf16_f32 v127, 0, v127\n"
      "v_pk_mul_f32 v[104:105], v[104:105], v[120:121]\n"
      "v_pk_mul_f32 v[106:107], v[106:107], v[122:123]\n"
      "global_load_dwordx4 a[108:111], v34, s[8:9] offset:-1024 sc1\n"
      "v_pk_mul_f32 v[108:109], v[108:109], v[124:125]\n"
      "v_pk_mul_f32 v[110:111], v[110:111], v[126:127]\n"
      "v_cvt_pk_bf16_f32 v104, 0, v104\n"
      "v_cvt_pk_bf16_f32 v105, 0, v105\n"
      "v_cvt_pk_bf16_f32 v106, 0, v106\n"
      "v_cvt_pk_bf16_f32 v107, 0, v107\n"
      "v_cvt_pk_bf16_f32 v108, 0, v108\n"
      "v_cvt_pk_bf16_f32 v109, 0, v109\n"
      "v_cvt_pk_bf16_f32 v110, 0, v110\n"
      "global_load_dwordx4 a[112:115], v34, s[8:9] offset:0 sc1\n"
      "v_cvt_pk_bf16_f32 v111, 0, v111\n"
      "v_pk_mul_f32 v[104:105], v[104:105], s[58:59] op_sel_hi:[1,0]\n"
      "v_pk_mul_f32 v[106:107], v[106:107], s[58:59] op_sel_hi:[1,0]\n"
      "v_pk_mul_f32 v[108:109], v[108:109], s[58:59] op_sel_hi:[1,0]\n"
      "v_pk_mul_f32 v[110:111], v[110:111], s[58:59] op_sel_hi:[1,0]\n"
      "v_cvt_pk_bf16_f32 v104, 0, v104\n"
      "v_cvt_pk_bf16_f32 v105, 0, v105\n"
      "v_cvt_pk_bf16_f32 v106, 0, v106\n"
      "v_cvt_pk_bf16_f32 v107, 0, v107\n"
      "global_load_dwordx4 a[116:119], v34, s[8:9] offset:1024 sc1\n"
      "v_cvt_pk_bf16_f32 v108, 0, v108\n"
      "v_cvt_pk_bf16_f32 v109, 0, v109\n"
      "v_cvt_pk_bf16_f32 v110, 0, v110\n"
      "v_cvt_pk_bf16_f32 v111, 0, v111\n"
      "v_pk_add_f32 v[104:105], v[104:105], v[112:113]\n"
      "v_pk_add_f32 v[106:107], v[106:107], v[114:115]\n"
      "v_pk_add_f32 v[108:109], v[108:109], v[116:117]\n"
      "v_pk_add_f32 v[110:111], v[110:111], v[118:119]\n"
      "v_cvt_pk_bf16_f32 v104, 0, v104\n"
      "global_load_dwordx4 a[120:123], v34, s[8:9] offset:2048 sc1\n"
      "v_cvt_pk_bf16_f32 v105, 0, v105\n"
      "v_cvt_pk_bf16_f32 v106, 0, v106\n"
      "v_cvt_pk_bf16_f32 v107, 0, v107\n"
      "v_cvt_pk_bf16_f32 v108, 0, v108\n"
      "v_cvt_pk_bf16_f32 v109, 0, v109\n"
      "v_cvt_pk_bf16_f32 v110, 0, v110\n"
      "v_cvt_pk_bf16_f32 v111, 0, v111\n"
      "v_pk_mul_f32 v[120:121], v[104:105], s[40:41] op_sel:[0,1] op_sel_hi:[1,1]\n"
      "v_pk_mul_f32 v[122:123], v[106:107], s[40:41] op_sel:[0,1] op_sel_hi:[1,1]\n"
      "v_pk_mul_f32 v[124:125], v[108:109], s[40:41] op_sel:[0,1] op_sel_hi:[1,1]\n"
      "v_pk_mul_f32 v[126:127], v[110:111], s[40:41] op_sel:[0,1] op_sel_hi:[1,1]\n"
      "v_cvt_pk_bf16_f32 v128, v120, v121\n"
      "v_cvt_pk_bf16_f32 v129, v122, v123\n"
      "v_cvt_pk_bf16_f32 v130, v124, v125\n"
      "v_cvt_pk_bf16_f32 v131, v126, v127\n"
      "buffer_store_dwordx4 v[128:131], v29, s[32:35], s89 offen sc1\n"
      "v_cvt_pk_bf16_f32 v132, v104, v105\n"
      "v_cvt_pk_bf16_f32 v133, v106, v107\n"
      "v_cvt_pk_bf16_f32 v134, v108, v109\n"
      "v_cvt_pk_bf16_f32 v135, v110, v111\n"
      "v_add3_u32 v35, v128, v129, v130\n"
      "v_add_u32 v35, v35, v131\n"
      "v_add3_u32 v38, v132, v133, v134\n"
      "v_add_u32 v38, v38, v135\n"
      "v_lshlrev_b32 v112, 16, v196\n"
      "v_add_u32_dpp v35, v35, v35 quad_perm:[1,0,3,2] row_mask:0xf bank_mask:0xf\n"
      "v_and_b32 v113, 0xffff0000, v196\n"
      "v_add_u32_dpp v38, v38, v38 quad_perm:[1,0,3,2] row_mask:0xf bank_mask:0xf\n"
      "v_lshlrev_b32 v114, 16, v197\n"
      "v_add_u32_dpp v35, v35, v35 quad_perm:[2,3,0,1] row_mask:0xf bank_mask:0xf\n"
      "v_and_b32 v115, 0xffff0000, v197\n"
      "v_add_u32_dpp v38, v38, v38 quad_perm:[2,3,0,1] row_mask:0xf bank_mask:0xf\n"
      "v_lshlrev_b32 v116, 16, v198\n"
      "v_add_u32_dpp v35, v35, v35 row_half_mirror row_mask:0xf bank_mask:0xf\n"
      "v_and_b32 v117, 0xffff0000, v198\n"
      "v_add_u32_dpp v38, v38, v38 row_ror:8 row_mask:0xf bank_mask:0xf\n"
      "v_lshlrev_b32 v118, 16, v199\n"
      "v_cndmask_b32 v35, v35, v38, vcc\n"
      "v_and_b32 v119, 0xffff0000, v199\n"
      "v_sub_u32 v35, s30, v35\n"
      "v_pk_add_f32 v[104:105], v[112:113], v[96:97] neg_lo:[0,1] neg_hi:[0,1]\n"
      "buffer_store_dword v35, v30, s[32:35], s81 offen sc1\n"
      "s_waitcnt vmcnt(26)\n"
      "buffer_store_dwordx2 v[52:53], v48, s[32:35], s88 offen sc1\n"
      "v_pk_add_f32 v[106:107], v[114:115], v[98:99] neg_lo:[0,1] neg_hi:[0,1]\n"
      "v_pk_add_f32 v[108:109], v[116:117], v[100:101] neg_lo:[0,1] neg_hi:[0,1]\n"
      "v_pk_add_f32 v[110:111], v[118:119], v[102:103] neg_lo:[0,1] neg_hi:[0,1]\n"
      "v_cvt_pk_bf16_f32 v104, 0, v104\n"
      "v_cvt_pk_bf16_f32 v105, 0, v105\n"
      "v_cvt_pk_bf16_f32 v106, 0, v106\n"
      "v_cvt_pk_bf16_f32 v107, 0, v107\n"
      "v_cvt_pk_bf16_f32 v108, 0, v108\n"
      "v_cvt_pk_bf16_f32 v109, 0, v109\n"
      "v_cvt_pk_bf16_f32 v110, 0, v110\n"
      "v_cvt_pk_bf16_f32 v111, 0, v111\n"
      "buffer_store_dwordx2 v[52:53], v48, s[32:35], s55 offen sc1\n"
      "v_pk_mul_f32 v[104:105], v[104:105], v[208:209]\n"
      "v_pk_mul_f32 v[106:107], v[106:107], v[212:213]\n"
      "v_pk_mul_f32 v[108:109], v[108:109], v[216:217]\n"
      "v_pk_mul_f32 v[110:111], v[110:111], v[220:221]\n"
      "v_cvt_pk_bf16_f32 v104, 0, v104\n"
      "v_cvt_pk_bf16_f32 v105, 0, v105\n"
      "v_cvt_pk_bf16_f32 v106, 0, v106\n"
      "v_cvt_pk_bf16_f32 v107, 0, v107\n"
      "v_cvt_pk_bf16_f32 v108, 0, v108\n"
      "v_cvt_pk_bf16_f32 v109, 0, v109\n"
      "v_cvt_pk_bf16_f32 v110, 0, v110\n"
      "global_load_dwordx4 a[124:127], v34, s[8:9] offset:3072 sc1\n"
      "v_cvt_pk_bf16_f32 v111, 0, v111\n"
      "v_pk_mul_f32 v[104:105], v[104:105], s[50:51] op_sel_hi:[1,0]\n"
      "v_pk_mul_f32 v[106:107], v[106:107], s[50:51] op_sel_hi:[1,0]\n"
      "v_pk_mul_f32 v[108:109], v[108:109], s[50:51] op_sel_hi:[1,0]\n"
      "v_pk_mul_f32 v[110:111], v[110:111], s[50:51] op_sel_hi:[1,0]\n"
      "v_cvt_pk_bf16_f32 v104, 0, v104\n"
      "v_cvt_pk_bf16_f32 v105, 0, v105\n"
      "v_cvt_pk_bf16_f32 v106, 0, v106\n"
      "v_cvt_pk_bf16_f32 v107, 0, v107\n"
      "v_cvt_pk_bf16_f32 v108, 0, v108\n"
      "v_cvt_pk_bf16_f32 v109, 0, v109\n"
      "s_add_u32 m0, s73, 0\n"
      "v_add_u32 v43, 0, v40\n"
      "global_load_lds_dwordx4 v43, s[4:5] offset:0 sc1\n"
      "v_cvt_pk_bf16_f32 v110, 0, v110\n"
      "v_cvt_pk_bf16_f32 v111, 0, v111\n"
      "v_pk_add_f32 v[104:105], v[104:105], s[48:49] op_sel_hi:[1,0]\n"
      "v_pk_add_f32 v[106:107], v[106:107], s[48:49] op_sel_hi:[1,0]\n"
      "v_pk_add_f32 v[108:109], v[108:109], s[48:49] op_sel_hi:[1,0]\n"
      "v_pk_add_f32 v[110:111], v[110:111], s[48:49] op_sel_hi:[1,0]\n"
      "v_cvt_pk_bf16_f32 v104, 0, v104\n"
      "v_cvt_pk_bf16_f32 v105, 0, v105\n"
      "v_cvt_pk_bf16_f32 v106, 0, v106\n"
      "v_cvt_pk_bf16_f32 v107, 0, v107\n"
      "v_cvt_pk_bf16_f32 v108, 0, v108\n"
      "global_load_lds_dwordx4 v43, s[4:5] offset:1024 sc1\n"
      "v_cvt_pk_bf16_f32 v109, 0, v109\n"
      "v_cvt_pk_bf16_f32 v110, 0, v110\n"
      "v_cvt_pk_bf16_f32 v111, 0, v111\n"
      "v_and_b32 v120, 0xffff0000, v239\n"
      "v_lshlrev_b32 v121, 16, v239\n"
      "v_and_b32 v122, 0xffff0000, v238\n"
      "v_lshlrev_b32 v123, 16, v238\n"
      "v_and_b32 v124, 0xffff0000, v237\n"
      "v_lshlrev_b32 v125, 16, v237\n"
      "v_and_b32 v126, 0xffff0000, v236\n"
      "v_lshlrev_b32 v127, 16, v236\n"
      "global_load_lds_dwordx4 v43, s[4:5] offset:2048 sc1\n"
      "v_pk_mul_f32 v[120:121], v[120:121], s[14:15] op_sel:[0,1] op_sel_hi:[1,1]\n"
      "v_pk_mul_f32 v[122:123], v[122:123], s[14:15] op_sel:[0,1] op_sel_hi:[1,1]\n"
      "v_pk_mul_f32 v[124:125], v[124:125], s[14:15] op_sel:[0,1] op_sel_hi:[1,1]\n"
      "v_pk_mul_f32 v[126:127], v[126:127], s[14:15] op_sel:[0,1] op_sel_hi:[1,1]\n"
      "v_cvt_pk_bf16_f32 v120, 0, v120\n"
      "v_cvt_pk_bf16_f32 v121, 0, v121\n"
      "v_cvt_pk_bf16_f32 v122, 0, v122\n"
      "v_cvt_pk_bf16_f32 v123, 0, v123\n"
      "v_cvt_pk_bf16_f32 v124, 0, v124\n"
      "v_cvt_pk_bf16_f32 v125, 0, v125\n"
      "v_cvt_pk_bf16_f32 v126, 0, v126\n"
      "global_load_lds_dwordx4 v43, s[4:5] offset:3072 sc1\n"
      "v_cvt_pk_bf16_f32 v127, 0, v127\n"
      "v_pk_add_f32 v[80:81], v[80:81], v[88:89]\n"
      "v_pk_add_f32 v[82:83], v[82:83], v[90:91]\n"
      "v_pk_add_f32 v[84:85], v[84:85], v[92:93]\n"
      "v_pk_add_f32 v[86:87], v[86:87], v[94:95]\n"
      "v_cvt_pk_bf16_f32 v80, 0, v80\n"
      "v_cvt_pk_bf16_f32 v81, 0, v81\n"
      "v_cvt_pk_bf16_f32 v82, 0, v82\n"
      "v_cvt_pk_bf16_f32 v83, 0, v83\n"
      "v_cvt_pk_bf16_f32 v84, 0, v84\n"
      "v_cvt_pk_bf16_f32 v85, 0, v85\n"
      "s_mov_b32 s47, 0\n"
      "buffer_load_dwordx2 v[2:3], v5, s[32:35], s47 offen sc1\n"
      "v_cvt_pk_bf16_f32 v86, 0, v86\n"
      "v_cvt_pk_bf16_f32 v87, 0, v87\n"
      "v_pk_add_f32 v[120:121], v[120:121], v[80:81]\n"
      "v_pk_add_f32 v[122:123], v[122:123], v[82:83]\n"
      "v_pk_add_f32 v[124:125], v[124:125], v[84:85]\n"
      "v_pk_add_f32 v[126:127], v[126:127], v[86:87]\n"
      "v_cvt_pk_bf16_f32 v120, 0, v120\n"
      "v_cvt_pk_bf16_f32 v121, 0, v121\n"
      "v_cvt_pk_bf16_f32 v122, 0, v122\n"
      "v_cvt_pk_bf16_f32 v123, 0, v123\n"
      "v_cvt_pk_bf16_f32 v124, 0, v124\n"
      "s_add_u32 m0, s73, 16384\n"
      "v_add_u32 v43, 81920, v40\n"
      "global_load_lds_dwordx4 v43, s[4:5] offset:0 sc1\n"
      "v_cvt_pk_bf16_f32 v125, 0, v125\n"
      "v_cvt_pk_bf16_f32 v126, 0, v126\n"
      "v_cvt_pk_bf16_f32 v127, 0, v127\n"
      "v_pk_mul_f32 v[104:105], v[104:105], v[120:121]\n"
      "v_pk_mul_f32 v[106:107], v[106:107], v[122:123]\n"
      "v_pk_mul_f32 v[108:109], v[108:109], v[124:125]\n"
      "v_pk_mul_f32 v[110:111], v[110:111], v[126:127]\n"
      "v_cvt_pk_bf16_f32 v104, 0, v104\n"
      "v_cvt_pk_bf16_f32 v105, 0, v105\n"
      "v_cvt_pk_bf16_f32 v106, 0, v106\n"
      "v_cvt_pk_bf16_f32 v107, 0, v107\n"
      "global_load_lds_dwordx4 v43, s[4:5] offset:1024 sc1\n"
      "v_cvt_pk_bf16_f32 v108, 0, v108\n"
      "v_cvt_pk_bf16_f32 v109, 0, v109\n"
      "v_cvt_pk_bf16_f32 v110, 0, v110\n"
      "v_cvt_pk_bf16_f32 v111, 0, v111\n"
      "v_pk_mul_f32 v[104:105], v[104:105], s[52:53] op_sel_hi:[1,0]\n"
      "v_pk_mul_f32 v[106:107], v[106:107], s[52:53] op_sel_hi:[1,0]\n"
      "v_pk_mul_f32 v[108:109], v[108:109], s[52:53] op_sel_hi:[1,0]\n"
      "v_pk_mul_f32 v[110:111], v[110:111], s[52:53] op_sel_hi:[1,0]\n"
      "v_cvt_pk_bf16_f32 v104, 0, v104\n"
      "v_cvt_pk_bf16_f32 v105, 0, v105\n"
      "v_cvt_pk_bf16_f32 v106, 0, v106\n"
      "global_load_lds_dwordx4 v43, s[4:5] offset:2048 sc1\n"
      "v_cvt_pk_bf16_f32 v107, 0, v107\n"
      "v_cvt_pk_bf16_f32 v108, 0, v108\n"
      "v_cvt_pk_bf16_f32 v109, 0, v109\n"
      "v_cvt_pk_bf16_f32 v110, 0, v110\n"
      "v_cvt_pk_bf16_f32 v111, 0, v111\n"
      "v_pk_add_f32 v[104:105], v[104:105], v[112:113]\n"
      "v_pk_add_f32 v[106:107], v[106:107], v[114:115]\n"
      "v_pk_add_f32 v[108:109], v[108:109], v[116:117]\n"
      "v_pk_add_f32 v[110:111], v[110:111], v[118:119]\n"
      "v_cvt_pk_bf16_f32 v104, 0, v104\n"
      "v_cvt_pk_bf16_f32 v105, 0, v105\n"
      "global_load_lds_dwordx4 v43, s[4:5] offset:3072 sc1\n"
      "v_cvt_pk_bf16_f32 v106, 0, v106\n"
      "v_cvt_pk_bf16_f32 v107, 0, v107\n"
      "v_cvt_pk_bf16_f32 v108, 0, v108\n"
      "v_cvt_pk_bf16_f32 v109, 0, v109\n"
      "v_cvt_pk_bf16_f32 v110, 0, v110\n"
      "v_cvt_pk_bf16_f32 v111, 0, v111\n"
      "v_pk_mul_f32 v[120:121], v[104:105], s[48:49] op_sel:[0,1] op_sel_hi:[1,1]\n"
      "v_pk_mul_f32 v[122:123], v[106:107], s[48:49] op_sel:[0,1] op_sel_hi:[1,1]\n"
      "v_pk_mul_f32 v[124:125], v[108:109], s[48:49] op_sel:[0,1] op_sel_hi:[1,1]\n"
      "v_pk_mul_f32 v[126:127], v[110:111], s[48:49] op_sel:[0,1] op_sel_hi:[1,1]\n"
      "v_cvt_pk_bf16_f32 v136, v120, v121\n"
      "v_cvt_pk_bf16_f32 v137, v122, v123\n"
      "v_cvt_pk_bf16_f32 v138, v124, v125\n"
      "v_cvt_pk_bf16_f32 v139, v126, v127\n"
      "buffer_store_dwordx4 v[136:139], v29, s[32:35], s43 offen sc1\n"
      "v_cvt_pk_bf16_f32 v140, v104, v105\n"
      "v_cvt_pk_bf16_f32 v141, v106, v107\n"
      "v_cvt_pk_bf16_f32 v142, v108, v109\n"
      "v_cvt_pk_bf16_f32 v143, v110, v111\n"
      "v_add3_u32 v35, v136, v137, v138\n"
      "v_add_u32 v35, v35, v139\n"
      "v_add3_u32 v38, v140, v141, v142\n"
      "v_add_u32 v38, v38, v143\n"
      "v_add_u32_dpp v35, v35, v35 quad_perm:[1,0,3,2] row_mask:0xf bank_mask:0xf\n"
      "s_nop 0\n"
      "v_add_u32_dpp v38, v38, v38 quad_perm:[1,0,3,2] row_mask:0xf bank_mask:0xf\n"
      "v_add_u32_dpp v35, v35, v35 quad_perm:[2,3,0,1] row_mask:0xf bank_mask:0xf\n"
      "s_nop 0\n"
      "v_add_u32_dpp v38, v38, v38 quad_perm:[2,3,0,1] row_mask:0xf bank_mask:0xf\n"
      "v_add_u32_dpp v35, v35, v35 row_half_mirror row_mask:0xf bank_mask:0xf\n"
      "s_nop 0\n"
      "v_add_u32_dpp v38, v38, v38 row_ror:8 row_mask:0xf bank_mask:0xf\n"
      "v_cndmask_b32 v35, v35, v38, vcc\n"
      "v_sub_u32 v35, s30, v35\n"
      "buffer_store_dword v35, v30, s[32:35], s45 offen sc1\n"
      "v_add_u32 v34, 36864, v7\n"
      "global_load_dwordx4 a[128:131], v34, s[8:9] offset:-4096 sc1\n"
      "global_load_dwordx4 a[132:135], v34, s[8:9] offset:-3072 sc1\n"
      "global_load_dwordx4 a[136:139], v34, s[8:9] offset:-2048 sc1\n"
      "global_load_dwordx4 a[140:143], v34, s[8:9] offset:-1024 sc1\n"
      "global_load_dwordx4 a[144:147], v34, s[8:9] offset:0 sc1\n"
      "global_load_dwordx4 a[148:151], v34, s[8:9] offset:1024 sc1\n"
      "global_load_dwordx4 a[152:155], v34, s[8:9] offset:2048 sc1\n"
      "global_load_dwordx4 a[156:159], v34, s[8:9] offset:3072 sc1\n"
      "s_add_u32 m0, s73, 32768\n"
      "v_add_u32 v43, 16384, v40\n"
      "global_load_lds_dwordx4 v43, s[4:5] offset:0 sc1\n"
      "global_load_lds_dwordx4 v43, s[4:5] offset:1024 sc1\n"
      "global_load_lds_dwordx4 v43, s[4:5] offset:2048 sc1\n"
      "global_load_lds_dwordx4 v43, s[4:5] offset:3072 sc1\n"
      "s_mov_b32 s47, 32768\n"
      "buffer_load_dwordx2 v[48:49], v5, s[32:35], s47 offen sc1\n"
      "buffer_store_dwordx4 v[132:135], v36, s[32:35], s53 offen sc1\n"
      "buffer_store_dwordx4 v[140:143], v36, s[32:35], s54 offen sc1\n"
      "v_xor_b32 v33, 32, v6\n"
      "v_xor_b32 v34, 64, v6\n"
      "v_xor_b32 v35, 96, v6\n"
      "v_add_u32 v45, 45056, v7\n"
      "s_waitcnt vmcnt(21)\n"
      "ds_read_b128 v[248:251], v40 offset:0\n"
      "ds_read_b128 v[252:255], v40 offset:1024\n"
      "ds_read_b128 v[36:39], v40 offset:2048\n"
      "ds_read_b128 v[60:63], v40 offset:3072\n"
      "s_waitcnt lgkmcnt(3)\n"
      "v_add3_u32 v2, v2, v248, v249\n"
      "v_add3_u32 v2, v2, v250, v251\n"
      "s_waitcnt lgkmcnt(2)\n"
      "v_add3_u32 v2, v2, v252, v253\n"
      "v_add3_u32 v2, v2, v254, v255\n"
      "s_waitcnt lgkmcnt(1)\n"
      "v_add3_u32 v2, v2, v36, v37\n"
      "v_add3_u32 v2, v2, v38, v39\n"
      "s_waitcnt lgkmcnt(0)\n"
      "v_add3_u32 v2, v2, v60, v61\n"
      "v_add3_u32 v2, v2, v62, v63\n"
      "s_nop 1\n"
      "v_add_u32_dpp v2, v2, v2 quad_perm:[1,0,3,2] row_mask:0xf bank_mask:0xf\n"
      "s_nop 1\n"
      "v_add_u32_dpp v2, v2, v2 quad_perm:[2,3,0,1] row_mask:0xf bank_mask:0xf\n"
      "s_nop 1\n"
      "v_add_u32_dpp v2, v2, v2 row_half_mirror row_mask:0xf bank_mask:0xf\n"
      "v_cmp_ne_u32 vcc, s71, v2\n"
      "s_cbranch_vccnz L_sx0_%=\n"
      "L_bx0_%=:\n"
      "s_barrier\n"
      "ds_read_b64_tr_b16 v[64:65], v6 offset:0\n"
      "ds_read_b64_tr_b16 v[66:67], v6 offset:2048\n"
      "ds_read_b64_tr_b16 v[80:81], v6 offset:4096\n"
      "ds_read_b64_tr_b16 v[82:83], v6 offset:6144\n"
      "ds_read_b64_tr_b16 v[68:69], v33 offset:0\n"
      "ds_read_b64_tr_b16 v[70:71], v33 offset:2048\n"
      "ds_read_b64_tr_b16 v[84:85], v33 offset:4096\n"
      "ds_read_b64_tr_b16 v[86:87], v33 offset:6144\n"
      "ds_read_b64_tr_b16 v[72:73], v34 offset:0\n"
      "ds_read_b64_tr_b16 v[74:75], v34 offset:2048\n"
      "ds_read_b64_tr_b16 v[88:89], v34 offset:4096\n"
      "ds_read_b64_tr_b16 v[90:91], v34 offset:6144\n"
      "ds_read_b64_tr_b16 v[76:77], v35 offset:0\n"
      "ds_read_b64_tr_b16 v[78:79], v35 offset:2048\n"
      "ds_read_b64_tr_b16 v[92:93], v35 offset:4096\n"
      "ds_read_b64_tr_b16 v[94:95], v35 offset:6144\n"
      "s_waitcnt lgkmcnt(0)\n"
      "v_mfma_f32_16x16x32_bf16 v[128:131], a[0:3], v[64:67], 0\n"
      "global_load_dwordx4 a[160:163], v45, s[8:9] offset:-4096 sc1\n"
      "v_lshrrev_b32 v42, 8, v7\n"
      "v_mfma_f32_16x16x32_bf16 v[128:131], a[12:15], v[80:83], v[128:131]\n"
      "ds_read_b64_tr_b16 v[96:97], v6 offset:8192\n"
      "v_bfe_u32 v41, v7, 4, 4\n"
      "v_mfma_f32_16x16x32_bf16 v[144:147], a[4:7], v[64:67], 0\n"
      "ds_read_b64_tr_b16 v[98:99], v6 offset:10240\n"
      "s_waitcnt vmcnt(18)\n"
      "v_mfma_f32_16x16x32_bf16 v[144:147], a[16:19], v[80:83], v[144:147]\n"
      "ds_read_b128 v[248:251], v40 offset:16384\n"
      "v_min_u32 v42, 2, v42\n"
      "v_mfma_f32_16x16x32_bf16 v[160:163], a[8:11], v[64:67], 0\n"
      "global_load_dwordx4 a[164:167], v45, s[8:9] offset:-3072 sc1\n"
      "ds_read_b128 v[252:255], v40 offset:17408\n"
      "ds_read_b64_tr_b16 v[100:101], v33 offset:8192\n"
      "v_min_u32 v41, 11, v41\n"
      "v_mfma_f32_16x16x32_bf16 v[160:163], a[20:23], v[80:83], v[160:163]\n"
      "ds_read_b128 v[36:39], v40 offset:18432\n"
      "ds_read_b64_tr_b16 v[102:103], v33 offset:10240\n"
      "v_mad_u32_u24 v42, v42, 48, v41\n"
      "v_mfma_f32_16x16x32_bf16 v[132:135], a[0:3], v[68:71], 0\n"
      "ds_read_b128 v[60:63], v40 offset:19456\n"
      "ds_read_b64_tr_b16 v[104:105], v34 offset:8192\n"
      "v_lshl_add_u32 v42, v42, 6, s64\n"
      "v_mfma_f32_16x16x32_bf16 v[132:135], a[12:15], v[84:87], v[132:135]\n"
      "ds_read_b64_tr_b16 v[106:107], v34 offset:10240\n"
      "s_add_u32 s75, s76, 176128\n"
      "v_mfma_f32_16x16x32_bf16 v[148:151], a[4:7], v[68:71], 0\n"
      "global_load_dwordx4 a[168:171], v45, s[8:9] offset:-2048 sc1\n"
      "ds_read_b64_tr_b16 v[108:109], v35 offset:8192\n"
      "v_mfma_f32_16x16x32_bf16 v[148:151], a[16:19], v[84:87], v[148:151]\n"
      "ds_read_b64_tr_b16 v[110:111], v35 offset:10240\n"
      "v_mfma_f32_16x16x32_bf16 v[164:167], a[8:11], v[68:71], 0\n"
      "ds_read_b64_tr_b16 v[112:113], v6 offset:12288\n"
      "s_waitcnt lgkmcnt(5)\n"
      "v_add3_u32 v3, v3, v248, v249\n"
      "v_add3_u32 v3, v3, v250, v251\n"
      "v_mfma_f32_16x16x32_bf16 v[164:167], a[20:23], v[84:87], v[164:167]\n"
      "ds_read_b64_tr_b16 v[114:115], v6 offset:14336\n"
      "v_add3_u32 v3, v3, v252, v253\n"
      "v_add3_u32 v3, v3, v254, v255\n"
      "v_mfma_f32_16x16x32_bf16 v[136:139], a[0:3], v[72:75], 0\n"
      "global_load_dwordx4 a[172:175], v45, s[8:9] offset:-1024 sc1\n"
      "v_add3_u32 v3, v3, v36, v37\n"
      "v_add3_u32 v3, v3, v38, v39\n"
      "v_mfma_f32_16x16x32_bf16 v[136:139], a[12:15], v[88:91], v[136:139]\n"
      "ds_read_b64_tr_b16 v[116:117], v33 offset:12288\n"
      "v_add3_u32 v3, v3, v60, v61\n"
      "v_mfma_f32_16x16x32_bf16 v[152:155], a[4:7], v[72:75], 0\n"
      "ds_read_b64_tr_b16 v[118:119], v33 offset:14336\n"
      "v_add3_u32 v3, v3, v62, v63\n"
      "v_mfma_f32_16x16x32_bf16 v[152:155], a[16:19], v[88:91], v[152:155]\n"
      "L_pg3_%=:\n"
      "s_cbranch_execz L_pg7_%=\n"
      "ds_read_b64_tr_b16 v[120:121], v34 offset:12288\n"
      "v_mfma_f32_16x16x32_bf16 v[168:171], a[8:11], v[72:75], 0\n"
      "global_load_dwordx4 a[176:179], v45, s[8:9] offset:0 sc1\n"
      "ds_read_b64_tr_b16 v[122:123], v34 offset:14336\n"
      "v_add_u32_dpp v3, v3, v3 quad_perm:[1,0,3,2] row_mask:0xf bank_mask:0xf\n"
      "v_mfma_f32_16x16x32_bf16 v[168:171], a[20:23], v[88:91], v[168:171]\n"
      "ds_read_b64_tr_b16 v[124:125], v35 offset:12288\n"
      "v_mfma_f32_16x16x32_bf16 v[140:143], a[0:3], v[76:79], 0\n"
      "ds_read_b64_tr_b16 v[126:127], v35 offset:14336\n"
      "v_add_u32_dpp v3, v3, v3 quad_perm:[2,3,0,1] row_mask:0xf bank_mask:0xf\n"
      "v_mfma_f32_16x16x32_bf16 v[140:143], a[12:15], v[92:95], v[140:143]\n"
      "v_mfma_f32_16x16x32_bf16 v[156:159], a[4:7], v[76:79], 0\n"
      "global_load_dwordx4 a[180:183], v45, s[8:9] offset:1024 sc1\n"
      "v_add_u32_dpp v3, v3, v3 row_half_mirror row_mask:0xf bank_mask:0xf\n"
      "v_cmp_ne_u32 vcc, s71, v3\n"
      "v_mfma_f32_16x16x32_bf16 v[156:159], a[16:19], v[92:95], v[156:159]\n"
      "v_mfma_f32_16x16x32_bf16 v[172:175], a[8:11], v[76:79], 0\n"
      "s_cbranch_vccnz L_sx1_%=\n"
      "L_bx1_%=:\n"
      "v_mfma_f32_16x16x32_bf16 v[172:175], a[20:23], v[92:95], v[172:175]\n"
      "s_waitcnt lgkmcnt(0)\n"
      "s_barrier\n"
      "v_mfma_f32_16x16x32_bf16 v[128:131], a[24:27], v[96:99], v[128:131]\n"
      "global_load_dwordx4 a[184:187], v45, s[8:9] offset:2048 sc1\n"
      "v_and_b32 v41, 0x1f0, v7\n"
      "v_mfma_f32_16x16x32_bf16 v[128:131], a[36:39], v[112:115], v[128:131]\n"
      "ds_read_b64_tr_b16 v[64:65], v6 offset:16384\n"
      "v_min_u32 v41, 0x170, v41\n"
      "v_mfma_f32_16x16x32_bf16 v[144:147], a[28:31], v[96:99], v[144:147]\n"
      "ds_read_b64_tr_b16 v[66:67], v6 offset:18432\n"
      "v_lshrrev_b32 v52, 9, v7\n"
      "v_mfma_f32_16x16x32_bf16 v[144:147], a[40:43], v[112:115], v[144:147]\n"
      "ds_read_b64_tr_b16 v[68:69], v34 offset:16384\n"
      "v_mul_u32_u24 v52, 0x3c000, v52\n"
      "v_mfma_f32_16x16x32_bf16 v[160:163], a[32:35], v[96:99], v[160:163]\n"
      "global_load_dwordx2 v[58:59], v42, s[26:27] sc1\n"
      "v_add3_u32 v53, v52, v41, s65\n"
      "v_mfma_f32_16x16x32_bf16 v[160:163], a[44:47], v[112:115], v[160:163]\n"
      "ds_read_b64_tr_b16 v[70:71], v34 offset:18432\n"
      "s_mul_i32 s96, s60, 384\n"
      "v_mfma_f32_16x16x32_bf16 v[132:135], a[24:27], v[100:103], v[132:135]\n"
      "ds_read_b64_tr_b16 v[72:73], v33 offset:16384\n"
      "s_add_u32 s96, s96, 160768\n"
      "v_mfma_f32_16x16x32_bf16 v[132:135], a[36:39], v[116:119], v[132:135]\n"
      "ds_read_b64_tr_b16 v[74:75], v33 offset:18432\n"
      "v_lshrrev_b32 v52, 4, v7\n"
      "v_mfma_f32_16x16x32_bf16 v[148:151], a[28:31], v[100:103], v[148:151]\n"
      "ds_read_b64_tr_b16 v[76:77], v35 offset:16384\n"
      "v_and_b32 v52, 0x30, v52\n"
      "v_mfma_f32_16x16x32_bf16 v[148:151], a[40:43], v[116:119], v[148:151]\n"
      "ds_read_b64_tr_b16 v[78:79], v35 offset:18432\n"
      "v_add_u32 v52, s96, v52\n"
      "v_mfma_f32_16x16x32_bf16 v[164:167], a[32:35], v[100:103], v[164:167]\n"
      "ds_read_b64_tr_b16 v[80:81], v6 offset:20480\n"
      "s_mul_i32 s40, s60, 9216\n"
      "v_mfma_f32_16x16x32_bf16 v[164:167], a[44:47], v[116:119], v[164:167]\n"
      "ds_read_b64_tr_b16 v[82:83], v6 offset:22528\n"
      "s_add_u32 s40, s40, 122880\n"
      "v_mfma_f32_16x16x32_bf16 v[136:139], a[24:27], v[104:107], v[136:139]\n"
      "ds_read_b64_tr_b16 v[84:85], v34 offset:20480\n"
      "v_bfe_u32 v30, v7, 4, 3\n"
      "v_mfma_f32_16x16x32_bf16 v[136:139], a[36:39], v[120:123], v[136:139]\n"
      "ds_read_b64_tr_b16 v[86:87], v34 offset:22528\n"
      "v_lshrrev_b32 v31, 8, v7\n"
      "v_mfma_f32_16x16x32_bf16 v[152:155], a[28:31], v[104:107], v[152:155]\n"
      "buffer_load_dwordx2 v[46:47], v51, s[32:35], s75 offen sc1\n"
      "v_lshl_add_u32 v31, v30, 2, v31\n"
      "v_mfma_f32_16x16x32_bf16 v[152:155], a[40:43], v[120:123], v[152:155]\n"
      "ds_read_b64_tr_b16 v[88:89], v33 offset:20480\n"
      "v_sub_u32 v30, 2, v30\n"
      "v_mfma_f32_16x16x32_bf16 v[168:171], a[32:35], v[104:107], v[168:171]\n"
      "ds_read_b64_tr_b16 v[90:91], v33 offset:22528\n"
      "v_and_b32 v29, 64, v4\n"
      "v_mfma_f32_16x16x32_bf16 v[168:171], a[44:47], v[120:123], v[168:171]\n"
      "ds_read_b64_tr_b16 v[92:93], v35 offset:20480\n"
      "s_add_u32 m0, s73, 49152\n"
      "v_add_u32 v43, 32768, v40\n"
      "v_mfma_f32_16x16x32_bf16 v[140:143], a[24:27], v[108:111], v[140:143]\n"
      "global_load_lds_dwordx4 v43, s[4:5] offset:0 sc1\n"
      "v_lshl_add_u32 v31, v31, 9, v29\n"
      "v_mfma_f32_16x16x32_bf16 v[140:143], a[36:39], v[124:127], v[140:143]\n"
      "ds_read_b64_tr_b16 v[94:95], v35 offset:22528\n"
      "v_and_b32 v30, 0x80000000, v30\n"
      "v_mfma_f32_16x16x32_bf16 v[156:159], a[28:31], v[108:111], v[156:159]\n"
      "v_or_b32 v31, v31, v30\n"
      "v_add_u32 v50, s94, v7\n"
      "v_mfma_f32_16x16x32_bf16 v[156:159], a[40:43], v[124:127], v[156:159]\n"
      "s_mul_i32 s72, s30, 3\n"
      "s_lshl_b32 s42, s60, 5\n"
      "v_mfma_f32_16x16x32_bf16 v[172:175], a[32:35], v[108:111], v[172:175]\n"
      "global_load_lds_dwordx4 v43, s[4:5] offset:1024 sc1\n"
      "v_xor_b32 v27, s42, v6\n"
      "v_mfma_f32_16x16x32_bf16 v[172:175], a[44:47], v[124:127], v[172:175]\n"
      "v_add_u32 v27, 110592, v27\n"
      "s_waitcnt lgkmcnt(0)\n"
      "v_mfma_f32_16x16x32_bf16 v[176:179], a[0:3], v[64:67], 0\n"
      "ds_read_b64_tr_b16 v[96:97], v6 offset:24576\n"
      "v_mov_b32 v18, 0\n"
      "v_mfma_f32_16x16x32_bf16 v[176:179], a[12:15], v[80:83], v[176:179]\n"
      "ds_read_b64_tr_b16 v[98:99], v6 offset:26624\n"
      "v_mov_b32 v19, 0\n"
      "v_mfma_f32_16x16x32_bf16 v[184:187], a[4:7], v[64:67], 0\n"
      "global_load_lds_dwordx4 v43, s[4:5] offset:2048 sc1\n"
      "s_waitcnt vmcnt(14)\n"
      "v_mfma_f32_16x16x32_bf16 v[184:187], a[16:19], v[80:83], v[184:187]\n"
      "ds_read_b128 v[248:251], v40 offset:32768\n"
      "v_mov_b32 v28, 0x3f803f80\n"
      "v_mfma_f32_16x16x32_bf16 v[192:195], a[8:11], v[64:67], 0\n"
      "ds_read_b128 v[252:255], v40 offset:33792\n"
      "ds_read_b64_tr_b16 v[100:101], v34 offset:24576\n"
      "v_mov_b32 v29, 0x3f803f80\n"
      "v_mov_b32 v30, 0x3f803f80\n"
      "v_mfma_f32_16x16x32_bf16 v[192:195], a[20:23], v[80:83], v[192:195]\n"
      "global_load_lds_dwordx4 v43, s[4:5] offset:3072 sc1\n"
      "ds_read_b128 v[36:39], v40 offset:34816\n"
      "ds_read_b64_tr_b16 v[102:103], v34 offset:26624\n"
      "s_mov_b32 s47, 65536\n"
      "s_lshl_b32 s41, s60, 13\n"
      "v_mfma_f32_16x16x32_bf16 v[180:183], a[0:3], v[68:71], 0\n"
      "buffer_load_dwordx2 v[54:55], v5, s[32:35], s47 offen sc1\n"
      "ds_read_b128 v[60:63], v40 offset:35840\n"
      "ds_read_b64_tr_b16 v[104:105], v33 offset:24576\n"
      "s_mov_b32 m0, s94\n"
      "v_mfma_f32_16x16x32_bf16 v[180:183], a[12:15], v[84:87], v[180:183]\n"
      "buffer_load_dwordx4 v7, s[32:35], s67 offen offset:0 sc1 lds\n"
      "ds_read_b64_tr_b16 v[106:107], v33 offset:26624\n"
      "s_add_u32 s41, s41, 65536\n"
      "v_mfma_f32_16x16x32_bf16 v[188:191], a[4:7], v[68:71], 0\n"
      "ds_read_b64_tr_b16 v[108:109], v35 offset:24576\n"
      "s_mov_b32 s48, 0xaaaaaaaa\n"
      "v_mfma_f32_16x16x32_bf16 v[188:191], a[16:19], v[84:87], v[188:191]\n"
      "ds_read_b64_tr_b16 v[110:111], v35 offset:26624\n"
      "s_mov_b32 s49, s48\n"
      "v_mfma_f32_16x16x32_bf16 v[196:199], a[8:11], v[68:71], 0\n"
      "ds_read_b64_tr_b16 v[112:113], v6 offset:28672\n"
      "s_waitcnt lgkmcnt(5)\n"
      "v_add3_u32 v48, v48, v248, v249\n"
      "v_add3_u32 v48, v48, v250, v251\n"
      "v_mfma_f32_16x16x32_bf16 v[196:199], a[20:23], v[84:87], v[196:199]\n"
      "buffer_load_dwordx4 v7, s[32:35], s67 offen offset:1024 sc1 lds\n"
      "v_add3_u32 v48, v48, v252, v253\n"
      "v_add3_u32 v48, v48, v254, v255\n"
      "v_mfma_f32_16x16x32_bf16 v[200:203], a[0:3], v[72:75], 0\n"
      "ds_read_b64_tr_b16 v[114:115], v6 offset:30720\n"
      "v_add3_u32 v48, v48, v36, v37\n"
      "v_add3_u32 v48, v48, v38, v39\n"
      "v_mfma_f32_16x16x32_bf16 v[200:203], a[12:15], v[88:91], v[200:203]\n"
      "ds_read_b64_tr_b16 v[116:117], v34 offset:28672\n"
      "v_add3_u32 v48, v48, v60, v61\n"
      "v_mfma_f32_16x16x32_bf16 v[208:211], a[4:7], v[72:75], 0\n"
      "ds_read_b64_tr_b16 v[118:119], v34 offset:30720\n"
      "v_add3_u32 v48, v48, v62, v63\n"
      "s_mov_b32 s50, 0xcccccccc\n"
      "v_mfma_f32_16x16x32_bf16 v[208:211], a[16:19], v[88:91], v[208:211]\n"
      "buffer_load_dwordx4 v7, s[32:35], s67 offen offset:2048 sc1 lds\n"
      "ds_read_b64_tr_b16 v[120:121], v33 offset:28672\n"
      "s_mov_b32 s51, s50\n"
      "v_mfma_f32_16x16x32_bf16 v[216:219], a[8:11], v[72:75], 0\n"
      "ds_read_b64_tr_b16 v[122:123], v33 offset:30720\n"
      "v_add_u32_dpp v48, v48, v48 quad_perm:[1,0,3,2] row_mask:0xf bank_mask:0xf\n"
      "v_mfma_f32_16x16x32_bf16 v[216:219], a[20:23], v[88:91], v[216:219]\n"
      "ds_read_b64_tr_b16 v[124:125], v35 offset:28672\n"
      "s_lshl_b32 s59, s60, 31\n"
      "v_mfma_f32_16x16x32_bf16 v[204:207], a[0:3], v[76:79], 0\n"
      "ds_read_b64_tr_b16 v[126:127], v35 offset:30720\n"
      "v_add_u32_dpp v48, v48, v48 quad_perm:[2,3,0,1] row_mask:0xf bank_mask:0xf\n"
      "v_mfma_f32_16x16x32_bf16 v[204:207], a[12:15], v[92:95], v[204:207]\n"
      "buffer_load_dword v32, v31, s[32:35], s68 offen sc1\n"
      "s_lshl_b32 s54, s30, 1\n"
      "v_mfma_f32_16x16x32_bf16 v[212:215], a[4:7], v[76:79], 0\n"
      "v_add_u32_dpp v48, v48, v48 row_half_mirror row_mask:0xf bank_mask:0xf\n"
      "v_cmp_ne_u32 vcc, s71, v48\n"
      "v_mfma_f32_16x16x32_bf16 v[212:215], a[16:19], v[92:95], v[212:215]\n"
      "s_lshl_b32 s55, s60, 6\n"
      "s_add_u32 s55, s55, 163328\n"
      "v_mfma_f32_16x16x32_bf16 v[220:223], a[8:11], v[76:79], 0\n"
      "s_cbranch_vccnz L_sx2_%=\n"
      "L_bx2_%=:\n"
      "v_mfma_f32_16x16x32_bf16 v[220:223], a[20:23], v[92:95], v[220:223]\n"
      "s_waitcnt lgkmcnt(0)\n"
      "s_barrier\n"
      "v_mfma_f32_16x16x32_bf16 v[176:179], a[24:27], v[96:99], v[176:179]\n"
      "ds_read_b64_tr_b16 v[64:65], v6 offset:32768\n"
      "v_mfma_f32_16x16x32_bf16 v[176:179], a[36:39], v[112:115], v[176:179]\n"
      "ds_read_b64_tr_b16 v[66:67], v6 offset:34816\n"
      "v_mfma_f32_16x16x32_bf16 v[184:187], a[28:31], v[96:99], v[184:187]\n"
      "ds_read_b64_tr_b16 v[68:69], v33 offset:32768\n"
      "s_waitcnt vmcnt(9)\n"
      "v_cmp_ne_u64 s[52:53], s[30:31], v[46:47]\n"
      "v_mfma_f32_16x16x32_bf16 v[184:187], a[40:43], v[112:115], v[184:187]\n"
      "ds_read_b64_tr_b16 v[70:71], v33 offset:34816\n"
      "s_add_u32 m0, s73, 0\n"
      "v_add_u32 v43, 49152, v40\n"
      "v_add_u32 v44, 4096, v7\n"
      "v_cmp_ne_u64 vcc, s[30:31], v[58:59]\n"
      "v_mfma_f32_16x16x32_bf16 v[192:195], a[32:35], v[96:99], v[192:195]\n"
      "ds_read_b64_tr_b16 v[72:73], v34 offset:32768\n"
      "s_waitcnt vmcnt(4)\n"
      "v_mfma_f32_16x16x32_bf16 v[192:195], a[44:47], v[112:115], v[192:195]\n"
      "global_load_lds_dwordx4 v43, s[4:5] offset:0 sc1\n"
      "ds_read_b128 v[248:251], v40 offset:49152\n"
      "s_or_b64 vcc, vcc, s[52:53]\n"
      "v_mfma_f32_16x16x32_bf16 v[180:183], a[24:27], v[100:103], v[180:183]\n"
      "ds_read_b128 v[252:255], v40 offset:50176\n"
      "s_cbranch_vccnz L_sfd_%=\n"
      "L_bfd_%=:\n"
      "v_mfma_f32_16x16x32_bf16 v[180:183], a[36:39], v[116:119], v[180:183]\n"
      "ds_read_b128 v[36:39], v40 offset:51200\n"
      "ds_read_b64_tr_b16 v[74:75], v34 offset:34816\n"
      "v_add_u32 v51, s96, v41\n"
      "v_mfma_f32_16x16x32_bf16 v[188:191], a[28:31], v[100:103], v[188:191]\n"
      "ds_read_b128 v[60:63], v40 offset:52224\n"
      "ds_read_b64_tr_b16 v[76:77], v35 offset:32768\n"
      "v_lshrrev_b32 v42, 2, v7\n"
      "v_mfma_f32_16x16x32_bf16 v[188:191], a[40:43], v[116:119], v[188:191]\n"
      "global_load_lds_dwordx4 v43, s[4:5] offset:1024 sc1\n"
      "ds_read_b64_tr_b16 v[78:79], v35 offset:34816\n"
      "s_lshl_b32 s97, s60, 10\n"
      "v_mfma_f32_16x16x32_bf16 v[196:199], a[32:35], v[100:103], v[196:199]\n"
      "ds_read_b64_tr_b16 v[80:81], v6 offset:36864\n"
      "s_add_u32 s96, s97, 98304\n"
      "v_mfma_f32_16x16x32_bf16 v[196:199], a[44:47], v[116:119], v[196:199]\n"
      "ds_read_b64_tr_b16 v[82:83], v6 offset:38912\n"
      "v_add_u32 v47, s96, v7\n"
      "v_mfma_f32_16x16x32_bf16 v[200:203], a[24:27], v[104:107], v[200:203]\n"
      "ds_read_b64_tr_b16 v[84:85], v33 offset:36864\n"
      "s_waitcnt lgkmcnt(5)\n"
      "v_add3_u32 v54, v54, v248, v249\n"
      "v_add3_u32 v54, v54, v250, v251\n"
      "v_mfma_f32_16x16x32_bf16 v[200:203], a[36:39], v[120:123], v[200:203]\n"
      "global_load_lds_dwordx4 v43, s[4:5] offset:2048 sc1\n"
      "v_add3_u32 v54, v54, v252, v253\n"
      "v_add3_u32 v54, v54, v254, v255\n"
      "v_mfma_f32_16x16x32_bf16 v[208:211], a[28:31], v[104:107], v[208:211]\n"
      "ds_read_b64_tr_b16 v[86:87], v33 offset:38912\n"
      "v_add3_u32 v54, v54, v36, v37\n"
      "v_add3_u32 v54, v54, v38, v39\n"
      "v_mfma_f32_16x16x32_bf16 v[208:211], a[40:43], v[120:123], v[208:211]\n"
      "ds_read_b64_tr_b16 v[88:89], v34 offset:36864\n"
      "v_add3_u32 v54, v54, v60, v61\n"
      "v_mfma_f32_16x16x32_bf16 v[216:219], a[32:35], v[104:107], v[216:219]\n"
      "ds_read_b64_tr_b16 v[90:91], v34 offset:38912\n"
      "v_add3_u32 v54, v54, v62, v63\n"
      "v_mfma_f32_16x16x32_bf16 v[216:219], a[44:47], v[120:123], v[216:219]\n"
      "global_load_lds_dwordx4 v43, s[4:5] offset:3072 sc1\n"
      "ds_read_b64_tr_b16 v[92:93], v35 offset:36864\n"
      "s_mov_b32 s47, 98304\n"
      "v_mfma_f32_16x16x32_bf16 v[204:207], a[24:27], v[108:111], v[204:207]\n"
      "ds_read_b64_tr_b16 v[94:95], v35 offset:38912\n"
      "v_add_u32_dpp v54, v54, v54 quad_perm:[1,0,3,2] row_mask:0xf bank_mask:0xf\n"
      "v_mfma_f32_16x16x32_bf16 v[204:207], a[36:39], v[124:127], v[204:207]\n"
      "v_bfe_u32 v46, v7, 8, 1\n"
      "v_xor_b32 v46, s60, v46\n"
      "v_mfma_f32_16x16x32_bf16 v[212:215], a[28:31], v[108:111], v[212:215]\n"
      "v_add_u32_dpp v54, v54, v54 quad_perm:[2,3,0,1] row_mask:0xf bank_mask:0xf\n"
      "v_lshl_add_u32 v46, v46, 2, v7\n"
      "v_mfma_f32_16x16x32_bf16 v[212:215], a[40:43], v[124:127], v[212:215]\n"
      "buffer_load_dwordx2 v[56:57], v5, s[32:35], s47 offen sc1\n"
      "v_add_u32 v46, 98304, v46\n"
      "v_mfma_f32_16x16x32_bf16 v[220:223], a[32:35], v[108:111], v[220:223]\n"
      "v_add_u32_dpp v54, v54, v54 row_half_mirror row_mask:0xf bank_mask:0xf\n"
      "v_cmp_ne_u32 vcc, s71, v54\n"
      "v_mfma_f32_16x16x32_bf16 v[220:223], a[44:47], v[124:127], v[220:223]\n"
      "s_waitcnt lgkmcnt(0)\n"
      "v_mfma_f32_16x16x32_bf16 v[128:131], a[48:51], v[64:67], v[128:131]\n"
      "ds_read_b64_tr_b16 v[96:97], v6 offset:40960\n"
      "s_cbranch_vccnz L_sx3_%=\n"
      "L_bx3_%=:\n"
      "v_and_b32 v41, 0x78, v4\n"
      "v_mfma_f32_16x16x32_bf16 v[128:131], a[60:63], v[80:83], v[128:131]\n"
      "ds_read_b64_tr_b16 v[98:99], v6 offset:43008\n"
      "v_add_u32 v41, 162304, v41\n"
      "v_mfma_f32_16x16x32_bf16 v[144:147], a[52:55], v[64:67], v[144:147]\n"
      "ds_read_b64_tr_b16 v[100:101], v33 offset:40960\n"
      "s_waitcnt vmcnt(5)\n"
      "v_mfma_f32_16x16x32_bf16 v[144:147], a[64:67], v[80:83], v[144:147]\n"
      "ds_read_b128 v[248:251], v50 offset:0\n"
      "s_add_u32 m0, s73, 16384\n"
      "v_add_u32 v43, 65536, v40\n"
      "v_mfma_f32_16x16x32_bf16 v[160:163], a[56:59], v[64:67], v[160:163]\n"
      "ds_read_b128 v[252:255], v50 offset:1024\n"
      "v_mfma_f32_16x16x32_bf16 v[160:163], a[68:71], v[80:83], v[160:163]\n"
      "global_load_dwordx4 v[224:227], v53, s[26:27] sc1\n"
      "ds_read_b64_tr_b16 v[102:103], v33 offset:43008\n"
      "v_mfma_f32_16x16x32_bf16 v[132:135], a[48:51], v[68:71], v[132:135]\n"
      "ds_read_b128 v[36:39], v50 offset:2048\n"
      "ds_read_b64_tr_b16 v[104:105], v34 offset:40960\n"
      "v_add_u32 v53, 0x6000, v53\n"
      "v_mfma_f32_16x16x32_bf16 v[132:135], a[60:63], v[84:87], v[132:135]\n"
      "ds_read_b64_tr_b16 v[106:107], v34 offset:43008\n"
      "v_mfma_f32_16x16x32_bf16 v[148:151], a[52:55], v[68:71], v[148:151]\n"
      "global_load_dwordx4 v[228:231], v53, s[26:27] sc1\n"
      "ds_read_b64_tr_b16 v[108:109], v35 offset:40960\n"
      "v_mfma_f32_16x16x32_bf16 v[148:151], a[64:67], v[84:87], v[148:151]\n"
      "ds_read_b64_tr_b16 v[110:111], v35 offset:43008\n"
      "v_add_u32 v53, 0x6000, v53\n"
      "v_mfma_f32_16x16x32_bf16 v[164:167], a[56:59], v[68:71], v[164:167]\n"
      "ds_read_b64_tr_b16 v[112:113], v6 offset:45056\n"
      "s_waitcnt lgkmcnt(5)\n"
      "v_add3_u32 v32, v32, v248, v249\n"
      "v_add3_u32 v32, v32, v250, v251\n"
      "v_mfma_f32_16x16x32_bf16 v[164:167], a[68:71], v[84:87], v[164:167]\n"
      "global_load_dwordx4 v[232:235], v53, s[26:27] sc1\n"
      "v_add3_u32 v32, v32, v252, v253\n"
      "v_mfma_f32_16x16x32_bf16 v[136:139], a[48:51], v[72:75], v[136:139]\n"
      "ds_read_b64_tr_b16 v[114:115], v6 offset:47104\n"
      "v_add_u32 v53, 0x6000, v53\n"
      "v_add3_u32 v32, v32, v254, v255\n"
      "v_mfma_f32_16x16x32_bf16 v[136:139], a[60:63], v[88:91], v[136:139]\n"
      "ds_read_b64_tr_b16 v[116:117], v33 offset:45056\n"
      "v_add3_u32 v32, v32, v36, v37\n"
      "v_mfma_f32_16x16x32_bf16 v[152:155], a[52:55], v[72:75], v[152:155]\n"
      "global_load_dwordx4 v[236:239], v53, s[26:27] sc1\n"
      "ds_read_b64_tr_b16 v[118:119], v33 offset:47104\n"
      "v_add3_u32 v32, v32, v38, v39\n"
      "v_mfma_f32_16x16x32_bf16 v[152:155], a[64:67], v[88:91], v[152:155]\n"
      "ds_read_b64_tr_b16 v[120:121], v34 offset:45056\n"
      "v_add_u32 v53, 0x6000, v53\n"
      "v_mfma_f32_16x16x32_bf16 v[168:171], a[56:59], v[72:75], v[168:171]\n"
      "ds_read_b64_tr_b16 v[122:123], v34 offset:47104\n"
      "v_add_u32_dpp v32, v32, v32 quad_perm:[1,0,3,2] row_mask:0xf bank_mask:0xf\n"
      "v_mfma_f32_16x16x32_bf16 v[168:171], a[68:71], v[88:91], v[168:171]\n"
      "global_load_dwordx4 v[240:243], v53, s[26:27] sc1\n"
      "ds_read_b64_tr_b16 v[124:125], v35 offset:45056\n"
      "v_mfma_f32_16x16x32_bf16 v[140:143], a[48:51], v[76:79], v[140:143]\n"
      "ds_read_b64_tr_b16 v[126:127], v35 offset:47104\n"
      "v_add_u32 v53, 0x6000, v53\n"
      "v_add_u32_dpp v32, v32, v32 quad_perm:[2,3,0,1] row_mask:0xf bank_mask:0xf\n"
      "v_mfma_f32_16x16x32_bf16 v[140:143], a[60:63], v[92:95], v[140:143]\n"
      "v_mfma_f32_16x16x32_bf16 v[156:159], a[52:55], v[76:79], v[156:159]\n"
      "global_load_dwordx4 v[244:247], v53, s[26:27] sc1\n"
      "v_add_u32_dpp v32, v32, v32 row_half_mirror row_mask:0xf bank_mask:0xf\n"
      "v_cmp_ne_u32 vcc, s72, v32\n"
      "v_mfma_f32_16x16x32_bf16 v[156:159], a[64:67], v[92:95], v[156:159]\n"
      "v_mfma_f32_16x16x32_bf16 v[172:175], a[56:59], v[76:79], v[172:175]\n"
      "s_cbranch_vccnz L_sxp_%=\n"
      "L_bxp_%=:\n"
      "v_mfma_f32_16x16x32_bf16 v[172:175], a[68:71], v[92:95], v[172:175]\n"
      "global_load_lds_dwordx4 v43, s[4:5] offset:0 sc1\n"
      "s_waitcnt lgkmcnt(0)\n"
      "s_barrier\n"
      "v_mfma_f32_16x16x32_bf16 v[128:131], a[72:75], v[96:99], v[128:131]\n"
      "ds_read_b64_tr_b16 v[8:9], v27 offset:0\n"
      "v_mfma_f32_16x16x32_bf16 v[128:131], a[84:87], v[112:115], v[128:131]\n"
      "ds_read_b64_tr_b16 v[10:11], v27 offset:2048\n"
      "v_mfma_f32_16x16x32_bf16 v[144:147], a[76:79], v[96:99], v[144:147]\n"
      "ds_read_b64_tr_b16 v[12:13], v27 offset:4096\n"
      "ds_read_b64_tr_b16 v[64:65], v6 offset:49152\n"
      "v_mfma_f32_16x16x32_bf16 v[144:147], a[88:91], v[112:115], v[144:147]\n"
      "global_load_lds_dwordx4 v43, s[4:5] offset:1024 sc1\n"
      "ds_read_b64_tr_b16 v[14:15], v27 offset:6144\n"
      "ds_read_b64_tr_b16 v[66:67], v6 offset:51200\n"
      "v_mfma_f32_16x16x32_bf16 v[160:163], a[80:83], v[96:99], v[160:163]\n"
      "ds_read_b64_tr_b16 v[16:17], v27 offset:8192\n"
      "v_mov_b32 v31, 0x3f803f80\n"
      "v_mfma_f32_16x16x32_bf16 v[160:163], a[92:95], v[112:115], v[160:163]\n"
      "ds_read_b64_tr_b16 v[68:69], v33 offset:49152\n"
      "v_and_b32 v50, 0xf0, v7\n"
      "v_mfma_f32_16x16x32_bf16 v[132:135], a[72:75], v[100:103], v[132:135]\n"
      "ds_read_b64_tr_b16 v[70:71], v33 offset:51200\n"
      "s_mov_b32 s42, 0x00f0000f\n"
      "v_mfma_f32_16x16x32_bf16 v[132:135], a[84:87], v[116:119], v[132:135]\n"
      "global_load_lds_dwordx4 v43, s[4:5] offset:2048 sc1\n"
      "s_mov_b32 s43, 0xf0000f00\n"
      "v_mfma_f32_16x16x32_bf16 v[148:151], a[76:79], v[100:103], v[148:151]\n"
      "ds_read_b64_tr_b16 v[72:73], v34 offset:49152\n"
      "v_bfrev_b32 v32, 1\n"
      "v_mfma_f32_16x16x32_bf16 v[148:151], a[88:91], v[116:119], v[148:151]\n"
      "ds_read_b64_tr_b16 v[74:75], v34 offset:51200\n"
      "v_cndmask_b32 v50, v32, v50, s[42:43]\n"
      "v_mfma_f32_16x16x32_bf16 v[164:167], a[80:83], v[100:103], v[164:167]\n"
      "ds_read_b64_tr_b16 v[76:77], v35 offset:49152\n"
      "v_mfma_f32_16x16x32_bf16 v[164:167], a[92:95], v[116:119], v[164:167]\n"
      "global_load_lds_dwordx4 v43, s[4:5] offset:3072 sc1\n"
      "s_waitcnt lgkmcnt(5)\n"
      "v_mfma_f32_16x16x32_bf16 v[20:23], v[28:31], v[8:11], 0\n"
      "ds_read_b64_tr_b16 v[78:79], v35 offset:51200\n"
      "s_mov_b32 s47, 131072\n"
      "v_mfma_f32_16x16x32_bf16 v[24:27], v[8:11], v[8:11], 0\n"
      "ds_read_b64_tr_b16 v[80:81], v6 offset:53248\n"
      "v_mfma_f32_16x16x32_bf16 v[20:23], v[28:31], v[12:15], v[20:23]\n"
      "ds_read_b64_tr_b16 v[82:83], v6 offset:55296\n"
      "v_mfma_f32_16x16x32_bf16 v[24:27], v[12:15], v[12:15], v[24:27]\n"
      "buffer_load_dwordx2 v[2:3], v5, s[32:35], s47 offen sc1\n"
      "v_mfma_f32_16x16x32_bf16 v[20:23], v[28:31], v[16:19], v[20:23]\n"
      "ds_read_b64_tr_b16 v[84:85], v33 offset:53248\n"
      "v_mfma_f32_16x16x32_bf16 v[24:27], v[16:19], v[16:19], v[24:27]\n"
      "ds_read_b64_tr_b16 v[86:87], v33 offset:55296\n"
      "v_mfma_f32_16x16x32_bf16 v[136:139], a[72:75], v[104:107], v[136:139]\n"
      "ds_read_b64_tr_b16 v[88:89], v34 offset:53248\n"
      "v_mfma_f32_16x16x32_bf16 v[136:139], a[84:87], v[120:123], v[136:139]\n"
      "ds_read_b64_tr_b16 v[90:91], v34 offset:55296\n"
      "v_mfma_f32_16x16x32_bf16 v[152:155], a[76:79], v[104:107], v[152:155]\n"
      "ds_read_b64_tr_b16 v[92:93], v35 offset:53248\n"
      "v_cndmask_b32 v24, v24, v25, s[48:49]\n"
      "v_cndmask_b32 v26, v26, v27, s[48:49]\n"
      "v_mfma_f32_16x16x32_bf16 v[152:155], a[88:91], v[120:123], v[152:155]\n"
      "ds_read_b64_tr_b16 v[94:95], v35 offset:55296\n"
      "s_add_u32 m0, s73, 32768\n"
      "v_add_u32 v43, 98304, v40\n"
      "v_mov_b32 v22, s30\n"
      "v_cndmask_b32 v21, v24, v26, s[50:51]\n"
      "v_mfma_f32_16x16x32_bf16 v[168:171], a[80:83], v[104:107], v[168:171]\n"
      "v_mov_b32 v23, s31\n"
      "v_mfma_f32_16x16x32_bf16 v[168:171], a[92:95], v[120:123], v[168:171]\n"
      "buffer_store_dwordx4 v[20:23], v50, s[32:35], s92 offen sc1\n"
      "v_and_b32 v32, 0x200, v7\n"
      "v_mfma_f32_16x16x32_bf16 v[140:143], a[72:75], v[108:111], v[140:143]\n"
      "v_and_b32 v50, 0x1f0, v7\n"
      "v_lshl_add_u32 v50, v32, 3, v50\n"
      "v_mfma_f32_16x16x32_bf16 v[140:143], a[84:87], v[124:127], v[140:143]\n"
      "global_load_dwordx4 a[188:191], v45, s[8:9] offset:3072 sc1\n"
      "v_and_b32 v32, 0xf8, v4\n"
      "v_mfma_f32_16x16x32_bf16 v[156:159], a[76:79], v[108:111], v[156:159]\n"
      "v_add_u32 v32, s95, v32\n"
      "v_mfma_f32_16x16x32_bf16 v[156:159], a[88:91], v[124:127], v[156:159]\n"
      "v_add_u32 v45, 53248, v7\n"
      "v_mfma_f32_16x16x32_bf16 v[172:175], a[80:83], v[108:111], v[172:175]\n"
      "v_mfma_f32_16x16x32_bf16 v[172:175], a[92:95], v[124:127], v[172:175]\n"
      "global_load_dwordx4 a[192:195], v45, s[8:9] offset:-4096 sc1\n"
      "s_waitcnt lgkmcnt(0)\n"
      "v_mfma_f32_16x16x32_bf16 v[128:131], a[96:99], v[64:67], v[128:131]\n"
      "ds_read_b64_tr_b16 v[96:97], v6 offset:57344\n"
      "v_mfma_f32_16x16x32_bf16 v[128:131], a[108:111], v[80:83], v[128:131]\n"
      "ds_read_b64_tr_b16 v[98:99], v6 offset:59392\n"
      "v_mfma_f32_16x16x32_bf16 v[144:147], a[100:103], v[64:67], v[144:147]\n"
      "ds_read_b64_tr_b16 v[100:101], v33 offset:57344\n"
      "s_waitcnt vmcnt(14)\n"
      "v_mfma_f32_16x16x32_bf16 v[144:147], a[112:115], v[80:83], v[144:147]\n"
      "global_load_dwordx4 a[196:199], v45, s[8:9] offset:-3072 sc1\n"
      "ds_read_b128 v[248:251], v40 offset:0\n"
      "v_mfma_f32_16x16x32_bf16 v[160:163], a[104:107], v[64:67], v[160:163]\n"
      "ds_read_b128 v[252:255], v40 offset:1024\n"
      "v_mfma_f32_16x16x32_bf16 v[160:163], a[116:119], v[80:83], v[160:163]\n"
      "ds_read_b128 v[36:39], v40 offset:2048\n"
      "ds_read_b64_tr_b16 v[102:103], v33 offset:59392\n"
      "v_mfma_f32_16x16x32_bf16 v[132:135], a[96:99], v[68:71], v[132:135]\n"
      "ds_read_b128 v[60:63], v40 offset:3072\n"
      "ds_read_b64_tr_b16 v[104:105], v34 offset:57344\n"
      "v_mfma_f32_16x16x32_bf16 v[132:135], a[108:111], v[84:87], v[132:135]\n"
      "global_load_dwordx4 a[200:203], v45, s[8:9] offset:-2048 sc1\n"
      "ds_read_b64_tr_b16 v[106:107], v34 offset:59392\n"
      "v_mfma_f32_16x16x32_bf16 v[148:151], a[100:103], v[68:71], v[148:151]\n"
      "ds_read_b64_tr_b16 v[108:109], v35 offset:57344\n"
      "v_mfma_f32_16x16x32_bf16 v[148:151], a[112:115], v[84:87], v[148:151]\n"
      "ds_read_b64_tr_b16 v[110:111], v35 offset:59392\n"
      "v_mfma_f32_16x16x32_bf16 v[164:167], a[104:107], v[68:71], v[164:167]\n"
      "ds_read_b64_tr_b16 v[112:113], v6 offset:61440\n"
      "s_waitcnt lgkmcnt(5)\n"
      "v_add3_u32 v56, v56, v248, v249\n"
      "v_add3_u32 v56, v56, v250, v251\n"
      "v_mfma_f32_16x16x32_bf16 v[164:167], a[116:119], v[84:87], v[164:167]\n"
      "global_load_dwordx4 a[204:207], v45, s[8:9] offset:-1024 sc1\n"
      "v_add3_u32 v56, v56, v252, v253\n"
      "v_add3_u32 v56, v56, v254, v255\n"
      "v_mfma_f32_16x16x32_bf16 v[136:139], a[96:99], v[72:75], v[136:139]\n"
      "ds_read_b64_tr_b16 v[114:115], v6 offset:63488\n"
      "v_add3_u32 v56, v56, v36, v37\n"
      "v_add3_u32 v56, v56, v38, v39\n"
      "v_mfma_f32_16x16x32_bf16 v[136:139], a[108:111], v[88:91], v[136:139]\n"
      "ds_read_b64_tr_b16 v[116:117], v33 offset:61440\n"
      "v_add3_u32 v56, v56, v60, v61\n"
      "v_mfma_f32_16x16x32_bf16 v[152:155], a[100:103], v[72:75], v[152:155]\n"
      "ds_read_b64_tr_b16 v[118:119], v33 offset:63488\n"
      "v_add3_u32 v56, v56, v62, v63\n"
      "v_mfma_f32_16x16x32_bf16 v[152:155], a[112:115], v[88:91], v[152:155]\n"
      "global_load_dwordx4 a[208:211], v45, s[8:9] offset:0 sc1\n"
      "ds_read_b64_tr_b16 v[120:121], v34 offset:61440\n"
      "v_mfma_f32_16x16x32_bf16 v[168:171], a[104:107], v[72:75], v[168:171]\n"
      "ds_read_b64_tr_b16 v[122:123], v34 offset:63488\n"
      "v_add_u32_dpp v56, v56, v56 quad_perm:[1,0,3,2] row_mask:0xf bank_mask:0xf\n"
      "v_mfma_f32_16x16x32_bf16 v[168:171], a[116:119], v[88:91], v[168:171]\n"
      "ds_read_b64_tr_b16 v[124:125], v35 offset:61440\n"
      "s_waitcnt vmcnt(12)\n"
      "v_mfma_f32_16x16x32_bf16 v[140:143], a[96:99], v[76:79], v[140:143]\n"
      "ds_read_b64_tr_b16 v[126:127], v35 offset:63488\n"
      "v_add_u32_dpp v56, v56, v56 quad_perm:[2,3,0,1] row_mask:0xf bank_mask:0xf\n"
      "v_mfma_f32_16x16x32_bf16 v[140:143], a[108:111], v[92:95], v[140:143]\n"
      "global_load_dwordx4 a[212:215], v45, s[8:9] offset:1024 sc1\n"
      "v_add_f32 v224, v224, v228\n"
      "v_mfma_f32_16x16x32_bf16 v[156:159], a[100:103], v[76:79], v[156:159]\n"
      "v_add_u32_dpp v56, v56, v56 row_half_mirror row_mask:0xf bank_mask:0xf\n"
      "v_cmp_ne_u32 vcc, s71, v56\n"
      "v_add_f32 v225, v225, v229\n"
      "v_mfma_f32_16x16x32_bf16 v[156:159], a[112:115], v[92:95], v[156:159]\n"
      "v_add_f32 v226, v226, v230\n"
      "v_add_f32 v227, v227, v231\n"
      "v_mfma_f32_16x16x32_bf16 v[172:175], a[104:107], v[76:79], v[172:175]\n"
      "s_cbranch_vccnz L_sx4_%=\n"
      "L_bx4_%=:\n"
      "v_add_f32 v232, v232, v236\n"
      "v_mfma_f32_16x16x32_bf16 v[172:175], a[116:119], v[92:95], v[172:175]\n"
      "global_load_lds_dwordx4 v43, s[4:5] offset:0 sc1\n"
      "s_waitcnt lgkmcnt(0)\n"
      "s_barrier\n"
      "v_mfma_f32_16x16x32_bf16 v[128:131], a[120:123], v[96:99], v[128:131]\n"
      "ds_read_b64_tr_b16 v[64:65], v6 offset:0\n"
      "v_add_f32 v233, v233, v237\n"
      "v_mfma_f32_16x16x32_bf16 v[128:131], a[132:135], v[112:115], v[128:131]\n"
      "ds_read_b64_tr_b16 v[66:67], v6 offset:2048\n"
      "v_add_f32 v234, v234, v238\n"
      "v_mfma_f32_16x16x32_bf16 v[144:147], a[124:127], v[96:99], v[144:147]\n"
      "ds_read_b64_tr_b16 v[68:69], v33 offset:0\n"
      "v_add_f32 v235, v235, v239\n"
      "v_mfma_f32_16x16x32_bf16 v[144:147], a[136:139], v[112:115], v[144:147]\n"
      "global_load_dwordx4 a[216:219], v45, s[8:9] offset:2048 sc1\n"
      "v_add_f32 v240, v240, v244\n"
      "v_mfma_f32_16x16x32_bf16 v[160:163], a[128:131], v[96:99], v[160:163]\n"
      "ds_read_b64_tr_b16 v[70:71], v33 offset:2048\n"
      "v_add_f32 v241, v241, v245\n"
      "v_mfma_f32_16x16x32_bf16 v[160:163], a[140:143], v[112:115], v[160:163]\n"
      "ds_read_b64_tr_b16 v[72:73], v34 offset:0\n"
      "v_add_f32 v242, v242, v246\n"
      "v_mfma_f32_16x16x32_bf16 v[132:135], a[120:123], v[100:103], v[132:135]\n"
      "ds_read_b64_tr_b16 v[74:75], v34 offset:2048\n"
      "v_add_f32 v243, v243, v247\n"
      "v_mfma_f32_16x16x32_bf16 v[132:135], a[132:135], v[116:119], v[132:135]\n"
      "global_load_lds_dwordx4 v43, s[4:5] offset:1024 sc1\n"
      "v_add_f32 v224, v224, v232\n"
      "v_mfma_f32_16x16x32_bf16 v[148:151], a[124:127], v[100:103], v[148:151]\n"
      "ds_read_b64_tr_b16 v[76:77], v35 offset:0\n"
      "v_add_f32 v225, v225, v233\n"
      "v_mfma_f32_16x16x32_bf16 v[148:151], a[136:139], v[116:119], v[148:151]\n"
      "ds_read_b64_tr_b16 v[78:79], v35 offset:2048\n"
      "v_add_f32 v226, v226, v234\n"
      "v_mfma_f32_16x16x32_bf16 v[164:167], a[128:131], v[100:103], v[164:167]\n"
      "ds_read_b64_tr_b16 v[80:81], v6 offset:4096\n"
      "v_add_f32 v227, v227, v235\n"
      "v_mfma_f32_16x16x32_bf16 v[164:167], a[140:143], v[116:119], v[164:167]\n"
      "global_load_dwordx4 a[220:223], v45, s[8:9] offset:3072 sc1\n"
      "v_add_f32 v224, v224, v240\n"
      "v_mfma_f32_16x16x32_bf16 v[136:139], a[120:123], v[104:107], v[136:139]\n"
      "ds_read_b64_tr_b16 v[82:83], v6 offset:6144\n"
      "v_add_f32 v225, v225, v241\n"
      "v_mfma_f32_16x16x32_bf16 v[136:139], a[132:135], v[120:123], v[136:139]\n"
      "ds_read_b64_tr_b16 v[84:85], v33 offset:4096\n"
      "v_add_u32 v45, 61440, v7\n"
      "v_mfma_f32_16x16x32_bf16 v[152:155], a[124:127], v[104:107], v[152:155]\n"
      "ds_read_b64_tr_b16 v[86:87], v33 offset:6144\n"
      "v_add_f32 v226, v226, v242\n"
      "v_mfma_f32_16x16x32_bf16 v[152:155], a[136:139], v[120:123], v[152:155]\n"
      "global_load_lds_dwordx4 v43, s[4:5] offset:2048 sc1\n"
      "ds_read_b64_tr_b16 v[88:89], v34 offset:4096\n"
      "v_add_f32 v227, v227, v243\n"
      "v_mfma_f32_16x16x32_bf16 v[168:171], a[128:131], v[104:107], v[168:171]\n"
      "ds_read_b64_tr_b16 v[90:91], v34 offset:6144\n"
      "v_add_u32 v53, 0x6000, v53\n"
      "v_mfma_f32_16x16x32_bf16 v[168:171], a[140:143], v[120:123], v[168:171]\n"
      "global_load_dwordx4 v[228:231], v53, s[26:27] sc1\n"
      "ds_read_b64_tr_b16 v[92:93], v35 offset:4096\n"
      "v_add_u32 v53, 0x6000, v53\n"
      "v_mfma_f32_16x16x32_bf16 v[140:143], a[120:123], v[108:111], v[140:143]\n"
      "global_load_dwordx4 v[232:235], v53, s[26:27] sc1\n"
      "v_add_u32 v53, 0x6000, v53\n"
      "v_mfma_f32_16x16x32_bf16 v[140:143], a[132:135], v[124:127], v[140:143]\n"
      "global_load_dwordx4 v[236:239], v53, s[26:27] sc1\n"
      "ds_read_b64_tr_b16 v[94:95], v35 offset:6144\n"
      "v_add_u32 v53, 0x6000, v53\n"
      "v_mfma_f32_16x16x32_bf16 v[156:159], a[124:127], v[108:111], v[156:159]\n"
      "global_load_dwordx4 v[240:243], v53, s[26:27] sc1\n"
      "L_pg4_%=:\n"
      "s_cbranch_execz L_pgr_%=\n"
      "v_and_b32 v53, 0x300, v7\n"
      "v_mfma_f32_16x16x32_bf16 v[156:159], a[136:139], v[124:127], v[156:159]\n"
      "v_and_b32 v59, 0x30, v7\n"
      "v_lshl_add_u32 v53, v59, 2, v53\n"
      "v_mfma_f32_16x16x32_bf16 v[172:175], a[128:131], v[108:111], v[172:175]\n"
      "v_bfe_u32 v59, v7, 6, 2\n"
      "v_lshl_add_u32 v53, v59, 4, v53\n"
      "v_lshrrev_b32 v59, 6, v7\n"
      "v_mfma_f32_16x16x32_bf16 v[172:175], a[140:143], v[124:127], v[172:175]\n"
      "s_waitcnt lgkmcnt(0)\n"
      "v_mfma_f32_16x16x32_bf16 v[128:131], a[144:147], v[64:67], v[128:131]\n"
      "global_load_dwordx4 a[224:227], v45, s[8:9] offset:-4096 sc1\n"
      "buffer_load_dwordx4 v[244:247], v53, s[32:35], s66 offen sc1\n"
      "v_mfma_f32_16x16x32_bf16 v[128:131], a[156:159], v[80:83], v[128:131]\n"
      "ds_read_b64_tr_b16 v[96:97], v6 offset:8192\n"
      "v_mfma_f32_16x16x32_bf16 v[144:147], a[148:151], v[64:67], v[144:147]\n"
      "ds_read_b64_tr_b16 v[98:99], v6 offset:10240\n"
      "s_waitcnt vmcnt(19)\n"
      "v_mfma_f32_16x16x32_bf16 v[144:147], a[160:163], v[80:83], v[144:147]\n"
      "global_load_lds_dwordx4 v43, s[4:5] offset:3072 sc1\n"
      "ds_read_b128 v[248:251], v40 offset:16384\n"
      "v_mfma_f32_16x16x32_bf16 v[160:163], a[152:155], v[64:67], v[160:163]\n"
      "global_load_dwordx4 a[228:231], v45, s[8:9] offset:-3072 sc1\n"
      "ds_read_b128 v[252:255], v40 offset:17408\n"
      "ds_read_b64_tr_b16 v[100:101], v33 offset:8192\n"
      "v_mfma_f32_16x16x32_bf16 v[160:163], a[164:167], v[80:83], v[160:163]\n"
      "ds_read_b128 v[36:39], v40 offset:18432\n"
      "ds_read_b64_tr_b16 v[102:103], v33 offset:10240\n"
      "v_mfma_f32_16x16x32_bf16 v[132:135], a[144:147], v[68:71], v[132:135]\n"
      "ds_read_b128 v[60:63], v40 offset:19456\n"
      "ds_read_b64_tr_b16 v[104:105], v34 offset:8192\n"
      "v_mfma_f32_16x16x32_bf16 v[132:135], a[156:159], v[84:87], v[132:135]\n"
      "ds_read_b64_tr_b16 v[106:107], v34 offset:10240\n"
      "v_mfma_f32_16x16x32_bf16 v[148:151], a[148:151], v[68:71], v[148:151]\n"
      "global_load_dwordx4 a[232:235], v45, s[8:9] offset:-2048 sc1\n"
      "ds_read_b64_tr_b16 v[108:109], v35 offset:8192\n"
      "v_mfma_f32_16x16x32_bf16 v[148:151], a[160:163], v[84:87], v[148:151]\n"
      "ds_read_b64_tr_b16 v[110:111], v35 offset:10240\n"
      "v_mfma_f32_16x16x32_bf16 v[164:167], a[152:155], v[68:71], v[164:167]\n"
      "ds_read_b64_tr_b16 v[112:113], v6 offset:12288\n"
      "s_waitcnt lgkmcnt(5)\n"
      "v_add3_u32 v2, v2, v248, v249\n"
      "v_add3_u32 v2, v2, v250, v251\n"
      "v_mfma_f32_16x16x32_bf16 v[164:167], a[164:167], v[84:87], v[164:167]\n"
      "ds_read_b64_tr_b16 v[114:115], v6 offset:14336\n"
      "v_add3_u32 v2, v2, v252, v253\n"
      "v_add3_u32 v2, v2, v254, v255\n"
      "v_mfma_f32_16x16x32_bf16 v[136:139], a[144:147], v[72:75], v[136:139]\n"
      "global_load_dwordx4 a[236:239], v45, s[8:9] offset:-1024 sc1\n"
      "v_add3_u32 v2, v2, v36, v37\n"
      "v_add3_u32 v2, v2, v38, v39\n"
      "v_mfma_f32_16x16x32_bf16 v[136:139], a[156:159], v[88:91], v[136:139]\n"
      "ds_read_b64_tr_b16 v[116:117], v33 offset:12288\n"
      "v_add3_u32 v2, v2, v60, v61\n"
      "v_mfma_f32_16x16x32_bf16 v[152:155], a[148:151], v[72:75], v[152:155]\n"
      "ds_read_b64_tr_b16 v[118:119], v33 offset:14336\n"
      "v_add3_u32 v2, v2, v62, v63\n"
      "v_mfma_f32_16x16x32_bf16 v[152:155], a[160:163], v[88:91], v[152:155]\n"
      "ds_read_b64_tr_b16 v[120:121], v34 offset:12288\n"
      "v_mfma_f32_16x16x32_bf16 v[168:171], a[152:155], v[72:75], v[168:171]\n"
      "buffer_load_dwordx4 v[8:11], v50, s[32:35], s93 offen offset:0 sc1\n"
      "ds_read_b64_tr_b16 v[122:123], v34 offset:14336\n"
      "v_add_u32_dpp v2, v2, v2 quad_perm:[1,0,3,2] row_mask:0xf bank_mask:0xf\n"
      "v_mfma_f32_16x16x32_bf16 v[168:171], a[164:167], v[88:91], v[168:171]\n"
      "ds_read_b64_tr_b16 v[124:125], v35 offset:12288\n"
      "v_mfma_f32_16x16x32_bf16 v[140:143], a[144:147], v[76:79], v[140:143]\n"
      "ds_read_b64_tr_b16 v[126:127], v35 offset:14336\n"
      "v_add_u32_dpp v2, v2, v2 quad_perm:[2,3,0,1] row_mask:0xf bank_mask:0xf\n"
      "v_mfma_f32_16x16x32_bf16 v[140:143], a[156:159], v[92:95], v[140:143]\n"
      "v_mfma_f32_16x16x32_bf16 v[156:159], a[148:151], v[76:79], v[156:159]\n"
      "buffer_load_dwordx4 v[12:15], v50, s[32:35], s93 offen offset:1024 sc1\n"
      "v_add_u32_dpp v2, v2, v2 row_half_mirror row_mask:0xf bank_mask:0xf\n"
      "v_cmp_ne_u32 vcc, s71, v2\n"
      "v_mfma_f32_16x16x32_bf16 v[156:159], a[160:163], v[92:95], v[156:159]\n"
      "v_mfma_f32_16x16x32_bf16 v[172:175], a[152:155], v[76:79], v[172:175]\n"
      "s_cbranch_vccnz L_sx5_%=\n"
      "L_bx5_%=:\n"
      "v_mfma_f32_16x16x32_bf16 v[172:175], a[164:167], v[92:95], v[172:175]\n"
      "s_waitcnt vmcnt(23)\n"
      "s_waitcnt lgkmcnt(0)\n"
      "s_barrier\n"
      "v_mfma_f32_16x16x32_bf16 v[128:131], a[168:171], v[96:99], v[128:131]\n"
      "buffer_load_dwordx4 v[16:19], v50, s[32:35], s93 offen offset:2048 sc1\n"
      "v_mfma_f32_16x16x32_bf16 v[128:131], a[180:183], v[112:115], v[128:131]\n"
      "ds_read_b64_tr_b16 v[64:65], v6 offset:16384\n"
      "v_mfma_f32_16x16x32_bf16 v[144:147], a[172:175], v[96:99], v[144:147]\n"
      "ds_read_b64_tr_b16 v[66:67], v6 offset:18432\n"
      "v_mfma_f32_16x16x32_bf16 v[144:147], a[184:187], v[112:115], v[144:147]\n"
      "ds_read_b64_tr_b16 v[68:69], v33 offset:16384\n"
      "v_mfma_f32_16x16x32_bf16 v[160:163], a[176:179], v[96:99], v[160:163]\n"
      "buffer_load_dwordx4 v[20:23], v50, s[32:35], s93 offen offset:3072 sc1\n"
      "v_mfma_f32_16x16x32_bf16 v[160:163], a[188:191], v[112:115], v[160:163]\n"
      "ds_read_b64_tr_b16 v[70:71], v33 offset:18432\n"
      "v_mfma_f32_16x16x32_bf16 v[132:135], a[168:171], v[100:103], v[132:135]\n"
      "ds_read_b64_tr_b16 v[72:73], v34 offset:16384\n"
      "v_mfma_f32_16x16x32_bf16 v[132:135], a[180:183], v[116:119], v[132:135]\n"
      "ds_read_b64_tr_b16 v[74:75], v34 offset:18432\n"
      "v_mfma_f32_16x16x32_bf16 v[148:151], a[172:175], v[100:103], v[148:151]\n"
      "ds_read_b64_tr_b16 v[76:77], v35 offset:16384\n"
      "v_mfma_f32_16x16x32_bf16 v[148:151], a[184:187], v[116:119], v[148:151]\n"
      "ds_read_b64_tr_b16 v[78:79], v35 offset:18432\n"
      "v_mfma_f32_16x16x32_bf16 v[164:167], a[176:179], v[100:103], v[164:167]\n"
      "ds_read_b64_tr_b16 v[80:81], v6 offset:20480\n"
      "s_add_u32 m0, s73, 49152\n"
      "v_add_u32 v43, 114688, v40\n"
      "v_mfma_f32_16x16x32_bf16 v[164:167], a[188:191], v[116:119], v[164:167]\n"
      "ds_read_b64_tr_b16 v[82:83], v6 offset:22528\n"
      "v_mfma_f32_16x16x32_bf16 v[136:139], a[168:171], v[104:107], v[136:139]\n"
      "global_load_lds_dwordx4 v43, s[4:5] offset:0 sc1\n"
      "s_waitcnt vmcnt(9)\n"
      "v_mfma_f32_16x16x32_bf16 v[136:139], a[180:183], v[120:123], v[136:139]\n"
      "ds_read_b64_tr_b16 v[84:85], v33 offset:20480\n"
      "v_add_f32 v228, v228, v232\n"
      "v_mfma_f32_16x16x32_bf16 v[152:155], a[172:175], v[104:107], v[152:155]\n"
      "ds_read_b64_tr_b16 v[86:87], v33 offset:22528\n"
      "v_add_f32 v229, v229, v233\n"
      "v_mfma_f32_16x16x32_bf16 v[152:155], a[184:187], v[120:123], v[152:155]\n"
      "ds_read_b64_tr_b16 v[88:89], v34 offset:20480\n"
      "v_add_f32 v230, v230, v234\n"
      "v_mfma_f32_16x16x32_bf16 v[168:171], a[176:179], v[104:107], v[168:171]\n"
      "global_load_lds_dwordx4 v43, s[4:5] offset:1024 sc1\n"
      "ds_read_b64_tr_b16 v[90:91], v34 offset:22528\n"
      "v_add_f32 v231, v231, v235\n"
      "v_mfma_f32_16x16x32_bf16 v[168:171], a[188:191], v[120:123], v[168:171]\n"
      "ds_read_b64_tr_b16 v[92:93], v35 offset:20480\n"
      "v_add_f32 v236, v236, v240\n"
      "v_mfma_f32_16x16x32_bf16 v[140:143], a[168:171], v[108:111], v[140:143]\n"
      "ds_read_b64_tr_b16 v[94:95], v35 offset:22528\n"
      "v_add_f32 v237, v237, v241\n"
      "v_mfma_f32_16x16x32_bf16 v[140:143], a[180:183], v[124:127], v[140:143]\n"
      "v_add_f32 v238, v238, v242\n"
      "v_add_f32 v239, v239, v243\n"
      "v_mfma_f32_16x16x32_bf16 v[156:159], a[172:175], v[108:111], v[156:159]\n"
      "global_load_lds_dwordx4 v43, s[4:5] offset:2048 sc1\n"
      "v_add_f32 v224, v224, v228\n"
      "v_mfma_f32_16x16x32_bf16 v[156:159], a[184:187], v[124:127], v[156:159]\n"
      "v_add_f32 v225, v225, v229\n"
      "v_add_f32 v226, v226, v230\n"
      "v_mfma_f32_16x16x32_bf16 v[172:175], a[176:179], v[108:111], v[172:175]\n"
      "v_add_f32 v227, v227, v231\n"
      "v_add_f32 v224, v224, v236\n"
      "v_mfma_f32_16x16x32_bf16 v[172:175], a[188:191], v[124:127], v[172:175]\n"
      "s_waitcnt lgkmcnt(0)\n"
      "v_mfma_f32_16x16x32_bf16 v[128:131], a[192:195], v[64:67], v[128:131]\n"
      "global_load_lds_dwordx4 v43, s[4:5] offset:3072 sc1\n"
      "v_add_f32 v225, v225, v237\n"
      "v_mfma_f32_16x16x32_bf16 v[128:131], a[204:207], v[80:83], v[128:131]\n"
      "ds_read_b64_tr_b16 v[96:97], v6 offset:24576\n"
      "v_add_f32 v226, v226, v238\n"
      "v_mfma_f32_16x16x32_bf16 v[144:147], a[196:199], v[64:67], v[144:147]\n"
      "ds_read_b64_tr_b16 v[98:99], v6 offset:26624\n"
      "s_add_u32 m0, s73, 0\n"
      "v_add_u32 v43, 131072, v40\n"
      "s_waitcnt vmcnt(11)\n"
      "v_mfma_f32_16x16x32_bf16 v[144:147], a[208:211], v[80:83], v[144:147]\n"
      "ds_read_b128 v[248:251], v40 offset:32768\n"
      "v_add_f32 v227, v227, v239\n"
      "v_mfma_f32_16x16x32_bf16 v[160:163], a[200:203], v[64:67], v[160:163]\n"
      "global_load_dwordx4 a[240:243], v44, s[6:7] offset:-4096 sc1\n"
      "ds_read_b128 v[252:255], v40 offset:33792\n"
      "ds_read_b64_tr_b16 v[100:101], v33 offset:24576\n"
      "v_mov_b32 v228, v224\n"
      "v_mfma_f32_16x16x32_bf16 v[160:163], a[212:215], v[80:83], v[160:163]\n"
      "ds_read_b128 v[36:39], v40 offset:34816\n"
      "ds_read_b64_tr_b16 v[102:103], v33 offset:26624\n"
      "v_mov_b32 v229, v225\n"
      "v_mfma_f32_16x16x32_bf16 v[132:135], a[192:195], v[68:71], v[132:135]\n"
      "ds_read_b128 v[60:63], v40 offset:35840\n"
      "ds_read_b64_tr_b16 v[104:105], v34 offset:24576\n"
      "v_mov_b32 v230, v226\n"
      "v_mfma_f32_16x16x32_bf16 v[132:135], a[204:207], v[84:87], v[132:135]\n"
      "ds_read_b64_tr_b16 v[106:107], v34 offset:26624\n"
      "v_mov_b32 v231, v227\n"
      "v_mfma_f32_16x16x32_bf16 v[148:151], a[196:199], v[68:71], v[148:151]\n"
      "global_load_dwordx4 a[244:247], v44, s[6:7] offset:-3072 sc1\n"
      "ds_read_b64_tr_b16 v[108:109], v35 offset:24576\n"
      "v_lshl_add_u32 v59, v59, 3, s55\n"
      "v_mfma_f32_16x16x32_bf16 v[148:151], a[208:211], v[84:87], v[148:151]\n"
      "ds_read_b64_tr_b16 v[110:111], v35 offset:26624\n"
      "v_permlane32_swap_b32 v224, v228\n"
      "v_mfma_f32_16x16x32_bf16 v[164:167], a[200:203], v[68:71], v[164:167]\n"
      "ds_read_b64_tr_b16 v[112:113], v6 offset:28672\n"
      "s_waitcnt lgkmcnt(5)\n"
      "v_add3_u32 v49, v49, v248, v249\n"
      "v_add3_u32 v49, v49, v250, v251\n"
      "v_mfma_f32_16x16x32_bf16 v[164:167], a[212:215], v[84:87], v[164:167]\n"
      "ds_read_b64_tr_b16 v[114:115], v6 offset:30720\n"
      "v_add3_u32 v49, v49, v252, v253\n"
      "v_add3_u32 v49, v49, v254, v255\n"
      "v_mfma_f32_16x16x32_bf16 v[136:139], a[192:195], v[72:75], v[136:139]\n"
      "global_load_dwordx4 a[248:251], v44, s[6:7] offset:-2048 sc1\n"
      "v_add3_u32 v49, v49, v36, v37\n"
      "v_add3_u32 v49, v49, v38, v39\n"
      "v_mfma_f32_16x16x32_bf16 v[136:139], a[204:207], v[88:91], v[136:139]\n"
      "ds_read_b64_tr_b16 v[116:117], v33 offset:28672\n"
      "v_add3_u32 v49, v49, v60, v61\n"
      "v_mfma_f32_16x16x32_bf16 v[152:155], a[196:199], v[72:75], v[152:155]\n"
      "ds_read_b64_tr_b16 v[118:119], v33 offset:30720\n"
      "v_add3_u32 v49, v49, v62, v63\n"
      "v_mfma_f32_16x16x32_bf16 v[152:155], a[208:211], v[88:91], v[152:155]\n"
      "ds_read_b64_tr_b16 v[120:121], v34 offset:28672\n"
      "v_permlane32_swap_b32 v225, v229\n"
      "v_mfma_f32_16x16x32_bf16 v[168:171], a[200:203], v[72:75], v[168:171]\n"
      "global_load_lds_dwordx4 v43, s[4:5] offset:0 sc1\n"
      "ds_read_b64_tr_b16 v[122:123], v34 offset:30720\n"
      "v_add_u32_dpp v49, v49, v49 quad_perm:[1,0,3,2] row_mask:0xf bank_mask:0xf\n"
      "v_permlane32_swap_b32 v226, v230\n"
      "v_mfma_f32_16x16x32_bf16 v[168:171], a[212:215], v[88:91], v[168:171]\n"
      "ds_read_b64_tr_b16 v[124:125], v35 offset:28672\n"
      "v_permlane32_swap_b32 v227, v231\n"
      "v_mfma_f32_16x16x32_bf16 v[140:143], a[192:195], v[76:79], v[140:143]\n"
      "ds_read_b64_tr_b16 v[126:127], v35 offset:30720\n"
      "v_add_u32_dpp v49, v49, v49 quad_perm:[2,3,0,1] row_mask:0xf bank_mask:0xf\n"
      "v_add_f32 v224, v224, v228\n"
      "v_mfma_f32_16x16x32_bf16 v[140:143], a[204:207], v[92:95], v[140:143]\n"
      "v_add_f32 v225, v225, v229\n"
      "v_add_f32 v226, v226, v230\n"
      "v_mfma_f32_16x16x32_bf16 v[156:159], a[196:199], v[76:79], v[156:159]\n"
      "global_load_dwordx4 a[252:255], v44, s[6:7] offset:-1024 sc1\n"
      "v_add_u32_dpp v49, v49, v49 row_half_mirror row_mask:0xf bank_mask:0xf\n"
      "v_cmp_ne_u32 vcc, s71, v49\n"
      "v_mfma_f32_16x16x32_bf16 v[156:159], a[208:211], v[92:95], v[156:159]\n"
      "global_load_lds_dwordx4 v43, s[4:5] offset:1024 sc1\n"
      "v_add_f32 v227, v227, v231\n"
      "v_mfma_f32_16x16x32_bf16 v[172:175], a[200:203], v[76:79], v[172:175]\n"
      "ds_write_b128 v51, v[224:227]\n"
      "s_cbranch_vccnz L_sx6_%=\n"
      "L_bx6_%=:\n"
      "v_mfma_f32_16x16x32_bf16 v[172:175], a[212:215], v[92:95], v[172:175]\n"
      "s_waitcnt vmcnt(14)\n"
      "s_waitcnt lgkmcnt(1)\n"
      "s_barrier\n"
      "v_mfma_f32_16x16x32_bf16 v[128:131], a[216:219], v[96:99], v[128:131]\n"
      "global_load_dwordx4 a[0:3], v44, s[6:7] offset:0 sc1\n"
      "v_mfma_f32_16x16x32_bf16 v[128:131], a[228:231], v[112:115], v[128:131]\n"
      "global_load_lds_dwordx4 v43, s[4:5] offset:2048 sc1\n"
      "v_mfma_f32_16x16x32_bf16 v[144:147], a[220:223], v[96:99], v[144:147]\n"
      "ds_read_b64_tr_b16 v[64:65], v6 offset:32768\n"
      "s_waitcnt vmcnt(12)\n"
      "v_add3_u32 v10, v10, v14, v18\n"
      "v_mfma_f32_16x16x32_bf16 v[144:147], a[232:235], v[112:115], v[144:147]\n"
      "ds_read_b64_tr_b16 v[66:67], v6 offset:34816\n"
      "v_add_u32 v10, v10, v22\n"
      "v_cmp_ne_u32 vcc, s71, v10\n"
      "v_mfma_f32_16x16x32_bf16 v[160:163], a[224:227], v[96:99], v[160:163]\n"
      "global_load_dwordx4 a[4:7], v44, s[6:7] offset:1024 sc1\n"
      "ds_read_b64_tr_b16 v[68:69], v34 offset:32768\n"
      "v_mfma_f32_16x16x32_bf16 v[160:163], a[236:239], v[112:115], v[160:163]\n"
      "global_load_lds_dwordx4 v43, s[4:5] offset:3072 sc1\n"
      "ds_read_b64_tr_b16 v[70:71], v34 offset:34816\n"
      "s_cbranch_vccnz L_sg_%=\n"
      "L_bg_%=:\n"
      "v_add_f32 v8, v8, v12\n"
      "v_add_f32 v9, v9, v13\n"
      "v_mfma_f32_16x16x32_bf16 v[132:135], a[216:219], v[100:103], v[132:135]\n"
      "ds_read_b64_tr_b16 v[72:73], v33 offset:32768\n"
      "v_add_f32 v16, v16, v20\n"
      "v_add_f32 v17, v17, v21\n"
      "v_mfma_f32_16x16x32_bf16 v[132:135], a[228:231], v[116:119], v[132:135]\n"
      "ds_read_b64_tr_b16 v[74:75], v33 offset:34816\n"
      "v_add_f32 v8, v8, v16\n"
      "v_add_u32 v245, v245, v247\n"
      "v_mfma_f32_16x16x32_bf16 v[148:151], a[220:223], v[100:103], v[148:151]\n"
      "global_load_dwordx4 a[8:11], v44, s[6:7] offset:2048 sc1\n"
      "ds_read_b64_tr_b16 v[76:77], v35 offset:32768\n"
      "v_add_f32 v9, v9, v17\n"
      "v_mov_b32 v10, v8\n"
      "v_cmp_ne_u32 vcc, s54, v245\n"
      "v_mfma_f32_16x16x32_bf16 v[148:151], a[232:235], v[116:119], v[148:151]\n"
      "ds_read_b64_tr_b16 v[78:79], v35 offset:34816\n"
      "v_mov_b32 v11, v9\n"
      "s_nop 0\n"
      "v_mfma_f32_16x16x32_bf16 v[164:167], a[224:227], v[100:103], v[164:167]\n"
      "ds_read_b64_tr_b16 v[80:81], v6 offset:36864\n"
      "v_permlane32_swap_b32 v8, v10\n"
      "v_permlane32_swap_b32 v9, v11\n"
      "s_cbranch_vccnz L_sgb_%=\n"
      "L_bgb_%=:\n"
      "v_add_f32 v8, v8, v10\n"
      "v_mfma_f32_16x16x32_bf16 v[164:167], a[236:239], v[116:119], v[164:167]\n"
      "ds_read_b64_tr_b16 v[82:83], v6 offset:38912\n"
      "v_add_f32_dpp v244, v244, v244 quad_perm:[1,0,3,2] row_mask:0xf bank_mask:0xf\n"
      "v_add_f32_dpp v246, v246, v246 quad_perm:[1,0,3,2] row_mask:0xf bank_mask:0xf\n"
      "v_mfma_f32_16x16x32_bf16 v[136:139], a[216:219], v[104:107], v[136:139]\n"
      "global_load_dwordx4 a[12:15], v44, s[6:7] offset:3072 sc1\n"
      "v_add_f32 v9, v9, v11\n"
      "v_mul_f32 v8, 0x3acccccd, v8\n"
      "v_add_f32_dpp v244, v244, v244 quad_perm:[2,3,0,1] row_mask:0xf bank_mask:0xf\n"
      "v_add_f32_dpp v246, v246, v246 quad_perm:[2,3,0,1] row_mask:0xf bank_mask:0xf\n"
      "v_mfma_f32_16x16x32_bf16 v[136:139], a[228:231], v[120:123], v[136:139]\n"
      "ds_read_b64_tr_b16 v[84:85], v34 offset:36864\n"
      "v_mul_f32 v9, 0x3acccccd, v9\n"
      "v_fma_f32 v9, -v8, v8, v9\n"
      "s_add_u32 m0, s40, 0\n"
      "v_add_u32 v43, 40960, v7\n"
      "v_mfma_f32_16x16x32_bf16 v[152:155], a[220:223], v[104:107], v[152:155]\n"
      "global_load_lds_dwordx4 v43, s[6:7] offset:0 sc1\n"
      "global_load_lds_dwordx4 v43, s[6:7] offset:1024 sc1\n"
      "global_load_lds_dwordx4 v43, s[6:7] offset:2048 sc1\n"
      "global_load_lds_dwordx4 v43, s[6:7] offset:3072 sc1\n"
      "ds_read_b64_tr_b16 v[86:87], v34 offset:38912\n"
      "v_max_f32 v9, 0, v9\n"
      "s_add_u32 m0, s73, 16384\n"
      "v_add_u32 v43, 147456, v40\n"
      "v_cvt_pk_bf16_f32 v244, 0, v244\n"
      "v_cvt_pk_bf16_f32 v245, 0, v246\n"
      "v_mfma_f32_16x16x32_bf16 v[152:155], a[232:235], v[120:123], v[152:155]\n"
      "global_load_lds_dwordx4 v43, s[4:5] offset:0 sc1\n"
      "ds_read_b64_tr_b16 v[88:89], v33 offset:36864\n"
      "ds_write_b64 v59, v[244:245]\n"
      "v_cvt_pk_bf16_f32 v9, 0, v9\n"
      "v_add_f32 v9, 0x3727c5ac, v9\n"
      "v_add_u32 v44, 12288, v7\n"
      "v_mfma_f32_16x16x32_bf16 v[168:171], a[224:227], v[104:107], v[168:171]\n"
      "global_load_dwordx4 a[16:19], v44, s[6:7] offset:-4096 sc1\n"
      "ds_read_b64_tr_b16 v[90:91], v33 offset:38912\n"
      "ds_read_b128 v[224:227], v52 offset:0\n"
      "v_cvt_pk_bf16_f32 v9, 0, v9\n"
      "v_mfma_f32_16x16x32_bf16 v[168:171], a[236:239], v[120:123], v[168:171]\n"
      "ds_read_b64_tr_b16 v[92:93], v35 offset:36864\n"
      "ds_read_b128 v[228:231], v52 offset:128\n"
      "v_sqrt_f32 v9, v9\n"
      "v_sub_f32 v8, 0, v8\n"
      "v_cvt_pk_bf16_f32 v9, 0, v9\n"
      "v_mfma_f32_16x16x32_bf16 v[140:143], a[216:219], v[108:111], v[140:143]\n"
      "ds_read_b128 v[232:235], v52 offset:256\n"
      "v_rcp_f32 v9, v9\n"
      "v_cvt_pk_bf16_f32 v8, 0, v8\n"
      "v_mfma_f32_16x16x32_bf16 v[140:143], a[228:231], v[124:127], v[140:143]\n"
      "global_load_lds_dwordx4 v43, s[4:5] offset:1024 sc1\n"
      "ds_write_b64 v32, v[8:9]\n"
      "ds_read_b64_tr_b16 v[94:95], v35 offset:38912\n"
      "v_mfma_f32_16x16x32_bf16 v[156:159], a[220:223], v[108:111], v[156:159]\n"
      "global_load_dwordx4 a[20:23], v44, s[6:7] offset:-3072 sc1\n"
      "ds_read_b128 v[236:239], v52 offset:64\n"
      "v_mfma_f32_16x16x32_bf16 v[156:159], a[232:235], v[124:127], v[156:159]\n"
      "ds_read_b128 v[240:243], v52 offset:192\n"
      "v_mfma_f32_16x16x32_bf16 v[172:175], a[224:227], v[108:111], v[172:175]\n"
      "ds_read_b128 v[244:247], v52 offset:320\n"
      "v_mfma_f32_16x16x32_bf16 v[172:175], a[236:239], v[124:127], v[172:175]\n"
      "global_load_lds_dwordx4 v43, s[4:5] offset:2048 sc1\n"
      "s_waitcnt lgkmcnt(3)\n"
      "s_barrier\n"
      "v_mfma_f32_16x16x32_bf16 v[176:179], a[48:51], v[64:67], v[176:179]\n"
      "global_load_dwordx4 a[24:27], v44, s[6:7] offset:-2048 sc1\n"
      "ds_read_b64 v[16:17], v41 offset:0\n"
      "v_mfma_f32_16x16x32_bf16 v[176:179], a[60:63], v[80:83], v[176:179]\n"
      "ds_read_b64 v[18:19], v41 offset:128\n"
      "v_mfma_f32_16x16x32_bf16 v[184:187], a[52:55], v[64:67], v[184:187]\n"
      "ds_read_b64 v[20:21], v41 offset:256\n"
      "ds_read_b64_tr_b16 v[96:97], v6 offset:40960\n"
      "s_waitcnt vmcnt(22)\n"
      "v_mfma_f32_16x16x32_bf16 v[184:187], a[64:67], v[80:83], v[184:187]\n"
      "global_load_lds_dwordx4 v43, s[4:5] offset:3072 sc1\n"
      "ds_read_b128 v[248:251], v40 offset:49152\n"
      "ds_read_b64 v[22:23], v41 offset:384\n"
      "ds_read_b64_tr_b16 v[98:99], v6 offset:43008\n"
      "v_mfma_f32_16x16x32_bf16 v[192:195], a[56:59], v[64:67], v[192:195]\n"
      "global_load_dwordx4 a[28:31], v44, s[6:7] offset:-1024 sc1\n"
      "ds_read_b128 v[252:255], v40 offset:50176\n"
      "ds_read_b64_tr_b16 v[100:101], v34 offset:40960\n"
      "v_mfma_f32_16x16x32_bf16 v[192:195], a[68:71], v[80:83], v[192:195]\n"
      "ds_read_b128 v[36:39], v40 offset:51200\n"
      "ds_read_b64_tr_b16 v[102:103], v34 offset:43008\n"
      "v_mfma_f32_16x16x32_bf16 v[180:183], a[48:51], v[68:71], v[180:183]\n"
      "ds_read_b128 v[60:63], v40 offset:52224\n"
      "ds_read_b64_tr_b16 v[104:105], v33 offset:40960\n"
      "s_waitcnt lgkmcnt(7)\n"
      "v_mfma_f32_16x16x32_bf16 v[180:183], a[60:63], v[84:87], v[180:183]\n"
      "ds_read_b64_tr_b16 v[106:107], v33 offset:43008\n"
      "v_fmac_f32 v128, v16, v224\n"
      "v_fmac_f32 v129, v16, v225\n"
      "v_mfma_f32_16x16x32_bf16 v[188:191], a[52:55], v[68:71], v[188:191]\n"
      "global_load_dwordx4 a[32:35], v44, s[6:7] offset:0 sc1\n"
      "ds_read_b64_tr_b16 v[108:109], v35 offset:40960\n"
      "v_fmac_f32 v130, v16, v226\n"
      "v_fmac_f32 v131, v16, v227\n"
      "v_mfma_f32_16x16x32_bf16 v[188:191], a[64:67], v[84:87], v[188:191]\n"
      "ds_read_b64_tr_b16 v[110:111], v35 offset:43008\n"
      "v_fma_f32 v128, v128, v17, v236\n"
      "v_fma_f32 v129, v129, v17, v237\n"
      "v_fma_f32 v130, v130, v17, v238\n"
      "v_mfma_f32_16x16x32_bf16 v[196:199], a[56:59], v[68:71], v[196:199]\n"
      "ds_read_b64_tr_b16 v[112:113], v6 offset:45056\n"
      "s_waitcnt lgkmcnt(5)\n"
      "v_add3_u32 v55, v55, v248, v249\n"
      "v_add3_u32 v55, v55, v250, v251\n"
      "v_fma_f32 v131, v131, v17, v239\n"
      "v_mul_f32 v128, v128, v130\n"
      "v_mfma_f32_16x16x32_bf16 v[196:199], a[68:71], v[84:87], v[196:199]\n"
      "ds_read_b64_tr_b16 v[114:115], v6 offset:47104\n"
      "v_add3_u32 v55, v55, v252, v253\n"
      "v_add3_u32 v55, v55, v254, v255\n"
      "v_mul_f32 v129, v129, v131\n"
      "v_cvt_pk_bf16_f32 v128, v128, v129\n"
      "v_mfma_f32_16x16x32_bf16 v[200:203], a[48:51], v[72:75], v[200:203]\n"
      "global_load_dwordx4 a[36:39], v44, s[6:7] offset:1024 sc1\n"
      "ds_write_b32 v46, v128 offset:0\n"
      "v_add3_u32 v55, v55, v36, v37\n"
      "v_add3_u32 v55, v55, v38, v39\n"
      "v_fmac_f32 v144, v16, v228\n"
      "v_fmac_f32 v145, v16, v229\n"
      "v_mfma_f32_16x16x32_bf16 v[200:203], a[60:63], v[88:91], v[200:203]\n"
      "ds_read_b64_tr_b16 v[116:117], v34 offset:45056\n"
      "v_add3_u32 v55, v55, v60, v61\n"
      "v_fmac_f32 v146, v16, v230\n"
      "v_fmac_f32 v147, v16, v231\n"
      "v_mfma_f32_16x16x32_bf16 v[208:211], a[52:55], v[72:75], v[208:211]\n"
      "ds_read_b64_tr_b16 v[118:119], v34 offset:47104\n"
      "v_add3_u32 v55, v55, v62, v63\n"
      "v_fma_f32 v144, v144, v17, v240\n"
      "v_fma_f32 v145, v145, v17, v241\n"
      "v_mfma_f32_16x16x32_bf16 v[208:211], a[64:67], v[88:91], v[208:211]\n"
      "ds_read_b64_tr_b16 v[120:121], v33 offset:45056\n"
      "v_fma_f32 v146, v146, v17, v242\n"
      "v_fma_f32 v147, v147, v17, v243\n"
      "v_mul_f32 v144, v144, v146\n"
      "v_mfma_f32_16x16x32_bf16 v[216:219], a[56:59], v[72:75], v[216:219]\n"
      "global_load_dwordx4 a[40:43], v44, s[6:7] offset:2048 sc1\n"
      "ds_read_b64_tr_b16 v[122:123], v33 offset:47104\n"
      "v_add_u32_dpp v55, v55, v55 quad_perm:[1,0,3,2] row_mask:0xf bank_mask:0xf\n"
      "v_mul_f32 v145, v145, v147\n"
      "v_cvt_pk_bf16_f32 v144, v144, v145\n"
      "v_mfma_f32_16x16x32_bf16 v[216:219], a[68:71], v[88:91], v[216:219]\n"
      "ds_write_b32 v46, v144 offset:4096\n"
      "ds_read_b64_tr_b16 v[124:125], v35 offset:45056\n"
      "v_fmac_f32 v160, v16, v232\n"
      "v_mfma_f32_16x16x32_bf16 v[204:207], a[48:51], v[76:79], v[204:207]\n"
      "ds_read_b64_tr_b16 v[126:127], v35 offset:47104\n"
      "v_add_u32_dpp v55, v55, v55 quad_perm:[2,3,0,1] row_mask:0xf bank_mask:0xf\n"
      "v_fmac_f32 v161, v16, v233\n"
      "v_fmac_f32 v162, v16, v234\n"
      "v_fmac_f32 v163, v16, v235\n"
      "v_mfma_f32_16x16x32_bf16 v[204:207], a[60:63], v[92:95], v[204:207]\n"
      "v_fma_f32 v160, v160, v17, v244\n"
      "v_fma_f32 v161, v161, v17, v245\n"
      "v_mfma_f32_16x16x32_bf16 v[212:215], a[52:55], v[76:79], v[212:215]\n"
      "global_load_dwordx4 a[44:47], v44, s[6:7] offset:3072 sc1\n"
      "v_add_u32_dpp v55, v55, v55 row_half_mirror row_mask:0xf bank_mask:0xf\n"
      "v_cmp_ne_u32 vcc, s71, v55\n"
      "v_fma_f32 v162, v162, v17, v246\n"
      "v_fma_f32 v163, v163, v17, v247\n"
      "v_mul_f32 v160, v160, v162\n"
      "v_mfma_f32_16x16x32_bf16 v[212:215], a[64:67], v[92:95], v[212:215]\n"
      "v_mul_f32 v161, v161, v163\n"
      "v_cvt_pk_bf16_f32 v160, v160, v161\n"
      "v_mfma_f32_16x16x32_bf16 v[220:223], a[56:59], v[76:79], v[220:223]\n"
      "ds_write_b32 v46, v160 offset:8192\n"
      "s_cbranch_vccnz L_sx7_%=\n"
      "L_bx7_%=:\n"
      "v_fmac_f32 v132, v18, v224\n"
      "v_mfma_f32_16x16x32_bf16 v[220:223], a[68:71], v[92:95], v[220:223]\n"
      "v_fmac_f32 v133, v18, v225\n"
      "v_fmac_f32 v134, v18, v226\n"
      "v_fmac_f32 v135, v18, v227\n"
      "s_waitcnt lgkmcnt(1)\n"
      "s_barrier\n"
      "v_mfma_f32_16x16x32_bf16 v[176:179], a[72:75], v[96:99], v[176:179]\n"
      "ds_read_b64_tr_b16 v[64:65], v6 offset:49152\n"
      "v_fma_f32 v132, v132, v19, v236\n"
      "v_fma_f32 v133, v133, v19, v237\n"
      "v_mfma_f32_16x16x32_bf16 v[176:179], a[84:87], v[112:115], v[176:179]\n"
      "ds_read_b64_tr_b16 v[66:67], v6 offset:51200\n"
      "v_fma_f32 v134, v134, v19, v238\n"
      "v_fma_f32 v135, v135, v19, v239\n"
      "v_add_u32 v44, 20480, v7\n"
      "v_mfma_f32_16x16x32_bf16 v[184:187], a[76:79], v[96:99], v[184:187]\n"
      "global_load_dwordx4 a[48:51], v44, s[6:7] offset:-4096 sc1\n"
      "v_mul_f32 v132, v132, v134\n"
      "v_mul_f32 v133, v133, v135\n"
      "v_cvt_pk_bf16_f32 v132, v132, v133\n"
      "v_mfma_f32_16x16x32_bf16 v[184:187], a[88:91], v[112:115], v[184:187]\n"
      "ds_write_b32 v46, v132 offset:1024\n"
      "ds_read_b64_tr_b16 v[68:69], v34 offset:49152\n"
      "v_fmac_f32 v148, v18, v228\n"
      "v_mfma_f32_16x16x32_bf16 v[192:195], a[80:83], v[96:99], v[192:195]\n"
      "global_load_dwordx4 a[52:55], v44, s[6:7] offset:-3072 sc1\n"
      "s_waitcnt vmcnt(20)\n"
      "v_fmac_f32 v149, v18, v229\n"
      "v_fmac_f32 v150, v18, v230\n"
      "v_mfma_f32_16x16x32_bf16 v[192:195], a[92:95], v[112:115], v[192:195]\n"
      "ds_read_b128 v[248:251], v40 offset:0\n"
      "ds_read_b64_tr_b16 v[70:71], v34 offset:51200\n"
      "v_fmac_f32 v151, v18, v231\n"
      "v_fma_f32 v148, v148, v19, v240\n"
      "v_fma_f32 v149, v149, v19, v241\n"
      "v_mfma_f32_16x16x32_bf16 v[180:183], a[72:75], v[100:103], v[180:183]\n"
      "ds_read_b128 v[252:255], v40 offset:1024\n"
      "ds_read_b64_tr_b16 v[72:73], v33 offset:49152\n"
      "v_fma_f32 v150, v150, v19, v242\n"
      "v_fma_f32 v151, v151, v19, v243\n"
      "v_mfma_f32_16x16x32_bf16 v[180:183], a[84:87], v[116:119], v[180:183]\n"
      "ds_read_b128 v[36:39], v40 offset:2048\n"
      "ds_read_b64_tr_b16 v[74:75], v33 offset:51200\n"
      "v_mul_f32 v148, v148, v150\n"
      "v_mul_f32 v149, v149, v151\n"
      "v_cvt_pk_bf16_f32 v148, v148, v149\n"
      "v_mfma_f32_16x16x32_bf16 v[188:191], a[76:79], v[100:103], v[188:191]\n"
      "global_load_dwordx4 a[56:59], v44, s[6:7] offset:-2048 sc1\n"
      "ds_read_b128 v[60:63], v40 offset:3072\n"
      "ds_write_b32 v46, v148 offset:5120\n"
      "ds_read_b64_tr_b16 v[76:77], v35 offset:49152\n"
      "v_fmac_f32 v164, v18, v232\n"
      "v_mfma_f32_16x16x32_bf16 v[188:191], a[88:91], v[116:119], v[188:191]\n"
      "ds_read_b64_tr_b16 v[78:79], v35 offset:51200\n"
      "v_fmac_f32 v165, v18, v233\n"
      "v_fmac_f32 v166, v18, v234\n"
      "v_mfma_f32_16x16x32_bf16 v[196:199], a[80:83], v[100:103], v[196:199]\n"
      "ds_read_b64_tr_b16 v[80:81], v6 offset:53248\n"
      "v_fmac_f32 v167, v18, v235\n"
      "v_fma_f32 v164, v164, v19, v244\n"
      "v_fma_f32 v165, v165, v19, v245\n"
      "v_mfma_f32_16x16x32_bf16 v[196:199], a[92:95], v[116:119], v[196:199]\n"
      "ds_read_b64_tr_b16 v[82:83], v6 offset:55296\n"
      "v_fma_f32 v166, v166, v19, v246\n"
      "v_fma_f32 v167, v167, v19, v247\n"
      "v_mfma_f32_16x16x32_bf16 v[200:203], a[72:75], v[104:107], v[200:203]\n"
      "global_load_dwordx4 a[60:63], v44, s[6:7] offset:-1024 sc1\n"
      "s_waitcnt lgkmcnt(5)\n"
      "v_add3_u32 v57, v57, v248, v249\n"
      "v_add3_u32 v57, v57, v250, v251\n"
      "v_mul_f32 v164, v164, v166\n"
      "v_mul_f32 v165, v165, v167\n"
      "v_cvt_pk_bf16_f32 v164, v164, v165\n"
      "v_mfma_f32_16x16x32_bf16 v[200:203], a[84:87], v[120:123], v[200:203]\n"
      "ds_write_b32 v46, v164 offset:9216\n"
      "ds_read_b64_tr_b16 v[84:85], v34 offset:53248\n"
      "v_add3_u32 v57, v57, v252, v253\n"
      "v_add3_u32 v57, v57, v254, v255\n"
      "v_fmac_f32 v136, v20, v224\n"
      "v_mfma_f32_16x16x32_bf16 v[208:211], a[76:79], v[104:107], v[208:211]\n"
      "ds_read_b64_tr_b16 v[86:87], v34 offset:55296\n"
      "v_add3_u32 v57, v57, v36, v37\n"
      "v_add3_u32 v57, v57, v38, v39\n"
      "v_fmac_f32 v137, v20, v225\n"
      "v_fmac_f32 v138, v20, v226\n"
      "v_mfma_f32_16x16x32_bf16 v[208:211], a[88:91], v[120:123], v[208:211]\n"
      "ds_read_b64_tr_b16 v[88:89], v33 offset:53248\n"
      "v_add3_u32 v57, v57, v60, v61\n"
      "v_fmac_f32 v139, v20, v227\n"
      "v_fma_f32 v136, v136, v21, v236\n"
      "v_mfma_f32_16x16x32_bf16 v[216:219], a[80:83], v[104:107], v[216:219]\n"
      "global_load_dwordx4 a[64:67], v44, s[6:7] offset:0 sc1\n"
      "ds_read_b64_tr_b16 v[90:91], v33 offset:55296\n"
      "v_add3_u32 v57, v57, v62, v63\n"
      "v_fma_f32 v137, v137, v21, v237\n"
      "v_fma_f32 v138, v138, v21, v238\n"
      "v_fma_f32 v139, v139, v21, v239\n"
      "v_mfma_f32_16x16x32_bf16 v[216:219], a[92:95], v[120:123], v[216:219]\n"
      "ds_read_b64_tr_b16 v[92:93], v35 offset:53248\n"
      "v_mul_f32 v136, v136, v138\n"
      "v_mul_f32 v137, v137, v139\n"
      "v_cvt_pk_bf16_f32 v136, v136, v137\n"
      "v_mfma_f32_16x16x32_bf16 v[204:207], a[72:75], v[108:111], v[204:207]\n"
      "ds_write_b32 v46, v136 offset:2048\n"
      "ds_read_b64_tr_b16 v[94:95], v35 offset:55296\n"
      "v_add_u32_dpp v57, v57, v57 quad_perm:[1,0,3,2] row_mask:0xf bank_mask:0xf\n"
      "v_mfma_f32_16x16x32_bf16 v[204:207], a[84:87], v[124:127], v[204:207]\n"
      "v_fmac_f32 v152, v20, v228\n"
      "v_fmac_f32 v153, v20, v229\n"
      "v_fmac_f32 v154, v20, v230\n"
      "v_mfma_f32_16x16x32_bf16 v[212:215], a[76:79], v[108:111], v[212:215]\n"
      "global_load_dwordx4 a[68:71], v44, s[6:7] offset:1024 sc1\n"
      "v_add_u32_dpp v57, v57, v57 quad_perm:[2,3,0,1] row_mask:0xf bank_mask:0xf\n"
      "v_fmac_f32 v155, v20, v231\n"
      "v_fma_f32 v152, v152, v21, v240\n"
      "v_mfma_f32_16x16x32_bf16 v[212:215], a[88:91], v[124:127], v[212:215]\n"
      "v_fma_f32 v153, v153, v21, v241\n"
      "v_fma_f32 v154, v154, v21, v242\n"
      "v_fma_f32 v155, v155, v21, v243\n"
      "v_mfma_f32_16x16x32_bf16 v[220:223], a[80:83], v[108:111], v[220:223]\n"
      "v_add_u32_dpp v57, v57, v57 row_half_mirror row_mask:0xf bank_mask:0xf\n"
      "v_cmp_ne_u32 vcc, s71, v57\n"
      "v_mul_f32 v152, v152, v154\n"
      "v_mul_f32 v153, v153, v155\n"
      "v_cvt_pk_bf16_f32 v152, v152, v153\n"
      "v_mfma_f32_16x16x32_bf16 v[220:223], a[92:95], v[124:127], v[220:223]\n"
      "ds_write_b32 v46, v152 offset:6144\n"
      "v_fmac_f32 v168, v20, v232\n"
      "s_waitcnt lgkmcnt(1)\n"
      "v_mfma_f32_16x16x32_bf16 v[176:179], a[96:99], v[64:67], v[176:179]\n"
      "ds_read_b64_tr_b16 v[96:97], v6 offset:57344\n"
      "s_cbranch_vccnz L_sx8_%=\n"
      "L_bx8_%=:\n"
      "v_fmac_f32 v169, v20, v233\n"
      "v_fmac_f32 v170, v20, v234\n"
      "s_add_u32 m0, s40, 4096\n"
      "L_pg5_%=:\n"
      "s_cbranch_execz L_pgr_%=\n"
      "v_add_u32 v43, 45056, v7\n"
      "v_mfma_f32_16x16x32_bf16 v[176:179], a[108:111], v[80:83], v[176:179]\n"
      "global_load_lds_dwordx4 v43, s[6:7] offset:0 sc1\n"
      "v_fmac_f32 v171, v20, v235\n"
      "v_fma_f32 v168, v168, v21, v244\n"
      "v_mfma_f32_16x16x32_bf16 v[184:187], a[100:103], v[64:67], v[184:187]\n"
      "global_load_dwordx4 a[72:75], v44, s[6:7] offset:2048 sc1\n"
      "s_waitcnt vmcnt(13)\n"
      "v_fma_f32 v169, v169, v21, v245\n"
      "v_fma_f32 v170, v170, v21, v246\n"
      "v_mfma_f32_16x16x32_bf16 v[184:187], a[112:115], v[80:83], v[184:187]\n"
      "ds_read_b128 v[248:251], v40 offset:16384\n"
      "ds_read_b64_tr_b16 v[98:99], v6 offset:59392\n"
      "v_fma_f32 v171, v171, v21, v247\n"
      "v_mul_f32 v168, v168, v170\n"
      "v_mul_f32 v169, v169, v171\n"
      "v_cvt_pk_bf16_f32 v168, v168, v169\n"
      "v_mfma_f32_16x16x32_bf16 v[192:195], a[104:107], v[64:67], v[192:195]\n"
      "global_load_dwordx4 a[76:79], v44, s[6:7] offset:3072 sc1\n"
      "ds_read_b128 v[252:255], v40 offset:17408\n"
      "ds_write_b32 v46, v168 offset:10240\n"
      "ds_read_b64_tr_b16 v[100:101], v34 offset:57344\n"
      "v_fmac_f32 v140, v22, v224\n"
      "v_mfma_f32_16x16x32_bf16 v[192:195], a[116:119], v[80:83], v[192:195]\n"
      "global_load_lds_dwordx4 v43, s[6:7] offset:1024 sc1\n"
      "ds_read_b128 v[36:39], v40 offset:18432\n"
      "ds_read_b64_tr_b16 v[102:103], v34 offset:59392\n"
      "v_fmac_f32 v141, v22, v225\n"
      "v_mfma_f32_16x16x32_bf16 v[180:183], a[96:99], v[68:71], v[180:183]\n"
      "ds_read_b128 v[60:63], v40 offset:19456\n"
      "ds_read_b64_tr_b16 v[104:105], v33 offset:57344\n"
      "v_fmac_f32 v142, v22, v226\n"
      "v_fmac_f32 v143, v22, v227\n"
      "v_fma_f32 v140, v140, v23, v236\n"
      "v_mfma_f32_16x16x32_bf16 v[180:183], a[108:111], v[84:87], v[180:183]\n"
      "ds_read_b64_tr_b16 v[106:107], v33 offset:59392\n"
      "v_fma_f32 v141, v141, v23, v237\n"
      "v_fma_f32 v142, v142, v23, v238\n"
      "v_add_u32 v44, 28672, v7\n"
      "v_mfma_f32_16x16x32_bf16 v[188:191], a[100:103], v[68:71], v[188:191]\n"
      "global_load_dwordx4 a[80:83], v44, s[6:7] offset:-4096 sc1\n"
      "ds_read_b64_tr_b16 v[108:109], v35 offset:57344\n"
      "v_fma_f32 v143, v143, v23, v239\n"
      "v_mul_f32 v140, v140, v142\n"
      "v_mul_f32 v141, v141, v143\n"
      "v_cvt_pk_bf16_f32 v140, v140, v141\n"
      "v_mfma_f32_16x16x32_bf16 v[188:191], a[112:115], v[84:87], v[188:191]\n"
      "global_load_lds_dwordx4 v43, s[6:7] offset:2048 sc1\n"
      "ds_write_b32 v46, v140 offset:3072\n"
      "ds_read_b64_tr_b16 v[110:111], v35 offset:59392\n"
      "v_fmac_f32 v156, v22, v228\n"
      "v_mfma_f32_16x16x32_bf16 v[196:199], a[104:107], v[68:71], v[196:199]\n"
      "ds_read_b64_tr_b16 v[112:113], v6 offset:61440\n"
      "s_waitcnt lgkmcnt(6)\n"
      "v_add3_u32 v3, v3, v248, v249\n"
      "v_add3_u32 v3, v3, v250, v251\n"
      "v_fmac_f32 v157, v22, v229\n"
      "v_mfma_f32_16x16x32_bf16 v[196:199], a[116:119], v[84:87], v[196:199]\n"
      "ds_read_b64_tr_b16 v[114:115], v6 offset:63488\n"
      "v_add3_u32 v3, v3, v252, v253\n"
      "v_add3_u32 v3, v3, v254, v255\n"
      "v_fmac_f32 v158, v22, v230\n"
      "v_fmac_f32 v159, v22, v231\n"
      "v_fma_f32 v156, v156, v23, v240\n"
      "v_mfma_f32_16x16x32_bf16 v[200:203], a[96:99], v[72:75], v[200:203]\n"
      "global_load_dwordx4 a[84:87], v44, s[6:7] offset:-3072 sc1\n"
      "v_add3_u32 v3, v3, v36, v37\n"
      "v_add3_u32 v3, v3, v38, v39\n"
      "v_fma_f32 v157, v157, v23, v241\n"
      "v_fma_f32 v158, v158, v23, v242\n"
      "v_mfma_f32_16x16x32_bf16 v[200:203], a[108:111], v[88:91], v[200:203]\n"
      "global_load_lds_dwordx4 v43, s[6:7] offset:3072 sc1\n"
      "ds_read_b64_tr_b16 v[116:117], v34 offset:61440\n"
      "v_add3_u32 v3, v3, v60, v61\n"
      "v_fma_f32 v159, v159, v23, v243\n"
      "v_mul_f32 v156, v156, v158\n"
      "v_mul_f32 v157, v157, v159\n"
      "v_cvt_pk_bf16_f32 v156, v156, v157\n"
      "v_mfma_f32_16x16x32_bf16 v[208:211], a[100:103], v[72:75], v[208:211]\n"
      "ds_write_b32 v46, v156 offset:7168\n"
      "ds_read_b64_tr_b16 v[118:119], v34 offset:63488\n"
      "v_add3_u32 v3, v3, v62, v63\n"
      "v_mfma_f32_16x16x32_bf16 v[208:211], a[112:115], v[88:91], v[208:211]\n"
      "ds_read_b64_tr_b16 v[120:121], v33 offset:61440\n"
      "v_fmac_f32 v172, v22, v232\n"
      "v_fmac_f32 v173, v22, v233\n"
      "v_mfma_f32_16x16x32_bf16 v[216:219], a[104:107], v[72:75], v[216:219]\n"
      "global_load_dwordx4 a[88:91], v44, s[6:7] offset:-2048 sc1\n"
      "ds_read_b64_tr_b16 v[122:123], v33 offset:63488\n"
      "v_add_u32_dpp v3, v3, v3 quad_perm:[1,0,3,2] row_mask:0xf bank_mask:0xf\n"
      "v_fmac_f32 v174, v22, v234\n"
      "v_fmac_f32 v175, v22, v235\n"
      "s_add_u32 m0, s40, 8192\n"
      "v_add_u32 v43, 49152, v7\n"
      "v_mfma_f32_16x16x32_bf16 v[216:219], a[116:119], v[88:91], v[216:219]\n"
      "global_load_lds_dwordx4 v43, s[6:7] offset:0 sc1\n"
      "ds_read_b64_tr_b16 v[124:125], v35 offset:61440\n"
      "v_fma_f32 v172, v172, v23, v244\n"
      "v_fma_f32 v173, v173, v23, v245\n"
      "v_fma_f32 v174, v174, v23, v246\n"
      "v_mfma_f32_16x16x32_bf16 v[204:207], a[96:99], v[76:79], v[204:207]\n"
      "ds_read_b64_tr_b16 v[126:127], v35 offset:63488\n"
      "v_add_u32_dpp v3, v3, v3 quad_perm:[2,3,0,1] row_mask:0xf bank_mask:0xf\n"
      "v_fma_f32 v175, v175, v23, v247\n"
      "v_mul_f32 v172, v172, v174\n"
      "v_mfma_f32_16x16x32_bf16 v[204:207], a[108:111], v[92:95], v[204:207]\n"
      "v_mul_f32 v173, v173, v175\n"
      "v_cvt_pk_bf16_f32 v172, v172, v173\n"
      "v_mfma_f32_16x16x32_bf16 v[212:215], a[100:103], v[76:79], v[212:215]\n"
      "global_load_dwordx4 a[92:95], v44, s[6:7] offset:-1024 sc1\n"
      "ds_write_b32 v46, v172 offset:11264\n"
      "v_add_u32_dpp v3, v3, v3 row_half_mirror row_mask:0xf bank_mask:0xf\n"
      "v_cmp_ne_u32 vcc, s71, v3\n"
      "v_mfma_f32_16x16x32_bf16 v[212:215], a[112:115], v[92:95], v[212:215]\n"
      "v_mfma_f32_16x16x32_bf16 v[220:223], a[104:107], v[76:79], v[220:223]\n"
      "s_cbranch_vccnz L_sx9_%=\n"
      "L_bx9_%=:\n"
      "v_mfma_f32_16x16x32_bf16 v[220:223], a[116:119], v[92:95], v[220:223]\n"
      "s_waitcnt lgkmcnt(0)\n"
      "s_barrier\n"
      "v_mfma_f32_16x16x32_bf16 v[176:179], a[120:123], v[96:99], v[176:179]\n"
      "ds_read_b128 v[160:163], v47 offset:0\n"
      "v_add_u32 v57, s40, v7\n"
      "v_mfma_f32_16x16x32_bf16 v[176:179], a[132:135], v[112:115], v[176:179]\n"
      "ds_read_b128 v[164:167], v47 offset:4096\n"
      "v_mfma_f32_16x16x32_bf16 v[184:187], a[124:127], v[96:99], v[184:187]\n"
      "global_load_dwordx4 a[96:99], v44, s[6:7] offset:0 sc1\n"
      "ds_read_b128 v[168:171], v47 offset:8192\n"
      "ds_read_b64_tr_b16 v[64:65], v6 offset:0\n"
      "v_mfma_f32_16x16x32_bf16 v[184:187], a[136:139], v[112:115], v[184:187]\n"
      "ds_read_b64_tr_b16 v[66:67], v6 offset:2048\n"
      "v_mfma_f32_16x16x32_bf16 v[192:195], a[128:131], v[96:99], v[192:195]\n"
      "global_load_dwordx4 a[100:103], v44, s[6:7] offset:1024 sc1\n"
      "ds_read_b64_tr_b16 v[68:69], v34 offset:0\n"
      "v_mfma_f32_16x16x32_bf16 v[192:195], a[140:143], v[112:115], v[192:195]\n"
      "ds_read_b64_tr_b16 v[70:71], v34 offset:2048\n"
      "s_waitcnt lgkmcnt(4)\n"
      "v_mfma_f32_16x16x32_bf16 v[180:183], a[120:123], v[100:103], v[180:183]\n"
      "ds_read_b64_tr_b16 v[72:73], v6 offset:4096\n"
      "v_mfma_f32_16x16x32_bf16 v[180:183], a[132:135], v[116:119], v[180:183]\n"
      "buffer_store_dwordx4 v[160:163], v7, s[32:35], s61 offen offset:0 sc1\n"
      "ds_read_b64_tr_b16 v[74:75], v6 offset:6144\n"
      "v_add3_u32 v58, v160, v161, s30\n"
      "v_mfma_f32_16x16x32_bf16 v[188:191], a[124:127], v[100:103], v[188:191]\n"
      "global_load_dwordx4 a[104:107], v44, s[6:7] offset:2048 sc1\n"
      "ds_read_b64_tr_b16 v[76:77], v34 offset:4096\n"
      "v_add3_u32 v58, v58, v162, v163\n"
      "v_add3_u32 v58, v58, v164, v165\n"
      "s_add_u32 m0, s41, 0\n"
      "v_add_u32 v43, 50176, v7\n"
      "v_mfma_f32_16x16x32_bf16 v[188:191], a[136:139], v[116:119], v[188:191]\n"
      "buffer_store_dwordx4 v[164:167], v7, s[32:35], s61 offen offset:1024 sc1\n"
      "global_load_lds_dwordx4 v43, s[6:7] offset:0 sc1\n"
      "ds_read_b64_tr_b16 v[78:79], v34 offset:6144\n"
      "v_add3_u32 v58, v58, v166, v167\n"
      "v_mfma_f32_16x16x32_bf16 v[196:199], a[128:131], v[100:103], v[196:199]\n"
      "ds_read_b64_tr_b16 v[80:81], v6 offset:8192\n"
      "v_add3_u32 v58, v58, v168, v169\n"
      "v_add3_u32 v58, v58, v170, v171\n"
      "v_mfma_f32_16x16x32_bf16 v[196:199], a[140:143], v[116:119], v[196:199]\n"
      "buffer_store_dwordx4 v[168:171], v7, s[32:35], s61 offen offset:2048 sc1\n"
      "v_mfma_f32_16x16x32_bf16 v[200:203], a[120:123], v[104:107], v[200:203]\n"
      "global_load_dwordx4 a[108:111], v44, s[6:7] offset:3072 sc1\n"
      "ds_read_b64_tr_b16 v[82:83], v6 offset:10240\n"
      "v_mfma_f32_16x16x32_bf16 v[200:203], a[132:135], v[120:123], v[200:203]\n"
      "buffer_store_dword v58, v42, s[32:35], s62 offen sc1\n"
      "global_load_lds_dwordx4 v43, s[6:7] offset:1024 sc1\n"
      "ds_read_b64_tr_b16 v[84:85], v34 offset:8192\n"
      "v_mfma_f32_16x16x32_bf16 v[208:211], a[124:127], v[104:107], v[208:211]\n"
      "ds_read_b64_tr_b16 v[86:87], v34 offset:10240\n"
      "v_mfma_f32_16x16x32_bf16 v[208:211], a[136:139], v[120:123], v[208:211]\n"
      "ds_read_b64_tr_b16 v[88:89], v6 offset:12288\n"
      "v_add_u32 v44, 36864, v7\n"
      "v_mfma_f32_16x16x32_bf16 v[216:219], a[128:131], v[104:107], v[216:219]\n"
      "global_load_dwordx4 a[112:115], v44, s[6:7] offset:-4096 sc1\n"
      "ds_read_b64_tr_b16 v[90:91], v6 offset:14336\n"
      "v_mfma_f32_16x16x32_bf16 v[216:219], a[140:143], v[120:123], v[216:219]\n"
      "global_load_lds_dwordx4 v43, s[6:7] offset:2048 sc1\n"
      "ds_read_b64_tr_b16 v[92:93], v34 offset:12288\n"
      "v_mfma_f32_16x16x32_bf16 v[204:207], a[120:123], v[108:111], v[204:207]\n"
      "ds_read_b64_tr_b16 v[94:95], v34 offset:14336\n"
      "v_mfma_f32_16x16x32_bf16 v[204:207], a[132:135], v[124:127], v[204:207]\n"
      "ds_read_b64 v[24:25], v41 offset:512\n"
      "v_mfma_f32_16x16x32_bf16 v[212:215], a[124:127], v[108:111], v[212:215]\n"
      "global_load_dwordx4 a[116:119], v44, s[6:7] offset:-3072 sc1\n"
      "v_mfma_f32_16x16x32_bf16 v[212:215], a[136:139], v[124:127], v[212:215]\n"
      "ds_read_b64 v[26:27], v41 offset:768\n"
      "v_mfma_f32_16x16x32_bf16 v[220:223], a[128:131], v[108:111], v[220:223]\n"
      "ds_read_b64 v[28:29], v41 offset:640\n"
      "v_mfma_f32_16x16x32_bf16 v[220:223], a[140:143], v[124:127], v[220:223]\n"
      "ds_read_b64 v[30:31], v41 offset:896\n"
      "s_waitcnt lgkmcnt(4)\n"
      "v_mfma_f32_16x16x32_bf16 v[176:179], a[144:147], v[64:67], v[176:179]\n"
      "ds_read_b64_tr_b16 v[96:97], v6 offset:16384\n"
      "v_mfma_f32_16x16x32_bf16 v[176:179], a[156:159], v[72:75], v[176:179]\n"
      "ds_read_b64_tr_b16 v[98:99], v6 offset:18432\n"
      "v_mfma_f32_16x16x32_bf16 v[184:187], a[148:151], v[64:67], v[184:187]\n"
      "global_load_dwordx4 a[120:123], v44, s[6:7] offset:-2048 sc1\n"
      "v_mfma_f32_16x16x32_bf16 v[184:187], a[160:163], v[72:75], v[184:187]\n"
      "ds_read_b64_tr_b16 v[100:101], v34 offset:16384\n"
      "v_mfma_f32_16x16x32_bf16 v[192:195], a[152:155], v[64:67], v[192:195]\n"
      "global_load_dwordx4 a[124:127], v44, s[6:7] offset:-1024 sc1\n"
      "v_mfma_f32_16x16x32_bf16 v[192:195], a[164:167], v[72:75], v[192:195]\n"
      "ds_read_b64_tr_b16 v[102:103], v34 offset:18432\n"
      "v_mfma_f32_16x16x32_bf16 v[180:183], a[144:147], v[68:71], v[180:183]\n"
      "ds_read_b64_tr_b16 v[104:105], v6 offset:20480\n"
      "v_mfma_f32_16x16x32_bf16 v[180:183], a[156:159], v[76:79], v[180:183]\n"
      "ds_read_b64_tr_b16 v[106:107], v6 offset:22528\n"
      "v_mfma_f32_16x16x32_bf16 v[188:191], a[148:151], v[68:71], v[188:191]\n"
      "global_load_dwordx4 a[128:131], v44, s[6:7] offset:0 sc1\n"
      "ds_read_b64_tr_b16 v[108:109], v34 offset:20480\n"
      "v_mfma_f32_16x16x32_bf16 v[188:191], a[160:163], v[76:79], v[188:191]\n"
      "ds_read_b64_tr_b16 v[110:111], v34 offset:22528\n"
      "v_mfma_f32_16x16x32_bf16 v[196:199], a[152:155], v[68:71], v[196:199]\n"
      "ds_read_b64_tr_b16 v[112:113], v6 offset:24576\n"
      "v_mfma_f32_16x16x32_bf16 v[196:199], a[164:167], v[76:79], v[196:199]\n"
      "ds_read_b64_tr_b16 v[114:115], v6 offset:26624\n"
      "v_mfma_f32_16x16x32_bf16 v[176:179], a[168:171], v[80:83], v[176:179]\n"
      "global_load_dwordx4 a[132:135], v44, s[6:7] offset:1024 sc1\n"
      "v_mfma_f32_16x16x32_bf16 v[176:179], a[180:183], v[88:91], v[176:179]\n"
      "ds_read_b64_tr_b16 v[116:117], v34 offset:24576\n"
      "v_mfma_f32_16x16x32_bf16 v[184:187], a[172:175], v[80:83], v[184:187]\n"
      "ds_read_b64_tr_b16 v[118:119], v34 offset:26624\n"
      "v_mfma_f32_16x16x32_bf16 v[184:187], a[184:187], v[88:91], v[184:187]\n"
      "ds_read_b64_tr_b16 v[120:121], v6 offset:28672\n"
      "v_mfma_f32_16x16x32_bf16 v[192:195], a[176:179], v[80:83], v[192:195]\n"
      "global_load_dwordx4 a[136:139], v44, s[6:7] offset:2048 sc1\n"
      "ds_read_b64_tr_b16 v[122:123], v6 offset:30720\n"
      "v_mfma_f32_16x16x32_bf16 v[192:195], a[188:191], v[88:91], v[192:195]\n"
      "ds_read_b64_tr_b16 v[124:125], v34 offset:28672\n"
      "v_mfma_f32_16x16x32_bf16 v[180:183], a[168:171], v[84:87], v[180:183]\n"
      "ds_read_b64_tr_b16 v[126:127], v34 offset:30720\n"
      "v_mfma_f32_16x16x32_bf16 v[180:183], a[180:183], v[92:95], v[180:183]\n"
      "v_mfma_f32_16x16x32_bf16 v[188:191], a[172:175], v[84:87], v[188:191]\n"
      "global_load_dwordx4 a[140:143], v44, s[6:7] offset:3072 sc1\n"
      "v_mfma_f32_16x16x32_bf16 v[188:191], a[184:187], v[92:95], v[188:191]\n"
      "v_mfma_f32_16x16x32_bf16 v[196:199], a[176:179], v[84:87], v[196:199]\n"
      "v_mfma_f32_16x16x32_bf16 v[196:199], a[188:191], v[92:95], v[196:199]\n"
      "s_waitcnt lgkmcnt(0)\n"
      "v_mfma_f32_16x16x32_bf16 v[176:179], a[192:195], v[96:99], v[176:179]\n"
      "ds_read_b64_tr_b16 v[64:65], v33 offset:0\n"
      "v_mfma_f32_16x16x32_bf16 v[176:179], a[204:207], v[104:107], v[176:179]\n"
      "ds_read_b64_tr_b16 v[66:67], v33 offset:2048\n"
      "v_mfma_f32_16x16x32_bf16 v[184:187], a[196:199], v[96:99], v[184:187]\n"
      "ds_read_b64_tr_b16 v[68:69], v35 offset:0\n"
      "v_mfma_f32_16x16x32_bf16 v[184:187], a[208:211], v[104:107], v[184:187]\n"
      "ds_read_b64_tr_b16 v[70:71], v35 offset:2048\n"
      "v_mfma_f32_16x16x32_bf16 v[192:195], a[200:203], v[96:99], v[192:195]\n"
      "ds_read_b64_tr_b16 v[72:73], v33 offset:4096\n"
      "v_mfma_f32_16x16x32_bf16 v[192:195], a[212:215], v[104:107], v[192:195]\n"
      "ds_read_b64_tr_b16 v[74:75], v33 offset:6144\n"
      "v_mfma_f32_16x16x32_bf16 v[180:183], a[192:195], v[100:103], v[180:183]\n"
      "ds_read_b64_tr_b16 v[76:77], v35 offset:4096\n"
      "v_mfma_f32_16x16x32_bf16 v[180:183], a[204:207], v[108:111], v[180:183]\n"
      "ds_read_b64_tr_b16 v[78:79], v35 offset:6144\n"
      "v_mfma_f32_16x16x32_bf16 v[188:191], a[196:199], v[100:103], v[188:191]\n"
      "ds_read_b64_tr_b16 v[80:81], v33 offset:8192\n"
      "v_mfma_f32_16x16x32_bf16 v[188:191], a[208:211], v[108:111], v[188:191]\n"
      "ds_read_b64_tr_b16 v[82:83], v33 offset:10240\n"
      "v_mfma_f32_16x16x32_bf16 v[196:199], a[200:203], v[100:103], v[196:199]\n"
      "ds_read_b64_tr_b16 v[84:85], v35 offset:8192\n"
      "v_mfma_f32_16x16x32_bf16 v[196:199], a[212:215], v[108:111], v[196:199]\n"
      "ds_read_b64_tr_b16 v[86:87], v35 offset:10240\n"
      "v_mfma_f32_16x16x32_bf16 v[176:179], a[216:219], v[112:115], v[176:179]\n"
      "ds_read_b64_tr_b16 v[88:89], v33 offset:12288\n"
      "v_mfma_f32_16x16x32_bf16 v[176:179], a[228:231], v[120:123], v[176:179]\n"
      "ds_read_b64_tr_b16 v[90:91], v33 offset:14336\n"
      "v_mfma_f32_16x16x32_bf16 v[184:187], a[220:223], v[112:115], v[184:187]\n"
      "ds_read_b64_tr_b16 v[92:93], v35 offset:12288\n"
      "v_mfma_f32_16x16x32_bf16 v[184:187], a[232:235], v[120:123], v[184:187]\n"
      "ds_read_b64_tr_b16 v[94:95], v35 offset:14336\n"
      "v_mfma_f32_16x16x32_bf16 v[192:195], a[224:227], v[112:115], v[192:195]\n"
      "v_mfma_f32_16x16x32_bf16 v[192:195], a[236:239], v[120:123], v[192:195]\n"
      "v_mfma_f32_16x16x32_bf16 v[180:183], a[216:219], v[116:119], v[180:183]\n"
      "v_add_u32 v45, 4096, v7\n"
      "v_mfma_f32_16x16x32_bf16 v[180:183], a[228:231], v[124:127], v[180:183]\n"
      "v_mfma_f32_16x16x32_bf16 v[188:191], a[220:223], v[116:119], v[188:191]\n"
      "global_load_dwordx4 v[8:11], v45, s[10:11] offset:-4096 sc1\n"
      "v_mfma_f32_16x16x32_bf16 v[188:191], a[232:235], v[124:127], v[188:191]\n"
      "v_mfma_f32_16x16x32_bf16 v[196:199], a[224:227], v[116:119], v[196:199]\n"
      "global_load_dwordx4 v[12:15], v45, s[12:13] offset:-4096 sc1\n"
      "v_mfma_f32_16x16x32_bf16 v[196:199], a[236:239], v[124:127], v[196:199]\n"
      "s_waitcnt lgkmcnt(0)\n"
      "s_barrier\n"
      "v_mfma_f32_16x16x32_bf16 v[200:203], a[144:147], v[64:67], v[200:203]\n"
      "ds_read_b64_tr_b16 v[96:97], v33 offset:16384\n"
      "v_mfma_f32_16x16x32_bf16 v[200:203], a[156:159], v[72:75], v[200:203]\n"
      "global_load_dwordx4 v[16:19], v45, s[10:11] offset:-3072 sc1\n"
      "v_mfma_f32_16x16x32_bf16 v[208:211], a[148:151], v[64:67], v[208:211]\n"
      "ds_read_b64_tr_b16 v[98:99], v33 offset:18432\n"
      "v_mfma_f32_16x16x32_bf16 v[208:211], a[160:163], v[72:75], v[208:211]\n"
      "ds_read_b64_tr_b16 v[100:101], v35 offset:16384\n"
      "v_fmac_f32 v176, v24, v224\n"
      "v_fmac_f32 v177, v24, v225\n"
      "v_mfma_f32_16x16x32_bf16 v[216:219], a[152:155], v[64:67], v[216:219]\n"
      "global_load_dwordx4 v[20:23], v45, s[12:13] offset:-3072 sc1\n"
      "v_fmac_f32 v178, v24, v226\n"
      "v_fmac_f32 v179, v24, v227\n"
      "v_mfma_f32_16x16x32_bf16 v[216:219], a[164:167], v[72:75], v[216:219]\n"
      "ds_read_b64_tr_b16 v[102:103], v35 offset:18432\n"
      "v_fma_f32 v176, v176, v25, v236\n"
      "v_fma_f32 v177, v177, v25, v237\n"
      "v_fma_f32 v178, v178, v25, v238\n"
      "v_mfma_f32_16x16x32_bf16 v[204:207], a[144:147], v[68:71], v[204:207]\n"
      "ds_read_b64_tr_b16 v[104:105], v33 offset:20480\n"
      "v_fma_f32 v179, v179, v25, v239\n"
      "v_mul_f32 v176, v176, v178\n"
      "v_mfma_f32_16x16x32_bf16 v[204:207], a[156:159], v[76:79], v[204:207]\n"
      "global_load_dwordx4 v[128:131], v45, s[10:11] offset:-2048 sc1\n"
      "ds_read_b64_tr_b16 v[106:107], v33 offset:22528\n"
      "v_mul_f32 v177, v177, v179\n"
      "v_cvt_pk_bf16_f32 v176, v176, v177\n"
      "v_mfma_f32_16x16x32_bf16 v[212:215], a[148:151], v[68:71], v[212:215]\n"
      "ds_write_b32 v46, v176 offset:0\n"
      "ds_read_b64_tr_b16 v[108:109], v35 offset:20480\n"
      "v_fmac_f32 v184, v24, v228\n"
      "v_fmac_f32 v185, v24, v229\n"
      "v_mfma_f32_16x16x32_bf16 v[212:215], a[160:163], v[76:79], v[212:215]\n"
      "ds_read_b64_tr_b16 v[110:111], v35 offset:22528\n"
      "v_fmac_f32 v186, v24, v230\n"
      "v_fmac_f32 v187, v24, v231\n"
      "v_mfma_f32_16x16x32_bf16 v[220:223], a[152:155], v[68:71], v[220:223]\n"
      "global_load_dwordx4 v[132:135], v45, s[12:13] offset:-2048 sc1\n"
      "v_fma_f32 v184, v184, v25, v240\n"
      "v_fma_f32 v185, v185, v25, v241\n"
      "v_fma_f32 v186, v186, v25, v242\n"
      "v_mfma_f32_16x16x32_bf16 v[220:223], a[164:167], v[76:79], v[220:223]\n"
      "buffer_load_dword v36, v42, s[32:35], s63 offen offset:0 sc1\n"
      "buffer_load_dword v37, v42, s[32:35], s63 offen offset:256 sc1\n"
      "ds_read_b64_tr_b16 v[112:113], v33 offset:24576\n"
      "v_fma_f32 v187, v187, v25, v243\n"
      "v_mul_f32 v184, v184, v186\n"
      "v_mfma_f32_16x16x32_bf16 v[200:203], a[168:171], v[80:83], v[200:203]\n"
      "ds_read_b64_tr_b16 v[114:115], v33 offset:26624\n"
      "v_mul_f32 v185, v185, v187\n"
      "v_cvt_pk_bf16_f32 v184, v184, v185\n"
      "v_mfma_f32_16x16x32_bf16 v[200:203], a[180:183], v[88:91], v[200:203]\n"
      "global_load_dwordx4 v[136:139], v45, s[10:11] offset:-1024 sc1\n"
      "ds_write_b32 v46, v184 offset:4096\n"
      "ds_read_b64_tr_b16 v[116:117], v35 offset:24576\n"
      "v_fmac_f32 v192, v24, v232\n"
      "v_fmac_f32 v193, v24, v233\n"
      "v_mfma_f32_16x16x32_bf16 v[208:211], a[172:175], v[80:83], v[208:211]\n"
      "ds_read_b64_tr_b16 v[118:119], v35 offset:26624\n"
      "v_fmac_f32 v194, v24, v234\n"
      "v_fmac_f32 v195, v24, v235\n"
      "v_mfma_f32_16x16x32_bf16 v[208:211], a[184:187], v[88:91], v[208:211]\n"
      "ds_read_b64_tr_b16 v[120:121], v33 offset:28672\n"
      "v_fma_f32 v192, v192, v25, v244\n"
      "v_fma_f32 v193, v193, v25, v245\n"
      "v_fma_f32 v194, v194, v25, v246\n"
      "v_mfma_f32_16x16x32_bf16 v[216:219], a[176:179], v[80:83], v[216:219]\n"
      "global_load_dwordx4 v[140:143], v45, s[12:13] offset:-1024 sc1\n"
      "ds_read_b64_tr_b16 v[122:123], v33 offset:30720\n"
      "v_fma_f32 v195, v195, v25, v247\n"
      "v_mul_f32 v192, v192, v194\n"
      "v_mfma_f32_16x16x32_bf16 v[216:219], a[188:191], v[88:91], v[216:219]\n"
      "ds_read_b64_tr_b16 v[124:125], v35 offset:28672\n"
      "v_mul_f32 v193, v193, v195\n"
      "v_cvt_pk_bf16_f32 v192, v192, v193\n"
      "v_mfma_f32_16x16x32_bf16 v[204:207], a[168:171], v[84:87], v[204:207]\n"
      "ds_write_b32 v46, v192 offset:8192\n"
      "v_fmac_f32 v180, v26, v224\n"
      "v_fmac_f32 v181, v26, v225\n"
      "v_mfma_f32_16x16x32_bf16 v[204:207], a[180:183], v[92:95], v[204:207]\n"
      "global_load_dwordx4 v[144:147], v45, s[10:11] offset:0 sc1\n"
      "ds_read_b64_tr_b16 v[126:127], v35 offset:30720\n"
      "v_fmac_f32 v182, v26, v226\n"
      "v_fmac_f32 v183, v26, v227\n"
      "v_mfma_f32_16x16x32_bf16 v[212:215], a[172:175], v[84:87], v[212:215]\n"
      "v_fma_f32 v180, v180, v27, v236\n"
      "v_fma_f32 v181, v181, v27, v237\n"
      "v_fma_f32 v182, v182, v27, v238\n"
      "v_mfma_f32_16x16x32_bf16 v[212:215], a[184:187], v[92:95], v[212:215]\n"
      "v_fma_f32 v183, v183, v27, v239\n"
      "v_mul_f32 v180, v180, v182\n"
      "v_mfma_f32_16x16x32_bf16 v[220:223], a[176:179], v[84:87], v[220:223]\n"
      "global_load_dwordx4 v[148:151], v45, s[12:13] offset:0 sc1\n"
      "v_mul_f32 v181, v181, v183\n"
      "v_cvt_pk_bf16_f32 v180, v180, v181\n"
      "v_mfma_f32_16x16x32_bf16 v[220:223], a[188:191], v[92:95], v[220:223]\n"
      "ds_write_b32 v46, v180 offset:2048\n"
      "v_fmac_f32 v188, v26, v228\n"
      "v_fmac_f32 v189, v26, v229\n"
      "s_waitcnt lgkmcnt(1)\n"
      "v_mfma_f32_16x16x32_bf16 v[200:203], a[192:195], v[96:99], v[200:203]\n"
      "v_fmac_f32 v190, v26, v230\n"
      "v_fmac_f32 v191, v26, v231\n"
      "v_mfma_f32_16x16x32_bf16 v[200:203], a[204:207], v[104:107], v[200:203]\n"
      "global_load_dwordx4 v[152:155], v45, s[10:11] offset:1024 sc1\n"
      "v_fma_f32 v188, v188, v27, v240\n"
      "v_fma_f32 v189, v189, v27, v241\n"
      "v_fma_f32 v190, v190, v27, v242\n"
      "v_mfma_f32_16x16x32_bf16 v[208:211], a[196:199], v[96:99], v[208:211]\n"
      "v_fma_f32 v191, v191, v27, v243\n"
      "v_mul_f32 v188, v188, v190\n"
      "v_mfma_f32_16x16x32_bf16 v[208:211], a[208:211], v[104:107], v[208:211]\n"
      "v_mul_f32 v189, v189, v191\n"
      "v_cvt_pk_bf16_f32 v188, v188, v189\n"
      "v_mfma_f32_16x16x32_bf16 v[216:219], a[200:203], v[96:99], v[216:219]\n"
      "global_load_dwordx4 v[156:159], v45, s[12:13] offset:1024 sc1\n"
      "ds_write_b32 v46, v188 offset:6144\n"
      "v_fmac_f32 v196, v26, v232\n"
      "v_fmac_f32 v197, v26, v233\n"
      "v_mfma_f32_16x16x32_bf16 v[216:219], a[212:215], v[104:107], v[216:219]\n"
      "buffer_load_dword v38, v42, s[32:35], s63 offen offset:512 sc1\n"
      "buffer_load_dword v39, v42, s[32:35], s63 offen offset:768 sc1\n"
      "v_fmac_f32 v198, v26, v234\n"
      "v_fmac_f32 v199, v26, v235\n"
      "v_mfma_f32_16x16x32_bf16 v[204:207], a[192:195], v[100:103], v[204:207]\n"
      "v_fma_f32 v196, v196, v27, v244\n"
      "v_fma_f32 v197, v197, v27, v245\n"
      "v_fma_f32 v198, v198, v27, v246\n"
      "v_add_u32 v45, 10240, v7\n"
      "v_mfma_f32_16x16x32_bf16 v[204:207], a[204:207], v[108:111], v[204:207]\n"
      "global_load_dwordx4 v[172:175], v45, s[10:11] offset:-4096 sc1\n"
      "v_fma_f32 v199, v199, v27, v247\n"
      "v_mul_f32 v196, v196, v198\n"
      "v_mfma_f32_16x16x32_bf16 v[212:215], a[196:199], v[100:103], v[212:215]\n"
      "v_mul_f32 v197, v197, v199\n"
      "v_cvt_pk_bf16_f32 v196, v196, v197\n"
      "v_mfma_f32_16x16x32_bf16 v[212:215], a[208:211], v[108:111], v[212:215]\n"
      "ds_write_b32 v46, v196 offset:10240\n"
      "s_waitcnt vmcnt(37)\n"
      "v_mfma_f32_16x16x32_bf16 v[220:223], a[200:203], v[100:103], v[220:223]\n"
      "global_load_dwordx4 v[160:163], v45, s[12:13] offset:-4096 sc1\n"
      "v_mfma_f32_16x16x32_bf16 v[220:223], a[212:215], v[108:111], v[220:223]\n"
      "ds_read_b128 a[144:147], v57 offset:0\n"
      "s_waitcnt lgkmcnt(1)\n"
      "s_barrier\n"
      "v_mfma_f32_16x16x32_bf16 v[200:203], a[216:219], v[112:115], v[200:203]\n"
      "ds_read_b128 v[176:179], v47 offset:0\n"
      "v_mfma_f32_16x16x32_bf16 v[200:203], a[228:231], v[120:123], v[200:203]\n"
      "global_load_dwordx4 v[164:167], v45, s[10:11] offset:-3072 sc1\n"
      "ds_read_b128 v[180:183], v47 offset:4096\n"
      "v_mfma_f32_16x16x32_bf16 v[208:211], a[220:223], v[112:115], v[208:211]\n"
      "ds_read_b128 v[184:187], v47 offset:8192\n"
      "v_add_u32 v43, s59, v7\n"
      "v_mfma_f32_16x16x32_bf16 v[208:211], a[232:235], v[120:123], v[208:211]\n"
      "ds_read_b128 a[148:151], v57 offset:1024\n"
      "s_waitcnt vmcnt(11)\n"
      "v_add_u32 v59, s59, v42\n"
      "v_mfma_f32_16x16x32_bf16 v[216:219], a[224:227], v[112:115], v[216:219]\n"
      "global_load_dwordx4 v[168:171], v45, s[12:13] offset:-3072 sc1\n"
      "v_add3_u32 v2, v8, v9, s30\n"
      "v_add3_u32 v2, v2, v10, v11\n"
      "v_add3_u32 v2, v2, v16, v17\n"
      "v_mfma_f32_16x16x32_bf16 v[216:219], a[236:239], v[120:123], v[216:219]\n"
      "ds_read_b128 a[152:155], v57 offset:2048\n"
      "v_add3_u32 v2, v2, v18, v19\n"
      "v_add3_u32 v2, v2, v128, v129\n"
      "v_add3_u32 v2, v2, v130, v131\n"
      "v_mfma_f32_16x16x32_bf16 v[204:207], a[216:219], v[116:119], v[204:207]\n"
      "ds_read_b128 a[156:159], v57 offset:3072\n"
      "v_xor_b32 v2, v2, v36\n"
      "v_add3_u32 v3, v12, v13, s30\n"
      "v_add3_u32 v3, v3, v14, v15\n"
      "s_waitcnt lgkmcnt(3)\n"
      "v_mfma_f32_16x16x32_bf16 v[204:207], a[228:231], v[124:127], v[204:207]\n"
      "buffer_store_dwordx4 v[176:179], v43, s[32:35], s69 offen offset:0 sc1\n"
      "global_load_dwordx4 v[64:67], v45, s[10:11] offset:-2048 sc1\n"
      "v_add3_u32 v3, v3, v20, v21\n"
      "v_add3_u32 v3, v3, v22, v23\n"
      "v_add3_u32 v3, v3, v132, v133\n"
      "v_add3_u32 v58, v176, v177, s30\n"
      "v_mfma_f32_16x16x32_bf16 v[212:215], a[220:223], v[116:119], v[212:215]\n"
      "ds_read_b128 a[160:163], v57 offset:4096\n"
      "v_add3_u32 v3, v3, v134, v135\n"
      "v_xor_b32 v3, v3, v37\n"
      "v_or_b32 v2, v2, v3\n"
      "v_add3_u32 v58, v58, v178, v179\n"
      "v_add3_u32 v58, v58, v180, v181\n"
      "v_mfma_f32_16x16x32_bf16 v[212:215], a[232:235], v[124:127], v[212:215]\n"
      "buffer_store_dwordx4 v[180:183], v43, s[32:35], s69 offen offset:1024 sc1\n"
      "v_cmp_ne_u32 vcc, 0, v2\n"
      "v_add3_u32 v58, v58, v182, v183\n"
      "v_mfma_f32_16x16x32_bf16 v[220:223], a[224:227], v[116:119], v[220:223]\n"
      "global_load_dwordx4 v[68:71], v45, s[12:13] offset:-2048 sc1\n"
      "v_add3_u32 v58, v58, v184, v185\n"
      "v_add3_u32 v58, v58, v186, v187\n"
      "v_mfma_f32_16x16x32_bf16 v[220:223], a[236:239], v[124:127], v[220:223]\n"
      "buffer_store_dwordx4 v[184:187], v43, s[32:35], s69 offen offset:2048 sc1\n"
      "buffer_load_dword v60, v42, s[32:35], s63 offen offset:1024 sc1\n"
      "buffer_load_dword v61, v42, s[32:35], s63 offen offset:1280 sc1\n"
      "s_cbranch_vccnz L_shA0_%=\n"
      "L_bhA0_%=:\n"
      "v_mfma_f32_16x16x32_bf16 v[96:99], a[240:243], v[8:11], 0\n"
      "ds_read_b128 a[164:167], v57 offset:5120\n"
      "v_mfma_f32_16x16x32_bf16 v[96:99], a[4:7], v[16:19], v[96:99]\n"
      "buffer_store_dword v58, v59, s[32:35], s70 offen sc1\n"
      "global_load_dwordx4 v[72:75], v45, s[10:11] offset:-1024 sc1\n"
      "v_fmac_f32 v200, v28, v224\n"
      "v_mfma_f32_16x16x32_bf16 v[96:99], a[24:27], v[128:131], v[96:99]\n"
      "ds_read_b128 a[168:171], v57 offset:6144\n"
      "v_fmac_f32 v201, v28, v225\n"
      "v_fmac_f32 v202, v28, v226\n"
      "v_fmac_f32 v203, v28, v227\n"
      "v_fma_f32 v200, v200, v29, v236\n"
      "v_fma_f32 v201, v201, v29, v237\n"
      "v_fma_f32 v202, v202, v29, v238\n"
      "v_fma_f32 v203, v203, v29, v239\n"
      "v_mul_f32 v200, v200, v202\n"
      "v_mul_f32 v201, v201, v203\n"
      "v_cvt_pk_bf16_f32 v200, v200, v201\n"
      "v_mfma_f32_16x16x32_bf16 v[104:107], a[244:247], v[8:11], 0\n"
      "ds_write_b32 v46, v200 offset:1024\n"
      "v_fmac_f32 v208, v28, v228\n"
      "v_fmac_f32 v209, v28, v229\n"
      "v_fmac_f32 v210, v28, v230\n"
      "v_fmac_f32 v211, v28, v231\n"
      "v_fma_f32 v208, v208, v29, v240\n"
      "v_fma_f32 v209, v209, v29, v241\n"
      "v_fma_f32 v210, v210, v29, v242\n"
      "v_fma_f32 v211, v211, v29, v243\n"
      "v_mul_f32 v208, v208, v210\n"
      "v_mul_f32 v209, v209, v211\n"
      "v_cvt_pk_bf16_f32 v208, v208, v209\n"
      "v_mfma_f32_16x16x32_bf16 v[104:107], a[8:11], v[16:19], v[104:107]\n"
      "global_load_dwordx4 v[76:79], v45, s[12:13] offset:-1024 sc1\n"
      "ds_write_b32 v46, v208 offset:5120\n"
      "ds_read_b128 a[172:175], v57 offset:7168\n"
      "v_fmac_f32 v216, v28, v232\n"
      "v_mfma_f32_16x16x32_bf16 v[104:107], a[28:31], v[128:131], v[104:107]\n"
      "ds_read_b128 a[176:179], v57 offset:8192\n"
      "v_fmac_f32 v217, v28, v233\n"
      "v_fmac_f32 v218, v28, v234\n"
      "v_fmac_f32 v219, v28, v235\n"
      "v_fma_f32 v216, v216, v29, v244\n"
      "v_fma_f32 v217, v217, v29, v245\n"
      "v_fma_f32 v218, v218, v29, v246\n"
      "v_fma_f32 v219, v219, v29, v247\n"
      "v_mul_f32 v216, v216, v218\n"
      "v_mul_f32 v217, v217, v219\n"
      "v_cvt_pk_bf16_f32 v216, v216, v217\n"
      "v_mfma_f32_16x16x32_bf16 v[112:115], a[248:251], v[8:11], 0\n"
      "ds_write_b32 v46, v216 offset:9216\n"
      "v_fmac_f32 v204, v30, v224\n"
      "v_fmac_f32 v205, v30, v225\n"
      "v_fmac_f32 v206, v30, v226\n"
      "v_fmac_f32 v207, v30, v227\n"
      "v_fma_f32 v204, v204, v31, v236\n"
      "v_fma_f32 v205, v205, v31, v237\n"
      "v_fma_f32 v206, v206, v31, v238\n"
      "v_fma_f32 v207, v207, v31, v239\n"
      "v_mul_f32 v204, v204, v206\n"
      "v_mul_f32 v205, v205, v207\n"
      "v_cvt_pk_bf16_f32 v204, v204, v205\n"
      "v_mfma_f32_16x16x32_bf16 v[112:115], a[12:15], v[16:19], v[112:115]\n"
      "global_load_dwordx4 v[80:83], v45, s[10:11] offset:0 sc1\n"
      "ds_write_b32 v46, v204 offset:3072\n"
      "v_fmac_f32 v212, v30, v228\n"
      "v_fmac_f32 v213, v30, v229\n"
      "v_mfma_f32_16x16x32_bf16 v[112:115], a[32:35], v[128:131], v[112:115]\n"
      "v_fmac_f32 v214, v30, v230\n"
      "v_fmac_f32 v215, v30, v231\n"
      "v_fma_f32 v212, v212, v31, v240\n"
      "v_fma_f32 v213, v213, v31, v241\n"
      "v_fma_f32 v214, v214, v31, v242\n"
      "v_fma_f32 v215, v215, v31, v243\n"
      "v_mul_f32 v212, v212, v214\n"
      "v_mul_f32 v213, v213, v215\n"
      "v_cvt_pk_bf16_f32 v212, v212, v213\n"
      "v_mfma_f32_16x16x32_bf16 v[120:123], a[252:255], v[8:11], 0\n"
      "ds_write_b32 v46, v212 offset:7168\n"
      "v_fmac_f32 v220, v30, v232\n"
      "v_fmac_f32 v221, v30, v233\n"
      "v_fmac_f32 v222, v30, v234\n"
      "v_fmac_f32 v223, v30, v235\n"
      "v_mfma_f32_16x16x32_bf16 v[120:123], a[16:19], v[16:19], v[120:123]\n"
      "global_load_dwordx4 v[84:87], v45, s[12:13] offset:0 sc1\n"
      "v_fma_f32 v220, v220, v31, v244\n"
      "v_fma_f32 v221, v221, v31, v245\n"
      "L_pg6_%=:\n"
      "s_cbranch_execz L_pgr_%=\n"
      "v_fma_f32 v222, v222, v31, v246\n"
      "v_fma_f32 v223, v223, v31, v247\n"
      "v_mul_f32 v220, v220, v222\n"
      "v_mul_f32 v221, v221, v223\n"
      "v_cvt_pk_bf16_f32 v220, v220, v221\n"
      "v_mfma_f32_16x16x32_bf16 v[120:123], a[36:39], v[128:131], v[120:123]\n"
      "ds_write_b32 v46, v220 offset:11264\n"
      "v_add_u32 v57, s41, v7\n"
      "v_mfma_f32_16x16x32_bf16 v[248:251], a[0:3], v[8:11], 0\n"
      "ds_read_b128 a[180:183], v57 offset:0\n"
      "v_mfma_f32_16x16x32_bf16 v[248:251], a[20:23], v[16:19], v[248:251]\n"
      "global_load_dwordx4 v[88:91], v45, s[10:11] offset:1024 sc1\n"
      "v_mfma_f32_16x16x32_bf16 v[248:251], a[40:43], v[128:131], v[248:251]\n"
      "ds_read_b128 a[184:187], v57 offset:1024\n"
      "s_waitcnt lgkmcnt(2)\n"
      "s_barrier\n"
      "v_mfma_f32_16x16x32_bf16 v[100:103], a[240:243], v[12:15], 0\n"
      "ds_read_b128 v[200:203], v47 offset:0\n"
      "v_mfma_f32_16x16x32_bf16 v[100:103], a[4:7], v[20:23], v[100:103]\n"
      "global_load_dwordx4 v[92:95], v45, s[12:13] offset:1024 sc1\n"
      "ds_read_b128 v[204:207], v47 offset:4096\n"
      "v_mfma_f32_16x16x32_bf16 v[100:103], a[24:27], v[132:135], v[100:103]\n"
      "buffer_load_dword v62, v42, s[32:35], s63 offen offset:1536 sc1\n"
      "buffer_load_dword v63, v42, s[32:35], s63 offen offset:1792 sc1\n"
      "ds_read_b128 v[208:211], v47 offset:8192\n"
      "v_xor_b32 v43, 0x80000000, v43\n"
      "v_mfma_f32_16x16x32_bf16 v[108:111], a[244:247], v[12:15], 0\n"
      "ds_read_b128 a[188:191], v57 offset:2048\n"
      "v_xor_b32 v59, 0x80000000, v59\n"
      "v_mfma_f32_16x16x32_bf16 v[108:111], a[8:11], v[20:23], v[108:111]\n"
      "v_mfma_f32_16x16x32_bf16 v[108:111], a[28:31], v[132:135], v[108:111]\n"
      "v_mfma_f32_16x16x32_bf16 v[116:119], a[248:251], v[12:15], 0\n"
      "s_waitcnt vmcnt(20)\n"
      "s_waitcnt lgkmcnt(1)\n"
      "v_mfma_f32_16x16x32_bf16 v[116:119], a[12:15], v[20:23], v[116:119]\n"
      "buffer_store_dwordx4 v[200:203], v43, s[32:35], s69 offen offset:0 sc1\n"
      "v_add3_u32 v2, v136, v137, s30\n"
      "v_add3_u32 v2, v2, v138, v139\n"
      "v_add3_u32 v2, v2, v144, v145\n"
      "v_add3_u32 v58, v200, v201, s30\n"
      "v_mfma_f32_16x16x32_bf16 v[116:119], a[32:35], v[132:135], v[116:119]\n"
      "v_add3_u32 v2, v2, v146, v147\n"
      "v_add3_u32 v2, v2, v152, v153\n"
      "v_add3_u32 v2, v2, v154, v155\n"
      "v_add3_u32 v58, v58, v202, v203\n"
      "v_add3_u32 v58, v58, v204, v205\n"
      "v_mfma_f32_16x16x32_bf16 v[124:127], a[252:255], v[12:15], 0\n"
      "buffer_store_dwordx4 v[204:207], v43, s[32:35], s69 offen offset:1024 sc1\n"
      "v_xor_b32 v2, v2, v38\n"
      "v_add3_u32 v3, v140, v141, s30\n"
      "v_add3_u32 v3, v3, v142, v143\n"
      "v_add3_u32 v58, v58, v206, v207\n"
      "v_mfma_f32_16x16x32_bf16 v[124:127], a[16:19], v[20:23], v[124:127]\n"
      "v_add3_u32 v3, v3, v148, v149\n"
      "v_add3_u32 v3, v3, v150, v151\n"
      "v_add3_u32 v3, v3, v156, v157\n"
      "v_add3_u32 v58, v58, v208, v209\n"
      "v_add3_u32 v58, v58, v210, v211\n"
      "v_mfma_f32_16x16x32_bf16 v[124:127], a[36:39], v[132:135], v[124:127]\n"
      "buffer_store_dwordx4 v[208:211], v43, s[32:35], s69 offen offset:2048 sc1\n"
      "v_add3_u32 v3, v3, v158, v159\n"
      "v_xor_b32 v3, v3, v39\n"
      "v_or_b32 v2, v2, v3\n"
      "v_mfma_f32_16x16x32_bf16 v[252:255], a[0:3], v[12:15], 0\n"
      "v_cmp_ne_u32 vcc, 0, v2\n"
      "v_mfma_f32_16x16x32_bf16 v[252:255], a[20:23], v[20:23], v[252:255]\n"
      "buffer_store_dword v58, v59, s[32:35], s70 offen sc1\n"
      "v_mfma_f32_16x16x32_bf16 v[252:255], a[40:43], v[132:135], v[252:255]\n"
      "s_cbranch_vccnz L_shA1_%=\n"
      "L_bhA1_%=:\n"
      "v_mfma_f32_16x16x32_bf16 v[96:99], a[44:47], v[136:139], v[96:99]\n"
      "s_add_u32 s43, s73, s97\n"
      "v_add_u32 v56, s43, v7\n"
      "v_mfma_f32_16x16x32_bf16 v[96:99], a[64:67], v[144:147], v[96:99]\n"
      "s_and_b32 s52, s60, 1\n"
      "s_lshr_b32 s53, s60, 1\n"
      "v_mfma_f32_16x16x32_bf16 v[96:99], a[84:87], v[152:155], v[96:99]\n"
      "v_add_u32 v44, 53248, v7\n"
      "s_mul_i32 s54, s52, 20480\n"
      "v_mfma_f32_16x16x32_bf16 v[104:107], a[48:51], v[136:139], v[104:107]\n"
      "s_lshl_b32 s53, s53, 11\n"
      "s_add_u32 s54, s54, s53\n"
      "v_mfma_f32_16x16x32_bf16 v[104:107], a[68:71], v[144:147], v[104:107]\n"
      "global_load_dwordx4 a[192:195], v44, s[6:7] offset:0 sc1\n"
      "v_add_u32 v49, s54, v7\n"
      "v_mfma_f32_16x16x32_bf16 v[104:107], a[88:91], v[152:155], v[104:107]\n"
      "s_lshl_b32 s52, s52, 5\n"
      "v_xor_b32 v53, s52, v6\n"
      "v_mfma_f32_16x16x32_bf16 v[112:115], a[52:55], v[136:139], v[112:115]\n"
      "s_lshl_b32 s52, s53, 1\n"
      "s_add_u32 s52, s52, 110592\n"
      "v_mfma_f32_16x16x32_bf16 v[112:115], a[72:75], v[144:147], v[112:115]\n"
      "global_load_dwordx4 a[196:199], v44, s[6:7] offset:1024 sc1\n"
      "v_add_u32 v53, s52, v53\n"
      "v_mfma_f32_16x16x32_bf16 v[112:115], a[92:95], v[152:155], v[112:115]\n"
      "s_lshr_b32 s53, s53, 4\n"
      "s_add_u32 s53, s53, 163328\n"
      "v_mfma_f32_16x16x32_bf16 v[120:123], a[56:59], v[136:139], v[120:123]\n"
      "v_lshrrev_b32 v48, 4, v7\n"
      "v_and_b32 v50, 0x30, v48\n"
      "v_mfma_f32_16x16x32_bf16 v[120:123], a[76:79], v[144:147], v[120:123]\n"
      "global_load_dwordx4 a[200:203], v44, s[6:7] offset:2048 sc1\n"
      "v_add_u32 v54, s53, v50\n"
      "v_mfma_f32_16x16x32_bf16 v[120:123], a[96:99], v[152:155], v[120:123]\n"
      "v_and_b32 v48, 15, v48\n"
      "v_lshrrev_b32 v50, 1, v50\n"
      "v_mfma_f32_16x16x32_bf16 v[248:251], a[60:63], v[136:139], v[248:251]\n"
      "v_mul_u32_u24 v48, 1280, v48\n"
      "v_add3_u32 v55, v48, v50, s74\n"
      "v_mfma_f32_16x16x32_bf16 v[248:251], a[80:83], v[144:147], v[248:251]\n"
      "global_load_dwordx4 a[204:207], v44, s[6:7] offset:3072 sc1\n"
      "v_mfma_f32_16x16x32_bf16 v[248:251], a[100:103], v[152:155], v[248:251]\n"
      "v_mfma_f32_16x16x32_bf16 v[100:103], a[44:47], v[140:143], v[100:103]\n"
      "v_add_u32 v44, 61440, v7\n"
      "v_mfma_f32_16x16x32_bf16 v[100:103], a[64:67], v[148:151], v[100:103]\n"
      "global_load_dwordx4 a[208:211], v44, s[6:7] offset:-4096 sc1\n"
      "v_mfma_f32_16x16x32_bf16 v[100:103], a[84:87], v[156:159], v[100:103]\n"
      "v_mfma_f32_16x16x32_bf16 v[108:111], a[48:51], v[140:143], v[108:111]\n"
      "v_mfma_f32_16x16x32_bf16 v[108:111], a[68:71], v[148:151], v[108:111]\n"
      "global_load_dwordx4 a[212:215], v44, s[6:7] offset:-3072 sc1\n"
      "v_mfma_f32_16x16x32_bf16 v[108:111], a[88:91], v[156:159], v[108:111]\n"
      "v_mfma_f32_16x16x32_bf16 v[116:119], a[52:55], v[140:143], v[116:119]\n"
      "s_waitcnt vmcnt(19)\n"
      "s_waitcnt lgkmcnt(0)\n"
      "v_mfma_f32_16x16x32_bf16 v[116:119], a[72:75], v[148:151], v[116:119]\n"
      "global_load_dwordx4 a[216:219], v44, s[6:7] offset:-2048 sc1\n"
      "v_add3_u32 v2, v172, v173, s30\n"
      "v_add3_u32 v2, v2, v174, v175\n"
      "v_add3_u32 v2, v2, v164, v165\n"
      "v_mfma_f32_16x16x32_bf16 v[116:119], a[92:95], v[156:159], v[116:119]\n"
      "v_add3_u32 v2, v2, v166, v167\n"
      "v_add3_u32 v2, v2, v64, v65\n"
      "v_add3_u32 v2, v2, v66, v67\n"
      "v_mfma_f32_16x16x32_bf16 v[124:127], a[56:59], v[140:143], v[124:127]\n"
      "v_xor_b32 v2, v2, v60\n"
      "v_add3_u32 v3, v160, v161, s30\n"
      "v_add3_u32 v3, v3, v162, v163\n"
      "v_mfma_f32_16x16x32_bf16 v[124:127], a[76:79], v[148:151], v[124:127]\n"
      "global_load_dwordx4 a[220:223], v44, s[6:7] offset:-1024 sc1\n"
      "v_add3_u32 v3, v3, v168, v169\n"
      "v_add3_u32 v3, v3, v170, v171\n"
      "v_add3_u32 v3, v3, v68, v69\n"
      "v_mfma_f32_16x16x32_bf16 v[124:127], a[96:99], v[156:159], v[124:127]\n"
      "v_add3_u32 v3, v3, v70, v71\n"
      "v_xor_b32 v3, v3, v61\n"
      "v_or_b32 v2, v2, v3\n"
      "v_mfma_f32_16x16x32_bf16 v[252:255], a[60:63], v[140:143], v[252:255]\n"
      "v_cmp_ne_u32 vcc, 0, v2\n"
      "v_mfma_f32_16x16x32_bf16 v[252:255], a[80:83], v[148:151], v[252:255]\n"
      "v_mfma_f32_16x16x32_bf16 v[252:255], a[100:103], v[156:159], v[252:255]\n"
      "s_cbranch_vccnz L_shA2_%=\n"
      "L_bhA2_%=:\n"
      "v_mfma_f32_16x16x32_bf16 v[96:99], a[104:107], v[172:175], v[96:99]\n"
      "v_mfma_f32_16x16x32_bf16 v[96:99], a[124:127], v[164:167], v[96:99]\n"
      "v_mfma_f32_16x16x32_bf16 v[96:99], a[144:147], v[64:67], v[96:99]\n"
      "v_mfma_f32_16x16x32_bf16 v[104:107], a[108:111], v[172:175], v[104:107]\n"
      "v_mfma_f32_16x16x32_bf16 v[104:107], a[128:131], v[164:167], v[104:107]\n"
      "v_add_u32 v45, 102400, v7\n"
      "v_mfma_f32_16x16x32_bf16 v[104:107], a[148:151], v[64:67], v[104:107]\n"
      "v_mfma_f32_16x16x32_bf16 v[112:115], a[112:115], v[172:175], v[112:115]\n"
      "global_load_dwordx4 v[224:227], v45, s[10:11] offset:-4096 sc1\n"
      "v_mfma_f32_16x16x32_bf16 v[112:115], a[132:135], v[164:167], v[112:115]\n"
      "v_mfma_f32_16x16x32_bf16 v[112:115], a[152:155], v[64:67], v[112:115]\n"
      "v_mfma_f32_16x16x32_bf16 v[120:123], a[116:119], v[172:175], v[120:123]\n"
      "v_mfma_f32_16x16x32_bf16 v[120:123], a[136:139], v[164:167], v[120:123]\n"
      "global_load_dwordx4 v[228:231], v45, s[10:11] offset:-3072 sc1\n"
      "v_mfma_f32_16x16x32_bf16 v[120:123], a[156:159], v[64:67], v[120:123]\n"
      "v_mfma_f32_16x16x32_bf16 v[248:251], a[120:123], v[172:175], v[248:251]\n"
      "v_mfma_f32_16x16x32_bf16 v[248:251], a[140:143], v[164:167], v[248:251]\n"
      "v_mfma_f32_16x16x32_bf16 v[248:251], a[160:163], v[64:67], v[248:251]\n"
      "global_load_dwordx4 v[232:235], v45, s[10:11] offset:-2048 sc1\n"
      "v_mfma_f32_16x16x32_bf16 v[100:103], a[104:107], v[160:163], v[100:103]\n"
      "v_mfma_f32_16x16x32_bf16 v[100:103], a[124:127], v[168:171], v[100:103]\n"
      "v_mfma_f32_16x16x32_bf16 v[100:103], a[144:147], v[68:71], v[100:103]\n"
      "global_load_dwordx4 v[236:239], v45, s[10:11] offset:-1024 sc1\n"
      "v_mfma_f32_16x16x32_bf16 v[108:111], a[108:111], v[160:163], v[108:111]\n"
      "v_mfma_f32_16x16x32_bf16 v[108:111], a[128:131], v[168:171], v[108:111]\n"
      "v_mfma_f32_16x16x32_bf16 v[108:111], a[148:151], v[68:71], v[108:111]\n"
      "global_load_dwordx4 v[240:243], v45, s[10:11] offset:0 sc1\n"
      "v_mfma_f32_16x16x32_bf16 v[116:119], a[112:115], v[160:163], v[116:119]\n"
      "s_waitcnt vmcnt(17)\n"
      "v_add3_u32 v2, v72, v73, s30\n"
      "v_mfma_f32_16x16x32_bf16 v[116:119], a[132:135], v[168:171], v[116:119]\n"
      "v_add3_u32 v2, v2, v74, v75\n"
      "v_add3_u32 v2, v2, v80, v81\n"
      "v_mfma_f32_16x16x32_bf16 v[116:119], a[152:155], v[68:71], v[116:119]\n"
      "global_load_dwordx4 v[244:247], v45, s[10:11] offset:1024 sc1\n"
      "v_add3_u32 v2, v2, v82, v83\n"
      "v_add3_u32 v2, v2, v88, v89\n"
      "v_add3_u32 v2, v2, v90, v91\n"
      "v_mfma_f32_16x16x32_bf16 v[124:127], a[116:119], v[160:163], v[124:127]\n"
      "buffer_load_dword v32, v42, s[32:35], s63 offen offset:2048 sc1\n"
      "buffer_load_dword v33, v42, s[32:35], s63 offen offset:2304 sc1\n"
      "v_xor_b32 v2, v2, v62\n"
      "v_add3_u32 v3, v76, v77, s30\n"
      "v_add3_u32 v3, v3, v78, v79\n"
      "v_mfma_f32_16x16x32_bf16 v[124:127], a[136:139], v[168:171], v[124:127]\n"
      "v_add3_u32 v3, v3, v84, v85\n"
      "v_add3_u32 v3, v3, v86, v87\n"
      "v_add3_u32 v3, v3, v92, v93\n"
      "v_mfma_f32_16x16x32_bf16 v[124:127], a[156:159], v[68:71], v[124:127]\n"
      "v_add3_u32 v3, v3, v94, v95\n"
      "v_xor_b32 v3, v3, v63\n"
      "v_or_b32 v2, v2, v3\n"
      "v_add_u32 v45, 108544, v7\n"
      "v_mfma_f32_16x16x32_bf16 v[252:255], a[120:123], v[160:163], v[252:255]\n"
      "global_load_dwordx4 v[8:11], v45, s[10:11] offset:-4096 sc1\n"
      "v_cmp_ne_u32 vcc, 0, v2\n"
      "v_mfma_f32_16x16x32_bf16 v[252:255], a[140:143], v[168:171], v[252:255]\n"
      "v_mfma_f32_16x16x32_bf16 v[252:255], a[160:163], v[68:71], v[252:255]\n"
      "s_cbranch_vccnz L_shA3_%=\n"
      "L_bhA3_%=:\n"
      "v_mfma_f32_16x16x32_bf16 v[96:99], a[164:167], v[72:75], v[96:99]\n"
      "global_load_dwordx4 v[12:15], v45, s[10:11] offset:-3072 sc1\n"
      "v_mfma_f32_16x16x32_bf16 v[104:107], a[168:171], v[72:75], v[104:107]\n"
      "v_mfma_f32_16x16x32_bf16 v[112:115], a[172:175], v[72:75], v[112:115]\n"
      "v_mfma_f32_16x16x32_bf16 v[120:123], a[176:179], v[72:75], v[120:123]\n"
      "global_load_dwordx4 v[16:19], v45, s[10:11] offset:-2048 sc1\n"
      "v_mfma_f32_16x16x32_bf16 v[248:251], a[180:183], v[72:75], v[248:251]\n"
      "v_mfma_f32_16x16x32_bf16 v[100:103], a[164:167], v[76:79], v[100:103]\n"
      "v_mfma_f32_16x16x32_bf16 v[108:111], a[168:171], v[76:79], v[108:111]\n"
      "global_load_dwordx4 v[20:23], v45, s[10:11] offset:-1024 sc1\n"
      "v_mfma_f32_16x16x32_bf16 v[116:119], a[172:175], v[76:79], v[116:119]\n"
      "v_mfma_f32_16x16x32_bf16 v[124:127], a[176:179], v[76:79], v[124:127]\n"
      "v_mfma_f32_16x16x32_bf16 v[252:255], a[180:183], v[76:79], v[252:255]\n"
      "global_load_dwordx4 v[128:131], v45, s[10:11] offset:0 sc1\n"
      "v_mfma_f32_16x16x32_bf16 v[96:99], a[184:187], v[80:83], v[96:99]\n"
      "v_mfma_f32_16x16x32_bf16 v[104:107], a[188:191], v[80:83], v[104:107]\n"
      "s_waitcnt vmcnt(13)\n"
      "v_mfma_f32_16x16x32_bf16 v[112:115], a[192:195], v[80:83], v[112:115]\n"
      "global_load_dwordx4 v[132:135], v45, s[10:11] offset:1024 sc1\n"
      "v_mfma_f32_16x16x32_bf16 v[120:123], a[196:199], v[80:83], v[120:123]\n"
      "buffer_load_dword v34, v42, s[32:35], s63 offen offset:2560 sc1\n"
      "buffer_load_dword v35, v42, s[32:35], s63 offen offset:2816 sc1\n"
      "v_mfma_f32_16x16x32_bf16 v[248:251], a[200:203], v[80:83], v[248:251]\n"
      "v_mfma_f32_16x16x32_bf16 v[100:103], a[184:187], v[84:87], v[100:103]\n"
      "v_mfma_f32_16x16x32_bf16 v[108:111], a[188:191], v[84:87], v[108:111]\n"
      "v_add_u32 v45, 102400, v7\n"
      "v_mfma_f32_16x16x32_bf16 v[116:119], a[192:195], v[84:87], v[116:119]\n"
      "v_mfma_f32_16x16x32_bf16 v[124:127], a[196:199], v[84:87], v[124:127]\n"
      "global_load_dwordx4 v[136:139], v45, s[12:13] offset:-4096 sc1\n"
      "v_mfma_f32_16x16x32_bf16 v[252:255], a[200:203], v[84:87], v[252:255]\n"
      "v_mfma_f32_16x16x32_bf16 v[96:99], a[204:207], v[88:91], v[96:99]\n"
      "v_mfma_f32_16x16x32_bf16 v[104:107], a[208:211], v[88:91], v[104:107]\n"
      "s_waitcnt vmcnt(9)\n"
      "v_add3_u32 v2, v224, v225, s30\n"
      "v_mfma_f32_16x16x32_bf16 v[112:115], a[212:215], v[88:91], v[112:115]\n"
      "global_load_dwordx4 v[140:143], v45, s[12:13] offset:-3072 sc1\n"
      "v_add3_u32 v2, v2, v226, v227\n"
      "v_add3_u32 v2, v2, v228, v229\n"
      "v_mfma_f32_16x16x32_bf16 v[120:123], a[216:219], v[88:91], v[120:123]\n"
      "v_add3_u32 v2, v2, v230, v231\n"
      "v_add3_u32 v2, v2, v232, v233\n"
      "v_add3_u32 v2, v2, v234, v235\n"
      "v_mfma_f32_16x16x32_bf16 v[248:251], a[220:223], v[88:91], v[248:251]\n"
      "v_xor_b32 v2, v2, v32\n"
      "v_add3_u32 v3, v236, v237, s30\n"
      "v_add3_u32 v3, v3, v238, v239\n"
      "v_mfma_f32_16x16x32_bf16 v[100:103], a[204:207], v[92:95], v[100:103]\n"
      "v_add3_u32 v3, v3, v240, v241\n"
      "v_add3_u32 v3, v3, v242, v243\n"
      "v_add3_u32 v3, v3, v244, v245\n"
      "v_mfma_f32_16x16x32_bf16 v[108:111], a[208:211], v[92:95], v[108:111]\n"
      "global_load_dwordx4 v[144:147], v45, s[12:13] offset:-2048 sc1\n"
      "v_add3_u32 v3, v3, v246, v247\n"
      "v_xor_b32 v3, v3, v33\n"
      "v_or_b32 v2, v2, v3\n"
      "v_mfma_f32_16x16x32_bf16 v[116:119], a[212:215], v[92:95], v[116:119]\n"
      "v_cmp_ne_u32 vcc, 0, v2\n"
      "v_mfma_f32_16x16x32_bf16 v[124:127], a[216:219], v[92:95], v[124:127]\n"
      "v_mfma_f32_16x16x32_bf16 v[252:255], a[220:223], v[92:95], v[252:255]\n"
      "s_cbranch_vccnz L_shB0_%=\n"
      "L_bhB0_%=:\n"
      "v_mfma_f32_16x16x32_bf16 v[176:179], a[240:243], v[224:227], 0\n"
      "global_load_dwordx4 v[148:151], v45, s[12:13] offset:-1024 sc1\n"
      "v_mfma_f32_16x16x32_bf16 v[176:179], a[4:7], v[228:231], v[176:179]\n"
      "ds_read_b128 v[80:83], v54 offset:0\n"
      "v_mfma_f32_16x16x32_bf16 v[176:179], a[24:27], v[232:235], v[176:179]\n"
      "ds_read_b128 v[84:87], v54 offset:64\n"
      "v_mfma_f32_16x16x32_bf16 v[180:183], a[244:247], v[224:227], 0\n"
      "ds_write_b128 v56, v[96:99] offset:0\n"
      "ds_read_b128 v[88:91], v54 offset:128\n"
      "v_mfma_f32_16x16x32_bf16 v[180:183], a[8:11], v[228:231], v[180:183]\n"
      "global_load_dwordx4 v[152:155], v45, s[12:13] offset:0 sc1\n"
      "v_mfma_f32_16x16x32_bf16 v[180:183], a[28:31], v[232:235], v[180:183]\n"
      "v_mfma_f32_16x16x32_bf16 v[184:187], a[248:251], v[224:227], 0\n"
      "ds_write_b128 v56, v[100:103] offset:20480\n"
      "v_mfma_f32_16x16x32_bf16 v[184:187], a[12:15], v[228:231], v[184:187]\n"
      "v_mfma_f32_16x16x32_bf16 v[184:187], a[32:35], v[232:235], v[184:187]\n"
      "global_load_dwordx4 v[156:159], v45, s[12:13] offset:1024 sc1\n"
      "v_mfma_f32_16x16x32_bf16 v[188:191], a[252:255], v[224:227], 0\n"
      "ds_write_b128 v56, v[104:107] offset:1024\n"
      "v_mfma_f32_16x16x32_bf16 v[188:191], a[16:19], v[228:231], v[188:191]\n"
      "v_mfma_f32_16x16x32_bf16 v[188:191], a[36:39], v[232:235], v[188:191]\n"
      "v_mfma_f32_16x16x32_bf16 v[192:195], a[0:3], v[224:227], 0\n"
      "buffer_load_dword v24, v42, s[32:35], s63 offen offset:3072 sc1\n"
      "buffer_load_dword v25, v42, s[32:35], s63 offen offset:3328 sc1\n"
      "v_add_u32 v45, 108544, v7\n"
      "v_mfma_f32_16x16x32_bf16 v[192:195], a[20:23], v[228:231], v[192:195]\n"
      "ds_write_b128 v56, v[108:111] offset:21504\n"
      "v_mfma_f32_16x16x32_bf16 v[192:195], a[40:43], v[232:235], v[192:195]\n"
      "global_load_dwordx4 v[172:175], v45, s[12:13] offset:-4096 sc1\n"
      "v_mfma_f32_16x16x32_bf16 v[176:179], a[44:47], v[236:239], v[176:179]\n"
      "v_mfma_f32_16x16x32_bf16 v[176:179], a[64:67], v[240:243], v[176:179]\n"
      "ds_write_b128 v56, v[112:115] offset:2048\n"
      "v_mfma_f32_16x16x32_bf16 v[176:179], a[84:87], v[244:247], v[176:179]\n"
      "v_mfma_f32_16x16x32_bf16 v[180:183], a[48:51], v[236:239], v[180:183]\n"
      "global_load_dwordx4 v[160:163], v45, s[12:13] offset:-3072 sc1\n"
      "v_mfma_f32_16x16x32_bf16 v[180:183], a[68:71], v[240:243], v[180:183]\n"
      "ds_write_b128 v56, v[116:119] offset:22528\n"
      "v_mfma_f32_16x16x32_bf16 v[180:183], a[88:91], v[244:247], v[180:183]\n"
      "v_mfma_f32_16x16x32_bf16 v[184:187], a[52:55], v[236:239], v[184:187]\n"
      "s_waitcnt vmcnt(10)\n"
      "v_add3_u32 v2, v8, v9, s30\n"
      "v_mfma_f32_16x16x32_bf16 v[184:187], a[72:75], v[240:243], v[184:187]\n"
      "global_load_dwordx4 v[164:167], v45, s[12:13] offset:-2048 sc1\n"
      "v_add3_u32 v2, v2, v10, v11\n"
      "v_add3_u32 v2, v2, v12, v13\n"
      "v_mfma_f32_16x16x32_bf16 v[184:187], a[92:95], v[244:247], v[184:187]\n"
      "ds_write_b128 v56, v[120:123] offset:3072\n"
      "v_add3_u32 v2, v2, v14, v15\n"
      "v_add3_u32 v2, v2, v16, v17\n"
      "v_add3_u32 v2, v2, v18, v19\n"
      "v_xor_b32 v2, v2, v34\n"
      "v_mfma_f32_16x16x32_bf16 v[188:191], a[56:59], v[236:239], v[188:191]\n"
      "v_add3_u32 v3, v20, v21, s30\n"
      "v_add3_u32 v3, v3, v22, v23\n"
      "v_mfma_f32_16x16x32_bf16 v[188:191], a[76:79], v[240:243], v[188:191]\n"
      "v_add3_u32 v3, v3, v128, v129\n"
      "v_add3_u32 v3, v3, v130, v131\n"
      "v_add3_u32 v3, v3, v132, v133\n"
      "v_mfma_f32_16x16x32_bf16 v[188:191], a[96:99], v[244:247], v[188:191]\n"
      "global_load_dwordx4 v[168:171], v45, s[12:13] offset:-1024 sc1\n"
      "v_add3_u32 v3, v3, v134, v135\n"
      "v_xor_b32 v3, v3, v35\n"
      "v_or_b32 v2, v2, v3\n"
      "v_mfma_f32_16x16x32_bf16 v[192:195], a[60:63], v[236:239], v[192:195]\n"
      "ds_write_b128 v56, v[124:127] offset:23552\n"
      "v_cmp_ne_u32 vcc, 0, v2\n"
      "v_mfma_f32_16x16x32_bf16 v[192:195], a[80:83], v[240:243], v[192:195]\n"
      "v_mfma_f32_16x16x32_bf16 v[192:195], a[100:103], v[244:247], v[192:195]\n"
      "s_cbranch_vccnz L_shB1_%=\n"
      "L_bhB1_%=:\n"
      "v_mfma_f32_16x16x32_bf16 v[176:179], a[104:107], v[8:11], v[176:179]\n"
      "global_load_dwordx4 v[64:67], v45, s[12:13] offset:0 sc1\n"
      "v_mfma_f32_16x16x32_bf16 v[176:179], a[124:127], v[12:15], v[176:179]\n"
      "ds_write_b128 v56, v[248:251] offset:4096\n"
      "v_mfma_f32_16x16x32_bf16 v[176:179], a[144:147], v[16:19], v[176:179]\n"
      "v_mfma_f32_16x16x32_bf16 v[180:183], a[108:111], v[8:11], v[180:183]\n"
      "v_mfma_f32_16x16x32_bf16 v[180:183], a[128:131], v[12:15], v[180:183]\n"
      "global_load_dwordx4 v[68:71], v45, s[12:13] offset:1024 sc1\n"
      "ds_write_b128 v56, v[252:255] offset:24576\n"
      "v_mfma_f32_16x16x32_bf16 v[180:183], a[148:151], v[16:19], v[180:183]\n"
      "v_mfma_f32_16x16x32_bf16 v[184:187], a[112:115], v[8:11], v[184:187]\n"
      "v_mfma_f32_16x16x32_bf16 v[184:187], a[132:135], v[12:15], v[184:187]\n"
      "v_mfma_f32_16x16x32_bf16 v[184:187], a[152:155], v[16:19], v[184:187]\n"
      "buffer_load_dword v26, v42, s[32:35], s63 offen offset:3584 sc1\n"
      "v_mfma_f32_16x16x32_bf16 v[188:191], a[116:119], v[8:11], v[188:191]\n"
      "buffer_load_dword v27, v42, s[32:35], s63 offen offset:3840 sc1\n"
      "v_mfma_f32_16x16x32_bf16 v[188:191], a[136:139], v[12:15], v[188:191]\n"
      "s_waitcnt lgkmcnt(0)\n"
      "s_barrier\n"
      "v_mfma_f32_16x16x32_bf16 v[188:191], a[156:159], v[16:19], v[188:191]\n"
      "ds_read_b128 v[96:99], v49 offset:0\n"
      "v_add_u32 v56, 0xa000, v56\n"
      "v_mfma_f32_16x16x32_bf16 v[192:195], a[120:123], v[8:11], v[192:195]\n"
      "ds_read_b128 v[100:103], v49 offset:5120\n"
      "v_mfma_f32_16x16x32_bf16 v[192:195], a[140:143], v[12:15], v[192:195]\n"
      "ds_read_b128 v[104:107], v49 offset:10240\n"
      "v_mfma_f32_16x16x32_bf16 v[192:195], a[160:163], v[16:19], v[192:195]\n"
      "ds_read_b128 v[108:111], v49 offset:15360\n"
      "v_mfma_f32_16x16x32_bf16 v[176:179], a[164:167], v[20:23], v[176:179]\n"
      "ds_read_b64_tr_b16 v[72:73], v53 offset:0\n"
      "v_mfma_f32_16x16x32_bf16 v[176:179], a[184:187], v[128:131], v[176:179]\n"
      "ds_read_b64_tr_b16 v[74:75], v53 offset:2048\n"
      "v_mfma_f32_16x16x32_bf16 v[176:179], a[204:207], v[132:135], v[176:179]\n"
      "ds_read_b64_tr_b16 v[76:77], v53 offset:4096\n"
      "v_mfma_f32_16x16x32_bf16 v[180:183], a[168:171], v[20:23], v[180:183]\n"
      "ds_read_b128 v[112:115], v49 offset:1024\n"
      "v_mfma_f32_16x16x32_bf16 v[180:183], a[188:191], v[128:131], v[180:183]\n"
      "ds_read_b128 v[116:119], v49 offset:6144\n"
      "v_mfma_f32_16x16x32_bf16 v[180:183], a[208:211], v[132:135], v[180:183]\n"
      "ds_read_b128 v[120:123], v49 offset:11264\n"
      "v_mfma_f32_16x16x32_bf16 v[184:187], a[172:175], v[20:23], v[184:187]\n"
      "ds_read_b128 v[124:127], v49 offset:16384\n"
      "s_waitcnt vmcnt(8)\n"
      "v_mfma_f32_16x16x32_bf16 v[184:187], a[192:195], v[128:131], v[184:187]\n"
      "v_add3_u32 v2, v136, v137, s30\n"
      "v_add3_u32 v2, v2, v138, v139\n"
      "v_add3_u32 v2, v2, v140, v141\n"
      "s_waitcnt lgkmcnt(4)\n"
      "v_mfma_f32_16x16x32_bf16 v[184:187], a[212:215], v[132:135], v[184:187]\n"
      "v_add3_u32 v2, v2, v142, v143\n"
      "v_add3_u32 v2, v2, v144, v145\n"
      "v_add3_u32 v2, v2, v146, v147\n"
      "v_add_f32 v96, v96, v100\n"
      "v_add_f32 v97, v97, v101\n"
      "v_mfma_f32_16x16x32_bf16 v[188:191], a[176:179], v[20:23], v[188:191]\n"
      "v_xor_b32 v2, v2, v24\n"
      "v_add3_u32 v3, v148, v149, s30\n"
      "v_add3_u32 v3, v3, v150, v151\n"
      "v_add_f32 v98, v98, v102\n"
      "v_add_f32 v99, v99, v103\n"
      "v_mfma_f32_16x16x32_bf16 v[188:191], a[196:199], v[128:131], v[188:191]\n"
      "v_add3_u32 v3, v3, v152, v153\n"
      "v_add3_u32 v3, v3, v154, v155\n"
      "v_add3_u32 v3, v3, v156, v157\n"
      "v_add_f32 v104, v104, v108\n"
      "v_add_f32 v105, v105, v109\n"
      "v_mfma_f32_16x16x32_bf16 v[188:191], a[216:219], v[132:135], v[188:191]\n"
      "v_add3_u32 v3, v3, v158, v159\n"
      "v_xor_b32 v3, v3, v25\n"
      "v_or_b32 v2, v2, v3\n"
      "v_add_f32 v106, v106, v110\n"
      "v_add_f32 v107, v107, v111\n"
      "v_mfma_f32_16x16x32_bf16 v[192:195], a[180:183], v[20:23], v[192:195]\n"
      "v_cmp_ne_u32 vcc, 0, v2\n"
      "v_add_f32 v96, v96, v104\n"
      "v_add_f32 v97, v97, v105\n"
      "v_mfma_f32_16x16x32_bf16 v[192:195], a[200:203], v[128:131], v[192:195]\n"
      "v_add_f32 v98, v98, v106\n"
      "v_add_f32 v99, v99, v107\n"
      "v_mfma_f32_16x16x32_bf16 v[192:195], a[220:223], v[132:135], v[192:195]\n"
      "s_cbranch_vccnz L_shB2_%=\n"
      "L_bhB2_%=:\n"
      "v_cvt_pk_bf16_f32 v96, 0, v96\n"
      "v_mfma_f32_16x16x32_bf16 v[200:203], a[240:243], v[136:139], 0\n"
      "v_cvt_pk_bf16_f32 v97, 0, v97\n"
      "v_cvt_pk_bf16_f32 v98, 0, v98\n"
      "v_mfma_f32_16x16x32_bf16 v[200:203], a[4:7], v[140:143], v[200:203]\n"
      "v_cvt_pk_bf16_f32 v99, 0, v99\n"
      "v_mul_f32 v96, v96, v80\n"
      "v_mfma_f32_16x16x32_bf16 v[200:203], a[24:27], v[144:147], v[200:203]\n"
      "v_mul_f32 v97, v97, v81\n"
      "v_mul_f32 v98, v98, v82\n"
      "v_mfma_f32_16x16x32_bf16 v[204:207], a[244:247], v[136:139], 0\n"
      "v_mul_f32 v99, v99, v83\n"
      "v_cvt_pk_bf16_f32 v96, 0, v96\n"
      "v_mfma_f32_16x16x32_bf16 v[204:207], a[8:11], v[140:143], v[204:207]\n"
      "v_cvt_pk_bf16_f32 v97, 0, v97\n"
      "v_cvt_pk_bf16_f32 v98, 0, v98\n"
      "v_mfma_f32_16x16x32_bf16 v[204:207], a[28:31], v[144:147], v[204:207]\n"
      "v_cvt_pk_bf16_f32 v99, 0, v99\n"
      "v_lshlrev_b32 v100, 16, v72\n"
      "v_mfma_f32_16x16x32_bf16 v[208:211], a[248:251], v[136:139], 0\n"
      "v_and_b32 v101, 0xffff0000, v72\n"
      "v_lshlrev_b32 v102, 16, v73\n"
      "v_mfma_f32_16x16x32_bf16 v[208:211], a[12:15], v[140:143], v[208:211]\n"
      "v_and_b32 v103, 0xffff0000, v73\n"
      "v_add_f32 v96, v96, v100\n"
      "v_mfma_f32_16x16x32_bf16 v[208:211], a[32:35], v[144:147], v[208:211]\n"
      "v_add_f32 v97, v97, v101\n"
      "v_add_f32 v98, v98, v102\n"
      "v_mfma_f32_16x16x32_bf16 v[212:215], a[252:255], v[136:139], 0\n"
      "v_add_f32 v99, v99, v103\n"
      "v_cvt_pk_bf16_f32 v72, v96, v97\n"
      "v_mfma_f32_16x16x32_bf16 v[212:215], a[16:19], v[140:143], v[212:215]\n"
      "ds_write_b128 v56, v[176:179] offset:0\n"
      "v_cvt_pk_bf16_f32 v73, v98, v99\n"
      "v_mfma_f32_16x16x32_bf16 v[212:215], a[36:39], v[144:147], v[212:215]\n"
      "global_store_dwordx2 v55, v[72:73], s[28:29] offset:0 sc1\n"
      "s_waitcnt lgkmcnt(1)\n"
      "v_mfma_f32_16x16x32_bf16 v[216:219], a[0:3], v[136:139], 0\n"
      "ds_read_b128 v[96:99], v49 offset:2048\n"
      "v_add_f32 v112, v112, v116\n"
      "v_mfma_f32_16x16x32_bf16 v[216:219], a[20:23], v[140:143], v[216:219]\n"
      "ds_read_b128 v[100:103], v49 offset:7168\n"
      "v_add_f32 v113, v113, v117\n"
      "v_mfma_f32_16x16x32_bf16 v[216:219], a[40:43], v[144:147], v[216:219]\n"
      "ds_read_b128 v[104:107], v49 offset:12288\n"
      "v_add_f32 v114, v114, v118\n"
      "v_mfma_f32_16x16x32_bf16 v[200:203], a[44:47], v[148:151], v[200:203]\n"
      "ds_read_b128 v[108:111], v49 offset:17408\n"
      "v_add_f32 v115, v115, v119\n"
      "v_mfma_f32_16x16x32_bf16 v[200:203], a[64:67], v[152:155], v[200:203]\n"
      "v_add_f32 v120, v120, v124\n"
      "v_add_f32 v121, v121, v125\n"
      "v_mfma_f32_16x16x32_bf16 v[200:203], a[84:87], v[156:159], v[200:203]\n"
      "v_add_f32 v122, v122, v126\n"
      "v_add_f32 v123, v123, v127\n"
      "v_mfma_f32_16x16x32_bf16 v[204:207], a[48:51], v[148:151], v[204:207]\n"
      "v_add_f32 v112, v112, v120\n"
      "v_add_f32 v113, v113, v121\n"
      "v_mfma_f32_16x16x32_bf16 v[204:207], a[68:71], v[152:155], v[204:207]\n"
      "v_add_f32 v114, v114, v122\n"
      "v_add_f32 v115, v115, v123\n"
      "v_mfma_f32_16x16x32_bf16 v[204:207], a[88:91], v[156:159], v[204:207]\n"
      "v_cvt_pk_bf16_f32 v112, 0, v112\n"
      "v_cvt_pk_bf16_f32 v113, 0, v113\n"
      "v_mfma_f32_16x16x32_bf16 v[208:211], a[52:55], v[148:151], v[208:211]\n"
      "s_waitcnt vmcnt(1)\n"
      "v_cvt_pk_bf16_f32 v114, 0, v114\n"
      "v_mfma_f32_16x16x32_bf16 v[208:211], a[72:75], v[152:155], v[208:211]\n"
      "v_add3_u32 v2, v172, v173, s30\n"
      "v_add3_u32 v2, v2, v174, v175\n"
      "v_add3_u32 v2, v2, v160, v161\n"
      "v_cvt_pk_bf16_f32 v115, 0, v115\n"
      "v_mul_f32 v112, v112, v84\n"
      "v_mfma_f32_16x16x32_bf16 v[208:211], a[92:95], v[156:159], v[208:211]\n"
      "v_add3_u32 v2, v2, v162, v163\n"
      "v_add3_u32 v2, v2, v164, v165\n"
      "v_add3_u32 v2, v2, v166, v167\n"
      "v_mul_f32 v113, v113, v85\n"
      "v_mul_f32 v114, v114, v86\n"
      "v_mfma_f32_16x16x32_bf16 v[212:215], a[56:59], v[148:151], v[212:215]\n"
      "v_xor_b32 v2, v2, v26\n"
      "v_add3_u32 v3, v168, v169, s30\n"
      "v_add3_u32 v3, v3, v170, v171\n"
      "v_mul_f32 v115, v115, v87\n"
      "v_cvt_pk_bf16_f32 v112, 0, v112\n"
      "v_mfma_f32_16x16x32_bf16 v[212:215], a[76:79], v[152:155], v[212:215]\n"
      "ds_write_b128 v56, v[180:183] offset:1024\n"
      "v_add3_u32 v3, v3, v64, v65\n"
      "v_add3_u32 v3, v3, v66, v67\n"
      "v_add3_u32 v3, v3, v68, v69\n"
      "v_cvt_pk_bf16_f32 v113, 0, v113\n"
      "v_mfma_f32_16x16x32_bf16 v[212:215], a[96:99], v[156:159], v[212:215]\n"
      "v_add3_u32 v3, v3, v70, v71\n"
      "v_xor_b32 v3, v3, v27\n"
      "v_or_b32 v2, v2, v3\n"
      "v_cvt_pk_bf16_f32 v114, 0, v114\n"
      "v_cvt_pk_bf16_f32 v115, 0, v115\n"
      "v_mfma_f32_16x16x32_bf16 v[216:219], a[60:63], v[148:151], v[216:219]\n"
      "v_cmp_ne_u32 vcc, 0, v2\n"
      "v_lshlrev_b32 v116, 16, v74\n"
      "v_and_b32 v117, 0xffff0000, v74\n"
      "v_mfma_f32_16x16x32_bf16 v[216:219], a[80:83], v[152:155], v[216:219]\n"
      "v_lshlrev_b32 v118, 16, v75\n"
      "v_and_b32 v119, 0xffff0000, v75\n"
      "v_mfma_f32_16x16x32_bf16 v[216:219], a[100:103], v[156:159], v[216:219]\n"
      "ds_write_b128 v56, v[184:187] offset:2048\n"
      "s_cbranch_vccnz L_shB3_%=\n"
      "L_bhB3_%=:\n"
      "v_add_f32 v112, v112, v116\n"
      "v_add_f32 v113, v113, v117\n"
      "v_add_f32 v114, v114, v118\n"
      "v_mfma_f32_16x16x32_bf16 v[200:203], a[104:107], v[172:175], v[200:203]\n"
      "v_add_f32 v115, v115, v119\n"
      "v_cvt_pk_bf16_f32 v74, v112, v113\n"
      "v_mfma_f32_16x16x32_bf16 v[200:203], a[124:127], v[160:163], v[200:203]\n"
      "v_cvt_pk_bf16_f32 v75, v114, v115\n"
      "v_mfma_f32_16x16x32_bf16 v[200:203], a[144:147], v[164:167], v[200:203]\n"
      "global_store_dwordx2 v55, v[74:75], s[28:29] offset:32 sc1\n"
      "s_waitcnt lgkmcnt(2)\n"
      "v_mfma_f32_16x16x32_bf16 v[204:207], a[108:111], v[172:175], v[204:207]\n"
      "ds_write_b128 v56, v[188:191] offset:3072\n"
      "v_add_f32 v96, v96, v100\n"
      "v_add_f32 v97, v97, v101\n"
      "v_add_f32 v98, v98, v102\n"
      "v_add_f32 v99, v99, v103\n"
      "v_mfma_f32_16x16x32_bf16 v[204:207], a[128:131], v[160:163], v[204:207]\n"
      "v_add_f32 v104, v104, v108\n"
      "v_add_f32 v105, v105, v109\n"
      "v_mfma_f32_16x16x32_bf16 v[204:207], a[148:151], v[164:167], v[204:207]\n"
      "v_add_f32 v106, v106, v110\n"
      "v_add_f32 v107, v107, v111\n"
      "v_mfma_f32_16x16x32_bf16 v[208:211], a[112:115], v[172:175], v[208:211]\n"
      "v_add_f32 v96, v96, v104\n"
      "v_add_f32 v97, v97, v105\n"
      "v_mfma_f32_16x16x32_bf16 v[208:211], a[132:135], v[160:163], v[208:211]\n"
      "ds_write_b128 v56, v[192:195] offset:4096\n"
      "v_add_f32 v98, v98, v106\n"
      "v_add_f32 v99, v99, v107\n"
      "v_cvt_pk_bf16_f32 v96, 0, v96\n"
      "v_cvt_pk_bf16_f32 v97, 0, v97\n"
      "v_mfma_f32_16x16x32_bf16 v[208:211], a[152:155], v[164:167], v[208:211]\n"
      "v_cvt_pk_bf16_f32 v98, 0, v98\n"
      "v_cvt_pk_bf16_f32 v99, 0, v99\n"
      "v_mfma_f32_16x16x32_bf16 v[212:215], a[116:119], v[172:175], v[212:215]\n"
      "v_mul_f32 v96, v96, v88\n"
      "v_mul_f32 v97, v97, v89\n"
      "v_mfma_f32_16x16x32_bf16 v[212:215], a[136:139], v[160:163], v[212:215]\n"
      "v_mul_f32 v98, v98, v90\n"
      "v_mul_f32 v99, v99, v91\n"
      "v_mfma_f32_16x16x32_bf16 v[212:215], a[156:159], v[164:167], v[212:215]\n"
      "v_cvt_pk_bf16_f32 v96, 0, v96\n"
      "v_cvt_pk_bf16_f32 v97, 0, v97\n"
      "v_mfma_f32_16x16x32_bf16 v[216:219], a[120:123], v[172:175], v[216:219]\n"
      "v_cvt_pk_bf16_f32 v98, 0, v98\n"
      "v_cvt_pk_bf16_f32 v99, 0, v99\n"
      "v_mfma_f32_16x16x32_bf16 v[216:219], a[140:143], v[160:163], v[216:219]\n"
      "v_lshlrev_b32 v100, 16, v76\n"
      "v_and_b32 v101, 0xffff0000, v76\n"
      "v_mfma_f32_16x16x32_bf16 v[216:219], a[160:163], v[164:167], v[216:219]\n"
      "v_lshlrev_b32 v102, 16, v77\n"
      "L_pg7_%=:\n"
      "s_cbranch_execz L_pgr_%=\n"
      "v_and_b32 v103, 0xffff0000, v77\n"
      "v_mfma_f32_16x16x32_bf16 v[200:203], a[164:167], v[168:171], v[200:203]\n"
      "v_add_f32 v96, v96, v100\n"
      "v_add_f32 v97, v97, v101\n"
      "v_mfma_f32_16x16x32_bf16 v[200:203], a[184:187], v[64:67], v[200:203]\n"
      "v_add_f32 v98, v98, v102\n"
      "v_add_f32 v99, v99, v103\n"
      "v_mfma_f32_16x16x32_bf16 v[200:203], a[204:207], v[68:71], v[200:203]\n"
      "v_cvt_pk_bf16_f32 v76, v96, v97\n"
      "v_cvt_pk_bf16_f32 v77, v98, v99\n"
      "v_mfma_f32_16x16x32_bf16 v[204:207], a[168:171], v[168:171], v[204:207]\n"
      "global_store_dwordx2 v55, v[76:77], s[28:29] offset:64 sc1\n"
      "v_mfma_f32_16x16x32_bf16 v[204:207], a[188:191], v[64:67], v[204:207]\n"
      "v_mfma_f32_16x16x32_bf16 v[204:207], a[208:211], v[68:71], v[204:207]\n"
      "ds_write_b128 v56, v[200:203] offset:20480\n"
      "v_mfma_f32_16x16x32_bf16 v[208:211], a[172:175], v[168:171], v[208:211]\n"
      "v_mfma_f32_16x16x32_bf16 v[208:211], a[192:195], v[64:67], v[208:211]\n"
      "v_mfma_f32_16x16x32_bf16 v[208:211], a[212:215], v[68:71], v[208:211]\n"
      "ds_write_b128 v56, v[204:207] offset:21504\n"
      "v_mfma_f32_16x16x32_bf16 v[212:215], a[176:179], v[168:171], v[212:215]\n"
      "v_mfma_f32_16x16x32_bf16 v[212:215], a[196:199], v[64:67], v[212:215]\n"
      "v_mfma_f32_16x16x32_bf16 v[212:215], a[216:219], v[68:71], v[212:215]\n"
      "ds_write_b128 v56, v[208:211] offset:22528\n"
      "v_mfma_f32_16x16x32_bf16 v[216:219], a[180:183], v[168:171], v[216:219]\n"
      "v_mfma_f32_16x16x32_bf16 v[216:219], a[200:203], v[64:67], v[216:219]\n"
      "v_mfma_f32_16x16x32_bf16 v[216:219], a[220:223], v[68:71], v[216:219]\n"
      "ds_write_b128 v56, v[212:215] offset:23552\n"
      "v_xor_b32 v53, 64, v53\n"
      "ds_read_b64_tr_b16 v[64:65], v53 offset:0\n"
      "ds_read_b64_tr_b16 v[66:67], v53 offset:2048\n"
      "ds_read_b64_tr_b16 v[68:69], v53 offset:4096\n"
      "v_add_u32 v49, 0xa000, v49\n"
      "v_add_u32 v55, 81920, v55\n"
      "s_nop 0\n"
      "ds_write_b128 v56, v[216:219] offset:24576\n"
      "s_waitcnt lgkmcnt(0)\n"
      "s_barrier\n"
      "ds_read_b128 v[96:99], v49 offset:0\n"
      "ds_read_b128 v[100:103], v49 offset:5120\n"
      "ds_read_b128 v[104:107], v49 offset:10240\n"
      "ds_read_b128 v[108:111], v49 offset:15360\n"
      "ds_read_b128 v[112:115], v49 offset:1024\n"
      "ds_read_b128 v[116:119], v49 offset:6144\n"
      "ds_read_b128 v[120:123], v49 offset:11264\n"
      "ds_read_b128 v[124:127], v49 offset:16384\n"
      "ds_read_b128 v[128:131], v49 offset:2048\n"
      "ds_read_b128 v[132:135], v49 offset:7168\n"
      "ds_read_b128 v[136:139], v49 offset:12288\n"
      "ds_read_b128 v[140:143], v49 offset:17408\n"
      "v_lshlrev_b32 v144, 16, v64\n"
      "v_and_b32 v145, 0xffff0000, v64\n"
      "v_lshlrev_b32 v146, 16, v65\n"
      "v_and_b32 v147, 0xffff0000, v65\n"
      "v_lshlrev_b32 v148, 16, v66\n"
      "v_and_b32 v149, 0xffff0000, v66\n"
      "v_lshlrev_b32 v150, 16, v67\n"
      "v_and_b32 v151, 0xffff0000, v67\n"
      "v_lshlrev_b32 v152, 16, v68\n"
      "v_and_b32 v153, 0xffff0000, v68\n"
      "v_lshlrev_b32 v154, 16, v69\n"
      "v_and_b32 v155, 0xffff0000, v69\n"
      "s_waitcnt lgkmcnt(8)\n"
      "v_pk_add_f32 v[96:97], v[96:97], v[100:101]\n"
      "v_pk_add_f32 v[98:99], v[98:99], v[102:103]\n"
      "v_pk_add_f32 v[104:105], v[104:105], v[108:109]\n"
      "v_pk_add_f32 v[106:107], v[106:107], v[110:111]\n"
      "v_pk_add_f32 v[96:97], v[96:97], v[104:105]\n"
      "v_pk_add_f32 v[98:99], v[98:99], v[106:107]\n"
      "v_cvt_pk_bf16_f32 v96, 0, v96\n"
      "v_cvt_pk_bf16_f32 v97, 0, v97\n"
      "v_cvt_pk_bf16_f32 v98, 0, v98\n"
      "v_cvt_pk_bf16_f32 v99, 0, v99\n"
      "v_pk_mul_f32 v[96:97], v[96:97], v[80:81]\n"
      "v_pk_mul_f32 v[98:99], v[98:99], v[82:83]\n"
      "v_cvt_pk_bf16_f32 v96, 0, v96\n"
      "v_cvt_pk_bf16_f32 v97, 0, v97\n"
      "v_cvt_pk_bf16_f32 v98, 0, v98\n"
      "v_cvt_pk_bf16_f32 v99, 0, v99\n"
      "v_pk_add_f32 v[96:97], v[96:97], v[144:145]\n"
      "v_pk_add_f32 v[98:99], v[98:99], v[146:147]\n"
      "v_cvt_pk_bf16_f32 v96, v96, v97\n"
      "v_cvt_pk_bf16_f32 v97, v98, v99\n"
      "global_store_dwordx2 v55, v[96:97], s[28:29] offset:0\n"
      "s_waitcnt lgkmcnt(4)\n"
      "v_pk_add_f32 v[112:113], v[112:113], v[116:117]\n"
      "v_pk_add_f32 v[114:115], v[114:115], v[118:119]\n"
      "v_pk_add_f32 v[120:121], v[120:121], v[124:125]\n"
      "v_pk_add_f32 v[122:123], v[122:123], v[126:127]\n"
      "v_pk_add_f32 v[112:113], v[112:113], v[120:121]\n"
      "v_pk_add_f32 v[114:115], v[114:115], v[122:123]\n"
      "v_cvt_pk_bf16_f32 v112, 0, v112\n"
      "v_cvt_pk_bf16_f32 v113, 0, v113\n"
      "v_cvt_pk_bf16_f32 v114, 0, v114\n"
      "v_cvt_pk_bf16_f32 v115, 0, v115\n"
      "v_pk_mul_f32 v[112:113], v[112:113], v[84:85]\n"
      "v_pk_mul_f32 v[114:115], v[114:115], v[86:87]\n"
      "v_cvt_pk_bf16_f32 v112, 0, v112\n"
      "v_cvt_pk_bf16_f32 v113, 0, v113\n"
      "v_cvt_pk_bf16_f32 v114, 0, v114\n"
      "v_cvt_pk_bf16_f32 v115, 0, v115\n"
      "v_pk_add_f32 v[112:113], v[112:113], v[148:149]\n"
      "v_pk_add_f32 v[114:115], v[114:115], v[150:151]\n"
      "v_cvt_pk_bf16_f32 v112, v112, v113\n"
      "v_cvt_pk_bf16_f32 v113, v114, v115\n"
      "global_store_dwordx2 v55, v[112:113], s[28:29] offset:32\n"
      "s_waitcnt lgkmcnt(0)\n"
      "v_pk_add_f32 v[128:129], v[128:129], v[132:133]\n"
      "v_pk_add_f32 v[130:131], v[130:131], v[134:135]\n"
      "v_pk_add_f32 v[136:137], v[136:137], v[140:141]\n"
      "v_pk_add_f32 v[138:139], v[138:139], v[142:143]\n"
      "v_pk_add_f32 v[128:129], v[128:129], v[136:137]\n"
      "v_pk_add_f32 v[130:131], v[130:131], v[138:139]\n"
      "v_cvt_pk_bf16_f32 v128, 0, v128\n"
      "v_cvt_pk_bf16_f32 v129, 0, v129\n"
      "v_cvt_pk_bf16_f32 v130, 0, v130\n"
      "v_cvt_pk_bf16_f32 v131, 0, v131\n"
      "v_pk_mul_f32 v[128:129], v[128:129], v[88:89]\n"
      "v_pk_mul_f32 v[130:131], v[130:131], v[90:91]\n"
      "v_cvt_pk_bf16_f32 v128, 0, v128\n"
      "v_cvt_pk_bf16_f32 v129, 0, v129\n"
      "v_cvt_pk_bf16_f32 v130, 0, v130\n"
      "v_cvt_pk_bf16_f32 v131, 0, v131\n"
      "v_pk_add_f32 v[128:129], v[128:129], v[152:153]\n"
      "v_pk_add_f32 v[130:131], v[130:131], v[154:155]\n"
      "v_cvt_pk_bf16_f32 v128, v128, v129\n"
      "v_cvt_pk_bf16_f32 v129, v130, v131\n"
      "global_store_dwordx2 v55, v[128:129], s[28:29] offset:64\n"
      "s_or_b32 s40, s2, s60\n"
      "s_cmp_lg_u32 s40, 0\n"
      "s_cbranch_scc1 L_fin%=\n"
      "v_mov_b32 v8, s30\n"
      "v_mov_b32 v9, s31\n"
      "v_mov_b32 v10, 0\n"
      "s_mov_b64 exec, 1\n"
      "global_store_dwordx2 v10, v[8:9], s[26:27] sc1\n"
      "s_mov_b64 exec, -1\n"
      "L_fin%=:\n"
      "s_endpgm\n"
      "L_psx_%=:\n"
      "s_lshr_b32 s40, s92, 12\n"
      "s_lshr_b32 s41, s93, 12\n"
      "s_mulk_i32 s40, 4224\n"
      "s_mulk_i32 s41, 4224\n"
      "s_add_u32 s40, s40, 0x100000\n"
      "s_add_u32 s41, s41, 0x100000\n"
      "buffer_load_dwordx4 v[128:131], v54, s[32:35], s40 offen sc1\n"
      "s_add_u32 s40, s40, 1081344\n"
      "buffer_load_dwordx4 v[136:139], v54, s[32:35], s41 offen sc1\n"
      "buffer_load_dwordx4 v[132:135], v54, s[32:35], s40 offen sc1\n"
      "buffer_load_dword v140, v237, s[32:35], s70 offen sc1\n"
      "buffer_load_dword v141, v237, s[32:35], s98 offen sc1\n"
      "buffer_load_dword v142, v237, s[32:35], s71 offen sc1\n"
      "s_waitcnt vmcnt(0)\n"
      "v_add3_u32 v143, v128, v129, v130\n"
      "v_add3_u32 v143, v143, v131, v132\n"
      "v_add3_u32 v143, v143, v133, v134\n"
      "v_add3_u32 v143, v143, v135, v136\n"
      "v_add3_u32 v143, v143, v137, v138\n"
      "v_add_u32 v143, v143, v139\n"
      "v_add3_u32 v140, v140, v141, v142\n"
      "s_mul_i32 s47, s30, 3\n"
      "v_add_u32_dpp v143, v143, v143 quad_perm:[1,0,3,2] row_mask:0xf bank_mask:0xf\n"
      "s_nop 1\n"
      "v_add_u32_dpp v143, v143, v143 quad_perm:[2,3,0,1] row_mask:0xf bank_mask:0xf\n"
      "s_nop 1\n"
      "v_add_u32_dpp v143, v143, v143 row_half_mirror row_mask:0xf bank_mask:0xf\n"
      "v_add_u32 v143, v143, v140\n"
      "v_cmp_ne_u32 vcc, s47, v143\n"
      "s_sub_u32 s44, s44, 1\n"
      "s_cbranch_vccz L_pbx_%=\n"
      "s_cmp_gt_i32 s44, 0\n"
      "s_cbranch_scc1 L_psx_%=\n"
      "s_branch L_pbx_%=\n"
      "L_cv_s%=:\n"
      "buffer_load_dwordx2 v[200:201], v4, s[32:35], s80 offen offset:0 sc1\n"
      "buffer_load_dword v250, v61, s[32:35], s85 offen offset:0 sc1\n"
      "buffer_load_dwordx4 v[224:227], v57, s[32:35], s82 offen offset:0 sc1\n"
      "buffer_load_dwordx4 v[228:231], v57, s[32:35], s82 offen offset:1024 sc1\n"
      "buffer_load_dwordx4 v[192:195], v54, s[32:35], s53 offen offset:0 sc1\n"
      "buffer_load_dwordx4 v[196:199], v54, s[32:35], s54 offen offset:0 sc1\n"
      "buffer_load_dword v248, v60, s[32:35], s84 offen offset:0 sc1\n"
      "buffer_load_dword v249, v60, s[32:35], s84 offen offset:2048 sc1\n"
      "buffer_load_dwordx4 v[208:211], v55, s[32:35], 0 offen offset:0 sc1\n"
      "buffer_load_dwordx4 v[212:215], v55, s[32:35], 0 offen offset:1024 sc1\n"
      "buffer_load_dwordx4 v[216:219], v55, s[32:35], 0 offen offset:2048 sc1\n"
      "buffer_load_dwordx4 v[220:223], v55, s[32:35], 0 offen offset:3072 sc1\n"
      "buffer_load_dwordx2 v[204:205], v58, s[32:35], s83 offen offset:0 sc1\n"
      "s_waitcnt vmcnt(0)\n"
      "v_add_u32 v251, v200, v201\n"
      "v_add3_u32 v34, v192, v193, v194\n"
      "v_add3_u32 v34, v34, v195, v196\n"
      "v_add_u32_dpp v251, v251, v251 row_half_mirror row_mask:0xf bank_mask:0xf\n"
      "v_add3_u32 v34, v34, v197, v198\n"
      "v_add3_u32 v251, v251, v34, v199\n"
      "v_add3_u32 v34, v248, v249, v250\n"
      "v_add3_u32 v34, v34, v211, v215\n"
      "v_add_u32_dpp v251, v251, v251 quad_perm:[1,0,3,2] row_mask:0xf bank_mask:0xf\n"
      "v_add3_u32 v34, v34, v219, v223\n"
      "v_add3_u32 v34, v34, v227, v231\n"
      "v_add_u32_dpp v251, v251, v251 quad_perm:[2,3,0,1] row_mask:0xf bank_mask:0xf\n"
      "v_add3_u32 v251, v251, v34, v205\n"
      "v_cmp_ne_u32 vcc, s24, v251\n"
      "s_sub_u32 s44, s44, 1\n"
      "s_cbranch_vccz L_cv_r%=\n"
      "s_cmp_gt_i32 s44, 0\n"
      "s_cbranch_scc1 L_cv_s%=\n"
      "s_branch L_cv_r%=\n"
      "L_cw_s%=:\n"
      "buffer_load_dwordx2 v[252:253], v51, s[32:35], s76 offen offset:0 sc1\n"
      "s_waitcnt vmcnt(0)\n"
      "v_cmp_ne_u64 vcc, s[30:31], v[252:253]\n"
      "s_sub_u32 s44, s44, 1\n"
      "s_cbranch_vccz L_cw_r%=\n"
      "s_cmp_gt_i32 s44, 0\n"
      "s_cbranch_scc1 L_cw_s%=\n"
      "s_branch L_cw_r%=\n"
      "L_cm_s%=:\n"
      "buffer_load_dwordx2 v[254:255], v2, s[32:35], s45 offen sc1\n"
      "s_waitcnt vmcnt(0)\n"
      "v_cmp_ne_u32 vcc, s30, v255\n"
      "s_sub_u32 s44, s44, 1\n"
      "s_cbranch_vccz L_cm_r%=\n"
      "s_cmp_gt_i32 s44, 0\n"
      "s_cbranch_scc1 L_cm_s%=\n"
      "s_branch L_cm_r%=\n"
      "L_sx1_%=:\n"
      "s_mov_b32 s45, m0\n"
      "s_mov_b32 s58, s47\n"
      "s_add_u32 m0, s73, 16384\n"
      "v_add_u32 v248, 81920, v40\n"
      "global_load_lds_dwordx4 v248, s[4:5] offset:0 sc1\n"
      "global_load_lds_dwordx4 v248, s[4:5] offset:1024 sc1\n"
      "global_load_lds_dwordx4 v248, s[4:5] offset:2048 sc1\n"
      "global_load_lds_dwordx4 v248, s[4:5] offset:3072 sc1\n"
      "s_mov_b32 s47, 4\n"
      "buffer_load_dword v3, v5, s[32:35], s47 offen sc1\n"
      "s_waitcnt vmcnt(0)\n"
      "s_mov_b32 m0, s45\n"
      "s_mov_b32 s47, s58\n"
      "ds_read_b128 v[248:251], v40 offset:16384\n"
      "ds_read_b128 v[252:255], v40 offset:17408\n"
      "ds_read_b128 v[36:39], v40 offset:18432\n"
      "ds_read_b128 v[60:63], v40 offset:19456\n"
      "s_waitcnt lgkmcnt(0)\n"
      "v_add3_u32 v3, v3, v248, v249\n"
      "v_add3_u32 v3, v3, v250, v251\n"
      "v_add3_u32 v3, v3, v252, v253\n"
      "v_add3_u32 v3, v3, v254, v255\n"
      "v_add3_u32 v3, v3, v36, v37\n"
      "v_add3_u32 v3, v3, v38, v39\n"
      "v_add3_u32 v3, v3, v60, v61\n"
      "v_add3_u32 v3, v3, v62, v63\n"
      "s_nop 1\n"
      "v_add_u32_dpp v3, v3, v3 quad_perm:[1,0,3,2] row_mask:0xf bank_mask:0xf\n"
      "s_nop 1\n"
      "v_add_u32_dpp v3, v3, v3 quad_perm:[2,3,0,1] row_mask:0xf bank_mask:0xf\n"
      "s_nop 1\n"
      "v_add_u32_dpp v3, v3, v3 row_half_mirror row_mask:0xf bank_mask:0xf\n"
      "v_cmp_ne_u32 vcc, s71, v3\n"
      "s_sub_u32 s44, s44, 1\n"
      "s_cbranch_vccz L_bx1_%=\n"
      "s_cmp_gt_i32 s44, 0\n"
      "s_cbranch_scc1 L_sx1_%=\n"
      "s_branch L_bx1_%=\n"
      "L_sx2_%=:\n"
      "s_mov_b32 s45, m0\n"
      "s_mov_b32 s58, s47\n"
      "s_add_u32 m0, s73, 32768\n"
      "v_add_u32 v248, 16384, v40\n"
      "global_load_lds_dwordx4 v248, s[4:5] offset:0 sc1\n"
      "global_load_lds_dwordx4 v248, s[4:5] offset:1024 sc1\n"
      "global_load_lds_dwordx4 v248, s[4:5] offset:2048 sc1\n"
      "global_load_lds_dwordx4 v248, s[4:5] offset:3072 sc1\n"
      "s_mov_b32 s47, 32768\n"
      "buffer_load_dword v48, v5, s[32:35], s47 offen sc1\n"
      "s_waitcnt vmcnt(0)\n"
      "s_mov_b32 m0, s45\n"
      "s_mov_b32 s47, s58\n"
      "ds_read_b128 v[248:251], v40 offset:32768\n"
      "ds_read_b128 v[252:255], v40 offset:33792\n"
      "ds_read_b128 v[36:39], v40 offset:34816\n"
      "ds_read_b128 v[60:63], v40 offset:35840\n"
      "s_waitcnt lgkmcnt(0)\n"
      "v_add3_u32 v48, v48, v248, v249\n"
      "v_add3_u32 v48, v48, v250, v251\n"
      "v_add3_u32 v48, v48, v252, v253\n"
      "v_add3_u32 v48, v48, v254, v255\n"
      "v_add3_u32 v48, v48, v36, v37\n"
      "v_add3_u32 v48, v48, v38, v39\n"
      "v_add3_u32 v48, v48, v60, v61\n"
      "v_add3_u32 v48, v48, v62, v63\n"
      "s_nop 1\n"
      "v_add_u32_dpp v48, v48, v48 quad_perm:[1,0,3,2] row_mask:0xf bank_mask:0xf\n"
      "s_nop 1\n"
      "v_add_u32_dpp v48, v48, v48 quad_perm:[2,3,0,1] row_mask:0xf bank_mask:0xf\n"
      "s_nop 1\n"
      "v_add_u32_dpp v48, v48, v48 row_half_mirror row_mask:0xf bank_mask:0xf\n"
      "v_cmp_ne_u32 vcc, s71, v48\n"
      "s_sub_u32 s44, s44, 1\n"
      "s_cbranch_vccz L_bx2_%=\n"
      "s_cmp_gt_i32 s44, 0\n"
      "s_cbranch_scc1 L_sx2_%=\n"
      "s_branch L_bx2_%=\n"
      "L_sx3_%=:\n"
      "s_mov_b32 s45, m0\n"
      "s_mov_b32 s58, s47\n"
      "s_add_u32 m0, s73, 49152\n"
      "v_add_u32 v248, 32768, v40\n"
      "global_load_lds_dwordx4 v248, s[4:5] offset:0 sc1\n"
      "global_load_lds_dwordx4 v248, s[4:5] offset:1024 sc1\n"
      "global_load_lds_dwordx4 v248, s[4:5] offset:2048 sc1\n"
      "global_load_lds_dwordx4 v248, s[4:5] offset:3072 sc1\n"
      "s_mov_b32 s47, 65536\n"
      "buffer_load_dword v54, v5, s[32:35], s47 offen sc1\n"
      "s_waitcnt vmcnt(0)\n"
      "s_mov_b32 m0, s45\n"
      "s_mov_b32 s47, s58\n"
      "ds_read_b128 v[248:251], v40 offset:49152\n"
      "ds_read_b128 v[252:255], v40 offset:50176\n"
      "ds_read_b128 v[36:39], v40 offset:51200\n"
      "ds_read_b128 v[60:63], v40 offset:52224\n"
      "s_waitcnt lgkmcnt(0)\n"
      "v_add3_u32 v54, v54, v248, v249\n"
      "v_add3_u32 v54, v54, v250, v251\n"
      "v_add3_u32 v54, v54, v252, v253\n"
      "v_add3_u32 v54, v54, v254, v255\n"
      "v_add3_u32 v54, v54, v36, v37\n"
      "v_add3_u32 v54, v54, v38, v39\n"
      "v_add3_u32 v54, v54, v60, v61\n"
      "v_add3_u32 v54, v54, v62, v63\n"
      "s_nop 1\n"
      "v_add_u32_dpp v54, v54, v54 quad_perm:[1,0,3,2] row_mask:0xf bank_mask:0xf\n"
      "s_nop 1\n"
      "v_add_u32_dpp v54, v54, v54 quad_perm:[2,3,0,1] row_mask:0xf bank_mask:0xf\n"
      "s_nop 1\n"
      "v_add_u32_dpp v54, v54, v54 row_half_mirror row_mask:0xf bank_mask:0xf\n"
      "v_cmp_ne_u32 vcc, s71, v54\n"
      "s_sub_u32 s44, s44, 1\n"
      "s_cbranch_vccz L_bx3_%=\n"
      "s_cmp_gt_i32 s44, 0\n"
      "s_cbranch_scc1 L_sx3_%=\n"
      "s_branch L_bx3_%=\n"
      "L_sx4_%=:\n"
      "s_mov_b32 s45, m0\n"
      "s_mov_b32 s58, s47\n"
      "s_add_u32 m0, s73, 0\n"
      "v_add_u32 v248, 49152, v40\n"
      "global_load_lds_dwordx4 v248, s[4:5] offset:0 sc1\n"
      "global_load_lds_dwordx4 v248, s[4:5] offset:1024 sc1\n"
      "global_load_lds_dwordx4 v248, s[4:5] offset:2048 sc1\n"
      "global_load_lds_dwordx4 v248, s[4:5] offset:3072 sc1\n"
      "s_mov_b32 s47, 98304\n"
      "buffer_load_dword v56, v5, s[32:35], s47 offen sc1\n"
      "s_waitcnt vmcnt(0)\n"
      "s_mov_b32 m0, s45\n"
      "s_mov_b32 s47, s58\n"
      "ds_read_b128 v[248:251], v40 offset:0\n"
      "ds_read_b128 v[252:255], v40 offset:1024\n"
      "ds_read_b128 v[36:39], v40 offset:2048\n"
      "ds_read_b128 v[60:63], v40 offset:3072\n"
      "s_waitcnt lgkmcnt(0)\n"
      "v_add3_u32 v56, v56, v248, v249\n"
      "v_add3_u32 v56, v56, v250, v251\n"
      "v_add3_u32 v56, v56, v252, v253\n"
      "v_add3_u32 v56, v56, v254, v255\n"
      "v_add3_u32 v56, v56, v36, v37\n"
      "v_add3_u32 v56, v56, v38, v39\n"
      "v_add3_u32 v56, v56, v60, v61\n"
      "v_add3_u32 v56, v56, v62, v63\n"
      "s_nop 1\n"
      "v_add_u32_dpp v56, v56, v56 quad_perm:[1,0,3,2] row_mask:0xf bank_mask:0xf\n"
      "s_nop 1\n"
      "v_add_u32_dpp v56, v56, v56 quad_perm:[2,3,0,1] row_mask:0xf bank_mask:0xf\n"
      "s_nop 1\n"
      "v_add_u32_dpp v56, v56, v56 row_half_mirror row_mask:0xf bank_mask:0xf\n"
      "v_cmp_ne_u32 vcc, s71, v56\n"
      "s_sub_u32 s44, s44, 1\n"
      "s_cbranch_vccz L_bx4_%=\n"
      "s_cmp_gt_i32 s44, 0\n"
      "s_cbranch_scc1 L_sx4_%=\n"
      "s_branch L_bx4_%=\n"
      "L_sx5_%=:\n"
      "s_mov_b32 s45, m0\n"
      "s_mov_b32 s58, s47\n"
      "s_add_u32 m0, s73, 16384\n"
      "v_add_u32 v248, 65536, v40\n"
      "global_load_lds_dwordx4 v248, s[4:5] offset:0 sc1\n"
      "global_load_lds_dwordx4 v248, s[4:5] offset:1024 sc1\n"
      "global_load_lds_dwordx4 v248, s[4:5] offset:2048 sc1\n"
      "global_load_lds_dwordx4 v248, s[4:5] offset:3072 sc1\n"
      "s_mov_b32 s47, 131072\n"
      "buffer_load_dword v2, v5, s[32:35], s47 offen sc1\n"
      "s_waitcnt vmcnt(0)\n"
      "s_mov_b32 m0, s45\n"
      "s_mov_b32 s47, s58\n"
      "ds_read_b128 v[248:251], v40 offset:16384\n"
      "ds_read_b128 v[252:255], v40 offset:17408\n"
      "ds_read_b128 v[36:39], v40 offset:18432\n"
      "ds_read_b128 v[60:63], v40 offset:19456\n"
      "s_waitcnt lgkmcnt(0)\n"
      "v_add3_u32 v2, v2, v248, v249\n"
      "v_add3_u32 v2, v2, v250, v251\n"
      "v_add3_u32 v2, v2, v252, v253\n"
      "v_add3_u32 v2, v2, v254, v255\n"
      "v_add3_u32 v2, v2, v36, v37\n"
      "v_add3_u32 v2, v2, v38, v39\n"
      "v_add3_u32 v2, v2, v60, v61\n"
      "v_add3_u32 v2, v2, v62, v63\n"
      "s_nop 1\n"
      "v_add_u32_dpp v2, v2, v2 quad_perm:[1,0,3,2] row_mask:0xf bank_mask:0xf\n"
      "s_nop 1\n"
      "v_add_u32_dpp v2, v2, v2 quad_perm:[2,3,0,1] row_mask:0xf bank_mask:0xf\n"
      "s_nop 1\n"
      "v_add_u32_dpp v2, v2, v2 row_half_mirror row_mask:0xf bank_mask:0xf\n"
      "v_cmp_ne_u32 vcc, s71, v2\n"
      "s_sub_u32 s44, s44, 1\n"
      "s_cbranch_vccz L_bx5_%=\n"
      "s_cmp_gt_i32 s44, 0\n"
      "s_cbranch_scc1 L_sx5_%=\n"
      "s_branch L_bx5_%=\n"
      "L_sx6_%=:\n"
      "s_mov_b32 s45, m0\n"
      "s_mov_b32 s58, s47\n"
      "s_add_u32 m0, s73, 32768\n"
      "v_add_u32 v248, 98304, v40\n"
      "global_load_lds_dwordx4 v248, s[4:5] offset:0 sc1\n"
      "global_load_lds_dwordx4 v248, s[4:5] offset:1024 sc1\n"
      "global_load_lds_dwordx4 v248, s[4:5] offset:2048 sc1\n"
      "global_load_lds_dwordx4 v248, s[4:5] offset:3072 sc1\n"
      "s_mov_b32 s47, 32772\n"
      "buffer_load_dword v49, v5, s[32:35], s47 offen sc1\n"
      "s_waitcnt vmcnt(0)\n"
      "s_mov_b32 m0, s45\n"
      "s_mov_b32 s47, s58\n"
      "ds_read_b128 v[248:251], v40 offset:32768\n"
      "ds_read_b128 v[252:255], v40 offset:33792\n"
      "ds_read_b128 v[36:39], v40 offset:34816\n"
      "ds_read_b128 v[60:63], v40 offset:35840\n"
      "s_waitcnt lgkmcnt(0)\n"
      "v_add3_u32 v49, v49, v248, v249\n"
      "v_add3_u32 v49, v49, v250, v251\n"
      "v_add3_u32 v49, v49, v252, v253\n"
      "v_add3_u32 v49, v49, v254, v255\n"
      "v_add3_u32 v49, v49, v36, v37\n"
      "v_add3_u32 v49, v49, v38, v39\n"
      "v_add3_u32 v49, v49, v60, v61\n"
      "v_add3_u32 v49, v49, v62, v63\n"
      "s_nop 1\n"
      "v_add_u32_dpp v49, v49, v49 quad_perm:[1,0,3,2] row_mask:0xf bank_mask:0xf\n"
      "s_nop 1\n"
      "v_add_u32_dpp v49, v49, v49 quad_perm:[2,3,0,1] row_mask:0xf bank_mask:0xf\n"
      "s_nop 1\n"
      "v_add_u32_dpp v49, v49, v49 row_half_mirror row_mask:0xf bank_mask:0xf\n"
      "v_cmp_ne_u32 vcc, s71, v49\n"
      "s_sub_u32 s44, s44, 1\n"
      "s_cbranch_vccz L_bx6_%=\n"
      "s_cmp_gt_i32 s44, 0\n"
      "s_cbranch_scc1 L_sx6_%=\n"
      "s_branch L_bx6_%=\n"
      "L_sx7_%=:\n"
      "s_mov_b32 s45, m0\n"
      "s_mov_b32 s58, s47\n"
      "s_add_u32 m0, s73, 49152\n"
      "v_add_u32 v248, 114688, v40\n"
      "global_load_lds_dwordx4 v248, s[4:5] offset:0 sc1\n"
      "global_load_lds_dwordx4 v248, s[4:5] offset:1024 sc1\n"
      "global_load_lds_dwordx4 v248, s[4:5] offset:2048 sc1\n"
      "global_load_lds_dwordx4 v248, s[4:5] offset:3072 sc1\n"
      "s_mov_b32 s47, 65540\n"
      "buffer_load_dword v55, v5, s[32:35], s47 offen sc1\n"
      "s_waitcnt vmcnt(0)\n"
      "s_mov_b32 m0, s45\n"
      "s_mov_b32 s47, s58\n"
      "ds_read_b128 v[248:251], v40 offset:49152\n"
      "ds_read_b128 v[252:255], v40 offset:50176\n"
      "ds_read_b128 v[36:39], v40 offset:51200\n"
      "ds_read_b128 v[60:63], v40 offset:52224\n"
      "s_waitcnt lgkmcnt(0)\n"
      "v_add3_u32 v55, v55, v248, v249\n"
      "v_add3_u32 v55, v55, v250, v251\n"
      "v_add3_u32 v55, v55, v252, v253\n"
      "v_add3_u32 v55, v55, v254, v255\n"
      "v_add3_u32 v55, v55, v36, v37\n"
      "v_add3_u32 v55, v55, v38, v39\n"
      "v_add3_u32 v55, v55, v60, v61\n"
      "v_add3_u32 v55, v55, v62, v63\n"
      "s_nop 1\n"
      "v_add_u32_dpp v55, v55, v55 quad_perm:[1,0,3,2] row_mask:0xf bank_mask:0xf\n"
      "s_nop 1\n"
      "v_add_u32_dpp v55, v55, v55 quad_perm:[2,3,0,1] row_mask:0xf bank_mask:0xf\n"
      "s_nop 1\n"
      "v_add_u32_dpp v55, v55, v55 row_half_mirror row_mask:0xf bank_mask:0xf\n"
      "v_cmp_ne_u32 vcc, s71, v55\n"
      "s_sub_u32 s44, s44, 1\n"
      "s_cbranch_vccz L_bx7_%=\n"
      "s_cmp_gt_i32 s44, 0\n"
      "s_cbranch_scc1 L_sx7_%=\n"
      "s_branch L_bx7_%=\n"
      "L_sx8_%=:\n"
      "s_mov_b32 s45, m0\n"
      "s_mov_b32 s58, s47\n"
      "s_add_u32 m0, s73, 0\n"
      "v_add_u32 v248, 131072, v40\n"
      "global_load_lds_dwordx4 v248, s[4:5] offset:0 sc1\n"
      "global_load_lds_dwordx4 v248, s[4:5] offset:1024 sc1\n"
      "global_load_lds_dwordx4 v248, s[4:5] offset:2048 sc1\n"
      "global_load_lds_dwordx4 v248, s[4:5] offset:3072 sc1\n"
      "s_mov_b32 s47, 98308\n"
      "buffer_load_dword v57, v5, s[32:35], s47 offen sc1\n"
      "s_waitcnt vmcnt(0)\n"
      "s_mov_b32 m0, s45\n"
      "s_mov_b32 s47, s58\n"
      "ds_read_b128 v[248:251], v40 offset:0\n"
      "ds_read_b128 v[252:255], v40 offset:1024\n"
      "ds_read_b128 v[36:39], v40 offset:2048\n"
      "ds_read_b128 v[60:63], v40 offset:3072\n"
      "s_waitcnt lgkmcnt(0)\n"
      "v_add3_u32 v57, v57, v248, v249\n"
      "v_add3_u32 v57, v57, v250, v251\n"
      "v_add3_u32 v57, v57, v252, v253\n"
      "v_add3_u32 v57, v57, v254, v255\n"
      "v_add3_u32 v57, v57, v36, v37\n"
      "v_add3_u32 v57, v57, v38, v39\n"
      "v_add3_u32 v57, v57, v60, v61\n"
      "v_add3_u32 v57, v57, v62, v63\n"
      "s_nop 1\n"
      "v_add_u32_dpp v57, v57, v57 quad_perm:[1,0,3,2] row_mask:0xf bank_mask:0xf\n"
      "s_nop 1\n"
      "v_add_u32_dpp v57, v57, v57 quad_perm:[2,3,0,1] row_mask:0xf bank_mask:0xf\n"
      "s_nop 1\n"
      "v_add_u32_dpp v57, v57, v57 row_half_mirror row_mask:0xf bank_mask:0xf\n"
      "v_cmp_ne_u32 vcc, s71, v57\n"
      "s_sub_u32 s44, s44, 1\n"
      "s_cbranch_vccz L_bx8_%=\n"
      "s_cmp_gt_i32 s44, 0\n"
      "s_cbranch_scc1 L_sx8_%=\n"
      "s_branch L_bx8_%=\n"
      "L_sx9_%=:\n"
      "s_mov_b32 s45, m0\n"
      "s_mov_b32 s58, s47\n"
      "s_add_u32 m0, s73, 16384\n"
      "v_add_u32 v248, 147456, v40\n"
      "global_load_lds_dwordx4 v248, s[4:5] offset:0 sc1\n"
      "global_load_lds_dwordx4 v248, s[4:5] offset:1024 sc1\n"
      "global_load_lds_dwordx4 v248, s[4:5] offset:2048 sc1\n"
      "global_load_lds_dwordx4 v248, s[4:5] offset:3072 sc1\n"
      "s_mov_b32 s47, 131076\n"
      "buffer_load_dword v3, v5, s[32:35], s47 offen sc1\n"
      "s_waitcnt vmcnt(0)\n"
      "s_mov_b32 m0, s45\n"
      "s_mov_b32 s47, s58\n"
      "ds_read_b128 v[248:251], v40 offset:16384\n"
      "ds_read_b128 v[252:255], v40 offset:17408\n"
      "ds_read_b128 v[36:39], v40 offset:18432\n"
      "ds_read_b128 v[60:63], v40 offset:19456\n"
      "s_waitcnt lgkmcnt(0)\n"
      "v_add3_u32 v3, v3, v248, v249\n"
      "v_add3_u32 v3, v3, v250, v251\n"
      "v_add3_u32 v3, v3, v252, v253\n"
      "v_add3_u32 v3, v3, v254, v255\n"
      "v_add3_u32 v3, v3, v36, v37\n"
      "v_add3_u32 v3, v3, v38, v39\n"
      "v_add3_u32 v3, v3, v60, v61\n"
      "v_add3_u32 v3, v3, v62, v63\n"
      "s_nop 1\n"
      "v_add_u32_dpp v3, v3, v3 quad_perm:[1,0,3,2] row_mask:0xf bank_mask:0xf\n"
      "s_nop 1\n"
      "v_add_u32_dpp v3, v3, v3 quad_perm:[2,3,0,1] row_mask:0xf bank_mask:0xf\n"
      "s_nop 1\n"
      "v_add_u32_dpp v3, v3, v3 row_half_mirror row_mask:0xf bank_mask:0xf\n"
      "v_cmp_ne_u32 vcc, s71, v3\n"
      "s_sub_u32 s44, s44, 1\n"
      "s_cbranch_vccz L_bx9_%=\n"
      "s_cmp_gt_i32 s44, 0\n"
      "s_cbranch_scc1 L_sx9_%=\n"
      "s_branch L_bx9_%=\n"
      "L_sx0_%=:\n"
      "s_mov_b32 s45, m0\n"
      "s_mov_b32 s58, s47\n"
      "s_add_u32 m0, s73, 0\n"
      "v_add_u32 v248, 0, v40\n"
      "global_load_lds_dwordx4 v248, s[4:5] offset:0 sc1\n"
      "global_load_lds_dwordx4 v248, s[4:5] offset:1024 sc1\n"
      "global_load_lds_dwordx4 v248, s[4:5] offset:2048 sc1\n"
      "global_load_lds_dwordx4 v248, s[4:5] offset:3072 sc1\n"
      "s_mov_b32 s47, 0\n"
      "buffer_load_dword v2, v5, s[32:35], s47 offen sc1\n"
      "s_waitcnt vmcnt(0)\n"
      "s_mov_b32 m0, s45\n"
      "s_mov_b32 s47, s58\n"
      "ds_read_b128 v[248:251], v40 offset:0\n"
      "ds_read_b128 v[252:255], v40 offset:1024\n"
      "ds_read_b128 v[36:39], v40 offset:2048\n"
      "ds_read_b128 v[60:63], v40 offset:3072\n"
      "s_waitcnt lgkmcnt(0)\n"
      "v_add3_u32 v2, v2, v248, v249\n"
      "v_add3_u32 v2, v2, v250, v251\n"
      "v_add3_u32 v2, v2, v252, v253\n"
      "v_add3_u32 v2, v2, v254, v255\n"
      "v_add3_u32 v2, v2, v36, v37\n"
      "v_add3_u32 v2, v2, v38, v39\n"
      "v_add3_u32 v2, v2, v60, v61\n"
      "v_add3_u32 v2, v2, v62, v63\n"
      "s_nop 1\n"
      "v_add_u32_dpp v2, v2, v2 quad_perm:[1,0,3,2] row_mask:0xf bank_mask:0xf\n"
      "s_nop 1\n"
      "v_add_u32_dpp v2, v2, v2 quad_perm:[2,3,0,1] row_mask:0xf bank_mask:0xf\n"
      "s_nop 1\n"
      "v_add_u32_dpp v2, v2, v2 row_half_mirror row_mask:0xf bank_mask:0xf\n"
      "v_cmp_ne_u32 vcc, s71, v2\n"
      "s_sub_u32 s44, s44, 1\n"
      "s_cbranch_vccz L_bx0_%=\n"
      "s_cmp_gt_i32 s44, 0\n"
      "s_cbranch_scc1 L_sx0_%=\n"
      "s_branch L_bx0_%=\n"
      "L_sfd_%=:\n"
      "global_load_dwordx2 v[58:59], v42, s[26:27] sc1\n"
      "buffer_load_dwordx2 v[46:47], v51, s[32:35], s75 offen sc1\n"
      "s_waitcnt vmcnt(0)\n"
      "v_cmp_ne_u64 s[52:53], s[30:31], v[46:47]\n"
      "v_cmp_ne_u64 vcc, s[30:31], v[58:59]\n"
      "s_or_b64 vcc, vcc, s[52:53]\n"
      "s_sub_u32 s44, s44, 1\n"
      "s_cbranch_vccz L_bfd_%=\n"
      "s_cmp_gt_i32 s44, 0\n"
      "s_cbranch_scc1 L_sfd_%=\n"
      "s_branch L_bfd_%=\n"
      "L_sxp_%=:\n"
      "s_mov_b32 s45, m0\n"
      "s_mov_b32 s58, s47\n"
      "s_mov_b32 m0, s94\n"
      "s_nop 0\n"
      "buffer_load_dwordx4 v7, s[32:35], s67 offen offset:0 sc1 lds\n"
      "buffer_load_dwordx4 v7, s[32:35], s67 offen offset:1024 sc1 lds\n"
      "buffer_load_dwordx4 v7, s[32:35], s67 offen offset:2048 sc1 lds\n"
      "buffer_load_dword v32, v31, s[32:35], s68 offen sc1\n"
      "s_waitcnt vmcnt(0)\n"
      "s_mov_b32 m0, s45\n"
      "s_mov_b32 s47, s58\n"
      "ds_read_b128 v[248:251], v50 offset:0\n"
      "ds_read_b128 v[252:255], v50 offset:1024\n"
      "ds_read_b128 v[36:39], v50 offset:2048\n"
      "s_waitcnt lgkmcnt(0)\n"
      "v_add3_u32 v32, v32, v248, v249\n"
      "v_add3_u32 v32, v32, v250, v251\n"
      "v_add3_u32 v32, v32, v252, v253\n"
      "v_add3_u32 v32, v32, v254, v255\n"
      "v_add3_u32 v32, v32, v36, v37\n"
      "v_add3_u32 v32, v32, v38, v39\n"
      "s_nop 1\n"
      "v_add_u32_dpp v32, v32, v32 quad_perm:[1,0,3,2] row_mask:0xf bank_mask:0xf\n"
      "s_nop 1\n"
      "v_add_u32_dpp v32, v32, v32 quad_perm:[2,3,0,1] row_mask:0xf bank_mask:0xf\n"
      "s_nop 1\n"
      "v_add_u32_dpp v32, v32, v32 row_half_mirror row_mask:0xf bank_mask:0xf\n"
      "v_cmp_ne_u32 vcc, s72, v32\n"
      "s_sub_u32 s44, s44, 1\n"
      "s_cbranch_vccz L_bxp_%=\n"
      "s_cmp_gt_i32 s44, 0\n"
      "s_cbranch_scc1 L_sxp_%=\n"
      "s_branch L_bxp_%=\n"
      "L_sg_%=:\n"
      "buffer_load_dwordx4 v[8:11], v50, s[32:35], s93 offen offset:0 sc1\n"
      "buffer_load_dwordx4 v[12:15], v50, s[32:35], s93 offen offset:1024 sc1\n"
      "buffer_load_dwordx4 v[16:19], v50, s[32:35], s93 offen offset:2048 sc1\n"
      "buffer_load_dwordx4 v[20:23], v50, s[32:35], s93 offen offset:3072 sc1\n"
      "s_waitcnt vmcnt(0)\n"
      "v_add3_u32 v10, v10, v14, v18\n"
      "v_add_u32 v10, v10, v22\n"
      "v_cmp_ne_u32 vcc, s71, v10\n"
      "s_sub_u32 s44, s44, 1\n"
      "s_cbranch_vccz L_bg_%=\n"
      "s_cmp_gt_i32 s44, 0\n"
      "s_cbranch_scc1 L_sg_%=\n"
      "s_branch L_bg_%=\n"
      "L_shA0_%=:\n"
      "v_add_u32 v2, 4096, v7\n"
      "global_load_dwordx4 v[8:11], v2, s[10:11] offset:-4096 sc1\n"
      "global_load_dwordx4 v[16:19], v2, s[10:11] offset:-3072 sc1\n"
      "global_load_dwordx4 v[128:131], v2, s[10:11] offset:-2048 sc1\n"
      "v_add_u32 v2, 4096, v7\n"
      "global_load_dwordx4 v[12:15], v2, s[12:13] offset:-4096 sc1\n"
      "global_load_dwordx4 v[20:23], v2, s[12:13] offset:-3072 sc1\n"
      "global_load_dwordx4 v[132:135], v2, s[12:13] offset:-2048 sc1\n"
      "buffer_load_dword v36, v42, s[32:35], s63 offen offset:0 sc1\n"
      "buffer_load_dword v37, v42, s[32:35], s63 offen offset:256 sc1\n"
      "s_waitcnt vmcnt(0)\n"
      "v_add3_u32 v2, v8, v9, s30\n"
      "v_add3_u32 v2, v2, v10, v11\n"
      "v_add3_u32 v2, v2, v16, v17\n"
      "v_add3_u32 v2, v2, v18, v19\n"
      "v_add3_u32 v2, v2, v128, v129\n"
      "v_add3_u32 v2, v2, v130, v131\n"
      "v_xor_b32 v2, v2, v36\n"
      "v_add3_u32 v3, v12, v13, s30\n"
      "v_add3_u32 v3, v3, v14, v15\n"
      "v_add3_u32 v3, v3, v20, v21\n"
      "v_add3_u32 v3, v3, v22, v23\n"
      "v_add3_u32 v3, v3, v132, v133\n"
      "v_add3_u32 v3, v3, v134, v135\n"
      "v_xor_b32 v3, v3, v37\n"
      "v_or_b32 v2, v2, v3\n"
      "v_cmp_ne_u32 vcc, 0, v2\n"
      "s_sub_u32 s44, s44, 1\n"
      "s_cbranch_vccz L_bhA0_%=\n"
      "s_cmp_gt_i32 s44, 0\n"
      "s_cbranch_scc1 L_shA0_%=\n"
      "s_branch L_bhA0_%=\n"
      "L_shA1_%=:\n"
      "v_add_u32 v2, 4096, v7\n"
      "global_load_dwordx4 v[136:139], v2, s[10:11] offset:-1024 sc1\n"
      "global_load_dwordx4 v[144:147], v2, s[10:11] offset:0 sc1\n"
      "global_load_dwordx4 v[152:155], v2, s[10:11] offset:1024 sc1\n"
      "v_add_u32 v2, 4096, v7\n"
      "global_load_dwordx4 v[140:143], v2, s[12:13] offset:-1024 sc1\n"
      "global_load_dwordx4 v[148:151], v2, s[12:13] offset:0 sc1\n"
      "global_load_dwordx4 v[156:159], v2, s[12:13] offset:1024 sc1\n"
      "buffer_load_dword v38, v42, s[32:35], s63 offen offset:512 sc1\n"
      "buffer_load_dword v39, v42, s[32:35], s63 offen offset:768 sc1\n"
      "s_waitcnt vmcnt(0)\n"
      "v_add3_u32 v2, v136, v137, s30\n"
      "v_add3_u32 v2, v2, v138, v139\n"
      "v_add3_u32 v2, v2, v144, v145\n"
      "v_add3_u32 v2, v2, v146, v147\n"
      "v_add3_u32 v2, v2, v152, v153\n"
      "v_add3_u32 v2, v2, v154, v155\n"
      "v_xor_b32 v2, v2, v38\n"
      "v_add3_u32 v3, v140, v141, s30\n"
      "v_add3_u32 v3, v3, v142, v143\n"
      "v_add3_u32 v3, v3, v148, v149\n"
      "v_add3_u32 v3, v3, v150, v151\n"
      "v_add3_u32 v3, v3, v156, v157\n"
      "v_add3_u32 v3, v3, v158, v159\n"
      "v_xor_b32 v3, v3, v39\n"
      "v_or_b32 v2, v2, v3\n"
      "v_cmp_ne_u32 vcc, 0, v2\n"
      "s_sub_u32 s44, s44, 1\n"
      "s_cbranch_vccz L_bhA1_%=\n"
      "s_cmp_gt_i32 s44, 0\n"
      "s_cbranch_scc1 L_shA1_%=\n"
      "s_branch L_bhA1_%=\n"
      "L_shA2_%=:\n"
      "v_add_u32 v2, 10240, v7\n"
      "global_load_dwordx4 v[172:175], v2, s[10:11] offset:-4096 sc1\n"
      "global_load_dwordx4 v[164:167], v2, s[10:11] offset:-3072 sc1\n"
      "global_load_dwordx4 v[64:67], v2, s[10:11] offset:-2048 sc1\n"
      "v_add_u32 v2, 10240, v7\n"
      "global_load_dwordx4 v[160:163], v2, s[12:13] offset:-4096 sc1\n"
      "global_load_dwordx4 v[168:171], v2, s[12:13] offset:-3072 sc1\n"
      "global_load_dwordx4 v[68:71], v2, s[12:13] offset:-2048 sc1\n"
      "buffer_load_dword v60, v42, s[32:35], s63 offen offset:1024 sc1\n"
      "buffer_load_dword v61, v42, s[32:35], s63 offen offset:1280 sc1\n"
      "s_waitcnt vmcnt(0)\n"
      "v_add3_u32 v2, v172, v173, s30\n"
      "v_add3_u32 v2, v2, v174, v175\n"
      "v_add3_u32 v2, v2, v164, v165\n"
      "v_add3_u32 v2, v2, v166, v167\n"
      "v_add3_u32 v2, v2, v64, v65\n"
      "v_add3_u32 v2, v2, v66, v67\n"
      "v_xor_b32 v2, v2, v60\n"
      "v_add3_u32 v3, v160, v161, s30\n"
      "v_add3_u32 v3, v3, v162, v163\n"
      "v_add3_u32 v3, v3, v168, v169\n"
      "v_add3_u32 v3, v3, v170, v171\n"
      "v_add3_u32 v3, v3, v68, v69\n"
      "v_add3_u32 v3, v3, v70, v71\n"
      "v_xor_b32 v3, v3, v61\n"
      "v_or_b32 v2, v2, v3\n"
      "v_cmp_ne_u32 vcc, 0, v2\n"
      "s_sub_u32 s44, s44, 1\n"
      "s_cbranch_vccz L_bhA2_%=\n"
      "s_cmp_gt_i32 s44, 0\n"
      "s_cbranch_scc1 L_shA2_%=\n"
      "s_branch L_bhA2_%=\n"
      "L_shA3_%=:\n"
      "v_add_u32 v2, 10240, v7\n"
      "global_load_dwordx4 v[72:75], v2, s[10:11] offset:-1024 sc1\n"
      "global_load_dwordx4 v[80:83], v2, s[10:11] offset:0 sc1\n"
      "global_load_dwordx4 v[88:91], v2, s[10:11] offset:1024 sc1\n"
      "v_add_u32 v2, 10240, v7\n"
      "global_load_dwordx4 v[76:79], v2, s[12:13] offset:-1024 sc1\n"
      "global_load_dwordx4 v[84:87], v2, s[12:13] offset:0 sc1\n"
      "global_load_dwordx4 v[92:95], v2, s[12:13] offset:1024 sc1\n"
      "buffer_load_dword v62, v42, s[32:35], s63 offen offset:1536 sc1\n"
      "buffer_load_dword v63, v42, s[32:35], s63 offen offset:1792 sc1\n"
      "s_waitcnt vmcnt(0)\n"
      "v_add3_u32 v2, v72, v73, s30\n"
      "v_add3_u32 v2, v2, v74, v75\n"
      "v_add3_u32 v2, v2, v80, v81\n"
      "v_add3_u32 v2, v2, v82, v83\n"
      "v_add3_u32 v2, v2, v88, v89\n"
      "v_add3_u32 v2, v2, v90, v91\n"
      "v_xor_b32 v2, v2, v62\n"
      "v_add3_u32 v3, v76, v77, s30\n"
      "v_add3_u32 v3, v3, v78, v79\n"
      "v_add3_u32 v3, v3, v84, v85\n"
      "v_add3_u32 v3, v3, v86, v87\n"
      "v_add3_u32 v3, v3, v92, v93\n"
      "v_add3_u32 v3, v3, v94, v95\n"
      "v_xor_b32 v3, v3, v63\n"
      "v_or_b32 v2, v2, v3\n"
      "v_cmp_ne_u32 vcc, 0, v2\n"
      "s_sub_u32 s44, s44, 1\n"
      "s_cbranch_vccz L_bhA3_%=\n"
      "s_cmp_gt_i32 s44, 0\n"
      "s_cbranch_scc1 L_shA3_%=\n"
      "s_branch L_bhA3_%=\n"
      "L_shB0_%=:\n"
      "v_add_u32 v2, 102400, v7\n"
      "global_load_dwordx4 v[224:227], v2, s[10:11] offset:-4096 sc1\n"
      "global_load_dwordx4 v[228:231], v2, s[10:11] offset:-3072 sc1\n"
      "global_load_dwordx4 v[232:235], v2, s[10:11] offset:-2048 sc1\n"
      "v_add_u32 v2, 102400, v7\n"
      "global_load_dwordx4 v[236:239], v2, s[10:11] offset:-1024 sc1\n"
      "global_load_dwordx4 v[240:243], v2, s[10:11] offset:0 sc1\n"
      "global_load_dwordx4 v[244:247], v2, s[10:11] offset:1024 sc1\n"
      "buffer_load_dword v32, v42, s[32:35], s63 offen offset:2048 sc1\n"
      "buffer_load_dword v33, v42, s[32:35], s63 offen offset:2304 sc1\n"
      "s_waitcnt vmcnt(0)\n"
      "v_add3_u32 v2, v224, v225, s30\n"
      "v_add3_u32 v2, v2, v226, v227\n"
      "v_add3_u32 v2, v2, v228, v229\n"
      "v_add3_u32 v2, v2, v230, v231\n"
      "v_add3_u32 v2, v2, v232, v233\n"
      "v_add3_u32 v2, v2, v234, v235\n"
      "v_xor_b32 v2, v2, v32\n"
      "v_add3_u32 v3, v236, v237, s30\n"
      "v_add3_u32 v3, v3, v238, v239\n"
      "v_add3_u32 v3, v3, v240, v241\n"
      "v_add3_u32 v3, v3, v242, v243\n"
      "v_add3_u32 v3, v3, v244, v245\n"
      "v_add3_u32 v3, v3, v246, v247\n"
      "v_xor_b32 v3, v3, v33\n"
      "v_or_b32 v2, v2, v3\n"
      "v_cmp_ne_u32 vcc, 0, v2\n"
      "s_sub_u32 s44, s44, 1\n"
      "s_cbranch_vccz L_bhB0_%=\n"
      "s_cmp_gt_i32 s44, 0\n"
      "s_cbranch_scc1 L_shB0_%=\n"
      "s_branch L_bhB0_%=\n"
      "L_shB1_%=:\n"
      "v_add_u32 v2, 108544, v7\n"
      "global_load_dwordx4 v[8:11], v2, s[10:11] offset:-4096 sc1\n"
      "global_load_dwordx4 v[12:15], v2, s[10:11] offset:-3072 sc1\n"
      "global_load_dwordx4 v[16:19], v2, s[10:11] offset:-2048 sc1\n"
      "v_add_u32 v2, 108544, v7\n"
      "global_load_dwordx4 v[20:23], v2, s[10:11] offset:-1024 sc1\n"
      "global_load_dwordx4 v[128:131], v2, s[10:11] offset:0 sc1\n"
      "global_load_dwordx4 v[132:135], v2, s[10:11] offset:1024 sc1\n"
      "buffer_load_dword v34, v42, s[32:35], s63 offen offset:2560 sc1\n"
      "buffer_load_dword v35, v42, s[32:35], s63 offen offset:2816 sc1\n"
      "s_waitcnt vmcnt(0)\n"
      "v_add3_u32 v2, v8, v9, s30\n"
      "v_add3_u32 v2, v2, v10, v11\n"
      "v_add3_u32 v2, v2, v12, v13\n"
      "v_add3_u32 v2, v2, v14, v15\n"
      "v_add3_u32 v2, v2, v16, v17\n"
      "v_add3_u32 v2, v2, v18, v19\n"
      "v_xor_b32 v2, v2, v34\n"
      "v_add3_u32 v3, v20, v21, s30\n"
      "v_add3_u32 v3, v3, v22, v23\n"
      "v_add3_u32 v3, v3, v128, v129\n"
      "v_add3_u32 v3, v3, v130, v131\n"
      "v_add3_u32 v3, v3, v132, v133\n"
      "v_add3_u32 v3, v3, v134, v135\n"
      "v_xor_b32 v3, v3, v35\n"
      "v_or_b32 v2, v2, v3\n"
      "v_cmp_ne_u32 vcc, 0, v2\n"
      "s_sub_u32 s44, s44, 1\n"
      "s_cbranch_vccz L_bhB1_%=\n"
      "s_cmp_gt_i32 s44, 0\n"
      "s_cbranch_scc1 L_shB1_%=\n"
      "s_branch L_bhB1_%=\n"
      "L_shB2_%=:\n"
      "v_add_u32 v2, 102400, v7\n"
      "global_load_dwordx4 v[136:139], v2, s[12:13] offset:-4096 sc1\n"
      "global_load_dwordx4 v[140:143], v2, s[12:13] offset:-3072 sc1\n"
      "global_load_dwordx4 v[144:147], v2, s[12:13] offset:-2048 sc1\n"
      "v_add_u32 v2, 102400, v7\n"
      "global_load_dwordx4 v[148:151], v2, s[12:13] offset:-1024 sc1\n"
      "global_load_dwordx4 v[152:155], v2, s[12:13] offset:0 sc1\n"
      "global_load_dwordx4 v[156:159], v2, s[12:13] offset:1024 sc1\n"
      "buffer_load_dword v24, v42, s[32:35], s63 offen offset:3072 sc1\n"
      "buffer_load_dword v25, v42, s[32:35], s63 offen offset:3328 sc1\n"
      "s_waitcnt vmcnt(0)\n"
      "v_add3_u32 v2, v136, v137, s30\n"
      "v_add3_u32 v2, v2, v138, v139\n"
      "v_add3_u32 v2, v2, v140, v141\n"
      "v_add3_u32 v2, v2, v142, v143\n"
      "v_add3_u32 v2, v2, v144, v145\n"
      "v_add3_u32 v2, v2, v146, v147\n"
      "v_xor_b32 v2, v2, v24\n"
      "v_add3_u32 v3, v148, v149, s30\n"
      "v_add3_u32 v3, v3, v150, v151\n"
      "v_add3_u32 v3, v3, v152, v153\n"
      "v_add3_u32 v3, v3, v154, v155\n"
      "v_add3_u32 v3, v3, v156, v157\n"
      "v_add3_u32 v3, v3, v158, v159\n"
      "v_xor_b32 v3, v3, v25\n"
      "v_or_b32 v2, v2, v3\n"
      "v_cmp_ne_u32 vcc, 0, v2\n"
      "s_sub_u32 s44, s44, 1\n"
      "s_cbranch_vccz L_bhB2_%=\n"
      "s_cmp_gt_i32 s44, 0\n"
      "s_cbranch_scc1 L_shB2_%=\n"
      "s_branch L_bhB2_%=\n"
      "L_shB3_%=:\n"
      "v_add_u32 v2, 108544, v7\n"
      "global_load_dwordx4 v[172:175], v2, s[12:13] offset:-4096 sc1\n"
      "global_load_dwordx4 v[160:163], v2, s[12:13] offset:-3072 sc1\n"
      "global_load_dwordx4 v[164:167], v2, s[12:13] offset:-2048 sc1\n"
      "v_add_u32 v2, 108544, v7\n"
      "global_load_dwordx4 v[168:171], v2, s[12:13] offset:-1024 sc1\n"
      "global_load_dwordx4 v[64:67], v2, s[12:13] offset:0 sc1\n"
      "global_load_dwordx4 v[68:71], v2, s[12:13] offset:1024 sc1\n"
      "buffer_load_dword v26, v42, s[32:35], s63 offen offset:3584 sc1\n"
      "buffer_load_dword v27, v42, s[32:35], s63 offen offset:3840 sc1\n"
      "s_waitcnt vmcnt(0)\n"
      "v_add3_u32 v2, v172, v173, s30\n"
      "v_add3_u32 v2, v2, v174, v175\n"
      "v_add3_u32 v2, v2, v160, v161\n"
      "v_add3_u32 v2, v2, v162, v163\n"
      "v_add3_u32 v2, v2, v164, v165\n"
      "v_add3_u32 v2, v2, v166, v167\n"
      "v_xor_b32 v2, v2, v26\n"
      "v_add3_u32 v3, v168, v169, s30\n"
      "v_add3_u32 v3, v3, v170, v171\n"
      "v_add3_u32 v3, v3, v64, v65\n"
      "v_add3_u32 v3, v3, v66, v67\n"
      "v_add3_u32 v3, v3, v68, v69\n"
      "v_add3_u32 v3, v3, v70, v71\n"
      "v_xor_b32 v3, v3, v27\n"
      "v_or_b32 v2, v2, v3\n"
      "v_cmp_ne_u32 vcc, 0, v2\n"
      "s_sub_u32 s44, s44, 1\n"
      "s_cbranch_vccz L_bhB3_%=\n"
      "s_cmp_gt_i32 s44, 0\n"
      "s_cbranch_scc1 L_shB3_%=\n"
      "s_branch L_bhB3_%=\n"
      "L_sgb_%=:\n"
      "buffer_load_dwordx4 v[244:247], v53, s[32:35], s66 offen sc1\n"
      "s_waitcnt vmcnt(0)\n"
      "v_add_u32 v245, v245, v247\n"
      "v_cmp_ne_u32 vcc, s54, v245\n"
      "s_sub_u32 s44, s44, 1\n"
      "s_cbranch_vccz L_bgb_%=\n"
      "s_cmp_gt_i32 s44, 0\n"
      "s_cbranch_scc1 L_sgb_%=\n"
      "s_branch L_bgb_%=\n"
      : "+{s3}"(w), "+{v0}"(tid), "+{s[4:19]}"(s4), "+{s[20:27]}"(s20), "+{s[28:31]}"(s28), "+{s[36:39]}"(s36), "+{s[40:41]}"(tab), "+{s[52:59]}"(s52), "+{s[60:75]}"(s60),
        "+{s[76:91]}"(s76), "+{s[92:99]}"(s92), "+{v[232:235]}"(v232), "+{v1}"(v1), "+{v[192:195]}"(t[0]), "+{v[240:243]}"(t[1]), "+{v[196:199]}"(t[2]), "+{v[200:203]}"(t[3]), "+{v[204:207]}"(t[4]), "+{v[208:211]}"(t[5]), "+{v[212:215]}"(t[6]), "+{v[216:219]}"(t[7]), "+{v[220:223]}"(t[8]), "+{v[224:227]}"(t[9]), "+{v[236:239]}"(t[10]), "+{v[4:7]}"(t[11])
      : "{s2}"(blockIdx.x)
      : "memory", "scc", "vcc", "s0", "s1", "s32", "s33", "s34", "s35", "s42", "s43", "s44", "s45", "s46", "s47", "s48", "s49", "s50", "s51", "v2", "v3", "v8", "v9", "v10", "v11", "v12", "v13", "v14", "v15", "v16", "v17", "v18", "v19", "v20", "v21", "v22", "v23", "v24", "v25", "v26", "v27", "v28", "v29", "v30", "v31", "v32", "v33", "v34", "v35", "v36", "v37", "v38", "v39", "v40", "v41", "v42", "v43", "v44", "v45", "v46", "v47", "v48", "v49", "v50", "v51", "v52", "v53", "v54", "v55", "v56", "v57", "v58", "v59", "v60", "v61", "v62", "v63", "v64", "v65", "v66", "v67", "v68", "v69", "v70", "v71", "v72", "v73", "v74", "v75", "v76", "v77", "v78", "v79", "v80", "v81", "v82", "v83", "v84", "v85", "v86", "v87", "v88", "v89", "v90", "v91", "v92", "v93", "v94", "v95", "v96", "v97", "v98", "v99", "v100", "v101", "v102", "v103", "v104", "v105", "v106", "v107", "v108", "v109", "v110", "v111", "v112", "v113", "v114", "v115", "v116", "v117", "v118", "v119", "v120", "v121", "v122", "v123", "v124", "v125", "v126", "v127", "v128", "v129", "v130", "v131", "v132", "v133", "v134", "v135", "v136", "v137", "v138", "v139", "v140", "v141", "v142", "v143", "v144", "v145", "v146", "v147", "v148", "v149", "v150", "v151", "v152", "v153", "v154", "v155", "v156", "v157", "v158", "v159", "v160", "v161", "v162", "v163", "v164", "v165", "v166", "v167", "v168", "v169", "v170", "v171", "v172", "v173", "v174", "v175", "v176", "v177", "v178", "v179", "v180", "v181", "v182", "v183", "v184", "v185", "v186", "v187", "v188", "v189", "v190", "v191", "v228", "v229", "v230", "v231", "v244", "v245", "v246", "v247", "v248", "v249", "v250", "v251", "v252", "v253", "v254", "v255", "a0", "a1", "a2", "a3", "a4", "a5", "a6", "a7", "a8", "a9", "a10", "a11", "a12", "a13", "a14", "a15", "a16", "a17", "a18", "a19", "a20", "a21", "a22", "a23", "a24", "a25", "a26", "a27", "a28", "a29", "a30", "a31", "a32", "a33", "a34", "a35", "a36", "a37", "a38", "a39", "a40", "a41", "a42", "a43", "a44", "a45", "a46", "a47", "a48", "a49", "a50", "a51", "a52", "a53", "a54", "a55", "a56", "a57", "a58", "a59", "a60", "a61", "a62", "a63", "a64", "a65", "a66", "a67", "a68", "a69", "a70", "a71", "a72", "a73", "a74", "a75", "a76", "a77", "a78", "a79", "a80", "a81", "a82", "a83", "a84", "a85", "a86", "a87", "a88", "a89", "a90", "a91", "a92", "a93", "a94", "a95", "a96", "a97", "a98", "a99", "a100", "a101", "a102", "a103", "a104", "a105", "a106", "a107", "a108", "a109", "a110", "a111", "a112", "a113", "a114", "a115", "a116", "a117", "a118", "a119", "a120", "a121", "a122", "a123", "a124", "a125", "a126", "a127", "a128", "a129", "a130", "a131", "a132", "a133", "a134", "a135", "a136", "a137", "a138", "a139", "a140", "a141", "a142", "a143", "a144", "a145", "a146", "a147", "a148", "a149", "a150", "a151", "a152", "a153", "a154", "a155", "a156", "a157", "a158", "a159", "a160", "a161", "a162", "a163", "a164", "a165", "a166", "a167", "a168", "a169", "a170", "a171", "a172", "a173", "a174", "a175", "a176", "a177", "a178", "a179", "a180", "a181", "a182", "a183", "a184", "a185", "a186", "a187", "a188", "a189", "a190", "a191", "a192", "a193", "a194", "a195", "a196", "a197", "a198", "a199", "a200", "a201", "a202", "a203", "a204", "a205", "a206", "a207", "a208", "a209", "a210", "a211", "a212", "a213", "a214", "a215", "a216", "a217", "a218", "a219", "a220", "a221", "a222", "a223", "a224", "a225", "a226", "a227", "a228", "a229", "a230", "a231", "a232", "a233", "a234", "a235", "a236", "a237", "a238", "a239", "a240", "a241", "a242", "a243", "a244", "a245", "a246", "a247", "a248", "a249", "a250", "a251", "a252", "a253", "a254", "a255");
}

extern "C" void kernel_launch(void* const* d_in, const int* in_sizes, int n_in, void* d_out, int out_size,
                              void* d_ws, size_t ws_size, hipStream_t stream) {
  mega<<<256, 256, 0, stream>>>((const u16*)d_in[0], (const u16*)d_in[1], (const u16*)d_in[2], (const u16*)d_in[3],
                                (const u16*)d_in[4], (const u16*)d_in[5], (const u16*)d_in[7], (const u16*)d_in[8],
                                (const u16*)d_in[9], (const u16*)d_in[10], (const u16*)d_in[11], (char*)d_ws, (u16*)d_out);
}
```

```cpp
#include <hip/hip_runtime.h>
#include <stdint.h>

typedef unsigned short u16;
typedef __attribute__((ext_vector_type(4))) unsigned int u32x4;
typedef __attribute__((ext_vector_type(8))) unsigned int u32x8;
typedef __attribute__((ext_vector_type(16))) unsigned int u32x16;
static constexpr int D_ = 640, F_ = 1536, RS_ = 4224;

static constexpr unsigned WS_FW = 20480;
static constexpr unsigned WS_FD = WS_FW + 61440;
static constexpr unsigned WS_STAT = 147456;
static constexpr unsigned WS_PART = 262144;
static constexpr unsigned WS_MOD = 786432;
static constexpr unsigned WS_KVT = 1u << 20;
static constexpr unsigned WS_QT = 4u << 20;
static constexpr unsigned WS_X1T = 7u << 20;
static constexpr unsigned WS_CK = 2752512;
static constexpr unsigned WS_WP = 10u << 20;
static constexpr unsigned WS_WD = 14u << 20;
static constexpr unsigned WS_HH = 16u << 20;
static constexpr unsigned WS_HC = 22u << 20;
static constexpr unsigned WS_ST2 = WS_HC + (512u << 10);
static constexpr unsigned WS_X1R = 23u << 20;
static constexpr unsigned WS_DUMMY = WS_HC + (768u << 10);

static constexpr int KCP = 4160, KCH = 4 * KCP;
static constexpr int XR_CH = 7680;
static constexpr int L_XI = 3 * KCH;
static constexpr int L_EX = 72960;
static constexpr int L_KV = L_EX, KVW = 6400;
static constexpr int L_KT = L_KV + 4 * KVW;
static constexpr int L_QT = L_KT + 84 * 144;
static constexpr int L_WR = L_QT + 10240;
static constexpr int L_GW = L_WR + 16384;
static constexpr int L_GS = L_GW + 20480;
static constexpr int L_SC = L_GS + 320;
static constexpr int L_XR = 110592, L_RS = 162304;
static constexpr int L_WD1 = 122880;
static_assert(L_KT == 98560 && L_QT == 110656 && L_WR == 120896 && L_GW == 137280, "");

struct __attribute__((aligned(256))) Tab { unsigned s[1024][64], l[4][6][64][4]; };
static constexpr Tab make_tab() {
  Tab t = {};
  for (int tid = 0; tid < 256; ++tid) {
    const int lane = tid & 63, ip = lane & 15, fq = lane >> 4, q4 = ip >> 2, p4 = lane & 3;
    const int fo = (ip & 3) * KCP + 2 * ((ip & 12) + 8 * (fq & 1) + 256 * (fq >> 1)), go = L_XI + (15 - ip + 8 * (fq >> 1)) * 96 + 16 * (fq & 1);
    const int w = tid >> 6, exw = L_EX + ip * 128 + (fq ^ (ip & 7)) * 16, rowA = 8 * tid;
    t.l[w][0][lane][0] = fo + (w >> 1) * KCH + (w & 1) * 256;
    t.l[w][0][lane][1] = go + (w >> 1) * XR_CH + (w & 1) * 384;
    t.l[w][0][lane][2] = fo + 2 * KCH + w * 128;
    t.l[w][0][lane][3] = go + 2 * XR_CH + w * 192;
    t.l[w][1][lane][0] = exw + w * 8192;
    t.l[w][1][lane][1] = L_EX + 32768 + w * 1024 + (lane >> 3) * 128 + ((lane & 7) ^ (lane >> 3)) * 16;
    t.l[w][1][lane][2] = L_XI + ((2040 - rowA) >> 5) * 96 + ((2040 - rowA) & 31) * 2;
    t.l[w][1][lane][3] = L_XI + 2 * XR_CH + ((252 - 4 * lane) >> 5) * 96 + ((252 - 4 * lane) & 31) * 2;
    t.l[w][2][lane][0] = lane < 8 ? lane * 8 : 0x80000000u;
    t.l[w][2][lane][1] = (ip >> 1) == 4 * (fq & 1) ? fq * 8 + (ip & 1) * 4 : 0x80000000u;
    t.l[w][2][lane][2] = fq * D_ * 128 + q4 * 32 + p4 * 8;
    t.l[w][2][lane][3] = (lane < 40 ? lane : 39) * 64;
    t.l[w][3][lane][0] = exw + 32768 + w * 4096;
    t.l[w][3][lane][1] = (lane & 1) * 2048 + (lane >> 1) * 16;
    t.l[w][3][lane][2] = p4 * 64 + (fq == 1 ? 256 * 32 : 0) + (q4 < 3 ? q4 : 4) * 20480;
    t.l[w][3][lane][3] = (exw + w * 8192) ^ 64;
    t.l[w][4][lane][0] = (tid >> 3) * 5120 + ((tid >> 2) & 1) * 64;
    t.l[w][4][lane][1] = fq * 5120 + ((lane >> 3) & 1) * 64;
    t.l[w][4][lane][2] = (exw + 32768 + w * 4096) ^ 64;
    t.l[w][4][lane][3] = (2 * (lane >> 5) + (q4 >> 1)) * (D_ * 128) + (2 * (fq & 1) + (q4 & 1)) * 32 + p4 * 8;
    t.l[w][5][lane][0] = L_GW + w * 2048 + (4 * fq + q4) * 64 + p4 * 8;
    t.l[w][5][lane][1] = (rowA >> 6) * D_ * 128 + ((lane >> 1) & 3) * 32 + (lane & 1) * 16;
    t.l[w][5][lane][2] = ((lane >> 1) & 3) == 2 * ((lane >> 3) & 1) ? w * 128 + (lane >> 3) * 8 + (lane & 1) * 4 : 0x80000000u;
    t.l[w][5][lane][3] = L_EX + w * 2048 + (lane >> 2) * 128 + ((2 * p4) ^ ((lane >> 2) & 7)) * 16;
  }
  for (int i = 0; i < 1024; ++i) {
    const int b = i >> 2, w = i & 3, xcd = b & 7, idx = b >> 3, L = xcd * 32 + idx, side = L & 1;
    const int ct = L & 7, rt = L >> 3, srow = xcd * 256 + idx * 8;
    const int chA = 128 + L, chH = L >> 1, rH = 512 * (side + (3 - 2 * side) * (w >> 1)) + 256 * (w & 1);
    const int id = idx < 29 ? idx : 29, gj = xcd * 30 + id, j_ks = 5 * (xcd >> 1) + (id * 4 + w) / 24, j_b = 24 * (xcd & 1) + (id * 4 + w) % 24;
    const int d_ks = gj / 5, d_cb = 4 * (gj % 5) + w;
    const unsigned dummy = WS_DUMMY + (i & 63) * 1024;
    const int bm = (xcd >> 2) * 8 + (idx >> 2), bn = (xcd & 3) * 4 + (idx & 3), bm2 = (xcd >> 1) * 8 + (idx >> 2), bn2 = (xcd & 1) * 4 + (idx & 3);
    t.s[i][0] = WS_KVT + (1984 - 64 * rt) * 2 + ct * 80 * RS_;
    t.s[i][1] = WS_KVT + WS_CK + (31 - rt) * RS_ + ct * 512;
    t.s[i][2] = idx < 30 ? WS_MOD + (gj >> 2) * 2048 + (gj & 3) * 64 : dummy;
    t.s[i][3] = 32 * j_ks * (F_ * 2) + (j_b - w) * 64;
    t.s[i][4] = WS_QT + rt * 128 + ct * 80 * RS_;
    t.s[i][5] = WS_QT + WS_CK + (rt * 40 + ct * 5) * 128;
    t.s[i][6] = srow * 1280;
    t.s[i][7] = WS_STAT + (srow >> 9) * 4096 + ((srow >> 3) & 63) * 16;
    t.s[i][8] = WS_MOD + (1920 + 640 * (w >> 1) + 32 * j_ks + 16 * (w & 1)) * 32;
    t.s[i][9] = idx < 30 ? WS_PART + j_ks * 24576 + j_b * 512 : dummy;
    t.s[i][10] = WS_KVT + WS_CK + chA / 20 * 128 + chA % 20 * 4;
    t.s[i][11] = WS_KVT + WS_CK + chH / 20 * 128 + chH % 20 * 4;
    t.s[i][12] = idx < 30 ? WS_WP + (4 * j_b + 0) / 3 * 61440 + (j_ks * 3 + (4 * j_b + 0) % 3) * 1024 : dummy;
    t.s[i][13] = idx < 30 ? WS_WP + (4 * j_b + 1) / 3 * 61440 + (j_ks * 3 + (4 * j_b + 1) % 3) * 1024 : dummy;
    t.s[i][14] = idx < 30 ? WS_WP + (4 * j_b + 2) / 3 * 61440 + (j_ks * 3 + (4 * j_b + 2) % 3) * 1024 : dummy;
    t.s[i][15] = idx < 30 ? WS_WP + (4 * j_b + 3) / 3 * 61440 + (j_ks * 3 + (4 * j_b + 3) % 3) * 1024 : dummy;
    t.s[i][16] = WS_FW + (3 * (bn * 4 + w) / 4 < 46 ? 3 * (bn * 4 + w) / 4 : 46) * 1280;
    t.s[i][17] = WS_X1T + WS_CK + chH / 2 * 512 + chH % 2 * 64 + (rH >> 9) * 128 + ((rH >> 6) & 7) * 8;
    t.s[i][18] = WS_X1T + ((rH >> 6) * D_ + chH) * 128;
    t.s[i][19] = idx < 30 ? WS_FW + (j_b * 20 + j_ks) * 64 : dummy;
    t.s[i][20] = WS_QT + chH * RS_ + rH * 2;
    t.s[i][21] = WS_X1T + WS_CK + chA / 2 * 512 + chA % 2 * 64;
    t.s[i][22] = WS_STAT + (rH >> 9) * 4096 + ((rH >> 3) & 63) * 16;
    t.s[i][23] = WS_MOD + chH / 8 * 256 + chH % 8 * 8;
    t.s[i][24] = WS_QT + WS_CK + chA / 16 * 128 + chA % 16 * 4;
    t.s[i][25] = WS_QT + WS_CK + (rH >> 6) * 5120 + chH / 16 * 128 + chH % 16 * 4;
    t.s[i][26] = idx < 30 ? WS_WD + ((2 * d_cb / 5 * 48 + d_ks) * 5 + 2 * d_cb % 5) * 1024 : dummy;
    t.s[i][27] = idx < 30 ? WS_WD + (((2 * d_cb + 1) / 5 * 48 + d_ks) * 5 + (2 * d_cb + 1) % 5) * 1024 : dummy;
    t.s[i][28] = idx < 30 ? WS_FD + (d_cb * 48 + d_ks) * 64 : dummy;
    t.s[i][29] = WS_X1T + chA * 128;
    t.s[i][30] = WS_X1T + WS_CK + bm / 4 * 128 + bm % 4 * 16 + 4;
    t.s[i][31] = WS_WP + (bn * 4 + w) * 61440;
    t.s[i][32] = chA * 4096;
    t.s[i][33] = chH * 4096;
    t.s[i][34] = ((1792 - rH) >> 5) * 96;
    t.s[i][35] = (96 * (d_ks / 3) + 16 * (d_ks % 3 >> 1) + 2 * (d_ks % 3 & 1)) * (D_ * 2) + d_cb * 64;
    t.s[i][36] = (d_ks % 3 == 1 ? 30 : 18) * (D_ * 2) + ((chA / 8 * 256 + chA % 8 * 8 - chH / 8 * 256 - chH % 8 * 8) << 16);
    t.s[i][37] = ((chA >> 1) & 3) * 32;
    t.s[i][38] = WS_KVT + WS_CK + (chA + 256) / 20 * 128 + (chA + 256) % 20 * 4;
    t.s[i][39] = side;
    t.s[i][40] = WS_ST2 + (bm2 * 8 + bn2) * 1024 + w * 256;
    t.s[i][41] = WS_ST2 + (2 * bm + (w >> 1)) * 8192 + (w & 1) * 512;
    t.s[i][42] = L_XR + (w < 3 ? 20 * w : 56) * 128;
    t.s[i][43] = L_RS + (w & 1) * 512 + (w >> 1) * 256;
    t.s[i][48] = w;
    t.s[i][49] = WS_HH + (((2 * bm + (w >> 1)) * 4 + (w & 1)) * 48 + 3 * bn) * 1024;
    t.s[i][50] = WS_HC + (((2 * bm + (w >> 1)) * 4 + (bn >> 2)) * 8 + (bn & 3)) * 512 + (w & 1) * 256;
    t.s[i][51] = WS_HC + (bm2 * 4 + w) * 4096;
    t.s[i][52] = WS_FD + (((5 * bn2) >> 1) * 48 + 12 * w) * 64;
    t.s[i][53] = WS_PART + (bn * 4 + w) * 384;
    t.s[i][54] = WS_MOD + (3200 + bn2 * 80 + (w < 3 ? 16 * w : 48)) * 32;
    t.s[i][55] = WS_X1R + (bm2 * D_ + bn2 * 80 + (w < 3 ? 20 * w : 56)) * 128;
    t.s[i][56] = WS_X1T + WS_CK + (bn2 * 80 + (w < 3 ? 20 * w : 56)) * 256 + bm2 / 8 * 128 + bm2 % 8 * 8 + bm2 % 2 * 4;
    t.s[i][57] = WS_HH + (((2 * bm + (w >> 1)) * 4 + 2 + (w & 1)) * 48 + 3 * bn) * 1024;
    t.s[i][58] = WS_HC + (((2 * bm + (w >> 1)) * 4 + (bn >> 2)) * 8 + 4) * 512 + (4 * (w & 1) + (bn & 3)) * 256;
    t.s[i][59] = WS_X1T + bm * 163840;
    t.s[i][60] = WS_WD + (bn2 * 48 + 12 * w) * 5120;
    t.s[i][61] = w * 4096;
    t.s[i][62] = ((bm2 >> 1) * 128 + (bm2 & 1) * 32 + (w & 1) * 16) * (D_ * 2) + bn2 * 160 + (w >> 1) * 64;
    t.s[i][63] = WS_HH + (bm2 * 192 + 12 * w) * 1024;
  }
  return t;
}
static __device__ const Tab TAB = make_tab();

__global__ __launch_bounds__(256, 1) __attribute__((aligned(4096))) void mega(const u16* q, const u16* k, const u16* v, const u16* shift_k, const u16* ssm_k, const u16* ssm_D, const u16* w_gate,
                                               const u16* w_up, const u16* w_down, const u16* c, const u16* w_mod, char* ws, u16* out) {
  __shared__ __attribute__((aligned(16))) char lds[163840];
  int w = (int)(size_t)lds, tid = threadIdx.x, v1;
  const Tab* tab = &TAB;
  u32x16 s4, s60, s76;
  u32x8 s20, s52, s92;
  u32x4 s28, s36, v232;
  asm volatile(
      "s_load_dwordx16 s[4:19], s[0:1], 0x0\n"
      "s_load_dwordx8 s[20:27], s[0:1], 0x40\n"
      "s_load_dwordx2 s[28:29], s[0:1], 0x60\n"
      "v_readfirstlane_b32 s3, v0\n"
      "v_and_b32 v2, 63, v0\n"
      "s_lshl_b32 s42, s2, 4\n"
      "s_and_b32 s43, s2, 7\n"
      "v_mul_u32_u24 v3, 205, v2\n"
      "s_and_b32 s52, s2, 0xc0\n"
      "s_lshr_b32 s3, s3, 6\n"
      "v_lshlrev_b32 v8, 4, v2\n"
      "s_cmp_lt_u32 s3, 2\n"
      "s_cbranch_scc1 L_pf%=\n"
      "s_getpc_b64 s[54:55]\n"
      "s_and_b32 s47, s2, 0xf8\n"
      "s_and_b32 s54, s54, 0xffffff00\n"
      "s_add_u32 s56, s40, 0x40000\n"
      "s_addc_u32 s57, s41, 0\n"
      "s_lshl_b32 s47, s47, 10\n"
      "s_mov_b32 s53, 38848\n"
      "s_cmp_eq_u32 s3, 2\n"
      "s_cselect_b64 s[54:55], s[56:57], s[54:55]\n"
      "s_cmov_b32 s53, 24512\n"
      "s_cmp_gt_u32 s47, s53\n"
      "s_cbranch_scc1 L_pf%=\n"
      "v_lshl_add_u32 v9, v8, 3, s47\n"
      "s_mov_b32 m0, 160512\n"
      "v_min_u32 v9, s53, v9\n"
      "global_load_lds_dword v9, s[54:55]\n"
      "L_pf%=:\n"
      "s_lshl_b32 s47, s43, 8\n"
      "v_add_u32 v9, 13120, v3\n"
      "v_add_u32 v10, 26240, v3\n"
      "s_add_u32 s52, s52, s47\n"
      "s_lshl_b32 s47, s3, 4\n"
      "v_lshrrev_b32 v3, 11, v3\n"
      "v_lshrrev_b32 v9, 11, v9\n"
      "s_add_u32 s52, s52, s47\n"
      "s_movk_i32 s47, 1120\n"
      "v_lshrrev_b32 v10, 11, v10\n"
      "s_mulk_i32 s52, 1280\n"
      "s_bfe_u32 s54, s2, 0x30003\n"
      "v_mad_u32_u24 v232, v3, s47, v8\n"
      "v_mad_u32_u24 v233, v9, s47, v8\n"
      "v_mad_u32_u24 v234, v10, s47, v8\n"
      "s_mul_i32 s47, s54, 160\n"
      "s_mul_i32 s53, s3, 6400\n"
      "s_add_u32 s52, s52, s47\n"
      "v_subrev_u32 v233, 6656, v233\n"
      "v_subrev_u32 v234, 5632, v234\n"
      "s_sub_u32 s47, s52, 3840\n"
      "s_add_u32 s53, s53, 72960\n"
      "v_add_u32 v3, s47, v232\n"
      "s_add_u32 s47, s52, 3840\n"
      "s_add_u32 s55, s52, 7680\n"
      "s_mov_b32 s38, 2621440\n"
      "s_mov_b32 s39, 0x20000\n"
      "s_mulk_i32 s54, 640\n"
      "s_waitcnt lgkmcnt(0)\n"
      "s_mov_b32 m0, s53\n"
      "s_mov_b32 s36, s6\n"
      "s_and_b32 s37, s7, 0xffff\n"
      "buffer_load_dwordx4 v3, s[36:39], 0 offen lds\n"
      "s_add_u32 m0, s53, 1024\n"
      "s_nop 0\n"
      "buffer_load_dwordx4 v233, s[36:39], s47 offen lds\n"
      "s_add_u32 m0, s53, 2048\n"
      "v_add_u32 v9, s54, v8\n"
      "buffer_load_dwordx4 v234, s[36:39], s47 offen lds\n"
      "s_add_u32 m0, s53, 3072\n"
      "s_mov_b32 s36, s8\n"
      "s_and_b32 s37, s9, 0xffff\n"
      "buffer_load_dwordx4 v232, s[36:39], s52 offen lds\n"
      "s_add_u32 m0, s53, 4096\n"
      "s_nop 0\n"
      "buffer_load_dwordx4 v233, s[36:39], s55 offen lds\n"
      "s_add_u32 m0, s53, 5120\n"
      "s_mov_b32 exec_hi, 0\n"
      "buffer_load_dwordx4 v234, s[36:39], s55 offen lds\n"
      "s_add_u32 m0, s53, 5632\n"
      "s_mov_b32 exec_hi, 0xff\n"
      "global_load_lds_dwordx4 v9, s[10:11]\n"
      "s_mov_b32 exec_hi, -1\n"
      "s_load_dwordx2 s[30:31], s[26:27], 0x0\n"
      "s_lshl_b32 s51, s3, 2\n"
      "s_add_u32 s42, s42, s51\n"
      "s_lshl_b32 s42, s42, 6\n"
      "s_add_u32 s56, s40, s42\n"
      "s_addc_u32 s57, s41, 0\n"
      "s_load_dwordx16 s[60:75], s[56:57], 0x0\n"
      "s_load_dwordx16 s[76:91], s[56:57], 0x40\n"
      "s_load_dwordx8 s[92:99], s[56:57], 0x80\n"
      : "+{s3}"(w), "+{v0}"(tid), "=&{s[4:19]}"(s4), "=&{s[20:27]}"(s20), "=&{s[28:31]}"(s28), "=&{s[36:39]}"(s36), "=&{s[52:59]}"(s52), "=&{s[60:75]}"(s60), "=&{s[76:91]}"(s76),
        "=&{s[92:99]}"(s92), "=&{v[232:235]}"(v232), "=&{v1}"(v1)
      : "{s[0:1]}"(__builtin_amdgcn_kernarg_segment_ptr()), "{s2}"(blockIdx.x), "{s[40:41]}"(tab)
      : "memory", "scc", "s42", "s43", "s47", "s48", "s49", "s50", "s51", "v2", "v3", "v8", "v9", "v10", "v11", "v12", "v13");
  u32x4 t[12];
  const int lane = tid & 63, ip = lane & 15, fq = lane >> 4, q4 = ip >> 2, p4 = lane & 3;
  const int ck = 40 * w + (lane < 40 ? lane : 39), z7 = ((tid < 84 ? tid : 83) * 37) >> 8;
  const bool dg = fq == q4;
  const unsigned msk = dg ? ((ip & 1) ? 0xFFFF0000u : 0xFFFFu) : 0u, mk0 = (ip & 2) ? 0u : msk, mk1 = (ip & 2) ? msk : 0u;
  t[0][0] = dg && p4 == 0 ? 0x3F803F80u : 0u;
  t[0][1] = dg && p4 == 1 ? 0x3F803F80u : 0u;
  t[0][2] = dg && p4 == 2 ? 0x3F803F80u : 0u;
  t[0][3] = dg && p4 == 3 ? 0x3F803F80u : 0u;
  t[1][0] = (mk0 & 0x0D0C0D0Cu) ^ 0x0C0C0C0Cu;
  t[1][1] = (mk1 & 0x0D0C0D0Cu) ^ 0x0C0C0C0Cu;
  t[1][2] = (mk0 & 0x0F0E0F0Eu) ^ 0x0C0C0C0Cu;
  t[1][3] = (mk1 & 0x0F0E0F0Eu) ^ 0x0C0C0C0Cu;
  t[2][0] = L_KV + w * KVW + ip * 160 + fq * 8;
  t[2][1] = L_KV + w * KVW + 5632 + ip * 8;
  t[2][2] = mk0;
  t[2][3] = mk1;
  t[3][0] = L_KT + ip * 144 + (60 - 16 * w - 4 * fq) * 2;
  t[3][1] = L_KT + (20 * w + (lane >> 3)) * 144 + (lane & 7) * 16;
  t[3][2] = (20 * w + (lane >> 3)) * RS_ + (lane & 7) * 16;
  t[3][3] = lane & 4 ? 0x80000000u : w * 128 + (8 * (lane & 7) + (lane >> 3)) * 4;
  t[4][0] = L_GS + ((ck & ~31) + ((ck >> 2) & 3) * 8 + ((ck >> 4) & 1) * 4 + (ck & 3)) * 2;
  t[4][1] = L_GW + (4 * fq + q4) * 128 + p4 * 8 + ((w ^ (2 * fq + (q4 >> 1))) & 3) * 32;
  t[4][2] = L_GS + fq * 16;
  t[4][3] = lane < 16 ? (2 * w + (lane >> 3)) * 256 + (lane & 7) * 8 : 0x80000000u;
  t[5][0] = (lane & 12) == 4 ? 1 << 20 : 0;
  t[5][1] = lane & 8 ? 0u : ~0u;
  t[5][2] = (8 * w + fq) * (F_ * 2) + (ip ^ (2 * fq)) * 16;
  t[5][3] = (2 * w + (lane >> 5)) * 1280 + (lane & 31) * 16;
  t[6][0] = p4 * 64 + (lane >> 5) * 256 + ((lane >> 2) & 7) * 8;
  t[6][1] = L_WR + (fq >> 1) * 4096 + (fq & 1) * 1024 + q4 * 256 + (((2 * w + 1) ^ q4) & 7) * 32 + p4 * 8;
  t[6][2] = lane == 0 ? w * 1024 : 0x80000000u;
  t[6][3] = ((fq & 1 ? w * 64 + 512 : fq * 32 + 128 * w) + ip * 4) | ((unsigned)(fq & (fq >> 1 | w >> 1)) << 31);
  t[7][0] = (ip < 8 ? ip : 8 + p4) * KCP + 56;
  t[7][1] = z7 * (KCP - 56) + (tid < 84 ? tid : 83) * 8;
  t[7][2] = L_XI + 64 * 96 + (tid < 90 ? tid : 89) * 16;
  t[7][3] = L_WR + (fq >> 1) * 4096 + (fq & 1) * 1024 + q4 * 256 + (((2 * w) ^ q4) & 7) * 32 + p4 * 8;
  t[8][0] = L_SC + w * 32 + (lane >> 2) * 2;
  t[8][1] = (2 * (tid >> 4) + ((tid >> 2) & 1)) * (D_ * 128) + (2 * ((tid >> 3) & 1) + ((tid >> 1) & 1)) * 32 + (tid & 1) * 16;
  t[8][2] = (4 * fq + ((lane >> 2) & 1)) * (D_ * 2) + p4 * 16;
  t[8][3] = ((lane >> 3) ^ (lane >> 4)) & 1;
  t[9][0] = L_QT + (8 * fq + q4) * 160 + p4 * 8 + w * 32;
  t[9][1] = L_QT + (8 * fq + q4) * 160 + p4 * 8 + 128 + (w & 1) * 5120;
  t[9][2] = (16 * w + ip) * RS_ + fq * 16;
  t[9][3] = w < 2 ? (64 + ip) * RS_ + fq * 16 + w * 64 : 0x80000000u;
  t[10][0] = L_SC + (ip & 4 ? 0 : 64) + fq * 8;
  t[10][1] = (tid >> 3) * RS_;
  t[10][2] = L_XI + (tid >> 2) * 96 + (tid & 3) * 16;
  t[10][3] = (fq >> 1) * 256 + (fq & 1) * 64 + ip * 4;
  t[11][0] = lane * 8;
  t[11][1] = lane & 4 ? 0x80000000u : (16 * w + 4 * (lane & 7) + fq) * 512 + ((lane >> 3) & 1) * 64;
  t[11][2] = (4 * fq + q4) * 128 + ((2 * fq + (q4 >> 1)) & 3) * 32 + p4 * 8;
  t[11][3] = lane * 16;
  asm volatile(
      "v_and_b32 v2, 63, v0\n"
      "s_and_b32 s43, s2, 7\n"
      "v_mul_u32_u24 v3, 205, v2\n"
      "v_lshlrev_b32 v8, 4, v2\n"
      "s_lshr_b32 s48, s2, 3\n"
      "s_min_u32 s48, s48, 29\n"
      "s_mul_i32 s49, s43, 30\n"
      "v_lshrrev_b32 v12, 3, v2\n"
      "s_add_u32 s48, s48, s49\n"
      "v_xor_b32 v13, v12, v2\n"
      "s_and_b32 s49, s48, 3\n"
      "v_and_b32 v11, 1, v2\n"
      "s_lshl_b32 s49, s49, 2\n"
      "v_and_or_b32 v13, v13, 6, v11\n"
      "s_add_u32 s49, s49, s3\n"
      "v_mul_u32_u24 v12, 7680, v12\n"
      "s_lshr_b32 s48, s48, 2\n"
      "s_mul_i32 s50, s49, 307200\n"
      "s_lshl_b32 s48, s48, 7\n"
      "v_lshl_add_u32 v12, v13, 4, v12\n"
      "s_add_u32 s48, s48, s50\n"
      "s_mul_i32 s50, s3, 5120\n"
      "s_mulk_i32 s49, 80\n"
      "v_min_u32 v13, 39, v2\n"
      "v_add_u32 v12, s48, v12\n"
      "v_lshl_add_u32 v13, v13, 1, s49\n"
      "s_add_u32 m0, s50, 137280\n"
      "global_load_ushort v235, v13, s[22:23]\n"
      "global_load_lds_dwordx4 v12, s[24:25]\n"
      "s_add_u32 m0, m0, 1024\n"
      "v_add_u32 v12, 61440, v12\n"
      "global_load_lds_dwordx4 v12, s[24:25]\n"
      "s_add_u32 m0, m0, 1024\n"
      "v_add_u32 v12, 61440, v12\n"
      "global_load_lds_dwordx4 v12, s[24:25]\n"
      "s_add_u32 m0, m0, 1024\n"
      "v_add_u32 v12, 61440, v12\n"
      "global_load_lds_dwordx4 v12, s[24:25]\n"
      "s_add_u32 m0, m0, 1024\n"
      "v_add_u32 v12, 61440, v12\n"
      "global_load_lds_dwordx4 v12, s[24:25]\n"
      "s_mov_b32 s32, s26\n"
      "s_and_b32 s33, s27, 0xffff\n"
      "s_mov_b32 s34, 27262976\n"
      "s_mov_b32 s35, 0x20000\n"
      "v_lshlrev_b32 v54, 4, v0\n"
      "s_sub_u32 s18, s18, 2048\n"
      "s_subb_u32 s19, s19, 0\n"
      "v_mov_b32 v66, 0\n"
      "v_mov_b32 v67, 0\n"
      "v_mov_b32 v72, 0\n"
      "v_mov_b32 v73, 0\n"
      "v_mov_b32 v76, 0\n"
      "v_mov_b32 v77, 0\n"
      "v_mov_b32 v82, 0\n"
      "v_mov_b32 v83, 0\n"
      "v_mov_b32 v88, 0\n"
      "v_mov_b32 v89, 0\n"
      "v_mov_b32 v92, 0\n"
      "v_mov_b32 v93, 0\n"
      "v_mov_b32 v98, 0\n"
      "v_mov_b32 v99, 0\n"
      "v_mov_b32 v104, 0\n"
      "v_mov_b32 v105, 0\n"
      "v_mov_b32 v108, 0\n"
      "v_mov_b32 v109, 0\n"
      "v_mov_b32 v114, 0\n"
      "v_mov_b32 v115, 0\n"
      "v_mov_b32 v120, 0\n"
      "v_mov_b32 v121, 0\n"
      "v_mov_b32 v124, 0\n"
      "v_mov_b32 v125, 0\n"
      "v_mov_b32 v34, 0\n"
      "v_mov_b32 v35, 0\n"
      "v_and_b32 v32, 0x3f803f80, v198\n"
      "v_and_b32 v33, 0x3f803f80, v199\n"
      "v_and_b32 v36, 0x3f803f80, v198\n"
      "v_and_b32 v37, 0x3f803f80, v199\n"
      "s_mov_b32 s48, 0xaaaaaaaa\n"
      "s_mov_b32 s49, s48\n"
      "s_mov_b32 s50, 0xcccccccc\n"
      "s_mov_b32 s51, s50\n"
      "v_mov_b32 v128, 0\n"
      "v_mov_b32 v129, 0\n"
      "v_mov_b32 v130, 0\n"
      "v_mov_b32 v131, 0\n"
      "s_mov_b32 s44, 0x40000\n"
      "s_mov_b32 s46, 0\n"
      "s_mul_i32 s42, s3, 6144\n"
      "s_add_u32 s42, s42, 0x41000\n"
      "s_add_u32 s0, s40, s42\n"
      "s_addc_u32 s1, s41, 0\n"
      "ds_write_b64 v217, v[128:129]\n"
      "ds_write_b128 v218, v[128:131] offset:0\n"
      "ds_write_b128 v218, v[128:131] offset:7680\n"
      "ds_write_b128 v218, v[128:131] offset:15360\n"
      "s_waitcnt lgkmcnt(0)\n"
      "s_add_u32 s30, s30, 1\n"
      "s_addc_u32 s31, s31, 0\n"
      "v_mov_b32 v52, s30\n"
      "v_mov_b32 v53, s31\n"
      "s_and_b32 s42, s96, 0xffff\n"
      "s_lshl_b32 s43, s3, 11\n"
      "v_mad_u32_u24 v222, v223, s42, v222\n"
      "s_cmp_eq_u32 s3, 3\n"
      "s_cmov_b32 s43, 7680\n"
      "v_add_u32 v222, s95, v222\n"
      "s_add_u32 s43, s43, 49920\n"
      "s_mov_b32 s36, s12\n"
      "s_and_b32 s37, s13, 0xffff\n"
      "s_add_u32 s40, s60, 33792\n"
      "s_add_u32 s41, s60, 67584\n"
      "s_add_u32 s47, s92, 0x100000\n"
      "s_sub_u32 s54, s92, s93\n"
      "s_lshl_b32 s45, s3, 12\n"
      "v_and_b32 v39, s54, v209\n"
      "v_add_u32 v210, s63, v210\n"
      "v_add_u32 v208, v39, v208\n"
      "v_add_u32 v39, 11264, v210\n"
      "s_waitcnt vmcnt(6)\n"
      "ds_read_b64 v[244:245], v197 offset:0\n"
      "ds_read_b64 v[246:247], v197 offset:128\n"
      "ds_read_b64 v[248:249], v197 offset:256\n"
      "ds_read_b64 v[250:251], v197 offset:384\n"
      "ds_read_b64 v[252:253], v197 offset:512\n"
      "ds_read_b64 a[0:1], v196 offset:0\n"
      "ds_read_b64 a[2:3], v196 offset:32\n"
      "ds_read_b64 a[4:5], v196 offset:64\n"
      "ds_read_b64 a[6:7], v196 offset:96\n"
      "ds_read_b64 a[8:9], v196 offset:128\n"
      "ds_read_b64 a[10:11], v196 offset:160\n"
      "ds_read_b64 a[12:13], v196 offset:192\n"
      "ds_read_b64 a[14:15], v196 offset:224\n"
      "ds_read_b64 a[16:17], v196 offset:256\n"
      "ds_read_b64 a[18:19], v196 offset:288\n"
      "s_waitcnt lgkmcnt(10)\n"
      "ds_read_b64 a[20:21], v196 offset:320\n"
      "ds_read_b64 a[22:23], v196 offset:352\n"
      "ds_read_b64 a[24:25], v196 offset:384\n"
      "v_perm_b32 v64, v245, v245, v242\n"
      "v_perm_b32 v65, v245, v245, v243\n"
      "v_perm_b32 v80, v245, v245, v240\n"
      "v_perm_b32 v81, v245, v245, v241\n"
      "ds_read_b64 a[26:27], v196 offset:416\n"
      "ds_read_b64 a[28:29], v196 offset:448\n"
      "ds_read_b64 a[30:31], v196 offset:480\n"
      "v_perm_b32 v68, v247, v247, v242\n"
      "v_perm_b32 v69, v247, v247, v243\n"
      "v_perm_b32 v84, v247, v247, v240\n"
      "v_perm_b32 v85, v247, v247, v241\n"
      "ds_read_b64 a[32:33], v196 offset:512\n"
      "ds_read_b64 a[34:35], v196 offset:544\n"
      "ds_read_b64 a[36:37], v196 offset:576\n"
      "v_perm_b32 v70, v249, v249, v242\n"
      "v_perm_b32 v71, v249, v249, v243\n"
      "v_perm_b32 v86, v249, v249, v240\n"
      "v_perm_b32 v87, v249, v249, v241\n"
      "ds_read_b64 a[38:39], v196 offset:608\n"
      "ds_read_b64 a[40:41], v196 offset:3072\n"
      "ds_read_b64 a[42:43], v196 offset:3104\n"
      "v_perm_b32 v74, v251, v251, v242\n"
      "v_perm_b32 v75, v251, v251, v243\n"
      "v_perm_b32 v90, v251, v251, v240\n"
      "v_perm_b32 v91, v251, v251, v241\n"
      "ds_read_b64 a[44:45], v196 offset:3136\n"
      "ds_read_b64 a[46:47], v196 offset:3168\n"
      "ds_read_b64 a[48:49], v196 offset:3200\n"
      "v_perm_b32 v78, v253, v253, v242\n"
      "v_perm_b32 v79, v253, v253, v243\n"
      "v_perm_b32 v94, v253, v253, v240\n"
      "v_perm_b32 v95, v253, v253, v241\n"
      "s_waitcnt lgkmcnt(14)\n"
      "v_mfma_f32_16x16x32_bf16 v[128:131], a[0:3], v[64:67], 0\n"
      "v_perm_b32 v96, v244, v244, v242\n"
      "v_perm_b32 v97, v244, v244, v243\n"
      "v_mfma_f32_16x16x32_bf16 v[132:135], a[10:13], v[80:83], 0\n"
      "buffer_load_dwordx4 v[8:11], v54, s[36:39], s92 offen\n"
      "v_perm_b32 v112, v244, v244, v240\n"
      "v_perm_b32 v113, v244, v244, v241\n"
      "v_mfma_f32_16x16x32_bf16 v[140:143], a[0:3], v[66:69], 0\n"
      "v_perm_b32 v100, v246, v246, v242\n"
      "v_perm_b32 v101, v246, v246, v243\n"
      "v_mfma_f32_16x16x32_bf16 v[144:147], a[10:13], v[82:85], 0\n"
      "buffer_load_dwordx4 v[14:17], v54, s[36:39], s47 offen\n"
      "v_perm_b32 v116, v246, v246, v240\n"
      "v_perm_b32 v117, v246, v246, v241\n"
      "v_mfma_f32_16x16x32_bf16 v[152:155], a[4:7], v[70:73], 0\n"
      "v_perm_b32 v102, v248, v248, v242\n"
      "v_perm_b32 v103, v248, v248, v243\n"
      "v_mfma_f32_16x16x32_bf16 v[156:159], a[14:17], v[86:89], 0\n"
      "buffer_load_dwordx4 v[20:23], v54, s[36:39], s93 offen\n"
      "v_perm_b32 v118, v248, v248, v240\n"
      "v_perm_b32 v119, v248, v248, v241\n"
      "v_mfma_f32_16x16x32_bf16 v[164:167], a[4:7], v[72:75], 0\n"
      "v_perm_b32 v106, v250, v250, v242\n"
      "v_perm_b32 v107, v250, v250, v243\n"
      "v_mfma_f32_16x16x32_bf16 v[168:171], a[14:17], v[88:91], 0\n"
      "buffer_load_dwordx2 v[26:27], v208, s[36:39], s93 offen\n"
      "v_perm_b32 v122, v250, v250, v240\n"
      "v_perm_b32 v123, v250, v250, v241\n"
      "v_mfma_f32_16x16x32_bf16 v[176:179], a[6:9], v[76:79], 0\n"
      "v_perm_b32 v110, v252, v252, v242\n"
      "v_perm_b32 v111, v252, v252, v243\n"
      "v_mfma_f32_16x16x32_bf16 v[180:183], a[16:19], v[92:95], 0\n"
      "v_perm_b32 v126, v252, v252, v240\n"
      "v_perm_b32 v127, v252, v252, v241\n"
      "s_waitcnt lgkmcnt(0)\n"
      "v_cvt_pk_bf16_f32 v136, v128, v132\n"
      "v_cvt_pk_bf16_f32 v137, v129, v133\n"
      "v_cvt_pk_bf16_f32 v138, v130, v134\n"
      "v_cvt_pk_bf16_f32 v139, v131, v135\n"
      "v_cvt_pk_bf16_f32 v148, v140, v144\n"
      "v_cvt_pk_bf16_f32 v149, v141, v145\n"
      "v_mfma_f32_16x16x32_bf16 v[128:131], v[192:195], v[136:139], 0\n"
      "v_cvt_pk_bf16_f32 v150, v142, v146\n"
      "v_cvt_pk_bf16_f32 v151, v143, v147\n"
      "v_mfma_f32_16x16x32_bf16 v[132:135], a[20:23], v[96:99], 0\n"
      "s_add_u32 m0, s45, 120896\n"
      "s_nop 0\n"
      "global_load_lds_dwordx4 v210, s[16:17]\n"
      "v_cvt_pk_bf16_f32 v160, v152, v156\n"
      "v_cvt_pk_bf16_f32 v161, v153, v157\n"
      "v_mfma_f32_16x16x32_bf16 v[140:143], v[192:195], v[148:151], 0\n"
      "v_cvt_pk_bf16_f32 v162, v154, v158\n"
      "v_cvt_pk_bf16_f32 v163, v155, v159\n"
      "v_mfma_f32_16x16x32_bf16 v[144:147], a[20:23], v[98:101], 0\n"
      "global_load_lds_dwordx4 v39, s[16:17] offset:1024\n"
      "v_cvt_pk_bf16_f32 v172, v164, v168\n"
      "v_cvt_pk_bf16_f32 v173, v165, v169\n"
      "v_mfma_f32_16x16x32_bf16 v[152:155], v[192:195], v[160:163], 0\n"
      "v_cvt_pk_bf16_f32 v174, v166, v170\n"
      "v_cvt_pk_bf16_f32 v175, v167, v171\n"
      "v_mfma_f32_16x16x32_bf16 v[156:159], a[24:27], v[102:105], 0\n"
      "global_load_lds_dwordx4 v210, s[18:19] offset:2048\n"
      "v_cvt_pk_bf16_f32 v184, v176, v180\n"
      "v_cvt_pk_bf16_f32 v185, v177, v181\n"
      "v_mfma_f32_16x16x32_bf16 v[164:167], v[192:195], v[172:175], 0\n"
      "v_cvt_pk_bf16_f32 v186, v178, v182\n"
      "v_cvt_pk_bf16_f32 v187, v179, v183\n"
      "v_mfma_f32_16x16x32_bf16 v[168:171], a[24:27], v[104:107], 0\n"
      "global_load_lds_dwordx4 v39, s[18:19] offset:3072\n"
      "v_cvt_pk_bf16_f32 v136, v128, v132\n"
      "v_cvt_pk_bf16_f32 v137, v129, v133\n"
      "v_mfma_f32_16x16x32_bf16 v[176:179], v[192:195], v[184:187], 0\n"
      "v_cvt_pk_bf16_f32 v138, v130, v134\n"
      "v_cvt_pk_bf16_f32 v139, v131, v135\n"
      "v_mfma_f32_16x16x32_bf16 v[180:183], a[26:29], v[108:111], 0\n"
      "v_cvt_pk_bf16_f32 v148, v140, v144\n"
      "v_cvt_pk_bf16_f32 v149, v141, v145\n"
      "v_mfma_f32_16x16x32_bf16 v[128:131], v[192:195], v[136:139], 0\n"
      "v_cvt_pk_bf16_f32 v150, v142, v146\n"
      "v_cvt_pk_bf16_f32 v151, v143, v147\n"
      "v_mfma_f32_16x16x32_bf16 v[132:135], a[30:33], v[112:115], 0\n"
      "v_cvt_pk_bf16_f32 v160, v152, v156\n"
      "v_cvt_pk_bf16_f32 v161, v153, v157\n"
      "v_mfma_f32_16x16x32_bf16 v[140:143], v[192:195], v[148:151], 0\n"
      "v_cvt_pk_bf16_f32 v162, v154, v158\n"
      "v_cvt_pk_bf16_f32 v163, v155, v159\n"
      "v_mfma_f32_16x16x32_bf16 v[144:147], a[30:33], v[114:117], 0\n"
      "v_cvt_pk_bf16_f32 v172, v164, v168\n"
      "v_cvt_pk_bf16_f32 v173, v165, v169\n"
      "v_mfma_f32_16x16x32_bf16 v[152:155], v[192:195], v[160:163], 0\n"
      "v_cvt_pk_bf16_f32 v174, v166, v170\n"
      "v_cvt_pk_bf16_f32 v175, v167, v171\n"
      "v_mfma_f32_16x16x32_bf16 v[156:159], a[34:37], v[118:121], 0\n"
      "v_cvt_pk_bf16_f32 v184, v176, v180\n"
      "v_cvt_pk_bf16_f32 v185, v177, v181\n"
      "v_mfma_f32_16x16x32_bf16 v[164:167], v[192:195], v[172:175], 0\n"
      "v_cvt_pk_bf16_f32 v186, v178, v182\n"
      "v_cvt_pk_bf16_f32 v187, v179, v183\n"
      "v_mfma_f32_16x16x32_bf16 v[168:171], a[34:37], v[120:123], 0\n"
      "v_cvt_pk_bf16_f32 v136, v128, v132\n"
      "v_cvt_pk_bf16_f32 v137, v129, v133\n"
      "v_mfma_f32_16x16x32_bf16 v[176:179], v[192:195], v[184:187], 0\n"
      "v_cvt_pk_bf16_f32 v138, v130, v134\n"
      "v_cvt_pk_bf16_f32 v139, v131, v135\n"
      "v_mfma_f32_16x16x32_bf16 v[180:183], a[36:39], v[124:127], 0\n"
      "v_cvt_pk_bf16_f32 v148, v140, v144\n"
      "v_cvt_pk_bf16_f32 v149, v141, v145\n"
      "v_mfma_f32_16x16x32_bf16 v[128:131], v[192:195], v[136:139], 0\n"
      "v_cvt_pk_bf16_f32 v150, v142, v146\n"
      "v_cvt_pk_bf16_f32 v151, v143, v147\n"
      "v_mfma_f32_16x16x32_bf16 v[132:135], a[40:43], v[32:35], 0\n"
      "v_cvt_pk_bf16_f32 v160, v152, v156\n"
      "v_cvt_pk_bf16_f32 v161, v153, v157\n"
      "v_mfma_f32_16x16x32_bf16 v[140:143], v[192:195], v[148:151], 0\n"
      "v_cvt_pk_bf16_f32 v162, v154, v158\n"
      "v_cvt_pk_bf16_f32 v163, v155, v159\n"
      "v_mfma_f32_16x16x32_bf16 v[144:147], a[40:43], v[34:37], 0\n"
      "v_cvt_pk_bf16_f32 v172, v164, v168\n"
      "v_cvt_pk_bf16_f32 v173, v165, v169\n"
      "v_mfma_f32_16x16x32_bf16 v[152:155], v[192:195], v[160:163], 0\n"
      "v_cvt_pk_bf16_f32 v174, v166, v170\n"
      "v_cvt_pk_bf16_f32 v175, v167, v171\n"
      "v_mfma_f32_16x16x32_bf16 v[156:159], a[44:47], v[32:35], 0\n"
      "v_cvt_pk_bf16_f32 v184, v176, v180\n"
      "v_cvt_pk_bf16_f32 v185, v177, v181\n"
      "v_mfma_f32_16x16x32_bf16 v[164:167], v[192:195], v[172:175], 0\n"
      "v_cvt_pk_bf16_f32 v186, v178, v182\n"
      "v_cvt_pk_bf16_f32 v187, v179, v183\n"
      "v_mfma_f32_16x16x32_bf16 v[168:171], a[44:47], v[34:37], 0\n"
      "v_cvt_pk_bf16_f32 v128, 0, v128\n"
      "v_cvt_pk_bf16_f32 v129, 0, v129\n"
      "L_pg0_%=:\n"
      "s_cbranch_execz L_pg4_%=\n"
      "v_mfma_f32_16x16x32_bf16 v[176:179], v[192:195], v[184:187], 0\n"
      "v_cvt_pk_bf16_f32 v130, 0, v130\n"
      "v_cvt_pk_bf16_f32 v131, 0, v131\n"
      "v_mfma_f32_16x16x32_bf16 v[180:183], a[46:49], v[34:37], 0\n"
      "v_mul_f32 v128, v128, v132\n"
      "v_mul_f32 v129, v129, v133\n"
      "v_mul_f32 v130, v130, v134\n"
      "v_mul_f32 v131, v131, v135\n"
      "v_cvt_pk_bf16_f32 v136, v131, v130\n"
      "v_cvt_pk_bf16_f32 v137, v129, v128\n"
      "v_cvt_pk_bf16_f32 v140, 0, v140\n"
      "v_cvt_pk_bf16_f32 v141, 0, v141\n"
      "v_cvt_pk_bf16_f32 v142, 0, v142\n"
      "v_cvt_pk_bf16_f32 v143, 0, v143\n"
      "ds_write_b64 v200, v[136:137] offset:0\n"
      "v_mul_f32 v140, v140, v144\n"
      "v_mul_f32 v141, v141, v145\n"
      "v_mul_f32 v142, v142, v146\n"
      "v_mul_f32 v143, v143, v147\n"
      "v_cvt_pk_bf16_f32 v148, v143, v142\n"
      "v_cvt_pk_bf16_f32 v149, v141, v140\n"
      "v_cvt_pk_bf16_f32 v152, 0, v152\n"
      "v_cvt_pk_bf16_f32 v153, 0, v153\n"
      "v_cvt_pk_bf16_f32 v154, 0, v154\n"
      "v_cvt_pk_bf16_f32 v155, 0, v155\n"
      "ds_write_b64 v200, v[148:149] offset:2304\n"
      "v_mul_f32 v152, v152, v156\n"
      "v_mul_f32 v153, v153, v157\n"
      "v_mul_f32 v154, v154, v158\n"
      "v_mul_f32 v155, v155, v159\n"
      "v_cvt_pk_bf16_f32 v160, v155, v154\n"
      "v_cvt_pk_bf16_f32 v161, v153, v152\n"
      "v_cvt_pk_bf16_f32 v164, 0, v164\n"
      "v_cvt_pk_bf16_f32 v165, 0, v165\n"
      "v_cvt_pk_bf16_f32 v166, 0, v166\n"
      "v_cvt_pk_bf16_f32 v167, 0, v167\n"
      "ds_write_b64 v200, v[160:161] offset:4608\n"
      "v_mul_f32 v164, v164, v168\n"
      "v_mul_f32 v165, v165, v169\n"
      "v_mul_f32 v166, v166, v170\n"
      "v_mul_f32 v167, v167, v171\n"
      "v_cvt_pk_bf16_f32 v172, v167, v166\n"
      "v_cvt_pk_bf16_f32 v173, v165, v164\n"
      "v_cvt_pk_bf16_f32 v176, 0, v176\n"
      "v_cvt_pk_bf16_f32 v177, 0, v177\n"
      "v_cvt_pk_bf16_f32 v178, 0, v178\n"
      "v_cvt_pk_bf16_f32 v179, 0, v179\n"
      "ds_write_b64 v200, v[172:173] offset:6912\n"
      "v_mul_f32 v176, v176, v180\n"
      "v_mul_f32 v177, v177, v181\n"
      "v_mul_f32 v178, v178, v182\n"
      "v_mul_f32 v179, v179, v183\n"
      "v_cvt_pk_bf16_f32 v184, v179, v178\n"
      "v_cvt_pk_bf16_f32 v185, v177, v176\n"
      "ds_write_b64 v200, v[184:185] offset:9216\n"
      "s_waitcnt lgkmcnt(0)\n"
      "s_barrier\n"
      "ds_read_b128 v[64:67], v201 offset:0\n"
      "ds_read_b128 v[68:71], v201 offset:1152\n"
      "ds_read_b128 v[72:75], v201 offset:2304\n"
      "s_waitcnt lgkmcnt(2)\n"
      "buffer_store_dwordx4 v[64:67], v202, s[32:35], s60 offen offset:0 sc1\n"
      "v_add3_u32 v76, v64, v65, v66\n"
      "v_add_u32 v76, v76, v67\n"
      "s_waitcnt lgkmcnt(1)\n"
      "buffer_store_dwordx4 v[68:71], v202, s[32:35], s40 offen offset:0 sc1\n"
      "v_add3_u32 v77, v68, v69, v70\n"
      "v_add_u32 v77, v77, v71\n"
      "s_waitcnt lgkmcnt(0)\n"
      "s_mov_b32 exec_hi, 0\n"
      "buffer_store_dwordx4 v[72:75], v202, s[32:35], s41 offen offset:0 sc1\n"
      "s_mov_b32 exec_hi, -1\n"
      "v_add3_u32 v78, v72, v73, v74\n"
      "v_add_u32 v78, v78, v75\n"
      "v_add_u32_dpp v76, v76, v76 quad_perm:[1,0,3,2] row_mask:0xf bank_mask:0xf\n"
      "v_add_u32_dpp v77, v77, v77 quad_perm:[1,0,3,2] row_mask:0xf bank_mask:0xf\n"
      "v_add_u32_dpp v78, v78, v78 quad_perm:[1,0,3,2] row_mask:0xf bank_mask:0xf\n"
      "v_add_u32_dpp v76, v76, v76 quad_perm:[2,3,0,1] row_mask:0xf bank_mask:0xf\n"
      "v_add_u32_dpp v77, v77, v77 quad_perm:[2,3,0,1] row_mask:0xf bank_mask:0xf\n"
      "v_add_u32_dpp v78, v78, v78 quad_perm:[2,3,0,1] row_mask:0xf bank_mask:0xf\n"
      "v_add_u32_dpp v76, v76, v76 row_half_mirror row_mask:0xf bank_mask:0xf\n"
      "v_add_u32_dpp v77, v77, v77 row_half_mirror row_mask:0xf bank_mask:0xf\n"
      "v_add_u32_dpp v78, v78, v78 row_half_mirror row_mask:0xf bank_mask:0xf\n"
      "v_cndmask_b32 v76, v76, v77, s[48:49]\n"
      "v_cndmask_b32 v76, v76, v78, s[50:51]\n"
      "v_sub_u32 v76, s30, v76\n"
      "buffer_store_dword v76, v203, s[32:35], s61 offen offset:0 sc1\n"
      "s_waitcnt vmcnt(12)\n"
      "v_lshlrev_b32 v38, 16, v235\n"
      "v_mul_f32 v172, 0xbfb8aa3b, v38\n"
      "v_exp_f32 v172, v172\n"
      "s_nop 0\n"
      "v_add_f32 v172, 1.0, v172\n"
      "v_rcp_f32 v172, v172\n"
      "s_nop 0\n"
      "v_mul_f32 v38, v38, v172\n"
      "v_cvt_pk_bf16_f32 v38, v38, v38\n"
      "ds_write_b16 v204, v38\n"
      "s_waitcnt lgkmcnt(0)\n"
      "s_barrier\n"
      "ds_read_b128 v[144:147], v206 offset:0\n"
      "ds_read_b64_tr_b16 v[148:149], v205 offset:0\n"
      "ds_read_b64_tr_b16 v[150:151], v205 offset:2048\n"
      "ds_read_b128 v[152:155], v206 offset:64\n"
      "ds_read_b64_tr_b16 v[156:157], v205 offset:4096\n"
      "ds_read_b64_tr_b16 v[158:159], v205 offset:6144\n"
      "ds_read_b128 v[160:163], v206 offset:128\n"
      "ds_read_b64_tr_b16 v[164:165], v205 offset:8192\n"
      "ds_read_b64_tr_b16 v[166:167], v205 offset:10240\n"
      "ds_read_b128 v[168:171], v206 offset:192\n"
      "ds_read_b64_tr_b16 v[172:173], v205 offset:12288\n"
      "ds_read_b64_tr_b16 v[174:175], v205 offset:14336\n"
      "ds_read_b128 v[176:179], v206 offset:256\n"
      "ds_read_b64_tr_b16 v[180:181], v205 offset:16384\n"
      "ds_read_b64_tr_b16 v[182:183], v205 offset:18432\n"
      "s_waitcnt lgkmcnt(12)\n"
      "v_mfma_f32_16x16x32_bf16 v[184:187], v[144:147], v[148:151], 0\n"
      "s_waitcnt lgkmcnt(9)\n"
      "v_mfma_f32_16x16x32_bf16 v[184:187], v[152:155], v[156:159], v[184:187]\n"
      "s_waitcnt lgkmcnt(6)\n"
      "v_mfma_f32_16x16x32_bf16 v[184:187], v[160:163], v[164:167], v[184:187]\n"
      "s_waitcnt lgkmcnt(3)\n"
      "v_mfma_f32_16x16x32_bf16 v[184:187], v[168:171], v[172:175], v[184:187]\n"
      "s_waitcnt lgkmcnt(0)\n"
      "v_mfma_f32_16x16x32_bf16 v[184:187], v[176:179], v[180:183], v[184:187]\n"
      "s_nop 7\n"
      "v_mov_b32 v185, s30\n"
      "buffer_store_dwordx2 v[184:185], v207, s[32:35], s62 offen offset:0 sc1\n"
      "s_mul_i32 s42, s3, 2560\n"
      "s_add_u32 m0, s42, 110656\n"
      "s_mov_b32 s36, s4\n"
      "s_and_b32 s37, s5, 0xffff\n"
      "buffer_load_dwordx4 v232, s[36:39], s52 offen lds\n"
      "s_add_u32 m0, m0, 1024\n"
      "s_nop 0\n"
      "buffer_load_dwordx4 v233, s[36:39], s55 offen lds\n"
      "s_add_u32 m0, m0, 1024\n"
      "s_mov_b32 exec_hi, 0\n"
      "buffer_load_dwordx4 v234, s[36:39], s55 offen lds\n"
      "s_mov_b32 exec_hi, -1\n"
      "buffer_load_dwordx4 v[112:115], v211, s[36:39], s66 offen\n"
      "buffer_load_dwordx4 v[116:119], v211, s[36:39], s66 offen offset:512\n"
      "v_bfe_u32 v2, v54, 1, 8\n"
      "v_sub_u32 v2, v211, v2\n"
      "buffer_load_dwordx2 v[120:121], v2, s[36:39], s66 offen offset:1024\n"
      "s_and_b32 s43, s2, 7\n"
      "s_lshr_b32 s42, s2, 3\n"
      "s_lshl_b32 s47, s43, 5\n"
      "s_add_u32 s42, s47, s42\n"
      "s_and_b32 s54, s42, -4\n"
      "s_lshl_b32 s47, s42, 1\n"
      "s_and_b32 s47, s47, -4\n"
      "s_load_dword s53, s[14:15], s54\n"
      "s_load_dword s58, s[14:15], s47 offset:0x100\n"
      "s_load_dword s59, s[14:15], s47 offset:0x300\n"
      "s_min_u32 s54, s3, 2\n"
      "s_lshl_b32 s54, s54, 10\n"
      "s_lshr_b32 s10, s42, 1\n"
      "s_lshl_b32 s47, s42, 12\n"
      "s_lshl_b32 s10, s10, 12\n"
      "s_add_u32 s47, s47, s54\n"
      "s_add_u32 s54, s54, s10\n"
      "s_load_dwordx2 s[6:7], s[12:13], s47 offset:0x80400\n"
      "s_add_u32 s47, s47, 0x100000\n"
      "s_load_dwordx2 s[10:11], s[12:13], s54 offset:0x400\n"
      "s_load_dwordx2 s[8:9], s[12:13], s47 offset:0x80400\n"
      "global_load_dwordx4 v[40:43], v7, s[0:1] offset:-4096\n"
      "global_load_dwordx4 v[44:47], v7, s[0:1] offset:-3072\n"
      "global_load_dwordx4 v[48:51], v7, s[0:1] offset:-2048\n"
      "global_load_dwordx4 v[56:59], v7, s[0:1] offset:-1024\n"
      "global_load_dwordx4 v[60:63], v7, s[0:1] offset:0\n"
      "global_load_dwordx4 v[28:31], v7, s[0:1] offset:1024\n"
      "s_mov_b64 exec, 0\n"
      "s_cmp_eq_u32 s3, 0\n"
      "s_cbranch_scc1 L_pg0_%=\n"
      "s_cmp_eq_u32 s3, 1\n"
      "s_cbranch_scc1 L_pg1_%=\n"
      "s_cmp_eq_u32 s3, 2\n"
      "s_cbranch_scc1 L_pg2_%=\n"
      "s_branch L_pg3_%=\n"
      "L_pgr_%=:\n"
      "s_mov_b64 exec, -1\n"
      "s_waitcnt vmcnt(6)\n"
      "s_waitcnt lgkmcnt(0)\n"
      "s_barrier\n"
      "ds_read_b64_tr_b16 v[64:65], v224 offset:0\n"
      "ds_read_b64_tr_b16 v[66:67], v224 offset:640\n"
      "ds_read_b64_tr_b16 v[68:69], v224 offset:5120\n"
      "ds_read_b64_tr_b16 v[70:71], v224 offset:5760\n"
      "ds_read_b64_tr_b16 v[72:73], v225 offset:0\n"
      "ds_read_b64_tr_b16 v[74:75], v225 offset:640\n"
      "ds_read_b64_tr_b16 v[144:145], v219 offset:0\n"
      "ds_read_b64_tr_b16 v[146:147], v219 offset:8192\n"
      "ds_read_b64_tr_b16 v[148:149], v219 offset:2048\n"
      "ds_read_b64_tr_b16 v[150:151], v219 offset:10240\n"
      "ds_read_b64_tr_b16 v[152:153], v213 offset:0\n"
      "ds_read_b64_tr_b16 v[154:155], v213 offset:8192\n"
      "ds_read_b64_tr_b16 v[156:157], v213 offset:2048\n"
      "ds_read_b64_tr_b16 v[158:159], v213 offset:10240\n"
      "v_mov_b32 v12, s6\n"
      "v_mov_b32 v13, s7\n"
      "v_mov_b32 v18, s8\n"
      "v_mov_b32 v19, s9\n"
      "v_mov_b32 v24, s10\n"
      "v_mov_b32 v25, s11\n"
      "v_and_b32 v104, 48, v7\n"
      "v_xor_b32 v104, 48, v104\n"
      "v_lshlrev_b64 v[26:27], v104, v[26:27]\n"
      "v_mov_b32_dpp v12, v8 wave_shl:1 row_mask:0xf bank_mask:0xf\n"
      "v_mov_b32_dpp v13, v9 wave_shl:1 row_mask:0xf bank_mask:0xf\n"
      "v_mov_b32_dpp v18, v14 wave_shl:1 row_mask:0xf bank_mask:0xf\n"
      "v_mov_b32_dpp v19, v15 wave_shl:1 row_mask:0xf bank_mask:0xf\n"
      "v_mov_b32_dpp v24, v20 wave_shl:1 row_mask:0xf bank_mask:0xf\n"
      "v_mov_b32_dpp v25, v21 wave_shl:1 row_mask:0xf bank_mask:0xf\n"
      "v_mov_b32 v122, 0\n"
      "v_mov_b32 v123, 0\n"
      "v_mov_b32 v96, 0x3f803f80\n"
      "v_dot2c_f32_bf16 v122, v112, v96\n"
      "v_dot2c_f32_bf16 v123, v112, v112\n"
      "v_dot2c_f32_bf16 v122, v113, v96\n"
      "v_dot2c_f32_bf16 v123, v113, v113\n"
      "v_dot2c_f32_bf16 v122, v114, v96\n"
      "v_dot2c_f32_bf16 v123, v114, v114\n"
      "v_dot2c_f32_bf16 v122, v115, v96\n"
      "v_dot2c_f32_bf16 v123, v115, v115\n"
      "v_dot2c_f32_bf16 v122, v116, v96\n"
      "v_dot2c_f32_bf16 v123, v116, v116\n"
      "v_dot2c_f32_bf16 v122, v117, v96\n"
      "v_dot2c_f32_bf16 v123, v117, v117\n"
      "v_dot2c_f32_bf16 v122, v118, v96\n"
      "v_dot2c_f32_bf16 v123, v118, v118\n"
      "v_dot2c_f32_bf16 v122, v119, v96\n"
      "v_dot2c_f32_bf16 v123, v119, v119\n"
      "v_dot2c_f32_bf16 v122, v120, v96\n"
      "v_dot2c_f32_bf16 v123, v120, v120\n"
      "v_dot2c_f32_bf16 v122, v121, v96\n"
      "v_dot2c_f32_bf16 v123, v121, v121\n"
      "s_lshr_b32 s40, s92, 12\n"
      "s_lshr_b32 s41, s93, 12\n"
      "s_mulk_i32 s40, 4224\n"
      "s_mulk_i32 s41, 4224\n"
      "s_add_u32 s40, s40, 0x100000\n"
      "s_add_u32 s41, s41, 0x100000\n"
      "buffer_load_dwordx4 v[128:131], v54, s[32:35], s40 offen sc1\n"
      "s_add_u32 s40, s40, 1081344\n"
      "buffer_load_dwordx4 v[136:139], v54, s[32:35], s41 offen sc1\n"
      "buffer_load_dwordx4 v[132:135], v54, s[32:35], s40 offen sc1\n"
      "buffer_load_dword v140, v237, s[32:35], s70 offen sc1\n"
      "buffer_load_dword v141, v237, s[32:35], s98 offen sc1\n"
      "buffer_load_dword v142, v237, s[32:35], s71 offen sc1\n"
      "s_waitcnt lgkmcnt(0)\n"
      "ds_write_b128 v54, v[10:13] offset:12544\n"
      "v_alignbit_b32 v160, v9, v8, 16\n"
      "v_alignbit_b32 v161, v10, v9, 16\n"
      "v_alignbit_b32 v162, v11, v10, 16\n"
      "v_alignbit_b32 v163, v12, v11, 16\n"
      "s_mov_b32 vcc_lo, 0x33333333\n"
      "s_mov_b32 vcc_hi, 0x33333333\n"
      "v_cndmask_b32_dpp v80, v148, v144, vcc quad_perm:[0,1,0,1] row_mask:0xf bank_mask:0xf\n"
      "v_cndmask_b32_dpp v81, v149, v145, vcc quad_perm:[0,1,0,1] row_mask:0xf bank_mask:0xf\n"
      "v_cndmask_b32_dpp v82, v150, v146, vcc quad_perm:[0,1,0,1] row_mask:0xf bank_mask:0xf\n"
      "v_cndmask_b32_dpp v83, v151, v147, vcc quad_perm:[0,1,0,1] row_mask:0xf bank_mask:0xf\n"
      "ds_write_b128 v54, v[160:163] offset:64\n"
      "v_alignbit_b32 v164, v10, v9, 16\n"
      "v_alignbit_b32 v165, v11, v10, 16\n"
      "v_alignbit_b32 v166, v12, v11, 16\n"
      "v_alignbit_b32 v167, v13, v12, 16\n"
      "v_cndmask_b32_dpp v88, v156, v152, vcc quad_perm:[0,1,0,1] row_mask:0xf bank_mask:0xf\n"
      "v_cndmask_b32_dpp v89, v157, v153, vcc quad_perm:[0,1,0,1] row_mask:0xf bank_mask:0xf\n"
      "v_cndmask_b32_dpp v90, v158, v154, vcc quad_perm:[0,1,0,1] row_mask:0xf bank_mask:0xf\n"
      "v_cndmask_b32_dpp v91, v159, v155, vcc quad_perm:[0,1,0,1] row_mask:0xf bank_mask:0xf\n"
      "s_not_b64 vcc, vcc\n"
      "v_cndmask_b32_dpp v84, v144, v148, vcc quad_perm:[2,3,2,3] row_mask:0xf bank_mask:0xf\n"
      "ds_write_b128 v54, v[164:167] offset:8384\n"
      "v_mov_b32 v168, v9\n"
      "v_mov_b32 v169, v10\n"
      "v_mov_b32 v170, v11\n"
      "v_mov_b32 v171, v12\n"
      "v_cndmask_b32_dpp v85, v145, v149, vcc quad_perm:[2,3,2,3] row_mask:0xf bank_mask:0xf\n"
      "v_cndmask_b32_dpp v86, v146, v150, vcc quad_perm:[2,3,2,3] row_mask:0xf bank_mask:0xf\n"
      "v_cndmask_b32_dpp v87, v147, v151, vcc quad_perm:[2,3,2,3] row_mask:0xf bank_mask:0xf\n"
      "v_cndmask_b32_dpp v92, v152, v156, vcc quad_perm:[2,3,2,3] row_mask:0xf bank_mask:0xf\n"
      "v_cndmask_b32_dpp v93, v153, v157, vcc quad_perm:[2,3,2,3] row_mask:0xf bank_mask:0xf\n"
      "v_cndmask_b32_dpp v94, v154, v158, vcc quad_perm:[2,3,2,3] row_mask:0xf bank_mask:0xf\n"
      "ds_write_b128 v54, v[168:171] offset:4224\n"
      "v_cndmask_b32_dpp v95, v155, v159, vcc quad_perm:[2,3,2,3] row_mask:0xf bank_mask:0xf\n"
      "buffer_store_dwordx4 v[80:83], v7, s[32:35], s72 offen offset:0 sc1\n"
      "buffer_store_dwordx4 v[84:87], v7, s[32:35], s73 offen offset:0 sc1\n"
      "buffer_store_dwordx4 v[88:91], v7, s[32:35], s74 offen offset:0 sc1\n"
      "buffer_store_dwordx4 v[92:95], v7, s[32:35], s75 offen offset:0 sc1\n"
      "v_add3_u32 v76, v64, v65, v66\n"
      "ds_write_b128 v54, v[16:19] offset:29184\n"
      "v_alignbit_b32 v172, v15, v14, 16\n"
      "v_alignbit_b32 v173, v16, v15, 16\n"
      "v_alignbit_b32 v174, v17, v16, 16\n"
      "v_alignbit_b32 v175, v18, v17, 16\n"
      "v_add_u32 v76, v76, v67\n"
      "v_add3_u32 v77, v68, v69, v70\n"
      "v_add_f32_dpp v122, v122, v122 quad_perm:[1,0,3,2] row_mask:0xf bank_mask:0xf\n"
      "v_add_u32 v77, v77, v71\n"
      "v_add_f32_dpp v123, v123, v123 quad_perm:[1,0,3,2] row_mask:0xf bank_mask:0xf\n"
      "v_add3_u32 v78, v72, v73, v74\n"
      "ds_write_b128 v54, v[172:175] offset:16704\n"
      "v_alignbit_b32 v176, v16, v15, 16\n"
      "v_alignbit_b32 v177, v17, v16, 16\n"
      "v_alignbit_b32 v178, v18, v17, 16\n"
      "v_alignbit_b32 v179, v19, v18, 16\n"
      "v_add_f32_dpp v122, v122, v122 quad_perm:[2,3,0,1] row_mask:0xf bank_mask:0xf\n"
      "v_add_u32 v78, v78, v75\n"
      "v_add_f32_dpp v123, v123, v123 quad_perm:[2,3,0,1] row_mask:0xf bank_mask:0xf\n"
      "v_mov_b32 v79, v78\n"
      "v_add_f32_dpp v122, v122, v122 row_half_mirror row_mask:0xf bank_mask:0xf\n"
      "v_permlane32_swap_b32 v76, v77\n"
      "ds_write_b128 v54, v[176:179] offset:25024\n"
      "v_mov_b32 v180, v15\n"
      "v_mov_b32 v181, v16\n"
      "v_mov_b32 v182, v17\n"
      "v_mov_b32 v183, v18\n"
      "v_add_f32_dpp v123, v123, v123 row_half_mirror row_mask:0xf bank_mask:0xf\n"
      "v_permlane32_swap_b32 v78, v79\n"
      "v_add_f32_dpp v122, v122, v122 row_mirror row_mask:0xf bank_mask:0xf\n"
      "v_add_u32 v76, v76, v77\n"
      "v_add_f32_dpp v123, v123, v123 row_mirror row_mask:0xf bank_mask:0xf\n"
      "v_add_u32 v78, v78, v79\n"
      "ds_write_b128 v54, v[180:183] offset:20864\n"
      "v_mov_b32 v96, v122\n"
      "v_permlane16_swap_b32 v76, v78\n"
      "v_mov_b32 v97, v123\n"
      "v_add_u32 v76, v76, v78\n"
      "v_permlane16_swap_b32 v122, v96\n"
      "v_sub_u32 v76, s30, v76\n"
      "ds_write_b128 v54, v[22:25] offset:45824\n"
      "v_alignbit_b32 v160, v21, v20, 16\n"
      "v_alignbit_b32 v161, v22, v21, 16\n"
      "v_alignbit_b32 v162, v23, v22, 16\n"
      "v_alignbit_b32 v163, v24, v23, 16\n"
      "v_permlane16_swap_b32 v123, v97\n"
      "v_add_f32 v122, v122, v96\n"
      "v_add_f32 v123, v123, v97\n"
      "v_mul_f32 v122, 0x3acccccd, v122\n"
      "v_mul_f32 v123, 0x3acccccd, v123\n"
      "v_fma_f32 v123, -v122, v122, v123\n"
      "ds_write_b128 v54, v[160:163] offset:33344\n"
      "v_alignbit_b32 v164, v22, v21, 16\n"
      "v_alignbit_b32 v165, v23, v22, 16\n"
      "v_alignbit_b32 v166, v24, v23, 16\n"
      "v_alignbit_b32 v167, v25, v24, 16\n"
      "v_max_f32 v123, 0, v123\n"
      "v_cvt_pk_bf16_f32 v123, 0, v123\n"
      "v_add_f32 v123, 0x3727c5ac, v123\n"
      "v_cvt_pk_bf16_f32 v123, 0, v123\n"
      "v_sqrt_f32 v123, v123\n"
      "v_readlane_b32 s42, v122, 32\n"
      "ds_write_b128 v54, v[164:167] offset:41664\n"
      "v_mov_b32 v168, v21\n"
      "v_mov_b32 v169, v22\n"
      "v_mov_b32 v170, v23\n"
      "v_mov_b32 v171, v24\n"
      "v_cvt_pk_bf16_f32 v123, 0, v123\n"
      "v_rcp_f32 v96, v123\n"
      "v_mov_b32 v99, s30\n"
      "v_cvt_pk_bf16_f32 v98, v122, s42\n"
      "v_readlane_b32 s42, v96, 32\n"
      "s_nop 1\n"
      "v_mov_b32 v97, s42\n"
      "ds_write_b128 v54, v[168:171] offset:37504\n"
      "buffer_store_dwordx4 v[96:99], v214, s[32:35], s67 offen offset:0 sc1\n"
      "buffer_store_dwordx4 v[64:67], v226, s[32:35], s64 offen offset:0 sc1\n"
      "buffer_store_dwordx4 v[68:71], v226, s[32:35], s64 offen offset:64 sc1\n"
      "buffer_store_dwordx4 v[72:75], v227, s[32:35], s64 offen offset:0 sc1\n"
      "buffer_store_dword v76, v215, s[32:35], s65 offen offset:0 sc1\n"
      "ds_write_b64 v216, v[26:27]\n"
      "s_waitcnt vmcnt(9)\n"
      "v_add3_u32 v143, v128, v129, v130\n"
      "v_add3_u32 v143, v143, v131, v132\n"
      "v_add3_u32 v143, v143, v133, v134\n"
      "v_add3_u32 v143, v143, v135, v136\n"
      "v_add3_u32 v143, v143, v137, v138\n"
      "v_add_u32 v143, v143, v139\n"
      "v_add3_u32 v140, v140, v141, v142\n"
      "s_mul_i32 s47, s30, 3\n"
      "v_add_u32_dpp v143, v143, v143 quad_perm:[1,0,3,2] row_mask:0xf bank_mask:0xf\n"
      "s_nop 1\n"
      "v_add_u32_dpp v143, v143, v143 quad_perm:[2,3,0,1] row_mask:0xf bank_mask:0xf\n"
      "s_nop 1\n"
      "v_add_u32_dpp v143, v143, v143 row_half_mirror row_mask:0xf bank_mask:0xf\n"
      "v_add_u32 v143, v143, v140\n"
      "v_cmp_ne_u32 vcc, s47, v143\n"
      "s_cbranch_vccnz L_psx_%=\n"
      "L_pbx_%=:\n"
      "ds_write_b128 v238, v[128:131] offset:0\n"
      "ds_write_b128 v238, v[132:135] offset:7680\n"
      "ds_write_b128 v238, v[136:139] offset:15360\n"
      "s_mul_i32 s42, s3, 2048\n"
      "s_add_u32 m0, s42, 137280\n"
      "s_waitcnt lgkmcnt(0)\n"
      "s_barrier\n"
      "s_cmp_lg_u32 s99, 0\n"
      "s_cbranch_scc1 L_cv_b%=\n"
      "ds_read_b128 v[160:163], v43 offset:4608\n"
      "ds_read_b64 v[112:113], v42 offset:0\n"
      "ds_read_b64 v[114:115], v42 offset:8\n"
      "ds_read_b64 v[148:149], v42 offset:3072\n"
      "ds_read_b64 v[150:151], v42 offset:3080\n"
      "ds_read_b128 v[172:175], v43 offset:0\n"
      "ds_read_b128 v[168:171], v43 offset:1536\n"
      "ds_read_b64 v[124:125], v42 offset:1024\n"
      "ds_read_b64 v[126:127], v42 offset:1032\n"
      "ds_read_b128 v[164:167], v43 offset:3072\n"
      "ds_read_b64 v[136:137], v42 offset:2048\n"
      "ds_read_b64 v[138:139], v42 offset:2056\n"
      "ds_read_b64 v[116:117], v42 offset:32\n"
      "ds_read_b64 v[118:119], v42 offset:40\n"
      "ds_read_b64 v[128:129], v42 offset:1056\n"
      "ds_read_b64 v[130:131], v42 offset:1064\n"
      "ds_read_b64 v[140:141], v42 offset:2080\n"
      "ds_read_b64 v[142:143], v42 offset:2088\n"
      "ds_read_b64 v[152:153], v42 offset:3104\n"
      "ds_read_b64 v[154:155], v42 offset:3112\n"
      "s_waitcnt lgkmcnt(8)\n"
      "v_mfma_f32_16x16x32_bf16 v[96:99], v[112:115], v[160:163], 0\n"
      "ds_read_b128 v[176:179], v43 offset:4640\n"
      "ds_read_b128 v[180:183], v43 offset:3104\n"
      "v_mfma_f32_16x16x32_bf16 v[104:107], v[148:151], v[160:163], 0\n"
      "ds_read_b128 v[184:187], v43 offset:1568\n"
      "v_mfma_f32_16x16x32_bf16 v[104:107], v[112:115], v[172:175], v[104:107]\n"
      "ds_read_b128 v[188:191], v43 offset:32\n"
      "v_mfma_f32_16x16x32_bf16 v[104:107], v[124:127], v[168:171], v[104:107]\n"
      "ds_read_b64 v[120:121], v42 offset:64\n"
      "ds_read_b64 v[122:123], v42 offset:72\n"
      "v_mfma_f32_16x16x32_bf16 v[104:107], v[136:139], v[164:167], v[104:107]\n"
      "ds_read_b64 v[132:133], v42 offset:1088\n"
      "s_add_u32 s22, s20, 60416\n"
      "s_addc_u32 s23, s21, 0\n"
      "v_mov_b64 v[8:9], v[80:81]\n"
      "s_waitcnt lgkmcnt(7)\n"
      "v_mfma_f32_16x16x32_bf16 v[100:103], v[116:119], v[160:163], 0\n"
      "ds_read_b64 v[134:135], v42 offset:1096\n"
      "v_mov_b64 v[10:11], v[82:83]\n"
      "v_mfma_f32_16x16x32_bf16 v[108:111], v[152:155], v[160:163], 0\n"
      "ds_read_b64 v[144:145], v42 offset:2112\n"
      "v_mov_b64 v[12:13], v[84:85]\n"
      "v_mfma_f32_16x16x32_bf16 v[108:111], v[116:119], v[172:175], v[108:111]\n"
      "ds_read_b64 v[146:147], v42 offset:2120\n"
      "v_mov_b64 v[14:15], v[86:87]\n"
      "v_mfma_f32_16x16x32_bf16 v[108:111], v[128:131], v[168:171], v[108:111]\n"
      "ds_read_b64 v[156:157], v42 offset:3136\n"
      "v_mov_b64 v[16:17], v[88:89]\n"
      "v_mfma_f32_16x16x32_bf16 v[108:111], v[140:143], v[164:167], v[108:111]\n"
      "ds_read_b64 v[158:159], v42 offset:3144\n"
      "v_mov_b64 v[18:19], v[90:91]\n"
      "s_waitcnt lgkmcnt(8)\n"
      "v_mfma_f32_16x16x32_bf16 v[96:99], v[116:119], v[176:179], v[96:99]\n"
      "ds_read_b128 v[160:163], v43 offset:4704\n"
      "ds_read_b128 v[164:167], v43 offset:3168\n"
      "s_waitcnt vmcnt(5)\n"
      "buffer_store_dwordx2 v[52:53], v48, s[32:35], s79 offen sc1\n"
      "v_mov_b64 v[20:21], v[92:93]\n"
      "v_mfma_f32_16x16x32_bf16 v[104:107], v[152:155], v[176:179], v[104:107]\n"
      "ds_read_b128 v[168:171], v43 offset:1632\n"
      "v_mov_b64 v[22:23], v[94:95]\n"
      "v_mov_b32 v2, v212\n"
      "v_mfma_f32_16x16x32_bf16 v[104:107], v[116:119], v[188:191], v[104:107]\n"
      "ds_read_b128 v[172:175], v43 offset:96\n"
      "v_mov_b32 v3, v220\n"
      "v_mfma_f32_16x16x32_bf16 v[104:107], v[128:131], v[184:187], v[104:107]\n"
      "ds_read_b64 v[112:113], v42 offset:96\n"
      "ds_read_b64 v[114:115], v42 offset:104\n"
      "s_mov_b32 s45, s68\n"
      "v_mfma_f32_16x16x32_bf16 v[104:107], v[140:143], v[180:183], v[104:107]\n"
      "ds_read_b64 v[124:125], v42 offset:1120\n"
      "s_lshl_b32 s42, s99, 4\n"
      "s_waitcnt lgkmcnt(7)\n"
      "v_mfma_f32_16x16x32_bf16 v[100:103], v[120:123], v[176:179], v[100:103]\n"
      "ds_read_b64 v[126:127], v42 offset:1128\n"
      "s_lshr_b32 s58, s58, s42\n"
      "v_mfma_f32_16x16x32_bf16 v[108:111], v[156:159], v[176:179], v[108:111]\n"
      "ds_read_b64 v[136:137], v42 offset:2144\n"
      "s_lshr_b32 s59, s59, s42\n"
      "v_mfma_f32_16x16x32_bf16 v[108:111], v[120:123], v[188:191], v[108:111]\n"
      "ds_read_b64 v[138:139], v42 offset:2152\n"
      "s_lshr_b32 s42, s93, 8\n"
      "v_mfma_f32_16x16x32_bf16 v[108:111], v[132:135], v[184:187], v[108:111]\n"
      "ds_read_b64 v[148:149], v42 offset:3168\n"
      "s_and_b32 s42, s42, 16\n"
      "s_lshr_b32 s25, s53, s42\n"
      "v_mfma_f32_16x16x32_bf16 v[108:111], v[144:147], v[180:183], v[108:111]\n"
      "ds_read_b64 v[150:151], v42 offset:3176\n"
      "s_lshl_b32 s14, s58, 16\n"
      "s_waitcnt lgkmcnt(8)\n"
      "v_mfma_f32_16x16x32_bf16 v[96:99], v[120:123], v[160:163], v[96:99]\n"
      "ds_read_b128 v[176:179], v43 offset:4736\n"
      "ds_read_b128 v[180:183], v43 offset:3200\n"
      "s_lshl_b32 s15, s59, 16\n"
      "v_mfma_f32_16x16x32_bf16 v[104:107], v[156:159], v[160:163], v[104:107]\n"
      "ds_read_b128 v[184:187], v43 offset:1664\n"
      "s_lshl_b32 s25, s25, 16\n"
      "v_mfma_f32_16x16x32_bf16 v[104:107], v[120:123], v[172:175], v[104:107]\n"
      "ds_read_b128 v[188:191], v43 offset:128\n"
      "s_lshl_b32 s24, s30, 2\n"
      "v_mfma_f32_16x16x32_bf16 v[104:107], v[132:135], v[168:171], v[104:107]\n"
      "ds_read_b64 v[116:117], v42 offset:128\n"
      "ds_read_b64 v[118:119], v42 offset:136\n"
      "s_lshr_b32 s42, s93, 8\n"
      "v_mfma_f32_16x16x32_bf16 v[104:107], v[144:147], v[164:167], v[104:107]\n"
      "ds_read_b64 v[128:129], v42 offset:1152\n"
      "s_and_b32 s42, s42, 0x60\n"
      "s_waitcnt lgkmcnt(7)\n"
      "v_mfma_f32_16x16x32_bf16 v[100:103], v[112:115], v[160:163], v[100:103]\n"
      "ds_read_b64 v[130:131], v42 offset:1160\n"
      "v_xor_b32 v36, s97, v221\n"
      "v_xor_b32 v37, s42, v63\n"
      "v_mfma_f32_16x16x32_bf16 v[108:111], v[148:151], v[160:163], v[108:111]\n"
      "ds_read_b64 v[140:141], v42 offset:2176\n"
      "v_xor_b32 v50, s42, v50\n"
      "v_mfma_f32_16x16x32_bf16 v[108:111], v[112:115], v[172:175], v[108:111]\n"
      "ds_read_b64 v[142:143], v42 offset:2184\n"
      "v_xor_b32 v29, s97, v29\n"
      "v_mfma_f32_16x16x32_bf16 v[108:111], v[124:127], v[168:171], v[108:111]\n"
      "ds_read_b64 v[152:153], v42 offset:3200\n"
      "v_add_u32 v47, s94, v47\n"
      "v_mfma_f32_16x16x32_bf16 v[108:111], v[136:139], v[164:167], v[108:111]\n"
      "ds_read_b64 v[154:155], v42 offset:3208\n"
      "v_add_u32 v5, s90, v5\n"
      "s_waitcnt lgkmcnt(8)\n"
      "v_mfma_f32_16x16x32_bf16 v[96:99], v[112:115], v[176:179], v[96:99]\n"
      "ds_read_b128 v[160:163], v41 offset:4608\n"
      "ds_read_b64 v[120:121], v40 offset:0\n"
      "s_lshl_b32 s42, s3, 12\n"
      "v_mfma_f32_16x16x32_bf16 v[104:107], v[148:151], v[176:179], v[104:107]\n"
      "ds_read_b64 v[122:123], v40 offset:8\n"
      "ds_read_b128 v[164:167], v41 offset:3072\n"
      "s_add_u32 s42, s42, 147456\n"
      "v_mfma_f32_16x16x32_bf16 v[104:107], v[112:115], v[188:191], v[104:107]\n"
      "ds_read_b64 v[132:133], v40 offset:1024\n"
      "ds_read_b64 v[134:135], v40 offset:1032\n"
      "v_add_u32 v55, s42, v7\n"
      "s_lshr_b32 s43, s96, 16\n"
      "v_mfma_f32_16x16x32_bf16 v[104:107], v[124:127], v[184:187], v[104:107]\n"
      "ds_read_b128 v[168:171], v41 offset:1536\n"
      "ds_read_b64 v[144:145], v40 offset:2048\n"
      "s_add_u32 s55, s78, 0x1000000\n"
      "v_mfma_f32_16x16x32_bf16 v[104:107], v[136:139], v[180:183], v[104:107]\n"
      "ds_read_b64 v[146:147], v40 offset:2056\n"
      "ds_read_b128 v[172:175], v41 offset:0\n"
      "s_mov_b32 s59, s69\n"
      "s_waitcnt lgkmcnt(10)\n"
      "v_mfma_f32_16x16x32_bf16 v[100:103], v[116:119], v[176:179], v[100:103]\n"
      "ds_read_b64 v[156:157], v40 offset:3072\n"
      "s_lshr_b32 s53, s92, 12\n"
      "v_mfma_f32_16x16x32_bf16 v[108:111], v[152:155], v[176:179], v[108:111]\n"
      "ds_read_b64 v[158:159], v40 offset:3080\n"
      "s_mulk_i32 s53, 4224\n"
      "v_mfma_f32_16x16x32_bf16 v[108:111], v[116:119], v[188:191], v[108:111]\n"
      "s_add_u32 s53, s53, 0x400000\n"
      "s_add_u32 s54, s53, 1081344\n"
      "v_mfma_f32_16x16x32_bf16 v[108:111], v[128:131], v[184:187], v[108:111]\n"
      "s_mov_b32 exec_hi, 0\n"
      "v_add_u32 v58, s43, v58\n"
      "s_mov_b32 exec_hi, -1\n"
      "v_mfma_f32_16x16x32_bf16 v[108:111], v[140:143], v[180:183], v[108:111]\n"
      "s_waitcnt lgkmcnt(0)\n"
      "s_branch L_cv_e%=\n"
      "L_cv_b%=:\n"
      "ds_read_b128 v[164:167], v43 offset:3072\n"
      "ds_read_b64 v[112:113], v42 offset:0\n"
      "ds_read_b64 v[114:115], v42 offset:8\n"
      "ds_read_b128 v[160:163], v43 offset:4608\n"
      "ds_read_b64 v[124:125], v42 offset:1024\n"
      "ds_read_b64 v[126:127], v42 offset:1032\n"
      "ds_read_b64 v[136:137], v42 offset:2048\n"
      "ds_read_b64 v[138:139], v42 offset:2056\n"
      "ds_read_b128 v[168:171], v43 offset:1536\n"
      "ds_read_b64 v[116:117], v42 offset:32\n"
      "ds_read_b64 v[118:119], v42 offset:40\n"
      "ds_read_b64 v[128:129], v42 offset:1056\n"
      "ds_read_b64 v[130:131], v42 offset:1064\n"
      "ds_read_b64 v[140:141], v42 offset:2080\n"
      "ds_read_b64 v[142:143], v42 offset:2088\n"
      "s_waitcnt lgkmcnt(6)\n"
      "v_mfma_f32_16x16x32_bf16 v[96:99], v[112:115], v[164:167], 0\n"
      "ds_read_b128 v[176:179], v43 offset:4640\n"
      "v_mfma_f32_16x16x32_bf16 v[96:99], v[124:127], v[160:163], v[96:99]\n"
      "ds_read_b128 v[180:183], v43 offset:3104\n"
      "v_mfma_f32_16x16x32_bf16 v[104:107], v[124:127], v[164:167], 0\n"
      "ds_read_b128 v[184:187], v43 offset:1568\n"
      "v_mfma_f32_16x16x32_bf16 v[104:107], v[136:139], v[160:163], v[104:107]\n"
      "ds_read_b64 v[120:121], v42 offset:64\n"
      "v_mfma_f32_16x16x32_bf16 v[104:107], v[112:115], v[168:171], v[104:107]\n"
      "ds_read_b64 v[122:123], v42 offset:72\n"
      "s_add_u32 s22, s20, 60416\n"
      "s_addc_u32 s23, s21, 0\n"
      "v_mov_b64 v[8:9], v[80:81]\n"
      "s_waitcnt lgkmcnt(5)\n"
      "v_mfma_f32_16x16x32_bf16 v[100:103], v[116:119], v[164:167], 0\n"
      "ds_read_b64 v[132:133], v42 offset:1088\n"
      "v_mov_b64 v[10:11], v[82:83]\n"
      "v_mfma_f32_16x16x32_bf16 v[100:103], v[128:131], v[160:163], v[100:103]\n"
      "ds_read_b64 v[134:135], v42 offset:1096\n"
      "v_mov_b64 v[12:13], v[84:85]\n"
      "v_mfma_f32_16x16x32_bf16 v[108:111], v[128:131], v[164:167], 0\n"
      "ds_read_b64 v[144:145], v42 offset:2112\n"
      "v_mov_b64 v[14:15], v[86:87]\n"
      "v_mfma_f32_16x16x32_bf16 v[108:111], v[140:143], v[160:163], v[108:111]\n"
      "ds_read_b64 v[146:147], v42 offset:2120\n"
      "v_mov_b64 v[16:17], v[88:89]\n"
      "v_mfma_f32_16x16x32_bf16 v[108:111], v[116:119], v[168:171], v[108:111]\n"
      "v_mov_b64 v[18:19], v[90:91]\n"
      "s_waitcnt lgkmcnt(6)\n"
      "v_mfma_f32_16x16x32_bf16 v[96:99], v[116:119], v[180:183], v[96:99]\n"
      "ds_read_b128 v[160:163], v43 offset:4704\n"
      "s_waitcnt vmcnt(5)\n"
      "buffer_store_dwordx2 v[52:53], v48, s[32:35], s79 offen sc1\n"
      "v_mov_b64 v[20:21], v[92:93]\n"
      "v_mfma_f32_16x16x32_bf16 v[96:99], v[128:131], v[176:179], v[96:99]\n"
      "ds_read_b128 v[164:167], v43 offset:3168\n"
      "v_mov_b64 v[22:23], v[94:95]\n"
      "v_mov_b32 v2, v212\n"
      "v_mfma_f32_16x16x32_bf16 v[104:107], v[128:131], v[180:183], v[104:107]\n"
      "ds_read_b128 v[168:171], v43 offset:1632\n"
      "v_mov_b32 v3, v220\n"
      "v_mfma_f32_16x16x32_bf16 v[104:107], v[140:143], v[176:179], v[104:107]\n"
      "ds_read_b64 v[112:113], v42 offset:96\n"
      "s_mov_b32 s45, s68\n"
      "v_mfma_f32_16x16x32_bf16 v[104:107], v[116:119], v[184:187], v[104:107]\n"
      "ds_read_b64 v[114:115], v42 offset:104\n"
      "s_lshl_b32 s42, s99, 4\n"
      "s_waitcnt lgkmcnt(5)\n"
      "v_mfma_f32_16x16x32_bf16 v[100:103], v[120:123], v[180:183], v[100:103]\n"
      "L_pg1_%=:\n"
      "s_cbranch_execz L_pg5_%=\n"
      "ds_read_b64 v[124:125], v42 offset:1120\n"
      "s_lshr_b32 s58, s58, s42\n"
      "v_mfma_f32_16x16x32_bf16 v[100:103], v[132:135], v[176:179], v[100:103]\n"
      "ds_read_b64 v[126:127], v42 offset:1128\n"
      "s_lshr_b32 s59, s59, s42\n"
      "v_mfma_f32_16x16x32_bf16 v[108:111], v[132:135], v[180:183], v[108:111]\n"
      "ds_read_b64 v[136:137], v42 offset:2144\n"
      "s_lshr_b32 s42, s93, 8\n"
      "v_mfma_f32_16x16x32_bf16 v[108:111], v[144:147], v[176:179], v[108:111]\n"
      "ds_read_b64 v[138:139], v42 offset:2152\n"
      "s_and_b32 s42, s42, 16\n"
      "s_lshr_b32 s25, s53, s42\n"
      "v_mfma_f32_16x16x32_bf16 v[108:111], v[120:123], v[184:187], v[108:111]\n"
      "s_lshl_b32 s14, s58, 16\n"
      "s_waitcnt lgkmcnt(6)\n"
      "v_mfma_f32_16x16x32_bf16 v[96:99], v[120:123], v[164:167], v[96:99]\n"
      "ds_read_b128 v[176:179], v43 offset:4736\n"
      "s_lshl_b32 s15, s59, 16\n"
      "v_mfma_f32_16x16x32_bf16 v[96:99], v[132:135], v[160:163], v[96:99]\n"
      "ds_read_b128 v[180:183], v43 offset:3200\n"
      "s_lshl_b32 s25, s25, 16\n"
      "v_mfma_f32_16x16x32_bf16 v[104:107], v[132:135], v[164:167], v[104:107]\n"
      "ds_read_b128 v[184:187], v43 offset:1664\n"
      "s_lshl_b32 s24, s30, 2\n"
      "v_mfma_f32_16x16x32_bf16 v[104:107], v[144:147], v[160:163], v[104:107]\n"
      "ds_read_b64 v[116:117], v42 offset:128\n"
      "s_lshr_b32 s42, s93, 8\n"
      "v_mfma_f32_16x16x32_bf16 v[104:107], v[120:123], v[168:171], v[104:107]\n"
      "ds_read_b64 v[118:119], v42 offset:136\n"
      "s_and_b32 s42, s42, 0x60\n"
      "s_waitcnt lgkmcnt(5)\n"
      "v_mfma_f32_16x16x32_bf16 v[100:103], v[112:115], v[164:167], v[100:103]\n"
      "ds_read_b64 v[128:129], v42 offset:1152\n"
      "v_xor_b32 v36, s97, v221\n"
      "v_xor_b32 v37, s42, v63\n"
      "v_mfma_f32_16x16x32_bf16 v[100:103], v[124:127], v[160:163], v[100:103]\n"
      "ds_read_b64 v[130:131], v42 offset:1160\n"
      "v_xor_b32 v50, s42, v50\n"
      "v_mfma_f32_16x16x32_bf16 v[108:111], v[124:127], v[164:167], v[108:111]\n"
      "ds_read_b64 v[140:141], v42 offset:2176\n"
      "v_xor_b32 v29, s97, v29\n"
      "v_mfma_f32_16x16x32_bf16 v[108:111], v[136:139], v[160:163], v[108:111]\n"
      "ds_read_b64 v[142:143], v42 offset:2184\n"
      "v_add_u32 v47, s94, v47\n"
      "v_mfma_f32_16x16x32_bf16 v[108:111], v[112:115], v[168:171], v[108:111]\n"
      "v_add_u32 v5, s90, v5\n"
      "s_waitcnt lgkmcnt(6)\n"
      "v_mfma_f32_16x16x32_bf16 v[96:99], v[112:115], v[180:183], v[96:99]\n"
      "ds_read_b128 v[160:163], v41 offset:4608\n"
      "ds_read_b64 v[120:121], v40 offset:0\n"
      "s_lshl_b32 s42, s3, 12\n"
      "v_mfma_f32_16x16x32_bf16 v[96:99], v[124:127], v[176:179], v[96:99]\n"
      "ds_read_b64 v[122:123], v40 offset:8\n"
      "ds_read_b128 v[164:167], v41 offset:3072\n"
      "s_add_u32 s42, s42, 147456\n"
      "v_mfma_f32_16x16x32_bf16 v[104:107], v[124:127], v[180:183], v[104:107]\n"
      "ds_read_b64 v[132:133], v40 offset:1024\n"
      "ds_read_b64 v[134:135], v40 offset:1032\n"
      "v_add_u32 v55, s42, v7\n"
      "s_lshr_b32 s43, s96, 16\n"
      "v_mfma_f32_16x16x32_bf16 v[104:107], v[136:139], v[176:179], v[104:107]\n"
      "ds_read_b128 v[168:171], v41 offset:1536\n"
      "ds_read_b64 v[144:145], v40 offset:2048\n"
      "s_add_u32 s55, s78, 0x1000000\n"
      "v_mfma_f32_16x16x32_bf16 v[104:107], v[112:115], v[184:187], v[104:107]\n"
      "ds_read_b64 v[146:147], v40 offset:2056\n"
      "ds_read_b128 v[172:175], v41 offset:0\n"
      "s_mov_b32 s59, s69\n"
      "s_waitcnt lgkmcnt(10)\n"
      "v_mfma_f32_16x16x32_bf16 v[100:103], v[116:119], v[180:183], v[100:103]\n"
      "ds_read_b64 v[156:157], v40 offset:3072\n"
      "s_lshr_b32 s53, s92, 12\n"
      "v_mfma_f32_16x16x32_bf16 v[100:103], v[128:131], v[176:179], v[100:103]\n"
      "ds_read_b64 v[158:159], v40 offset:3080\n"
      "s_mulk_i32 s53, 4224\n"
      "v_mfma_f32_16x16x32_bf16 v[108:111], v[128:131], v[180:183], v[108:111]\n"
      "s_add_u32 s53, s53, 0x400000\n"
      "s_add_u32 s54, s53, 1081344\n"
      "v_mfma_f32_16x16x32_bf16 v[108:111], v[140:143], v[176:179], v[108:111]\n"
      "s_mov_b32 exec_hi, 0\n"
      "v_add_u32 v58, s43, v58\n"
      "s_mov_b32 exec_hi, -1\n"
      "v_mfma_f32_16x16x32_bf16 v[108:111], v[116:119], v[184:187], v[108:111]\n"
      "s_waitcnt lgkmcnt(0)\n"
      "L_cv_e%=:\n"
      "ds_read_b64 v[112:113], v40 offset:32\n"
      "ds_read_b64 v[114:115], v40 offset:40\n"
      "s_add_u32 s8, s26, s91\n"
      "s_addc_u32 s9, s27, 0\n"
      "v_mfma_f32_16x16x32_bf16 v[64:67], v[120:123], v[160:163], 0\n"
      "global_load_lds_dwordx4 v222, s[20:21]\n"
      "ds_read_b64 v[124:125], v40 offset:1056\n"
      "v_mfma_f32_16x16x32_bf16 v[72:75], v[120:123], v[164:167], 0\n"
      "ds_read_b64 v[126:127], v40 offset:1064\n"
      "ds_read_b64 v[136:137], v40 offset:2080\n"
      "v_mfma_f32_16x16x32_bf16 v[72:75], v[132:135], v[160:163], v[72:75]\n"
      "ds_read_b64 v[138:139], v40 offset:2088\n"
      "v_mfma_f32_16x16x32_bf16 v[80:83], v[120:123], v[168:171], 0\n"
      "ds_read_b64 v[148:149], v40 offset:3104\n"
      "v_mfma_f32_16x16x32_bf16 v[80:83], v[132:135], v[164:167], v[80:83]\n"
      "ds_read_b64 v[150:151], v40 offset:3112\n"
      "v_mfma_f32_16x16x32_bf16 v[80:83], v[144:147], v[160:163], v[80:83]\n"
      "ds_write_b128 v56, v[96:99]\n"
      "ds_read_b128 v[176:179], v41 offset:4640\n"
      "ds_read_b128 v[180:183], v41 offset:3104\n"
      "ds_read_b128 v[184:187], v41 offset:1568\n"
      "v_mfma_f32_16x16x32_bf16 v[88:91], v[120:123], v[172:175], 0\n"
      "global_load_lds_dwordx4 v222, s[22:23] offset:1024\n"
      "v_mfma_f32_16x16x32_bf16 v[88:91], v[132:135], v[168:171], v[88:91]\n"
      "ds_read_b128 v[188:191], v41 offset:32\n"
      "v_mfma_f32_16x16x32_bf16 v[88:91], v[144:147], v[164:167], v[88:91]\n"
      "ds_read_b64 v[116:117], v40 offset:64\n"
      "v_mfma_f32_16x16x32_bf16 v[88:91], v[156:159], v[160:163], v[88:91]\n"
      "ds_write_b128 v56, v[104:107] offset:2048\n"
      "ds_read_b64 v[118:119], v40 offset:72\n"
      "ds_read_b64 v[128:129], v40 offset:1088\n"
      "ds_read_b64 v[130:131], v40 offset:1096\n"
      "s_waitcnt lgkmcnt(10)\n"
      "v_mfma_f32_16x16x32_bf16 v[68:71], v[112:115], v[160:163], 0\n"
      "ds_read_b64 v[140:141], v40 offset:2112\n"
      "v_mfma_f32_16x16x32_bf16 v[76:79], v[112:115], v[164:167], 0\n"
      "ds_read_b64 v[142:143], v40 offset:2120\n"
      "v_mfma_f32_16x16x32_bf16 v[76:79], v[124:127], v[160:163], v[76:79]\n"
      "ds_read_b64 v[152:153], v40 offset:3136\n"
      "s_load_dwordx16 s[60:75], s[56:57], 0xc0\n"
      "v_mfma_f32_16x16x32_bf16 v[84:87], v[112:115], v[168:171], 0\n"
      "ds_write_b128 v62, v[100:103]\n"
      "ds_read_b64 v[154:155], v40 offset:3144\n"
      "s_load_dwordx4 s[92:95], s[56:57], 0xa0\n"
      "v_mfma_f32_16x16x32_bf16 v[84:87], v[124:127], v[164:167], v[84:87]\n"
      "v_mfma_f32_16x16x32_bf16 v[84:87], v[136:139], v[160:163], v[84:87]\n"
      "v_mfma_f32_16x16x32_bf16 v[92:95], v[112:115], v[172:175], 0\n"
      "v_mfma_f32_16x16x32_bf16 v[92:95], v[124:127], v[168:171], v[92:95]\n"
      "ds_write_b128 v62, v[108:111] offset:2048\n"
      "v_mfma_f32_16x16x32_bf16 v[92:95], v[136:139], v[164:167], v[92:95]\n"
      "v_mfma_f32_16x16x32_bf16 v[92:95], v[148:151], v[160:163], v[92:95]\n"
      "s_waitcnt lgkmcnt(11)\n"
      "v_mfma_f32_16x16x32_bf16 v[64:67], v[112:115], v[176:179], v[64:67]\n"
      "buffer_load_dwordx2 v[254:255], v2, s[32:35], s45 offen sc1\n"
      "v_mfma_f32_16x16x32_bf16 v[72:75], v[112:115], v[180:183], v[72:75]\n"
      "ds_read_b128 v[160:163], v41 offset:4704\n"
      "v_mfma_f32_16x16x32_bf16 v[72:75], v[124:127], v[176:179], v[72:75]\n"
      "ds_read_b128 v[164:167], v41 offset:3168\n"
      "v_mfma_f32_16x16x32_bf16 v[80:83], v[112:115], v[184:187], v[80:83]\n"
      "ds_read_b128 v[168:171], v41 offset:1632\n"
      "v_mfma_f32_16x16x32_bf16 v[80:83], v[124:127], v[180:183], v[80:83]\n"
      "buffer_load_dwordx2 v[204:205], v58, s[32:35], s83 offen offset:0 sc1\n"
      "v_mfma_f32_16x16x32_bf16 v[80:83], v[136:139], v[176:179], v[80:83]\n"
      "ds_read_b128 v[172:175], v41 offset:96\n"
      "v_mfma_f32_16x16x32_bf16 v[88:91], v[112:115], v[188:191], v[88:91]\n"
      "s_waitcnt lgkmcnt(4)\n"
      "s_barrier\n"
      "v_mfma_f32_16x16x32_bf16 v[88:91], v[124:127], v[184:187], v[88:91]\n"
      "ds_read_b128 v[96:99], v45 offset:0\n"
      "v_mfma_f32_16x16x32_bf16 v[88:91], v[136:139], v[180:183], v[88:91]\n"
      "ds_read_b128 v[100:103], v45 offset:4096\n"
      "v_mfma_f32_16x16x32_bf16 v[88:91], v[148:151], v[176:179], v[88:91]\n"
      "ds_read_b128 v[104:107], v45 offset:8192\n"
      "v_mfma_f32_16x16x32_bf16 v[68:71], v[116:119], v[176:179], v[68:71]\n"
      "ds_read_b128 v[108:111], v45 offset:12288\n"
      "v_mfma_f32_16x16x32_bf16 v[76:79], v[116:119], v[180:183], v[76:79]\n"
      "ds_read_b64 v[202:203], v47\n"
      "v_mfma_f32_16x16x32_bf16 v[76:79], v[128:131], v[176:179], v[76:79]\n"
      "ds_read_b64 v[120:121], v40 offset:96\n"
      "v_mfma_f32_16x16x32_bf16 v[84:87], v[116:119], v[184:187], v[84:87]\n"
      "ds_read_b64 v[122:123], v40 offset:104\n"
      "v_mfma_f32_16x16x32_bf16 v[84:87], v[128:131], v[180:183], v[84:87]\n"
      "ds_read_b64 v[132:133], v40 offset:1120\n"
      "v_mfma_f32_16x16x32_bf16 v[84:87], v[140:143], v[176:179], v[84:87]\n"
      "ds_read_b64 v[134:135], v40 offset:1128\n"
      "v_mfma_f32_16x16x32_bf16 v[92:95], v[116:119], v[188:191], v[92:95]\n"
      "ds_read_b64 v[144:145], v40 offset:2144\n"
      "s_waitcnt lgkmcnt(5)\n"
      "v_mfma_f32_16x16x32_bf16 v[92:95], v[128:131], v[184:187], v[92:95]\n"
      "ds_read_b64 v[146:147], v40 offset:2152\n"
      "v_add_f32 v96, v96, v100\n"
      "v_mfma_f32_16x16x32_bf16 v[92:95], v[140:143], v[180:183], v[92:95]\n"
      "ds_read_b64 v[156:157], v40 offset:3168\n"
      "v_add_f32 v97, v97, v101\n"
      "v_mfma_f32_16x16x32_bf16 v[92:95], v[152:155], v[176:179], v[92:95]\n"
      "ds_read_b64 v[158:159], v40 offset:3176\n"
      "v_mfma_f32_16x16x32_bf16 v[64:67], v[116:119], v[160:163], v[64:67]\n"
      "ds_read_b128 v[176:179], v41 offset:4736\n"
      "v_add_f32 v98, v98, v102\n"
      "v_mfma_f32_16x16x32_bf16 v[72:75], v[116:119], v[164:167], v[72:75]\n"
      "ds_read_b128 v[180:183], v41 offset:3200\n"
      "v_add_f32 v99, v99, v103\n"
      "v_mfma_f32_16x16x32_bf16 v[72:75], v[128:131], v[160:163], v[72:75]\n"
      "ds_read_b128 v[184:187], v41 offset:1664\n"
      "v_add_f32 v104, v104, v108\n"
      "v_mfma_f32_16x16x32_bf16 v[80:83], v[116:119], v[168:171], v[80:83]\n"
      "ds_read_b128 v[188:191], v41 offset:128\n"
      "v_add_f32 v105, v105, v109\n"
      "v_mfma_f32_16x16x32_bf16 v[80:83], v[128:131], v[164:167], v[80:83]\n"
      "ds_read_b64 v[112:113], v40 offset:128\n"
      "v_add_f32 v106, v106, v110\n"
      "v_mfma_f32_16x16x32_bf16 v[80:83], v[140:143], v[160:163], v[80:83]\n"
      "ds_read_b64 v[114:115], v40 offset:136\n"
      "v_add_f32 v107, v107, v111\n"
      "v_mfma_f32_16x16x32_bf16 v[88:91], v[116:119], v[172:175], v[88:91]\n"
      "ds_read_b64 v[124:125], v40 offset:1152\n"
      "v_add_f32 v96, v96, v104\n"
      "v_mfma_f32_16x16x32_bf16 v[88:91], v[128:131], v[168:171], v[88:91]\n"
      "ds_read_b64 v[126:127], v40 offset:1160\n"
      "v_add_f32 v97, v97, v105\n"
      "v_mfma_f32_16x16x32_bf16 v[88:91], v[140:143], v[164:167], v[88:91]\n"
      "ds_read_b64 v[136:137], v40 offset:2176\n"
      "v_add_f32 v98, v98, v106\n"
      "v_mfma_f32_16x16x32_bf16 v[88:91], v[152:155], v[160:163], v[88:91]\n"
      "ds_read_b64 v[138:139], v40 offset:2184\n"
      "s_waitcnt lgkmcnt(10)\n"
      "v_mfma_f32_16x16x32_bf16 v[68:71], v[120:123], v[160:163], v[68:71]\n"
      "buffer_load_dwordx2 v[200:201], v4, s[32:35], s80 offen offset:0 sc1\n"
      "v_add_f32 v99, v99, v107\n"
      "v_mfma_f32_16x16x32_bf16 v[76:79], v[120:123], v[164:167], v[76:79]\n"
      "ds_read_b64 v[148:149], v40 offset:3200\n"
      "v_cvt_pk_bf16_f32 v96, 0, v96\n"
      "v_mfma_f32_16x16x32_bf16 v[76:79], v[132:135], v[160:163], v[76:79]\n"
      "buffer_load_dword v250, v61, s[32:35], s85 offen offset:0 sc1\n"
      "v_cvt_pk_bf16_f32 v97, 0, v97\n"
      "v_mfma_f32_16x16x32_bf16 v[84:87], v[120:123], v[168:171], v[84:87]\n"
      "ds_read_b64 v[150:151], v40 offset:3208\n"
      "v_cvt_pk_bf16_f32 v98, 0, v98\n"
      "v_mfma_f32_16x16x32_bf16 v[84:87], v[132:135], v[164:167], v[84:87]\n"
      "buffer_load_dwordx4 v[224:227], v57, s[32:35], s82 offen offset:0 sc1\n"
      "v_cvt_pk_bf16_f32 v99, 0, v99\n"
      "v_mfma_f32_16x16x32_bf16 v[84:87], v[144:147], v[160:163], v[84:87]\n"
      "s_lshr_b32 s58, s3, 1\n"
      "s_mul_i32 s58, s58, 0x3f800000\n"
      "v_mfma_f32_16x16x32_bf16 v[92:95], v[120:123], v[172:175], v[92:95]\n"
      "v_and_b32 v100, 0xffff0000, v203\n"
      "v_lshlrev_b32 v101, 16, v203\n"
      "v_mfma_f32_16x16x32_bf16 v[92:95], v[132:135], v[168:171], v[92:95]\n"
      "buffer_load_dwordx4 v[228:231], v57, s[32:35], s82 offen offset:1024 sc1\n"
      "v_and_b32 v102, 0xffff0000, v202\n"
      "v_mfma_f32_16x16x32_bf16 v[92:95], v[144:147], v[164:167], v[92:95]\n"
      "v_lshlrev_b32 v103, 16, v202\n"
      "v_mul_f32 v100, s25, v100\n"
      "v_mfma_f32_16x16x32_bf16 v[92:95], v[156:159], v[160:163], v[92:95]\n"
      "v_mul_f32 v101, s25, v101\n"
      "s_waitcnt lgkmcnt(8)\n"
      "v_mfma_f32_16x16x32_bf16 v[64:67], v[120:123], v[176:179], v[64:67]\n"
      "buffer_load_dwordx4 v[192:195], v54, s[32:35], s53 offen offset:0 sc1\n"
      "s_waitcnt vmcnt(6)\n"
      "v_cmp_ne_u32 vcc, s30, v255\n"
      "v_mfma_f32_16x16x32_bf16 v[72:75], v[120:123], v[180:183], v[72:75]\n"
      "ds_read_b128 v[160:163], v41 offset:4800\n"
      "v_mul_f32 v102, s25, v102\n"
      "v_mfma_f32_16x16x32_bf16 v[72:75], v[132:135], v[176:179], v[72:75]\n"
      "ds_read_b128 v[164:167], v41 offset:3264\n"
      "s_cbranch_vccnz L_cm_s%=\n"
      "L_cm_r%=:\n"
      "v_add_f32_dpp v254, v254, v254 quad_perm:[1,0,3,2] row_mask:0xf bank_mask:0xf\n"
      "v_mfma_f32_16x16x32_bf16 v[80:83], v[120:123], v[184:187], v[80:83]\n"
      "buffer_load_dwordx4 v[196:199], v54, s[32:35], s54 offen offset:0 sc1\n"
      "v_mul_f32 v103, s25, v103\n"
      "v_mfma_f32_16x16x32_bf16 v[80:83], v[132:135], v[180:183], v[80:83]\n"
      "ds_read_b128 v[168:171], v41 offset:1728\n"
      "v_add_f32_dpp v254, v254, v254 quad_perm:[2,3,0,1] row_mask:0xf bank_mask:0xf\n"
      "v_mfma_f32_16x16x32_bf16 v[80:83], v[144:147], v[176:179], v[80:83]\n"
      "ds_read_b128 v[172:175], v41 offset:192\n"
      "v_cvt_pk_bf16_f32 v254, 0, v254\n"
      "v_mfma_f32_16x16x32_bf16 v[88:91], v[120:123], v[188:191], v[88:91]\n"
      "buffer_load_dword v248, v60, s[32:35], s84 offen offset:0 sc1\n"
      "v_add_f32 v254, s58, v254\n"
      "v_mfma_f32_16x16x32_bf16 v[88:91], v[132:135], v[184:187], v[88:91]\n"
      "ds_read_b64 v[116:117], v40 offset:160\n"
      "v_cvt_pk_bf16_f32 v254, v254, v254\n"
      "v_mfma_f32_16x16x32_bf16 v[88:91], v[144:147], v[180:183], v[88:91]\n"
      "ds_write_b16 v3, v254\n"
      "ds_read_b64 v[118:119], v40 offset:168\n"
      "v_cvt_pk_bf16_f32 v100, 0, v100\n"
      "v_mfma_f32_16x16x32_bf16 v[88:91], v[156:159], v[176:179], v[88:91]\n"
      "buffer_load_dword v249, v60, s[32:35], s84 offen offset:2048 sc1\n"
      "s_waitcnt lgkmcnt(7)\n"
      "v_mfma_f32_16x16x32_bf16 v[68:71], v[112:115], v[176:179], v[68:71]\n"
      "ds_read_b64 v[128:129], v40 offset:1184\n"
      "v_cvt_pk_bf16_f32 v101, 0, v101\n"
      "v_mfma_f32_16x16x32_bf16 v[76:79], v[112:115], v[180:183], v[76:79]\n"
      "ds_read_b64 v[130:131], v40 offset:1192\n"
      "v_cvt_pk_bf16_f32 v102, 0, v102\n"
      "v_mfma_f32_16x16x32_bf16 v[76:79], v[124:127], v[176:179], v[76:79]\n"
      "buffer_load_dwordx4 v[208:211], v55, s[32:35], 0 offen offset:0 sc1\n"
      "v_cvt_pk_bf16_f32 v103, 0, v103\n"
      "v_mfma_f32_16x16x32_bf16 v[84:87], v[112:115], v[184:187], v[84:87]\n"
      "ds_read_b64 v[140:141], v40 offset:2208\n"
      "s_waitcnt lgkmcnt(4)\n"
      "s_barrier\n"
      "v_mfma_f32_16x16x32_bf16 v[84:87], v[124:127], v[180:183], v[84:87]\n"
      "ds_read2_b64 v[24:27], v236 offset0:0 offset1:4\n"
      "v_add_f32 v96, v96, v100\n"
      "v_mfma_f32_16x16x32_bf16 v[84:87], v[136:139], v[176:179], v[84:87]\n"
      "buffer_load_dwordx2 v[252:253], v51, s[32:35], s76 offen offset:0 sc1\n"
      "v_add_f32 v97, v97, v101\n"
      "v_mfma_f32_16x16x32_bf16 v[92:95], v[112:115], v[188:191], v[92:95]\n"
      "ds_read_b64 v[142:143], v40 offset:2216\n"
      "v_add_f32 v98, v98, v102\n"
      "v_mfma_f32_16x16x32_bf16 v[92:95], v[124:127], v[184:187], v[92:95]\n"
      "ds_read_b64 v[152:153], v40 offset:3232\n"
      "v_add_f32 v99, v99, v103\n"
      "v_mfma_f32_16x16x32_bf16 v[92:95], v[136:139], v[180:183], v[92:95]\n"
      "ds_read_b64 v[154:155], v40 offset:3240\n"
      "s_mov_b32 s48, 0\n"
      "s_mov_b32 s49, -1\n"
      "v_mfma_f32_16x16x32_bf16 v[92:95], v[148:151], v[176:179], v[92:95]\n"
      "s_waitcnt lgkmcnt(3)\n"
      "v_mfma_f32_16x16x32_bf16 v[64:67], v[112:115], v[160:163], v[64:67]\n"
      "ds_read_b128 v[176:179], v41 offset:4832\n"
      "v_mfma_f32_16x16x32_bf16 v[232:235], v[24:27], v[8:11], 0\n"
      "v_mfma_f32_16x16x32_bf16 v[72:75], v[112:115], v[164:167], v[72:75]\n"
      "ds_read_b128 v[180:183], v41 offset:3296\n"
      "v_mfma_f32_16x16x32_bf16 v[8:11], v[24:27], v[12:15], 0\n"
      "v_mfma_f32_16x16x32_bf16 v[72:75], v[124:127], v[160:163], v[72:75]\n"
      "ds_read_b128 v[184:187], v41 offset:1760\n"
      "v_mfma_f32_16x16x32_bf16 v[12:15], v[24:27], v[16:19], 0\n"
      "v_mfma_f32_16x16x32_bf16 v[80:83], v[112:115], v[168:171], v[80:83]\n"
      "buffer_load_dwordx4 v[212:215], v55, s[32:35], 0 offen offset:1024 sc1\n"
      "v_mfma_f32_16x16x32_bf16 v[16:19], v[24:27], v[20:23], 0\n"
      "v_mfma_f32_16x16x32_bf16 v[80:83], v[124:127], v[164:167], v[80:83]\n"
      "ds_read_b128 v[188:191], v41 offset:224\n"
      "v_cvt_pk_bf16_f32 v96, 0, v96\n"
      "v_mfma_f32_16x16x32_bf16 v[80:83], v[136:139], v[160:163], v[80:83]\n"
      "ds_read_b64 v[120:121], v40 offset:192\n"
      "v_cvt_pk_bf16_f32 v97, 0, v97\n"
      "v_mfma_f32_16x16x32_bf16 v[88:91], v[112:115], v[172:175], v[88:91]\n"
      "buffer_load_dwordx4 v[216:219], v55, s[32:35], 0 offen offset:2048 sc1\n"
      "v_cndmask_b32 v206, v232, v12, s[48:49]\n"
      "v_cndmask_b32 v207, v8, v16, s[48:49]\n"
      "v_mfma_f32_16x16x32_bf16 v[88:91], v[124:127], v[168:171], v[88:91]\n"
      "ds_read_b64 v[122:123], v40 offset:200\n"
      "v_cvt_pk_bf16_f32 v98, 0, v98\n"
      "v_mfma_f32_16x16x32_bf16 v[88:91], v[136:139], v[164:167], v[88:91]\n"
      "ds_read_b64 v[132:133], v40 offset:1216\n"
      "v_cvt_pk_bf16_f32 v99, 0, v99\n"
      "v_mfma_f32_16x16x32_bf16 v[88:91], v[148:151], v[160:163], v[88:91]\n"
      "buffer_load_dwordx4 v[220:223], v55, s[32:35], 0 offen offset:3072 sc1\n"
      "s_waitcnt lgkmcnt(7)\n"
      "v_mfma_f32_16x16x32_bf16 v[68:71], v[116:119], v[160:163], v[68:71]\n"
      "ds_read_b64 v[134:135], v40 offset:1224\n"
      "v_mfma_f32_16x16x32_bf16 v[76:79], v[116:119], v[164:167], v[76:79]\n"
      "ds_read_b64 v[144:145], v40 offset:2240\n"
      "v_mfma_f32_16x16x32_bf16 v[76:79], v[128:131], v[160:163], v[76:79]\n"
      "ds_read_b64 v[146:147], v40 offset:2248\n"
      "v_mfma_f32_16x16x32_bf16 v[84:87], v[116:119], v[168:171], v[84:87]\n"
      "ds_read_b64 v[156:157], v40 offset:3264\n"
      "v_mfma_f32_16x16x32_bf16 v[84:87], v[128:131], v[164:167], v[84:87]\n"
      "ds_read_b64 v[158:159], v40 offset:3272\n"
      "s_waitcnt vmcnt(9)\n"
      "v_mfma_f32_16x16x32_bf16 v[84:87], v[140:143], v[160:163], v[84:87]\n"
      "v_add_u32 v251, v200, v201\n"
      "v_add3_u32 v34, v250, v227, v231\n"
      "s_nop 0\n"
      "v_add_u32_dpp v251, v251, v251 row_half_mirror row_mask:0xf bank_mask:0xf\n"
      "s_nop 1\n"
      "v_mfma_f32_16x16x32_bf16 v[92:95], v[116:119], v[172:175], v[92:95]\n"
      "v_add_u32_dpp v251, v251, v251 quad_perm:[1,0,3,2] row_mask:0xf bank_mask:0xf\n"
      "s_nop 1\n"
      "v_add_u32_dpp v251, v251, v251 quad_perm:[2,3,0,1] row_mask:0xf bank_mask:0xf\n"
      "v_add3_u32 v251, v251, v34, v205\n"
      "v_mfma_f32_16x16x32_bf16 v[92:95], v[128:131], v[168:171], v[92:95]\n"
      "v_cmp_ne_u32 vcc, s24, v251\n"
      "v_mfma_f32_16x16x32_bf16 v[92:95], v[140:143], v[164:167], v[92:95]\n"
      "v_mfma_f32_16x16x32_bf16 v[92:95], v[152:155], v[160:163], v[92:95]\n"
      "s_cbranch_vccnz L_cv_s%=\n"
      "L_cv_r%=:\n"
      "v_add_f32_dpp v204, v204, v204 quad_perm:[1,0,3,2] row_mask:0xf bank_mask:0xf\n"
      "v_lshlrev_b32 v100, 16, v200\n"
      "v_and_b32 v101, 0xffff0000, v200\n"
      "s_waitcnt lgkmcnt(8)\n"
      "v_mfma_f32_16x16x32_bf16 v[64:67], v[116:119], v[176:179], v[64:67]\n"
      "v_add_f32_dpp v204, v204, v204 quad_perm:[2,3,0,1] row_mask:0xf bank_mask:0xf\n"
      "v_cvt_pk_bf16_f32 v204, 0, v204\n"
      "v_mfma_f32_16x16x32_bf16 v[72:75], v[116:119], v[180:183], v[72:75]\n"
      "ds_read_b128 v[160:163], v41 offset:4896\n"
      "v_lshlrev_b32 v102, 16, v201\n"
      "v_add_f32 v205, 1.0, v204\n"
      "v_and_b32 v103, 0xffff0000, v201\n"
      "v_mfma_f32_16x16x32_bf16 v[72:75], v[128:131], v[176:179], v[72:75]\n"
      "ds_read_b128 v[164:167], v41 offset:3360\n"
      "v_cvt_pk_bf16_f32 v205, 0, v205\n"
      "v_lshlrev_b32 v104, 16, v226\n"
      "v_mfma_f32_16x16x32_bf16 v[80:83], v[116:119], v[184:187], v[80:83]\n"
      "v_readlane_b32 s40, v204, 32\n"
      "v_and_b32 v105, 0xffff0000, v226\n"
      "v_readlane_b32 s41, v205, 36\n"
      "v_mfma_f32_16x16x32_bf16 v[80:83], v[128:131], v[180:183], v[80:83]\n"
      "ds_read_b128 v[168:171], v41 offset:1824\n"
      "v_lshlrev_b32 v106, 16, v230\n"
      "v_readlane_b32 s42, v204, 40\n"
      "v_mfma_f32_16x16x32_bf16 v[80:83], v[140:143], v[176:179], v[80:83]\n"
      "v_and_b32 v107, 0xffff0000, v230\n"
      "v_readlane_b32 s43, v205, 44\n"
      "v_mfma_f32_16x16x32_bf16 v[88:91], v[116:119], v[188:191], v[88:91]\n"
      "ds_read_b128 v[172:175], v41 offset:288\n"
      "v_sub_f32 v104, v100, v104\n"
      "v_sub_f32 v105, v101, v105\n"
      "v_sub_f32 v106, v102, v106\n"
      "s_waitcnt vmcnt(3)\n"
      "v_mfma_f32_16x16x32_bf16 v[88:91], v[128:131], v[184:187], v[88:91]\n"
      "ds_read_b64 v[112:113], v40 offset:224\n"
      "v_sub_f32 v107, v103, v107\n"
      "v_cvt_pk_bf16_f32 v104, 0, v104\n"
      "v_cmp_ne_u64 vcc, s[30:31], v[252:253]\n"
      "v_mfma_f32_16x16x32_bf16 v[88:91], v[140:143], v[180:183], v[88:91]\n"
      "ds_read_b64 v[114:115], v40 offset:232\n"
      "v_cvt_pk_bf16_f32 v105, 0, v105\n"
      "v_cvt_pk_bf16_f32 v106, 0, v106\n"
      "v_cvt_pk_bf16_f32 v107, 0, v107\n"
      "v_mfma_f32_16x16x32_bf16 v[88:91], v[152:155], v[176:179], v[88:91]\n"
      "ds_read_b64 v[124:125], v40 offset:1248\n"
      "v_mul_f32 v104, v104, v224\n"
      "v_mul_f32 v105, v105, v225\n"
      "s_cbranch_vccnz L_cw_s%=\n"
      "L_cw_r%=:\n"
      "v_add_u32 v35, 4096, v7\n"
      "s_waitcnt lgkmcnt(7)\n"
      "v_mfma_f32_16x16x32_bf16 v[68:71], v[120:123], v[176:179], v[68:71]\n"
      "global_load_dwordx4 a[0:3], v35, s[8:9] offset:-4096 sc1\n"
      "v_mul_f32 v106, v106, v228\n"
      "v_mul_f32 v107, v107, v229\n"
      "v_mfma_f32_16x16x32_bf16 v[76:79], v[120:123], v[180:183], v[76:79]\n"
      "ds_read_b64 v[126:127], v40 offset:1256\n"
      "v_cvt_pk_bf16_f32 v104, 0, v104\n"
      "v_cvt_pk_bf16_f32 v105, 0, v105\n"
      "v_cvt_pk_bf16_f32 v106, 0, v106\n"
      "v_mfma_f32_16x16x32_bf16 v[76:79], v[132:135], v[176:179], v[76:79]\n"
      "ds_read_b64 v[136:137], v40 offset:2272\n"
      "v_cvt_pk_bf16_f32 v107, 0, v107\n"
      "v_mul_f32 v104, s41, v104\n"
      "v_mfma_f32_16x16x32_bf16 v[84:87], v[120:123], v[184:187], v[84:87]\n"
      "global_load_dwordx4 a[4:7], v35, s[8:9] offset:-3072 sc1\n"
      "v_mul_f32 v105, s41, v105\n"
      "v_mul_f32 v106, s41, v106\n"
      "v_mul_f32 v107, s41, v107\n"
      "v_mfma_f32_16x16x32_bf16 v[84:87], v[132:135], v[180:183], v[84:87]\n"
      "ds_read_b64 v[138:139], v40 offset:2280\n"
      "v_cvt_pk_bf16_f32 v104, 0, v104\n"
      "v_cvt_pk_bf16_f32 v105, 0, v105\n"
      "v_mfma_f32_16x16x32_bf16 v[84:87], v[144:147], v[176:179], v[84:87]\n"
      "ds_read_b64 v[148:149], v40 offset:3296\n"
      "v_cvt_pk_bf16_f32 v106, 0, v106\n"
      "v_cvt_pk_bf16_f32 v107, 0, v107\n"
      "v_mfma_f32_16x16x32_bf16 v[92:95], v[120:123], v[188:191], v[92:95]\n"
      "global_load_dwordx4 a[8:11], v35, s[8:9] offset:-2048 sc1\n"
      "v_add_f32 v104, s40, v104\n"
      "v_add_f32 v105, s40, v105\n"
      "v_add_f32 v106, s40, v106\n"
      "v_mfma_f32_16x16x32_bf16 v[92:95], v[132:135], v[184:187], v[92:95]\n"
      "ds_read_b64 v[150:151], v40 offset:3304\n"
      "v_add_f32 v107, s40, v107\n"
      "v_cvt_pk_bf16_f32 v104, 0, v104\n"
      "v_mfma_f32_16x16x32_bf16 v[92:95], v[144:147], v[180:183], v[92:95]\n"
      "v_cvt_pk_bf16_f32 v105, 0, v105\n"
      "v_cvt_pk_bf16_f32 v106, 0, v106\n"
      "v_cvt_pk_bf16_f32 v107, 0, v107\n"
      "v_mfma_f32_16x16x32_bf16 v[92:95], v[156:159], v[176:179], v[92:95]\n"
      "global_load_dwordx4 a[12:15], v35, s[8:9] offset:-1024 sc1\n"
      "v_mul_f32 v104, v104, v96\n"
      "v_mul_f32 v105, v105, v97\n"
      "s_waitcnt lgkmcnt(8)\n"
      "v_mfma_f32_16x16x32_bf16 v[64:67], v[120:123], v[160:163], v[64:67]\n"
      "ds_read_b128 v[176:179], v41 offset:4928\n"
      "v_mul_f32 v106, v106, v98\n"
      "v_mul_f32 v107, v107, v99\n"
      "v_cvt_pk_bf16_f32 v104, 0, v104\n"
      "v_mfma_f32_16x16x32_bf16 v[72:75], v[120:123], v[164:167], v[72:75]\n"
      "ds_read_b128 v[180:183], v41 offset:3392\n"
      "v_cvt_pk_bf16_f32 v105, 0, v105\n"
      "v_cvt_pk_bf16_f32 v106, 0, v106\n"
      "v_mfma_f32_16x16x32_bf16 v[72:75], v[132:135], v[160:163], v[72:75]\n"
      "global_load_dwordx4 a[16:19], v35, s[8:9] offset:0 sc1\n"
      "v_cvt_pk_bf16_f32 v107, 0, v107\n"
      "v_mul_f32 v104, s42, v104\n"
      "v_mfma_f32_16x16x32_bf16 v[80:83], v[120:123], v[168:171], v[80:83]\n"
      "ds_read_b128 v[184:187], v41 offset:1856\n"
      "v_mul_f32 v105, s42, v105\n"
      "v_mul_f32 v106, s42, v106\n"
      "v_mul_f32 v107, s42, v107\n"
      "v_mfma_f32_16x16x32_bf16 v[80:83], v[132:135], v[164:167], v[80:83]\n"
      "ds_read_b128 v[188:191], v41 offset:320\n"
      "v_cvt_pk_bf16_f32 v104, 0, v104\n"
      "v_cvt_pk_bf16_f32 v105, 0, v105\n"
      "v_mfma_f32_16x16x32_bf16 v[80:83], v[144:147], v[160:163], v[80:83]\n"
      "global_load_dwordx4 a[20:23], v35, s[8:9] offset:1024 sc1\n"
      "v_cvt_pk_bf16_f32 v106, 0, v106\n"
      "v_cvt_pk_bf16_f32 v107, 0, v107\n"
      "v_add_f32 v104, v104, v100\n"
      "v_mfma_f32_16x16x32_bf16 v[88:91], v[120:123], v[172:175], v[88:91]\n"
      "ds_read_b64 v[116:117], v40 offset:256\n"
      "v_add_f32 v105, v105, v101\n"
      "v_add_f32 v106, v106, v102\n"
      "v_mfma_f32_16x16x32_bf16 v[88:91], v[132:135], v[168:171], v[88:91]\n"
      "ds_read_b64 v[118:119], v40 offset:264\n"
      "v_add_f32 v107, v107, v103\n"
      "v_cvt_pk_bf16_f32 v104, 0, v104\n"
      "v_mfma_f32_16x16x32_bf16 v[88:91], v[144:147], v[164:167], v[88:91]\n"
      "global_load_dwordx4 a[24:27], v35, s[8:9] offset:2048 sc1\n"
      "ds_read_b64_tr_b16 v[240:241], v28 offset:0\n"
      "ds_read_b64_tr_b16 v[242:243], v28 offset:1024\n"
      "ds_read_b64_tr_b16 v[244:245], v28 offset:32\n"
      "ds_read_b64_tr_b16 v[246:247], v28 offset:1056\n"
      "v_cvt_pk_bf16_f32 v105, 0, v105\n"
      "v_cvt_pk_bf16_f32 v106, 0, v106\n"
      "v_cvt_pk_bf16_f32 v107, 0, v107\n"
      "v_mfma_f32_16x16x32_bf16 v[88:91], v[156:159], v[160:163], v[88:91]\n"
      "ds_read_b64 v[128:129], v40 offset:1280\n"
      "v_cvt_pk_bf16_f32 v108, v104, v105\n"
      "v_cvt_pk_bf16_f32 v109, v106, v107\n"
      "s_waitcnt lgkmcnt(11)\n"
      "v_mfma_f32_16x16x32_bf16 v[68:71], v[112:115], v[160:163], v[68:71]\n"
      "ds_read_b64 v[130:131], v40 offset:1288\n"
      "v_mul_f32 v104, s43, v104\n"
      "v_mul_f32 v105, s43, v105\n"
      "v_mul_f32 v106, s43, v106\n"
      "v_mfma_f32_16x16x32_bf16 v[76:79], v[112:115], v[164:167], v[76:79]\n"
      "global_load_dwordx4 a[28:31], v35, s[8:9] offset:3072 sc1\n"
      "ds_read_b64 v[140:141], v40 offset:2304\n"
      "v_mul_f32 v107, s43, v107\n"
      "v_cvt_pk_bf16_f32 v110, v104, v105\n"
      "v_mfma_f32_16x16x32_bf16 v[76:79], v[124:127], v[160:163], v[76:79]\n"
      "ds_read_b64 v[142:143], v40 offset:2312\n"
      "v_add_u32 v35, 12288, v7\n"
      "v_cvt_pk_bf16_f32 v111, v106, v107\n"
      "v_add_u32 v96, v108, v109\n"
      "v_mfma_f32_16x16x32_bf16 v[84:87], v[112:115], v[168:171], v[84:87]\n"
      "ds_read_b64 v[152:153], v40 offset:3328\n"
      "v_add_u32 v97, v110, v111\n"
      "v_mfma_f32_16x16x32_bf16 v[84:87], v[124:127], v[164:167], v[84:87]\n"
      "buffer_store_dwordx2 v[110:111], v50, s[32:35], s78 offen sc1\n"
      "ds_read_b64 v[154:155], v40 offset:3336\n"
      "v_add_u32_dpp v96, v96, v96 quad_perm:[1,0,3,2] row_mask:0xf bank_mask:0xf\n"
      "v_add_u32_dpp v97, v97, v97 quad_perm:[1,0,3,2] row_mask:0xf bank_mask:0xf\n"
      "v_mfma_f32_16x16x32_bf16 v[84:87], v[136:139], v[160:163], v[84:87]\n"
      "v_add_u32_dpp v96, v96, v96 quad_perm:[2,3,0,1] row_mask:0xf bank_mask:0xf\n"
      "v_add_u32_dpp v97, v97, v97 quad_perm:[2,3,0,1] row_mask:0xf bank_mask:0xf\n"
      "s_mul_i32 s47, s30, 6\n"
      "v_mfma_f32_16x16x32_bf16 v[92:95], v[112:115], v[172:175], v[92:95]\n"
      "global_load_dwordx4 a[32:35], v35, s[8:9] offset:-4096 sc1\n"
      "v_add_u32_dpp v96, v96, v96 row_half_mirror row_mask:0xf bank_mask:0xf\n"
      "v_add_u32_dpp v97, v97, v97 row_half_mirror row_mask:0xf bank_mask:0xf\n"
      "s_waitcnt vmcnt(10)\n"
      "s_waitcnt lgkmcnt(6)\n"
      "v_mfma_f32_16x16x32_bf16 v[92:95], v[124:127], v[168:171], v[92:95]\n"
      "buffer_store_dwordx2 v[108:109], v37, s[32:35], s55 offen sc1\n"
      "v_mov_b32 v98, v96\n"
      "v_add_u32_dpp v97, v97, v97 row_mirror row_mask:0xf bank_mask:0xf\n"
      "v_add3_u32 v251, v192, v193, v194\n"
      "v_add3_u32 v251, v251, v195, v196\n"
      "v_mfma_f32_16x16x32_bf16 v[92:95], v[136:139], v[164:167], v[92:95]\n"
      "s_mov_b32 s42, 0xaaaa5555\n"
      "s_mov_b32 s43, s42\n"
      "v_add3_u32 v251, v251, v197, v198\n"
      "v_mfma_f32_16x16x32_bf16 v[92:95], v[148:151], v[160:163], v[92:95]\n"
      "v_permlane16_swap_b32 v96, v98\n"
      "v_add_u32 v96, v96, v98\n"
      "v_add_u32 v251, v251, v199\n"
      "s_waitcnt lgkmcnt(0)\n"
      "v_mfma_f32_16x16x32_bf16 v[64:67], v[112:115], v[176:179], v[64:67]\n"
      "v_cndmask_b32 v96, v97, v96, s[42:43]\n"
      "v_sub_u32 v96, v52, v96\n"
      "v_add3_u32 v34, v248, v249, v211\n"
      "v_mfma_f32_16x16x32_bf16 v[68:71], v[116:119], v[176:179], v[68:71]\n"
      "buffer_store_dword v96, v49, s[32:35], s77 offen sc1\n"
      "v_add3_u32 v34, v34, v215, v219\n"
      "v_mfma_f32_16x16x32_bf16 v[72:75], v[112:115], v[180:183], v[72:75]\n"
      "v_add_u32_dpp v251, v251, v251 quad_perm:[1,0,3,2] row_mask:0xf bank_mask:0xf\n"
      "s_nop 1\n"
      "v_mfma_f32_16x16x32_bf16 v[72:75], v[124:127], v[176:179], v[72:75]\n"
      "buffer_store_dwordx4 v[240:243], v7, s[32:35], s86 offen sc1\n"
      "ds_write_b128 v44, v[64:67] offset:0\n"
      "v_add_u32_dpp v251, v251, v251 quad_perm:[2,3,0,1] row_mask:0xf bank_mask:0xf\n"
      "v_add3_u32 v251, v251, v34, v223\n"
      "v_cmp_ne_u32 vcc, s47, v251\n"
      "v_mfma_f32_16x16x32_bf16 v[76:79], v[116:119], v[180:183], v[76:79]\n"
      "buffer_store_dwordx4 v[244:247], v7, s[32:35], s87 offen sc1\n"
      "global_load_dwordx4 a[36:39], v35, s[8:9] offset:-3072 sc1\n"
      "ds_write_b128 v59, v[68:71] offset:0\n"
      "s_add_u32 s4, s26, s71\n"
      "s_addc_u32 s5, s27, 0\n"
      "s_add_u32 s6, s26, s72\n"
      "s_addc_u32 s7, s27, 0\n"
      "v_mfma_f32_16x16x32_bf16 v[76:79], v[128:131], v[176:179], v[76:79]\n"
      "s_cbranch_vccnz L_c2_s%=\n"
      "L_c2_r%=:\n"
      "v_mfma_f32_16x16x32_bf16 v[80:83], v[112:115], v[184:187], v[80:83]\n"
      "buffer_store_dword v206, v239, s[32:35], s59 offen offset:0 sc1\n"
      "buffer_store_dword v207, v239, s[32:35], s59 offen offset:128 sc1\n"
      "ds_write_b128 v44, v[72:75] offset:2048\n"
      "s_add_u32 s10, s26, s75\n"
      "L_pg2_%=:\n"
      "s_cbranch_execz L_pg6_%=\n"
      "s_addc_u32 s11, s27, 0\n"
      "s_add_u32 s12, s10, 49152\n"
      "s_addc_u32 s13, s11, 0\n"
      "v_mfma_f32_16x16x32_bf16 v[80:83], v[124:127], v[180:183], v[80:83]\n"
      "global_load_dwordx4 a[40:43], v35, s[8:9] offset:-2048 sc1\n"
      "s_lshl_b32 s71, s30, 2\n"
      "v_mfma_f32_16x16x32_bf16 v[80:83], v[136:139], v[176:179], v[80:83]\n"
      "ds_write_b128 v59, v[76:79] offset:2048\n"
      "ds_read_b128 v[232:235], v46\n"
      "ds_read_b128 v[236:239], v46 offset:7680\n"
      "v_mfma_f32_16x16x32_bf16 v[84:87], v[116:119], v[184:187], v[84:87]\n"
      "v_mfma_f32_16x16x32_bf16 v[84:87], v[128:131], v[180:183], v[84:87]\n"
      "global_load_dwordx4 a[44:47], v35, s[8:9] offset:-1024 sc1\n"
      "v_mfma_f32_16x16x32_bf16 v[84:87], v[140:143], v[176:179], v[84:87]\n"
      "ds_write_b128 v44, v[80:83] offset:4096\n"
      "v_mfma_f32_16x16x32_bf16 v[88:91], v[112:115], v[188:191], v[88:91]\n"
      "v_mfma_f32_16x16x32_bf16 v[88:91], v[124:127], v[184:187], v[88:91]\n"
      "global_load_dwordx4 a[48:51], v35, s[8:9] offset:0 sc1\n"
      "v_mfma_f32_16x16x32_bf16 v[88:91], v[136:139], v[180:183], v[88:91]\n"
      "ds_write_b128 v59, v[84:87] offset:4096\n"
      "v_mfma_f32_16x16x32_bf16 v[88:91], v[148:151], v[176:179], v[88:91]\n"
      "v_mfma_f32_16x16x32_bf16 v[92:95], v[116:119], v[188:191], v[92:95]\n"
      "global_load_dwordx4 a[52:55], v35, s[8:9] offset:1024 sc1\n"
      "v_mfma_f32_16x16x32_bf16 v[92:95], v[128:131], v[184:187], v[92:95]\n"
      "v_mfma_f32_16x16x32_bf16 v[92:95], v[140:143], v[180:183], v[92:95]\n"
      "ds_write_b128 v44, v[88:91] offset:6144\n"
      "v_mfma_f32_16x16x32_bf16 v[92:95], v[152:155], v[176:179], v[92:95]\n"
      "global_load_dwordx4 a[56:59], v35, s[8:9] offset:2048 sc1\n"
      "s_nop 7\n"
      "ds_write_b128 v59, v[92:95] offset:6144\n"
      "v_readlane_b32 s40, v204, 0\n"
      "v_readlane_b32 s42, v205, 4\n"
      "v_xor_b32 v33, 16, v31\n"
      "v_readlane_b32 s58, v204, 8\n"
      "v_readlane_b32 s48, v204, 16\n"
      "v_readlane_b32 s50, v205, 20\n"
      "v_readlane_b32 s52, v204, 24\n"
      "v_readlane_b32 s41, v205, 12\n"
      "v_readlane_b32 s49, v205, 28\n"
      "s_mov_b32 vcc_lo, 0xaa55aa55\n"
      "s_mov_b32 vcc_hi, 0xaa55aa55\n"
      "v_add_u32 v40, s73, v7\n"
      "v_lshlrev_b32 v96, 16, v210\n"
      "v_and_b32 v97, 0xffff0000, v210\n"
      "v_lshlrev_b32 v98, 16, v214\n"
      "v_and_b32 v99, 0xffff0000, v214\n"
      "v_lshlrev_b32 v100, 16, v218\n"
      "v_and_b32 v101, 0xffff0000, v218\n"
      "v_lshlrev_b32 v102, 16, v222\n"
      "v_and_b32 v103, 0xffff0000, v222\n"
      "s_waitcnt lgkmcnt(0)\n"
      "s_barrier\n"
      "ds_read_b128 v[64:67], v31 offset:0\n"
      "ds_read_b128 v[68:71], v33 offset:0\n"
      "ds_read_b128 v[72:75], v31 offset:8192\n"
      "ds_read_b128 v[76:79], v33 offset:8192\n"
      "ds_read_b128 v[80:83], v31 offset:16384\n"
      "ds_read_b128 v[84:87], v33 offset:16384\n"
      "ds_read_b128 v[88:91], v31 offset:24576\n"
      "ds_read_b128 v[92:95], v33 offset:24576\n"
      "v_add_u32 v34, 12288, v7\n"
      "global_load_dwordx4 a[60:63], v34, s[8:9] offset:3072 sc1\n"
      "v_lshlrev_b32 v112, 16, v192\n"
      "v_and_b32 v113, 0xffff0000, v192\n"
      "v_lshlrev_b32 v114, 16, v193\n"
      "v_and_b32 v115, 0xffff0000, v193\n"
      "v_lshlrev_b32 v116, 16, v194\n"
      "v_and_b32 v117, 0xffff0000, v194\n"
      "v_lshlrev_b32 v118, 16, v195\n"
      "v_and_b32 v119, 0xffff0000, v195\n"
      "v_pk_add_f32 v[104:105], v[112:113], v[96:97] neg_lo:[0,1] neg_hi:[0,1]\n"
      "v_add_u32 v34, 20480, v7\n"
      "global_load_dwordx4 a[64:67], v34, s[8:9] offset:-4096 sc1\n"
      "v_pk_add_f32 v[106:107], v[114:115], v[98:99] neg_lo:[0,1] neg_hi:[0,1]\n"
      "v_pk_add_f32 v[108:109], v[116:117], v[100:101] neg_lo:[0,1] neg_hi:[0,1]\n"
      "v_pk_add_f32 v[110:111], v[118:119], v[102:103] neg_lo:[0,1] neg_hi:[0,1]\n"
      "v_cvt_pk_bf16_f32 v104, 0, v104\n"
      "v_cvt_pk_bf16_f32 v105, 0, v105\n"
      "v_cvt_pk_bf16_f32 v106, 0, v106\n"
      "v_cvt_pk_bf16_f32 v107, 0, v107\n"
      "v_cvt_pk_bf16_f32 v108, 0, v108\n"
      "v_cvt_pk_bf16_f32 v109, 0, v109\n"
      "global_load_dwordx4 a[68:71], v34, s[8:9] offset:-3072 sc1\n"
      "v_cvt_pk_bf16_f32 v110, 0, v110\n"
      "v_cvt_pk_bf16_f32 v111, 0, v111\n"
      "v_pk_mul_f32 v[104:105], v[104:105], v[208:209]\n"
      "v_pk_mul_f32 v[106:107], v[106:107], v[212:213]\n"
      "v_pk_mul_f32 v[108:109], v[108:109], v[216:217]\n"
      "v_pk_mul_f32 v[110:111], v[110:111], v[220:221]\n"
      "v_cvt_pk_bf16_f32 v104, 0, v104\n"
      "v_cvt_pk_bf16_f32 v105, 0, v105\n"
      "v_cvt_pk_bf16_f32 v106, 0, v106\n"
      "global_load_dwordx4 a[72:75], v34, s[8:9] offset:-2048 sc1\n"
      "v_cvt_pk_bf16_f32 v107, 0, v107\n"
      "v_cvt_pk_bf16_f32 v108, 0, v108\n"
      "v_cvt_pk_bf16_f32 v109, 0, v109\n"
      "v_cvt_pk_bf16_f32 v110, 0, v110\n"
      "v_cvt_pk_bf16_f32 v111, 0, v111\n"
      "v_pk_mul_f32 v[104:105], v[104:105], s[42:43] op_sel_hi:[1,0]\n"
      "v_pk_mul_f32 v[106:107], v[106:107], s[42:43] op_sel_hi:[1,0]\n"
      "v_pk_mul_f32 v[108:109], v[108:109], s[42:43] op_sel_hi:[1,0]\n"
      "v_pk_mul_f32 v[110:111], v[110:111], s[42:43] op_sel_hi:[1,0]\n"
      "global_load_dwordx4 a[76:79], v34, s[8:9] offset:-1024 sc1\n"
      "v_cvt_pk_bf16_f32 v104, 0, v104\n"
      "v_cvt_pk_bf16_f32 v105, 0, v105\n"
      "v_cvt_pk_bf16_f32 v106, 0, v106\n"
      "v_cvt_pk_bf16_f32 v107, 0, v107\n"
      "v_cvt_pk_bf16_f32 v108, 0, v108\n"
      "v_cvt_pk_bf16_f32 v109, 0, v109\n"
      "v_cvt_pk_bf16_f32 v110, 0, v110\n"
      "v_cvt_pk_bf16_f32 v111, 0, v111\n"
      "v_pk_add_f32 v[104:105], v[104:105], s[40:41] op_sel_hi:[1,0]\n"
      "global_load_dwordx4 a[80:83], v34, s[8:9] offset:0 sc1\n"
      "v_pk_add_f32 v[106:107], v[106:107], s[40:41] op_sel_hi:[1,0]\n"
      "v_pk_add_f32 v[108:109], v[108:109], s[40:41] op_sel_hi:[1,0]\n"
      "v_pk_add_f32 v[110:111], v[110:111], s[40:41] op_sel_hi:[1,0]\n"
      "v_cvt_pk_bf16_f32 v104, 0, v104\n"
      "v_cvt_pk_bf16_f32 v105, 0, v105\n"
      "v_cvt_pk_bf16_f32 v106, 0, v106\n"
      "v_cvt_pk_bf16_f32 v107, 0, v107\n"
      "v_cvt_pk_bf16_f32 v108, 0, v108\n"
      "v_cvt_pk_bf16_f32 v109, 0, v109\n"
      "global_load_dwordx4 a[84:87], v34, s[8:9] offset:1024 sc1\n"
      "v_cvt_pk_bf16_f32 v110, 0, v110\n"
      "v_cvt_pk_bf16_f32 v111, 0, v111\n"
      "v_and_b32 v120, 0xffff0000, v235\n"
      "v_lshlrev_b32 v121, 16, v235\n"
      "v_and_b32 v122, 0xffff0000, v234\n"
      "v_lshlrev_b32 v123, 16, v234\n"
      "v_and_b32 v124, 0xffff0000, v233\n"
      "v_lshlrev_b32 v125, 16, v233\n"
      "v_and_b32 v126, 0xffff0000, v232\n"
      "global_load_dwordx4 a[88:91], v34, s[8:9] offset:2048 sc1\n"
      "v_lshlrev_b32 v127, 16, v232\n"
      "v_pk_mul_f32 v[120:121], v[120:121], s[14:15] op_sel_hi:[1,0]\n"
      "v_pk_mul_f32 v[122:123], v[122:123], s[14:15] op_sel_hi:[1,0]\n"
      "v_pk_mul_f32 v[124:125], v[124:125], s[14:15] op_sel_hi:[1,0]\n"
      "v_pk_mul_f32 v[126:127], v[126:127], s[14:15] op_sel_hi:[1,0]\n"
      "v_cvt_pk_bf16_f32 v120, 0, v120\n"
      "v_cvt_pk_bf16_f32 v121, 0, v121\n"
      "v_cvt_pk_bf16_f32 v122, 0, v122\n"
      "v_cvt_pk_bf16_f32 v123, 0, v123\n"
      "global_load_dwordx4 a[92:95], v34, s[8:9] offset:3072 sc1\n"
      "v_cvt_pk_bf16_f32 v124, 0, v124\n"
      "v_cvt_pk_bf16_f32 v125, 0, v125\n"
      "v_cvt_pk_bf16_f32 v126, 0, v126\n"
      "v_cvt_pk_bf16_f32 v127, 0, v127\n"
      "s_add_u32 s43, s89, 32768\n"
      "s_add_u32 s53, s89, 0x1000000\n"
      "s_add_u32 s54, s43, 0x1000000\n"
      "s_add_u32 s55, s79, 176128\n"
      "s_add_u32 s45, s81, 65536\n"
      "v_add_u32 v34, 28672, v7\n"
      "global_load_dwordx4 a[96:99], v34, s[8:9] offset:-4096 sc1\n"
      "s_waitcnt lgkmcnt(0)\n"
      "v_pk_add_f32 v[64:65], v[64:65], v[72:73]\n"
      "v_pk_add_f32 v[66:67], v[66:67], v[74:75]\n"
      "v_pk_add_f32 v[68:69], v[68:69], v[76:77]\n"
      "v_pk_add_f32 v[70:71], v[70:71], v[78:79]\n"
      "v_cvt_pk_bf16_f32 v64, 0, v64\n"
      "v_cvt_pk_bf16_f32 v65, 0, v65\n"
      "v_cvt_pk_bf16_f32 v66, 0, v66\n"
      "v_cvt_pk_bf16_f32 v67, 0, v67\n"
      "global_load_dwordx4 a[100:103], v34, s[8:9] offset:-3072 sc1\n"
      "v_cvt_pk_bf16_f32 v68, 0, v68\n"
      "v_cvt_pk_bf16_f32 v69, 0, v69\n"
      "v_cvt_pk_bf16_f32 v70, 0, v70\n"
      "v_cvt_pk_bf16_f32 v71, 0, v71\n"
      "v_pk_add_f32 v[120:121], v[120:121], v[64:65]\n"
      "v_pk_add_f32 v[122:123], v[122:123], v[66:67]\n"
      "v_pk_add_f32 v[124:125], v[124:125], v[68:69]\n"
      "v_pk_add_f32 v[126:127], v[126:127], v[70:71]\n"
      "v_cvt_pk_bf16_f32 v120, 0, v120\n"
      "global_load_dwordx4 a[104:107], v34, s[8:9] offset:-2048 sc1\n"
      "v_cvt_pk_bf16_f32 v121, 0, v121\n"
      "v_cvt_pk_bf16_f32 v122, 0, v122\n"
      "v_cvt_pk_bf16_f32 v123, 0, v123\n"
      "v_cvt_pk_bf16_f32 v124, 0, v124\n"
      "v_cvt_pk_bf16_f32 v125, 0, v125\n"
      "v_cvt_pk_bf16_f32 v126, 0, v126\n"
      "v_cvt_pk_bf16_f32 v127, 0, v127\n"
      "v_pk_mul_f32 v[104:105], v[104:105], v[120:121]\n"
      "v_pk_mul_f32 v[106:107], v[106:107], v[122:123]\n"
      "global_load_dwordx4 a[108:111], v34, s[8:9] offset:-1024 sc1\n"
      "v_pk_mul_f32 v[108:109], v[108:109], v[124:125]\n"
      "v_pk_mul_f32 v[110:111], v[110:111], v[126:127]\n"
      "v_cvt_pk_bf16_f32 v104, 0, v104\n"
      "v_cvt_pk_bf16_f32 v105, 0, v105\n"
      "v_cvt_pk_bf16_f32 v106, 0, v106\n"
      "v_cvt_pk_bf16_f32 v107, 0, v107\n"
      "v_cvt_pk_bf16_f32 v108, 0, v108\n"
      "v_cvt_pk_bf16_f32 v109, 0, v109\n"
      "v_cvt_pk_bf16_f32 v110, 0, v110\n"
      "global_load_dwordx4 a[112:115], v34, s[8:9] offset:0 sc1\n"
      "v_cvt_pk_bf16_f32 v111, 0, v111\n"
      "v_pk_mul_f32 v[104:105], v[104:105], s[58:59] op_sel_hi:[1,0]\n"
      "v_pk_mul_f32 v[106:107], v[106:107], s[58:59] op_sel_hi:[1,0]\n"
      "v_pk_mul_f32 v[108:109], v[108:109], s[58:59] op_sel_hi:[1,0]\n"
      "v_pk_mul_f32 v[110:111], v[110:111], s[58:59] op_sel_hi:[1,0]\n"
      "v_cvt_pk_bf16_f32 v104, 0, v104\n"
      "v_cvt_pk_bf16_f32 v105, 0, v105\n"
      "v_cvt_pk_bf16_f32 v106, 0, v106\n"
      "v_cvt_pk_bf16_f32 v107, 0, v107\n"
      "global_load_dwordx4 a[116:119], v34, s[8:9] offset:1024 sc1\n"
      "v_cvt_pk_bf16_f32 v108, 0, v108\n"
      "v_cvt_pk_bf16_f32 v109, 0, v109\n"
      "v_cvt_pk_bf16_f32 v110, 0, v110\n"
      "v_cvt_pk_bf16_f32 v111, 0, v111\n"
      "v_pk_add_f32 v[104:105], v[104:105], v[112:113]\n"
      "v_pk_add_f32 v[106:107], v[106:107], v[114:115]\n"
      "v_pk_add_f32 v[108:109], v[108:109], v[116:117]\n"
      "v_pk_add_f32 v[110:111], v[110:111], v[118:119]\n"
      "v_cvt_pk_bf16_f32 v104, 0, v104\n"
      "global_load_dwordx4 a[120:123], v34, s[8:9] offset:2048 sc1\n"
      "v_cvt_pk_bf16_f32 v105, 0, v105\n"
      "v_cvt_pk_bf16_f32 v106, 0, v106\n"
      "v_cvt_pk_bf16_f32 v107, 0, v107\n"
      "v_cvt_pk_bf16_f32 v108, 0, v108\n"
      "v_cvt_pk_bf16_f32 v109, 0, v109\n"
      "v_cvt_pk_bf16_f32 v110, 0, v110\n"
      "v_cvt_pk_bf16_f32 v111, 0, v111\n"
      "v_pk_mul_f32 v[120:121], v[104:105], s[40:41] op_sel:[0,1] op_sel_hi:[1,1]\n"
      "v_pk_mul_f32 v[122:123], v[106:107], s[40:41] op_sel:[0,1] op_sel_hi:[1,1]\n"
      "v_pk_mul_f32 v[124:125], v[108:109], s[40:41] op_sel:[0,1] op_sel_hi:[1,1]\n"
      "v_pk_mul_f32 v[126:127], v[110:111], s[40:41] op_sel:[0,1] op_sel_hi:[1,1]\n"
      "v_cvt_pk_bf16_f32 v128, v120, v121\n"
      "v_cvt_pk_bf16_f32 v129, v122, v123\n"
      "v_cvt_pk_bf16_f32 v130, v124, v125\n"
      "v_cvt_pk_bf16_f32 v131, v126, v127\n"
      "buffer_store_dwordx4 v[128:131], v29, s[32:35], s89 offen sc1\n"
      "v_cvt_pk_bf16_f32 v132, v104, v105\n"
      "v_cvt_pk_bf16_f32 v133, v106, v107\n"
      "v_cvt_pk_bf16_f32 v134, v108, v109\n"
      "v_cvt_pk_bf16_f32 v135, v110, v111\n"
      "v_add3_u32 v35, v128, v129, v130\n"
      "v_add_u32 v35, v35, v131\n"
      "v_add3_u32 v38, v132, v133, v134\n"
      "v_add_u32 v38, v38, v135\n"
      "v_lshlrev_b32 v112, 16, v196\n"
      "v_add_u32_dpp v35, v35, v35 quad_perm:[1,0,3,2] row_mask:0xf bank_mask:0xf\n"
      "v_and_b32 v113, 0xffff0000, v196\n"
      "v_add_u32_dpp v38, v38, v38 quad_perm:[1,0,3,2] row_mask:0xf bank_mask:0xf\n"
      "v_lshlrev_b32 v114, 16, v197\n"
      "v_add_u32_dpp v35, v35, v35 quad_perm:[2,3,0,1] row_mask:0xf bank_mask:0xf\n"
      "v_and_b32 v115, 0xffff0000, v197\n"
      "v_add_u32_dpp v38, v38, v38 quad_perm:[2,3,0,1] row_mask:0xf bank_mask:0xf\n"
      "v_lshlrev_b32 v116, 16, v198\n"
      "v_add_u32_dpp v35, v35, v35 row_half_mirror row_mask:0xf bank_mask:0xf\n"
      "v_and_b32 v117, 0xffff0000, v198\n"
      "v_add_u32_dpp v38, v38, v38 row_ror:8 row_mask:0xf bank_mask:0xf\n"
      "v_lshlrev_b32 v118, 16, v199\n"
      "v_cndmask_b32 v35, v35, v38, vcc\n"
      "v_and_b32 v119, 0xffff0000, v199\n"
      "v_sub_u32 v35, s30, v35\n"
      "v_pk_add_f32 v[104:105], v[112:113], v[96:97] neg_lo:[0,1] neg_hi:[0,1]\n"
      "buffer_store_dword v35, v30, s[32:35], s81 offen sc1\n"
      "s_waitcnt vmcnt(23)\n"
      "buffer_store_dwordx2 v[52:53], v48, s[32:35], s88 offen sc1\n"
      "v_pk_add_f32 v[106:107], v[114:115], v[98:99] neg_lo:[0,1] neg_hi:[0,1]\n"
      "v_pk_add_f32 v[108:109], v[116:117], v[100:101] neg_lo:[0,1] neg_hi:[0,1]\n"
      "v_pk_add_f32 v[110:111], v[118:119], v[102:103] neg_lo:[0,1] neg_hi:[0,1]\n"
      "v_cvt_pk_bf16_f32 v104, 0, v104\n"
      "v_cvt_pk_bf16_f32 v105, 0, v105\n"
      "v_cvt_pk_bf16_f32 v106, 0, v106\n"
      "v_cvt_pk_bf16_f32 v107, 0, v107\n"
      "v_cvt_pk_bf16_f32 v108, 0, v108\n"
      "v_cvt_pk_bf16_f32 v109, 0, v109\n"
      "v_cvt_pk_bf16_f32 v110, 0, v110\n"
      "v_cvt_pk_bf16_f32 v111, 0, v111\n"
      "buffer_store_dwordx2 v[52:53], v48, s[32:35], s55 offen sc1\n"
      "v_pk_mul_f32 v[104:105], v[104:105], v[208:209]\n"
      "v_pk_mul_f32 v[106:107], v[106:107], v[212:213]\n"
      "v_pk_mul_f32 v[108:109], v[108:109], v[216:217]\n"
      "v_pk_mul_f32 v[110:111], v[110:111], v[220:221]\n"
      "v_cvt_pk_bf16_f32 v104, 0, v104\n"
      "v_cvt_pk_bf16_f32 v105, 0, v105\n"
      "v_cvt_pk_bf16_f32 v106, 0, v106\n"
      "v_cvt_pk_bf16_f32 v107, 0, v107\n"
      "v_cvt_pk_bf16_f32 v108, 0, v108\n"
      "v_cvt_pk_bf16_f32 v109, 0, v109\n"
      "v_cvt_pk_bf16_f32 v110, 0, v110\n"
      "global_load_dwordx4 a[124:127], v34, s[8:9] offset:3072 sc1\n"
      "v_cvt_pk_bf16_f32 v111, 0, v111\n"
      "v_pk_mul_f32 v[104:105], v[104:105], s[50:51] op_sel_hi:[1,0]\n"
      "v_pk_mul_f32 v[106:107], v[106:107], s[50:51] op_sel_hi:[1,0]\n"
      "v_pk_mul_f32 v[108:109], v[108:109], s[50:51] op_sel_hi:[1,0]\n"
      "v_pk_mul_f32 v[110:111], v[110:111], s[50:51] op_sel_hi:[1,0]\n"
      "v_cvt_pk_bf16_f32 v104, 0, v104\n"
      "v_cvt_pk_bf16_f32 v105, 0, v105\n"
      "v_cvt_pk_bf16_f32 v106, 0, v106\n"
      "v_cvt_pk_bf16_f32 v107, 0, v107\n"
      "v_cvt_pk_bf16_f32 v108, 0, v108\n"
      "v_cvt_pk_bf16_f32 v109, 0, v109\n"
      "s_add_u32 m0, s73, 0\n"
      "v_add_u32 v43, 0, v40\n"
      "global_load_lds_dwordx4 v43, s[4:5] offset:0 sc1\n"
      "v_cvt_pk_bf16_f32 v110, 0, v110\n"
      "v_cvt_pk_bf16_f32 v111, 0, v111\n"
      "v_pk_add_f32 v[104:105], v[104:105], s[48:49] op_sel_hi:[1,0]\n"
      "v_pk_add_f32 v[106:107], v[106:107], s[48:49] op_sel_hi:[1,0]\n"
      "v_pk_add_f32 v[108:109], v[108:109], s[48:49] op_sel_hi:[1,0]\n"
      "v_pk_add_f32 v[110:111], v[110:111], s[48:49] op_sel_hi:[1,0]\n"
      "v_cvt_pk_bf16_f32 v104, 0, v104\n"
      "v_cvt_pk_bf16_f32 v105, 0, v105\n"
      "v_cvt_pk_bf16_f32 v106, 0, v106\n"
      "v_cvt_pk_bf16_f32 v107, 0, v107\n"
      "v_cvt_pk_bf16_f32 v108, 0, v108\n"
      "global_load_lds_dwordx4 v43, s[4:5] offset:1024 sc1\n"
      "v_cvt_pk_bf16_f32 v109, 0, v109\n"
      "v_cvt_pk_bf16_f32 v110, 0, v110\n"
      "v_cvt_pk_bf16_f32 v111, 0, v111\n"
      "v_and_b32 v120, 0xffff0000, v239\n"
      "v_lshlrev_b32 v121, 16, v239\n"
      "v_and_b32 v122, 0xffff0000, v238\n"
      "v_lshlrev_b32 v123, 16, v238\n"
      "v_and_b32 v124, 0xffff0000, v237\n"
      "v_lshlrev_b32 v125, 16, v237\n"
      "v_and_b32 v126, 0xffff0000, v236\n"
      "v_lshlrev_b32 v127, 16, v236\n"
      "global_load_lds_dwordx4 v43, s[4:5] offset:2048 sc1\n"
      "v_pk_mul_f32 v[120:121], v[120:121], s[14:15] op_sel:[0,1] op_sel_hi:[1,1]\n"
      "v_pk_mul_f32 v[122:123], v[122:123], s[14:15] op_sel:[0,1] op_sel_hi:[1,1]\n"
      "v_pk_mul_f32 v[124:125], v[124:125], s[14:15] op_sel:[0,1] op_sel_hi:[1,1]\n"
      "v_pk_mul_f32 v[126:127], v[126:127], s[14:15] op_sel:[0,1] op_sel_hi:[1,1]\n"
      "v_cvt_pk_bf16_f32 v120, 0, v120\n"
      "v_cvt_pk_bf16_f32 v121, 0, v121\n"
      "v_cvt_pk_bf16_f32 v122, 0, v122\n"
      "v_cvt_pk_bf16_f32 v123, 0, v123\n"
      "v_cvt_pk_bf16_f32 v124, 0, v124\n"
      "v_cvt_pk_bf16_f32 v125, 0, v125\n"
      "v_cvt_pk_bf16_f32 v126, 0, v126\n"
      "global_load_lds_dwordx4 v43, s[4:5] offset:3072 sc1\n"
      "v_cvt_pk_bf16_f32 v127, 0, v127\n"
      "v_pk_add_f32 v[80:81], v[80:81], v[88:89]\n"
      "v_pk_add_f32 v[82:83], v[82:83], v[90:91]\n"
      "v_pk_add_f32 v[84:85], v[84:85], v[92:93]\n"
      "v_pk_add_f32 v[86:87], v[86:87], v[94:95]\n"
      "v_cvt_pk_bf16_f32 v80, 0, v80\n"
      "v_cvt_pk_bf16_f32 v81, 0, v81\n"
      "v_cvt_pk_bf16_f32 v82, 0, v82\n"
      "v_cvt_pk_bf16_f32 v83, 0, v83\n"
      "v_cvt_pk_bf16_f32 v84, 0, v84\n"
      "v_cvt_pk_bf16_f32 v85, 0, v85\n"
      "s_mov_b32 s47, 0\n"
      "buffer_load_dwordx2 v[2:3], v5, s[32:35], s47 offen sc1\n"
      "v_cvt_pk_bf16_f32 v86, 0, v86\n"
      "v_cvt_pk_bf16_f32 v87, 0, v87\n"
      "v_pk_add_f32 v[120:121], v[120:121], v[80:81]\n"
      "v_pk_add_f32 v[122:123], v[122:123], v[82:83]\n"
      "v_pk_add_f32 v[124:125], v[124:125], v[84:85]\n"
      "v_pk_add_f32 v[126:127], v[126:127], v[86:87]\n"
      "v_cvt_pk_bf16_f32 v120, 0, v120\n"
      "v_cvt_pk_bf16_f32 v121, 0, v121\n"
      "v_cvt_pk_bf16_f32 v122, 0, v122\n"
      "v_cvt_pk_bf16_f32 v123, 0, v123\n"
      "v_cvt_pk_bf16_f32 v124, 0, v124\n"
      "s_add_u32 m0, s73, 16384\n"
      "v_add_u32 v43, 81920, v40\n"
      "global_load_lds_dwordx4 v43, s[4:5] offset:0 sc1\n"
      "v_cvt_pk_bf16_f32 v125, 0, v125\n"
      "v_cvt_pk_bf16_f32 v126, 0, v126\n"
      "v_cvt_pk_bf16_f32 v127, 0, v127\n"
      "v_pk_mul_f32 v[104:105], v[104:105], v[120:121]\n"
      "v_pk_mul_f32 v[106:107], v[106:107], v[122:123]\n"
      "v_pk_mul_f32 v[108:109], v[108:109], v[124:125]\n"
      "v_pk_mul_f32 v[110:111], v[110:111], v[126:127]\n"
      "v_cvt_pk_bf16_f32 v104, 0, v104\n"
      "v_cvt_pk_bf16_f32 v105, 0, v105\n"
      "v_cvt_pk_bf16_f32 v106, 0, v106\n"
      "v_cvt_pk_bf16_f32 v107, 0, v107\n"
      "global_load_lds_dwordx4 v43, s[4:5] offset:1024 sc1\n"
      "v_cvt_pk_bf16_f32 v108, 0, v108\n"
      "v_cvt_pk_bf16_f32 v109, 0, v109\n"
      "v_cvt_pk_bf16_f32 v110, 0, v110\n"
      "v_cvt_pk_bf16_f32 v111, 0, v111\n"
      "v_pk_mul_f32 v[104:105], v[104:105], s[52:53] op_sel_hi:[1,0]\n"
      "v_pk_mul_f32 v[106:107], v[106:107], s[52:53] op_sel_hi:[1,0]\n"
      "v_pk_mul_f32 v[108:109], v[108:109], s[52:53] op_sel_hi:[1,0]\n"
      "v_pk_mul_f32 v[110:111], v[110:111], s[52:53] op_sel_hi:[1,0]\n"
      "v_cvt_pk_bf16_f32 v104, 0, v104\n"
      "v_cvt_pk_bf16_f32 v105, 0, v105\n"
      "v_cvt_pk_bf16_f32 v106, 0, v106\n"
      "global_load_lds_dwordx4 v43, s[4:5] offset:2048 sc1\n"
      "v_cvt_pk_bf16_f32 v107, 0, v107\n"
      "v_cvt_pk_bf16_f32 v108, 0, v108\n"
      "v_cvt_pk_bf16_f32 v109, 0, v109\n"
      "v_cvt_pk_bf16_f32 v110, 0, v110\n"
      "v_cvt_pk_bf16_f32 v111, 0, v111\n"
      "v_pk_add_f32 v[104:105], v[104:105], v[112:113]\n"
      "v_pk_add_f32 v[106:107], v[106:107], v[114:115]\n"
      "v_pk_add_f32 v[108:109], v[108:109], v[116:117]\n"
      "v_pk_add_f32 v[110:111], v[110:111], v[118:119]\n"
      "v_cvt_pk_bf16_f32 v104, 0, v104\n"
      "v_cvt_pk_bf16_f32 v105, 0, v105\n"
      "global_load_lds_dwordx4 v43, s[4:5] offset:3072 sc1\n"
      "v_cvt_pk_bf16_f32 v106, 0, v106\n"
      "v_cvt_pk_bf16_f32 v107, 0, v107\n"
      "v_cvt_pk_bf16_f32 v108, 0, v108\n"
      "v_cvt_pk_bf16_f32 v109, 0, v109\n"
      "v_cvt_pk_bf16_f32 v110, 0, v110\n"
      "v_cvt_pk_bf16_f32 v111, 0, v111\n"
      "v_pk_mul_f32 v[120:121], v[104:105], s[48:49] op_sel:[0,1] op_sel_hi:[1,1]\n"
      "v_pk_mul_f32 v[122:123], v[106:107], s[48:49] op_sel:[0,1] op_sel_hi:[1,1]\n"
      "v_pk_mul_f32 v[124:125], v[108:109], s[48:49] op_sel:[0,1] op_sel_hi:[1,1]\n"
      "v_pk_mul_f32 v[126:127], v[110:111], s[48:49] op_sel:[0,1] op_sel_hi:[1,1]\n"
      "v_cvt_pk_bf16_f32 v136, v120, v121\n"
      "v_cvt_pk_bf16_f32 v137, v122, v123\n"
      "v_cvt_pk_bf16_f32 v138, v124, v125\n"
      "v_cvt_pk_bf16_f32 v139, v126, v127\n"
      "buffer_store_dwordx4 v[136:139], v29, s[32:35], s43 offen sc1\n"
      "v_cvt_pk_bf16_f32 v140, v104, v105\n"
      "v_cvt_pk_bf16_f32 v141, v106, v107\n"
      "v_cvt_pk_bf16_f32 v142, v108, v109\n"
      "v_cvt_pk_bf16_f32 v143, v110, v111\n"
      "v_add3_u32 v35, v136, v137, v138\n"
      "v_add_u32 v35, v35, v139\n"
      "v_add3_u32 v38, v140, v141, v142\n"
      "v_add_u32 v38, v38, v143\n"
      "v_add_u32_dpp v35, v35, v35 quad_perm:[1,0,3,2] row_mask:0xf bank_mask:0xf\n"
      "s_nop 0\n"
      "v_add_u32_dpp v38, v38, v38 quad_perm:[1,0,3,2] row_mask:0xf bank_mask:0xf\n"
      "v_add_u32_dpp v35, v35, v35 quad_perm:[2,3,0,1] row_mask:0xf bank_mask:0xf\n"
      "s_nop 0\n"
      "v_add_u32_dpp v38, v38, v38 quad_perm:[2,3,0,1] row_mask:0xf bank_mask:0xf\n"
      "v_add_u32_dpp v35, v35, v35 row_half_mirror row_mask:0xf bank_mask:0xf\n"
      "s_nop 0\n"
      "v_add_u32_dpp v38, v38, v38 row_ror:8 row_mask:0xf bank_mask:0xf\n"
      "v_cndmask_b32 v35, v35, v38, vcc\n"
      "v_sub_u32 v35, s30, v35\n"
      "buffer_store_dword v35, v30, s[32:35], s45 offen sc1\n"
      "v_add_u32 v34, 36864, v7\n"
      "global_load_dwordx4 a[128:131], v34, s[8:9] offset:-4096 sc1\n"
      "global_load_dwordx4 a[132:135], v34, s[8:9] offset:-3072 sc1\n"
      "global_load_dwordx4 a[136:139], v34, s[8:9] offset:-2048 sc1\n"
      "global_load_dwordx4 a[140:143], v34, s[8:9] offset:-1024 sc1\n"
      "global_load_dwordx4 a[144:147], v34, s[8:9] offset:0 sc1\n"
      "global_load_dwordx4 a[148:151], v34, s[8:9] offset:1024 sc1\n"
      "global_load_dwordx4 a[152:155], v34, s[8:9] offset:2048 sc1\n"
      "global_load_dwordx4 a[156:159], v34, s[8:9] offset:3072 sc1\n"
      "buffer_store_dwordx4 v[132:135], v36, s[32:35], s53 offen sc1\n"
      "buffer_store_dwordx4 v[140:143], v36, s[32:35], s54 offen sc1\n"
      "v_xor_b32 v33, 32, v6\n"
      "v_xor_b32 v34, 64, v6\n"
      "v_xor_b32 v35, 96, v6\n"
      "v_add_u32 v45, 45056, v7\n"
      "s_waitcnt vmcnt(16)\n"
      "ds_read_b128 v[248:251], v40 offset:0\n"
      "ds_read_b128 v[252:255], v40 offset:1024\n"
      "ds_read_b128 v[36:39], v40 offset:2048\n"
      "ds_read_b128 v[60:63], v40 offset:3072\n"
      "s_waitcnt lgkmcnt(3)\n"
      "v_add3_u32 v2, v2, v248, v249\n"
      "v_add3_u32 v2, v2, v250, v251\n"
      "s_waitcnt lgkmcnt(2)\n"
      "v_add3_u32 v2, v2, v252, v253\n"
      "v_add3_u32 v2, v2, v254, v255\n"
      "s_waitcnt lgkmcnt(1)\n"
      "v_add3_u32 v2, v2, v36, v37\n"
      "v_add3_u32 v2, v2, v38, v39\n"
      "s_waitcnt lgkmcnt(0)\n"
      "v_add3_u32 v2, v2, v60, v61\n"
      "v_add3_u32 v2, v2, v62, v63\n"
      "s_nop 1\n"
      "v_add_u32_dpp v2, v2, v2 quad_perm:[1,0,3,2] row_mask:0xf bank_mask:0xf\n"
      "s_nop 1\n"
      "v_add_u32_dpp v2, v2, v2 quad_perm:[2,3,0,1] row_mask:0xf bank_mask:0xf\n"
      "s_nop 1\n"
      "v_add_u32_dpp v2, v2, v2 row_half_mirror row_mask:0xf bank_mask:0xf\n"
      "v_cmp_ne_u32 vcc, s71, v2\n"
      "s_cbranch_vccnz L_sx0_%=\n"
      "L_bx0_%=:\n"
      "s_barrier\n"
      "ds_read_b64_tr_b16 v[64:65], v6 offset:0\n"
      "ds_read_b64_tr_b16 v[66:67], v6 offset:2048\n"
      "ds_read_b64_tr_b16 v[80:81], v6 offset:4096\n"
      "ds_read_b64_tr_b16 v[82:83], v6 offset:6144\n"
      "ds_read_b64_tr_b16 v[68:69], v33 offset:0\n"
      "ds_read_b64_tr_b16 v[70:71], v33 offset:2048\n"
      "ds_read_b64_tr_b16 v[84:85], v33 offset:4096\n"
      "ds_read_b64_tr_b16 v[86:87], v33 offset:6144\n"
      "ds_read_b64_tr_b16 v[72:73], v34 offset:0\n"
      "ds_read_b64_tr_b16 v[74:75], v34 offset:2048\n"
      "ds_read_b64_tr_b16 v[88:89], v34 offset:4096\n"
      "ds_read_b64_tr_b16 v[90:91], v34 offset:6144\n"
      "ds_read_b64_tr_b16 v[76:77], v35 offset:0\n"
      "ds_read_b64_tr_b16 v[78:79], v35 offset:2048\n"
      "ds_read_b64_tr_b16 v[92:93], v35 offset:4096\n"
      "ds_read_b64_tr_b16 v[94:95], v35 offset:6144\n"
      "s_waitcnt lgkmcnt(0)\n"
      "v_mfma_f32_16x16x32_bf16 v[128:131], a[0:3], v[64:67], 0\n"
      "global_load_dwordx4 a[160:163], v45, s[8:9] offset:-4096 sc1\n"
      "s_add_u32 m0, s73, 32768\n"
      "v_add_u32 v43, 16384, v40\n"
      "v_mfma_f32_16x16x32_bf16 v[128:131], a[12:15], v[80:83], v[128:131]\n"
      "ds_read_b64_tr_b16 v[96:97], v6 offset:8192\n"
      "v_lshrrev_b32 v42, 8, v7\n"
      "v_mfma_f32_16x16x32_bf16 v[144:147], a[4:7], v[64:67], 0\n"
      "global_load_lds_dwordx4 v43, s[4:5] offset:0 sc1\n"
      "s_waitcnt vmcnt(14)\n"
      "v_mfma_f32_16x16x32_bf16 v[144:147], a[16:19], v[80:83], v[144:147]\n"
      "ds_read_b128 v[248:251], v40 offset:16384\n"
      "ds_read_b64_tr_b16 v[98:99], v6 offset:10240\n"
      "v_bfe_u32 v41, v7, 4, 4\n"
      "v_mfma_f32_16x16x32_bf16 v[160:163], a[8:11], v[64:67], 0\n"
      "ds_read_b128 v[252:255], v40 offset:17408\n"
      "ds_read_b64_tr_b16 v[100:101], v33 offset:8192\n"
      "v_min_u32 v42, 2, v42\n"
      "v_mfma_f32_16x16x32_bf16 v[160:163], a[20:23], v[80:83], v[160:163]\n"
      "ds_read_b128 v[36:39], v40 offset:18432\n"
      "ds_read_b64_tr_b16 v[102:103], v33 offset:10240\n"
      "v_min_u32 v41, 11, v41\n"
      "v_mfma_f32_16x16x32_bf16 v[132:135], a[0:3], v[68:71], 0\n"
      "global_load_lds_dwordx4 v43, s[4:5] offset:1024 sc1\n"
      "ds_read_b128 v[60:63], v40 offset:19456\n"
      "ds_read_b64_tr_b16 v[104:105], v34 offset:8192\n"
      "v_mad_u32_u24 v42, v42, 48, v41\n"
      "v_mfma_f32_16x16x32_bf16 v[132:135], a[12:15], v[84:87], v[132:135]\n"
      "ds_read_b64_tr_b16 v[106:107], v34 offset:10240\n"
      "v_lshl_add_u32 v42, v42, 6, s64\n"
      "v_mfma_f32_16x16x32_bf16 v[148:151], a[4:7], v[68:71], 0\n"
      "ds_read_b64_tr_b16 v[108:109], v35 offset:8192\n"
      "s_add_u32 s75, s76, 176128\n"
      "v_mfma_f32_16x16x32_bf16 v[148:151], a[16:19], v[84:87], v[148:151]\n"
      "ds_read_b64_tr_b16 v[110:111], v35 offset:10240\n"
      "v_mfma_f32_16x16x32_bf16 v[164:167], a[8:11], v[68:71], 0\n"
      "global_load_lds_dwordx4 v43, s[4:5] offset:2048 sc1\n"
      "s_waitcnt lgkmcnt(4)\n"
      "v_add3_u32 v3, v3, v248, v249\n"
      "v_add3_u32 v3, v3, v250, v251\n"
      "v_mfma_f32_16x16x32_bf16 v[164:167], a[20:23], v[84:87], v[164:167]\n"
      "ds_read_b64_tr_b16 v[112:113], v6 offset:12288\n"
      "v_add3_u32 v3, v3, v252, v253\n"
      "v_add3_u32 v3, v3, v254, v255\n"
      "v_mfma_f32_16x16x32_bf16 v[136:139], a[0:3], v[72:75], 0\n"
      "ds_read_b64_tr_b16 v[114:115], v6 offset:14336\n"
      "v_add3_u32 v3, v3, v36, v37\n"
      "v_add3_u32 v3, v3, v38, v39\n"
      "v_mfma_f32_16x16x32_bf16 v[136:139], a[12:15], v[88:91], v[136:139]\n"
      "ds_read_b64_tr_b16 v[116:117], v33 offset:12288\n"
      "v_add3_u32 v3, v3, v60, v61\n"
      "v_mfma_f32_16x16x32_bf16 v[152:155], a[4:7], v[72:75], 0\n"
      "global_load_lds_dwordx4 v43, s[4:5] offset:3072 sc1\n"
      "ds_read_b64_tr_b16 v[118:119], v33 offset:14336\n"
      "v_add3_u32 v3, v3, v62, v63\n"
      "L_pg3_%=:\n"
      "s_cbranch_execz L_pg7_%=\n"
      "v_mfma_f32_16x16x32_bf16 v[152:155], a[16:19], v[88:91], v[152:155]\n"
      "ds_read_b64_tr_b16 v[120:121], v34 offset:12288\n"
      "s_mov_b32 s47, 32768\n"
      "v_mfma_f32_16x16x32_bf16 v[168:171], a[8:11], v[72:75], 0\n"
      "buffer_load_dwordx2 v[48:49], v5, s[32:35], s47 offen sc1\n"
      "ds_read_b64_tr_b16 v[122:123], v34 offset:14336\n"
      "v_add_u32_dpp v3, v3, v3 quad_perm:[1,0,3,2] row_mask:0xf bank_mask:0xf\n"
      "v_mfma_f32_16x16x32_bf16 v[168:171], a[20:23], v[88:91], v[168:171]\n"
      "ds_read_b64_tr_b16 v[124:125], v35 offset:12288\n"
      "v_mfma_f32_16x16x32_bf16 v[140:143], a[0:3], v[76:79], 0\n"
      "ds_read_b64_tr_b16 v[126:127], v35 offset:14336\n"
      "v_add_u32_dpp v3, v3, v3 quad_perm:[2,3,0,1] row_mask:0xf bank_mask:0xf\n"
      "v_mfma_f32_16x16x32_bf16 v[140:143], a[12:15], v[92:95], v[140:143]\n"
      "v_mfma_f32_16x16x32_bf16 v[156:159], a[4:7], v[76:79], 0\n"
      "v_add_u32_dpp v3, v3, v3 row_half_mirror row_mask:0xf bank_mask:0xf\n"
      "v_cmp_ne_u32 vcc, s71, v3\n"
      "v_mfma_f32_16x16x32_bf16 v[156:159], a[16:19], v[92:95], v[156:159]\n"
      "v_mfma_f32_16x16x32_bf16 v[172:175], a[8:11], v[76:79], 0\n"
      "s_cbranch_vccnz L_sx1_%=\n"
      "L_bx1_%=:\n"
      "v_mfma_f32_16x16x32_bf16 v[172:175], a[20:23], v[92:95], v[172:175]\n"
      "s_waitcnt lgkmcnt(0)\n"
      "s_barrier\n"
      "v_mfma_f32_16x16x32_bf16 v[128:131], a[24:27], v[96:99], v[128:131]\n"
      "ds_read_b64_tr_b16 v[64:65], v6 offset:16384\n"
      "v_and_b32 v41, 0x1f0, v7\n"
      "v_mfma_f32_16x16x32_bf16 v[128:131], a[36:39], v[112:115], v[128:131]\n"
      "ds_read_b64_tr_b16 v[66:67], v6 offset:18432\n"
      "v_min_u32 v41, 0x170, v41\n"
      "v_mfma_f32_16x16x32_bf16 v[144:147], a[28:31], v[96:99], v[144:147]\n"
      "ds_read_b64_tr_b16 v[68:69], v34 offset:16384\n"
      "s_add_u32 m0, s73, 49152\n"
      "v_add_u32 v43, 32768, v40\n"
      "v_mfma_f32_16x16x32_bf16 v[144:147], a[40:43], v[112:115], v[144:147]\n"
      "ds_read_b64_tr_b16 v[70:71], v34 offset:18432\n"
      "v_lshrrev_b32 v52, 9, v7\n"
      "v_mfma_f32_16x16x32_bf16 v[160:163], a[32:35], v[96:99], v[160:163]\n"
      "global_load_dwordx2 v[58:59], v42, s[26:27] sc1\n"
      "v_mul_u32_u24 v52, 0x3c000, v52\n"
      "v_mfma_f32_16x16x32_bf16 v[160:163], a[44:47], v[112:115], v[160:163]\n"
      "ds_read_b64_tr_b16 v[72:73], v33 offset:16384\n"
      "v_add3_u32 v53, v52, v41, s65\n"
      "v_mfma_f32_16x16x32_bf16 v[132:135], a[24:27], v[100:103], v[132:135]\n"
      "ds_read_b64_tr_b16 v[74:75], v33 offset:18432\n"
      "s_mul_i32 s96, s60, 384\n"
      "v_mfma_f32_16x16x32_bf16 v[132:135], a[36:39], v[116:119], v[132:135]\n"
      "ds_read_b64_tr_b16 v[76:77], v35 offset:16384\n"
      "s_add_u32 s96, s96, 160768\n"
      "v_mfma_f32_16x16x32_bf16 v[148:151], a[28:31], v[100:103], v[148:151]\n"
      "ds_read_b64_tr_b16 v[78:79], v35 offset:18432\n"
      "v_lshrrev_b32 v52, 4, v7\n"
      "v_mfma_f32_16x16x32_bf16 v[148:151], a[40:43], v[116:119], v[148:151]\n"
      "ds_read_b64_tr_b16 v[80:81], v6 offset:20480\n"
      "v_and_b32 v52, 0x30, v52\n"
      "v_mfma_f32_16x16x32_bf16 v[164:167], a[32:35], v[100:103], v[164:167]\n"
      "ds_read_b64_tr_b16 v[82:83], v6 offset:22528\n"
      "v_add_u32 v52, s96, v52\n"
      "v_mfma_f32_16x16x32_bf16 v[164:167], a[44:47], v[116:119], v[164:167]\n"
      "ds_read_b64_tr_b16 v[84:85], v34 offset:20480\n"
      "s_mul_i32 s40, s60, 9216\n"
      "v_mfma_f32_16x16x32_bf16 v[136:139], a[24:27], v[104:107], v[136:139]\n"
      "ds_read_b64_tr_b16 v[86:87], v34 offset:22528\n"
      "s_add_u32 s40, s40, 122880\n"
      "v_mfma_f32_16x16x32_bf16 v[136:139], a[36:39], v[120:123], v[136:139]\n"
      "ds_read_b64_tr_b16 v[88:89], v33 offset:20480\n"
      "s_lshl_b32 s41, s60, 13\n"
      "v_mfma_f32_16x16x32_bf16 v[152:155], a[28:31], v[104:107], v[152:155]\n"
      "global_load_lds_dwordx4 v43, s[4:5] offset:0 sc1\n"
      "s_add_u32 s41, s41, 65536\n"
      "v_mfma_f32_16x16x32_bf16 v[152:155], a[40:43], v[120:123], v[152:155]\n"
      "ds_read_b64_tr_b16 v[90:91], v33 offset:22528\n"
      "s_mov_b32 s48, 0xaaaaaaaa\n"
      "v_mfma_f32_16x16x32_bf16 v[168:171], a[32:35], v[104:107], v[168:171]\n"
      "ds_read_b64_tr_b16 v[92:93], v35 offset:20480\n"
      "s_mov_b32 s49, s48\n"
      "v_mfma_f32_16x16x32_bf16 v[168:171], a[44:47], v[120:123], v[168:171]\n"
      "ds_read_b64_tr_b16 v[94:95], v35 offset:22528\n"
      "s_mov_b32 s50, 0xcccccccc\n"
      "v_mfma_f32_16x16x32_bf16 v[140:143], a[24:27], v[108:111], v[140:143]\n"
      "buffer_load_dwordx2 v[46:47], v51, s[32:35], s75 offen sc1\n"
      "s_mov_b32 s51, s50\n"
      "v_mfma_f32_16x16x32_bf16 v[140:143], a[36:39], v[124:127], v[140:143]\n"
      "global_load_lds_dwordx4 v43, s[4:5] offset:1024 sc1\n"
      "s_lshl_b32 s59, s60, 31\n"
      "v_mfma_f32_16x16x32_bf16 v[156:159], a[28:31], v[108:111], v[156:159]\n"
      "s_lshl_b32 s54, s30, 1\n"
      "s_lshl_b32 s55, s60, 6\n"
      "v_mfma_f32_16x16x32_bf16 v[156:159], a[40:43], v[124:127], v[156:159]\n"
      "v_bfe_u32 v30, v7, 4, 3\n"
      "v_lshrrev_b32 v31, 8, v7\n"
      "v_mfma_f32_16x16x32_bf16 v[172:175], a[32:35], v[108:111], v[172:175]\n"
      "v_lshl_add_u32 v31, v30, 2, v31\n"
      "v_sub_u32 v30, 2, v30\n"
      "v_mfma_f32_16x16x32_bf16 v[172:175], a[44:47], v[124:127], v[172:175]\n"
      "global_load_lds_dwordx4 v43, s[4:5] offset:2048 sc1\n"
      "s_waitcnt lgkmcnt(0)\n"
      "v_mfma_f32_16x16x32_bf16 v[176:179], a[0:3], v[64:67], 0\n"
      "ds_read_b64_tr_b16 v[96:97], v6 offset:24576\n"
      "v_and_b32 v29, 64, v4\n"
      "v_mfma_f32_16x16x32_bf16 v[176:179], a[12:15], v[80:83], v[176:179]\n"
      "ds_read_b64_tr_b16 v[98:99], v6 offset:26624\n"
      "v_lshl_add_u32 v31, v31, 9, v29\n"
      "v_mfma_f32_16x16x32_bf16 v[184:187], a[4:7], v[64:67], 0\n"
      "ds_read_b64_tr_b16 v[100:101], v34 offset:24576\n"
      "s_waitcnt vmcnt(5)\n"
      "v_mfma_f32_16x16x32_bf16 v[184:187], a[16:19], v[80:83], v[184:187]\n"
      "global_load_lds_dwordx4 v43, s[4:5] offset:3072 sc1\n"
      "ds_read_b128 v[248:251], v40 offset:32768\n"
      "s_mov_b32 s47, 65536\n"
      "v_mfma_f32_16x16x32_bf16 v[192:195], a[8:11], v[64:67], 0\n"
      "ds_read_b128 v[252:255], v40 offset:33792\n"
      "v_and_b32 v30, 0x80000000, v30\n"
      "v_mfma_f32_16x16x32_bf16 v[192:195], a[20:23], v[80:83], v[192:195]\n"
      "buffer_load_dwordx2 v[54:55], v5, s[32:35], s47 offen sc1\n"
      "ds_read_b128 v[36:39], v40 offset:34816\n"
      "ds_read_b64_tr_b16 v[102:103], v34 offset:26624\n"
      "v_or_b32 v31, v31, v30\n"
      "v_mfma_f32_16x16x32_bf16 v[180:183], a[0:3], v[68:71], 0\n"
      "ds_read_b128 v[60:63], v40 offset:35840\n"
      "ds_read_b64_tr_b16 v[104:105], v33 offset:24576\n"
      "v_add_u32 v50, s94, v7\n"
      "v_mfma_f32_16x16x32_bf16 v[180:183], a[12:15], v[84:87], v[180:183]\n"
      "ds_read_b64_tr_b16 v[106:107], v33 offset:26624\n"
      "s_mul_i32 s72, s30, 3\n"
      "v_mfma_f32_16x16x32_bf16 v[188:191], a[4:7], v[68:71], 0\n"
      "ds_read_b64_tr_b16 v[108:109], v35 offset:24576\n"
      "s_lshl_b32 s42, s60, 5\n"
      "v_mfma_f32_16x16x32_bf16 v[188:191], a[16:19], v[84:87], v[188:191]\n"
      "ds_read_b64_tr_b16 v[110:111], v35 offset:26624\n"
      "v_xor_b32 v27, s42, v6\n"
      "v_mfma_f32_16x16x32_bf16 v[196:199], a[8:11], v[68:71], 0\n"
      "ds_read_b64_tr_b16 v[112:113], v6 offset:28672\n"
      "s_waitcnt lgkmcnt(5)\n"
      "v_add3_u32 v48, v48, v248, v249\n"
      "v_add3_u32 v48, v48, v250, v251\n"
      "v_mfma_f32_16x16x32_bf16 v[196:199], a[20:23], v[84:87], v[196:199]\n"
      "ds_read_b64_tr_b16 v[114:115], v6 offset:30720\n"
      "v_add3_u32 v48, v48, v252, v253\n"
      "v_add3_u32 v48, v48, v254, v255\n"
      "v_mfma_f32_16x16x32_bf16 v[200:203], a[0:3], v[72:75], 0\n"
      "ds_read_b64_tr_b16 v[116:117], v34 offset:28672\n"
      "v_add3_u32 v48, v48, v36, v37\n"
      "v_add3_u32 v48, v48, v38, v39\n"
      "v_mfma_f32_16x16x32_bf16 v[200:203], a[12:15], v[88:91], v[200:203]\n"
      "ds_read_b64_tr_b16 v[118:119], v34 offset:30720\n"
      "v_add3_u32 v48, v48, v60, v61\n"
      "v_mfma_f32_16x16x32_bf16 v[208:211], a[4:7], v[72:75], 0\n"
      "ds_read_b64_tr_b16 v[120:121], v33 offset:28672\n"
      "v_add3_u32 v48, v48, v62, v63\n"
      "v_mfma_f32_16x16x32_bf16 v[208:211], a[16:19], v[88:91], v[208:211]\n"
      "ds_read_b64_tr_b16 v[122:123], v33 offset:30720\n"
      "s_add_u32 m0, s73, 0\n"
      "v_add_u32 v43, 49152, v40\n"
      "v_mfma_f32_16x16x32_bf16 v[216:219], a[8:11], v[72:75], 0\n"
      "ds_read_b64_tr_b16 v[124:125], v35 offset:28672\n"
      "v_add_u32_dpp v48, v48, v48 quad_perm:[1,0,3,2] row_mask:0xf bank_mask:0xf\n"
      "v_mfma_f32_16x16x32_bf16 v[216:219], a[20:23], v[88:91], v[216:219]\n"
      "global_load_lds_dwordx4 v43, s[4:5] offset:0 sc1\n"
      "v_add_u32 v27, 110592, v27\n"
      "v_mfma_f32_16x16x32_bf16 v[204:207], a[0:3], v[76:79], 0\n"
      "ds_read_b64_tr_b16 v[126:127], v35 offset:30720\n"
      "v_add_u32_dpp v48, v48, v48 quad_perm:[2,3,0,1] row_mask:0xf bank_mask:0xf\n"
      "v_mfma_f32_16x16x32_bf16 v[204:207], a[12:15], v[92:95], v[204:207]\n"
      "v_mov_b32 v18, 0\n"
      "v_mov_b32 v19, 0\n"
      "v_mfma_f32_16x16x32_bf16 v[212:215], a[4:7], v[76:79], 0\n"
      "v_add_u32_dpp v48, v48, v48 row_half_mirror row_mask:0xf bank_mask:0xf\n"
      "v_cmp_ne_u32 vcc, s71, v48\n"
      "v_mfma_f32_16x16x32_bf16 v[212:215], a[16:19], v[92:95], v[212:215]\n"
      "global_load_lds_dwordx4 v43, s[4:5] offset:1024 sc1\n"
      "v_mov_b32 v28, 0x3f803f80\n"
      "v_mfma_f32_16x16x32_bf16 v[220:223], a[8:11], v[76:79], 0\n"
      "s_cbranch_vccnz L_sx2_%=\n"
      "L_bx2_%=:\n"
      "v_mov_b32 v29, 0x3f803f80\n"
      "v_mfma_f32_16x16x32_bf16 v[220:223], a[20:23], v[92:95], v[220:223]\n"
      "s_waitcnt lgkmcnt(0)\n"
      "s_barrier\n"
      "v_mfma_f32_16x16x32_bf16 v[176:179], a[24:27], v[96:99], v[176:179]\n"
      "ds_read_b64_tr_b16 v[64:65], v6 offset:32768\n"
      "v_add_u32 v44, 4096, v7\n"
      "v_mfma_f32_16x16x32_bf16 v[176:179], a[36:39], v[112:115], v[176:179]\n"
      "global_load_lds_dwordx4 v43, s[4:5] offset:2048 sc1\n"
      "v_mov_b32 v30, 0x3f803f80\n"
      "v_mfma_f32_16x16x32_bf16 v[184:187], a[28:31], v[96:99], v[184:187]\n"
      "ds_read_b64_tr_b16 v[66:67], v6 offset:34816\n"
      "s_waitcnt vmcnt(7)\n"
      "v_cmp_ne_u64 s[52:53], s[30:31], v[46:47]\n"
      "s_add_u32 s55, s55, 163328\n"
      "v_mfma_f32_16x16x32_bf16 v[184:187], a[40:43], v[112:115], v[184:187]\n"
      "ds_read_b64_tr_b16 v[68:69], v33 offset:32768\n"
      "v_cmp_ne_u64 vcc, s[30:31], v[58:59]\n"
      "v_mfma_f32_16x16x32_bf16 v[192:195], a[32:35], v[96:99], v[192:195]\n"
      "ds_read_b64_tr_b16 v[70:71], v33 offset:34816\n"
      "v_mfma_f32_16x16x32_bf16 v[192:195], a[44:47], v[112:115], v[192:195]\n"
      "global_load_lds_dwordx4 v43, s[4:5] offset:3072 sc1\n"
      "s_or_b64 vcc, vcc, s[52:53]\n"
      "s_mov_b32 s47, 98304\n"
      "v_mfma_f32_16x16x32_bf16 v[180:183], a[24:27], v[100:103], v[180:183]\n"
      "buffer_load_dwordx2 v[56:57], v5, s[32:35], s47 offen sc1\n"
      "ds_read_b64_tr_b16 v[72:73], v34 offset:32768\n"
      "s_cbranch_vccnz L_sfd_%=\n"
      "L_bfd_%=:\n"
      "s_mov_b32 m0, s94\n"
      "v_mfma_f32_16x16x32_bf16 v[180:183], a[36:39], v[116:119], v[180:183]\n"
      "buffer_load_dwordx4 v7, s[32:35], s67 offen offset:0 sc1 lds\n"
      "ds_read_b64_tr_b16 v[74:75], v34 offset:34816\n"
      "v_add_u32 v51, s96, v41\n"
      "v_mfma_f32_16x16x32_bf16 v[188:191], a[28:31], v[100:103], v[188:191]\n"
      "ds_read_b64_tr_b16 v[76:77], v35 offset:32768\n"
      "v_lshrrev_b32 v42, 2, v7\n"
      "v_mfma_f32_16x16x32_bf16 v[188:191], a[40:43], v[116:119], v[188:191]\n"
      "ds_read_b64_tr_b16 v[78:79], v35 offset:34816\n"
      "s_lshl_b32 s97, s60, 10\n"
      "v_mfma_f32_16x16x32_bf16 v[196:199], a[32:35], v[100:103], v[196:199]\n"
      "ds_read_b64_tr_b16 v[80:81], v6 offset:36864\n"
      "s_add_u32 s96, s97, 98304\n"
      "v_mfma_f32_16x16x32_bf16 v[196:199], a[44:47], v[116:119], v[196:199]\n"
      "buffer_load_dwordx4 v7, s[32:35], s67 offen offset:1024 sc1 lds\n"
      "v_add_u32 v47, s96, v7\n"
      "v_mfma_f32_16x16x32_bf16 v[200:203], a[24:27], v[104:107], v[200:203]\n"
      "ds_read_b64_tr_b16 v[82:83], v6 offset:38912\n"
      "v_bfe_u32 v46, v7, 8, 1\n"
      "v_mfma_f32_16x16x32_bf16 v[200:203], a[36:39], v[120:123], v[200:203]\n"
      "ds_read_b64_tr_b16 v[84:85], v33 offset:36864\n"
      "v_xor_b32 v46, s60, v46\n"
      "v_mfma_f32_16x16x32_bf16 v[208:211], a[28:31], v[104:107], v[208:211]\n"
      "ds_read_b64_tr_b16 v[86:87], v33 offset:38912\n"
      "v_lshl_add_u32 v46, v46, 2, v7\n"
      "v_mfma_f32_16x16x32_bf16 v[208:211], a[40:43], v[120:123], v[208:211]\n"
      "buffer_load_dwordx4 v7, s[32:35], s67 offen offset:2048 sc1 lds\n"
      "ds_read_b64_tr_b16 v[88:89], v34 offset:36864\n"
      "v_add_u32 v46, 98304, v46\n"
      "v_mfma_f32_16x16x32_bf16 v[216:219], a[32:35], v[104:107], v[216:219]\n"
      "ds_read_b64_tr_b16 v[90:91], v34 offset:38912\n"
      "v_and_b32 v41, 0x78, v4\n"
      "v_mfma_f32_16x16x32_bf16 v[216:219], a[44:47], v[120:123], v[216:219]\n"
      "ds_read_b64_tr_b16 v[92:93], v35 offset:36864\n"
      "v_add_u32 v41, 162304, v41\n"
      "v_mfma_f32_16x16x32_bf16 v[204:207], a[24:27], v[108:111], v[204:207]\n"
      "ds_read_b64_tr_b16 v[94:95], v35 offset:38912\n"
      "v_mfma_f32_16x16x32_bf16 v[204:207], a[36:39], v[124:127], v[204:207]\n"
      "buffer_load_dword v32, v31, s[32:35], s68 offen sc1\n"
      "v_mfma_f32_16x16x32_bf16 v[212:215], a[28:31], v[108:111], v[212:215]\n"
      "v_mfma_f32_16x16x32_bf16 v[212:215], a[40:43], v[124:127], v[212:215]\n"
      "v_mfma_f32_16x16x32_bf16 v[220:223], a[32:35], v[108:111], v[220:223]\n"
      "v_mfma_f32_16x16x32_bf16 v[220:223], a[44:47], v[124:127], v[220:223]\n"
      "s_waitcnt lgkmcnt(0)\n"
      "v_mfma_f32_16x16x32_bf16 v[128:131], a[48:51], v[64:67], v[128:131]\n"
      "ds_read_b64_tr_b16 v[96:97], v6 offset:40960\n"
      "v_mfma_f32_16x16x32_bf16 v[128:131], a[60:63], v[80:83], v[128:131]\n"
      "ds_read_b64_tr_b16 v[98:99], v6 offset:43008\n"
      "v_mfma_f32_16x16x32_bf16 v[144:147], a[52:55], v[64:67], v[144:147]\n"
      "ds_read_b64_tr_b16 v[100:101], v33 offset:40960\n"
      "s_waitcnt vmcnt(9)\n"
      "v_mfma_f32_16x16x32_bf16 v[144:147], a[64:67], v[80:83], v[144:147]\n"
      "ds_read_b128 v[248:251], v40 offset:49152\n"
      "s_add_u32 m0, s73, 16384\n"
      "v_add_u32 v43, 65536, v40\n"
      "v_mfma_f32_16x16x32_bf16 v[160:163], a[56:59], v[64:67], v[160:163]\n"
      "ds_read_b128 v[252:255], v40 offset:50176\n"
      "v_mfma_f32_16x16x32_bf16 v[160:163], a[68:71], v[80:83], v[160:163]\n"
      "global_load_dwordx4 v[224:227], v53, s[26:27] sc1\n"
      "ds_read_b128 v[36:39], v40 offset:51200\n"
      "ds_read_b64_tr_b16 v[102:103], v33 offset:43008\n"
      "v_mfma_f32_16x16x32_bf16 v[132:135], a[48:51], v[68:71], v[132:135]\n"
      "ds_read_b128 v[60:63], v40 offset:52224\n"
      "ds_read_b64_tr_b16 v[104:105], v34 offset:40960\n"
      "v_add_u32 v53, 0x6000, v53\n"
      "v_mfma_f32_16x16x32_bf16 v[132:135], a[60:63], v[84:87], v[132:135]\n"
      "ds_read_b64_tr_b16 v[106:107], v34 offset:43008\n"
      "v_mfma_f32_16x16x32_bf16 v[148:151], a[52:55], v[68:71], v[148:151]\n"
      "global_load_dwordx4 v[228:231], v53, s[26:27] sc1\n"
      "ds_read_b64_tr_b16 v[108:109], v35 offset:40960\n"
      "v_mfma_f32_16x16x32_bf16 v[148:151], a[64:67], v[84:87], v[148:151]\n"
      "ds_read_b64_tr_b16 v[110:111], v35 offset:43008\n"
      "v_add_u32 v53, 0x6000, v53\n"
      "v_mfma_f32_16x16x32_bf16 v[164:167], a[56:59], v[68:71], v[164:167]\n"
      "ds_read_b64_tr_b16 v[112:113], v6 offset:45056\n"
      "s_waitcnt lgkmcnt(5)\n"
      "v_add3_u32 v54, v54, v248, v249\n"
      "v_add3_u32 v54, v54, v250, v251\n"
      "v_mfma_f32_16x16x32_bf16 v[164:167], a[68:71], v[84:87], v[164:167]\n"
      "global_load_dwordx4 v[232:235], v53, s[26:27] sc1\n"
      "v_add3_u32 v54, v54, v252, v253\n"
      "v_add3_u32 v54, v54, v254, v255\n"
      "v_mfma_f32_16x16x32_bf16 v[136:139], a[48:51], v[72:75], v[136:139]\n"
      "ds_read_b64_tr_b16 v[114:115], v6 offset:47104\n"
      "v_add_u32 v53, 0x6000, v53\n"
      "v_add3_u32 v54, v54, v36, v37\n"
      "v_add3_u32 v54, v54, v38, v39\n"
      "v_mfma_f32_16x16x32_bf16 v[136:139], a[60:63], v[88:91], v[136:139]\n"
      "ds_read_b64_tr_b16 v[116:117], v33 offset:45056\n"
      "v_add3_u32 v54, v54, v60, v61\n"
      "v_mfma_f32_16x16x32_bf16 v[152:155], a[52:55], v[72:75], v[152:155]\n"
      "global_load_dwordx4 v[236:239], v53, s[26:27] sc1\n"
      "ds_read_b64_tr_b16 v[118:119], v33 offset:47104\n"
      "v_add3_u32 v54, v54, v62, v63\n"
      "v_mfma_f32_16x16x32_bf16 v[152:155], a[64:67], v[88:91], v[152:155]\n"
      "ds_read_b64_tr_b16 v[120:121], v34 offset:45056\n"
      "v_add_u32 v53, 0x6000, v53\n"
      "v_mfma_f32_16x16x32_bf16 v[168:171], a[56:59], v[72:75], v[168:171]\n"
      "ds_read_b64_tr_b16 v[122:123], v34 offset:47104\n"
      "v_add_u32_dpp v54, v54, v54 quad_perm:[1,0,3,2] row_mask:0xf bank_mask:0xf\n"
      "v_mfma_f32_16x16x32_bf16 v[168:171], a[68:71], v[88:91], v[168:171]\n"
      "global_load_dwordx4 v[240:243], v53, s[26:27] sc1\n"
      "ds_read_b64_tr_b16 v[124:125], v35 offset:45056\n"
      "v_mfma_f32_16x16x32_bf16 v[140:143], a[48:51], v[76:79], v[140:143]\n"
      "ds_read_b64_tr_b16 v[126:127], v35 offset:47104\n"
      "v_add_u32 v53, 0x6000, v53\n"
      "v_add_u32_dpp v54, v54, v54 quad_perm:[2,3,0,1] row_mask:0xf bank_mask:0xf\n"
      "v_mfma_f32_16x16x32_bf16 v[140:143], a[60:63], v[92:95], v[140:143]\n"
      "v_mfma_f32_16x16x32_bf16 v[156:159], a[52:55], v[76:79], v[156:159]\n"
      "global_load_dwordx4 v[244:247], v53, s[26:27] sc1\n"
      "v_add_u32_dpp v54, v54, v54 row_half_mirror row_mask:0xf bank_mask:0xf\n"
      "v_cmp_ne_u32 vcc, s71, v54\n"
      "v_mfma_f32_16x16x32_bf16 v[156:159], a[64:67], v[92:95], v[156:159]\n"
      "v_mfma_f32_16x16x32_bf16 v[172:175], a[56:59], v[76:79], v[172:175]\n"
      "s_cbranch_vccnz L_sx3_%=\n"
      "L_bx3_%=:\n"
      "v_mfma_f32_16x16x32_bf16 v[172:175], a[68:71], v[92:95], v[172:175]\n"
      "global_load_dwordx4 a[164:167], v45, s[8:9] offset:-3072 sc1\n"
      "s_waitcnt lgkmcnt(0)\n"
      "s_barrier\n"
      "v_mfma_f32_16x16x32_bf16 v[128:131], a[72:75], v[96:99], v[128:131]\n"
      "ds_read_b64_tr_b16 v[64:65], v6 offset:49152\n"
      "v_mfma_f32_16x16x32_bf16 v[128:131], a[84:87], v[112:115], v[128:131]\n"
      "ds_read_b64_tr_b16 v[66:67], v6 offset:51200\n"
      "v_mfma_f32_16x16x32_bf16 v[144:147], a[76:79], v[96:99], v[144:147]\n"
      "ds_read_b64_tr_b16 v[68:69], v33 offset:49152\n"
      "s_waitcnt vmcnt(7)\n"
      "v_mfma_f32_16x16x32_bf16 v[144:147], a[88:91], v[112:115], v[144:147]\n"
      "global_load_lds_dwordx4 v43, s[4:5] offset:0 sc1\n"
      "ds_read_b128 v[248:251], v50 offset:0\n"
      "v_mfma_f32_16x16x32_bf16 v[160:163], a[80:83], v[96:99], v[160:163]\n"
      "ds_read_b128 v[252:255], v50 offset:1024\n"
      "v_mfma_f32_16x16x32_bf16 v[160:163], a[92:95], v[112:115], v[160:163]\n"
      "ds_read_b64_tr_b16 v[70:71], v33 offset:51200\n"
      "v_mfma_f32_16x16x32_bf16 v[132:135], a[72:75], v[100:103], v[132:135]\n"
      "ds_read_b128 v[36:39], v50 offset:2048\n"
      "ds_read_b64_tr_b16 v[72:73], v34 offset:49152\n"
      "v_mfma_f32_16x16x32_bf16 v[132:135], a[84:87], v[116:119], v[132:135]\n"
      "global_load_dwordx4 a[168:171], v45, s[8:9] offset:-2048 sc1\n"
      "ds_read_b64_tr_b16 v[74:75], v34 offset:51200\n"
      "v_mfma_f32_16x16x32_bf16 v[148:151], a[76:79], v[100:103], v[148:151]\n"
      "ds_read_b64_tr_b16 v[76:77], v35 offset:49152\n"
      "v_mfma_f32_16x16x32_bf16 v[148:151], a[88:91], v[116:119], v[148:151]\n"
      "ds_read_b64_tr_b16 v[78:79], v35 offset:51200\n"
      "v_mfma_f32_16x16x32_bf16 v[164:167], a[80:83], v[100:103], v[164:167]\n"
      "ds_read_b64_tr_b16 v[80:81], v6 offset:53248\n"
      "s_waitcnt lgkmcnt(5)\n"
      "v_add3_u32 v32, v32, v248, v249\n"
      "v_add3_u32 v32, v32, v250, v251\n"
      "v_mfma_f32_16x16x32_bf16 v[164:167], a[92:95], v[116:119], v[164:167]\n"
      "global_load_lds_dwordx4 v43, s[4:5] offset:1024 sc1\n"
      "v_add3_u32 v32, v32, v252, v253\n"
      "v_mfma_f32_16x16x32_bf16 v[136:139], a[72:75], v[104:107], v[136:139]\n"
      "ds_read_b64_tr_b16 v[82:83], v6 offset:55296\n"
      "v_add3_u32 v32, v32, v254, v255\n"
      "v_mfma_f32_16x16x32_bf16 v[136:139], a[84:87], v[120:123], v[136:139]\n"
      "ds_read_b64_tr_b16 v[84:85], v33 offset:53248\n"
      "v_add3_u32 v32, v32, v36, v37\n"
      "v_mfma_f32_16x16x32_bf16 v[152:155], a[76:79], v[104:107], v[152:155]\n"
      "ds_read_b64_tr_b16 v[86:87], v33 offset:55296\n"
      "v_add3_u32 v32, v32, v38, v39\n"
      "v_mfma_f32_16x16x32_bf16 v[152:155], a[88:91], v[120:123], v[152:155]\n"
      "global_load_dwordx4 a[172:175], v45, s[8:9] offset:-1024 sc1\n"
      "ds_read_b64_tr_b16 v[88:89], v34 offset:53248\n"
      "v_mfma_f32_16x16x32_bf16 v[168:171], a[80:83], v[104:107], v[168:171]\n"
      "ds_read_b64_tr_b16 v[90:91], v34 offset:55296\n"
      "v_add_u32_dpp v32, v32, v32 quad_perm:[1,0,3,2] row_mask:0xf bank_mask:0xf\n"
      "v_mfma_f32_16x16x32_bf16 v[168:171], a[92:95], v[120:123], v[168:171]\n"
      "ds_read_b64_tr_b16 v[92:93], v35 offset:53248\n"
      "v_mfma_f32_16x16x32_bf16 v[140:143], a[72:75], v[108:111], v[140:143]\n"
      "ds_read_b64_tr_b16 v[94:95], v35 offset:55296\n"
      "v_add_u32_dpp v32, v32, v32 quad_perm:[2,3,0,1] row_mask:0xf bank_mask:0xf\n"
      "v_mfma_f32_16x16x32_bf16 v[140:143], a[84:87], v[124:127], v[140:143]\n"
      "global_load_lds_dwordx4 v43, s[4:5] offset:2048 sc1\n"
      "v_mfma_f32_16x16x32_bf16 v[156:159], a[76:79], v[108:111], v[156:159]\n"
      "v_add_u32_dpp v32, v32, v32 row_half_mirror row_mask:0xf bank_mask:0xf\n"
      "v_cmp_ne_u32 vcc, s72, v32\n"
      "v_mfma_f32_16x16x32_bf16 v[156:159], a[88:91], v[124:127], v[156:159]\n"
      "v_mfma_f32_16x16x32_bf16 v[172:175], a[80:83], v[108:111], v[172:175]\n"
      "s_cbranch_vccnz L_sxp_%=\n"
      "L_bxp_%=:\n"
      "v_mfma_f32_16x16x32_bf16 v[172:175], a[92:95], v[124:127], v[172:175]\n"
      "global_load_dwordx4 a[176:179], v45, s[8:9] offset:0 sc1\n"
      "s_waitcnt lgkmcnt(0)\n"
      "s_barrier\n"
      "v_mfma_f32_16x16x32_bf16 v[128:131], a[96:99], v[64:67], v[128:131]\n"
      "ds_read_b64_tr_b16 v[8:9], v27 offset:0\n"
      "v_mfma_f32_16x16x32_bf16 v[128:131], a[108:111], v[80:83], v[128:131]\n"
      "ds_read_b64_tr_b16 v[10:11], v27 offset:2048\n"
      "v_mfma_f32_16x16x32_bf16 v[144:147], a[100:103], v[64:67], v[144:147]\n"
      "ds_read_b64_tr_b16 v[12:13], v27 offset:4096\n"
      "ds_read_b64_tr_b16 v[96:97], v6 offset:57344\n"
      "v_mfma_f32_16x16x32_bf16 v[144:147], a[112:115], v[80:83], v[144:147]\n"
      "global_load_lds_dwordx4 v43, s[4:5] offset:3072 sc1\n"
      "ds_read_b64_tr_b16 v[14:15], v27 offset:6144\n"
      "ds_read_b64_tr_b16 v[98:99], v6 offset:59392\n"
      "s_mov_b32 s47, 131072\n"
      "v_mfma_f32_16x16x32_bf16 v[160:163], a[104:107], v[64:67], v[160:163]\n"
      "ds_read_b64_tr_b16 v[16:17], v27 offset:8192\n"
      "v_mov_b32 v31, 0x3f803f80\n"
      "v_mfma_f32_16x16x32_bf16 v[160:163], a[116:119], v[80:83], v[160:163]\n"
      "ds_read_b64_tr_b16 v[100:101], v33 offset:57344\n"
      "v_and_b32 v50, 0xf0, v7\n"
      "v_mfma_f32_16x16x32_bf16 v[132:135], a[96:99], v[68:71], v[132:135]\n"
      "ds_read_b64_tr_b16 v[102:103], v33 offset:59392\n"
      "s_mov_b32 s42, 0x00f0000f\n"
      "v_mfma_f32_16x16x32_bf16 v[132:135], a[108:111], v[84:87], v[132:135]\n"
      "global_load_dwordx4 a[180:183], v45, s[8:9] offset:1024 sc1\n"
      "s_mov_b32 s43, 0xf0000f00\n"
      "v_mfma_f32_16x16x32_bf16 v[148:151], a[100:103], v[68:71], v[148:151]\n"
      "ds_read_b64_tr_b16 v[104:105], v34 offset:57344\n"
      "v_mfma_f32_16x16x32_bf16 v[148:151], a[112:115], v[84:87], v[148:151]\n"
      "ds_read_b128 v[248:251], v40 offset:0\n"
      "ds_read_b64_tr_b16 v[106:107], v34 offset:59392\n"
      "v_bfrev_b32 v32, 1\n"
      "v_cndmask_b32 v50, v32, v50, s[42:43]\n"
      "v_mfma_f32_16x16x32_bf16 v[164:167], a[104:107], v[68:71], v[164:167]\n"
      "ds_read_b128 v[252:255], v40 offset:1024\n"
      "v_mfma_f32_16x16x32_bf16 v[164:167], a[116:119], v[84:87], v[164:167]\n"
      "buffer_load_dwordx2 v[2:3], v5, s[32:35], s47 offen sc1\n"
      "ds_read_b128 v[36:39], v40 offset:2048\n"
      "ds_read_b64_tr_b16 v[108:109], v35 offset:57344\n"
      "s_waitcnt lgkmcnt(8)\n"
      "v_mfma_f32_16x16x32_bf16 v[20:23], v[28:31], v[8:11], 0\n"
      "ds_read_b128 v[60:63], v40 offset:3072\n"
      "ds_read_b64_tr_b16 v[110:111], v35 offset:59392\n"
      "v_mfma_f32_16x16x32_bf16 v[24:27], v[8:11], v[8:11], 0\n"
      "ds_read_b64_tr_b16 v[112:113], v6 offset:61440\n"
      "v_mfma_f32_16x16x32_bf16 v[20:23], v[28:31], v[12:15], v[20:23]\n"
      "global_load_dwordx4 a[184:187], v45, s[8:9] offset:2048 sc1\n"
      "v_mfma_f32_16x16x32_bf16 v[24:27], v[12:15], v[12:15], v[24:27]\n"
      "ds_read_b64_tr_b16 v[114:115], v6 offset:63488\n"
      "v_mfma_f32_16x16x32_bf16 v[20:23], v[28:31], v[16:19], v[20:23]\n"
      "ds_read_b64_tr_b16 v[116:117], v33 offset:61440\n"
      "s_waitcnt lgkmcnt(4)\n"
      "v_add3_u32 v56, v56, v248, v249\n"
      "v_add3_u32 v56, v56, v250, v251\n"
      "v_mfma_f32_16x16x32_bf16 v[24:27], v[16:19], v[16:19], v[24:27]\n"
      "ds_read_b64_tr_b16 v[118:119], v33 offset:63488\n"
      "v_add3_u32 v56, v56, v252, v253\n"
      "v_add3_u32 v56, v56, v254, v255\n"
      "v_mfma_f32_16x16x32_bf16 v[136:139], a[96:99], v[72:75], v[136:139]\n"
      "global_load_dwordx4 a[188:191], v45, s[8:9] offset:3072 sc1\n"
      "v_add3_u32 v56, v56, v36, v37\n"
      "v_add3_u32 v56, v56, v38, v39\n"
      "v_mfma_f32_16x16x32_bf16 v[136:139], a[108:111], v[88:91], v[136:139]\n"
      "ds_read_b64_tr_b16 v[120:121], v34 offset:61440\n"
      "v_add3_u32 v56, v56, v60, v61\n"
      "v_mfma_f32_16x16x32_bf16 v[152:155], a[100:103], v[72:75], v[152:155]\n"
      "ds_read_b64_tr_b16 v[122:123], v34 offset:63488\n"
      "v_add3_u32 v56, v56, v62, v63\n"
      "v_cndmask_b32 v24, v24, v25, s[48:49]\n"
      "v_cndmask_b32 v26, v26, v27, s[48:49]\n"
      "v_mfma_f32_16x16x32_bf16 v[152:155], a[112:115], v[88:91], v[152:155]\n"
      "ds_read_b64_tr_b16 v[124:125], v35 offset:61440\n"
      "v_mov_b32 v22, s30\n"
      "v_cndmask_b32 v21, v24, v26, s[50:51]\n"
      "v_add_u32 v45, 53248, v7\n"
      "v_mfma_f32_16x16x32_bf16 v[168:171], a[104:107], v[72:75], v[168:171]\n"
      "global_load_dwordx4 a[192:195], v45, s[8:9] offset:-4096 sc1\n"
      "v_add_u32_dpp v56, v56, v56 quad_perm:[1,0,3,2] row_mask:0xf bank_mask:0xf\n"
      "v_mov_b32 v23, s31\n"
      "v_mfma_f32_16x16x32_bf16 v[168:171], a[116:119], v[88:91], v[168:171]\n"
      "buffer_store_dwordx4 v[20:23], v50, s[32:35], s92 offen sc1\n"
      "s_waitcnt vmcnt(14)\n"
      "v_mfma_f32_16x16x32_bf16 v[140:143], a[96:99], v[76:79], v[140:143]\n"
      "ds_read_b64_tr_b16 v[126:127], v35 offset:63488\n"
      "v_add_u32_dpp v56, v56, v56 quad_perm:[2,3,0,1] row_mask:0xf bank_mask:0xf\n"
      "v_mfma_f32_16x16x32_bf16 v[140:143], a[108:111], v[92:95], v[140:143]\n"
      "v_add_f32 v224, v224, v228\n"
      "v_add_f32 v225, v225, v229\n"
      "v_mfma_f32_16x16x32_bf16 v[156:159], a[100:103], v[76:79], v[156:159]\n"
      "global_load_dwordx4 a[196:199], v45, s[8:9] offset:-3072 sc1\n"
      "v_add_u32_dpp v56, v56, v56 row_half_mirror row_mask:0xf bank_mask:0xf\n"
      "v_cmp_ne_u32 vcc, s71, v56\n"
      "v_mfma_f32_16x16x32_bf16 v[156:159], a[112:115], v[92:95], v[156:159]\n"
      "v_add_f32 v226, v226, v230\n"
      "v_add_f32 v227, v227, v231\n"
      "v_mfma_f32_16x16x32_bf16 v[172:175], a[104:107], v[76:79], v[172:175]\n"
      "s_cbranch_vccnz L_sx4_%=\n"
      "L_bx4_%=:\n"
      "v_add_f32 v232, v232, v236\n"
      "v_mfma_f32_16x16x32_bf16 v[172:175], a[116:119], v[92:95], v[172:175]\n"
      "s_waitcnt lgkmcnt(0)\n"
      "s_barrier\n"
      "v_mfma_f32_16x16x32_bf16 v[128:131], a[120:123], v[96:99], v[128:131]\n"
      "global_load_dwordx4 a[200:203], v45, s[8:9] offset:-2048 sc1\n"
      "v_add_f32 v233, v233, v237\n"
      "v_mfma_f32_16x16x32_bf16 v[128:131], a[132:135], v[112:115], v[128:131]\n"
      "ds_read_b64_tr_b16 v[64:65], v6 offset:0\n"
      "v_add_f32 v234, v234, v238\n"
      "v_mfma_f32_16x16x32_bf16 v[144:147], a[124:127], v[96:99], v[144:147]\n"
      "ds_read_b64_tr_b16 v[66:67], v6 offset:2048\n"
      "v_add_f32 v235, v235, v239\n"
      "v_mfma_f32_16x16x32_bf16 v[144:147], a[136:139], v[112:115], v[144:147]\n"
      "ds_read_b64_tr_b16 v[68:69], v33 offset:0\n"
      "v_add_f32 v240, v240, v244\n"
      "v_mfma_f32_16x16x32_bf16 v[160:163], a[128:131], v[96:99], v[160:163]\n"
      "global_load_dwordx4 a[204:207], v45, s[8:9] offset:-1024 sc1\n"
      "v_add_f32 v241, v241, v245\n"
      "v_mfma_f32_16x16x32_bf16 v[160:163], a[140:143], v[112:115], v[160:163]\n"
      "ds_read_b64_tr_b16 v[70:71], v33 offset:2048\n"
      "v_add_f32 v242, v242, v246\n"
      "v_mfma_f32_16x16x32_bf16 v[132:135], a[120:123], v[100:103], v[132:135]\n"
      "ds_read_b64_tr_b16 v[72:73], v34 offset:0\n"
      "v_add_f32 v243, v243, v247\n"
      "v_mfma_f32_16x16x32_bf16 v[132:135], a[132:135], v[116:119], v[132:135]\n"
      "ds_read_b64_tr_b16 v[74:75], v34 offset:2048\n"
      "v_add_f32 v224, v224, v232\n"
      "v_mfma_f32_16x16x32_bf16 v[148:151], a[124:127], v[100:103], v[148:151]\n"
      "global_load_dwordx4 a[208:211], v45, s[8:9] offset:0 sc1\n"
      "ds_read_b64_tr_b16 v[76:77], v35 offset:0\n"
      "v_add_f32 v225, v225, v233\n"
      "v_mfma_f32_16x16x32_bf16 v[148:151], a[136:139], v[116:119], v[148:151]\n"
      "ds_read_b64_tr_b16 v[78:79], v35 offset:2048\n"
      "v_add_f32 v226, v226, v234\n"
      "v_mfma_f32_16x16x32_bf16 v[164:167], a[128:131], v[100:103], v[164:167]\n"
      "ds_read_b64_tr_b16 v[80:81], v6 offset:4096\n"
      "v_add_f32 v227, v227, v235\n"
      "v_mfma_f32_16x16x32_bf16 v[164:167], a[140:143], v[116:119], v[164:167]\n"
      "ds_read_b64_tr_b16 v[82:83], v6 offset:6144\n"
      "v_add_f32 v224, v224, v240\n"
      "v_mfma_f32_16x16x32_bf16 v[136:139], a[120:123], v[104:107], v[136:139]\n"
      "global_load_dwordx4 a[212:215], v45, s[8:9] offset:1024 sc1\n"
      "v_add_f32 v225, v225, v241\n"
      "v_mfma_f32_16x16x32_bf16 v[136:139], a[132:135], v[120:123], v[136:139]\n"
      "ds_read_b64_tr_b16 v[84:85], v33 offset:4096\n"
      "v_add_f32 v226, v226, v242\n"
      "v_mfma_f32_16x16x32_bf16 v[152:155], a[124:127], v[104:107], v[152:155]\n"
      "ds_read_b64_tr_b16 v[86:87], v33 offset:6144\n"
      "s_add_u32 m0, s73, 32768\n"
      "v_add_u32 v43, 98304, v40\n"
      "v_mfma_f32_16x16x32_bf16 v[152:155], a[136:139], v[120:123], v[152:155]\n"
      "global_load_lds_dwordx4 v43, s[4:5] offset:0 sc1\n"
      "ds_read_b64_tr_b16 v[88:89], v34 offset:4096\n"
      "v_add_f32 v227, v227, v243\n"
      "v_mfma_f32_16x16x32_bf16 v[168:171], a[128:131], v[104:107], v[168:171]\n"
      "global_load_dwordx4 a[216:219], v45, s[8:9] offset:2048 sc1\n"
      "ds_read_b64_tr_b16 v[90:91], v34 offset:6144\n"
      "v_add_u32 v53, 0x6000, v53\n"
      "v_mfma_f32_16x16x32_bf16 v[168:171], a[140:143], v[120:123], v[168:171]\n"
      "global_load_dwordx4 v[228:231], v53, s[26:27] sc1\n"
      "ds_read_b64_tr_b16 v[92:93], v35 offset:4096\n"
      "v_add_u32 v53, 0x6000, v53\n"
      "v_mfma_f32_16x16x32_bf16 v[140:143], a[120:123], v[108:111], v[140:143]\n"
      "global_load_dwordx4 v[232:235], v53, s[26:27] sc1\n"
      "v_add_u32 v53, 0x6000, v53\n"
      "v_mfma_f32_16x16x32_bf16 v[140:143], a[132:135], v[124:127], v[140:143]\n"
      "global_load_lds_dwordx4 v43, s[4:5] offset:1024 sc1\n"
      "global_load_dwordx4 v[236:239], v53, s[26:27] sc1\n"
      "ds_read_b64_tr_b16 v[94:95], v35 offset:6144\n"
      "v_add_u32 v53, 0x6000, v53\n"
      "v_mfma_f32_16x16x32_bf16 v[156:159], a[124:127], v[108:111], v[156:159]\n"
      "global_load_dwordx4 a[220:223], v45, s[8:9] offset:3072 sc1\n"
      "global_load_dwordx4 v[240:243], v53, s[26:27] sc1\n"
      "v_and_b32 v53, 0x300, v7\n"
      "L_pg4_%=:\n"
      "s_cbranch_execz L_pgr_%=\n"
      "v_mfma_f32_16x16x32_bf16 v[156:159], a[136:139], v[124:127], v[156:159]\n"
      "v_and_b32 v59, 0x30, v7\n"
      "v_lshl_add_u32 v53, v59, 2, v53\n"
      "v_mfma_f32_16x16x32_bf16 v[172:175], a[128:131], v[108:111], v[172:175]\n"
      "v_bfe_u32 v59, v7, 6, 2\n"
      "v_lshl_add_u32 v53, v59, 4, v53\n"
      "v_lshrrev_b32 v59, 6, v7\n"
      "v_mfma_f32_16x16x32_bf16 v[172:175], a[140:143], v[124:127], v[172:175]\n"
      "global_load_lds_dwordx4 v43, s[4:5] offset:2048 sc1\n"
      "v_add_u32 v45, 61440, v7\n"
      "s_waitcnt vmcnt(27)\n"
      "s_waitcnt lgkmcnt(0)\n"
      "v_mfma_f32_16x16x32_bf16 v[128:131], a[144:147], v[64:67], v[128:131]\n"
      "global_load_dwordx4 a[224:227], v45, s[8:9] offset:-4096 sc1\n"
      "buffer_load_dwordx4 v[244:247], v53, s[32:35], s66 offen sc1\n"
      "v_and_b32 v32, 0x200, v7\n"
      "v_mfma_f32_16x16x32_bf16 v[128:131], a[156:159], v[80:83], v[128:131]\n"
      "ds_read_b64_tr_b16 v[96:97], v6 offset:8192\n"
      "v_and_b32 v50, 0x1f0, v7\n"
      "v_mfma_f32_16x16x32_bf16 v[144:147], a[148:151], v[64:67], v[144:147]\n"
      "ds_read_b64_tr_b16 v[98:99], v6 offset:10240\n"
      "s_waitcnt vmcnt(20)\n"
      "v_mfma_f32_16x16x32_bf16 v[144:147], a[160:163], v[80:83], v[144:147]\n"
      "global_load_lds_dwordx4 v43, s[4:5] offset:3072 sc1\n"
      "ds_read_b128 v[248:251], v40 offset:16384\n"
      "v_lshl_add_u32 v50, v32, 3, v50\n"
      "v_mfma_f32_16x16x32_bf16 v[160:163], a[152:155], v[64:67], v[160:163]\n"
      "global_load_dwordx4 a[228:231], v45, s[8:9] offset:-3072 sc1\n"
      "ds_read_b128 v[252:255], v40 offset:17408\n"
      "ds_read_b64_tr_b16 v[100:101], v33 offset:8192\n"
      "v_and_b32 v32, 0xf8, v4\n"
      "v_mfma_f32_16x16x32_bf16 v[160:163], a[164:167], v[80:83], v[160:163]\n"
      "ds_read_b128 v[36:39], v40 offset:18432\n"
      "ds_read_b64_tr_b16 v[102:103], v33 offset:10240\n"
      "v_add_u32 v32, s95, v32\n"
      "v_mfma_f32_16x16x32_bf16 v[132:135], a[144:147], v[68:71], v[132:135]\n"
      "ds_read_b128 v[60:63], v40 offset:19456\n"
      "ds_read_b64_tr_b16 v[104:105], v34 offset:8192\n"
      "v_mfma_f32_16x16x32_bf16 v[132:135], a[156:159], v[84:87], v[132:135]\n"
      "ds_read_b64_tr_b16 v[106:107], v34 offset:10240\n"
      "v_mfma_f32_16x16x32_bf16 v[148:151], a[148:151], v[68:71], v[148:151]\n"
      "global_load_dwordx4 a[232:235], v45, s[8:9] offset:-2048 sc1\n"
      "ds_read_b64_tr_b16 v[108:109], v35 offset:8192\n"
      "v_mfma_f32_16x16x32_bf16 v[148:151], a[160:163], v[84:87], v[148:151]\n"
      "ds_read_b64_tr_b16 v[110:111], v35 offset:10240\n"
      "v_mfma_f32_16x16x32_bf16 v[164:167], a[152:155], v[68:71], v[164:167]\n"
      "ds_read_b64_tr_b16 v[112:113], v6 offset:12288\n"
      "s_waitcnt lgkmcnt(5)\n"
      "v_add3_u32 v2, v2, v248, v249\n"
      "v_add3_u32 v2, v2, v250, v251\n"
      "v_mfma_f32_16x16x32_bf16 v[164:167], a[164:167], v[84:87], v[164:167]\n"
      "ds_read_b64_tr_b16 v[114:115], v6 offset:14336\n"
      "v_add3_u32 v2, v2, v252, v253\n"
      "v_add3_u32 v2, v2, v254, v255\n"
      "v_mfma_f32_16x16x32_bf16 v[136:139], a[144:147], v[72:75], v[136:139]\n"
      "global_load_dwordx4 a[236:239], v45, s[8:9] offset:-1024 sc1\n"
      "v_add3_u32 v2, v2, v36, v37\n"
      "v_add3_u32 v2, v2, v38, v39\n"
      "v_mfma_f32_16x16x32_bf16 v[136:139], a[156:159], v[88:91], v[136:139]\n"
      "ds_read_b64_tr_b16 v[116:117], v33 offset:12288\n"
      "v_add3_u32 v2, v2, v60, v61\n"
      "v_mfma_f32_16x16x32_bf16 v[152:155], a[148:151], v[72:75], v[152:155]\n"
      "ds_read_b64_tr_b16 v[118:119], v33 offset:14336\n"
      "v_add3_u32 v2, v2, v62, v63\n"
      "v_mfma_f32_16x16x32_bf16 v[152:155], a[160:163], v[88:91], v[152:155]\n"
      "ds_read_b64_tr_b16 v[120:121], v34 offset:12288\n"
      "v_mfma_f32_16x16x32_bf16 v[168:171], a[152:155], v[72:75], v[168:171]\n"
      "ds_read_b64_tr_b16 v[122:123], v34 offset:14336\n"
      "v_add_u32_dpp v2, v2, v2 quad_perm:[1,0,3,2] row_mask:0xf bank_mask:0xf\n"
      "v_mfma_f32_16x16x32_bf16 v[168:171], a[164:167], v[88:91], v[168:171]\n"
      "ds_read_b64_tr_b16 v[124:125], v35 offset:12288\n"
      "v_mfma_f32_16x16x32_bf16 v[140:143], a[144:147], v[76:79], v[140:143]\n"
      "ds_read_b64_tr_b16 v[126:127], v35 offset:14336\n"
      "s_add_u32 m0, s73, 49152\n"
      "v_add_u32 v43, 114688, v40\n"
      "v_add_u32_dpp v2, v2, v2 quad_perm:[2,3,0,1] row_mask:0xf bank_mask:0xf\n"
      "v_mfma_f32_16x16x32_bf16 v[140:143], a[156:159], v[92:95], v[140:143]\n"
      "v_mfma_f32_16x16x32_bf16 v[156:159], a[148:151], v[76:79], v[156:159]\n"
      "global_load_lds_dwordx4 v43, s[4:5] offset:0 sc1\n"
      "v_add_u32_dpp v2, v2, v2 row_half_mirror row_mask:0xf bank_mask:0xf\n"
      "v_cmp_ne_u32 vcc, s71, v2\n"
      "v_mfma_f32_16x16x32_bf16 v[156:159], a[160:163], v[92:95], v[156:159]\n"
      "v_mfma_f32_16x16x32_bf16 v[172:175], a[152:155], v[76:79], v[172:175]\n"
      "s_cbranch_vccnz L_sx5_%=\n"
      "L_bx5_%=:\n"
      "v_mfma_f32_16x16x32_bf16 v[172:175], a[164:167], v[92:95], v[172:175]\n"
      "s_waitcnt vmcnt(23)\n"
      "s_waitcnt lgkmcnt(0)\n"
      "s_barrier\n"
      "v_mfma_f32_16x16x32_bf16 v[128:131], a[168:171], v[96:99], v[128:131]\n"
      "global_load_lds_dwordx4 v43, s[4:5] offset:1024 sc1\n"
      "v_mfma_f32_16x16x32_bf16 v[128:131], a[180:183], v[112:115], v[128:131]\n"
      "ds_read_b64_tr_b16 v[64:65], v6 offset:16384\n"
      "v_mfma_f32_16x16x32_bf16 v[144:147], a[172:175], v[96:99], v[144:147]\n"
      "ds_read_b64_tr_b16 v[66:67], v6 offset:18432\n"
      "v_mfma_f32_16x16x32_bf16 v[144:147], a[184:187], v[112:115], v[144:147]\n"
      "ds_read_b64_tr_b16 v[68:69], v33 offset:16384\n"
      "v_mfma_f32_16x16x32_bf16 v[160:163], a[176:179], v[96:99], v[160:163]\n"
      "global_load_lds_dwordx4 v43, s[4:5] offset:2048 sc1\n"
      "v_mfma_f32_16x16x32_bf16 v[160:163], a[188:191], v[112:115], v[160:163]\n"
      "ds_read_b64_tr_b16 v[70:71], v33 offset:18432\n"
      "v_mfma_f32_16x16x32_bf16 v[132:135], a[168:171], v[100:103], v[132:135]\n"
      "ds_read_b64_tr_b16 v[72:73], v34 offset:16384\n"
      "v_mfma_f32_16x16x32_bf16 v[132:135], a[180:183], v[116:119], v[132:135]\n"
      "ds_read_b64_tr_b16 v[74:75], v34 offset:18432\n"
      "v_mfma_f32_16x16x32_bf16 v[148:151], a[172:175], v[100:103], v[148:151]\n"
      "global_load_lds_dwordx4 v43, s[4:5] offset:3072 sc1\n"
      "ds_read_b64_tr_b16 v[76:77], v35 offset:16384\n"
      "v_mfma_f32_16x16x32_bf16 v[148:151], a[184:187], v[116:119], v[148:151]\n"
      "ds_read_b64_tr_b16 v[78:79], v35 offset:18432\n"
      "v_mfma_f32_16x16x32_bf16 v[164:167], a[176:179], v[100:103], v[164:167]\n"
      "ds_read_b64_tr_b16 v[80:81], v6 offset:20480\n"
      "s_add_u32 m0, s73, 0\n"
      "v_add_u32 v43, 131072, v40\n"
      "v_mfma_f32_16x16x32_bf16 v[164:167], a[188:191], v[116:119], v[164:167]\n"
      "ds_read_b64_tr_b16 v[82:83], v6 offset:22528\n"
      "v_mfma_f32_16x16x32_bf16 v[136:139], a[168:171], v[104:107], v[136:139]\n"
      "buffer_load_dwordx4 v[8:11], v50, s[32:35], s93 offen offset:0 sc1\n"
      "s_waitcnt vmcnt(9)\n"
      "v_mfma_f32_16x16x32_bf16 v[136:139], a[180:183], v[120:123], v[136:139]\n"
      "ds_read_b64_tr_b16 v[84:85], v33 offset:20480\n"
      "v_add_f32 v228, v228, v232\n"
      "v_mfma_f32_16x16x32_bf16 v[152:155], a[172:175], v[104:107], v[152:155]\n"
      "ds_read_b64_tr_b16 v[86:87], v33 offset:22528\n"
      "v_add_f32 v229, v229, v233\n"
      "v_mfma_f32_16x16x32_bf16 v[152:155], a[184:187], v[120:123], v[152:155]\n"
      "ds_read_b64_tr_b16 v[88:89], v34 offset:20480\n"
      "v_add_f32 v230, v230, v234\n"
      "v_mfma_f32_16x16x32_bf16 v[168:171], a[176:179], v[104:107], v[168:171]\n"
      "buffer_load_dwordx4 v[12:15], v50, s[32:35], s93 offen offset:1024 sc1\n"
      "ds_read_b64_tr_b16 v[90:91], v34 offset:22528\n"
      "v_add_f32 v231, v231, v235\n"
      "v_mfma_f32_16x16x32_bf16 v[168:171], a[188:191], v[120:123], v[168:171]\n"
      "ds_read_b64_tr_b16 v[92:93], v35 offset:20480\n"
      "v_add_f32 v236, v236, v240\n"
      "v_mfma_f32_16x16x32_bf16 v[140:143], a[168:171], v[108:111], v[140:143]\n"
      "ds_read_b64_tr_b16 v[94:95], v35 offset:22528\n"
      "v_add_f32 v237, v237, v241\n"
      "v_mfma_f32_16x16x32_bf16 v[140:143], a[180:183], v[124:127], v[140:143]\n"
      "v_add_f32 v238, v238, v242\n"
      "v_add_f32 v239, v239, v243\n"
      "v_mfma_f32_16x16x32_bf16 v[156:159], a[172:175], v[108:111], v[156:159]\n"
      "buffer_load_dwordx4 v[16:19], v50, s[32:35], s93 offen offset:2048 sc1\n"
      "v_add_f32 v224, v224, v228\n"
      "v_mfma_f32_16x16x32_bf16 v[156:159], a[184:187], v[124:127], v[156:159]\n"
      "v_add_f32 v225, v225, v229\n"
      "v_add_f32 v226, v226, v230\n"
      "v_mfma_f32_16x16x32_bf16 v[172:175], a[176:179], v[108:111], v[172:175]\n"
      "v_add_f32 v227, v227, v231\n"
      "v_add_f32 v224, v224, v236\n"
      "v_mfma_f32_16x16x32_bf16 v[172:175], a[188:191], v[124:127], v[172:175]\n"
      "s_waitcnt lgkmcnt(0)\n"
      "v_mfma_f32_16x16x32_bf16 v[128:131], a[192:195], v[64:67], v[128:131]\n"
      "buffer_load_dwordx4 v[20:23], v50, s[32:35], s93 offen offset:3072 sc1\n"
      "v_add_f32 v225, v225, v237\n"
      "v_mfma_f32_16x16x32_bf16 v[128:131], a[204:207], v[80:83], v[128:131]\n"
      "ds_read_b64_tr_b16 v[96:97], v6 offset:24576\n"
      "v_add_f32 v226, v226, v238\n"
      "v_mfma_f32_16x16x32_bf16 v[144:147], a[196:199], v[64:67], v[144:147]\n"
      "ds_read_b64_tr_b16 v[98:99], v6 offset:26624\n"
      "s_waitcnt vmcnt(11)\n"
      "v_mfma_f32_16x16x32_bf16 v[144:147], a[208:211], v[80:83], v[144:147]\n"
      "ds_read_b128 v[248:251], v40 offset:32768\n"
      "v_add_f32 v227, v227, v239\n"
      "v_mfma_f32_16x16x32_bf16 v[160:163], a[200:203], v[64:67], v[160:163]\n"
      "global_load_dwordx4 a[240:243], v44, s[6:7] offset:-4096 sc1\n"
      "ds_read_b128 v[252:255], v40 offset:33792\n"
      "ds_read_b64_tr_b16 v[100:101], v33 offset:24576\n"
      "v_mov_b32 v228, v224\n"
      "v_mfma_f32_16x16x32_bf16 v[160:163], a[212:215], v[80:83], v[160:163]\n"
      "ds_read_b128 v[36:39], v40 offset:34816\n"
      "ds_read_b64_tr_b16 v[102:103], v33 offset:26624\n"
      "v_mov_b32 v229, v225\n"
      "v_mfma_f32_16x16x32_bf16 v[132:135], a[192:195], v[68:71], v[132:135]\n"
      "ds_read_b128 v[60:63], v40 offset:35840\n"
      "ds_read_b64_tr_b16 v[104:105], v34 offset:24576\n"
      "v_mov_b32 v230, v226\n"
      "v_mfma_f32_16x16x32_bf16 v[132:135], a[204:207], v[84:87], v[132:135]\n"
      "ds_read_b64_tr_b16 v[106:107], v34 offset:26624\n"
      "v_mov_b32 v231, v227\n"
      "v_mfma_f32_16x16x32_bf16 v[148:151], a[196:199], v[68:71], v[148:151]\n"
      "global_load_dwordx4 a[244:247], v44, s[6:7] offset:-3072 sc1\n"
      "ds_read_b64_tr_b16 v[108:109], v35 offset:24576\n"
      "v_lshl_add_u32 v59, v59, 3, s55\n"
      "v_mfma_f32_16x16x32_bf16 v[148:151], a[208:211], v[84:87], v[148:151]\n"
      "ds_read_b64_tr_b16 v[110:111], v35 offset:26624\n"
      "v_permlane32_swap_b32 v224, v228\n"
      "v_mfma_f32_16x16x32_bf16 v[164:167], a[200:203], v[68:71], v[164:167]\n"
      "ds_read_b64_tr_b16 v[112:113], v6 offset:28672\n"
      "s_waitcnt lgkmcnt(5)\n"
      "v_add3_u32 v49, v49, v248, v249\n"
      "v_add3_u32 v49, v49, v250, v251\n"
      "v_mfma_f32_16x16x32_bf16 v[164:167], a[212:215], v[84:87], v[164:167]\n"
      "ds_read_b64_tr_b16 v[114:115], v6 offset:30720\n"
      "v_add3_u32 v49, v49, v252, v253\n"
      "v_add3_u32 v49, v49, v254, v255\n"
      "v_mfma_f32_16x16x32_bf16 v[136:139], a[192:195], v[72:75], v[136:139]\n"
      "global_load_dwordx4 a[248:251], v44, s[6:7] offset:-2048 sc1\n"
      "v_add3_u32 v49, v49, v36, v37\n"
      "v_add3_u32 v49, v49, v38, v39\n"
      "v_mfma_f32_16x16x32_bf16 v[136:139], a[204:207], v[88:91], v[136:139]\n"
      "ds_read_b64_tr_b16 v[116:117], v33 offset:28672\n"
      "v_add3_u32 v49, v49, v60, v61\n"
      "v_mfma_f32_16x16x32_bf16 v[152:155], a[196:199], v[72:75], v[152:155]\n"
      "ds_read_b64_tr_b16 v[118:119], v33 offset:30720\n"
      "v_add3_u32 v49, v49, v62, v63\n"
      "v_mfma_f32_16x16x32_bf16 v[152:155], a[208:211], v[88:91], v[152:155]\n"
      "ds_read_b64_tr_b16 v[120:121], v34 offset:28672\n"
      "v_permlane32_swap_b32 v225, v229\n"
      "v_mfma_f32_16x16x32_bf16 v[168:171], a[200:203], v[72:75], v[168:171]\n"
      "global_load_lds_dwordx4 v43, s[4:5] offset:0 sc1\n"
      "ds_read_b64_tr_b16 v[122:123], v34 offset:30720\n"
      "v_add_u32_dpp v49, v49, v49 quad_perm:[1,0,3,2] row_mask:0xf bank_mask:0xf\n"
      "v_permlane32_swap_b32 v226, v230\n"
      "v_mfma_f32_16x16x32_bf16 v[168:171], a[212:215], v[88:91], v[168:171]\n"
      "ds_read_b64_tr_b16 v[124:125], v35 offset:28672\n"
      "v_permlane32_swap_b32 v227, v231\n"
      "v_mfma_f32_16x16x32_bf16 v[140:143], a[192:195], v[76:79], v[140:143]\n"
      "ds_read_b64_tr_b16 v[126:127], v35 offset:30720\n"
      "v_add_u32_dpp v49, v49, v49 quad_perm:[2,3,0,1] row_mask:0xf bank_mask:0xf\n"
      "v_add_f32 v224, v224, v228\n"
      "v_mfma_f32_16x16x32_bf16 v[140:143], a[204:207], v[92:95], v[140:143]\n"
      "v_add_f32 v225, v225, v229\n"
      "v_add_f32 v226, v226, v230\n"
      "v_mfma_f32_16x16x32_bf16 v[156:159], a[196:199], v[76:79], v[156:159]\n"
      "global_load_dwordx4 a[252:255], v44, s[6:7] offset:-1024 sc1\n"
      "v_add_u32_dpp v49, v49, v49 row_half_mirror row_mask:0xf bank_mask:0xf\n"
      "v_cmp_ne_u32 vcc, s71, v49\n"
      "v_mfma_f32_16x16x32_bf16 v[156:159], a[208:211], v[92:95], v[156:159]\n"
      "global_load_lds_dwordx4 v43, s[4:5] offset:1024 sc1\n"
      "v_add_f32 v227, v227, v231\n"
      "v_mfma_f32_16x16x32_bf16 v[172:175], a[200:203], v[76:79], v[172:175]\n"
      "ds_write_b128 v51, v[224:227]\n"
      "s_cbranch_vccnz L_sx6_%=\n"
      "L_bx6_%=:\n"
      "v_mfma_f32_16x16x32_bf16 v[172:175], a[212:215], v[92:95], v[172:175]\n"
      "s_waitcnt vmcnt(14)\n"
      "s_waitcnt lgkmcnt(1)\n"
      "s_barrier\n"
      "v_mfma_f32_16x16x32_bf16 v[128:131], a[216:219], v[96:99], v[128:131]\n"
      "global_load_dwordx4 a[0:3], v44, s[6:7] offset:0 sc1\n"
      "v_mfma_f32_16x16x32_bf16 v[128:131], a[228:231], v[112:115], v[128:131]\n"
      "global_load_lds_dwordx4 v43, s[4:5] offset:2048 sc1\n"
      "v_mfma_f32_16x16x32_bf16 v[144:147], a[220:223], v[96:99], v[144:147]\n"
      "ds_read_b64_tr_b16 v[64:65], v6 offset:32768\n"
      "s_waitcnt vmcnt(8)\n"
      "v_add3_u32 v10, v10, v14, v18\n"
      "v_mfma_f32_16x16x32_bf16 v[144:147], a[232:235], v[112:115], v[144:147]\n"
      "ds_read_b64_tr_b16 v[66:67], v6 offset:34816\n"
      "v_add_u32 v10, v10, v22\n"
      "v_cmp_ne_u32 vcc, s71, v10\n"
      "v_mfma_f32_16x16x32_bf16 v[160:163], a[224:227], v[96:99], v[160:163]\n"
      "global_load_dwordx4 a[4:7], v44, s[6:7] offset:1024 sc1\n"
      "ds_read_b64_tr_b16 v[68:69], v34 offset:32768\n"
      "v_mfma_f32_16x16x32_bf16 v[160:163], a[236:239], v[112:115], v[160:163]\n"
      "global_load_lds_dwordx4 v43, s[4:5] offset:3072 sc1\n"
      "ds_read_b64_tr_b16 v[70:71], v34 offset:34816\n"
      "s_cbranch_vccnz L_sg_%=\n"
      "L_bg_%=:\n"
      "v_add_f32 v8, v8, v12\n"
      "v_add_f32 v9, v9, v13\n"
      "v_mfma_f32_16x16x32_bf16 v[132:135], a[216:219], v[100:103], v[132:135]\n"
      "ds_read_b64_tr_b16 v[72:73], v33 offset:32768\n"
      "v_add_f32 v16, v16, v20\n"
      "v_add_f32 v17, v17, v21\n"
      "v_mfma_f32_16x16x32_bf16 v[132:135], a[228:231], v[116:119], v[132:135]\n"
      "ds_read_b64_tr_b16 v[74:75], v33 offset:34816\n"
      "v_add_f32 v8, v8, v16\n"
      "v_add_u32 v245, v245, v247\n"
      "v_mfma_f32_16x16x32_bf16 v[148:151], a[220:223], v[100:103], v[148:151]\n"
      "global_load_dwordx4 a[8:11], v44, s[6:7] offset:2048 sc1\n"
      "ds_read_b64_tr_b16 v[76:77], v35 offset:32768\n"
      "v_add_f32 v9, v9, v17\n"
      "v_mov_b32 v10, v8\n"
      "v_cmp_ne_u32 vcc, s54, v245\n"
      "v_mfma_f32_16x16x32_bf16 v[148:151], a[232:235], v[116:119], v[148:151]\n"
      "ds_read_b64_tr_b16 v[78:79], v35 offset:34816\n"
      "v_mov_b32 v11, v9\n"
      "s_nop 0\n"
      "v_mfma_f32_16x16x32_bf16 v[164:167], a[224:227], v[100:103], v[164:167]\n"
      "ds_read_b64_tr_b16 v[80:81], v6 offset:36864\n"
      "v_permlane32_swap_b32 v8, v10\n"
      "v_permlane32_swap_b32 v9, v11\n"
      "s_cbranch_vccnz L_sgb_%=\n"
      "L_bgb_%=:\n"
      "v_add_f32 v8, v8, v10\n"
      "v_mfma_f32_16x16x32_bf16 v[164:167], a[236:239], v[116:119], v[164:167]\n"
      "ds_read_b64_tr_b16 v[82:83], v6 offset:38912\n"
      "v_add_f32_dpp v244, v244, v244 quad_perm:[1,0,3,2] row_mask:0xf bank_mask:0xf\n"
      "v_add_f32_dpp v246, v246, v246 quad_perm:[1,0,3,2] row_mask:0xf bank_mask:0xf\n"
      "v_mfma_f32_16x16x32_bf16 v[136:139], a[216:219], v[104:107], v[136:139]\n"
      "global_load_dwordx4 a[12:15], v44, s[6:7] offset:3072 sc1\n"
      "v_add_f32 v9, v9, v11\n"
      "v_mul_f32 v8, 0x3acccccd, v8\n"
      "v_add_f32_dpp v244, v244, v244 quad_perm:[2,3,0,1] row_mask:0xf bank_mask:0xf\n"
      "v_add_f32_dpp v246, v246, v246 quad_perm:[2,3,0,1] row_mask:0xf bank_mask:0xf\n"
      "v_mfma_f32_16x16x32_bf16 v[136:139], a[228:231], v[120:123], v[136:139]\n"
      "ds_read_b64_tr_b16 v[84:85], v34 offset:36864\n"
      "v_mul_f32 v9, 0x3acccccd, v9\n"
      "v_fma_f32 v9, -v8, v8, v9\n"
      "v_mfma_f32_16x16x32_bf16 v[152:155], a[220:223], v[104:107], v[152:155]\n"
      "ds_read_b64_tr_b16 v[86:87], v34 offset:38912\n"
      "v_max_f32 v9, 0, v9\n"
      "s_add_u32 m0, s73, 16384\n"
      "v_add_u32 v43, 147456, v40\n"
      "v_cvt_pk_bf16_f32 v244, 0, v244\n"
      "v_cvt_pk_bf16_f32 v245, 0, v246\n"
      "v_mfma_f32_16x16x32_bf16 v[152:155], a[232:235], v[120:123], v[152:155]\n"
      "global_load_lds_dwordx4 v43, s[4:5] offset:0 sc1\n"
      "ds_read_b64_tr_b16 v[88:89], v33 offset:36864\n"
      "ds_write_b64 v59, v[244:245]\n"
      "v_cvt_pk_bf16_f32 v9, 0, v9\n"
      "v_add_f32 v9, 0x3727c5ac, v9\n"
      "v_add_u32 v44, 12288, v7\n"
      "v_mfma_f32_16x16x32_bf16 v[168:171], a[224:227], v[104:107], v[168:171]\n"
      "global_load_dwordx4 a[16:19], v44, s[6:7] offset:-4096 sc1\n"
      "ds_read_b64_tr_b16 v[90:91], v33 offset:38912\n"
      "ds_read_b128 v[224:227], v52 offset:0\n"
      "v_cvt_pk_bf16_f32 v9, 0, v9\n"
      "v_mfma_f32_16x16x32_bf16 v[168:171], a[236:239], v[120:123], v[168:171]\n"
      "ds_read_b64_tr_b16 v[92:93], v35 offset:36864\n"
      "ds_read_b128 v[228:231], v52 offset:128\n"
      "v_sqrt_f32 v9, v9\n"
      "v_sub_f32 v8, 0, v8\n"
      "v_cvt_pk_bf16_f32 v9, 0, v9\n"
      "v_mfma_f32_16x16x32_bf16 v[140:143], a[216:219], v[108:111], v[140:143]\n"
      "ds_read_b128 v[232:235], v52 offset:256\n"
      "v_rcp_f32 v9, v9\n"
      "v_cvt_pk_bf16_f32 v8, 0, v8\n"
      "v_mfma_f32_16x16x32_bf16 v[140:143], a[228:231], v[124:127], v[140:143]\n"
      "global_load_lds_dwordx4 v43, s[4:5] offset:1024 sc1\n"
      "ds_write_b64 v32, v[8:9]\n"
      "ds_read_b64_tr_b16 v[94:95], v35 offset:38912\n"
      "v_mfma_f32_16x16x32_bf16 v[156:159], a[220:223], v[108:111], v[156:159]\n"
      "global_load_dwordx4 a[20:23], v44, s[6:7] offset:-3072 sc1\n"
      "ds_read_b128 v[236:239], v52 offset:64\n"
      "v_mfma_f32_16x16x32_bf16 v[156:159], a[232:235], v[124:127], v[156:159]\n"
      "ds_read_b128 v[240:243], v52 offset:192\n"
      "v_mfma_f32_16x16x32_bf16 v[172:175], a[224:227], v[108:111], v[172:175]\n"
      "ds_read_b128 v[244:247], v52 offset:320\n"
      "v_mfma_f32_16x16x32_bf16 v[172:175], a[236:239], v[124:127], v[172:175]\n"
      "global_load_lds_dwordx4 v43, s[4:5] offset:2048 sc1\n"
      "s_waitcnt lgkmcnt(3)\n"
      "s_barrier\n"
      "v_mfma_f32_16x16x32_bf16 v[176:179], a[48:51], v[64:67], v[176:179]\n"
      "global_load_dwordx4 a[24:27], v44, s[6:7] offset:-2048 sc1\n"
      "ds_read_b64 v[16:17], v41 offset:0\n"
      "v_mfma_f32_16x16x32_bf16 v[176:179], a[60:63], v[80:83], v[176:179]\n"
      "ds_read_b64 v[18:19], v41 offset:128\n"
      "v_mfma_f32_16x16x32_bf16 v[184:187], a[52:55], v[64:67], v[184:187]\n"
      "ds_read_b64 v[20:21], v41 offset:256\n"
      "ds_read_b64_tr_b16 v[96:97], v6 offset:40960\n"
      "v_mfma_f32_16x16x32_bf16 v[184:187], a[64:67], v[80:83], v[184:187]\n"
      "global_load_lds_dwordx4 v43, s[4:5] offset:3072 sc1\n"
      "ds_read_b128 v[248:251], v40 offset:49152\n"
      "ds_read_b64 v[22:23], v41 offset:384\n"
      "ds_read_b64_tr_b16 v[98:99], v6 offset:43008\n"
      "v_mfma_f32_16x16x32_bf16 v[192:195], a[56:59], v[64:67], v[192:195]\n"
      "global_load_dwordx4 a[28:31], v44, s[6:7] offset:-1024 sc1\n"
      "ds_read_b128 v[252:255], v40 offset:50176\n"
      "ds_read_b64_tr_b16 v[100:101], v34 offset:40960\n"
      "v_mfma_f32_16x16x32_bf16 v[192:195], a[68:71], v[80:83], v[192:195]\n"
      "ds_read_b128 v[36:39], v40 offset:51200\n"
      "ds_read_b64_tr_b16 v[102:103], v34 offset:43008\n"
      "v_mfma_f32_16x16x32_bf16 v[180:183], a[48:51], v[68:71], v[180:183]\n"
      "ds_read_b128 v[60:63], v40 offset:52224\n"
      "ds_read_b64_tr_b16 v[104:105], v33 offset:40960\n"
      "s_waitcnt lgkmcnt(7)\n"
      "v_mfma_f32_16x16x32_bf16 v[180:183], a[60:63], v[84:87], v[180:183]\n"
      "ds_read_b64_tr_b16 v[106:107], v33 offset:43008\n"
      "v_fmac_f32 v128, v16, v224\n"
      "v_fmac_f32 v129, v16, v225\n"
      "v_mfma_f32_16x16x32_bf16 v[188:191], a[52:55], v[68:71], v[188:191]\n"
      "global_load_dwordx4 a[32:35], v44, s[6:7] offset:0 sc1\n"
      "ds_read_b64_tr_b16 v[108:109], v35 offset:40960\n"
      "v_fmac_f32 v130, v16, v226\n"
      "v_fmac_f32 v131, v16, v227\n"
      "v_mfma_f32_16x16x32_bf16 v[188:191], a[64:67], v[84:87], v[188:191]\n"
      "ds_read_b64_tr_b16 v[110:111], v35 offset:43008\n"
      "v_fma_f32 v128, v128, v17, v236\n"
      "v_fma_f32 v129, v129, v17, v237\n"
      "v_fma_f32 v130, v130, v17, v238\n"
      "v_mfma_f32_16x16x32_bf16 v[196:199], a[56:59], v[68:71], v[196:199]\n"
      "ds_read_b64_tr_b16 v[112:113], v6 offset:45056\n"
      "s_waitcnt lgkmcnt(5)\n"
      "v_add3_u32 v55, v55, v248, v249\n"
      "v_add3_u32 v55, v55, v250, v251\n"
      "v_fma_f32 v131, v131, v17, v239\n"
      "v_mul_f32 v128, v128, v130\n"
      "v_mfma_f32_16x16x32_bf16 v[196:199], a[68:71], v[84:87], v[196:199]\n"
      "ds_read_b64_tr_b16 v[114:115], v6 offset:47104\n"
      "v_add3_u32 v55, v55, v252, v253\n"
      "v_add3_u32 v55, v55, v254, v255\n"
      "v_mul_f32 v129, v129, v131\n"
      "v_cvt_pk_bf16_f32 v128, v128, v129\n"
      "v_mfma_f32_16x16x32_bf16 v[200:203], a[48:51], v[72:75], v[200:203]\n"
      "global_load_dwordx4 a[36:39], v44, s[6:7] offset:1024 sc1\n"
      "ds_write_b32 v46, v128 offset:0\n"
      "v_add3_u32 v55, v55, v36, v37\n"
      "v_add3_u32 v55, v55, v38, v39\n"
      "v_fmac_f32 v144, v16, v228\n"
      "v_fmac_f32 v145, v16, v229\n"
      "v_mfma_f32_16x16x32_bf16 v[200:203], a[60:63], v[88:91], v[200:203]\n"
      "ds_read_b64_tr_b16 v[116:117], v34 offset:45056\n"
      "v_add3_u32 v55, v55, v60, v61\n"
      "v_fmac_f32 v146, v16, v230\n"
      "v_fmac_f32 v147, v16, v231\n"
      "v_mfma_f32_16x16x32_bf16 v[208:211], a[52:55], v[72:75], v[208:211]\n"
      "ds_read_b64_tr_b16 v[118:119], v34 offset:47104\n"
      "v_add3_u32 v55, v55, v62, v63\n"
      "v_fma_f32 v144, v144, v17, v240\n"
      "v_fma_f32 v145, v145, v17, v241\n"
      "v_mfma_f32_16x16x32_bf16 v[208:211], a[64:67], v[88:91], v[208:211]\n"
      "ds_read_b64_tr_b16 v[120:121], v33 offset:45056\n"
      "v_fma_f32 v146, v146, v17, v242\n"
      "v_fma_f32 v147, v147, v17, v243\n"
      "v_mul_f32 v144, v144, v146\n"
      "v_mfma_f32_16x16x32_bf16 v[216:219], a[56:59], v[72:75], v[216:219]\n"
      "global_load_dwordx4 a[40:43], v44, s[6:7] offset:2048 sc1\n"
      "ds_read_b64_tr_b16 v[122:123], v33 offset:47104\n"
      "v_add_u32_dpp v55, v55, v55 quad_perm:[1,0,3,2] row_mask:0xf bank_mask:0xf\n"
      "v_mul_f32 v145, v145, v147\n"
      "v_cvt_pk_bf16_f32 v144, v144, v145\n"
      "v_mfma_f32_16x16x32_bf16 v[216:219], a[68:71], v[88:91], v[216:219]\n"
      "ds_write_b32 v46, v144 offset:4096\n"
      "ds_read_b64_tr_b16 v[124:125], v35 offset:45056\n"
      "v_fmac_f32 v160, v16, v232\n"
      "v_mfma_f32_16x16x32_bf16 v[204:207], a[48:51], v[76:79], v[204:207]\n"
      "ds_read_b64_tr_b16 v[126:127], v35 offset:47104\n"
      "v_add_u32_dpp v55, v55, v55 quad_perm:[2,3,0,1] row_mask:0xf bank_mask:0xf\n"
      "v_fmac_f32 v161, v16, v233\n"
      "v_fmac_f32 v162, v16, v234\n"
      "v_fmac_f32 v163, v16, v235\n"
      "v_mfma_f32_16x16x32_bf16 v[204:207], a[60:63], v[92:95], v[204:207]\n"
      "v_fma_f32 v160, v160, v17, v244\n"
      "v_fma_f32 v161, v161, v17, v245\n"
      "v_mfma_f32_16x16x32_bf16 v[212:215], a[52:55], v[76:79], v[212:215]\n"
      "global_load_dwordx4 a[44:47], v44, s[6:7] offset:3072 sc1\n"
      "v_add_u32_dpp v55, v55, v55 row_half_mirror row_mask:0xf bank_mask:0xf\n"
      "v_cmp_ne_u32 vcc, s71, v55\n"
      "v_fma_f32 v162, v162, v17, v246\n"
      "v_fma_f32 v163, v163, v17, v247\n"
      "v_mul_f32 v160, v160, v162\n"
      "v_mfma_f32_16x16x32_bf16 v[212:215], a[64:67], v[92:95], v[212:215]\n"
      "v_mul_f32 v161, v161, v163\n"
      "v_cvt_pk_bf16_f32 v160, v160, v161\n"
      "v_mfma_f32_16x16x32_bf16 v[220:223], a[56:59], v[76:79], v[220:223]\n"
      "ds_write_b32 v46, v160 offset:8192\n"
      "s_cbranch_vccnz L_sx7_%=\n"
      "L_bx7_%=:\n"
      "v_fmac_f32 v132, v18, v224\n"
      "v_mfma_f32_16x16x32_bf16 v[220:223], a[68:71], v[92:95], v[220:223]\n"
      "v_fmac_f32 v133, v18, v225\n"
      "v_fmac_f32 v134, v18, v226\n"
      "v_fmac_f32 v135, v18, v227\n"
      "s_waitcnt lgkmcnt(1)\n"
      "s_barrier\n"
      "v_mfma_f32_16x16x32_bf16 v[176:179], a[72:75], v[96:99], v[176:179]\n"
      "ds_read_b64_tr_b16 v[64:65], v6 offset:49152\n"
      "v_fma_f32 v132, v132, v19, v236\n"
      "v_fma_f32 v133, v133, v19, v237\n"
      "v_mfma_f32_16x16x32_bf16 v[176:179], a[84:87], v[112:115], v[176:179]\n"
      "ds_read_b64_tr_b16 v[66:67], v6 offset:51200\n"
      "v_fma_f32 v134, v134, v19, v238\n"
      "v_fma_f32 v135, v135, v19, v239\n"
      "v_add_u32 v44, 20480, v7\n"
      "v_mfma_f32_16x16x32_bf16 v[184:187], a[76:79], v[96:99], v[184:187]\n"
      "global_load_dwordx4 a[48:51], v44, s[6:7] offset:-4096 sc1\n"
      "v_mul_f32 v132, v132, v134\n"
      "v_mul_f32 v133, v133, v135\n"
      "v_cvt_pk_bf16_f32 v132, v132, v133\n"
      "v_mfma_f32_16x16x32_bf16 v[184:187], a[88:91], v[112:115], v[184:187]\n"
      "ds_write_b32 v46, v132 offset:1024\n"
      "ds_read_b64_tr_b16 v[68:69], v34 offset:49152\n"
      "v_fmac_f32 v148, v18, v228\n"
      "v_mfma_f32_16x16x32_bf16 v[192:195], a[80:83], v[96:99], v[192:195]\n"
      "global_load_dwordx4 a[52:55], v44, s[6:7] offset:-3072 sc1\n"
      "s_waitcnt vmcnt(16)\n"
      "v_fmac_f32 v149, v18, v229\n"
      "v_fmac_f32 v150, v18, v230\n"
      "v_mfma_f32_16x16x32_bf16 v[192:195], a[92:95], v[112:115], v[192:195]\n"
      "ds_read_b128 v[248:251], v40 offset:0\n"
      "ds_read_b64_tr_b16 v[70:71], v34 offset:51200\n"
      "v_fmac_f32 v151, v18, v231\n"
      "v_fma_f32 v148, v148, v19, v240\n"
      "v_fma_f32 v149, v149, v19, v241\n"
      "v_mfma_f32_16x16x32_bf16 v[180:183], a[72:75], v[100:103], v[180:183]\n"
      "ds_read_b128 v[252:255], v40 offset:1024\n"
      "ds_read_b64_tr_b16 v[72:73], v33 offset:49152\n"
      "v_fma_f32 v150, v150, v19, v242\n"
      "v_fma_f32 v151, v151, v19, v243\n"
      "v_mfma_f32_16x16x32_bf16 v[180:183], a[84:87], v[116:119], v[180:183]\n"
      "ds_read_b128 v[36:39], v40 offset:2048\n"
      "ds_read_b64_tr_b16 v[74:75], v33 offset:51200\n"
      "v_mul_f32 v148, v148, v150\n"
      "v_mul_f32 v149, v149, v151\n"
      "v_cvt_pk_bf16_f32 v148, v148, v149\n"
      "v_mfma_f32_16x16x32_bf16 v[188:191], a[76:79], v[100:103], v[188:191]\n"
      "global_load_dwordx4 a[56:59], v44, s[6:7] offset:-2048 sc1\n"
      "ds_read_b128 v[60:63], v40 offset:3072\n"
      "ds_write_b32 v46, v148 offset:5120\n"
      "ds_read_b64_tr_b16 v[76:77], v35 offset:49152\n"
      "v_fmac_f32 v164, v18, v232\n"
      "s_add_u32 m0, s40, 0\n"
      "v_add_u32 v43, 40960, v7\n"
      "v_mfma_f32_16x16x32_bf16 v[188:191], a[88:91], v[116:119], v[188:191]\n"
      "global_load_lds_dwordx4 v43, s[6:7] offset:0 sc1\n"
      "ds_read_b64_tr_b16 v[78:79], v35 offset:51200\n"
      "v_fmac_f32 v165, v18, v233\n"
      "v_fmac_f32 v166, v18, v234\n"
      "v_mfma_f32_16x16x32_bf16 v[196:199], a[80:83], v[100:103], v[196:199]\n"
      "ds_read_b64_tr_b16 v[80:81], v6 offset:53248\n"
      "v_fmac_f32 v167, v18, v235\n"
      "v_fma_f32 v164, v164, v19, v244\n"
      "v_fma_f32 v165, v165, v19, v245\n"
      "v_mfma_f32_16x16x32_bf16 v[196:199], a[92:95], v[116:119], v[196:199]\n"
      "ds_read_b64_tr_b16 v[82:83], v6 offset:55296\n"
      "v_fma_f32 v166, v166, v19, v246\n"
      "v_fma_f32 v167, v167, v19, v247\n"
      "v_mfma_f32_16x16x32_bf16 v[200:203], a[72:75], v[104:107], v[200:203]\n"
      "global_load_dwordx4 a[60:63], v44, s[6:7] offset:-1024 sc1\n"
      "s_waitcnt lgkmcnt(5)\n"
      "v_add3_u32 v57, v57, v248, v249\n"
      "v_add3_u32 v57, v57, v250, v251\n"
      "v_mul_f32 v164, v164, v166\n"
      "v_mul_f32 v165, v165, v167\n"
      "v_cvt_pk_bf16_f32 v164, v164, v165\n"
      "v_mfma_f32_16x16x32_bf16 v[200:203], a[84:87], v[120:123], v[200:203]\n"
      "global_load_lds_dwordx4 v43, s[6:7] offset:1024 sc1\n"
      "ds_write_b32 v46, v164 offset:9216\n"
      "ds_read_b64_tr_b16 v[84:85], v34 offset:53248\n"
      "v_add3_u32 v57, v57, v252, v253\n"
      "v_add3_u32 v57, v57, v254, v255\n"
      "v_fmac_f32 v136, v20, v224\n"
      "v_mfma_f32_16x16x32_bf16 v[208:211], a[76:79], v[104:107], v[208:211]\n"
      "ds_read_b64_tr_b16 v[86:87], v34 offset:55296\n"
      "v_add3_u32 v57, v57, v36, v37\n"
      "v_add3_u32 v57, v57, v38, v39\n"
      "v_fmac_f32 v137, v20, v225\n"
      "v_fmac_f32 v138, v20, v226\n"
      "v_mfma_f32_16x16x32_bf16 v[208:211], a[88:91], v[120:123], v[208:211]\n"
      "ds_read_b64_tr_b16 v[88:89], v33 offset:53248\n"
      "v_add3_u32 v57, v57, v60, v61\n"
      "v_fmac_f32 v139, v20, v227\n"
      "v_fma_f32 v136, v136, v21, v236\n"
      "v_mfma_f32_16x16x32_bf16 v[216:219], a[80:83], v[104:107], v[216:219]\n"
      "global_load_dwordx4 a[64:67], v44, s[6:7] offset:0 sc1\n"
      "ds_read_b64_tr_b16 v[90:91], v33 offset:55296\n"
      "v_add3_u32 v57, v57, v62, v63\n"
      "v_fma_f32 v137, v137, v21, v237\n"
      "v_fma_f32 v138, v138, v21, v238\n"
      "v_fma_f32 v139, v139, v21, v239\n"
      "v_mfma_f32_16x16x32_bf16 v[216:219], a[92:95], v[120:123], v[216:219]\n"
      "global_load_lds_dwordx4 v43, s[6:7] offset:2048 sc1\n"
      "ds_read_b64_tr_b16 v[92:93], v35 offset:53248\n"
      "v_mul_f32 v136, v136, v138\n"
      "v_mul_f32 v137, v137, v139\n"
      "v_cvt_pk_bf16_f32 v136, v136, v137\n"
      "v_mfma_f32_16x16x32_bf16 v[204:207], a[72:75], v[108:111], v[204:207]\n"
      "ds_write_b32 v46, v136 offset:2048\n"
      "ds_read_b64_tr_b16 v[94:95], v35 offset:55296\n"
      "v_add_u32_dpp v57, v57, v57 quad_perm:[1,0,3,2] row_mask:0xf bank_mask:0xf\n"
      "v_mfma_f32_16x16x32_bf16 v[204:207], a[84:87], v[124:127], v[204:207]\n"
      "v_fmac_f32 v152, v20, v228\n"
      "v_fmac_f32 v153, v20, v229\n"
      "v_fmac_f32 v154, v20, v230\n"
      "v_mfma_f32_16x16x32_bf16 v[212:215], a[76:79], v[108:111], v[212:215]\n"
      "global_load_dwordx4 a[68:71], v44, s[6:7] offset:1024 sc1\n"
      "v_add_u32_dpp v57, v57, v57 quad_perm:[2,3,0,1] row_mask:0xf bank_mask:0xf\n"
      "v_fmac_f32 v155, v20, v231\n"
      "v_fma_f32 v152, v152, v21, v240\n"
      "v_mfma_f32_16x16x32_bf16 v[212:215], a[88:91], v[124:127], v[212:215]\n"
      "global_load_lds_dwordx4 v43, s[6:7] offset:3072 sc1\n"
      "v_fma_f32 v153, v153, v21, v241\n"
      "v_fma_f32 v154, v154, v21, v242\n"
      "v_fma_f32 v155, v155, v21, v243\n"
      "v_mfma_f32_16x16x32_bf16 v[220:223], a[80:83], v[108:111], v[220:223]\n"
      "v_add_u32_dpp v57, v57, v57 row_half_mirror row_mask:0xf bank_mask:0xf\n"
      "v_cmp_ne_u32 vcc, s71, v57\n"
      "v_mul_f32 v152, v152, v154\n"
      "v_mul_f32 v153, v153, v155\n"
      "v_cvt_pk_bf16_f32 v152, v152, v153\n"
      "v_mfma_f32_16x16x32_bf16 v[220:223], a[92:95], v[124:127], v[220:223]\n"
      "ds_write_b32 v46, v152 offset:6144\n"
      "L_pg5_%=:\n"
      "s_cbranch_execz L_pgr_%=\n"
      "v_fmac_f32 v168, v20, v232\n"
      "s_waitcnt lgkmcnt(1)\n"
      "v_mfma_f32_16x16x32_bf16 v[176:179], a[96:99], v[64:67], v[176:179]\n"
      "ds_read_b64_tr_b16 v[96:97], v6 offset:57344\n"
      "s_cbranch_vccnz L_sx8_%=\n"
      "L_bx8_%=:\n"
      "v_fmac_f32 v169, v20, v233\n"
      "v_fmac_f32 v170, v20, v234\n"
      "s_add_u32 m0, s40, 4096\n"
      "v_add_u32 v43, 45056, v7\n"
      "v_mfma_f32_16x16x32_bf16 v[176:179], a[108:111], v[80:83], v[176:179]\n"
      "global_load_lds_dwordx4 v43, s[6:7] offset:0 sc1\n"
      "v_fmac_f32 v171, v20, v235\n"
      "v_fma_f32 v168, v168, v21, v244\n"
      "v_mfma_f32_16x16x32_bf16 v[184:187], a[100:103], v[64:67], v[184:187]\n"
      "global_load_dwordx4 a[72:75], v44, s[6:7] offset:2048 sc1\n"
      "s_waitcnt vmcnt(17)\n"
      "v_fma_f32 v169, v169, v21, v245\n"
      "v_fma_f32 v170, v170, v21, v246\n"
      "v_mfma_f32_16x16x32_bf16 v[184:187], a[112:115], v[80:83], v[184:187]\n"
      "ds_read_b128 v[248:251], v40 offset:16384\n"
      "ds_read_b64_tr_b16 v[98:99], v6 offset:59392\n"
      "v_fma_f32 v171, v171, v21, v247\n"
      "v_mul_f32 v168, v168, v170\n"
      "v_mul_f32 v169, v169, v171\n"
      "v_cvt_pk_bf16_f32 v168, v168, v169\n"
      "v_mfma_f32_16x16x32_bf16 v[192:195], a[104:107], v[64:67], v[192:195]\n"
      "global_load_dwordx4 a[76:79], v44, s[6:7] offset:3072 sc1\n"
      "ds_read_b128 v[252:255], v40 offset:17408\n"
      "ds_write_b32 v46, v168 offset:10240\n"
      "ds_read_b64_tr_b16 v[100:101], v34 offset:57344\n"
      "v_fmac_f32 v140, v22, v224\n"
      "v_mfma_f32_16x16x32_bf16 v[192:195], a[116:119], v[80:83], v[192:195]\n"
      "global_load_lds_dwordx4 v43, s[6:7] offset:1024 sc1\n"
      "ds_read_b128 v[36:39], v40 offset:18432\n"
      "ds_read_b64_tr_b16 v[102:103], v34 offset:59392\n"
      "v_fmac_f32 v141, v22, v225\n"
      "v_mfma_f32_16x16x32_bf16 v[180:183], a[96:99], v[68:71], v[180:183]\n"
      "ds_read_b128 v[60:63], v40 offset:19456\n"
      "ds_read_b64_tr_b16 v[104:105], v33 offset:57344\n"
      "v_fmac_f32 v142, v22, v226\n"
      "v_fmac_f32 v143, v22, v227\n"
      "v_fma_f32 v140, v140, v23, v236\n"
      "v_mfma_f32_16x16x32_bf16 v[180:183], a[108:111], v[84:87], v[180:183]\n"
      "ds_read_b64_tr_b16 v[106:107], v33 offset:59392\n"
      "v_fma_f32 v141, v141, v23, v237\n"
      "v_fma_f32 v142, v142, v23, v238\n"
      "v_add_u32 v44, 28672, v7\n"
      "v_mfma_f32_16x16x32_bf16 v[188:191], a[100:103], v[68:71], v[188:191]\n"
      "global_load_dwordx4 a[80:83], v44, s[6:7] offset:-4096 sc1\n"
      "ds_read_b64_tr_b16 v[108:109], v35 offset:57344\n"
      "v_fma_f32 v143, v143, v23, v239\n"
      "v_mul_f32 v140, v140, v142\n"
      "v_mul_f32 v141, v141, v143\n"
      "v_cvt_pk_bf16_f32 v140, v140, v141\n"
      "v_mfma_f32_16x16x32_bf16 v[188:191], a[112:115], v[84:87], v[188:191]\n"
      "global_load_lds_dwordx4 v43, s[6:7] offset:2048 sc1\n"
      "ds_write_b32 v46, v140 offset:3072\n"
      "ds_read_b64_tr_b16 v[110:111], v35 offset:59392\n"
      "v_fmac_f32 v156, v22, v228\n"
      "v_mfma_f32_16x16x32_bf16 v[196:199], a[104:107], v[68:71], v[196:199]\n"
      "ds_read_b64_tr_b16 v[112:113], v6 offset:61440\n"
      "s_waitcnt lgkmcnt(6)\n"
      "v_add3_u32 v3, v3, v248, v249\n"
      "v_add3_u32 v3, v3, v250, v251\n"
      "v_fmac_f32 v157, v22, v229\n"
      "v_mfma_f32_16x16x32_bf16 v[196:199], a[116:119], v[84:87], v[196:199]\n"
      "ds_read_b64_tr_b16 v[114:115], v6 offset:63488\n"
      "v_add3_u32 v3, v3, v252, v253\n"
      "v_add3_u32 v3, v3, v254, v255\n"
      "v_fmac_f32 v158, v22, v230\n"
      "v_fmac_f32 v159, v22, v231\n"
      "v_fma_f32 v156, v156, v23, v240\n"
      "v_mfma_f32_16x16x32_bf16 v[200:203], a[96:99], v[72:75], v[200:203]\n"
      "global_load_dwordx4 a[84:87], v44, s[6:7] offset:-3072 sc1\n"
      "v_add3_u32 v3, v3, v36, v37\n"
      "v_add3_u32 v3, v3, v38, v39\n"
      "v_fma_f32 v157, v157, v23, v241\n"
      "v_fma_f32 v158, v158, v23, v242\n"
      "v_mfma_f32_16x16x32_bf16 v[200:203], a[108:111], v[88:91], v[200:203]\n"
      "global_load_lds_dwordx4 v43, s[6:7] offset:3072 sc1\n"
      "ds_read_b64_tr_b16 v[116:117], v34 offset:61440\n"
      "v_add3_u32 v3, v3, v60, v61\n"
      "v_fma_f32 v159, v159, v23, v243\n"
      "v_mul_f32 v156, v156, v158\n"
      "v_mul_f32 v157, v157, v159\n"
      "v_cvt_pk_bf16_f32 v156, v156, v157\n"
      "v_mfma_f32_16x16x32_bf16 v[208:211], a[100:103], v[72:75], v[208:211]\n"
      "ds_write_b32 v46, v156 offset:7168\n"
      "ds_read_b64_tr_b16 v[118:119], v34 offset:63488\n"
      "v_add3_u32 v3, v3, v62, v63\n"
      "v_mfma_f32_16x16x32_bf16 v[208:211], a[112:115], v[88:91], v[208:211]\n"
      "ds_read_b64_tr_b16 v[120:121], v33 offset:61440\n"
      "v_fmac_f32 v172, v22, v232\n"
      "v_fmac_f32 v173, v22, v233\n"
      "v_mfma_f32_16x16x32_bf16 v[216:219], a[104:107], v[72:75], v[216:219]\n"
      "global_load_dwordx4 a[88:91], v44, s[6:7] offset:-2048 sc1\n"
      "ds_read_b64_tr_b16 v[122:123], v33 offset:63488\n"
      "v_add_u32_dpp v3, v3, v3 quad_perm:[1,0,3,2] row_mask:0xf bank_mask:0xf\n"
      "v_fmac_f32 v174, v22, v234\n"
      "v_fmac_f32 v175, v22, v235\n"
      "s_add_u32 m0, s40, 8192\n"
      "v_add_u32 v43, 49152, v7\n"
      "v_mfma_f32_16x16x32_bf16 v[216:219], a[116:119], v[88:91], v[216:219]\n"
      "global_load_lds_dwordx4 v43, s[6:7] offset:0 sc1\n"
      "ds_read_b64_tr_b16 v[124:125], v35 offset:61440\n"
      "v_fma_f32 v172, v172, v23, v244\n"
      "v_fma_f32 v173, v173, v23, v245\n"
      "v_fma_f32 v174, v174, v23, v246\n"
      "v_mfma_f32_16x16x32_bf16 v[204:207], a[96:99], v[76:79], v[204:207]\n"
      "ds_read_b64_tr_b16 v[126:127], v35 offset:63488\n"
      "v_add_u32_dpp v3, v3, v3 quad_perm:[2,3,0,1] row_mask:0xf bank_mask:0xf\n"
      "v_fma_f32 v175, v175, v23, v247\n"
      "v_mul_f32 v172, v172, v174\n"
      "v_mfma_f32_16x16x32_bf16 v[204:207], a[108:111], v[92:95], v[204:207]\n"
      "v_mul_f32 v173, v173, v175\n"
      "v_cvt_pk_bf16_f32 v172, v172, v173\n"
      "v_mfma_f32_16x16x32_bf16 v[212:215], a[100:103], v[76:79], v[212:215]\n"
      "global_load_dwordx4 a[92:95], v44, s[6:7] offset:-1024 sc1\n"
      "ds_write_b32 v46, v172 offset:11264\n"
      "v_add_u32_dpp v3, v3, v3 row_half_mirror row_mask:0xf bank_mask:0xf\n"
      "v_cmp_ne_u32 vcc, s71, v3\n"
      "v_mfma_f32_16x16x32_bf16 v[212:215], a[112:115], v[92:95], v[212:215]\n"
      "v_mfma_f32_16x16x32_bf16 v[220:223], a[104:107], v[76:79], v[220:223]\n"
      "s_cbranch_vccnz L_sx9_%=\n"
      "L_bx9_%=:\n"
      "v_mfma_f32_16x16x32_bf16 v[220:223], a[116:119], v[92:95], v[220:223]\n"
      "s_waitcnt lgkmcnt(0)\n"
      "s_barrier\n"
      "v_mfma_f32_16x16x32_bf16 v[176:179], a[120:123], v[96:99], v[176:179]\n"
      "ds_read_b128 v[160:163], v47 offset:0\n"
      "v_add_u32 v57, s40, v7\n"
      "v_mfma_f32_16x16x32_bf16 v[176:179], a[132:135], v[112:115], v[176:179]\n"
      "ds_read_b128 v[164:167], v47 offset:4096\n"
      "v_mfma_f32_16x16x32_bf16 v[184:187], a[124:127], v[96:99], v[184:187]\n"
      "global_load_dwordx4 a[96:99], v44, s[6:7] offset:0 sc1\n"
      "ds_read_b128 v[168:171], v47 offset:8192\n"
      "ds_read_b64_tr_b16 v[64:65], v6 offset:0\n"
      "v_mfma_f32_16x16x32_bf16 v[184:187], a[136:139], v[112:115], v[184:187]\n"
      "ds_read_b64_tr_b16 v[66:67], v6 offset:2048\n"
      "v_mfma_f32_16x16x32_bf16 v[192:195], a[128:131], v[96:99], v[192:195]\n"
      "global_load_dwordx4 a[100:103], v44, s[6:7] offset:1024 sc1\n"
      "ds_read_b64_tr_b16 v[68:69], v34 offset:0\n"
      "v_mfma_f32_16x16x32_bf16 v[192:195], a[140:143], v[112:115], v[192:195]\n"
      "ds_read_b64_tr_b16 v[70:71], v34 offset:2048\n"
      "s_waitcnt lgkmcnt(4)\n"
      "v_mfma_f32_16x16x32_bf16 v[180:183], a[120:123], v[100:103], v[180:183]\n"
      "ds_read_b64_tr_b16 v[72:73], v6 offset:4096\n"
      "v_mfma_f32_16x16x32_bf16 v[180:183], a[132:135], v[116:119], v[180:183]\n"
      "buffer_store_dwordx4 v[160:163], v7, s[32:35], s61 offen offset:0 sc1\n"
      "ds_read_b64_tr_b16 v[74:75], v6 offset:6144\n"
      "v_add3_u32 v58, v160, v161, s30\n"
      "v_mfma_f32_16x16x32_bf16 v[188:191], a[124:127], v[100:103], v[188:191]\n"
      "global_load_dwordx4 a[104:107], v44, s[6:7] offset:2048 sc1\n"
      "ds_read_b64_tr_b16 v[76:77], v34 offset:4096\n"
      "v_add3_u32 v58, v58, v162, v163\n"
      "v_add3_u32 v58, v58, v164, v165\n"
      "s_add_u32 m0, s41, 0\n"
      "v_add_u32 v43, 50176, v7\n"
      "v_mfma_f32_16x16x32_bf16 v[188:191], a[136:139], v[116:119], v[188:191]\n"
      "buffer_store_dwordx4 v[164:167], v7, s[32:35], s61 offen offset:1024 sc1\n"
      "global_load_lds_dwordx4 v43, s[6:7] offset:0 sc1\n"
      "ds_read_b64_tr_b16 v[78:79], v34 offset:6144\n"
      "v_add3_u32 v58, v58, v166, v167\n"
      "v_mfma_f32_16x16x32_bf16 v[196:199], a[128:131], v[100:103], v[196:199]\n"
      "ds_read_b64_tr_b16 v[80:81], v6 offset:8192\n"
      "v_add3_u32 v58, v58, v168, v169\n"
      "v_add3_u32 v58, v58, v170, v171\n"
      "v_mfma_f32_16x16x32_bf16 v[196:199], a[140:143], v[116:119], v[196:199]\n"
      "buffer_store_dwordx4 v[168:171], v7, s[32:35], s61 offen offset:2048 sc1\n"
      "v_mfma_f32_16x16x32_bf16 v[200:203], a[120:123], v[104:107], v[200:203]\n"
      "global_load_dwordx4 a[108:111], v44, s[6:7] offset:3072 sc1\n"
      "ds_read_b64_tr_b16 v[82:83], v6 offset:10240\n"
      "v_mfma_f32_16x16x32_bf16 v[200:203], a[132:135], v[120:123], v[200:203]\n"
      "buffer_store_dword v58, v42, s[32:35], s62 offen sc1\n"
      "global_load_lds_dwordx4 v43, s[6:7] offset:1024 sc1\n"
      "ds_read_b64_tr_b16 v[84:85], v34 offset:8192\n"
      "v_mfma_f32_16x16x32_bf16 v[208:211], a[124:127], v[104:107], v[208:211]\n"
      "ds_read_b64_tr_b16 v[86:87], v34 offset:10240\n"
      "v_mfma_f32_16x16x32_bf16 v[208:211], a[136:139], v[120:123], v[208:211]\n"
      "ds_read_b64_tr_b16 v[88:89], v6 offset:12288\n"
      "v_add_u32 v44, 36864, v7\n"
      "v_mfma_f32_16x16x32_bf16 v[216:219], a[128:131], v[104:107], v[216:219]\n"
      "global_load_dwordx4 a[112:115], v44, s[6:7] offset:-4096 sc1\n"
      "ds_read_b64_tr_b16 v[90:91], v6 offset:14336\n"
      "v_mfma_f32_16x16x32_bf16 v[216:219], a[140:143], v[120:123], v[216:219]\n"
      "global_load_lds_dwordx4 v43, s[6:7] offset:2048 sc1\n"
      "ds_read_b64_tr_b16 v[92:93], v34 offset:12288\n"
      "v_mfma_f32_16x16x32_bf16 v[204:207], a[120:123], v[108:111], v[204:207]\n"
      "ds_read_b64_tr_b16 v[94:95], v34 offset:14336\n"
      "v_mfma_f32_16x16x32_bf16 v[204:207], a[132:135], v[124:127], v[204:207]\n"
      "ds_read_b64 v[24:25], v41 offset:512\n"
      "v_mfma_f32_16x16x32_bf16 v[212:215], a[124:127], v[108:111], v[212:215]\n"
      "global_load_dwordx4 a[116:119], v44, s[6:7] offset:-3072 sc1\n"
      "v_mfma_f32_16x16x32_bf16 v[212:215], a[136:139], v[124:127], v[212:215]\n"
      "ds_read_b64 v[26:27], v41 offset:768\n"
      "v_mfma_f32_16x16x32_bf16 v[220:223], a[128:131], v[108:111], v[220:223]\n"
      "ds_read_b64 v[28:29], v41 offset:640\n"
      "v_mfma_f32_16x16x32_bf16 v[220:223], a[140:143], v[124:127], v[220:223]\n"
      "ds_read_b64 v[30:31], v41 offset:896\n"
      "s_waitcnt lgkmcnt(4)\n"
      "v_mfma_f32_16x16x32_bf16 v[176:179], a[144:147], v[64:67], v[176:179]\n"
      "ds_read_b64_tr_b16 v[96:97], v6 offset:16384\n"
      "v_mfma_f32_16x16x32_bf16 v[176:179], a[156:159], v[72:75], v[176:179]\n"
      "ds_read_b64_tr_b16 v[98:99], v6 offset:18432\n"
      "v_mfma_f32_16x16x32_bf16 v[184:187], a[148:151], v[64:67], v[184:187]\n"
      "global_load_dwordx4 a[120:123], v44, s[6:7] offset:-2048 sc1\n"
      "v_mfma_f32_16x16x32_bf16 v[184:187], a[160:163], v[72:75], v[184:187]\n"
      "ds_read_b64_tr_b16 v[100:101], v34 offset:16384\n"
      "v_mfma_f32_16x16x32_bf16 v[192:195], a[152:155], v[64:67], v[192:195]\n"
      "global_load_dwordx4 a[124:127], v44, s[6:7] offset:-1024 sc1\n"
      "v_mfma_f32_16x16x32_bf16 v[192:195], a[164:167], v[72:75], v[192:195]\n"
      "ds_read_b64_tr_b16 v[102:103], v34 offset:18432\n"
      "v_mfma_f32_16x16x32_bf16 v[180:183], a[144:147], v[68:71], v[180:183]\n"
      "ds_read_b64_tr_b16 v[104:105], v6 offset:20480\n"
      "v_mfma_f32_16x16x32_bf16 v[180:183], a[156:159], v[76:79], v[180:183]\n"
      "ds_read_b64_tr_b16 v[106:107], v6 offset:22528\n"
      "v_mfma_f32_16x16x32_bf16 v[188:191], a[148:151], v[68:71], v[188:191]\n"
      "global_load_dwordx4 a[128:131], v44, s[6:7] offset:0 sc1\n"
      "ds_read_b64_tr_b16 v[108:109], v34 offset:20480\n"
      "v_mfma_f32_16x16x32_bf16 v[188:191], a[160:163], v[76:79], v[188:191]\n"
      "ds_read_b64_tr_b16 v[110:111], v34 offset:22528\n"
      "v_mfma_f32_16x16x32_bf16 v[196:199], a[152:155], v[68:71], v[196:199]\n"
      "ds_read_b64_tr_b16 v[112:113], v6 offset:24576\n"
      "v_mfma_f32_16x16x32_bf16 v[196:199], a[164:167], v[76:79], v[196:199]\n"
      "ds_read_b64_tr_b16 v[114:115], v6 offset:26624\n"
      "v_mfma_f32_16x16x32_bf16 v[176:179], a[168:171], v[80:83], v[176:179]\n"
      "global_load_dwordx4 a[132:135], v44, s[6:7] offset:1024 sc1\n"
      "v_mfma_f32_16x16x32_bf16 v[176:179], a[180:183], v[88:91], v[176:179]\n"
      "ds_read_b64_tr_b16 v[116:117], v34 offset:24576\n"
      "v_mfma_f32_16x16x32_bf16 v[184:187], a[172:175], v[80:83], v[184:187]\n"
      "ds_read_b64_tr_b16 v[118:119], v34 offset:26624\n"
      "v_mfma_f32_16x16x32_bf16 v[184:187], a[184:187], v[88:91], v[184:187]\n"
      "ds_read_b64_tr_b16 v[120:121], v6 offset:28672\n"
      "v_mfma_f32_16x16x32_bf16 v[192:195], a[176:179], v[80:83], v[192:195]\n"
      "global_load_dwordx4 a[136:139], v44, s[6:7] offset:2048 sc1\n"
      "ds_read_b64_tr_b16 v[122:123], v6 offset:30720\n"
      "v_mfma_f32_16x16x32_bf16 v[192:195], a[188:191], v[88:91], v[192:195]\n"
      "ds_read_b64_tr_b16 v[124:125], v34 offset:28672\n"
      "v_mfma_f32_16x16x32_bf16 v[180:183], a[168:171], v[84:87], v[180:183]\n"
      "ds_read_b64_tr_b16 v[126:127], v34 offset:30720\n"
      "v_mfma_f32_16x16x32_bf16 v[180:183], a[180:183], v[92:95], v[180:183]\n"
      "v_mfma_f32_16x16x32_bf16 v[188:191], a[172:175], v[84:87], v[188:191]\n"
      "global_load_dwordx4 a[140:143], v44, s[6:7] offset:3072 sc1\n"
      "v_mfma_f32_16x16x32_bf16 v[188:191], a[184:187], v[92:95], v[188:191]\n"
      "v_mfma_f32_16x16x32_bf16 v[196:199], a[176:179], v[84:87], v[196:199]\n"
      "v_mfma_f32_16x16x32_bf16 v[196:199], a[188:191], v[92:95], v[196:199]\n"
      "s_waitcnt lgkmcnt(0)\n"
      "v_mfma_f32_16x16x32_bf16 v[176:179], a[192:195], v[96:99], v[176:179]\n"
      "ds_read_b64_tr_b16 v[64:65], v33 offset:0\n"
      "v_mfma_f32_16x16x32_bf16 v[176:179], a[204:207], v[104:107], v[176:179]\n"
      "ds_read_b64_tr_b16 v[66:67], v33 offset:2048\n"
      "v_mfma_f32_16x16x32_bf16 v[184:187], a[196:199], v[96:99], v[184:187]\n"
      "ds_read_b64_tr_b16 v[68:69], v35 offset:0\n"
      "v_mfma_f32_16x16x32_bf16 v[184:187], a[208:211], v[104:107], v[184:187]\n"
      "ds_read_b64_tr_b16 v[70:71], v35 offset:2048\n"
      "v_mfma_f32_16x16x32_bf16 v[192:195], a[200:203], v[96:99], v[192:195]\n"
      "ds_read_b64_tr_b16 v[72:73], v33 offset:4096\n"
      "v_mfma_f32_16x16x32_bf16 v[192:195], a[212:215], v[104:107], v[192:195]\n"
      "ds_read_b64_tr_b16 v[74:75], v33 offset:6144\n"
      "v_mfma_f32_16x16x32_bf16 v[180:183], a[192:195], v[100:103], v[180:183]\n"
      "ds_read_b64_tr_b16 v[76:77], v35 offset:4096\n"
      "v_mfma_f32_16x16x32_bf16 v[180:183], a[204:207], v[108:111], v[180:183]\n"
      "ds_read_b64_tr_b16 v[78:79], v35 offset:6144\n"
      "v_mfma_f32_16x16x32_bf16 v[188:191], a[196:199], v[100:103], v[188:191]\n"
      "ds_read_b64_tr_b16 v[80:81], v33 offset:8192\n"
      "v_mfma_f32_16x16x32_bf16 v[188:191], a[208:211], v[108:111], v[188:191]\n"
      "ds_read_b64_tr_b16 v[82:83], v33 offset:10240\n"
      "v_mfma_f32_16x16x32_bf16 v[196:199], a[200:203], v[100:103], v[196:199]\n"
      "ds_read_b64_tr_b16 v[84:85], v35 offset:8192\n"
      "v_mfma_f32_16x16x32_bf16 v[196:199], a[212:215], v[108:111], v[196:199]\n"
      "ds_read_b64_tr_b16 v[86:87], v35 offset:10240\n"
      "v_mfma_f32_16x16x32_bf16 v[176:179], a[216:219], v[112:115], v[176:179]\n"
      "ds_read_b64_tr_b16 v[88:89], v33 offset:12288\n"
      "v_mfma_f32_16x16x32_bf16 v[176:179], a[228:231], v[120:123], v[176:179]\n"
      "ds_read_b64_tr_b16 v[90:91], v33 offset:14336\n"
      "v_mfma_f32_16x16x32_bf16 v[184:187], a[220:223], v[112:115], v[184:187]\n"
      "ds_read_b64_tr_b16 v[92:93], v35 offset:12288\n"
      "v_mfma_f32_16x16x32_bf16 v[184:187], a[232:235], v[120:123], v[184:187]\n"
      "ds_read_b64_tr_b16 v[94:95], v35 offset:14336\n"
      "v_mfma_f32_16x16x32_bf16 v[192:195], a[224:227], v[112:115], v[192:195]\n"
      "v_add_u32 v45, 4096, v7\n"
      "v_mfma_f32_16x16x32_bf16 v[192:195], a[236:239], v[120:123], v[192:195]\n"
      "v_mfma_f32_16x16x32_bf16 v[180:183], a[216:219], v[116:119], v[180:183]\n"
      "global_load_dwordx4 v[8:11], v45, s[10:11] offset:-4096 sc1\n"
      "v_mfma_f32_16x16x32_bf16 v[180:183], a[228:231], v[124:127], v[180:183]\n"
      "v_mfma_f32_16x16x32_bf16 v[188:191], a[220:223], v[116:119], v[188:191]\n"
      "v_mfma_f32_16x16x32_bf16 v[188:191], a[232:235], v[124:127], v[188:191]\n"
      "v_mfma_f32_16x16x32_bf16 v[196:199], a[224:227], v[116:119], v[196:199]\n"
      "global_load_dwordx4 v[12:15], v45, s[12:13] offset:-4096 sc1\n"
      "v_mfma_f32_16x16x32_bf16 v[196:199], a[236:239], v[124:127], v[196:199]\n"
      "s_waitcnt lgkmcnt(0)\n"
      "s_barrier\n"
      "v_mfma_f32_16x16x32_bf16 v[200:203], a[144:147], v[64:67], v[200:203]\n"
      "ds_read_b64_tr_b16 v[96:97], v33 offset:16384\n"
      "v_mfma_f32_16x16x32_bf16 v[200:203], a[156:159], v[72:75], v[200:203]\n"
      "global_load_dwordx4 v[16:19], v45, s[10:11] offset:-3072 sc1\n"
      "v_mfma_f32_16x16x32_bf16 v[208:211], a[148:151], v[64:67], v[208:211]\n"
      "ds_read_b64_tr_b16 v[98:99], v33 offset:18432\n"
      "v_mfma_f32_16x16x32_bf16 v[208:211], a[160:163], v[72:75], v[208:211]\n"
      "ds_read_b64_tr_b16 v[100:101], v35 offset:16384\n"
      "v_fmac_f32 v176, v24, v224\n"
      "v_fmac_f32 v177, v24, v225\n"
      "v_mfma_f32_16x16x32_bf16 v[216:219], a[152:155], v[64:67], v[216:219]\n"
      "global_load_dwordx4 v[20:23], v45, s[12:13] offset:-3072 sc1\n"
      "v_fmac_f32 v178, v24, v226\n"
      "v_fmac_f32 v179, v24, v227\n"
      "v_mfma_f32_16x16x32_bf16 v[216:219], a[164:167], v[72:75], v[216:219]\n"
      "ds_read_b64_tr_b16 v[102:103], v35 offset:18432\n"
      "v_fma_f32 v176, v176, v25, v236\n"
      "v_fma_f32 v177, v177, v25, v237\n"
      "v_fma_f32 v178, v178, v25, v238\n"
      "v_mfma_f32_16x16x32_bf16 v[204:207], a[144:147], v[68:71], v[204:207]\n"
      "ds_read_b64_tr_b16 v[104:105], v33 offset:20480\n"
      "v_fma_f32 v179, v179, v25, v239\n"
      "v_mul_f32 v176, v176, v178\n"
      "v_mfma_f32_16x16x32_bf16 v[204:207], a[156:159], v[76:79], v[204:207]\n"
      "global_load_dwordx4 v[128:131], v45, s[10:11] offset:-2048 sc1\n"
      "ds_read_b64_tr_b16 v[106:107], v33 offset:22528\n"
      "v_mul_f32 v177, v177, v179\n"
      "v_cvt_pk_bf16_f32 v176, v176, v177\n"
      "v_mfma_f32_16x16x32_bf16 v[212:215], a[148:151], v[68:71], v[212:215]\n"
      "ds_write_b32 v46, v176 offset:0\n"
      "ds_read_b64_tr_b16 v[108:109], v35 offset:20480\n"
      "v_fmac_f32 v184, v24, v228\n"
      "v_fmac_f32 v185, v24, v229\n"
      "v_mfma_f32_16x16x32_bf16 v[212:215], a[160:163], v[76:79], v[212:215]\n"
      "ds_read_b64_tr_b16 v[110:111], v35 offset:22528\n"
      "v_fmac_f32 v186, v24, v230\n"
      "v_fmac_f32 v187, v24, v231\n"
      "v_mfma_f32_16x16x32_bf16 v[220:223], a[152:155], v[68:71], v[220:223]\n"
      "global_load_dwordx4 v[132:135], v45, s[12:13] offset:-2048 sc1\n"
      "v_fma_f32 v184, v184, v25, v240\n"
      "v_fma_f32 v185, v185, v25, v241\n"
      "v_fma_f32 v186, v186, v25, v242\n"
      "v_mfma_f32_16x16x32_bf16 v[220:223], a[164:167], v[76:79], v[220:223]\n"
      "buffer_load_dword v36, v42, s[32:35], s63 offen offset:0 sc1\n"
      "buffer_load_dword v37, v42, s[32:35], s63 offen offset:256 sc1\n"
      "ds_read_b64_tr_b16 v[112:113], v33 offset:24576\n"
      "v_fma_f32 v187, v187, v25, v243\n"
      "v_mul_f32 v184, v184, v186\n"
      "v_mfma_f32_16x16x32_bf16 v[200:203], a[168:171], v[80:83], v[200:203]\n"
      "ds_read_b64_tr_b16 v[114:115], v33 offset:26624\n"
      "v_mul_f32 v185, v185, v187\n"
      "v_cvt_pk_bf16_f32 v184, v184, v185\n"
      "v_mfma_f32_16x16x32_bf16 v[200:203], a[180:183], v[88:91], v[200:203]\n"
      "global_load_dwordx4 v[136:139], v45, s[10:11] offset:-1024 sc1\n"
      "ds_write_b32 v46, v184 offset:4096\n"
      "ds_read_b64_tr_b16 v[116:117], v35 offset:24576\n"
      "v_fmac_f32 v192, v24, v232\n"
      "v_fmac_f32 v193, v24, v233\n"
      "v_mfma_f32_16x16x32_bf16 v[208:211], a[172:175], v[80:83], v[208:211]\n"
      "ds_read_b64_tr_b16 v[118:119], v35 offset:26624\n"
      "v_fmac_f32 v194, v24, v234\n"
      "v_fmac_f32 v195, v24, v235\n"
      "v_mfma_f32_16x16x32_bf16 v[208:211], a[184:187], v[88:91], v[208:211]\n"
      "ds_read_b64_tr_b16 v[120:121], v33 offset:28672\n"
      "v_fma_f32 v192, v192, v25, v244\n"
      "v_fma_f32 v193, v193, v25, v245\n"
      "v_fma_f32 v194, v194, v25, v246\n"
      "v_mfma_f32_16x16x32_bf16 v[216:219], a[176:179], v[80:83], v[216:219]\n"
      "global_load_dwordx4 v[140:143], v45, s[12:13] offset:-1024 sc1\n"
      "ds_read_b64_tr_b16 v[122:123], v33 offset:30720\n"
      "v_fma_f32 v195, v195, v25, v247\n"
      "v_mul_f32 v192, v192, v194\n"
      "v_mfma_f32_16x16x32_bf16 v[216:219], a[188:191], v[88:91], v[216:219]\n"
      "ds_read_b64_tr_b16 v[124:125], v35 offset:28672\n"
      "v_mul_f32 v193, v193, v195\n"
      "v_cvt_pk_bf16_f32 v192, v192, v193\n"
      "v_mfma_f32_16x16x32_bf16 v[204:207], a[168:171], v[84:87], v[204:207]\n"
      "ds_write_b32 v46, v192 offset:8192\n"
      "v_fmac_f32 v180, v26, v224\n"
      "v_fmac_f32 v181, v26, v225\n"
      "v_mfma_f32_16x16x32_bf16 v[204:207], a[180:183], v[92:95], v[204:207]\n"
      "global_load_dwordx4 v[144:147], v45, s[10:11] offset:0 sc1\n"
      "ds_read_b64_tr_b16 v[126:127], v35 offset:30720\n"
      "v_fmac_f32 v182, v26, v226\n"
      "v_fmac_f32 v183, v26, v227\n"
      "v_mfma_f32_16x16x32_bf16 v[212:215], a[172:175], v[84:87], v[212:215]\n"
      "v_fma_f32 v180, v180, v27, v236\n"
      "v_fma_f32 v181, v181, v27, v237\n"
      "v_fma_f32 v182, v182, v27, v238\n"
      "v_mfma_f32_16x16x32_bf16 v[212:215], a[184:187], v[92:95], v[212:215]\n"
      "v_fma_f32 v183, v183, v27, v239\n"
      "v_mul_f32 v180, v180, v182\n"
      "v_mfma_f32_16x16x32_bf16 v[220:223], a[176:179], v[84:87], v[220:223]\n"
      "global_load_dwordx4 v[148:151], v45, s[12:13] offset:0 sc1\n"
      "v_mul_f32 v181, v181, v183\n"
      "v_cvt_pk_bf16_f32 v180, v180, v181\n"
      "v_mfma_f32_16x16x32_bf16 v[220:223], a[188:191], v[92:95], v[220:223]\n"
      "ds_write_b32 v46, v180 offset:2048\n"
      "v_fmac_f32 v188, v26, v228\n"
      "v_fmac_f32 v189, v26, v229\n"
      "s_waitcnt lgkmcnt(1)\n"
      "v_mfma_f32_16x16x32_bf16 v[200:203], a[192:195], v[96:99], v[200:203]\n"
      "v_fmac_f32 v190, v26, v230\n"
      "v_fmac_f32 v191, v26, v231\n"
      "v_mfma_f32_16x16x32_bf16 v[200:203], a[204:207], v[104:107], v[200:203]\n"
      "global_load_dwordx4 v[152:155], v45, s[10:11] offset:1024 sc1\n"
      "v_fma_f32 v188, v188, v27, v240\n"
      "v_fma_f32 v189, v189, v27, v241\n"
      "v_fma_f32 v190, v190, v27, v242\n"
      "v_mfma_f32_16x16x32_bf16 v[208:211], a[196:199], v[96:99], v[208:211]\n"
      "v_fma_f32 v191, v191, v27, v243\n"
      "v_mul_f32 v188, v188, v190\n"
      "v_mfma_f32_16x16x32_bf16 v[208:211], a[208:211], v[104:107], v[208:211]\n"
      "v_mul_f32 v189, v189, v191\n"
      "v_cvt_pk_bf16_f32 v188, v188, v189\n"
      "v_mfma_f32_16x16x32_bf16 v[216:219], a[200:203], v[96:99], v[216:219]\n"
      "global_load_dwordx4 v[156:159], v45, s[12:13] offset:1024 sc1\n"
      "ds_write_b32 v46, v188 offset:6144\n"
      "v_fmac_f32 v196, v26, v232\n"
      "v_fmac_f32 v197, v26, v233\n"
      "v_mfma_f32_16x16x32_bf16 v[216:219], a[212:215], v[104:107], v[216:219]\n"
      "buffer_load_dword v38, v42, s[32:35], s63 offen offset:512 sc1\n"
      "buffer_load_dword v39, v42, s[32:35], s63 offen offset:768 sc1\n"
      "v_fmac_f32 v198, v26, v234\n"
      "v_fmac_f32 v199, v26, v235\n"
      "v_mfma_f32_16x16x32_bf16 v[204:207], a[192:195], v[100:103], v[204:207]\n"
      "v_fma_f32 v196, v196, v27, v244\n"
      "v_fma_f32 v197, v197, v27, v245\n"
      "v_fma_f32 v198, v198, v27, v246\n"
      "v_add_u32 v45, 10240, v7\n"
      "v_mfma_f32_16x16x32_bf16 v[204:207], a[204:207], v[108:111], v[204:207]\n"
      "global_load_dwordx4 v[172:175], v45, s[10:11] offset:-4096 sc1\n"
      "v_fma_f32 v199, v199, v27, v247\n"
      "v_mul_f32 v196, v196, v198\n"
      "v_mfma_f32_16x16x32_bf16 v[212:215], a[196:199], v[100:103], v[212:215]\n"
      "v_mul_f32 v197, v197, v199\n"
      "v_cvt_pk_bf16_f32 v196, v196, v197\n"
      "v_mfma_f32_16x16x32_bf16 v[212:215], a[208:211], v[108:111], v[212:215]\n"
      "ds_write_b32 v46, v196 offset:10240\n"
      "s_waitcnt vmcnt(37)\n"
      "v_mfma_f32_16x16x32_bf16 v[220:223], a[200:203], v[100:103], v[220:223]\n"
      "global_load_dwordx4 v[160:163], v45, s[12:13] offset:-4096 sc1\n"
      "v_mfma_f32_16x16x32_bf16 v[220:223], a[212:215], v[108:111], v[220:223]\n"
      "ds_read_b128 a[144:147], v57 offset:0\n"
      "s_waitcnt lgkmcnt(1)\n"
      "s_barrier\n"
      "v_mfma_f32_16x16x32_bf16 v[200:203], a[216:219], v[112:115], v[200:203]\n"
      "ds_read_b128 v[176:179], v47 offset:0\n"
      "v_mfma_f32_16x16x32_bf16 v[200:203], a[228:231], v[120:123], v[200:203]\n"
      "global_load_dwordx4 v[164:167], v45, s[10:11] offset:-3072 sc1\n"
      "ds_read_b128 v[180:183], v47 offset:4096\n"
      "v_mfma_f32_16x16x32_bf16 v[208:211], a[220:223], v[112:115], v[208:211]\n"
      "ds_read_b128 v[184:187], v47 offset:8192\n"
      "v_add_u32 v43, s59, v7\n"
      "v_mfma_f32_16x16x32_bf16 v[208:211], a[232:235], v[120:123], v[208:211]\n"
      "ds_read_b128 a[148:151], v57 offset:1024\n"
      "s_waitcnt vmcnt(11)\n"
      "v_add_u32 v59, s59, v42\n"
      "v_mfma_f32_16x16x32_bf16 v[216:219], a[224:227], v[112:115], v[216:219]\n"
      "global_load_dwordx4 v[168:171], v45, s[12:13] offset:-3072 sc1\n"
      "v_add3_u32 v2, v8, v9, s30\n"
      "v_add3_u32 v2, v2, v10, v11\n"
      "v_add3_u32 v2, v2, v16, v17\n"
      "v_mfma_f32_16x16x32_bf16 v[216:219], a[236:239], v[120:123], v[216:219]\n"
      "ds_read_b128 a[152:155], v57 offset:2048\n"
      "v_add3_u32 v2, v2, v18, v19\n"
      "v_add3_u32 v2, v2, v128, v129\n"
      "v_add3_u32 v2, v2, v130, v131\n"
      "v_mfma_f32_16x16x32_bf16 v[204:207], a[216:219], v[116:119], v[204:207]\n"
      "ds_read_b128 a[156:159], v57 offset:3072\n"
      "v_xor_b32 v2, v2, v36\n"
      "v_add3_u32 v3, v12, v13, s30\n"
      "v_add3_u32 v3, v3, v14, v15\n"
      "s_waitcnt lgkmcnt(3)\n"
      "v_mfma_f32_16x16x32_bf16 v[204:207], a[228:231], v[124:127], v[204:207]\n"
      "buffer_store_dwordx4 v[176:179], v43, s[32:35], s69 offen offset:0 sc1\n"
      "global_load_dwordx4 v[64:67], v45, s[10:11] offset:-2048 sc1\n"
      "v_add3_u32 v3, v3, v20, v21\n"
      "v_add3_u32 v3, v3, v22, v23\n"
      "v_add3_u32 v3, v3, v132, v133\n"
      "v_add3_u32 v58, v176, v177, s30\n"
      "v_mfma_f32_16x16x32_bf16 v[212:215], a[220:223], v[116:119], v[212:215]\n"
      "ds_read_b128 a[160:163], v57 offset:4096\n"
      "v_add3_u32 v3, v3, v134, v135\n"
      "v_xor_b32 v3, v3, v37\n"
      "v_or_b32 v2, v2, v3\n"
      "v_add3_u32 v58, v58, v178, v179\n"
      "v_add3_u32 v58, v58, v180, v181\n"
      "v_mfma_f32_16x16x32_bf16 v[212:215], a[232:235], v[124:127], v[212:215]\n"
      "buffer_store_dwordx4 v[180:183], v43, s[32:35], s69 offen offset:1024 sc1\n"
      "v_cmp_ne_u32 vcc, 0, v2\n"
      "v_add3_u32 v58, v58, v182, v183\n"
      "v_mfma_f32_16x16x32_bf16 v[220:223], a[224:227], v[116:119], v[220:223]\n"
      "global_load_dwordx4 v[68:71], v45, s[12:13] offset:-2048 sc1\n"
      "v_add3_u32 v58, v58, v184, v185\n"
      "v_add3_u32 v58, v58, v186, v187\n"
      "v_mfma_f32_16x16x32_bf16 v[220:223], a[236:239], v[124:127], v[220:223]\n"
      "buffer_store_dwordx4 v[184:187], v43, s[32:35], s69 offen offset:2048 sc1\n"
      "buffer_load_dword v60, v42, s[32:35], s63 offen offset:1024 sc1\n"
      "buffer_load_dword v61, v42, s[32:35], s63 offen offset:1280 sc1\n"
      "s_cbranch_vccnz L_shA0_%=\n"
      "L_bhA0_%=:\n"
      "v_mfma_f32_16x16x32_bf16 v[96:99], a[240:243], v[8:11], 0\n"
      "ds_read_b128 a[164:167], v57 offset:5120\n"
      "v_mfma_f32_16x16x32_bf16 v[96:99], a[4:7], v[16:19], v[96:99]\n"
      "buffer_store_dword v58, v59, s[32:35], s70 offen sc1\n"
      "global_load_dwordx4 v[72:75], v45, s[10:11] offset:-1024 sc1\n"
      "v_fmac_f32 v200, v28, v224\n"
      "v_mfma_f32_16x16x32_bf16 v[96:99], a[24:27], v[128:131], v[96:99]\n"
      "ds_read_b128 a[168:171], v57 offset:6144\n"
      "v_fmac_f32 v201, v28, v225\n"
      "v_fmac_f32 v202, v28, v226\n"
      "v_fmac_f32 v203, v28, v227\n"
      "v_fma_f32 v200, v200, v29, v236\n"
      "v_fma_f32 v201, v201, v29, v237\n"
      "v_fma_f32 v202, v202, v29, v238\n"
      "v_fma_f32 v203, v203, v29, v239\n"
      "v_mul_f32 v200, v200, v202\n"
      "v_mul_f32 v201, v201, v203\n"
      "v_cvt_pk_bf16_f32 v200, v200, v201\n"
      "v_mfma_f32_16x16x32_bf16 v[104:107], a[244:247], v[8:11], 0\n"
      "ds_write_b32 v46, v200 offset:1024\n"
      "v_fmac_f32 v208, v28, v228\n"
      "v_fmac_f32 v209, v28, v229\n"
      "v_fmac_f32 v210, v28, v230\n"
      "v_fmac_f32 v211, v28, v231\n"
      "v_fma_f32 v208, v208, v29, v240\n"
      "v_fma_f32 v209, v209, v29, v241\n"
      "v_fma_f32 v210, v210, v29, v242\n"
      "v_fma_f32 v211, v211, v29, v243\n"
      "v_mul_f32 v208, v208, v210\n"
      "v_mul_f32 v209, v209, v211\n"
      "v_cvt_pk_bf16_f32 v208, v208, v209\n"
      "v_mfma_f32_16x16x32_bf16 v[104:107], a[8:11], v[16:19], v[104:107]\n"
      "global_load_dwordx4 v[76:79], v45, s[12:13] offset:-1024 sc1\n"
      "ds_write_b32 v46, v208 offset:5120\n"
      "ds_read_b128 a[172:175], v57 offset:7168\n"
      "v_fmac_f32 v216, v28, v232\n"
      "v_mfma_f32_16x16x32_bf16 v[104:107], a[28:31], v[128:131], v[104:107]\n"
      "ds_read_b128 a[176:179], v57 offset:8192\n"
      "v_fmac_f32 v217, v28, v233\n"
      "v_fmac_f32 v218, v28, v234\n"
      "v_fmac_f32 v219, v28, v235\n"
      "v_fma_f32 v216, v216, v29, v244\n"
      "v_fma_f32 v217, v217, v29, v245\n"
      "v_fma_f32 v218, v218, v29, v246\n"
      "v_fma_f32 v219, v219, v29, v247\n"
      "v_mul_f32 v216, v216, v218\n"
      "v_mul_f32 v217, v217, v219\n"
      "v_cvt_pk_bf16_f32 v216, v216, v217\n"
      "v_mfma_f32_16x16x32_bf16 v[112:115], a[248:251], v[8:11], 0\n"
      "ds_write_b32 v46, v216 offset:9216\n"
      "v_fmac_f32 v204, v30, v224\n"
      "v_fmac_f32 v205, v30, v225\n"
      "v_fmac_f32 v206, v30, v226\n"
      "v_fmac_f32 v207, v30, v227\n"
      "v_fma_f32 v204, v204, v31, v236\n"
      "v_fma_f32 v205, v205, v31, v237\n"
      "v_fma_f32 v206, v206, v31, v238\n"
      "v_fma_f32 v207, v207, v31, v239\n"
      "v_mul_f32 v204, v204, v206\n"
      "v_mul_f32 v205, v205, v207\n"
      "v_cvt_pk_bf16_f32 v204, v204, v205\n"
      "v_mfma_f32_16x16x32_bf16 v[112:115], a[12:15], v[16:19], v[112:115]\n"
      "global_load_dwordx4 v[80:83], v45, s[10:11] offset:0 sc1\n"
      "ds_write_b32 v46, v204 offset:3072\n"
      "v_fmac_f32 v212, v30, v228\n"
      "v_fmac_f32 v213, v30, v229\n"
      "v_mfma_f32_16x16x32_bf16 v[112:115], a[32:35], v[128:131], v[112:115]\n"
      "v_fmac_f32 v214, v30, v230\n"
      "v_fmac_f32 v215, v30, v231\n"
      "v_fma_f32 v212, v212, v31, v240\n"
      "v_fma_f32 v213, v213, v31, v241\n"
      "v_fma_f32 v214, v214, v31, v242\n"
      "v_fma_f32 v215, v215, v31, v243\n"
      "v_mul_f32 v212, v212, v214\n"
      "v_mul_f32 v213, v213, v215\n"
      "v_cvt_pk_bf16_f32 v212, v212, v213\n"
      "v_mfma_f32_16x16x32_bf16 v[120:123], a[252:255], v[8:11], 0\n"
      "ds_write_b32 v46, v212 offset:7168\n"
      "L_pg6_%=:\n"
      "s_cbranch_execz L_pgr_%=\n"
      "v_fmac_f32 v220, v30, v232\n"
      "v_fmac_f32 v221, v30, v233\n"
      "v_fmac_f32 v222, v30, v234\n"
      "v_fmac_f32 v223, v30, v235\n"
      "v_mfma_f32_16x16x32_bf16 v[120:123], a[16:19], v[16:19], v[120:123]\n"
      "global_load_dwordx4 v[84:87], v45, s[12:13] offset:0 sc1\n"
      "v_fma_f32 v220, v220, v31, v244\n"
      "v_fma_f32 v221, v221, v31, v245\n"
      "v_fma_f32 v222, v222, v31, v246\n"
      "v_fma_f32 v223, v223, v31, v247\n"
      "v_mul_f32 v220, v220, v222\n"
      "v_mul_f32 v221, v221, v223\n"
      "v_cvt_pk_bf16_f32 v220, v220, v221\n"
      "v_mfma_f32_16x16x32_bf16 v[120:123], a[36:39], v[128:131], v[120:123]\n"
      "ds_write_b32 v46, v220 offset:11264\n"
      "v_add_u32 v57, s41, v7\n"
      "v_mfma_f32_16x16x32_bf16 v[248:251], a[0:3], v[8:11], 0\n"
      "ds_read_b128 a[180:183], v57 offset:0\n"
      "v_mfma_f32_16x16x32_bf16 v[248:251], a[20:23], v[16:19], v[248:251]\n"
      "global_load_dwordx4 v[88:91], v45, s[10:11] offset:1024 sc1\n"
      "v_mfma_f32_16x16x32_bf16 v[248:251], a[40:43], v[128:131], v[248:251]\n"
      "ds_read_b128 a[184:187], v57 offset:1024\n"
      "s_waitcnt lgkmcnt(2)\n"
      "s_barrier\n"
      "v_mfma_f32_16x16x32_bf16 v[100:103], a[240:243], v[12:15], 0\n"
      "ds_read_b128 v[200:203], v47 offset:0\n"
      "v_mfma_f32_16x16x32_bf16 v[100:103], a[4:7], v[20:23], v[100:103]\n"
      "global_load_dwordx4 v[92:95], v45, s[12:13] offset:1024 sc1\n"
      "ds_read_b128 v[204:207], v47 offset:4096\n"
      "v_mfma_f32_16x16x32_bf16 v[100:103], a[24:27], v[132:135], v[100:103]\n"
      "buffer_load_dword v62, v42, s[32:35], s63 offen offset:1536 sc1\n"
      "buffer_load_dword v63, v42, s[32:35], s63 offen offset:1792 sc1\n"
      "ds_read_b128 v[208:211], v47 offset:8192\n"
      "v_xor_b32 v43, 0x80000000, v43\n"
      "v_mfma_f32_16x16x32_bf16 v[108:111], a[244:247], v[12:15], 0\n"
      "ds_read_b128 a[188:191], v57 offset:2048\n"
      "v_xor_b32 v59, 0x80000000, v59\n"
      "v_mfma_f32_16x16x32_bf16 v[108:111], a[8:11], v[20:23], v[108:111]\n"
      "v_mfma_f32_16x16x32_bf16 v[108:111], a[28:31], v[132:135], v[108:111]\n"
      "v_mfma_f32_16x16x32_bf16 v[116:119], a[248:251], v[12:15], 0\n"
      "s_waitcnt vmcnt(20)\n"
      "s_waitcnt lgkmcnt(1)\n"
      "v_mfma_f32_16x16x32_bf16 v[116:119], a[12:15], v[20:23], v[116:119]\n"
      "buffer_store_dwordx4 v[200:203], v43, s[32:35], s69 offen offset:0 sc1\n"
      "v_add3_u32 v2, v136, v137, s30\n"
      "v_add3_u32 v2, v2, v138, v139\n"
      "v_add3_u32 v2, v2, v144, v145\n"
      "v_add3_u32 v58, v200, v201, s30\n"
      "v_mfma_f32_16x16x32_bf16 v[116:119], a[32:35], v[132:135], v[116:119]\n"
      "v_add3_u32 v2, v2, v146, v147\n"
      "v_add3_u32 v2, v2, v152, v153\n"
      "v_add3_u32 v2, v2, v154, v155\n"
      "v_add3_u32 v58, v58, v202, v203\n"
      "v_add3_u32 v58, v58, v204, v205\n"
      "v_mfma_f32_16x16x32_bf16 v[124:127], a[252:255], v[12:15], 0\n"
      "buffer_store_dwordx4 v[204:207], v43, s[32:35], s69 offen offset:1024 sc1\n"
      "v_xor_b32 v2, v2, v38\n"
      "v_add3_u32 v3, v140, v141, s30\n"
      "v_add3_u32 v3, v3, v142, v143\n"
      "v_add3_u32 v58, v58, v206, v207\n"
      "v_mfma_f32_16x16x32_bf16 v[124:127], a[16:19], v[20:23], v[124:127]\n"
      "v_add3_u32 v3, v3, v148, v149\n"
      "v_add3_u32 v3, v3, v150, v151\n"
      "v_add3_u32 v3, v3, v156, v157\n"
      "v_add3_u32 v58, v58, v208, v209\n"
      "v_add3_u32 v58, v58, v210, v211\n"
      "v_mfma_f32_16x16x32_bf16 v[124:127], a[36:39], v[132:135], v[124:127]\n"
      "buffer_store_dwordx4 v[208:211], v43, s[32:35], s69 offen offset:2048 sc1\n"
      "v_add3_u32 v3, v3, v158, v159\n"
      "v_xor_b32 v3, v3, v39\n"
      "v_or_b32 v2, v2, v3\n"
      "v_mfma_f32_16x16x32_bf16 v[252:255], a[0:3], v[12:15], 0\n"
      "v_cmp_ne_u32 vcc, 0, v2\n"
      "v_mfma_f32_16x16x32_bf16 v[252:255], a[20:23], v[20:23], v[252:255]\n"
      "buffer_store_dword v58, v59, s[32:35], s70 offen sc1\n"
      "v_mfma_f32_16x16x32_bf16 v[252:255], a[40:43], v[132:135], v[252:255]\n"
      "s_cbranch_vccnz L_shA1_%=\n"
      "L_bhA1_%=:\n"
      "v_mfma_f32_16x16x32_bf16 v[96:99], a[44:47], v[136:139], v[96:99]\n"
      "s_add_u32 s43, s73, s97\n"
      "v_add_u32 v56, s43, v7\n"
      "v_mfma_f32_16x16x32_bf16 v[96:99], a[64:67], v[144:147], v[96:99]\n"
      "s_and_b32 s52, s60, 1\n"
      "s_lshr_b32 s53, s60, 1\n"
      "v_mfma_f32_16x16x32_bf16 v[96:99], a[84:87], v[152:155], v[96:99]\n"
      "v_add_u32 v44, 53248, v7\n"
      "s_mul_i32 s54, s52, 20480\n"
      "v_mfma_f32_16x16x32_bf16 v[104:107], a[48:51], v[136:139], v[104:107]\n"
      "s_lshl_b32 s53, s53, 11\n"
      "s_add_u32 s54, s54, s53\n"
      "v_mfma_f32_16x16x32_bf16 v[104:107], a[68:71], v[144:147], v[104:107]\n"
      "global_load_dwordx4 a[192:195], v44, s[6:7] offset:0 sc1\n"
      "v_add_u32 v49, s54, v7\n"
      "v_mfma_f32_16x16x32_bf16 v[104:107], a[88:91], v[152:155], v[104:107]\n"
      "s_lshl_b32 s52, s52, 5\n"
      "v_xor_b32 v53, s52, v6\n"
      "v_mfma_f32_16x16x32_bf16 v[112:115], a[52:55], v[136:139], v[112:115]\n"
      "s_lshl_b32 s52, s53, 1\n"
      "s_add_u32 s52, s52, 110592\n"
      "v_mfma_f32_16x16x32_bf16 v[112:115], a[72:75], v[144:147], v[112:115]\n"
      "global_load_dwordx4 a[196:199], v44, s[6:7] offset:1024 sc1\n"
      "v_add_u32 v53, s52, v53\n"
      "v_mfma_f32_16x16x32_bf16 v[112:115], a[92:95], v[152:155], v[112:115]\n"
      "s_lshr_b32 s53, s53, 4\n"
      "s_add_u32 s53, s53, 163328\n"
      "v_mfma_f32_16x16x32_bf16 v[120:123], a[56:59], v[136:139], v[120:123]\n"
      "v_lshrrev_b32 v48, 4, v7\n"
      "v_and_b32 v50, 0x30, v48\n"
      "v_mfma_f32_16x16x32_bf16 v[120:123], a[76:79], v[144:147], v[120:123]\n"
      "global_load_dwordx4 a[200:203], v44, s[6:7] offset:2048 sc1\n"
      "v_add_u32 v54, s53, v50\n"
      "v_mfma_f32_16x16x32_bf16 v[120:123], a[96:99], v[152:155], v[120:123]\n"
      "v_and_b32 v48, 15, v48\n"
      "v_lshrrev_b32 v50, 1, v50\n"
      "v_mfma_f32_16x16x32_bf16 v[248:251], a[60:63], v[136:139], v[248:251]\n"
      "v_mul_u32_u24 v48, 1280, v48\n"
      "v_add3_u32 v55, v48, v50, s74\n"
      "v_mfma_f32_16x16x32_bf16 v[248:251], a[80:83], v[144:147], v[248:251]\n"
      "global_load_dwordx4 a[204:207], v44, s[6:7] offset:3072 sc1\n"
      "v_mfma_f32_16x16x32_bf16 v[248:251], a[100:103], v[152:155], v[248:251]\n"
      "v_mfma_f32_16x16x32_bf16 v[100:103], a[44:47], v[140:143], v[100:103]\n"
      "v_add_u32 v44, 61440, v7\n"
      "v_mfma_f32_16x16x32_bf16 v[100:103], a[64:67], v[148:151], v[100:103]\n"
      "global_load_dwordx4 a[208:211], v44, s[6:7] offset:-4096 sc1\n"
      "v_mfma_f32_16x16x32_bf16 v[100:103], a[84:87], v[156:159], v[100:103]\n"
      "v_mfma_f32_16x16x32_bf16 v[108:111], a[48:51], v[140:143], v[108:111]\n"
      "v_mfma_f32_16x16x32_bf16 v[108:111], a[68:71], v[148:151], v[108:111]\n"
      "global_load_dwordx4 a[212:215], v44, s[6:7] offset:-3072 sc1\n"
      "v_mfma_f32_16x16x32_bf16 v[108:111], a[88:91], v[156:159], v[108:111]\n"
      "v_mfma_f32_16x16x32_bf16 v[116:119], a[52:55], v[140:143], v[116:119]\n"
      "s_waitcnt vmcnt(19)\n"
      "s_waitcnt lgkmcnt(0)\n"
      "v_mfma_f32_16x16x32_bf16 v[116:119], a[72:75], v[148:151], v[116:119]\n"
      "global_load_dwordx4 a[216:219], v44, s[6:7] offset:-2048 sc1\n"
      "v_add3_u32 v2, v172, v173, s30\n"
      "v_add3_u32 v2, v2, v174, v175\n"
      "v_add3_u32 v2, v2, v164, v165\n"
      "v_mfma_f32_16x16x32_bf16 v[116:119], a[92:95], v[156:159], v[116:119]\n"
      "v_add3_u32 v2, v2, v166, v167\n"
      "v_add3_u32 v2, v2, v64, v65\n"
      "v_add3_u32 v2, v2, v66, v67\n"
      "v_mfma_f32_16x16x32_bf16 v[124:127], a[56:59], v[140:143], v[124:127]\n"
      "v_xor_b32 v2, v2, v60\n"
      "v_add3_u32 v3, v160, v161, s30\n"
      "v_add3_u32 v3, v3, v162, v163\n"
      "v_mfma_f32_16x16x32_bf16 v[124:127], a[76:79], v[148:151], v[124:127]\n"
      "global_load_dwordx4 a[220:223], v44, s[6:7] offset:-1024 sc1\n"
      "v_add3_u32 v3, v3, v168, v169\n"
      "v_add3_u32 v3, v3, v170, v171\n"
      "v_add3_u32 v3, v3, v68, v69\n"
      "v_mfma_f32_16x16x32_bf16 v[124:127], a[96:99], v[156:159], v[124:127]\n"
      "v_add3_u32 v3, v3, v70, v71\n"
      "v_xor_b32 v3, v3, v61\n"
      "v_or_b32 v2, v2, v3\n"
      "v_mfma_f32_16x16x32_bf16 v[252:255], a[60:63], v[140:143], v[252:255]\n"
      "v_cmp_ne_u32 vcc, 0, v2\n"
      "v_mfma_f32_16x16x32_bf16 v[252:255], a[80:83], v[148:151], v[252:255]\n"
      "v_mfma_f32_16x16x32_bf16 v[252:255], a[100:103], v[156:159], v[252:255]\n"
      "s_cbranch_vccnz L_shA2_%=\n"
      "L_bhA2_%=:\n"
      "v_mfma_f32_16x16x32_bf16 v[96:99], a[104:107], v[172:175], v[96:99]\n"
      "v_mfma_f32_16x16x32_bf16 v[96:99], a[124:127], v[164:167], v[96:99]\n"
      "v_mfma_f32_16x16x32_bf16 v[96:99], a[144:147], v[64:67], v[96:99]\n"
      "v_add_u32 v45, 102400, v7\n"
      "v_mfma_f32_16x16x32_bf16 v[104:107], a[108:111], v[172:175], v[104:107]\n"
      "v_mfma_f32_16x16x32_bf16 v[104:107], a[128:131], v[164:167], v[104:107]\n"
      "global_load_dwordx4 v[224:227], v45, s[10:11] offset:-4096 sc1\n"
      "v_mfma_f32_16x16x32_bf16 v[104:107], a[148:151], v[64:67], v[104:107]\n"
      "v_mfma_f32_16x16x32_bf16 v[112:115], a[112:115], v[172:175], v[112:115]\n"
      "v_mfma_f32_16x16x32_bf16 v[112:115], a[132:135], v[164:167], v[112:115]\n"
      "v_mfma_f32_16x16x32_bf16 v[112:115], a[152:155], v[64:67], v[112:115]\n"
      "global_load_dwordx4 v[228:231], v45, s[10:11] offset:-3072 sc1\n"
      "v_mfma_f32_16x16x32_bf16 v[120:123], a[116:119], v[172:175], v[120:123]\n"
      "v_mfma_f32_16x16x32_bf16 v[120:123], a[136:139], v[164:167], v[120:123]\n"
      "v_mfma_f32_16x16x32_bf16 v[120:123], a[156:159], v[64:67], v[120:123]\n"
      "v_mfma_f32_16x16x32_bf16 v[248:251], a[120:123], v[172:175], v[248:251]\n"
      "global_load_dwordx4 v[232:235], v45, s[10:11] offset:-2048 sc1\n"
      "v_mfma_f32_16x16x32_bf16 v[248:251], a[140:143], v[164:167], v[248:251]\n"
      "v_mfma_f32_16x16x32_bf16 v[248:251], a[160:163], v[64:67], v[248:251]\n"
      "v_mfma_f32_16x16x32_bf16 v[100:103], a[104:107], v[160:163], v[100:103]\n"
      "global_load_dwordx4 v[236:239], v45, s[10:11] offset:-1024 sc1\n"
      "v_mfma_f32_16x16x32_bf16 v[100:103], a[124:127], v[168:171], v[100:103]\n"
      "v_mfma_f32_16x16x32_bf16 v[100:103], a[144:147], v[68:71], v[100:103]\n"
      "v_mfma_f32_16x16x32_bf16 v[108:111], a[108:111], v[160:163], v[108:111]\n"
      "global_load_dwordx4 v[240:243], v45, s[10:11] offset:0 sc1\n"
      "v_mfma_f32_16x16x32_bf16 v[108:111], a[128:131], v[168:171], v[108:111]\n"
      "v_mfma_f32_16x16x32_bf16 v[108:111], a[148:151], v[68:71], v[108:111]\n"
      "v_mfma_f32_16x16x32_bf16 v[116:119], a[112:115], v[160:163], v[116:119]\n"
      "global_load_dwordx4 v[244:247], v45, s[10:11] offset:1024 sc1\n"
      "s_waitcnt vmcnt(18)\n"
      "v_mfma_f32_16x16x32_bf16 v[116:119], a[132:135], v[168:171], v[116:119]\n"
      "buffer_load_dword v32, v42, s[32:35], s63 offen offset:2048 sc1\n"
      "buffer_load_dword v33, v42, s[32:35], s63 offen offset:2304 sc1\n"
      "v_add3_u32 v2, v72, v73, s30\n"
      "v_add3_u32 v2, v2, v74, v75\n"
      "v_add3_u32 v2, v2, v80, v81\n"
      "v_mfma_f32_16x16x32_bf16 v[116:119], a[152:155], v[68:71], v[116:119]\n"
      "v_add3_u32 v2, v2, v82, v83\n"
      "v_add3_u32 v2, v2, v88, v89\n"
      "v_add3_u32 v2, v2, v90, v91\n"
      "v_mfma_f32_16x16x32_bf16 v[124:127], a[116:119], v[160:163], v[124:127]\n"
      "v_xor_b32 v2, v2, v62\n"
      "v_add3_u32 v3, v76, v77, s30\n"
      "v_add3_u32 v3, v3, v78, v79\n"
      "v_mfma_f32_16x16x32_bf16 v[124:127], a[136:139], v[168:171], v[124:127]\n"
      "v_add_u32 v45, 108544, v7\n"
      "v_add3_u32 v3, v3, v84, v85\n"
      "v_add3_u32 v3, v3, v86, v87\n"
      "v_add3_u32 v3, v3, v92, v93\n"
      "v_mfma_f32_16x16x32_bf16 v[124:127], a[156:159], v[68:71], v[124:127]\n"
      "global_load_dwordx4 v[8:11], v45, s[10:11] offset:-4096 sc1\n"
      "v_add3_u32 v3, v3, v94, v95\n"
      "v_xor_b32 v3, v3, v63\n"
      "v_or_b32 v2, v2, v3\n"
      "v_mfma_f32_16x16x32_bf16 v[252:255], a[120:123], v[160:163], v[252:255]\n"
      "v_cmp_ne_u32 vcc, 0, v2\n"
      "v_mfma_f32_16x16x32_bf16 v[252:255], a[140:143], v[168:171], v[252:255]\n"
      "v_mfma_f32_16x16x32_bf16 v[252:255], a[160:163], v[68:71], v[252:255]\n"
      "global_load_dwordx4 v[12:15], v45, s[10:11] offset:-3072 sc1\n"
      "s_cbranch_vccnz L_shA3_%=\n"
      "L_bhA3_%=:\n"
      "v_mfma_f32_16x16x32_bf16 v[96:99], a[164:167], v[72:75], v[96:99]\n"
      "v_mfma_f32_16x16x32_bf16 v[104:107], a[168:171], v[72:75], v[104:107]\n"
      "v_mfma_f32_16x16x32_bf16 v[112:115], a[172:175], v[72:75], v[112:115]\n"
      "global_load_dwordx4 v[16:19], v45, s[10:11] offset:-2048 sc1\n"
      "v_mfma_f32_16x16x32_bf16 v[120:123], a[176:179], v[72:75], v[120:123]\n"
      "v_mfma_f32_16x16x32_bf16 v[248:251], a[180:183], v[72:75], v[248:251]\n"
      "v_mfma_f32_16x16x32_bf16 v[100:103], a[164:167], v[76:79], v[100:103]\n"
      "global_load_dwordx4 v[20:23], v45, s[10:11] offset:-1024 sc1\n"
      "v_mfma_f32_16x16x32_bf16 v[108:111], a[168:171], v[76:79], v[108:111]\n"
      "v_mfma_f32_16x16x32_bf16 v[116:119], a[172:175], v[76:79], v[116:119]\n"
      "v_mfma_f32_16x16x32_bf16 v[124:127], a[176:179], v[76:79], v[124:127]\n"
      "global_load_dwordx4 v[128:131], v45, s[10:11] offset:0 sc1\n"
      "v_mfma_f32_16x16x32_bf16 v[252:255], a[180:183], v[76:79], v[252:255]\n"
      "v_mfma_f32_16x16x32_bf16 v[96:99], a[184:187], v[80:83], v[96:99]\n"
      "v_mfma_f32_16x16x32_bf16 v[104:107], a[188:191], v[80:83], v[104:107]\n"
      "global_load_dwordx4 v[132:135], v45, s[10:11] offset:1024 sc1\n"
      "s_waitcnt vmcnt(14)\n"
      "v_mfma_f32_16x16x32_bf16 v[112:115], a[192:195], v[80:83], v[112:115]\n"
      "buffer_load_dword v34, v42, s[32:35], s63 offen offset:2560 sc1\n"
      "buffer_load_dword v35, v42, s[32:35], s63 offen offset:2816 sc1\n"
      "v_mfma_f32_16x16x32_bf16 v[120:123], a[196:199], v[80:83], v[120:123]\n"
      "v_mfma_f32_16x16x32_bf16 v[248:251], a[200:203], v[80:83], v[248:251]\n"
      "v_mfma_f32_16x16x32_bf16 v[100:103], a[184:187], v[84:87], v[100:103]\n"
      "v_add_u32 v45, 102400, v7\n"
      "v_mfma_f32_16x16x32_bf16 v[108:111], a[188:191], v[84:87], v[108:111]\n"
      "v_mfma_f32_16x16x32_bf16 v[116:119], a[192:195], v[84:87], v[116:119]\n"
      "global_load_dwordx4 v[136:139], v45, s[12:13] offset:-4096 sc1\n"
      "v_mfma_f32_16x16x32_bf16 v[124:127], a[196:199], v[84:87], v[124:127]\n"
      "v_mfma_f32_16x16x32_bf16 v[252:255], a[200:203], v[84:87], v[252:255]\n"
      "v_mfma_f32_16x16x32_bf16 v[96:99], a[204:207], v[88:91], v[96:99]\n"
      "v_mfma_f32_16x16x32_bf16 v[104:107], a[208:211], v[88:91], v[104:107]\n"
      "global_load_dwordx4 v[140:143], v45, s[12:13] offset:-3072 sc1\n"
      "s_waitcnt vmcnt(10)\n"
      "v_mfma_f32_16x16x32_bf16 v[112:115], a[212:215], v[88:91], v[112:115]\n"
      "v_add3_u32 v2, v224, v225, s30\n"
      "v_add3_u32 v2, v2, v226, v227\n"
      "v_add3_u32 v2, v2, v228, v229\n"
      "v_mfma_f32_16x16x32_bf16 v[120:123], a[216:219], v[88:91], v[120:123]\n"
      "v_add3_u32 v2, v2, v230, v231\n"
      "v_add3_u32 v2, v2, v232, v233\n"
      "v_add3_u32 v2, v2, v234, v235\n"
      "v_mfma_f32_16x16x32_bf16 v[248:251], a[220:223], v[88:91], v[248:251]\n"
      "v_xor_b32 v2, v2, v32\n"
      "v_add3_u32 v3, v236, v237, s30\n"
      "v_add3_u32 v3, v3, v238, v239\n"
      "v_mfma_f32_16x16x32_bf16 v[100:103], a[204:207], v[92:95], v[100:103]\n"
      "global_load_dwordx4 v[144:147], v45, s[12:13] offset:-2048 sc1\n"
      "v_add3_u32 v3, v3, v240, v241\n"
      "v_add3_u32 v3, v3, v242, v243\n"
      "v_add3_u32 v3, v3, v244, v245\n"
      "v_mfma_f32_16x16x32_bf16 v[108:111], a[208:211], v[92:95], v[108:111]\n"
      "v_add3_u32 v3, v3, v246, v247\n"
      "v_xor_b32 v3, v3, v33\n"
      "v_or_b32 v2, v2, v3\n"
      "v_mfma_f32_16x16x32_bf16 v[116:119], a[212:215], v[92:95], v[116:119]\n"
      "v_cmp_ne_u32 vcc, 0, v2\n"
      "v_mfma_f32_16x16x32_bf16 v[124:127], a[216:219], v[92:95], v[124:127]\n"
      "v_mfma_f32_16x16x32_bf16 v[252:255], a[220:223], v[92:95], v[252:255]\n"
      "global_load_dwordx4 v[148:151], v45, s[12:13] offset:-1024 sc1\n"
      "s_cbranch_vccnz L_shB0_%=\n"
      "L_bhB0_%=:\n"
      "v_mfma_f32_16x16x32_bf16 v[176:179], a[240:243], v[224:227], 0\n"
      "ds_read_b128 v[80:83], v54 offset:0\n"
      "v_mfma_f32_16x16x32_bf16 v[176:179], a[4:7], v[228:231], v[176:179]\n"
      "ds_read_b128 v[84:87], v54 offset:64\n"
      "v_mfma_f32_16x16x32_bf16 v[176:179], a[24:27], v[232:235], v[176:179]\n"
      "ds_read_b128 v[88:91], v54 offset:128\n"
      "v_mfma_f32_16x16x32_bf16 v[180:183], a[244:247], v[224:227], 0\n"
      "global_load_dwordx4 v[152:155], v45, s[12:13] offset:0 sc1\n"
      "v_mfma_f32_16x16x32_bf16 v[180:183], a[8:11], v[228:231], v[180:183]\n"
      "ds_write_b128 v56, v[96:99] offset:0\n"
      "v_mfma_f32_16x16x32_bf16 v[180:183], a[28:31], v[232:235], v[180:183]\n"
      "v_mfma_f32_16x16x32_bf16 v[184:187], a[248:251], v[224:227], 0\n"
      "v_mfma_f32_16x16x32_bf16 v[184:187], a[12:15], v[228:231], v[184:187]\n"
      "global_load_dwordx4 v[156:159], v45, s[12:13] offset:1024 sc1\n"
      "v_mfma_f32_16x16x32_bf16 v[184:187], a[32:35], v[232:235], v[184:187]\n"
      "ds_write_b128 v56, v[100:103] offset:20480\n"
      "v_mfma_f32_16x16x32_bf16 v[188:191], a[252:255], v[224:227], 0\n"
      "v_mfma_f32_16x16x32_bf16 v[188:191], a[16:19], v[228:231], v[188:191]\n"
      "v_mfma_f32_16x16x32_bf16 v[188:191], a[36:39], v[232:235], v[188:191]\n"
      "buffer_load_dword v24, v42, s[32:35], s63 offen offset:3072 sc1\n"
      "v_mfma_f32_16x16x32_bf16 v[192:195], a[0:3], v[224:227], 0\n"
      "buffer_load_dword v25, v42, s[32:35], s63 offen offset:3328 sc1\n"
      "v_add_u32 v45, 108544, v7\n"
      "v_mfma_f32_16x16x32_bf16 v[192:195], a[20:23], v[228:231], v[192:195]\n"
      "global_load_dwordx4 v[172:175], v45, s[12:13] offset:-4096 sc1\n"
      "ds_write_b128 v56, v[104:107] offset:1024\n"
      "v_mfma_f32_16x16x32_bf16 v[192:195], a[40:43], v[232:235], v[192:195]\n"
      "v_mfma_f32_16x16x32_bf16 v[176:179], a[44:47], v[236:239], v[176:179]\n"
      "v_mfma_f32_16x16x32_bf16 v[176:179], a[64:67], v[240:243], v[176:179]\n"
      "ds_write_b128 v56, v[108:111] offset:21504\n"
      "v_mfma_f32_16x16x32_bf16 v[176:179], a[84:87], v[244:247], v[176:179]\n"
      "global_load_dwordx4 v[160:163], v45, s[12:13] offset:-3072 sc1\n"
      "v_mfma_f32_16x16x32_bf16 v[180:183], a[48:51], v[236:239], v[180:183]\n"
      "v_mfma_f32_16x16x32_bf16 v[180:183], a[68:71], v[240:243], v[180:183]\n"
      "ds_write_b128 v56, v[112:115] offset:2048\n"
      "v_mfma_f32_16x16x32_bf16 v[180:183], a[88:91], v[244:247], v[180:183]\n"
      "v_mfma_f32_16x16x32_bf16 v[184:187], a[52:55], v[236:239], v[184:187]\n"
      "global_load_dwordx4 v[164:167], v45, s[12:13] offset:-2048 sc1\n"
      "s_waitcnt vmcnt(11)\n"
      "v_mfma_f32_16x16x32_bf16 v[184:187], a[72:75], v[240:243], v[184:187]\n"
      "ds_write_b128 v56, v[116:119] offset:22528\n"
      "v_add3_u32 v2, v8, v9, s30\n"
      "v_add3_u32 v2, v2, v10, v11\n"
      "v_add3_u32 v2, v2, v12, v13\n"
      "v_add3_u32 v2, v2, v14, v15\n"
      "v_mfma_f32_16x16x32_bf16 v[184:187], a[92:95], v[244:247], v[184:187]\n"
      "v_add3_u32 v2, v2, v16, v17\n"
      "v_add3_u32 v2, v2, v18, v19\n"
      "v_mfma_f32_16x16x32_bf16 v[188:191], a[56:59], v[236:239], v[188:191]\n"
      "v_xor_b32 v2, v2, v34\n"
      "v_add3_u32 v3, v20, v21, s30\n"
      "v_add3_u32 v3, v3, v22, v23\n"
      "v_mfma_f32_16x16x32_bf16 v[188:191], a[76:79], v[240:243], v[188:191]\n"
      "global_load_dwordx4 v[168:171], v45, s[12:13] offset:-1024 sc1\n"
      "ds_write_b128 v56, v[120:123] offset:3072\n"
      "v_add3_u32 v3, v3, v128, v129\n"
      "v_add3_u32 v3, v3, v130, v131\n"
      "v_add3_u32 v3, v3, v132, v133\n"
      "v_add3_u32 v3, v3, v134, v135\n"
      "v_mfma_f32_16x16x32_bf16 v[188:191], a[96:99], v[244:247], v[188:191]\n"
      "v_xor_b32 v3, v3, v35\n"
      "v_or_b32 v2, v2, v3\n"
      "v_mfma_f32_16x16x32_bf16 v[192:195], a[60:63], v[236:239], v[192:195]\n"
      "v_cmp_ne_u32 vcc, 0, v2\n"
      "v_mfma_f32_16x16x32_bf16 v[192:195], a[80:83], v[240:243], v[192:195]\n"
      "ds_write_b128 v56, v[124:127] offset:23552\n"
      "v_mfma_f32_16x16x32_bf16 v[192:195], a[100:103], v[244:247], v[192:195]\n"
      "global_load_dwordx4 v[64:67], v45, s[12:13] offset:0 sc1\n"
      "s_cbranch_vccnz L_shB1_%=\n"
      "L_bhB1_%=:\n"
      "v_mfma_f32_16x16x32_bf16 v[176:179], a[104:107], v[8:11], v[176:179]\n"
      "v_mfma_f32_16x16x32_bf16 v[176:179], a[124:127], v[12:15], v[176:179]\n"
      "ds_write_b128 v56, v[248:251] offset:4096\n"
      "v_mfma_f32_16x16x32_bf16 v[176:179], a[144:147], v[16:19], v[176:179]\n"
      "v_mfma_f32_16x16x32_bf16 v[180:183], a[108:111], v[8:11], v[180:183]\n"
      "global_load_dwordx4 v[68:71], v45, s[12:13] offset:1024 sc1\n"
      "v_mfma_f32_16x16x32_bf16 v[180:183], a[128:131], v[12:15], v[180:183]\n"
      "ds_write_b128 v56, v[252:255] offset:24576\n"
      "v_mfma_f32_16x16x32_bf16 v[180:183], a[148:151], v[16:19], v[180:183]\n"
      "v_mfma_f32_16x16x32_bf16 v[184:187], a[112:115], v[8:11], v[184:187]\n"
      "v_mfma_f32_16x16x32_bf16 v[184:187], a[132:135], v[12:15], v[184:187]\n"
      "buffer_load_dword v26, v42, s[32:35], s63 offen offset:3584 sc1\n"
      "v_mfma_f32_16x16x32_bf16 v[184:187], a[152:155], v[16:19], v[184:187]\n"
      "buffer_load_dword v27, v42, s[32:35], s63 offen offset:3840 sc1\n"
      "v_mfma_f32_16x16x32_bf16 v[188:191], a[116:119], v[8:11], v[188:191]\n"
      "v_mfma_f32_16x16x32_bf16 v[188:191], a[136:139], v[12:15], v[188:191]\n"
      "s_waitcnt lgkmcnt(0)\n"
      "s_barrier\n"
      "v_mfma_f32_16x16x32_bf16 v[188:191], a[156:159], v[16:19], v[188:191]\n"
      "ds_read_b128 v[96:99], v49 offset:0\n"
      "v_add_u32 v56, 0xa000, v56\n"
      "v_mfma_f32_16x16x32_bf16 v[192:195], a[120:123], v[8:11], v[192:195]\n"
      "ds_read_b128 v[100:103], v49 offset:5120\n"
      "v_mfma_f32_16x16x32_bf16 v[192:195], a[140:143], v[12:15], v[192:195]\n"
      "ds_read_b128 v[104:107], v49 offset:10240\n"
      "v_mfma_f32_16x16x32_bf16 v[192:195], a[160:163], v[16:19], v[192:195]\n"
      "ds_read_b128 v[108:111], v49 offset:15360\n"
      "v_mfma_f32_16x16x32_bf16 v[176:179], a[164:167], v[20:23], v[176:179]\n"
      "ds_read_b64_tr_b16 v[72:73], v53 offset:0\n"
      "v_mfma_f32_16x16x32_bf16 v[176:179], a[184:187], v[128:131], v[176:179]\n"
      "ds_read_b64_tr_b16 v[74:75], v53 offset:2048\n"
      "v_mfma_f32_16x16x32_bf16 v[176:179], a[204:207], v[132:135], v[176:179]\n"
      "ds_read_b64_tr_b16 v[76:77], v53 offset:4096\n"
      "v_mfma_f32_16x16x32_bf16 v[180:183], a[168:171], v[20:23], v[180:183]\n"
      "ds_read_b128 v[112:115], v49 offset:1024\n"
      "v_mfma_f32_16x16x32_bf16 v[180:183], a[188:191], v[128:131], v[180:183]\n"
      "ds_read_b128 v[116:119], v49 offset:6144\n"
      "v_mfma_f32_16x16x32_bf16 v[180:183], a[208:211], v[132:135], v[180:183]\n"
      "ds_read_b128 v[120:123], v49 offset:11264\n"
      "v_mfma_f32_16x16x32_bf16 v[184:187], a[172:175], v[20:23], v[184:187]\n"
      "ds_read_b128 v[124:127], v49 offset:16384\n"
      "s_waitcnt vmcnt(8)\n"
      "v_mfma_f32_16x16x32_bf16 v[184:187], a[192:195], v[128:131], v[184:187]\n"
      "v_add3_u32 v2, v136, v137, s30\n"
      "v_add3_u32 v2, v2, v138, v139\n"
      "v_add3_u32 v2, v2, v140, v141\n"
      "s_waitcnt lgkmcnt(4)\n"
      "v_mfma_f32_16x16x32_bf16 v[184:187], a[212:215], v[132:135], v[184:187]\n"
      "v_add3_u32 v2, v2, v142, v143\n"
      "v_add3_u32 v2, v2, v144, v145\n"
      "v_add3_u32 v2, v2, v146, v147\n"
      "v_add_f32 v96, v96, v100\n"
      "v_add_f32 v97, v97, v101\n"
      "v_mfma_f32_16x16x32_bf16 v[188:191], a[176:179], v[20:23], v[188:191]\n"
      "v_xor_b32 v2, v2, v24\n"
      "v_add3_u32 v3, v148, v149, s30\n"
      "v_add3_u32 v3, v3, v150, v151\n"
      "v_add_f32 v98, v98, v102\n"
      "v_add_f32 v99, v99, v103\n"
      "v_mfma_f32_16x16x32_bf16 v[188:191], a[196:199], v[128:131], v[188:191]\n"
      "v_add3_u32 v3, v3, v152, v153\n"
      "v_add3_u32 v3, v3, v154, v155\n"
      "v_add3_u32 v3, v3, v156, v157\n"
      "v_add_f32 v104, v104, v108\n"
      "v_add_f32 v105, v105, v109\n"
      "v_mfma_f32_16x16x32_bf16 v[188:191], a[216:219], v[132:135], v[188:191]\n"
      "v_add3_u32 v3, v3, v158, v159\n"
      "v_xor_b32 v3, v3, v25\n"
      "v_or_b32 v2, v2, v3\n"
      "v_add_f32 v106, v106, v110\n"
      "v_add_f32 v107, v107, v111\n"
      "v_mfma_f32_16x16x32_bf16 v[192:195], a[180:183], v[20:23], v[192:195]\n"
      "v_cmp_ne_u32 vcc, 0, v2\n"
      "v_add_f32 v96, v96, v104\n"
      "v_add_f32 v97, v97, v105\n"
      "v_mfma_f32_16x16x32_bf16 v[192:195], a[200:203], v[128:131], v[192:195]\n"
      "v_add_f32 v98, v98, v106\n"
      "v_add_f32 v99, v99, v107\n"
      "v_mfma_f32_16x16x32_bf16 v[192:195], a[220:223], v[132:135], v[192:195]\n"
      "s_cbranch_vccnz L_shB2_%=\n"
      "L_bhB2_%=:\n"
      "v_cvt_pk_bf16_f32 v96, 0, v96\n"
      "v_mfma_f32_16x16x32_bf16 v[200:203], a[240:243], v[136:139], 0\n"
      "v_cvt_pk_bf16_f32 v97, 0, v97\n"
      "v_cvt_pk_bf16_f32 v98, 0, v98\n"
      "v_mfma_f32_16x16x32_bf16 v[200:203], a[4:7], v[140:143], v[200:203]\n"
      "v_cvt_pk_bf16_f32 v99, 0, v99\n"
      "v_mul_f32 v96, v96, v80\n"
      "v_mfma_f32_16x16x32_bf16 v[200:203], a[24:27], v[144:147], v[200:203]\n"
      "v_mul_f32 v97, v97, v81\n"
      "v_mul_f32 v98, v98, v82\n"
      "v_mfma_f32_16x16x32_bf16 v[204:207], a[244:247], v[136:139], 0\n"
      "v_mul_f32 v99, v99, v83\n"
      "v_cvt_pk_bf16_f32 v96, 0, v96\n"
      "v_mfma_f32_16x16x32_bf16 v[204:207], a[8:11], v[140:143], v[204:207]\n"
      "v_cvt_pk_bf16_f32 v97, 0, v97\n"
      "v_cvt_pk_bf16_f32 v98, 0, v98\n"
      "v_mfma_f32_16x16x32_bf16 v[204:207], a[28:31], v[144:147], v[204:207]\n"
      "v_cvt_pk_bf16_f32 v99, 0, v99\n"
      "v_lshlrev_b32 v100, 16, v72\n"
      "v_mfma_f32_16x16x32_bf16 v[208:211], a[248:251], v[136:139], 0\n"
      "v_and_b32 v101, 0xffff0000, v72\n"
      "v_lshlrev_b32 v102, 16, v73\n"
      "v_mfma_f32_16x16x32_bf16 v[208:211], a[12:15], v[140:143], v[208:211]\n"
      "v_and_b32 v103, 0xffff0000, v73\n"
      "v_add_f32 v96, v96, v100\n"
      "v_mfma_f32_16x16x32_bf16 v[208:211], a[32:35], v[144:147], v[208:211]\n"
      "v_add_f32 v97, v97, v101\n"
      "v_add_f32 v98, v98, v102\n"
      "v_mfma_f32_16x16x32_bf16 v[212:215], a[252:255], v[136:139], 0\n"
      "v_add_f32 v99, v99, v103\n"
      "v_cvt_pk_bf16_f32 v72, v96, v97\n"
      "v_mfma_f32_16x16x32_bf16 v[212:215], a[16:19], v[140:143], v[212:215]\n"
      "ds_write_b128 v56, v[176:179] offset:0\n"
      "v_cvt_pk_bf16_f32 v73, v98, v99\n"
      "v_mfma_f32_16x16x32_bf16 v[212:215], a[36:39], v[144:147], v[212:215]\n"
      "global_store_dwordx2 v55, v[72:73], s[28:29] offset:0 sc1\n"
      "s_waitcnt lgkmcnt(1)\n"
      "v_mfma_f32_16x16x32_bf16 v[216:219], a[0:3], v[136:139], 0\n"
      "ds_read_b128 v[96:99], v49 offset:2048\n"
      "v_add_f32 v112, v112, v116\n"
      "v_mfma_f32_16x16x32_bf16 v[216:219], a[20:23], v[140:143], v[216:219]\n"
      "ds_read_b128 v[100:103], v49 offset:7168\n"
      "v_add_f32 v113, v113, v117\n"
      "v_mfma_f32_16x16x32_bf16 v[216:219], a[40:43], v[144:147], v[216:219]\n"
      "ds_read_b128 v[104:107], v49 offset:12288\n"
      "v_add_f32 v114, v114, v118\n"
      "v_mfma_f32_16x16x32_bf16 v[200:203], a[44:47], v[148:151], v[200:203]\n"
      "ds_read_b128 v[108:111], v49 offset:17408\n"
      "v_add_f32 v115, v115, v119\n"
      "v_mfma_f32_16x16x32_bf16 v[200:203], a[64:67], v[152:155], v[200:203]\n"
      "v_add_f32 v120, v120, v124\n"
      "v_add_f32 v121, v121, v125\n"
      "v_mfma_f32_16x16x32_bf16 v[200:203], a[84:87], v[156:159], v[200:203]\n"
      "v_add_f32 v122, v122, v126\n"
      "v_add_f32 v123, v123, v127\n"
      "v_mfma_f32_16x16x32_bf16 v[204:207], a[48:51], v[148:151], v[204:207]\n"
      "v_add_f32 v112, v112, v120\n"
      "v_add_f32 v113, v113, v121\n"
      "v_mfma_f32_16x16x32_bf16 v[204:207], a[68:71], v[152:155], v[204:207]\n"
      "v_add_f32 v114, v114, v122\n"
      "v_add_f32 v115, v115, v123\n"
      "v_mfma_f32_16x16x32_bf16 v[204:207], a[88:91], v[156:159], v[204:207]\n"
      "v_cvt_pk_bf16_f32 v112, 0, v112\n"
      "v_cvt_pk_bf16_f32 v113, 0, v113\n"
      "v_mfma_f32_16x16x32_bf16 v[208:211], a[52:55], v[148:151], v[208:211]\n"
      "s_waitcnt vmcnt(1)\n"
      "v_cvt_pk_bf16_f32 v114, 0, v114\n"
      "v_mfma_f32_16x16x32_bf16 v[208:211], a[72:75], v[152:155], v[208:211]\n"
      "v_add3_u32 v2, v172, v173, s30\n"
      "v_add3_u32 v2, v2, v174, v175\n"
      "v_add3_u32 v2, v2, v160, v161\n"
      "v_cvt_pk_bf16_f32 v115, 0, v115\n"
      "v_mul_f32 v112, v112, v84\n"
      "v_mfma_f32_16x16x32_bf16 v[208:211], a[92:95], v[156:159], v[208:211]\n"
      "v_add3_u32 v2, v2, v162, v163\n"
      "v_add3_u32 v2, v2, v164, v165\n"
      "v_add3_u32 v2, v2, v166, v167\n"
      "v_mul_f32 v113, v113, v85\n"
      "v_mul_f32 v114, v114, v86\n"
      "v_mfma_f32_16x16x32_bf16 v[212:215], a[56:59], v[148:151], v[212:215]\n"
      "v_xor_b32 v2, v2, v26\n"
      "v_add3_u32 v3, v168, v169, s30\n"
      "v_add3_u32 v3, v3, v170, v171\n"
      "v_mul_f32 v115, v115, v87\n"
      "v_cvt_pk_bf16_f32 v112, 0, v112\n"
      "v_mfma_f32_16x16x32_bf16 v[212:215], a[76:79], v[152:155], v[212:215]\n"
      "ds_write_b128 v56, v[180:183] offset:1024\n"
      "v_add3_u32 v3, v3, v64, v65\n"
      "v_add3_u32 v3, v3, v66, v67\n"
      "v_add3_u32 v3, v3, v68, v69\n"
      "v_cvt_pk_bf16_f32 v113, 0, v113\n"
      "v_mfma_f32_16x16x32_bf16 v[212:215], a[96:99], v[156:159], v[212:215]\n"
      "v_add3_u32 v3, v3, v70, v71\n"
      "v_xor_b32 v3, v3, v27\n"
      "v_or_b32 v2, v2, v3\n"
      "v_cvt_pk_bf16_f32 v114, 0, v114\n"
      "v_cvt_pk_bf16_f32 v115, 0, v115\n"
      "v_mfma_f32_16x16x32_bf16 v[216:219], a[60:63], v[148:151], v[216:219]\n"
      "v_cmp_ne_u32 vcc, 0, v2\n"
      "v_lshlrev_b32 v116, 16, v74\n"
      "v_and_b32 v117, 0xffff0000, v74\n"
      "v_mfma_f32_16x16x32_bf16 v[216:219], a[80:83], v[152:155], v[216:219]\n"
      "v_lshlrev_b32 v118, 16, v75\n"
      "v_and_b32 v119, 0xffff0000, v75\n"
      "v_mfma_f32_16x16x32_bf16 v[216:219], a[100:103], v[156:159], v[216:219]\n"
      "ds_write_b128 v56, v[184:187] offset:2048\n"
      "s_cbranch_vccnz L_shB3_%=\n"
      "L_bhB3_%=:\n"
      "v_add_f32 v112, v112, v116\n"
      "v_add_f32 v113, v113, v117\n"
      "v_add_f32 v114, v114, v118\n"
      "v_mfma_f32_16x16x32_bf16 v[200:203], a[104:107], v[172:175], v[200:203]\n"
      "v_add_f32 v115, v115, v119\n"
      "v_cvt_pk_bf16_f32 v74, v112, v113\n"
      "v_mfma_f32_16x16x32_bf16 v[200:203], a[124:127], v[160:163], v[200:203]\n"
      "v_cvt_pk_bf16_f32 v75, v114, v115\n"
      "v_mfma_f32_16x16x32_bf16 v[200:203], a[144:147], v[164:167], v[200:203]\n"
      "global_store_dwordx2 v55, v[74:75], s[28:29] offset:32 sc1\n"
      "s_waitcnt lgkmcnt(2)\n"
      "v_mfma_f32_16x16x32_bf16 v[204:207], a[108:111], v[172:175], v[204:207]\n"
      "ds_write_b128 v56, v[188:191] offset:3072\n"
      "v_add_f32 v96, v96, v100\n"
      "v_add_f32 v97, v97, v101\n"
      "v_add_f32 v98, v98, v102\n"
      "v_add_f32 v99, v99, v103\n"
      "v_mfma_f32_16x16x32_bf16 v[204:207], a[128:131], v[160:163], v[204:207]\n"
      "v_add_f32 v104, v104, v108\n"
      "v_add_f32 v105, v105, v109\n"
      "v_mfma_f32_16x16x32_bf16 v[204:207], a[148:151], v[164:167], v[204:207]\n"
      "v_add_f32 v106, v106, v110\n"
      "v_add_f32 v107, v107, v111\n"
      "v_mfma_f32_16x16x32_bf16 v[208:211], a[112:115], v[172:175], v[208:211]\n"
      "v_add_f32 v96, v96, v104\n"
      "v_add_f32 v97, v97, v105\n"
      "v_mfma_f32_16x16x32_bf16 v[208:211], a[132:135], v[160:163], v[208:211]\n"
      "ds_write_b128 v56, v[192:195] offset:4096\n"
      "v_add_f32 v98, v98, v106\n"
      "v_add_f32 v99, v99, v107\n"
      "v_cvt_pk_bf16_f32 v96, 0, v96\n"
      "v_cvt_pk_bf16_f32 v97, 0, v97\n"
      "v_mfma_f32_16x16x32_bf16 v[208:211], a[152:155], v[164:167], v[208:211]\n"
      "v_cvt_pk_bf16_f32 v98, 0, v98\n"
      "v_cvt_pk_bf16_f32 v99, 0, v99\n"
      "v_mfma_f32_16x16x32_bf16 v[212:215], a[116:119], v[172:175], v[212:215]\n"
      "v_mul_f32 v96, v96, v88\n"
      "v_mul_f32 v97, v97, v89\n"
      "v_mfma_f32_16x16x32_bf16 v[212:215], a[136:139], v[160:163], v[212:215]\n"
      "v_mul_f32 v98, v98, v90\n"
      "v_mul_f32 v99, v99, v91\n"
      "v_mfma_f32_16x16x32_bf16 v[212:215], a[156:159], v[164:167], v[212:215]\n"
      "v_cvt_pk_bf16_f32 v96, 0, v96\n"
      "v_cvt_pk_bf16_f32 v97, 0, v97\n"
      "v_mfma_f32_16x16x32_bf16 v[216:219], a[120:123], v[172:175], v[216:219]\n"
      "v_cvt_pk_bf16_f32 v98, 0, v98\n"
      "L_pg7_%=:\n"
      "s_cbranch_execz L_pgr_%=\n"
      "v_cvt_pk_bf16_f32 v99, 0, v99\n"
      "v_mfma_f32_16x16x32_bf16 v[216:219], a[140:143], v[160:163], v[216:219]\n"
      "v_lshlrev_b32 v100, 16, v76\n"
      "v_and_b32 v101, 0xffff0000, v76\n"
      "v_mfma_f32_16x16x32_bf16 v[216:219], a[160:163], v[164:167], v[216:219]\n"
      "v_lshlrev_b32 v102, 16, v77\n"
      "v_and_b32 v103, 0xffff0000, v77\n"
      "v_mfma_f32_16x16x32_bf16 v[200:203], a[164:167], v[168:171], v[200:203]\n"
      "v_add_f32 v96, v96, v100\n"
      "v_add_f32 v97, v97, v101\n"
      "v_mfma_f32_16x16x32_bf16 v[200:203], a[184:187], v[64:67], v[200:203]\n"
      "v_add_f32 v98, v98, v102\n"
      "v_add_f32 v99, v99, v103\n"
      "v_mfma_f32_16x16x32_bf16 v[200:203], a[204:207], v[68:71], v[200:203]\n"
      "v_cvt_pk_bf16_f32 v76, v96, v97\n"
      "v_cvt_pk_bf16_f32 v77, v98, v99\n"
      "v_mfma_f32_16x16x32_bf16 v[204:207], a[168:171], v[168:171], v[204:207]\n"
      "global_store_dwordx2 v55, v[76:77], s[28:29] offset:64 sc1\n"
      "v_mfma_f32_16x16x32_bf16 v[204:207], a[188:191], v[64:67], v[204:207]\n"
      "v_mfma_f32_16x16x32_bf16 v[204:207], a[208:211], v[68:71], v[204:207]\n"
      "ds_write_b128 v56, v[200:203] offset:20480\n"
      "v_mfma_f32_16x16x32_bf16 v[208:211], a[172:175], v[168:171], v[208:211]\n"
      "v_mfma_f32_16x16x32_bf16 v[208:211], a[192:195], v[64:67], v[208:211]\n"
      "v_mfma_f32_16x16x32_bf16 v[208:211], a[212:215], v[68:71], v[208:211]\n"
      "ds_write_b128 v56, v[204:207] offset:21504\n"
      "v_mfma_f32_16x16x32_bf16 v[212:215], a[176:179], v[168:171], v[212:215]\n"
      "v_mfma_f32_16x16x32_bf16 v[212:215], a[196:199], v[64:67], v[212:215]\n"
      "v_mfma_f32_16x16x32_bf16 v[212:215], a[216:219], v[68:71], v[212:215]\n"
      "ds_write_b128 v56, v[208:211] offset:22528\n"
      "v_mfma_f32_16x16x32_bf16 v[216:219], a[180:183], v[168:171], v[216:219]\n"
      "v_mfma_f32_16x16x32_bf16 v[216:219], a[200:203], v[64:67], v[216:219]\n"
      "v_mfma_f32_16x16x32_bf16 v[216:219], a[220:223], v[68:71], v[216:219]\n"
      "ds_write_b128 v56, v[212:215] offset:23552\n"
      "v_xor_b32 v53, 64, v53\n"
      "ds_read_b64_tr_b16 v[64:65], v53 offset:0\n"
      "ds_read_b64_tr_b16 v[66:67], v53 offset:2048\n"
      "ds_read_b64_tr_b16 v[68:69], v53 offset:4096\n"
      "v_add_u32 v49, 0xa000, v49\n"
      "v_add_u32 v55, 81920, v55\n"
      "s_nop 0\n"
      "ds_write_b128 v56, v[216:219] offset:24576\n"
      "s_waitcnt lgkmcnt(0)\n"
      "s_barrier\n"
      "ds_read_b128 v[96:99], v49 offset:0\n"
      "ds_read_b128 v[100:103], v49 offset:5120\n"
      "ds_read_b128 v[104:107], v49 offset:10240\n"
      "ds_read_b128 v[108:111], v49 offset:15360\n"
      "ds_read_b128 v[112:115], v49 offset:1024\n"
      "ds_read_b128 v[116:119], v49 offset:6144\n"
      "ds_read_b128 v[120:123], v49 offset:11264\n"
      "ds_read_b128 v[124:127], v49 offset:16384\n"
      "ds_read_b128 v[128:131], v49 offset:2048\n"
      "ds_read_b128 v[132:135], v49 offset:7168\n"
      "ds_read_b128 v[136:139], v49 offset:12288\n"
      "ds_read_b128 v[140:143], v49 offset:17408\n"
      "v_lshlrev_b32 v144, 16, v64\n"
      "v_and_b32 v145, 0xffff0000, v64\n"
      "v_lshlrev_b32 v146, 16, v65\n"
      "v_and_b32 v147, 0xffff0000, v65\n"
      "v_lshlrev_b32 v148, 16, v66\n"
      "v_and_b32 v149, 0xffff0000, v66\n"
      "v_lshlrev_b32 v150, 16, v67\n"
      "v_and_b32 v151, 0xffff0000, v67\n"
      "v_lshlrev_b32 v152, 16, v68\n"
      "v_and_b32 v153, 0xffff0000, v68\n"
      "v_lshlrev_b32 v154, 16, v69\n"
      "v_and_b32 v155, 0xffff0000, v69\n"
      "s_waitcnt lgkmcnt(8)\n"
      "v_pk_add_f32 v[96:97], v[96:97], v[100:101]\n"
      "v_pk_add_f32 v[98:99], v[98:99], v[102:103]\n"
      "v_pk_add_f32 v[104:105], v[104:105], v[108:109]\n"
      "v_pk_add_f32 v[106:107], v[106:107], v[110:111]\n"
      "v_pk_add_f32 v[96:97], v[96:97], v[104:105]\n"
      "v_pk_add_f32 v[98:99], v[98:99], v[106:107]\n"
      "v_cvt_pk_bf16_f32 v96, 0, v96\n"
      "v_cvt_pk_bf16_f32 v97, 0, v97\n"
      "v_cvt_pk_bf16_f32 v98, 0, v98\n"
      "v_cvt_pk_bf16_f32 v99, 0, v99\n"
      "v_pk_mul_f32 v[96:97], v[96:97], v[80:81]\n"
      "v_pk_mul_f32 v[98:99], v[98:99], v[82:83]\n"
      "v_cvt_pk_bf16_f32 v96, 0, v96\n"
      "v_cvt_pk_bf16_f32 v97, 0, v97\n"
      "v_cvt_pk_bf16_f32 v98, 0, v98\n"
      "v_cvt_pk_bf16_f32 v99, 0, v99\n"
      "v_pk_add_f32 v[96:97], v[96:97], v[144:145]\n"
      "v_pk_add_f32 v[98:99], v[98:99], v[146:147]\n"
      "v_cvt_pk_bf16_f32 v96, v96, v97\n"
      "v_cvt_pk_bf16_f32 v97, v98, v99\n"
      "global_store_dwordx2 v55, v[96:97], s[28:29] offset:0\n"
      "s_waitcnt lgkmcnt(4)\n"
      "v_pk_add_f32 v[112:113], v[112:113], v[116:117]\n"
      "v_pk_add_f32 v[114:115], v[114:115], v[118:119]\n"
      "v_pk_add_f32 v[120:121], v[120:121], v[124:125]\n"
      "v_pk_add_f32 v[122:123], v[122:123], v[126:127]\n"
      "v_pk_add_f32 v[112:113], v[112:113], v[120:121]\n"
      "v_pk_add_f32 v[114:115], v[114:115], v[122:123]\n"
      "v_cvt_pk_bf16_f32 v112, 0, v112\n"
      "v_cvt_pk_bf16_f32 v113, 0, v113\n"
      "v_cvt_pk_bf16_f32 v114, 0, v114\n"
      "v_cvt_pk_bf16_f32 v115, 0, v115\n"
      "v_pk_mul_f32 v[112:113], v[112:113], v[84:85]\n"
      "v_pk_mul_f32 v[114:115], v[114:115], v[86:87]\n"
      "v_cvt_pk_bf16_f32 v112, 0, v112\n"
      "v_cvt_pk_bf16_f32 v113, 0, v113\n"
      "v_cvt_pk_bf16_f32 v114, 0, v114\n"
      "v_cvt_pk_bf16_f32 v115, 0, v115\n"
      "v_pk_add_f32 v[112:113], v[112:113], v[148:149]\n"
      "v_pk_add_f32 v[114:115], v[114:115], v[150:151]\n"
      "v_cvt_pk_bf16_f32 v112, v112, v113\n"
      "v_cvt_pk_bf16_f32 v113, v114, v115\n"
      "global_store_dwordx2 v55, v[112:113], s[28:29] offset:32\n"
      "s_waitcnt lgkmcnt(0)\n"
      "v_pk_add_f32 v[128:129], v[128:129], v[132:133]\n"
      "v_pk_add_f32 v[130:131], v[130:131], v[134:135]\n"
      "v_pk_add_f32 v[136:137], v[136:137], v[140:141]\n"
      "v_pk_add_f32 v[138:139], v[138:139], v[142:143]\n"
      "v_pk_add_f32 v[128:129], v[128:129], v[136:137]\n"
      "v_pk_add_f32 v[130:131], v[130:131], v[138:139]\n"
      "v_cvt_pk_bf16_f32 v128, 0, v128\n"
      "v_cvt_pk_bf16_f32 v129, 0, v129\n"
      "v_cvt_pk_bf16_f32 v130, 0, v130\n"
      "v_cvt_pk_bf16_f32 v131, 0, v131\n"
      "v_pk_mul_f32 v[128:129], v[128:129], v[88:89]\n"
      "v_pk_mul_f32 v[130:131], v[130:131], v[90:91]\n"
      "v_cvt_pk_bf16_f32 v128, 0, v128\n"
      "v_cvt_pk_bf16_f32 v129, 0, v129\n"
      "v_cvt_pk_bf16_f32 v130, 0, v130\n"
      "v_cvt_pk_bf16_f32 v131, 0, v131\n"
      "v_pk_add_f32 v[128:129], v[128:129], v[152:153]\n"
      "v_pk_add_f32 v[130:131], v[130:131], v[154:155]\n"
      "v_cvt_pk_bf16_f32 v128, v128, v129\n"
      "v_cvt_pk_bf16_f32 v129, v130, v131\n"
      "global_store_dwordx2 v55, v[128:129], s[28:29] offset:64\n"
      "s_or_b32 s40, s2, s60\n"
      "s_cmp_lg_u32 s40, 0\n"
      "s_cbranch_scc1 L_fin%=\n"
      "v_mov_b32 v8, s30\n"
      "v_mov_b32 v9, s31\n"
      "v_mov_b32 v10, 0\n"
      "s_mov_b64 exec, 1\n"
      "global_store_dwordx2 v10, v[8:9], s[26:27] sc1\n"
      "s_mov_b64 exec, -1\n"
      "L_fin%=:\n"
      "s_endpgm\n"
      "L_psx_%=:\n"
      "s_lshr_b32 s40, s92, 12\n"
      "s_lshr_b32 s41, s93, 12\n"
      "s_mulk_i32 s40, 4224\n"
      "s_mulk_i32 s41, 4224\n"
      "s_add_u32 s40, s40, 0x100000\n"
      "s_add_u32 s41, s41, 0x100000\n"
      "buffer_load_dwordx4 v[128:131], v54, s[32:35], s40 offen sc1\n"
      "s_add_u32 s40, s40, 1081344\n"
      "buffer_load_dwordx4 v[136:139], v54, s[32:35], s41 offen sc1\n"
      "buffer_load_dwordx4 v[132:135], v54, s[32:35], s40 offen sc1\n"
      "buffer_load_dword v140, v237, s[32:35], s70 offen sc1\n"
      "buffer_load_dword v141, v237, s[32:35], s98 offen sc1\n"
      "buffer_load_dword v142, v237, s[32:35], s71 offen sc1\n"
      "s_waitcnt vmcnt(0)\n"
      "v_add3_u32 v143, v128, v129, v130\n"
      "v_add3_u32 v143, v143, v131, v132\n"
      "v_add3_u32 v143, v143, v133, v134\n"
      "v_add3_u32 v143, v143, v135, v136\n"
      "v_add3_u32 v143, v143, v137, v138\n"
      "v_add_u32 v143, v143, v139\n"
      "v_add3_u32 v140, v140, v141, v142\n"
      "s_mul_i32 s47, s30, 3\n"
      "v_add_u32_dpp v143, v143, v143 quad_perm:[1,0,3,2] row_mask:0xf bank_mask:0xf\n"
      "s_nop 1\n"
      "v_add_u32_dpp v143, v143, v143 quad_perm:[2,3,0,1] row_mask:0xf bank_mask:0xf\n"
      "s_nop 1\n"
      "v_add_u32_dpp v143, v143, v143 row_half_mirror row_mask:0xf bank_mask:0xf\n"
      "v_add_u32 v143, v143, v140\n"
      "v_cmp_ne_u32 vcc, s47, v143\n"
      "s_mov_b64 exec, -1\n"
      "s_sub_u32 s44, s44, 1\n"
      "s_cbranch_vccz L_pbx_%=\n"
      "s_cmp_gt_i32 s44, 0\n"
      "s_cbranch_scc1 L_psx_%=\n"
      "s_branch L_pbx_%=\n"
      "L_cv_s%=:\n"
      "buffer_load_dwordx2 v[200:201], v4, s[32:35], s80 offen offset:0 sc1\n"
      "buffer_load_dword v250, v61, s[32:35], s85 offen offset:0 sc1\n"
      "buffer_load_dwordx4 v[224:227], v57, s[32:35], s82 offen offset:0 sc1\n"
      "buffer_load_dwordx4 v[228:231], v57, s[32:35], s82 offen offset:1024 sc1\n"
      "buffer_load_dwordx2 v[204:205], v58, s[32:35], s83 offen offset:0 sc1\n"
      "s_mov_b64 exec, -1\n"
      "s_waitcnt vmcnt(0)\n"
      "v_add_u32 v251, v200, v201\n"
      "v_add3_u32 v34, v250, v227, v231\n"
      "s_nop 0\n"
      "v_add_u32_dpp v251, v251, v251 row_half_mirror row_mask:0xf bank_mask:0xf\n"
      "s_nop 1\n"
      "v_add_u32_dpp v251, v251, v251 quad_perm:[1,0,3,2] row_mask:0xf bank_mask:0xf\n"
      "s_nop 1\n"
      "v_add_u32_dpp v251, v251, v251 quad_perm:[2,3,0,1] row_mask:0xf bank_mask:0xf\n"
      "v_add3_u32 v251, v251, v34, v205\n"
      "v_cmp_ne_u32 vcc, s24, v251\n"
      "s_mov_b64 exec, -1\n"
      "s_sub_u32 s44, s44, 1\n"
      "s_cbranch_vccz L_cv_r%=\n"
      "s_cmp_gt_i32 s44, 0\n"
      "s_cbranch_scc1 L_cv_s%=\n"
      "s_branch L_cv_r%=\n"
      "L_c2_s%=:\n"
      "buffer_load_dwordx4 v[192:195], v54, s[32:35], s53 offen offset:0 sc1\n"
      "buffer_load_dwordx4 v[196:199], v54, s[32:35], s54 offen offset:0 sc1\n"
      "buffer_load_dword v248, v60, s[32:35], s84 offen offset:0 sc1\n"
      "buffer_load_dword v249, v60, s[32:35], s84 offen offset:2048 sc1\n"
      "buffer_load_dwordx4 v[208:211], v55, s[32:35], 0 offen offset:0 sc1\n"
      "buffer_load_dwordx4 v[212:215], v55, s[32:35], 0 offen offset:1024 sc1\n"
      "buffer_load_dwordx4 v[216:219], v55, s[32:35], 0 offen offset:2048 sc1\n"
      "buffer_load_dwordx4 v[220:223], v55, s[32:35], 0 offen offset:3072 sc1\n"
      "s_mov_b64 exec, -1\n"
      "s_waitcnt vmcnt(0)\n"
      "v_add3_u32 v251, v192, v193, v194\n"
      "v_add3_u32 v251, v251, v195, v196\n"
      "v_add3_u32 v251, v251, v197, v198\n"
      "v_add_u32 v251, v251, v199\n"
      "v_add3_u32 v34, v248, v249, v211\n"
      "v_add3_u32 v34, v34, v215, v219\n"
      "v_add_u32_dpp v251, v251, v251 quad_perm:[1,0,3,2] row_mask:0xf bank_mask:0xf\n"
      "s_nop 1\n"
      "v_add_u32_dpp v251, v251, v251 quad_perm:[2,3,0,1] row_mask:0xf bank_mask:0xf\n"
      "v_add3_u32 v251, v251, v34, v223\n"
      "v_cmp_ne_u32 vcc, s47, v251\n"
      "s_mov_b64 exec, -1\n"
      "s_sub_u32 s44, s44, 1\n"
      "s_cbranch_vccz L_c2_r%=\n"
      "s_cmp_gt_i32 s44, 0\n"
      "s_cbranch_scc1 L_c2_s%=\n"
      "s_branch L_c2_r%=\n"
      "L_cw_s%=:\n"
      "buffer_load_dwordx2 v[252:253], v51, s[32:35], s76 offen offset:0 sc1\n"
      "s_waitcnt vmcnt(0)\n"
      "v_cmp_ne_u64 vcc, s[30:31], v[252:253]\n"
      "s_mov_b64 exec, -1\n"
      "s_sub_u32 s44, s44, 1\n"
      "s_cbranch_vccz L_cw_r%=\n"
      "s_cmp_gt_i32 s44, 0\n"
      "s_cbranch_scc1 L_cw_s%=\n"
      "s_branch L_cw_r%=\n"
      "L_cm_s%=:\n"
      "buffer_load_dwordx2 v[254:255], v2, s[32:35], s45 offen sc1\n"
      "s_waitcnt vmcnt(0)\n"
      "v_cmp_ne_u32 vcc, s30, v255\n"
      "s_mov_b64 exec, -1\n"
      "s_sub_u32 s44, s44, 1\n"
      "s_cbranch_vccz L_cm_r%=\n"
      "s_cmp_gt_i32 s44, 0\n"
      "s_cbranch_scc1 L_cm_s%=\n"
      "s_branch L_cm_r%=\n"
      "L_sx1_%=:\n"
      "s_mov_b32 s45, m0\n"
      "s_mov_b32 s58, s47\n"
      "s_add_u32 m0, s73, 16384\n"
      "v_add_u32 v248, 81920, v40\n"
      "global_load_lds_dwordx4 v248, s[4:5] offset:0 sc1\n"
      "global_load_lds_dwordx4 v248, s[4:5] offset:1024 sc1\n"
      "global_load_lds_dwordx4 v248, s[4:5] offset:2048 sc1\n"
      "global_load_lds_dwordx4 v248, s[4:5] offset:3072 sc1\n"
      "s_mov_b32 s47, 4\n"
      "buffer_load_dword v3, v5, s[32:35], s47 offen sc1\n"
      "s_waitcnt vmcnt(0)\n"
      "s_mov_b32 m0, s45\n"
      "s_mov_b32 s47, s58\n"
      "ds_read_b128 v[248:251], v40 offset:16384\n"
      "ds_read_b128 v[252:255], v40 offset:17408\n"
      "ds_read_b128 v[36:39], v40 offset:18432\n"
      "ds_read_b128 v[60:63], v40 offset:19456\n"
      "s_waitcnt lgkmcnt(0)\n"
      "v_add3_u32 v3, v3, v248, v249\n"
      "v_add3_u32 v3, v3, v250, v251\n"
      "v_add3_u32 v3, v3, v252, v253\n"
      "v_add3_u32 v3, v3, v254, v255\n"
      "v_add3_u32 v3, v3, v36, v37\n"
      "v_add3_u32 v3, v3, v38, v39\n"
      "v_add3_u32 v3, v3, v60, v61\n"
      "v_add3_u32 v3, v3, v62, v63\n"
      "s_nop 1\n"
      "v_add_u32_dpp v3, v3, v3 quad_perm:[1,0,3,2] row_mask:0xf bank_mask:0xf\n"
      "s_nop 1\n"
      "v_add_u32_dpp v3, v3, v3 quad_perm:[2,3,0,1] row_mask:0xf bank_mask:0xf\n"
      "s_nop 1\n"
      "v_add_u32_dpp v3, v3, v3 row_half_mirror row_mask:0xf bank_mask:0xf\n"
      "v_cmp_ne_u32 vcc, s71, v3\n"
      "s_mov_b64 exec, -1\n"
      "s_sub_u32 s44, s44, 1\n"
      "s_cbranch_vccz L_bx1_%=\n"
      "s_cmp_gt_i32 s44, 0\n"
      "s_cbranch_scc1 L_sx1_%=\n"
      "s_branch L_bx1_%=\n"
      "L_sx2_%=:\n"
      "s_mov_b32 s45, m0\n"
      "s_mov_b32 s58, s47\n"
      "s_add_u32 m0, s73, 32768\n"
      "v_add_u32 v248, 16384, v40\n"
      "global_load_lds_dwordx4 v248, s[4:5] offset:0 sc1\n"
      "global_load_lds_dwordx4 v248, s[4:5] offset:1024 sc1\n"
      "global_load_lds_dwordx4 v248, s[4:5] offset:2048 sc1\n"
      "global_load_lds_dwordx4 v248, s[4:5] offset:3072 sc1\n"
      "s_mov_b32 s47, 32768\n"
      "buffer_load_dwordx2 v[48:49], v5, s[32:35], s47 offen sc1\n"
      "s_waitcnt vmcnt(0)\n"
      "s_mov_b32 m0, s45\n"
      "s_mov_b32 s47, s58\n"
      "ds_read_b128 v[248:251], v40 offset:32768\n"
      "ds_read_b128 v[252:255], v40 offset:33792\n"
      "ds_read_b128 v[36:39], v40 offset:34816\n"
      "ds_read_b128 v[60:63], v40 offset:35840\n"
      "s_waitcnt lgkmcnt(0)\n"
      "v_add3_u32 v48, v48, v248, v249\n"
      "v_add3_u32 v48, v48, v250, v251\n"
      "v_add3_u32 v48, v48, v252, v253\n"
      "v_add3_u32 v48, v48, v254, v255\n"
      "v_add3_u32 v48, v48, v36, v37\n"
      "v_add3_u32 v48, v48, v38, v39\n"
      "v_add3_u32 v48, v48, v60, v61\n"
      "v_add3_u32 v48, v48, v62, v63\n"
      "s_nop 1\n"
      "v_add_u32_dpp v48, v48, v48 quad_perm:[1,0,3,2] row_mask:0xf bank_mask:0xf\n"
      "s_nop 1\n"
      "v_add_u32_dpp v48, v48, v48 quad_perm:[2,3,0,1] row_mask:0xf bank_mask:0xf\n"
      "s_nop 1\n"
      "v_add_u32_dpp v48, v48, v48 row_half_mirror row_mask:0xf bank_mask:0xf\n"
      "v_cmp_ne_u32 vcc, s71, v48\n"
      "s_mov_b64 exec, -1\n"
      "s_sub_u32 s44, s44, 1\n"
      "s_cbranch_vccz L_bx2_%=\n"
      "s_cmp_gt_i32 s44, 0\n"
      "s_cbranch_scc1 L_sx2_%=\n"
      "s_branch L_bx2_%=\n"
      "L_sx3_%=:\n"
      "s_mov_b32 s45, m0\n"
      "s_mov_b32 s58, s47\n"
      "s_add_u32 m0, s73, 49152\n"
      "v_add_u32 v248, 32768, v40\n"
      "global_load_lds_dwordx4 v248, s[4:5] offset:0 sc1\n"
      "global_load_lds_dwordx4 v248, s[4:5] offset:1024 sc1\n"
      "global_load_lds_dwordx4 v248, s[4:5] offset:2048 sc1\n"
      "global_load_lds_dwordx4 v248, s[4:5] offset:3072 sc1\n"
      "s_mov_b32 s47, 65536\n"
      "buffer_load_dwordx2 v[54:55], v5, s[32:35], s47 offen sc1\n"
      "s_waitcnt vmcnt(0)\n"
      "s_mov_b32 m0, s45\n"
      "s_mov_b32 s47, s58\n"
      "ds_read_b128 v[248:251], v40 offset:49152\n"
      "ds_read_b128 v[252:255], v40 offset:50176\n"
      "ds_read_b128 v[36:39], v40 offset:51200\n"
      "ds_read_b128 v[60:63], v40 offset:52224\n"
      "s_waitcnt lgkmcnt(0)\n"
      "v_add3_u32 v54, v54, v248, v249\n"
      "v_add3_u32 v54, v54, v250, v251\n"
      "v_add3_u32 v54, v54, v252, v253\n"
      "v_add3_u32 v54, v54, v254, v255\n"
      "v_add3_u32 v54, v54, v36, v37\n"
      "v_add3_u32 v54, v54, v38, v39\n"
      "v_add3_u32 v54, v54, v60, v61\n"
      "v_add3_u32 v54, v54, v62, v63\n"
      "s_nop 1\n"
      "v_add_u32_dpp v54, v54, v54 quad_perm:[1,0,3,2] row_mask:0xf bank_mask:0xf\n"
      "s_nop 1\n"
      "v_add_u32_dpp v54, v54, v54 quad_perm:[2,3,0,1] row_mask:0xf bank_mask:0xf\n"
      "s_nop 1\n"
      "v_add_u32_dpp v54, v54, v54 row_half_mirror row_mask:0xf bank_mask:0xf\n"
      "v_cmp_ne_u32 vcc, s71, v54\n"
      "s_mov_b64 exec, -1\n"
      "s_sub_u32 s44, s44, 1\n"
      "s_cbranch_vccz L_bx3_%=\n"
      "s_cmp_gt_i32 s44, 0\n"
      "s_cbranch_scc1 L_sx3_%=\n"
      "s_branch L_bx3_%=\n"
      "L_sx4_%=:\n"
      "s_mov_b32 s45, m0\n"
      "s_mov_b32 s58, s47\n"
      "s_add_u32 m0, s73, 0\n"
      "v_add_u32 v248, 49152, v40\n"
      "global_load_lds_dwordx4 v248, s[4:5] offset:0 sc1\n"
      "global_load_lds_dwordx4 v248, s[4:5] offset:1024 sc1\n"
      "global_load_lds_dwordx4 v248, s[4:5] offset:2048 sc1\n"
      "global_load_lds_dwordx4 v248, s[4:5] offset:3072 sc1\n"
      "s_mov_b32 s47, 98304\n"
      "buffer_load_dwordx2 v[56:57], v5, s[32:35], s47 offen sc1\n"
      "s_waitcnt vmcnt(0)\n"
      "s_mov_b32 m0, s45\n"
      "s_mov_b32 s47, s58\n"
      "ds_read_b128 v[248:251], v40 offset:0\n"
      "ds_read_b128 v[252:255], v40 offset:1024\n"
      "ds_read_b128 v[36:39], v40 offset:2048\n"
      "ds_read_b128 v[60:63], v40 offset:3072\n"
      "s_waitcnt lgkmcnt(0)\n"
      "v_add3_u32 v56, v56, v248, v249\n"
      "v_add3_u32 v56, v56, v250, v251\n"
      "v_add3_u32 v56, v56, v252, v253\n"
      "v_add3_u32 v56, v56, v254, v255\n"
      "v_add3_u32 v56, v56, v36, v37\n"
      "v_add3_u32 v56, v56, v38, v39\n"
      "v_add3_u32 v56, v56, v60, v61\n"
      "v_add3_u32 v56, v56, v62, v63\n"
      "s_nop 1\n"
      "v_add_u32_dpp v56, v56, v56 quad_perm:[1,0,3,2] row_mask:0xf bank_mask:0xf\n"
      "s_nop 1\n"
      "v_add_u32_dpp v56, v56, v56 quad_perm:[2,3,0,1] row_mask:0xf bank_mask:0xf\n"
      "s_nop 1\n"
      "v_add_u32_dpp v56, v56, v56 row_half_mirror row_mask:0xf bank_mask:0xf\n"
      "v_cmp_ne_u32 vcc, s71, v56\n"
      "s_mov_b64 exec, -1\n"
      "s_sub_u32 s44, s44, 1\n"
      "s_cbranch_vccz L_bx4_%=\n"
      "s_cmp_gt_i32 s44, 0\n"
      "s_cbranch_scc1 L_sx4_%=\n"
      "s_branch L_bx4_%=\n"
      "L_sx5_%=:\n"
      "s_mov_b32 s45, m0\n"
      "s_mov_b32 s58, s47\n"
      "s_add_u32 m0, s73, 16384\n"
      "v_add_u32 v248, 65536, v40\n"
      "global_load_lds_dwordx4 v248, s[4:5] offset:0 sc1\n"
      "global_load_lds_dwordx4 v248, s[4:5] offset:1024 sc1\n"
      "global_load_lds_dwordx4 v248, s[4:5] offset:2048 sc1\n"
      "global_load_lds_dwordx4 v248, s[4:5] offset:3072 sc1\n"
      "s_mov_b32 s47, 131072\n"
      "buffer_load_dwordx2 v[2:3], v5, s[32:35], s47 offen sc1\n"
      "s_waitcnt vmcnt(0)\n"
      "s_mov_b32 m0, s45\n"
      "s_mov_b32 s47, s58\n"
      "ds_read_b128 v[248:251], v40 offset:16384\n"
      "ds_read_b128 v[252:255], v40 offset:17408\n"
      "ds_read_b128 v[36:39], v40 offset:18432\n"
      "ds_read_b128 v[60:63], v40 offset:19456\n"
      "s_waitcnt lgkmcnt(0)\n"
      "v_add3_u32 v2, v2, v248, v249\n"
      "v_add3_u32 v2, v2, v250, v251\n"
      "v_add3_u32 v2, v2, v252, v253\n"
      "v_add3_u32 v2, v2, v254, v255\n"
      "v_add3_u32 v2, v2, v36, v37\n"
      "v_add3_u32 v2, v2, v38, v39\n"
      "v_add3_u32 v2, v2, v60, v61\n"
      "v_add3_u32 v2, v2, v62, v63\n"
      "s_nop 1\n"
      "v_add_u32_dpp v2, v2, v2 quad_perm:[1,0,3,2] row_mask:0xf bank_mask:0xf\n"
      "s_nop 1\n"
      "v_add_u32_dpp v2, v2, v2 quad_perm:[2,3,0,1] row_mask:0xf bank_mask:0xf\n"
      "s_nop 1\n"
      "v_add_u32_dpp v2, v2, v2 row_half_mirror row_mask:0xf bank_mask:0xf\n"
      "v_cmp_ne_u32 vcc, s71, v2\n"
      "s_mov_b64 exec, -1\n"
      "s_sub_u32 s44, s44, 1\n"
      "s_cbranch_vccz L_bx5_%=\n"
      "s_cmp_gt_i32 s44, 0\n"
      "s_cbranch_scc1 L_sx5_%=\n"
      "s_branch L_bx5_%=\n"
      "L_sx6_%=:\n"
      "s_mov_b32 s45, m0\n"
      "s_mov_b32 s58, s47\n"
      "s_add_u32 m0, s73, 32768\n"
      "v_add_u32 v248, 98304, v40\n"
      "global_load_lds_dwordx4 v248, s[4:5] offset:0 sc1\n"
      "global_load_lds_dwordx4 v248, s[4:5] offset:1024 sc1\n"
      "global_load_lds_dwordx4 v248, s[4:5] offset:2048 sc1\n"
      "global_load_lds_dwordx4 v248, s[4:5] offset:3072 sc1\n"
      "s_mov_b32 s47, 32772\n"
      "buffer_load_dword v49, v5, s[32:35], s47 offen sc1\n"
      "s_waitcnt vmcnt(0)\n"
      "s_mov_b32 m0, s45\n"
      "s_mov_b32 s47, s58\n"
      "ds_read_b128 v[248:251], v40 offset:32768\n"
      "ds_read_b128 v[252:255], v40 offset:33792\n"
      "ds_read_b128 v[36:39], v40 offset:34816\n"
      "ds_read_b128 v[60:63], v40 offset:35840\n"
      "s_waitcnt lgkmcnt(0)\n"
      "v_add3_u32 v49, v49, v248, v249\n"
      "v_add3_u32 v49, v49, v250, v251\n"
      "v_add3_u32 v49, v49, v252, v253\n"
      "v_add3_u32 v49, v49, v254, v255\n"
      "v_add3_u32 v49, v49, v36, v37\n"
      "v_add3_u32 v49, v49, v38, v39\n"
      "v_add3_u32 v49, v49, v60, v61\n"
      "v_add3_u32 v49, v49, v62, v63\n"
      "s_nop 1\n"
      "v_add_u32_dpp v49, v49, v49 quad_perm:[1,0,3,2] row_mask:0xf bank_mask:0xf\n"
      "s_nop 1\n"
      "v_add_u32_dpp v49, v49, v49 quad_perm:[2,3,0,1] row_mask:0xf bank_mask:0xf\n"
      "s_nop 1\n"
      "v_add_u32_dpp v49, v49, v49 row_half_mirror row_mask:0xf bank_mask:0xf\n"
      "v_cmp_ne_u32 vcc, s71, v49\n"
      "s_mov_b64 exec, -1\n"
      "s_sub_u32 s44, s44, 1\n"
      "s_cbranch_vccz L_bx6_%=\n"
      "s_cmp_gt_i32 s44, 0\n"
      "s_cbranch_scc1 L_sx6_%=\n"
      "s_branch L_bx6_%=\n"
      "L_sx7_%=:\n"
      "s_mov_b32 s45, m0\n"
      "s_mov_b32 s58, s47\n"
      "s_add_u32 m0, s73, 49152\n"
      "v_add_u32 v248, 114688, v40\n"
      "global_load_lds_dwordx4 v248, s[4:5] offset:0 sc1\n"
      "global_load_lds_dwordx4 v248, s[4:5] offset:1024 sc1\n"
      "global_load_lds_dwordx4 v248, s[4:5] offset:2048 sc1\n"
      "global_load_lds_dwordx4 v248, s[4:5] offset:3072 sc1\n"
      "s_mov_b32 s47, 65540\n"
      "buffer_load_dword v55, v5, s[32:35], s47 offen sc1\n"
      "s_waitcnt vmcnt(0)\n"
      "s_mov_b32 m0, s45\n"
      "s_mov_b32 s47, s58\n"
      "ds_read_b128 v[248:251], v40 offset:49152\n"
      "ds_read_b128 v[252:255], v40 offset:50176\n"
      "ds_read_b128 v[36:39], v40 offset:51200\n"
      "ds_read_b128 v[60:63], v40 offset:52224\n"
      "s_waitcnt lgkmcnt(0)\n"
      "v_add3_u32 v55, v55, v248, v249\n"
      "v_add3_u32 v55, v55, v250, v251\n"
      "v_add3_u32 v55, v55, v252, v253\n"
      "v_add3_u32 v55, v55, v254, v255\n"
      "v_add3_u32 v55, v55, v36, v37\n"
      "v_add3_u32 v55, v55, v38, v39\n"
      "v_add3_u32 v55, v55, v60, v61\n"
      "v_add3_u32 v55, v55, v62, v63\n"
      "s_nop 1\n"
      "v_add_u32_dpp v55, v55, v55 quad_perm:[1,0,3,2] row_mask:0xf bank_mask:0xf\n"
      "s_nop 1\n"
      "v_add_u32_dpp v55, v55, v55 quad_perm:[2,3,0,1] row_mask:0xf bank_mask:0xf\n"
      "s_nop 1\n"
      "v_add_u32_dpp v55, v55, v55 row_half_mirror row_mask:0xf bank_mask:0xf\n"
      "v_cmp_ne_u32 vcc, s71, v55\n"
      "s_mov_b64 exec, -1\n"
      "s_sub_u32 s44, s44, 1\n"
      "s_cbranch_vccz L_bx7_%=\n"
      "s_cmp_gt_i32 s44, 0\n"
      "s_cbranch_scc1 L_sx7_%=\n"
      "s_branch L_bx7_%=\n"
      "L_sx8_%=:\n"
      "s_mov_b32 s45, m0\n"
      "s_mov_b32 s58, s47\n"
      "s_add_u32 m0, s73, 0\n"
      "v_add_u32 v248, 131072, v40\n"
      "global_load_lds_dwordx4 v248, s[4:5] offset:0 sc1\n"
      "global_load_lds_dwordx4 v248, s[4:5] offset:1024 sc1\n"
      "global_load_lds_dwordx4 v248, s[4:5] offset:2048 sc1\n"
      "global_load_lds_dwordx4 v248, s[4:5] offset:3072 sc1\n"
      "s_mov_b32 s47, 98308\n"
      "buffer_load_dword v57, v5, s[32:35], s47 offen sc1\n"
      "s_waitcnt vmcnt(0)\n"
      "s_mov_b32 m0, s45\n"
      "s_mov_b32 s47, s58\n"
      "ds_read_b128 v[248:251], v40 offset:0\n"
      "ds_read_b128 v[252:255], v40 offset:1024\n"
      "ds_read_b128 v[36:39], v40 offset:2048\n"
      "ds_read_b128 v[60:63], v40 offset:3072\n"
      "s_waitcnt lgkmcnt(0)\n"
      "v_add3_u32 v57, v57, v248, v249\n"
      "v_add3_u32 v57, v57, v250, v251\n"
      "v_add3_u32 v57, v57, v252, v253\n"
      "v_add3_u32 v57, v57, v254, v255\n"
      "v_add3_u32 v57, v57, v36, v37\n"
      "v_add3_u32 v57, v57, v38, v39\n"
      "v_add3_u32 v57, v57, v60, v61\n"
      "v_add3_u32 v57, v57, v62, v63\n"
      "s_nop 1\n"
      "v_add_u32_dpp v57, v57, v57 quad_perm:[1,0,3,2] row_mask:0xf bank_mask:0xf\n"
      "s_nop 1\n"
      "v_add_u32_dpp v57, v57, v57 quad_perm:[2,3,0,1] row_mask:0xf bank_mask:0xf\n"
      "s_nop 1\n"
      "v_add_u32_dpp v57, v57, v57 row_half_mirror row_mask:0xf bank_mask:0xf\n"
      "v_cmp_ne_u32 vcc, s71, v57\n"
      "s_mov_b64 exec, -1\n"
      "s_sub_u32 s44, s44, 1\n"
      "s_cbranch_vccz L_bx8_%=\n"
      "s_cmp_gt_i32 s44, 0\n"
      "s_cbranch_scc1 L_sx8_%=\n"
      "s_branch L_bx8_%=\n"
      "L_sx9_%=:\n"
      "s_mov_b32 s45, m0\n"
      "s_mov_b32 s58, s47\n"
      "s_add_u32 m0, s73, 16384\n"
      "v_add_u32 v248, 147456, v40\n"
      "global_load_lds_dwordx4 v248, s[4:5] offset:0 sc1\n"
      "global_load_lds_dwordx4 v248, s[4:5] offset:1024 sc1\n"
      "global_load_lds_dwordx4 v248, s[4:5] offset:2048 sc1\n"
      "global_load_lds_dwordx4 v248, s[4:5] offset:3072 sc1\n"
      "s_mov_b32 s47, 131076\n"
      "buffer_load_dword v3, v5, s[32:35], s47 offen sc1\n"
      "s_waitcnt vmcnt(0)\n"
      "s_mov_b32 m0, s45\n"
      "s_mov_b32 s47, s58\n"
      "ds_read_b128 v[248:251], v40 offset:16384\n"
      "ds_read_b128 v[252:255], v40 offset:17408\n"
      "ds_read_b128 v[36:39], v40 offset:18432\n"
      "ds_read_b128 v[60:63], v40 offset:19456\n"
      "s_waitcnt lgkmcnt(0)\n"
      "v_add3_u32 v3, v3, v248, v249\n"
      "v_add3_u32 v3, v3, v250, v251\n"
      "v_add3_u32 v3, v3, v252, v253\n"
      "v_add3_u32 v3, v3, v254, v255\n"
      "v_add3_u32 v3, v3, v36, v37\n"
      "v_add3_u32 v3, v3, v38, v39\n"
      "v_add3_u32 v3, v3, v60, v61\n"
      "v_add3_u32 v3, v3, v62, v63\n"
      "s_nop 1\n"
      "v_add_u32_dpp v3, v3, v3 quad_perm:[1,0,3,2] row_mask:0xf bank_mask:0xf\n"
      "s_nop 1\n"
      "v_add_u32_dpp v3, v3, v3 quad_perm:[2,3,0,1] row_mask:0xf bank_mask:0xf\n"
      "s_nop 1\n"
      "v_add_u32_dpp v3, v3, v3 row_half_mirror row_mask:0xf bank_mask:0xf\n"
      "v_cmp_ne_u32 vcc, s71, v3\n"
      "s_mov_b64 exec, -1\n"
      "s_sub_u32 s44, s44, 1\n"
      "s_cbranch_vccz L_bx9_%=\n"
      "s_cmp_gt_i32 s44, 0\n"
      "s_cbranch_scc1 L_sx9_%=\n"
      "s_branch L_bx9_%=\n"
      "L_sx0_%=:\n"
      "s_mov_b32 s45, m0\n"
      "s_mov_b32 s58, s47\n"
      "s_add_u32 m0, s73, 0\n"
      "v_add_u32 v248, 0, v40\n"
      "global_load_lds_dwordx4 v248, s[4:5] offset:0 sc1\n"
      "global_load_lds_dwordx4 v248, s[4:5] offset:1024 sc1\n"
      "global_load_lds_dwordx4 v248, s[4:5] offset:2048 sc1\n"
      "global_load_lds_dwordx4 v248, s[4:5] offset:3072 sc1\n"
      "s_mov_b32 s47, 0\n"
      "buffer_load_dwordx2 v[2:3], v5, s[32:35], s47 offen sc1\n"
      "s_waitcnt vmcnt(0)\n"
      "s_mov_b32 m0, s45\n"
      "s_mov_b32 s47, s58\n"
      "ds_read_b128 v[248:251], v40 offset:0\n"
      "ds_read_b128 v[252:255], v40 offset:1024\n"
      "ds_read_b128 v[36:39], v40 offset:2048\n"
      "ds_read_b128 v[60:63], v40 offset:3072\n"
      "s_waitcnt lgkmcnt(0)\n"
      "v_add3_u32 v2, v2, v248, v249\n"
      "v_add3_u32 v2, v2, v250, v251\n"
      "v_add3_u32 v2, v2, v252, v253\n"
      "v_add3_u32 v2, v2, v254, v255\n"
      "v_add3_u32 v2, v2, v36, v37\n"
      "v_add3_u32 v2, v2, v38, v39\n"
      "v_add3_u32 v2, v2, v60, v61\n"
      "v_add3_u32 v2, v2, v62, v63\n"
      "s_nop 1\n"
      "v_add_u32_dpp v2, v2, v2 quad_perm:[1,0,3,2] row_mask:0xf bank_mask:0xf\n"
      "s_nop 1\n"
      "v_add_u32_dpp v2, v2, v2 quad_perm:[2,3,0,1] row_mask:0xf bank_mask:0xf\n"
      "s_nop 1\n"
      "v_add_u32_dpp v2, v2, v2 row_half_mirror row_mask:0xf bank_mask:0xf\n"
      "v_cmp_ne_u32 vcc, s71, v2\n"
      "s_mov_b64 exec, -1\n"
      "s_sub_u32 s44, s44, 1\n"
      "s_cbranch_vccz L_bx0_%=\n"
      "s_cmp_gt_i32 s44, 0\n"
      "s_cbranch_scc1 L_sx0_%=\n"
      "s_branch L_bx0_%=\n"
      "L_sfd_%=:\n"
      "global_load_dwordx2 v[58:59], v42, s[26:27] sc1\n"
      "buffer_load_dwordx2 v[46:47], v51, s[32:35], s75 offen sc1\n"
      "s_waitcnt vmcnt(0)\n"
      "v_cmp_ne_u64 s[52:53], s[30:31], v[46:47]\n"
      "v_cmp_ne_u64 vcc, s[30:31], v[58:59]\n"
      "s_or_b64 vcc, vcc, s[52:53]\n"
      "s_mov_b64 exec, -1\n"
      "s_sub_u32 s44, s44, 1\n"
      "s_cbranch_vccz L_bfd_%=\n"
      "s_cmp_gt_i32 s44, 0\n"
      "s_cbranch_scc1 L_sfd_%=\n"
      "s_branch L_bfd_%=\n"
      "L_sxp_%=:\n"
      "s_mov_b32 s45, m0\n"
      "s_mov_b32 s58, s47\n"
      "s_mov_b32 m0, s94\n"
      "s_nop 0\n"
      "buffer_load_dwordx4 v7, s[32:35], s67 offen offset:0 sc1 lds\n"
      "buffer_load_dwordx4 v7, s[32:35], s67 offen offset:1024 sc1 lds\n"
      "buffer_load_dwordx4 v7, s[32:35], s67 offen offset:2048 sc1 lds\n"
      "buffer_load_dword v32, v31, s[32:35], s68 offen sc1\n"
      "s_waitcnt vmcnt(0)\n"
      "s_mov_b32 m0, s45\n"
      "s_mov_b32 s47, s58\n"
      "ds_read_b128 v[248:251], v50 offset:0\n"
      "ds_read_b128 v[252:255], v50 offset:1024\n"
      "ds_read_b128 v[36:39], v50 offset:2048\n"
      "s_waitcnt lgkmcnt(0)\n"
      "v_add3_u32 v32, v32, v248, v249\n"
      "v_add3_u32 v32, v32, v250, v251\n"
      "v_add3_u32 v32, v32, v252, v253\n"
      "v_add3_u32 v32, v32, v254, v255\n"
      "v_add3_u32 v32, v32, v36, v37\n"
      "v_add3_u32 v32, v32, v38, v39\n"
      "s_nop 1\n"
      "v_add_u32_dpp v32, v32, v32 quad_perm:[1,0,3,2] row_mask:0xf bank_mask:0xf\n"
      "s_nop 1\n"
      "v_add_u32_dpp v32, v32, v32 quad_perm:[2,3,0,1] row_mask:0xf bank_mask:0xf\n"
      "s_nop 1\n"
      "v_add_u32_dpp v32, v32, v32 row_half_mirror row_mask:0xf bank_mask:0xf\n"
      "v_cmp_ne_u32 vcc, s72, v32\n"
      "s_mov_b64 exec, -1\n"
      "s_sub_u32 s44, s44, 1\n"
      "s_cbranch_vccz L_bxp_%=\n"
      "s_cmp_gt_i32 s44, 0\n"
      "s_cbranch_scc1 L_sxp_%=\n"
      "s_branch L_bxp_%=\n"
      "L_sg_%=:\n"
      "buffer_load_dwordx4 v[8:11], v50, s[32:35], s93 offen offset:0 sc1\n"
      "buffer_load_dwordx4 v[12:15], v50, s[32:35], s93 offen offset:1024 sc1\n"
      "buffer_load_dwordx4 v[16:19], v50, s[32:35], s93 offen offset:2048 sc1\n"
      "buffer_load_dwordx4 v[20:23], v50, s[32:35], s93 offen offset:3072 sc1\n"
      "s_waitcnt vmcnt(0)\n"
      "v_add3_u32 v10, v10, v14, v18\n"
      "v_add_u32 v10, v10, v22\n"
      "v_cmp_ne_u32 vcc, s71, v10\n"
      "s_mov_b64 exec, -1\n"
      "s_sub_u32 s44, s44, 1\n"
      "s_cbranch_vccz L_bg_%=\n"
      "s_cmp_gt_i32 s44, 0\n"
      "s_cbranch_scc1 L_sg_%=\n"
      "s_branch L_bg_%=\n"
      "L_shA0_%=:\n"
      "v_add_u32 v2, 4096, v7\n"
      "global_load_dwordx4 v[8:11], v2, s[10:11] offset:-4096 sc1\n"
      "global_load_dwordx4 v[16:19], v2, s[10:11] offset:-3072 sc1\n"
      "global_load_dwordx4 v[128:131], v2, s[10:11] offset:-2048 sc1\n"
      "v_add_u32 v2, 4096, v7\n"
      "global_load_dwordx4 v[12:15], v2, s[12:13] offset:-4096 sc1\n"
      "global_load_dwordx4 v[20:23], v2, s[12:13] offset:-3072 sc1\n"
      "global_load_dwordx4 v[132:135], v2, s[12:13] offset:-2048 sc1\n"
      "buffer_load_dword v36, v42, s[32:35], s63 offen offset:0 sc1\n"
      "buffer_load_dword v37, v42, s[32:35], s63 offen offset:256 sc1\n"
      "s_waitcnt vmcnt(0)\n"
      "v_add3_u32 v2, v8, v9, s30\n"
      "v_add3_u32 v2, v2, v10, v11\n"
      "v_add3_u32 v2, v2, v16, v17\n"
      "v_add3_u32 v2, v2, v18, v19\n"
      "v_add3_u32 v2, v2, v128, v129\n"
      "v_add3_u32 v2, v2, v130, v131\n"
      "v_xor_b32 v2, v2, v36\n"
      "v_add3_u32 v3, v12, v13, s30\n"
      "v_add3_u32 v3, v3, v14, v15\n"
      "v_add3_u32 v3, v3, v20, v21\n"
      "v_add3_u32 v3, v3, v22, v23\n"
      "v_add3_u32 v3, v3, v132, v133\n"
      "v_add3_u32 v3, v3, v134, v135\n"
      "v_xor_b32 v3, v3, v37\n"
      "v_or_b32 v2, v2, v3\n"
      "v_cmp_ne_u32 vcc, 0, v2\n"
      "s_mov_b64 exec, -1\n"
      "s_sub_u32 s44, s44, 1\n"
      "s_cbranch_vccz L_bhA0_%=\n"
      "s_cmp_gt_i32 s44, 0\n"
      "s_cbranch_scc1 L_shA0_%=\n"
      "s_branch L_bhA0_%=\n"
      "L_shA1_%=:\n"
      "v_add_u32 v2, 4096, v7\n"
      "global_load_dwordx4 v[136:139], v2, s[10:11] offset:-1024 sc1\n"
      "global_load_dwordx4 v[144:147], v2, s[10:11] offset:0 sc1\n"
      "global_load_dwordx4 v[152:155], v2, s[10:11] offset:1024 sc1\n"
      "v_add_u32 v2, 4096, v7\n"
      "global_load_dwordx4 v[140:143], v2, s[12:13] offset:-1024 sc1\n"
      "global_load_dwordx4 v[148:151], v2, s[12:13] offset:0 sc1\n"
      "global_load_dwordx4 v[156:159], v2, s[12:13] offset:1024 sc1\n"
      "buffer_load_dword v38, v42, s[32:35], s63 offen offset:512 sc1\n"
      "buffer_load_dword v39, v42, s[32:35], s63 offen offset:768 sc1\n"
      "s_waitcnt vmcnt(0)\n"
      "v_add3_u32 v2, v136, v137, s30\n"
      "v_add3_u32 v2, v2, v138, v139\n"
      "v_add3_u32 v2, v2, v144, v145\n"
      "v_add3_u32 v2, v2, v146, v147\n"
      "v_add3_u32 v2, v2, v152, v153\n"
      "v_add3_u32 v2, v2, v154, v155\n"
      "v_xor_b32 v2, v2, v38\n"
      "v_add3_u32 v3, v140, v141, s30\n"
      "v_add3_u32 v3, v3, v142, v143\n"
      "v_add3_u32 v3, v3, v148, v149\n"
      "v_add3_u32 v3, v3, v150, v151\n"
      "v_add3_u32 v3, v3, v156, v157\n"
      "v_add3_u32 v3, v3, v158, v159\n"
      "v_xor_b32 v3, v3, v39\n"
      "v_or_b32 v2, v2, v3\n"
      "v_cmp_ne_u32 vcc, 0, v2\n"
      "s_mov_b64 exec, -1\n"
      "s_sub_u32 s44, s44, 1\n"
      "s_cbranch_vccz L_bhA1_%=\n"
      "s_cmp_gt_i32 s44, 0\n"
      "s_cbranch_scc1 L_shA1_%=\n"
      "s_branch L_bhA1_%=\n"
      "L_shA2_%=:\n"
      "v_add_u32 v2, 10240, v7\n"
      "global_load_dwordx4 v[172:175], v2, s[10:11] offset:-4096 sc1\n"
      "global_load_dwordx4 v[164:167], v2, s[10:11] offset:-3072 sc1\n"
      "global_load_dwordx4 v[64:67], v2, s[10:11] offset:-2048 sc1\n"
      "v_add_u32 v2, 10240, v7\n"
      "global_load_dwordx4 v[160:163], v2, s[12:13] offset:-4096 sc1\n"
      "global_load_dwordx4 v[168:171], v2, s[12:13] offset:-3072 sc1\n"
      "global_load_dwordx4 v[68:71], v2, s[12:13] offset:-2048 sc1\n"
      "buffer_load_dword v60, v42, s[32:35], s63 offen offset:1024 sc1\n"
      "buffer_load_dword v61, v42, s[32:35], s63 offen offset:1280 sc1\n"
      "s_waitcnt vmcnt(0)\n"
      "v_add3_u32 v2, v172, v173, s30\n"
      "v_add3_u32 v2, v2, v174, v175\n"
      "v_add3_u32 v2, v2, v164, v165\n"
      "v_add3_u32 v2, v2, v166, v167\n"
      "v_add3_u32 v2, v2, v64, v65\n"
      "v_add3_u32 v2, v2, v66, v67\n"
      "v_xor_b32 v2, v2, v60\n"
      "v_add3_u32 v3, v160, v161, s30\n"
      "v_add3_u32 v3, v3, v162, v163\n"
      "v_add3_u32 v3, v3, v168, v169\n"
      "v_add3_u32 v3, v3, v170, v171\n"
      "v_add3_u32 v3, v3, v68, v69\n"
      "v_add3_u32 v3, v3, v70, v71\n"
      "v_xor_b32 v3, v3, v61\n"
      "v_or_b32 v2, v2, v3\n"
      "v_cmp_ne_u32 vcc, 0, v2\n"
      "s_mov_b64 exec, -1\n"
      "s_sub_u32 s44, s44, 1\n"
      "s_cbranch_vccz L_bhA2_%=\n"
      "s_cmp_gt_i32 s44, 0\n"
      "s_cbranch_scc1 L_shA2_%=\n"
      "s_branch L_bhA2_%=\n"
      "L_shA3_%=:\n"
      "v_add_u32 v2, 10240, v7\n"
      "global_load_dwordx4 v[72:75], v2, s[10:11] offset:-1024 sc1\n"
      "global_load_dwordx4 v[80:83], v2, s[10:11] offset:0 sc1\n"
      "global_load_dwordx4 v[88:91], v2, s[10:11] offset:1024 sc1\n"
      "v_add_u32 v2, 10240, v7\n"
      "global_load_dwordx4 v[76:79], v2, s[12:13] offset:-1024 sc1\n"
      "global_load_dwordx4 v[84:87], v2, s[12:13] offset:0 sc1\n"
      "global_load_dwordx4 v[92:95], v2, s[12:13] offset:1024 sc1\n"
      "buffer_load_dword v62, v42, s[32:35], s63 offen offset:1536 sc1\n"
      "buffer_load_dword v63, v42, s[32:35], s63 offen offset:1792 sc1\n"
      "s_waitcnt vmcnt(0)\n"
      "v_add3_u32 v2, v72, v73, s30\n"
      "v_add3_u32 v2, v2, v74, v75\n"
      "v_add3_u32 v2, v2, v80, v81\n"
      "v_add3_u32 v2, v2, v82, v83\n"
      "v_add3_u32 v2, v2, v88, v89\n"
      "v_add3_u32 v2, v2, v90, v91\n"
      "v_xor_b32 v2, v2, v62\n"
      "v_add3_u32 v3, v76, v77, s30\n"
      "v_add3_u32 v3, v3, v78, v79\n"
      "v_add3_u32 v3, v3, v84, v85\n"
      "v_add3_u32 v3, v3, v86, v87\n"
      "v_add3_u32 v3, v3, v92, v93\n"
      "v_add3_u32 v3, v3, v94, v95\n"
      "v_xor_b32 v3, v3, v63\n"
      "v_or_b32 v2, v2, v3\n"
      "v_cmp_ne_u32 vcc, 0, v2\n"
      "s_mov_b64 exec, -1\n"
      "s_sub_u32 s44, s44, 1\n"
      "s_cbranch_vccz L_bhA3_%=\n"
      "s_cmp_gt_i32 s44, 0\n"
      "s_cbranch_scc1 L_shA3_%=\n"
      "s_branch L_bhA3_%=\n"
      "L_shB0_%=:\n"
      "v_add_u32 v2, 102400, v7\n"
      "global_load_dwordx4 v[224:227], v2, s[10:11] offset:-4096 sc1\n"
      "global_load_dwordx4 v[228:231], v2, s[10:11] offset:-3072 sc1\n"
      "global_load_dwordx4 v[232:235], v2, s[10:11] offset:-2048 sc1\n"
      "v_add_u32 v2, 102400, v7\n"
      "global_load_dwordx4 v[236:239], v2, s[10:11] offset:-1024 sc1\n"
      "global_load_dwordx4 v[240:243], v2, s[10:11] offset:0 sc1\n"
      "global_load_dwordx4 v[244:247], v2, s[10:11] offset:1024 sc1\n"
      "buffer_load_dword v32, v42, s[32:35], s63 offen offset:2048 sc1\n"
      "buffer_load_dword v33, v42, s[32:35], s63 offen offset:2304 sc1\n"
      "s_waitcnt vmcnt(0)\n"
      "v_add3_u32 v2, v224, v225, s30\n"
      "v_add3_u32 v2, v2, v226, v227\n"
      "v_add3_u32 v2, v2, v228, v229\n"
      "v_add3_u32 v2, v2, v230, v231\n"
      "v_add3_u32 v2, v2, v232, v233\n"
      "v_add3_u32 v2, v2, v234, v235\n"
      "v_xor_b32 v2, v2, v32\n"
      "v_add3_u32 v3, v236, v237, s30\n"
      "v_add3_u32 v3, v3, v238, v239\n"
      "v_add3_u32 v3, v3, v240, v241\n"
      "v_add3_u32 v3, v3, v242, v243\n"
      "v_add3_u32 v3, v3, v244, v245\n"
      "v_add3_u32 v3, v3, v246, v247\n"
      "v_xor_b32 v3, v3, v33\n"
      "v_or_b32 v2, v2, v3\n"
      "v_cmp_ne_u32 vcc, 0, v2\n"
      "s_mov_b64 exec, -1\n"
      "s_sub_u32 s44, s44, 1\n"
      "s_cbranch_vccz L_bhB0_%=\n"
      "s_cmp_gt_i32 s44, 0\n"
      "s_cbranch_scc1 L_shB0_%=\n"
      "s_branch L_bhB0_%=\n"
      "L_shB1_%=:\n"
      "v_add_u32 v2, 108544, v7\n"
      "global_load_dwordx4 v[8:11], v2, s[10:11] offset:-4096 sc1\n"
      "global_load_dwordx4 v[12:15], v2, s[10:11] offset:-3072 sc1\n"
      "global_load_dwordx4 v[16:19], v2, s[10:11] offset:-2048 sc1\n"
      "v_add_u32 v2, 108544, v7\n"
      "global_load_dwordx4 v[20:23], v2, s[10:11] offset:-1024 sc1\n"
      "global_load_dwordx4 v[128:131], v2, s[10:11] offset:0 sc1\n"
      "global_load_dwordx4 v[132:135], v2, s[10:11] offset:1024 sc1\n"
      "buffer_load_dword v34, v42, s[32:35], s63 offen offset:2560 sc1\n"
      "buffer_load_dword v35, v42, s[32:35], s63 offen offset:2816 sc1\n"
      "s_waitcnt vmcnt(0)\n"
      "v_add3_u32 v2, v8, v9, s30\n"
      "v_add3_u32 v2, v2, v10, v11\n"
      "v_add3_u32 v2, v2, v12, v13\n"
      "v_add3_u32 v2, v2, v14, v15\n"
      "v_add3_u32 v2, v2, v16, v17\n"
      "v_add3_u32 v2, v2, v18, v19\n"
      "v_xor_b32 v2, v2, v34\n"
      "v_add3_u32 v3, v20, v21, s30\n"
      "v_add3_u32 v3, v3, v22, v23\n"
      "v_add3_u32 v3, v3, v128, v129\n"
      "v_add3_u32 v3, v3, v130, v131\n"
      "v_add3_u32 v3, v3, v132, v133\n"
      "v_add3_u32 v3, v3, v134, v135\n"
      "v_xor_b32 v3, v3, v35\n"
      "v_or_b32 v2, v2, v3\n"
      "v_cmp_ne_u32 vcc, 0, v2\n"
      "s_mov_b64 exec, -1\n"
      "s_sub_u32 s44, s44, 1\n"
      "s_cbranch_vccz L_bhB1_%=\n"
      "s_cmp_gt_i32 s44, 0\n"
      "s_cbranch_scc1 L_shB1_%=\n"
      "s_branch L_bhB1_%=\n"
      "L_shB2_%=:\n"
      "v_add_u32 v2, 102400, v7\n"
      "global_load_dwordx4 v[136:139], v2, s[12:13] offset:-4096 sc1\n"
      "global_load_dwordx4 v[140:143], v2, s[12:13] offset:-3072 sc1\n"
      "global_load_dwordx4 v[144:147], v2, s[12:13] offset:-2048 sc1\n"
      "v_add_u32 v2, 102400, v7\n"
      "global_load_dwordx4 v[148:151], v2, s[12:13] offset:-1024 sc1\n"
      "global_load_dwordx4 v[152:155], v2, s[12:13] offset:0 sc1\n"
      "global_load_dwordx4 v[156:159], v2, s[12:13] offset:1024 sc1\n"
      "buffer_load_dword v24, v42, s[32:35], s63 offen offset:3072 sc1\n"
      "buffer_load_dword v25, v42, s[32:35], s63 offen offset:3328 sc1\n"
      "s_waitcnt vmcnt(0)\n"
      "v_add3_u32 v2, v136, v137, s30\n"
      "v_add3_u32 v2, v2, v138, v139\n"
      "v_add3_u32 v2, v2, v140, v141\n"
      "v_add3_u32 v2, v2, v142, v143\n"
      "v_add3_u32 v2, v2, v144, v145\n"
      "v_add3_u32 v2, v2, v146, v147\n"
      "v_xor_b32 v2, v2, v24\n"
      "v_add3_u32 v3, v148, v149, s30\n"
      "v_add3_u32 v3, v3, v150, v151\n"
      "v_add3_u32 v3, v3, v152, v153\n"
      "v_add3_u32 v3, v3, v154, v155\n"
      "v_add3_u32 v3, v3, v156, v157\n"
      "v_add3_u32 v3, v3, v158, v159\n"
      "v_xor_b32 v3, v3, v25\n"
      "v_or_b32 v2, v2, v3\n"
      "v_cmp_ne_u32 vcc, 0, v2\n"
      "s_mov_b64 exec, -1\n"
      "s_sub_u32 s44, s44, 1\n"
      "s_cbranch_vccz L_bhB2_%=\n"
      "s_cmp_gt_i32 s44, 0\n"
      "s_cbranch_scc1 L_shB2_%=\n"
      "s_branch L_bhB2_%=\n"
      "L_shB3_%=:\n"
      "v_add_u32 v2, 108544, v7\n"
      "global_load_dwordx4 v[172:175], v2, s[12:13] offset:-4096 sc1\n"
      "global_load_dwordx4 v[160:163], v2, s[12:13] offset:-3072 sc1\n"
      "global_load_dwordx4 v[164:167], v2, s[12:13] offset:-2048 sc1\n"
      "v_add_u32 v2, 108544, v7\n"
      "global_load_dwordx4 v[168:171], v2, s[12:13] offset:-1024 sc1\n"
      "global_load_dwordx4 v[64:67], v2, s[12:13] offset:0 sc1\n"
      "global_load_dwordx4 v[68:71], v2, s[12:13] offset:1024 sc1\n"
      "buffer_load_dword v26, v42, s[32:35], s63 offen offset:3584 sc1\n"
      "buffer_load_dword v27, v42, s[32:35], s63 offen offset:3840 sc1\n"
      "s_waitcnt vmcnt(0)\n"
      "v_add3_u32 v2, v172, v173, s30\n"
      "v_add3_u32 v2, v2, v174, v175\n"
      "v_add3_u32 v2, v2, v160, v161\n"
      "v_add3_u32 v2, v2, v162, v163\n"
      "v_add3_u32 v2, v2, v164, v165\n"
      "v_add3_u32 v2, v2, v166, v167\n"
      "v_xor_b32 v2, v2, v26\n"
      "v_add3_u32 v3, v168, v169, s30\n"
      "v_add3_u32 v3, v3, v170, v171\n"
      "v_add3_u32 v3, v3, v64, v65\n"
      "v_add3_u32 v3, v3, v66, v67\n"
      "v_add3_u32 v3, v3, v68, v69\n"
      "v_add3_u32 v3, v3, v70, v71\n"
      "v_xor_b32 v3, v3, v27\n"
      "v_or_b32 v2, v2, v3\n"
      "v_cmp_ne_u32 vcc, 0, v2\n"
      "s_mov_b64 exec, -1\n"
      "s_sub_u32 s44, s44, 1\n"
      "s_cbranch_vccz L_bhB3_%=\n"
      "s_cmp_gt_i32 s44, 0\n"
      "s_cbranch_scc1 L_shB3_%=\n"
      "s_branch L_bhB3_%=\n"
      "L_sgb_%=:\n"
      "buffer_load_dwordx4 v[244:247], v53, s[32:35], s66 offen sc1\n"
      "s_waitcnt vmcnt(0)\n"
      "v_add_u32 v245, v245, v247\n"
      "v_cmp_ne_u32 vcc, s54, v245\n"
      "s_mov_b64 exec, -1\n"
      "s_sub_u32 s44, s44, 1\n"
      "s_cbranch_vccz L_bgb_%=\n"
      "s_cmp_gt_i32 s44, 0\n"
      "s_cbranch_scc1 L_sgb_%=\n"
      "s_branch L_bgb_%=\n"
      : "+{s3}"(w), "+{v0}"(tid), "+{s[4:19]}"(s4), "+{s[20:27]}"(s20), "+{s[28:31]}"(s28), "+{s[36:39]}"(s36), "+{s[40:41]}"(tab), "+{s[52:59]}"(s52), "+{s[60:75]}"(s60),
        "+{s[76:91]}"(s76), "+{s[92:99]}"(s92), "+{v[232:235]}"(v232), "+{v1}"(v1), "+{v[192:195]}"(t[0]), "+{v[240:243]}"(t[1]), "+{v[196:199]}"(t[2]), "+{v[200:203]}"(t[3]), "+{v[204:207]}"(t[4]), "+{v[208:211]}"(t[5]), "+{v[212:215]}"(t[6]), "+{v[216:219]}"(t[7]), "+{v[220:223]}"(t[8]), "+{v[224:227]}"(t[9]), "+{v[236:239]}"(t[10]), "+{v[4:7]}"(t[11])
      : "{s2}"(blockIdx.x)
      : "memory", "scc", "vcc", "s0", "s1", "s32", "s33", "s34", "s35", "s42", "s43", "s44", "s45", "s46", "s47", "s48", "s49", "s50", "s51", "v2", "v3", "v8", "v9", "v10", "v11", "v12", "v13", "v14", "v15", "v16", "v17", "v18", "v19", "v20", "v21", "v22", "v23", "v24", "v25", "v26", "v27", "v28", "v29", "v30", "v31", "v32", "v33", "v34", "v35", "v36", "v37", "v38", "v39", "v40", "v41", "v42", "v43", "v44", "v45", "v46", "v47", "v48", "v49", "v50", "v51", "v52", "v53", "v54", "v55", "v56", "v57", "v58", "v59", "v60", "v61", "v62", "v63", "v64", "v65", "v66", "v67", "v68", "v69", "v70", "v71", "v72", "v73", "v74", "v75", "v76", "v77", "v78", "v79", "v80", "v81", "v82", "v83", "v84", "v85", "v86", "v87", "v88", "v89", "v90", "v91", "v92", "v93", "v94", "v95", "v96", "v97", "v98", "v99", "v100", "v101", "v102", "v103", "v104", "v105", "v106", "v107", "v108", "v109", "v110", "v111", "v112", "v113", "v114", "v115", "v116", "v117", "v118", "v119", "v120", "v121", "v122", "v123", "v124", "v125", "v126", "v127", "v128", "v129", "v130", "v131", "v132", "v133", "v134", "v135", "v136", "v137", "v138", "v139", "v140", "v141", "v142", "v143", "v144", "v145", "v146", "v147", "v148", "v149", "v150", "v151", "v152", "v153", "v154", "v155", "v156", "v157", "v158", "v159", "v160", "v161", "v162", "v163", "v164", "v165", "v166", "v167", "v168", "v169", "v170", "v171", "v172", "v173", "v174", "v175", "v176", "v177", "v178", "v179", "v180", "v181", "v182", "v183", "v184", "v185", "v186", "v187", "v188", "v189", "v190", "v191", "v228", "v229", "v230", "v231", "v244", "v245", "v246", "v247", "v248", "v249", "v250", "v251", "v252", "v253", "v254", "v255", "a0", "a1", "a2", "a3", "a4", "a5", "a6", "a7", "a8", "a9", "a10", "a11", "a12", "a13", "a14", "a15", "a16", "a17", "a18", "a19", "a20", "a21", "a22", "a23", "a24", "a25", "a26", "a27", "a28", "a29", "a30", "a31", "a32", "a33", "a34", "a35", "a36", "a37", "a38", "a39", "a40", "a41", "a42", "a43", "a44", "a45", "a46", "a47", "a48", "a49", "a50", "a51", "a52", "a53", "a54", "a55", "a56", "a57", "a58", "a59", "a60", "a61", "a62", "a63", "a64", "a65", "a66", "a67", "a68", "a69", "a70", "a71", "a72", "a73", "a74", "a75", "a76", "a77", "a78", "a79", "a80", "a81", "a82", "a83", "a84", "a85", "a86", "a87", "a88", "a89", "a90", "a91", "a92", "a93", "a94", "a95", "a96", "a97", "a98", "a99", "a100", "a101", "a102", "a103", "a104", "a105", "a106", "a107", "a108", "a109", "a110", "a111", "a112", "a113", "a114", "a115", "a116", "a117", "a118", "a119", "a120", "a121", "a122", "a123", "a124", "a125", "a126", "a127", "a128", "a129", "a130", "a131", "a132", "a133", "a134", "a135", "a136", "a137", "a138", "a139", "a140", "a141", "a142", "a143", "a144", "a145", "a146", "a147", "a148", "a149", "a150", "a151", "a152", "a153", "a154", "a155", "a156", "a157", "a158", "a159", "a160", "a161", "a162", "a163", "a164", "a165", "a166", "a167", "a168", "a169", "a170", "a171", "a172", "a173", "a174", "a175", "a176", "a177", "a178", "a179", "a180", "a181", "a182", "a183", "a184", "a185", "a186", "a187", "a188", "a189", "a190", "a191", "a192", "a193", "a194", "a195", "a196", "a197", "a198", "a199", "a200", "a201", "a202", "a203", "a204", "a205", "a206", "a207", "a208", "a209", "a210", "a211", "a212", "a213", "a214", "a215", "a216", "a217", "a218", "a219", "a220", "a221", "a222", "a223", "a224", "a225", "a226", "a227", "a228", "a229", "a230", "a231", "a232", "a233", "a234", "a235", "a236", "a237", "a238", "a239", "a240", "a241", "a242", "a243", "a244", "a245", "a246", "a247", "a248", "a249", "a250", "a251", "a252", "a253", "a254", "a255");
}

extern "C" void kernel_launch(void* const* d_in, const int* in_sizes, int n_in, void* d_out, int out_size,
                              void* d_ws, size_t ws_size, hipStream_t stream) {
  mega<<<256, 256, 0, stream>>>((const u16*)d_in[0], (const u16*)d_in[1], (const u16*)d_in[2], (const u16*)d_in[3],
                                (const u16*)d_in[4], (const u16*)d_in[5], (const u16*)d_in[7], (const u16*)d_in[8],
                                (const u16*)d_in[9], (const u16*)d_in[10], (const u16*)d_in[11], (char*)d_ws, (u16*)d_out);
}
```
